```python
import torch

def _softmax(x, axis=-1):
    return torch.softmax(x, dim=axis)

def _gelu(x):
    return torch.nn.functional.gelu(x, approximate='tanh')

def _silu(x):
    return torch.nn.functional.silu(x)

def _sigmoid(x):
    return torch.sigmoid(x)

def _layernorm(x, axis=-1, eps=1e-05):
    m = x.mean(dim=axis, keepdim=True)
    v = x.var(dim=axis, keepdim=True, unbiased=False)
    return (x - m) / torch.sqrt(v + eps)

def _scatter_add(tgt, idx, src, axis=0):
    return tgt.clone().index_add_(axis, idx, src)

def _segment_sum(src, seg, n):
    out = torch.zeros((n,) + src.shape[1:], dtype=src.dtype)
    return out.index_add_(0, seg, src)

def _pad(x, pw):
    flat = tuple((v for pair in reversed(pw) for v in pair))
    return torch.nn.functional.pad(x, flat)
kernel_name = 'adaLN_cond__spectral_gfnet__mixer_token_mlp__prompt'

def setup_inputs():

    _w = lambda a: (lambda t: t.to(torch.bfloat16) if t.is_floating_point() else t)(torch.as_tensor(a))

    def _A_setup():
        B, S, D = (1, 2048, 576)
        Df = D // 2 + 1
        return {'x': _w((lambda *s: torch.randn(*s, dtype=torch.bfloat16) * 0.05)(B, S, D)), 'w_r': _w((lambda *s: torch.randn(*s, dtype=torch.bfloat16) * 1.6)(S, Df) * 0.02), 'w_i': _w((lambda *s: torch.randn(*s, dtype=torch.bfloat16) * 1.6)(S, Df) * 0.02)}
    _A_in = _A_setup()

    def _B_setup():
        B, S, D, F = (1, 2048, 576, 4608)
        return {'x': _w((lambda *s: torch.randn(*s, dtype=torch.bfloat16) * 0.05)(B, S, D)), 'w1': _w((lambda *s: torch.randn(*s, dtype=torch.bfloat16) * 0.8)(S, F) * S ** (-0.5)), 'b1': _w((lambda *s: torch.randn(*s, dtype=torch.bfloat16) * 12.8)(F) * 0.02), 'w2': _w((lambda *s: torch.randn(*s, dtype=torch.bfloat16) * 0.2)(F, S) * F ** (-0.5)), 'b2': _w((lambda *s: torch.randn(*s, dtype=torch.bfloat16) * 1.6)(S) * 0.02)}
    _B_in = _B_setup()
    return {'A_x': _A_in['x'], 'A_w_r': _A_in['w_r'], 'A_w_i': _A_in['w_i'], 'B_x': _B_in['x'], 'B_w1': _B_in['w1'], 'B_b1': _B_in['b1'], 'B_w2': _B_in['w2'], 'B_b2': _B_in['b2'], 'c': _w((lambda *s: torch.randn(*s, dtype=torch.bfloat16) * 0.1)(1, 576) * 8.0), 'w_mod': _w((lambda *s: torch.randn(*s, dtype=torch.bfloat16) * 0.1)(576, 6 * 576) * 576 ** (-0.5) * 8.0)}

def reference(A_x, A_w_r, A_w_i, B_x, B_w1, B_b1, B_w2, B_b2, c, w_mod):

    def _A_ref(x, w_r, w_i):
        B, S, D = x.shape
        X = (lambda x, axes=(-2, -1): torch.fft.rfft2(x.float(), dim=axes))(x, axes=(-2, -1))
        W = w_r + 1j * w_i
        X = X * W
        y = (lambda x, s, axes=(-2, -1): torch.fft.irfft2(x, s=s, dim=axes).to(torch.bfloat16))(X, (S, D), axes=(-2, -1))
        return y

    def _B_ref(x, w1, b1, w2, b2):
        B, S, D = x.shape
        h = _layernorm(x, axis=-1)
        u = torch.einsum('bsd,sf->bdf', h, w1) + b1
        u = _gelu(u)
        y = torch.einsum('bdf,fs->bds', u, w2) + b2
        return (lambda x, *d: x.permute(*d))(y, 0, 2, 1)
    x = A_x
    mod = (_silu(c) @ w_mod).reshape(c.shape[0], 6, 1, -1)
    sa, ca, ga, sb, cb, gb = (mod[:, 0].clone(), mod[:, 1].clone(), mod[:, 2].clone(), mod[:, 3].clone(), mod[:, 4].clone(), mod[:, 5].clone())
    h = _layernorm(x, axis=-1) * (1 + ca) + sa
    x = x + ga * _A_ref(h, A_w_r, A_w_i)
    h = _layernorm(x, axis=-1) * (1 + cb) + sb
    x = x + gb * _B_ref(h, B_w1, B_b1, B_w2, B_b2)
    return x
```

```cpp
#include <hip/hip_runtime.h>
typedef unsigned short u16;
typedef unsigned long long u64;
static __device__ const unsigned __attribute__((aligned(128))) TAB[6736] = {
13312,13312,13312,13312,13312,0x2e1f3364,0x31a831a8,0x33642e1f,13312,0x31a831a8,0x34000000,0x31a8b1a8,13312,0x33642e1f,0x31a8b1a8,0xae1fb364,
13312,0x34000000,46080,0xb4000000,13312,0x3364ae1f,0xb1a8b1a8,0xae1f3364,13312,0x31a8b1a8,0xb4000000,0x31a831a8,13312,0x2e1fb364,0xb1a831a8,0x3364ae1f,
13312,46080,13312,46080,13312,0xae1fb364,0x31a831a8,0xb364ae1f,13312,0xb1a8b1a8,0x34000000,0xb1a831a8,13312,0xb364ae1f,0x31a8b1a8,0x2e1f3364,
13312,0xb4000000,46080,0x34000000,13312,0xb3642e1f,0xb1a8b1a8,0x2e1fb364,13312,0xb1a831a8,0xb4000000,0xb1a8b1a8,13312,0xae1f3364,0xb1a831a8,0xb3642e1f,
13312,13312,13312,13312,0x34000000,0x3364ae1f,0x31a8b1a8,0x2e1fb364,46080,0xb1a8b1a8,0xb4000000,0xb1a831a8,0xb4000000,0xae1f3364,0x31a831a8,0x3364ae1f,
13312,0x34000000,46080,0xb4000000,0x34000000,0xae1fb364,0xb1a831a8,0x33642e1f,46080,0xb1a831a8,0x34000000,0xb1a8b1a8,0xb4000000,0x33642e1f,0xb1a8b1a8,0x2e1f3364,
13312,46080,13312,46080,0x34000000,0xb3642e1f,0x31a8b1a8,0xae1f3364,46080,0x31a831a8,0xb4000000,0x31a8b1a8,0xb4000000,0x2e1fb364,0x31a831a8,0xb3642e1f,
13312,0xb4000000,46080,0x34000000,0x34000000,0x2e1f3364,0xb1a831a8,0xb364ae1f,46080,0x31a8b1a8,0x34000000,0x31a831a8,0xb4000000,0xb364ae1f,0xb1a8b1a8,0xae1fb364,
13312,13312,13312,13312,46080,0xae1fb364,0xb1a8b1a8,0xb364ae1f,13312,0x31a831a8,0x34000000,0x31a8b1a8,46080,0xb364ae1f,0xb1a831a8,0x2e1f3364,
13312,0x34000000,46080,0xb4000000,46080,0xb3642e1f,0x31a831a8,0x2e1fb364,13312,0x31a8b1a8,0xb4000000,0x31a831a8,46080,0xae1f3364,0x31a8b1a8,0xb3642e1f,
13312,46080,13312,46080,46080,0x2e1f3364,0xb1a8b1a8,0x33642e1f,13312,0xb1a8b1a8,0x34000000,0xb1a831a8,46080,0x33642e1f,0xb1a831a8,0xae1fb364,
13312,0xb4000000,46080,0x34000000,46080,0x3364ae1f,0x31a831a8,0xae1f3364,13312,0xb1a831a8,0xb4000000,0xb1a8b1a8,46080,0x2e1fb364,0x31a8b1a8,0x3364ae1f,
13312,13312,13312,13312,0xb4000000,0xb3642e1f,0xb1a831a8,0xae1f3364,46080,0xb1a8b1a8,0xb4000000,0xb1a831a8,0x34000000,0x2e1fb364,0xb1a8b1a8,0xb3642e1f,
13312,0x34000000,46080,0xb4000000,0xb4000000,0x2e1f3364,0x31a8b1a8,0xb364ae1f,46080,0xb1a831a8,0x34000000,0xb1a8b1a8,0x34000000,0xb364ae1f,0x31a831a8,0xae1fb364,
13312,46080,13312,46080,0xb4000000,0x3364ae1f,0xb1a831a8,0x2e1fb364,46080,0x31a831a8,0xb4000000,0x31a8b1a8,0x34000000,0xae1f3364,0xb1a8b1a8,0x3364ae1f,
13312,0xb4000000,46080,0x34000000,0xb4000000,0xae1fb364,0x31a8b1a8,0x33642e1f,46080,0x31a8b1a8,0x34000000,0x31a831a8,0x34000000,0x33642e1f,0x31a831a8,0x2e1f3364,
0x34000000,0x34000000,0x34000000,0x34000000,0x34000000,0x3364ae1f,0x31a8b1a8,0x2e1fb364,0x34000000,0x31a8b1a8,46080,0xb1a8b1a8,0x34000000,0x2e1fb364,0xb1a8b1a8,0xb3642e1f,
0x34000000,46080,0xb4000000,13312,0x34000000,0xae1fb364,0xb1a831a8,0x33642e1f,0x34000000,0xb1a8b1a8,13312,0x31a8b1a8,0x34000000,0xb364ae1f,0x31a831a8,0xae1fb364,
0x34000000,0xb4000000,0x34000000,0xb4000000,0x34000000,0xb3642e1f,0x31a8b1a8,0xae1f3364,0x34000000,0xb1a831a8,46080,0x31a831a8,0x34000000,0xae1f3364,0xb1a8b1a8,0x3364ae1f,
0x34000000,13312,0xb4000000,46080,0x34000000,0x2e1f3364,0xb1a831a8,0xb364ae1f,0x34000000,0x31a831a8,13312,0xb1a831a8,0x34000000,0x33642e1f,0x31a831a8,0x2e1f3364,
0x34000000,0x34000000,0x34000000,0x34000000,46080,0xae1fb364,0xb1a8b1a8,0xb364ae1f,0xb4000000,0xb1a831a8,13312,0x31a831a8,13312,0x33642e1f,0x31a8b1a8,0xae1fb364,
0x34000000,46080,0xb4000000,13312,46080,0xb3642e1f,0x31a831a8,0x2e1fb364,0xb4000000,0x31a831a8,46080,0xb1a831a8,13312,0x2e1fb364,0xb1a831a8,0x3364ae1f,
0x34000000,0xb4000000,0x34000000,0xb4000000,46080,0x2e1f3364,0xb1a8b1a8,0x33642e1f,0xb4000000,0x31a8b1a8,13312,0xb1a8b1a8,13312,0xb364ae1f,0x31a8b1a8,0x2e1f3364,
0x34000000,13312,0xb4000000,46080,46080,0x3364ae1f,0x31a831a8,0xae1f3364,0xb4000000,0xb1a8b1a8,46080,0x31a8b1a8,13312,0xae1f3364,0xb1a831a8,0xb3642e1f,
0x34000000,0x34000000,0x34000000,0x34000000,0xb4000000,0xb3642e1f,0xb1a831a8,0xae1f3364,0x34000000,0x31a8b1a8,46080,0xb1a8b1a8,0xb4000000,0xae1f3364,0x31a831a8,0x3364ae1f,
0x34000000,46080,0xb4000000,13312,0xb4000000,0x2e1f3364,0x31a8b1a8,0xb364ae1f,0x34000000,0xb1a8b1a8,13312,0x31a8b1a8,0xb4000000,0x33642e1f,0xb1a8b1a8,0x2e1f3364,
0x34000000,0xb4000000,0x34000000,0xb4000000,0xb4000000,0x3364ae1f,0xb1a831a8,0x2e1fb364,0x34000000,0xb1a831a8,46080,0x31a831a8,0xb4000000,0x2e1fb364,0x31a831a8,0xb3642e1f,
0x34000000,13312,0xb4000000,46080,0xb4000000,0xae1fb364,0x31a8b1a8,0x33642e1f,0x34000000,0x31a831a8,13312,0xb1a831a8,0xb4000000,0xb364ae1f,0xb1a8b1a8,0xae1fb364,
0x34000000,0x34000000,0x34000000,0x34000000,13312,0x2e1f3364,0x31a831a8,0x33642e1f,0xb4000000,0xb1a831a8,13312,0x31a831a8,46080,0xb364ae1f,0xb1a831a8,0x2e1f3364,
0x34000000,46080,0xb4000000,13312,13312,0x3364ae1f,0xb1a8b1a8,0xae1f3364,0xb4000000,0x31a831a8,46080,0xb1a831a8,46080,0xae1f3364,0x31a8b1a8,0xb3642e1f,
0x34000000,0xb4000000,0x34000000,0xb4000000,13312,0xae1fb364,0x31a831a8,0xb364ae1f,0xb4000000,0x31a8b1a8,13312,0xb1a8b1a8,46080,0x33642e1f,0xb1a831a8,0xae1fb364,
0x34000000,13312,0xb4000000,46080,13312,0xb3642e1f,0xb1a8b1a8,0x2e1fb364,0xb4000000,0xb1a8b1a8,46080,0x31a8b1a8,46080,0x2e1fb364,0x31a8b1a8,0x3364ae1f,
13312,13312,13312,13312,0x2a3e33d9,0x307232a7,0x32a73072,0x33d92a3e,0x2e1f3364,0x33642e1f,0x3364ae1f,0x2e1fb364,0x307232a7,0x33d9aa3e,0x2a3eb3d9,0xb2a7b072,
0x31a831a8,0x31a8b1a8,0xb1a8b1a8,0xb1a831a8,0x32a73072,0x2a3eb3d9,0xb3d92a3e,0x307232a7,0x33642e1f,0xae1fb364,0xae1f3364,0x3364ae1f,0x33d92a3e,0xb2a7b072,0x307232a7,0xaa3eb3d9,
0x34000000,0xb4000000,0x34000000,0xb4000000,0x33d9aa3e,0xb2a73072,0x3072b2a7,0xaa3e33d9,0x3364ae1f,0xae1f3364,0xae1fb364,0x33642e1f,0x32a7b072,0x2a3e33d9,0xb3d9aa3e,0x3072b2a7,
0x31a8b1a8,0x31a831a8,0xb1a831a8,0xb1a8b1a8,0x3072b2a7,0x33d92a3e,0x2a3e33d9,0xb2a73072,0x2e1fb364,0x3364ae1f,0x33642e1f,0x2e1f3364,0x2a3eb3d9,0x3072b2a7,0x32a7b072,0x33d9aa3e,
13312,13312,13312,13312,0x33d9aa3e,0x32a7b072,0x3072b2a7,0x2a3eb3d9,0xae1fb364,0xb364ae1f,0xb3642e1f,0xae1f3364,0xb2a73072,0x2a3e33d9,0x33d92a3e,0x3072b2a7,
0x31a831a8,0x31a8b1a8,0xb1a8b1a8,0xb1a831a8,0x3072b2a7,0xb3d9aa3e,0x2a3e33d9,0x32a7b072,0xb364ae1f,0x2e1f3364,0x2e1fb364,0xb3642e1f,0xaa3e33d9,0x3072b2a7,0xb2a73072,0x33d9aa3e,
0x34000000,0xb4000000,0x34000000,0xb4000000,0xaa3eb3d9,0x307232a7,0xb2a7b072,0x33d92a3e,0xb3642e1f,0x2e1fb364,0x2e1f3364,0xb364ae1f,0x307232a7,0xb3d92a3e,0x2a3eb3d9,0x32a73072,
0x31a8b1a8,0x31a831a8,0xb1a831a8,0xb1a8b1a8,0xb2a7b072,0x2a3eb3d9,0x33d9aa3e,0x307232a7,0xae1f3364,0xb3642e1f,0xb364ae1f,0xae1fb364,0x33d92a3e,0x32a73072,0x307232a7,0x2a3e33d9,
13312,13312,13312,13312,0xaa3eb3d9,0xb072b2a7,0xb2a7b072,0xb3d9aa3e,0x2e1f3364,0x33642e1f,0x3364ae1f,0x2e1fb364,0xb072b2a7,0xb3d92a3e,0xaa3e33d9,0x32a73072,
0x31a831a8,0x31a8b1a8,0xb1a8b1a8,0xb1a831a8,0xb2a7b072,0xaa3e33d9,0x33d9aa3e,0xb072b2a7,0x33642e1f,0xae1fb364,0xae1f3364,0x3364ae1f,0xb3d9aa3e,0x32a73072,0xb072b2a7,0x2a3e33d9,
0x34000000,0xb4000000,0x34000000,0xb4000000,0xb3d92a3e,0x32a7b072,0xb07232a7,0x2a3eb3d9,0x3364ae1f,0xae1f3364,0xae1fb364,0x33642e1f,0xb2a73072,0xaa3eb3d9,0x33d92a3e,0xb07232a7,
0x31a8b1a8,0x31a831a8,0xb1a831a8,0xb1a8b1a8,0xb07232a7,0xb3d9aa3e,0xaa3eb3d9,0x32a7b072,0x2e1fb364,0x3364ae1f,0x33642e1f,0x2e1f3364,0xaa3e33d9,0xb07232a7,0xb2a73072,0xb3d92a3e,
13312,13312,13312,13312,0xb3d92a3e,0xb2a73072,0xb07232a7,0xaa3e33d9,0xae1fb364,0xb364ae1f,0xb3642e1f,0xae1f3364,0x32a7b072,0xaa3eb3d9,0xb3d9aa3e,0xb07232a7,
0x31a831a8,0x31a8b1a8,0xb1a8b1a8,0xb1a831a8,0xb07232a7,0x33d92a3e,0xaa3eb3d9,0xb2a73072,0xb364ae1f,0x2e1f3364,0x2e1fb364,0xb3642e1f,0x2a3eb3d9,0xb07232a7,0x32a7b072,0xb3d92a3e,
0x34000000,0xb4000000,0x34000000,0xb4000000,0x2a3e33d9,0xb072b2a7,0x32a73072,0xb3d9aa3e,0xb3642e1f,0x2e1fb364,0x2e1f3364,0xb364ae1f,0xb072b2a7,0x33d9aa3e,0xaa3e33d9,0xb2a7b072,
0x31a8b1a8,0x31a831a8,0xb1a831a8,0xb1a8b1a8,0x32a73072,0xaa3e33d9,0xb3d92a3e,0xb072b2a7,0xae1f3364,0xb3642e1f,0xb364ae1f,0xae1fb364,0xb3d9aa3e,0xb2a7b072,0xb072b2a7,0xaa3eb3d9,
0x34000000,0x34000000,0x34000000,0x34000000,0x33d9aa3e,0x32a7b072,0x3072b2a7,0x2a3eb3d9,0x3364ae1f,0x2e1fb364,0xae1fb364,0xb364ae1f,0x32a7b072,0xaa3eb3d9,0xb3d9aa3e,0xb07232a7,
0x31a8b1a8,0xb1a8b1a8,0xb1a831a8,0x31a831a8,0x3072b2a7,0xb3d9aa3e,0x2a3e33d9,0x32a7b072,0x2e1fb364,0xb3642e1f,0x33642e1f,0xae1fb364,0x2a3eb3d9,0xb07232a7,0x32a7b072,0xb3d92a3e,
46080,13312,46080,13312,0xaa3eb3d9,0x307232a7,0xb2a7b072,0x33d92a3e,0xae1fb364,0x33642e1f,0xb3642e1f,0x2e1fb364,0xb072b2a7,0x33d9aa3e,0xaa3e33d9,0xb2a7b072,
0xb1a8b1a8,0x31a8b1a8,0x31a831a8,0xb1a831a8,0xb2a7b072,0x2a3eb3d9,0x33d9aa3e,0x307232a7,0xb364ae1f,0xae1fb364,0x2e1fb364,0x3364ae1f,0xb3d9aa3e,0xb2a7b072,0xb072b2a7,0xaa3eb3d9,
0x34000000,0x34000000,0x34000000,0x34000000,0xaa3eb3d9,0xb072b2a7,0xb2a7b072,0xb3d9aa3e,0xb3642e1f,0xae1f3364,0x2e1f3364,0x33642e1f,0x307232a7,0x33d9aa3e,0x2a3eb3d9,0xb2a7b072,
0x31a8b1a8,0xb1a8b1a8,0xb1a831a8,0x31a831a8,0xb2a7b072,0xaa3e33d9,0x33d9aa3e,0xb072b2a7,0xae1f3364,0x3364ae1f,0xb364ae1f,0x2e1f3364,0x33d92a3e,0xb2a7b072,0x307232a7,0xaa3eb3d9,
46080,13312,46080,13312,0xb3d92a3e,0x32a7b072,0xb07232a7,0x2a3eb3d9,0x2e1f3364,0xb364ae1f,0x3364ae1f,0xae1f3364,0x32a7b072,0x2a3e33d9,0xb3d9aa3e,0x3072b2a7,
0xb1a8b1a8,0x31a8b1a8,0x31a831a8,0xb1a831a8,0xb07232a7,0xb3d9aa3e,0xaa3eb3d9,0x32a7b072,0x33642e1f,0x2e1f3364,0xae1f3364,0xb3642e1f,0x2a3eb3d9,0x3072b2a7,0x32a7b072,0x33d9aa3e,
0x34000000,0x34000000,0x34000000,0x34000000,0xb3d92a3e,0xb2a73072,0xb07232a7,0xaa3e33d9,0x3364ae1f,0x2e1fb364,0xae1fb364,0xb364ae1f,0xb2a73072,0x2a3e33d9,0x33d92a3e,0x3072b2a7,
0x31a8b1a8,0xb1a8b1a8,0xb1a831a8,0x31a831a8,0xb07232a7,0x33d92a3e,0xaa3eb3d9,0xb2a73072,0x2e1fb364,0xb3642e1f,0x33642e1f,0xae1fb364,0xaa3e33d9,0x3072b2a7,0xb2a73072,0x33d9aa3e,
46080,13312,46080,13312,0x2a3e33d9,0xb072b2a7,0x32a73072,0xb3d9aa3e,0xae1fb364,0x33642e1f,0xb3642e1f,0x2e1fb364,0x307232a7,0xb3d92a3e,0x2a3eb3d9,0x32a73072,
0xb1a8b1a8,0x31a8b1a8,0x31a831a8,0xb1a831a8,0x32a73072,0xaa3e33d9,0xb3d92a3e,0xb072b2a7,0xb364ae1f,0xae1fb364,0x2e1fb364,0x3364ae1f,0x33d92a3e,0x32a73072,0x307232a7,0x2a3e33d9,
0x34000000,0x34000000,0x34000000,0x34000000,0x2a3e33d9,0x307232a7,0x32a73072,0x33d92a3e,0xb3642e1f,0xae1f3364,0x2e1f3364,0x33642e1f,0xb072b2a7,0xb3d92a3e,0xaa3e33d9,0x32a73072,
0x31a8b1a8,0xb1a8b1a8,0xb1a831a8,0x31a831a8,0x32a73072,0x2a3eb3d9,0xb3d92a3e,0x307232a7,0xae1f3364,0x3364ae1f,0xb364ae1f,0x2e1f3364,0xb3d9aa3e,0x32a73072,0xb072b2a7,0x2a3e33d9,
46080,13312,46080,13312,0x33d9aa3e,0xb2a73072,0x3072b2a7,0xaa3e33d9,0x2e1f3364,0xb364ae1f,0x3364ae1f,0xae1f3364,0xb2a73072,0xaa3eb3d9,0x33d92a3e,0xb07232a7,
0xb1a8b1a8,0x31a8b1a8,0x31a831a8,0xb1a831a8,0x3072b2a7,0x33d92a3e,0x2a3e33d9,0xb2a73072,0x33642e1f,0x2e1f3364,0xae1f3364,0xb3642e1f,0xaa3e33d9,0xb07232a7,0xb2a73072,0xb3d92a3e,
13312,13312,13312,0,13312,0x31243221,0x33e1298f,0,13312,0x33e1298f,0x2d79b384,0,0,0,0,0,
13312,0x32eeb000,0xb2eeb000,0,13312,0x2d79b384,0xb1243221,0,13312,0xad79b384,0x31243221,0,0,0,0,0,
13312,0xb2eeb000,0x32eeb000,0,13312,0xb3e1298f,0xad79b384,0,13312,0xb1243221,0xb3e1298f,0,0,0,0,0,
0,0,0,0,0,0,0,0,0,0,0,0,0,0,0,0,
13312,13312,13312,0,0x32eeb000,0x2d79b384,0xad79b384,0,0xb2eeb000,0xb1243221,0x31243221,0,0,0,0,0,
13312,0x32eeb000,0xb2eeb000,0,0x32eeb000,0xb3e1298f,0x33e1298f,0,0xb2eeb000,0x33e1298f,0xb3e1298f,0,0,0,0,0,
13312,0xb2eeb000,0x32eeb000,0,0x32eeb000,0x31243221,0xb1243221,0,0xb2eeb000,0xad79b384,0x2d79b384,0,0,0,0,0,
0,0,0,0,0,0,0,0,0,0,0,0,0,0,0,0,
13312,13312,13312,0,0xb2eeb000,0xb3e1298f,0xb1243221,0,0x32eeb000,0xad79b384,0xb3e1298f,0,0,0,0,0,
13312,0x32eeb000,0xb2eeb000,0,0xb2eeb000,0x31243221,0xad79b384,0,0x32eeb000,0xb1243221,0x2d79b384,0,0,0,0,0,
13312,0xb2eeb000,0x32eeb000,0,0xb2eeb000,0x2d79b384,0x33e1298f,0,0x32eeb000,0x33e1298f,0x31243221,0,0,0,0,0,
0,0,0,0,0,0,0,0,0,0,0,0,0,0,0,0,
0,0,0,0,0,0,0,0,0,0,0,0,0,0,0,0,
0,0,0,0,0,0,0,0,0,0,0,0,0,0,0,0,
0,0,0,0,0,0,0,0,0,0,0,0,0,0,0,0,
0,0,0,0,0,0,0,0,0,0,0,0,0,0,0,0,
0x34000000,0x34000000,0x34000000,0,0x34000000,0x3221b124,0x298fb3e1,0,0x34000000,0x298fb3e1,0xb384ad79,0,0,0,0,0,
0x34000000,0xb000b2ee,0xb00032ee,0,0x34000000,0xb384ad79,0x32213124,0,0x34000000,0xb3842d79,0x3221b124,0,0,0,0,0,
0x34000000,0xb00032ee,0xb000b2ee,0,0x34000000,0x298f33e1,0xb3842d79,0,0x34000000,0x32213124,0x298f33e1,0,0,0,0,0,
0,0,0,0,0,0,0,0,0,0,0,0,0,0,0,0,
0x34000000,0x34000000,0x34000000,0,0xb000b2ee,0xb384ad79,0xb3842d79,0,0xb00032ee,0x32213124,0x3221b124,0,0,0,0,0,
0x34000000,0xb000b2ee,0xb00032ee,0,0xb000b2ee,0x298f33e1,0x298fb3e1,0,0xb00032ee,0x298fb3e1,0x298f33e1,0,0,0,0,0,
0x34000000,0xb00032ee,0xb000b2ee,0,0xb000b2ee,0x3221b124,0x32213124,0,0xb00032ee,0xb3842d79,0xb384ad79,0,0,0,0,0,
0,0,0,0,0,0,0,0,0,0,0,0,0,0,0,0,
0x34000000,0x34000000,0x34000000,0,0xb00032ee,0x298f33e1,0x32213124,0,0xb000b2ee,0xb3842d79,0x298f33e1,0,0,0,0,0,
0x34000000,0xb000b2ee,0xb00032ee,0,0xb00032ee,0x3221b124,0xb3842d79,0,0xb000b2ee,0x32213124,0xb384ad79,0,0,0,0,0,
0x34000000,0xb00032ee,0xb000b2ee,0,0xb00032ee,0xb384ad79,0x298fb3e1,0,0xb000b2ee,0x298fb3e1,0x3221b124,0,0,0,0,0,
0,0,0,0,0,0,0,0,0,0,0,0,0,0,0,0,
0,0,0,0,0,0,0,0,0,0,0,0,0,0,0,0,
0,0,0,0,0,0,0,0,0,0,0,0,0,0,0,0,
0,0,0,0,0,0,0,0,0,0,0,0,0,0,0,0,
0,0,0,0,0,0,0,0,0,0,0,0,0,0,0,0,
13312,0x2a3e33d9,0x2e1f3364,0x307232a7,13312,0x307232a7,0x33642e1f,0x33d9aa3e,13312,0x32a73072,0x3364ae1f,0x2a3eb3d9,13312,0x33d92a3e,0x2e1fb364,0xb2a7b072,
13312,0x33d9aa3e,0xae1fb364,0xb2a73072,13312,0x32a7b072,0xb364ae1f,0x2a3e33d9,13312,0x3072b2a7,0xb3642e1f,0x33d92a3e,13312,0x2a3eb3d9,0xae1f3364,0x3072b2a7,
13312,0xaa3eb3d9,0x2e1f3364,0xb072b2a7,13312,0xb072b2a7,0x33642e1f,0xb3d92a3e,13312,0xb2a7b072,0x3364ae1f,0xaa3e33d9,13312,0xb3d9aa3e,0x2e1fb364,0x32a73072,
13312,0xb3d92a3e,0xae1fb364,0x32a7b072,13312,0xb2a73072,0xb364ae1f,0xaa3eb3d9,13312,0xb07232a7,0xb3642e1f,0xb3d9aa3e,13312,0xaa3e33d9,0xae1f3364,0xb07232a7,
0x31a831a8,0x32a73072,0x33642e1f,0x33d92a3e,0x31a8b1a8,0x2a3eb3d9,0xae1fb364,0xb2a7b072,0xb1a8b1a8,0xb3d92a3e,0xae1f3364,0x307232a7,0xb1a831a8,0x307232a7,0x3364ae1f,0xaa3eb3d9,
0x31a831a8,0x3072b2a7,0xb364ae1f,0xaa3e33d9,0x31a8b1a8,0xb3d9aa3e,0x2e1f3364,0x3072b2a7,0xb1a8b1a8,0x2a3e33d9,0x2e1fb364,0xb2a73072,0xb1a831a8,0x32a7b072,0xb3642e1f,0x33d9aa3e,
0x31a831a8,0xb2a7b072,0x33642e1f,0xb3d9aa3e,0x31a8b1a8,0xaa3e33d9,0xae1fb364,0x32a73072,0xb1a8b1a8,0x33d9aa3e,0xae1f3364,0xb072b2a7,0xb1a831a8,0xb072b2a7,0x3364ae1f,0x2a3e33d9,
0x31a831a8,0xb07232a7,0xb364ae1f,0x2a3eb3d9,0x31a8b1a8,0x33d92a3e,0x2e1f3364,0xb07232a7,0xb1a8b1a8,0xaa3eb3d9,0x2e1fb364,0x32a7b072,0xb1a831a8,0xb2a73072,0xb3642e1f,0xb3d92a3e,
0x34000000,0x33d9aa3e,0x3364ae1f,0x32a7b072,0xb4000000,0xb2a73072,0xae1f3364,0x2a3e33d9,0x34000000,0x3072b2a7,0xae1fb364,0xb3d9aa3e,0xb4000000,0xaa3e33d9,0x33642e1f,0x3072b2a7,
0x34000000,0xaa3eb3d9,0xb3642e1f,0x307232a7,0xb4000000,0x307232a7,0x2e1fb364,0xb3d92a3e,0x34000000,0xb2a7b072,0x2e1f3364,0x2a3eb3d9,0xb4000000,0x33d92a3e,0xb364ae1f,0x32a73072,
0x34000000,0xb3d92a3e,0x3364ae1f,0xb2a73072,0xb4000000,0x32a7b072,0xae1f3364,0xaa3eb3d9,0x34000000,0xb07232a7,0xae1fb364,0x33d92a3e,0xb4000000,0x2a3eb3d9,0x33642e1f,0xb07232a7,
0x34000000,0x2a3e33d9,0xb3642e1f,0xb072b2a7,0xb4000000,0xb072b2a7,0x2e1fb364,0x33d9aa3e,0x34000000,0x32a73072,0x2e1f3364,0xaa3e33d9,0xb4000000,0xb3d9aa3e,0xb364ae1f,0xb2a7b072,
0x31a8b1a8,0x3072b2a7,0x2e1fb364,0x2a3eb3d9,0x31a831a8,0x33d92a3e,0x3364ae1f,0x3072b2a7,0xb1a831a8,0x2a3e33d9,0x33642e1f,0x32a7b072,0xb1a8b1a8,0xb2a73072,0x2e1f3364,0x33d9aa3e,
0x31a8b1a8,0xb2a7b072,0xae1f3364,0x33d92a3e,0x31a831a8,0x2a3eb3d9,0xb3642e1f,0x32a73072,0xb1a831a8,0x33d9aa3e,0xb364ae1f,0x307232a7,0xb1a8b1a8,0x307232a7,0xae1fb364,0x2a3e33d9,
0x31a8b1a8,0xb07232a7,0x2e1fb364,0xaa3e33d9,0x31a831a8,0xb3d9aa3e,0x3364ae1f,0xb07232a7,0xb1a831a8,0xaa3eb3d9,0x33642e1f,0xb2a73072,0xb1a8b1a8,0x32a7b072,0x2e1f3364,0xb3d92a3e,
0x31a8b1a8,0x32a73072,0xae1f3364,0xb3d9aa3e,0x31a831a8,0xaa3e33d9,0xb3642e1f,0xb2a7b072,0xb1a831a8,0xb3d92a3e,0xb364ae1f,0xb072b2a7,0xb1a8b1a8,0xb072b2a7,0xae1fb364,0xaa3eb3d9,
0x34000000,0x33d9aa3e,0x3364ae1f,0x32a7b072,0x34000000,0x32a7b072,0x2e1fb364,0xaa3eb3d9,0x34000000,0x3072b2a7,0xae1fb364,0xb3d9aa3e,0x34000000,0x2a3eb3d9,0xb364ae1f,0xb07232a7,
0x34000000,0xaa3eb3d9,0xb3642e1f,0x307232a7,0x34000000,0xb072b2a7,0xae1f3364,0x33d9aa3e,0x34000000,0xb2a7b072,0x2e1f3364,0x2a3eb3d9,0x34000000,0xb3d9aa3e,0x33642e1f,0xb2a7b072,
0x34000000,0xb3d92a3e,0x3364ae1f,0xb2a73072,0x34000000,0xb2a73072,0x2e1fb364,0x2a3e33d9,0x34000000,0xb07232a7,0xae1fb364,0x33d92a3e,0x34000000,0xaa3e33d9,0xb364ae1f,0x3072b2a7,
0x34000000,0x2a3e33d9,0xb3642e1f,0xb072b2a7,0x34000000,0x307232a7,0xae1f3364,0xb3d92a3e,0x34000000,0x32a73072,0x2e1f3364,0xaa3e33d9,0x34000000,0x33d92a3e,0x33642e1f,0x32a73072,
0x31a8b1a8,0x3072b2a7,0x2e1fb364,0x2a3eb3d9,0xb1a8b1a8,0xb3d9aa3e,0xb3642e1f,0xb07232a7,0xb1a831a8,0x2a3e33d9,0x33642e1f,0x32a7b072,0x31a831a8,0x32a7b072,0xae1fb364,0xb3d92a3e,
0x31a8b1a8,0xb2a7b072,0xae1f3364,0x33d92a3e,0xb1a8b1a8,0xaa3e33d9,0x3364ae1f,0xb2a7b072,0xb1a831a8,0x33d9aa3e,0xb364ae1f,0x307232a7,0x31a831a8,0xb072b2a7,0x2e1f3364,0xaa3eb3d9,
0x31a8b1a8,0xb07232a7,0x2e1fb364,0xaa3e33d9,0xb1a8b1a8,0x33d92a3e,0xb3642e1f,0x3072b2a7,0xb1a831a8,0xaa3eb3d9,0x33642e1f,0xb2a73072,0x31a831a8,0xb2a73072,0xae1fb364,0x33d9aa3e,
0x31a8b1a8,0x32a73072,0xae1f3364,0xb3d9aa3e,0xb1a8b1a8,0x2a3eb3d9,0x3364ae1f,0x32a73072,0xb1a831a8,0xb3d92a3e,0xb364ae1f,0xb072b2a7,0x31a831a8,0x307232a7,0x2e1f3364,0x2a3e33d9,
46080,0xaa3eb3d9,0xae1fb364,0xb072b2a7,13312,0x307232a7,0x33642e1f,0x33d9aa3e,46080,0xb2a7b072,0xb3642e1f,0xaa3e33d9,13312,0x33d92a3e,0x2e1fb364,0xb2a7b072,
46080,0xb3d92a3e,0x2e1f3364,0x32a7b072,13312,0x32a7b072,0xb364ae1f,0x2a3e33d9,46080,0xb07232a7,0x3364ae1f,0xb3d9aa3e,13312,0x2a3eb3d9,0xae1f3364,0x3072b2a7,
46080,0x2a3e33d9,0xae1fb364,0x307232a7,13312,0xb072b2a7,0x33642e1f,0xb3d92a3e,46080,0x32a73072,0xb3642e1f,0x2a3eb3d9,13312,0xb3d9aa3e,0x2e1fb364,0x32a73072,
46080,0x33d9aa3e,0x2e1f3364,0xb2a73072,13312,0xb2a73072,0xb364ae1f,0xaa3eb3d9,46080,0x3072b2a7,0x3364ae1f,0x33d92a3e,13312,0xaa3e33d9,0xae1f3364,0xb07232a7,
0xb1a8b1a8,0xb2a7b072,0xb364ae1f,0xb3d9aa3e,0x31a8b1a8,0x2a3eb3d9,0xae1fb364,0xb2a7b072,0x31a831a8,0x33d9aa3e,0x2e1fb364,0xb072b2a7,0xb1a831a8,0x307232a7,0x3364ae1f,0xaa3eb3d9,
0xb1a8b1a8,0xb07232a7,0x33642e1f,0x2a3eb3d9,0x31a8b1a8,0xb3d9aa3e,0x2e1f3364,0x3072b2a7,0x31a831a8,0xaa3eb3d9,0xae1f3364,0x32a7b072,0xb1a831a8,0x32a7b072,0xb3642e1f,0x33d9aa3e,
0xb1a8b1a8,0x32a73072,0xb364ae1f,0x33d92a3e,0x31a8b1a8,0xaa3e33d9,0xae1fb364,0x32a73072,0x31a831a8,0xb3d92a3e,0x2e1fb364,0x307232a7,0xb1a831a8,0xb072b2a7,0x3364ae1f,0x2a3e33d9,
0xb1a8b1a8,0x3072b2a7,0x33642e1f,0xaa3e33d9,0x31a8b1a8,0x33d92a3e,0x2e1f3364,0xb07232a7,0x31a831a8,0x2a3e33d9,0xae1f3364,0xb2a73072,0xb1a831a8,0xb2a73072,0xb3642e1f,0xb3d92a3e,
0x3f800000,0x3f800000,0x3f800000,0x3f800000,0x3f800000,0x3f7fec43,0x3f7fb10f,0x3f7f4e6d,0x3f800000,0x3f7fb10f,0x3f7ec46d,0x3f7d3aac,0x3f800000,0x3f7f4e6d,0x3f7d3aac,0x3f79c79d,
0x3f800000,0x3f7ec46d,0x3f7b14be,0x3f74fa0b,0x3f800000,0x3f7e1324,0x3f7853f8,0x3f6ed89e,0x3f800000,0x3f7d3aac,0x3f74fa0b,0x3f676bd8,0x3f800000,0x3f7c3b28,0x3f710908,0x3f5ebe05,
0x3f800000,0x3f7b14be,0x3f6c835e,0x3f54db31,0x3f800000,0x3f79c79d,0x3f676bd8,0x3f49d112,0x3f800000,0x3f7853f8,0x3f61c598,0x3f3daef9,0x3f800000,0x3f76ba07,0x3f5b941a,0x3f3085bb,
0x3f800000,0x3f74fa0b,0x3f54db31,0x3f226799,0x3f800000,0x3f731447,0x3f4d9f02,0x3f13682a,0x3f800000,0x3f710908,0x3f45e403,0x3f039c3d,0x3f800000,0x3f6ed89e,0x3f3daef9,0x3ee63375,
0x3f800000,0x3f800000,0x3f800000,0x3f800000,0x3f7ec46d,0x3f7e1324,0x3f7d3aac,0x3f7c3b28,0x3f7b14be,0x3f7853f8,0x3f74fa0b,0x3f710908,0x3f74fa0b,0x3f6ed89e,0x3f676bd8,0x3f5ebe05,
0x3f6c835e,0x3f61c598,0x3f54db31,0x3f45e403,0x3f61c598,0x3f514d3d,0x3f3daef9,0x3f273656,0x3f54db31,0x3f3daef9,0x3f226799,0x3f039c3d,0x3f45e403,0x3f273656,0x3f039c3d,0x3eb8442a,
0x3f3504f3,0x3f0e39da,0x3ec3ef15,0x3e47c5c2,0x3f226799,0x3ee63375,0x3e78cfcc,0x3cc90ab0,0x3f0e39da,0x3eac7cd4,0x3dc8bd36,0xbe164083,0x3ef15aea,0x3e605c13,0xbd48fb30,0xbea09ae5,
0x3ec3ef15,0x3dc8bd36,0xbe47c5c2,0xbef15aea,0x3e94a031,0xbcc90ab0,0xbeac7cd4,0xbf1d7fd1,0x3e47c5c2,0xbe164083,0xbef15aea,0xbf3daef9,0x3dc8bd36,0xbe888e93,0xbf187fc0,0xbf584853,
0x3f800000,0x3f800000,0x3f800000,0x3f800000,0x3f7b14be,0x3f79c79d,0x3f7853f8,0x3f76ba07,0x3f6c835e,0x3f676bd8,0x3f61c598,0x3f5b941a,0x3f54db31,0x3f49d112,0x3f3daef9,0x3f3085bb,
0x3f3504f3,0x3f226799,0x3f0e39da,0x3ef15aea,0x3f0e39da,0x3ee63375,0x3eac7cd4,0x3e605c13,0x3ec3ef15,0x3e78cfcc,0x3dc8bd36,0xbd48fb30,0x3e47c5c2,0x3cc90ab0,0xbe164083,0xbea09ae5,
0x248d3132,0xbe47c5c2,0xbec3ef15,0xbf0e39da,0xbe47c5c2,0xbecf7bca,0xbf187fc0,0xbf41d870,0xbec3ef15,0xbf187fc0,0xbf45e403,0xbf676bd8,0xbf0e39da,0xbf41d870,0xbf676bd8,0xbf7c3b28,
0xbf3504f3,0xbf61c598,0xbf7b14be,0xbf7ec46d,0xbf54db31,0xbf76ba07,0xbf7fb10f,0xbf6ed89e,0xbf6c835e,0xbf7fb10f,0xbf74fa0b,0xbf4d9f02,0xbf7b14be,0xbf7c3b28,0xbf5b941a,0xbf1d7fd1,
0x3f800000,0x3f800000,0x3f800000,0x3f800000,0x3f74fa0b,0x3f731447,0x3f710908,0x3f6ed89e,0x3f54db31,0x3f4d9f02,0x3f45e403,0x3f3daef9,0x3f226799,0x3f13682a,0x3f039c3d,0x3ee63375,
0x3ec3ef15,0x3e94a031,0x3e47c5c2,0x3dc8bd36,0x3dc8bd36,0xbcc90ab0,0xbe164083,0xbe888e93,0xbe47c5c2,0xbeac7cd4,0xbef15aea,0xbf187fc0,0xbef15aea,0xbf1d7fd1,0xbf3daef9,0xbf584853,
0xbf3504f3,0xbf54db31,0xbf6c835e,0xbf7b14be,0xbf61c598,0xbf76ba07,0xbf7fb10f,0xbf7c3b28,0xbf7b14be,0xbf7fb10f,0xbf74fa0b,0xbf5b941a,0xbf7ec46d,0xbf6ed89e,0xbf4d9f02,0xbf1d7fd1,
0xbf6c835e,0xbf45e403,0xbf0e39da,0xbe94a031,0xbf45e403,0xbf08f59b,0xbe78cfcc,0x3d96a905,0xbf0e39da,0xbe78cfcc,0x3dc8bd36,0x3edae880,0xbe94a031,0x3d96a905,0x3edae880,0x3f396842,
0,0,0,0,0,0xbcc90ab0,0xbd48fb30,0xbd96a905,0,0xbd48fb30,0xbdc8bd36,0xbe164083,0,0xbd96a905,0xbe164083,0xbe605c13,
0,0xbdc8bd36,0xbe47c5c2,0xbe94a031,0,0xbdfab273,0xbe78cfcc,0xbeb8442a,0,0xbe164083,0xbe94a031,0xbedae880,0,0xbe2f10a2,0xbeac7cd4,0xbefc5d27,
0,0xbe47c5c2,0xbec3ef15,0xbf0e39da,0,0xbe605c13,0xbedae880,0xbf1d7fd1,0,0xbe78cfcc,0xbef15aea,0xbf2beb4a,0,0xbe888e93,0xbf039c3d,0xbf396842,
0,0xbe94a031,0xbf0e39da,0xbf45e403,0,0xbea09ae5,0xbf187fc0,0xbf514d3d,0,0xbeac7cd4,0xbf226799,0xbf5b941a,0,0xbeb8442a,0xbf2beb4a,0xbf64aa59,
0,0,0,0,0xbdc8bd36,0xbdfab273,0xbe164083,0xbe2f10a2,0xbe47c5c2,0xbe78cfcc,0xbe94a031,0xbeac7cd4,0xbe94a031,0xbeb8442a,0xbedae880,0xbefc5d27,
0xbec3ef15,0xbef15aea,0xbf0e39da,0xbf226799,0xbef15aea,0xbf13682a,0xbf2beb4a,0xbf41d870,0xbf0e39da,0xbf2beb4a,0xbf45e403,0xbf5b941a,0xbf226799,0xbf41d870,0xbf5b941a,0xbf6ed89e,
0xbf3504f3,0xbf54db31,0xbf6c835e,0xbf7b14be,0xbf45e403,0xbf64aa59,0xbf7853f8,0xbf7fec43,0xbf54db31,0xbf710908,0xbf7ec46d,0xbf7d3aac,0xbf61c598,0xbf79c79d,0xbf7fb10f,0xbf731447,
0xbf6c835e,0xbf7ec46d,0xbf7b14be,0xbf61c598,0xbf74fa0b,0xbf7fec43,0xbf710908,0xbf49d112,0xbf7b14be,0xbf7d3aac,0xbf61c598,0xbf2beb4a,0xbf7ec46d,0xbf76ba07,0xbf4d9f02,0xbf08f59b,
0,0,0,0,0xbe47c5c2,0xbe605c13,0xbe78cfcc,0xbe888e93,0xbec3ef15,0xbedae880,0xbef15aea,0xbf039c3d,0xbf0e39da,0xbf1d7fd1,0xbf2beb4a,0xbf396842,
0xbf3504f3,0xbf45e403,0xbf54db31,0xbf61c598,0xbf54db31,0xbf64aa59,0xbf710908,0xbf79c79d,0xbf6c835e,0xbf7853f8,0xbf7ec46d,0xbf7fb10f,0xbf7b14be,0xbf7fec43,0xbf7d3aac,0xbf731447,
0xbf800000,0xbf7b14be,0xbf6c835e,0xbf54db31,0xbf7b14be,0xbf6a09a7,0xbf4d9f02,0xbf273656,0xbf6c835e,0xbf4d9f02,0xbf226799,0xbedae880,0xbf54db31,0xbf273656,0xbedae880,0xbe2f10a2,
0xbf3504f3,0xbef15aea,0xbe47c5c2,0x3dc8bd36,0xbf0e39da,0xbe888e93,0x3d48fb30,0x3eb8442a,0xbec3ef15,0xbd48fb30,0x3e94a031,0x3f187fc0,0xbe47c5c2,0x3e2f10a2,0x3f039c3d,0x3f49d112,
0,0,0,0,0xbe94a031,0xbea09ae5,0xbeac7cd4,0xbeb8442a,0xbf0e39da,0xbf187fc0,0xbf226799,0xbf2beb4a,0xbf45e403,0xbf514d3d,0xbf5b941a,0xbf64aa59,
0xbf6c835e,0xbf74fa0b,0xbf7b14be,0xbf7ec46d,0xbf7ec46d,0xbf7fec43,0xbf7d3aac,0xbf76ba07,0xbf7b14be,0xbf710908,0xbf61c598,0xbf4d9f02,0xbf61c598,0xbf49d112,0xbf2beb4a,0xbf08f59b,
0xbf3504f3,0xbf0e39da,0xbec3ef15,0xbe47c5c2,0xbef15aea,0xbe888e93,0xbd48fb30,0x3e2f10a2,0xbe47c5c2,0x3d48fb30,0x3e94a031,0x3f039c3d,0x3dc8bd36,0x3eb8442a,0x3f187fc0,0x3f49d112,
0x3ec3ef15,0x3f226799,0x3f54db31,0x3f74fa0b,0x3f226799,0x3f584853,0x3f7853f8,0x3f7f4e6d,0x3f54db31,0x3f7853f8,0x3f7ec46d,0x3f676bd8,0x3f74fa0b,0x3f7f4e6d,0x3f676bd8,0x3f3085bb,
0x20000000,0x204001c2,0x7c000fe,0,0x20100b98,0x20500d5a,0x7d00c96,0,0x20200482,0x206002c0,0x7e00384,0,0x2030101a,0x20700e58,0x7f00f1c,0,
0x24400240,128,0x8000100,0,0x24500dd8,1051672,0x8100c98,0,0x24600242,2098178,0x8200382,0,0x24700dda,3149722,0x8300f1a,0,
4194306,0x8400082,0x10400102,0,5245850,0x8500c1a,0x10500c9a,0,6292608,0x8600400,0x10600380,0,7344152,0x8700f98,0x10700f18,0,
0x8800004,0x10800084,0x18800104,0,0x8900b9c,0x10900c1c,0x18900c9c,0,0x8a0047e,0x10a003fe,0x18a0037e,0,0x8b01016,0x10b00f96,0x18b00f16,0,
0x10c00006,0x18c00086,0x1f40013a,0,0x10d00b9e,0x18d00c1e,0x1f500cd2,0,0x10e0047c,0x18e003fc,0x1f600348,0,0x10f01014,0x18f00f94,0x1f700ee0,0,
0x19000008,0x1f0001b8,0x17000138,0,0x19100ba0,0x1f100d50,0x17100cd0,0,0x1920047a,0x1f2002ca,0x1720034a,0,0x19301012,0x1f300e62,0x17300ee2,0,
0x1ec00236,0x16c001b6,0xec00136,0,0x1ed00dce,0x16d00d4e,0xed00cce,0,0x1ee0024c,0x16e002cc,0xee0034c,0,0x1ef00de4,0x16f00e64,0xef00ee4,0,
0x16800234,0xe8001b4,0x6800134,0,0x16900dcc,0xe900d4c,0x6900ccc,0,0x16a0024e,0xea002ce,0x6a0034e,0,0x16b00de6,0xeb00e66,0x6b00ee6,0,
0xe400232,0x64001b2,0x21c0010e,0,0xe500dca,0x6500d4a,0x21d00ca6,0,0xe600250,0x66002d0,0x21e00374,0,0xe700de8,0x6700e68,0x21f00f0c,0,
0x6000230,0x22000090,0x2000110,0,0x6100dc8,0x22100c28,0x2100ca8,0,0x6200252,0x222003f2,0x2200372,0,0x6300dea,0x22300f8a,0x2300f0a,0,
0x22400012,0x2400092,0xa400112,0,0x22500baa,0x2500c2a,0xa500caa,0,0x22600470,0x26003f0,0xa600370,0,0x22701008,0x2700f88,0xa700f08,0,
0x2800014,0xa800094,0x12800114,0,0x2900bac,0xa900c2c,0x12900cac,0,0x2a0046e,0xaa003ee,0x12a0036e,0,0x2b01006,0xab00f86,0x12b00f06,0,
0xac00016,0x12c00096,0x1ac00116,0,0xad00bae,0x12d00c2e,0x1ad00cae,0,0xae0046c,0x12e003ec,0x1ae0036c,0,0xaf01004,0x12f00f84,0x1af00f04,0,
0x13000018,0x1b000098,0x1d000128,0,0x13100bb0,0x1b100c30,0x1d100cc0,0,0x1320046a,0x1b2003ea,0x1d20035a,0,0x13301002,0x1b300f82,0x1d300ef2,0,
0x1b40001a,0x1cc001a6,0x14c00126,0,0x1b500bb2,0x1cd00d3e,0x14d00cbe,0,0x1b600468,0x1ce002dc,0x14e0035c,0,0x1b701000,0x1cf00e74,0x14f00ef4,0,
0x1c800224,0x148001a4,0xc800124,0,0x1c900dbc,0x14900d3c,0xc900cbc,0,0x1ca0025e,0x14a002de,0xca0035e,0,0x1cb00df6,0x14b00e76,0xcb00ef6,0,
0x14400222,0xc4001a2,0x4400122,0,0x14500dba,0xc500d3a,0x4500cba,0,0x14600260,0xc6002e0,0x4600360,0,0x14700df8,0xc700e78,0x4700ef8,0,
0xc000220,0x40001a0,0x24000120,0,0xc100db8,0x4100d38,0x24100cb8,0,0xc200262,0x42002e2,0x24200362,0,0xc300dfa,0x4300e7a,0x24300efa,0,
0x3c0021e,0x23c0019e,0x24000120,0,0x3d00db6,0x23d00d36,0x24100cb8,0,0x3e00264,0x23e002e4,0x24200362,0,0x3f00dfc,0x23f00e7c,0x24300efa,0,
0x2380021c,0x48000a4,0x24000120,0,0x23900db4,0x4900c3c,0x24100cb8,0,0x23a00266,0x4a003de,0x24200362,0,0x23b00dfe,0x4b00f76,0x24300efa,0,
0x4c00026,0xcc000a6,0x24000120,0,0x4d00bbe,0xcd00c3e,0x24100cb8,0,0x4e0045c,0xce003dc,0x24200362,0,0x4f00ff4,0xcf00f74,0x24300efa,0,
0xd000028,0x150000a8,0x24000120,0,0xd100bc0,0x15100c40,0x24100cb8,0,0xd20045a,0x152003da,0x24200362,0,0xd300ff2,0x15300f72,0x24300efa,0,
0x1540002a,0x1d4000aa,0x24000120,0,0x15500bc2,0x1d500c42,0x24100cb8,0,0x15600458,0x1d6003d8,0x24200362,0,0x15700ff0,0x1d700f70,0x24300efa,0,
0x1d80002c,0x1a800194,0x24000120,0,0x1d900bc4,0x1a900d2c,0x24100cb8,0,0x1da00456,0x1aa002ee,0x24200362,0,0x1db00fee,0x1ab00e86,0x24300efa,0,
0x1a400212,0x12400192,0x24000120,0,0x1a500daa,0x12500d2a,0x24100cb8,0,0x1a600270,0x126002f0,0x24200362,0,0x1a700e08,0x12700e88,0x24300efa,0,
0x12000210,0xa000190,0x24000120,0,0x12100da8,0xa100d28,0x24100cb8,0,0x12200272,0xa2002f2,0x24200362,0,0x12300e0a,0xa300e8a,0x24300efa,0,
0x9c0020e,0x1c0018e,0x24000120,0,0x9d00da6,0x1d00d26,0x24100cb8,0,0x9e00274,0x1e002f4,0x24200362,0,0x9f00e0c,0x1f00e8c,0x24300efa,0,
0x180020c,0x2180018c,0x24000120,0,0x1900da4,0x21900d24,0x24100cb8,0,0x1a00276,0x21a002f6,0x24200362,0,0x1b00e0e,0x21b00e8e,0x24300efa,0,
0x2140020a,0x6c000b6,0x24000120,0,0x21500da2,0x6d00c4e,0x24100cb8,0,0x21600278,0x6e003cc,0x24200362,0,0x21700e10,0x6f00f64,0x24300efa,0,
0x7000038,0xf0000b8,0x24000120,0,0x7100bd0,0xf100c50,0x24100cb8,0,0x720044a,0xf2003ca,0x24200362,0,0x7300fe2,0xf300f62,0x24300efa,0,
0xf40003a,0x174000ba,0x24000120,0,0xf500bd2,0x17500c52,0x24100cb8,0,0xf600448,0x176003c8,0x24200362,0,0xf700fe0,0x17700f60,0x24300efa,0,
0x1780003c,0x1f8000bc,0x24000120,0,0x17900bd4,0x1f900c54,0x24100cb8,0,0x17a00446,0x1fa003c6,0x24200362,0,0x17b00fde,0x1fb00f5e,0x24300efa,0,
0x1fc0003e,0x18400182,0x24000120,0,0x1fd00bd6,0x18500d1a,0x24100cb8,0,0x1fe00444,0x18600300,0x24200362,0,0x1ff00fdc,0x18700e98,0x24300efa,0,
0x18000200,0x10000180,0x24000120,0,0x18100d98,0x10100d18,0x24100cb8,0,0x18200282,0x10200302,0x24200362,0,0x18300e1a,0x10300e9a,0x24300efa,0,
0x17c001fe,0xfc0017e,0x24000120,0,0x17d00d96,0xfd00d16,0x24100cb8,0,0x17e00284,0xfe00304,0x24200362,0,0x17f00e1c,0xff00e9c,0x24300efa,0,
0xf8001fc,0x780017c,0x24000120,0,0xf900d94,0x7900d14,0x24100cb8,0,0xfa00286,0x7a00306,0x24200362,0,0xfb00e1e,0x7b00e9e,0x24300efa,0,
0x74001fa,0x20c000c6,0x24000120,0,0x7500d92,0x20d00c5e,0x24100cb8,0,0x7600288,0x20e003bc,0x24200362,0,0x7700e20,0x20f00f54,0x24300efa,0,
0x21000048,0x10000c8,0x24000120,0,0x21100be0,0x1100c60,0x24100cb8,0,0x2120043a,0x12003ba,0x24200362,0,0x21300fd2,0x1300f52,0x24300efa,0,
0x140004a,0x94000ca,0x24000120,0,0x1500be2,0x9500c62,0x24100cb8,0,0x1600438,0x96003b8,0x24200362,0,0x1700fd0,0x9700f50,0x24300efa,0,
0x980004c,0x118000cc,0x24000120,0,0x9900be4,0x11900c64,0x24100cb8,0,0x9a00436,0x11a003b6,0x24200362,0,0x9b00fce,0x11b00f4e,0x24300efa,0,
0x11c0004e,0x19c000ce,0x24000120,0,0x11d00be6,0x19d00c66,0x24100cb8,0,0x11e00434,0x19e003b4,0x24200362,0,0x11f00fcc,0x19f00f4c,0x24300efa,0,
0x1a000050,0x1e000170,0x24000120,0,0x1a100be8,0x1e100d08,0x24100cb8,0,0x1a200432,0x1e200312,0x24200362,0,0x1a300fca,0x1e300eaa,0x24300efa,0,
0x1dc001ee,0x15c0016e,0x24000120,0,0x1dd00d86,0x15d00d06,0x24100cb8,0,0x1de00294,0x15e00314,0x24200362,0,0x1df00e2c,0x15f00eac,0x24300efa,0,
0x158001ec,0xd80016c,0x24000120,0,0x15900d84,0xd900d04,0x24100cb8,0,0x15a00296,0xda00316,0x24200362,0,0x15b00e2e,0xdb00eae,0x24300efa,0,
0xd4001ea,0x540016a,0x24000120,0,0xd500d82,0x5500d02,0x24100cb8,0,0xd600298,0x5600318,0x24200362,0,0xd700e30,0x5700eb0,0x24300efa,0,
0x50001e8,0x230000d8,0x24000120,0,0x5100d80,0x23100c70,0x24100cb8,0,0x520029a,0x232003aa,0x24200362,0,0x5300e32,0x23300f42,0x24300efa,0,
0x2340005a,0x34000da,0x24000120,0,0x23500bf2,0x3500c72,0x24100cb8,0,0x23600428,0x36003a8,0x24200362,0,0x23700fc0,0x3700f40,0x24300efa,0,
0x380005c,0xb8000dc,0x24000120,0,0x3900bf4,0xb900c74,0x24100cb8,0,0x3a00426,0xba003a6,0x24200362,0,0x3b00fbe,0xbb00f3e,0x24300efa,0,
0xbc0005e,0x13c000de,0x24000120,0,0xbd00bf6,0x13d00c76,0x24100cb8,0,0xbe00424,0x13e003a4,0x24200362,0,0xbf00fbc,0x13f00f3c,0x24300efa,0,
0x14000060,0x1c0000e0,0x24000120,0,0x14100bf8,0x1c100c78,0x24100cb8,0,0x14200422,0x1c2003a2,0x24200362,0,0x14300fba,0x1c300f3a,0x24300efa,0,
0x1c400062,0x1bc0015e,0x24000120,0,0x1c500bfa,0x1bd00cf6,0x24100cb8,0,0x1c600420,0x1be00324,0x24200362,0,0x1c700fb8,0x1bf00ebc,0x24300efa,0,
0x1b8001dc,0x1380015c,0x24000120,0,0x1b900d74,0x13900cf4,0x24100cb8,0,0x1ba002a6,0x13a00326,0x24200362,0,0x1bb00e3e,0x13b00ebe,0x24300efa,0,
0x134001da,0xb40015a,0x24000120,0,0x13500d72,0xb500cf2,0x24100cb8,0,0x136002a8,0xb600328,0x24200362,0,0x13700e40,0xb700ec0,0x24300efa,0,
0xb0001d8,0x3000158,0x24000120,0,0xb100d70,0x3100cf0,0x24100cb8,0,0xb2002aa,0x320032a,0x24200362,0,0xb300e42,0x3300ec2,0x24300efa,0,
0x2c001d6,0x22c00156,0x24000120,0,0x2d00d6e,0x22d00cee,0x24100cb8,0,0x2e002ac,0x22e0032c,0x24200362,0,0x2f00e44,0x22f00ec4,0x24300efa,0,
0x228001d4,0x58000ec,0x24000120,0,0x22900d6c,0x5900c84,0x24100cb8,0,0x22a002ae,0x5a00396,0x24200362,0,0x22b00e46,0x5b00f2e,0x24300efa,0,
0x5c0006e,0xdc000ee,0x24000120,0,0x5d00c06,0xdd00c86,0x24100cb8,0,0x5e00414,0xde00394,0x24200362,0,0x5f00fac,0xdf00f2c,0x24300efa,0,
0xe000070,0x160000f0,0x24000120,0,0xe100c08,0x16100c88,0x24100cb8,0,0xe200412,0x16200392,0x24200362,0,0xe300faa,0x16300f2a,0x24300efa,0,
0x16400072,0x1e4000f2,0x24000120,0,0x16500c0a,0x1e500c8a,0x24100cb8,0,0x16600410,0x1e600390,0x24200362,0,0x16700fa8,0x1e700f28,0x24300efa,0,
0x1e800074,0x1980014c,0x24000120,0,0x1e900c0c,0x19900ce4,0x24100cb8,0,0x1ea0040e,0x19a00336,0x24200362,0,0x1eb00fa6,0x19b00ece,0x24300efa,0,
0x194001ca,0x1140014a,0x24000120,0,0x19500d62,0x11500ce2,0x24100cb8,0,0x196002b8,0x11600338,0x24200362,0,0x19700e50,0x11700ed0,0x24300efa,0,
0x110001c8,0x9000148,0x24000120,0,0x11100d60,0x9100ce0,0x24100cb8,0,0x112002ba,0x920033a,0x24200362,0,0x11300e52,0x9300ed2,0x24300efa,0,
0x8c001c6,0xc00146,0x24000120,0,0x8d00d5e,0xd00cde,0x24100cb8,0,0x8e002bc,0xe0033c,0x24200362,0,0x8f00e54,0xf00ed4,0x24300efa,0,
8389060,0x20800144,0x24000120,0,9440604,0x20900cdc,0x24100cb8,0,0xa002be,0x20a0033e,0x24200362,0,0xb00e56,0x20b00ed6,0x24300efa,0,
0,512,1536,0,16,528,1552,16,32,544,1568,32,48,560,1584,48,
64,576,1600,64,80,592,1616,80,96,608,1632,96,112,624,1648,112,
128,640,1664,128,144,656,1680,144,160,672,1696,160,176,688,1712,176,
192,704,1728,192,208,720,1744,208,224,736,1760,224,240,752,1776,240,
1536,5120,6144,4608,1552,5136,6160,4624,1568,5152,6176,4640,1584,5168,6192,4656,
1600,5184,6208,4672,1616,5200,6224,4688,1632,5216,6240,4704,1648,5232,6256,4720,
1664,5248,6272,4736,1680,5264,6288,4752,1696,5280,6304,4768,1712,5296,6320,4784,
1728,5312,6336,4800,1744,5328,6352,4816,1760,5344,6368,4832,1776,5360,6384,4848,
3072,4096,3072,0,3088,4592,3568,496,3104,4576,3552,480,3120,4560,3536,464,
3136,4544,3520,448,3152,4528,3504,432,3168,4512,3488,416,3184,4496,3472,400,
3200,4480,3456,384,3216,4464,3440,368,3232,4448,3424,352,3248,4432,3408,336,
3264,4416,3392,320,3280,4400,3376,304,3296,4384,3360,288,3312,4368,3344,272,
3072,8704,7680,4608,3088,9200,8176,5104,3104,9184,8160,5088,3120,9168,8144,5072,
3136,9152,8128,5056,3152,9136,8112,5040,3168,9120,8096,5024,3184,9104,8080,5008,
3200,9088,8064,4992,3216,9072,8048,4976,3232,9056,8032,4960,3248,9040,8016,4944,
3264,9024,8000,4928,3280,9008,7984,4912,3296,8992,7968,4896,3312,8976,7952,4880,
8,768,1792,256,24,784,1808,272,40,800,1824,288,56,816,1840,304,
72,832,1856,320,88,848,1872,336,104,864,1888,352,120,880,1904,368,
136,896,1920,384,152,912,1936,400,168,928,1952,416,184,944,1968,432,
200,960,1984,448,216,976,2000,464,232,992,2016,480,248,1008,2032,496,
1544,5376,6400,4864,1560,5392,6416,4880,1576,5408,6432,4896,1592,5424,6448,4912,
1608,5440,6464,4928,1624,5456,6480,4944,1640,5472,6496,4960,1656,5488,6512,4976,
1672,5504,6528,4992,1688,5520,6544,5008,1704,5536,6560,5024,1720,5552,6576,5040,
1736,5568,6592,5056,1752,5584,6608,5072,1768,5600,6624,5088,1784,5616,6640,5104,
3080,4352,3328,256,3096,4336,3312,240,3112,4320,3296,224,3128,4304,3280,208,
3144,4288,3264,192,3160,4272,3248,176,3176,4256,3232,160,3192,4240,3216,144,
3208,4224,3200,128,3224,4208,3184,112,3240,4192,3168,96,3256,4176,3152,80,
3272,4160,3136,64,3288,4144,3120,48,3304,4128,3104,32,3320,4112,3088,16,
3080,8960,7936,4864,3096,8944,7920,4848,3112,8928,7904,4832,3128,8912,7888,4816,
3144,8896,7872,4800,3160,8880,7856,4784,3176,8864,7840,4768,3192,8848,7824,4752,
3208,8832,7808,4736,3224,8816,7792,4720,3240,8800,7776,4704,3256,8784,7760,4688,
3272,8768,7744,4672,3288,8752,7728,4656,3304,8736,7712,4640,3320,8720,7696,4624,
4608,1024,2048,0,4624,1040,2064,16,4640,1056,2080,32,4656,1072,2096,48,
4672,1088,2112,64,4688,1104,2128,80,4704,1120,2144,96,4720,1136,2160,112,
4736,1152,2176,128,4752,1168,2192,144,4768,1184,2208,160,4784,1200,2224,176,
4800,1216,2240,192,4816,1232,2256,208,4832,1248,2272,224,4848,1264,2288,240,
6144,5632,6656,4608,6160,5648,6672,4624,6176,5664,6688,4640,6192,5680,6704,4656,
6208,5696,6720,4672,6224,5712,6736,4688,6240,5728,6752,4704,6256,5744,6768,4720,
6272,5760,6784,4736,6288,5776,6800,4752,6304,5792,6816,4768,6320,5808,6832,4784,
6336,5824,6848,4800,6352,5840,6864,4816,6368,5856,6880,4832,6384,5872,6896,4848,
7680,3584,2560,0,7696,4080,3056,496,7712,4064,3040,480,7728,4048,3024,464,
7744,4032,3008,448,7760,4016,2992,432,7776,4000,2976,416,7792,3984,2960,400,
7808,3968,2944,384,7824,3952,2928,368,7840,3936,2912,352,7856,3920,2896,336,
7872,3904,2880,320,7888,3888,2864,304,7904,3872,2848,288,7920,3856,2832,272,
7680,8192,7168,4608,7696,8688,7664,5104,7712,8672,7648,5088,7728,8656,7632,5072,
7744,8640,7616,5056,7760,8624,7600,5040,7776,8608,7584,5024,7792,8592,7568,5008,
7808,8576,7552,4992,7824,8560,7536,4976,7840,8544,7520,4960,7856,8528,7504,4944,
7872,8512,7488,4928,7888,8496,7472,4912,7904,8480,7456,4896,7920,8464,7440,4880,
4616,1280,2304,256,4632,1296,2320,272,4648,1312,2336,288,4664,1328,2352,304,
4680,1344,2368,320,4696,1360,2384,336,4712,1376,2400,352,4728,1392,2416,368,
4744,1408,2432,384,4760,1424,2448,400,4776,1440,2464,416,4792,1456,2480,432,
4808,1472,2496,448,4824,1488,2512,464,4840,1504,2528,480,4856,1520,2544,496,
6152,5888,6912,4864,6168,5904,6928,4880,6184,5920,6944,4896,6200,5936,6960,4912,
6216,5952,6976,4928,6232,5968,6992,4944,6248,5984,7008,4960,6264,6000,7024,4976,
6280,6016,7040,4992,6296,6032,7056,5008,6312,6048,7072,5024,6328,6064,7088,5040,
6344,6080,7104,5056,6360,6096,7120,5072,6376,6112,7136,5088,6392,6128,7152,5104,
7688,3840,2816,256,7704,3824,2800,240,7720,3808,2784,224,7736,3792,2768,208,
7752,3776,2752,192,7768,3760,2736,176,7784,3744,2720,160,7800,3728,2704,144,
7816,3712,2688,128,7832,3696,2672,112,7848,3680,2656,96,7864,3664,2640,80,
7880,3648,2624,64,7896,3632,2608,48,7912,3616,2592,32,7928,3600,2576,16,
7688,8448,7424,4864,7704,8432,7408,4848,7720,8416,7392,4832,7736,8400,7376,4816,
7752,8384,7360,4800,7768,8368,7344,4784,7784,8352,7328,4768,7800,8336,7312,4752,
7816,8320,7296,4736,7832,8304,7280,4720,7848,8288,7264,4704,7864,8272,7248,4688,
7880,8256,7232,4672,7896,8240,7216,4656,7912,8224,7200,4640,7928,8208,7184,4624,
0,0,16384,29640,16,64,16448,29576,32,128,16512,29512,48,192,16576,29448,
64,272,16656,29400,80,336,16720,29336,96,400,16784,29272,112,464,16848,29208,
128,544,16928,29160,144,608,16992,29096,160,672,17056,29032,176,736,17120,28968,
192,816,17200,28920,208,880,17264,28856,224,944,17328,28792,240,1008,17392,28728,
272,4096,20480,25544,256,4160,20544,25480,304,4224,20608,25416,288,4288,20672,25352,
336,4368,20752,25304,320,4432,20816,25240,368,4496,20880,25176,352,4560,20944,25112,
400,4640,21024,25064,384,4704,21088,25000,432,4768,21152,24936,416,4832,21216,24872,
464,4912,21296,24824,448,4976,21360,24760,496,5040,21424,24696,480,5104,21488,24632,
544,8192,24576,21448,560,8256,24640,21384,512,8320,24704,21320,528,8384,24768,21256,
608,8464,24848,21208,624,8528,24912,21144,576,8592,24976,21080,592,8656,25040,21016,
672,8736,25120,20968,688,8800,25184,20904,640,8864,25248,20840,656,8928,25312,20776,
736,9008,25392,20728,752,9072,25456,20664,704,9136,25520,20600,720,9200,25584,20536,
816,12288,28672,17352,800,12352,28736,17288,784,12416,28800,17224,768,12480,28864,17160,
880,12560,28944,17112,864,12624,29008,17048,848,12688,29072,16984,832,12752,29136,16920,
944,12832,29216,16872,928,12896,29280,16808,912,12960,29344,16744,896,13024,29408,16680,
1008,13104,29488,16632,992,13168,29552,16568,976,13232,29616,16504,960,13296,29680,16440,
1024,16,16392,29632,1040,80,16456,29568,1056,144,16520,29504,1072,208,16584,29440,
1088,256,16664,29392,1104,320,16728,29328,1120,384,16792,29264,1136,448,16856,29200,
1152,560,16936,29152,1168,624,17000,29088,1184,688,17064,29024,1200,752,17128,28960,
1216,800,17208,28912,1232,864,17272,28848,1248,928,17336,28784,1264,992,17400,28720,
1296,4112,20488,25536,1280,4176,20552,25472,1328,4240,20616,25408,1312,4304,20680,25344,
1360,4352,20760,25296,1344,4416,20824,25232,1392,4480,20888,25168,1376,4544,20952,25104,
1424,4656,21032,25056,1408,4720,21096,24992,1456,4784,21160,24928,1440,4848,21224,24864,
1488,4896,21304,24816,1472,4960,21368,24752,1520,5024,21432,24688,1504,5088,21496,24624,
1568,8208,24584,21440,1584,8272,24648,21376,1536,8336,24712,21312,1552,8400,24776,21248,
1632,8448,24856,21200,1648,8512,24920,21136,1600,8576,24984,21072,1616,8640,25048,21008,
1696,8752,25128,20960,1712,8816,25192,20896,1664,8880,25256,20832,1680,8944,25320,20768,
1760,8992,25400,20720,1776,9056,25464,20656,1728,9120,25528,20592,1744,9184,25592,20528,
1840,12304,28680,17344,1824,12368,28744,17280,1808,12432,28808,17216,1792,12496,28872,17152,
1904,12544,28952,17104,1888,12608,29016,17040,1872,12672,29080,16976,1856,12736,29144,16912,
1968,12848,29224,16864,1952,12912,29288,16800,1936,12976,29352,16736,1920,13040,29416,16672,
2032,13088,29496,16624,2016,13152,29560,16560,2000,13216,29624,16496,1984,13280,29688,16432,
2048,32,16400,29656,2064,96,16464,29592,2080,160,16528,29528,2096,224,16592,29464,
2112,304,16640,29384,2128,368,16704,29320,2144,432,16768,29256,2160,496,16832,29192,
2176,512,16944,29176,2192,576,17008,29112,2208,640,17072,29048,2224,704,17136,28984,
2240,784,17184,28904,2256,848,17248,28840,2272,912,17312,28776,2288,976,17376,28712,
2320,4128,20496,25560,2304,4192,20560,25496,2352,4256,20624,25432,2336,4320,20688,25368,
2384,4400,20736,25288,2368,4464,20800,25224,2416,4528,20864,25160,2400,4592,20928,25096,
2448,4608,21040,25080,2432,4672,21104,25016,2480,4736,21168,24952,2464,4800,21232,24888,
2512,4880,21280,24808,2496,4944,21344,24744,2544,5008,21408,24680,2528,5072,21472,24616,
2592,8224,24592,21464,2608,8288,24656,21400,2560,8352,24720,21336,2576,8416,24784,21272,
2656,8496,24832,21192,2672,8560,24896,21128,2624,8624,24960,21064,2640,8688,25024,21000,
2720,8704,25136,20984,2736,8768,25200,20920,2688,8832,25264,20856,2704,8896,25328,20792,
2784,8976,25376,20712,2800,9040,25440,20648,2752,9104,25504,20584,2768,9168,25568,20520,
2864,12320,28688,17368,2848,12384,28752,17304,2832,12448,28816,17240,2816,12512,28880,17176,
2928,12592,28928,17096,2912,12656,28992,17032,2896,12720,29056,16968,2880,12784,29120,16904,
2992,12800,29232,16888,2976,12864,29296,16824,2960,12928,29360,16760,2944,12992,29424,16696,
3056,13072,29472,16616,3040,13136,29536,16552,3024,13200,29600,16488,3008,13264,29664,16424,
3072,48,16408,29648,3088,112,16472,29584,3104,176,16536,29520,3120,240,16600,29456,
3136,288,16648,29376,3152,352,16712,29312,3168,416,16776,29248,3184,480,16840,29184,
3200,528,16952,29168,3216,592,17016,29104,3232,656,17080,29040,3248,720,17144,28976,
3264,768,17192,28896,3280,832,17256,28832,3296,896,17320,28768,3312,960,17384,28704,
3344,4144,20504,25552,3328,4208,20568,25488,3376,4272,20632,25424,3360,4336,20696,25360,
3408,4384,20744,25280,3392,4448,20808,25216,3440,4512,20872,25152,3424,4576,20936,25088,
3472,4624,21048,25072,3456,4688,21112,25008,3504,4752,21176,24944,3488,4816,21240,24880,
3536,4864,21288,24800,3520,4928,21352,24736,3568,4992,21416,24672,3552,5056,21480,24608,
3616,8240,24600,21456,3632,8304,24664,21392,3584,8368,24728,21328,3600,8432,24792,21264,
3680,8480,24840,21184,3696,8544,24904,21120,3648,8608,24968,21056,3664,8672,25032,20992,
3744,8720,25144,20976,3760,8784,25208,20912,3712,8848,25272,20848,3728,8912,25336,20784,
3808,8960,25384,20704,3824,9024,25448,20640,3776,9088,25512,20576,3792,9152,25576,20512,
3888,12336,28696,17360,3872,12400,28760,17296,3856,12464,28824,17232,3840,12528,28888,17168,
3952,12576,28936,17088,3936,12640,29000,17024,3920,12704,29064,16960,3904,12768,29128,16896,
4016,12816,29240,16880,4000,12880,29304,16816,3984,12944,29368,16752,3968,13008,29432,16688,
4080,13056,29480,16608,4064,13120,29544,16544,4048,13184,29608,16480,4032,13248,29672,16416,
4718592,0xd80090,0x1780130,0x20801c0,0x1580110,0x1e801a0,0x2880240,0x31802d0,0x2680220,0x2f802b0,0x3980350,0x42803e0,0x3780330,0x40803c0,0x4a80460,0x53804f0,
0x3780330,0x40803c0,0x4a80460,0x53804f0,0x4880440,0x51804d0,0x5b80570,0x6480600,0x5980550,0x62805e0,0x6c80680,0x7580710,0x6a80660,0x73806f0,0x7d80790,0x8680820,
0x6a80660,0x73806f0,0x7d80790,0x8680820,0x7b80770,0x8480800,0x8e808a0,0x9780930,0x8c80880,0x9580910,6815776,0xf800b0,0x8c80880,0x9580910,6815776,0xf800b0,
0x8c80880,0x9580910,6815776,0xf800b0,0x8c80880,0x9580910,6815776,0xf800b0,0x8c80880,0x9580910,6815776,0xf800b0,0x8c80880,0x9580910,6815776,0xf800b0,
0x2a80260,0x34802f0,0x3d80390,0x4780420,0x3b80370,0x4580400,0x4e804a0,0x5880530,0x4c80480,0x5680510,0x5f805b0,0x6980640,0x5d80590,0x6780620,0x70806c0,0x7a80750,
0x5d80590,0x6780620,0x70806c0,0x7a80750,0x6e806a0,0x7880730,0x81807d0,0x8b80860,0x7f807b0,0x8980840,0x92808e0,3672432,0x90808c0,1575248,0xa80060,0x14800f0,
0x90808c0,1575248,0xa80060,0x14800f0,8912960,0x12800d0,0x1b80170,0x2580200,0x1980150,0x23801e0,0x2c80280,0x3680310,0x1980150,0x23801e0,0x2c80280,0x3680310,
0x1980150,0x23801e0,0x2c80280,0x3680310,0x1980150,0x23801e0,0x2c80280,0x3680310,0x1980150,0x23801e0,0x2c80280,0x3680310,0x1980150,0x23801e0,0x2c80280,0x3680310,
0x50804c0,0x5a80560,0x63805f0,0x6d80690,0x61805d0,0x6b80670,0x7480700,0x7e807a0,0x72806e0,0x7c80780,0x8580810,0x8f808b0,0x83807f0,0x8d80890,0x9680920,7864368,
0x83807f0,0x8d80890,0x9680920,7864368,0x9480900,5767184,0xe800a0,0x1880140,0xc80080,0x1680120,0x1f801b0,0x2980250,0x1d80190,0x2780230,0x30802c0,0x3a80360,
0x1d80190,0x2780230,0x30802c0,0x3a80360,0x2e802a0,0x3880340,0x41803d0,0x4b80470,0x3f803b0,0x4980450,0x52804e0,0x5c80580,0x3f803b0,0x4980450,0x52804e0,0x5c80580,
0x3f803b0,0x4980450,0x52804e0,0x5c80580,0x3f803b0,0x4980450,0x52804e0,0x5c80580,0x3f803b0,0x4980450,0x52804e0,0x5c80580,0x3f803b0,0x4980450,0x52804e0,0x5c80580,
0x7780720,0x80807c0,0x8a80850,0x93808f0,0x8880830,0x91808d0,2623840,0xb80070,526656,9961552,0x13800e0,0x1c80180,0x11800c0,0x1a80160,0x24801f0,0x2d80290,
0x11800c0,0x1a80160,0x24801f0,0x2d80290,0x22801d0,0x2b80270,0x3580300,0x3e803a0,0x33802e0,0x3c80380,0x4680410,0x4f804b0,0x44803f0,0x4d80490,0x5780520,0x60805c0,
0x44803f0,0x4d80490,0x5780520,0x60805c0,0x5580500,0x5e805a0,0x6880630,0x71806d0,0x6680610,0x6f806b0,0x7980740,0x82807e0,0x6680610,0x6f806b0,0x7980740,0x82807e0,
0x6680610,0x6f806b0,0x7980740,0x82807e0,0x6680610,0x6f806b0,0x7980740,0x82807e0,0x6680610,0x6f806b0,0x7980740,0x82807e0,0x6680610,0x6f806b0,0x7980740,0x82807e0,
2359296,7077960,0xc400a0,0x10c00e8,0xb40090,0xfc00d8,0x1540130,0x19c0178,0x1440120,0x18c0168,0x1e401c0,0x22c0208,0x1d401b0,0x21c01f8,0x2740250,0x2bc0298,
0x1d401b0,0x21c01f8,0x2740250,0x2bc0298,0x2640240,0x2ac0288,0x30402e0,0x34c0328,0x2f402d0,0x33c0318,0x3940370,0x3dc03b8,0x3840360,0x3cc03a8,0x4240400,0x46c0448,
0x3840360,0x3cc03a8,0x4240400,0x46c0448,0x41403f0,0x45c0438,0x4b40490,0x4fc04d8,0x4a40480,0x4ec04c8,3407888,8126552,0x4a40480,0x4ec04c8,3407888,8126552,
0x4a40480,0x4ec04c8,3407888,8126552,0x4a40480,0x4ec04c8,3407888,8126552,0x4a40480,0x4ec04c8,3407888,8126552,0x4a40480,0x4ec04c8,3407888,8126552,
0x1640140,0x1bc0188,0x20401e0,0x25c0228,0x1f401d0,0x24c0218,0x2940270,0x2ec02b8,0x2840260,0x2dc02a8,0x3240300,0x37c0348,0x31402f0,0x36c0338,0x3b40390,0x40c03d8,
0x31402f0,0x36c0338,0x3b40390,0x40c03d8,0x3a40380,0x3fc03c8,0x4440420,0x49c0468,0x4340410,0x48c0458,0x4d404b0,1836280,0x4c404a0,787688,5505072,0xac0078,
0x4c404a0,787688,5505072,0xac0078,4456480,0x9c0068,0xe400c0,0x13c0108,0xd400b0,0x12c00f8,0x1740150,0x1cc0198,0xd400b0,0x12c00f8,0x1740150,0x1cc0198,
0xd400b0,0x12c00f8,0x1740150,0x1cc0198,0xd400b0,0x12c00f8,0x1740150,0x1cc0198,0xd400b0,0x12c00f8,0x1740150,0x1cc0198,0xd400b0,0x12c00f8,0x1740150,0x1cc0198,
0x2a40280,0x2fc02d8,0x3440320,0x39c0378,0x3340310,0x38c0368,0x3d403b0,0x42c0408,0x3c403a0,0x41c03f8,0x4640440,0x4bc0498,0x4540430,0x4ac0488,0x4f404d0,3932184,
0x4540430,0x4ac0488,0x4f404d0,3932184,0x4e404c0,2883592,7602256,0xcc00a8,6553664,0xbc0098,0x10400e0,0x15c0138,0xf400d0,0x14c0128,0x1940170,0x1ec01c8,
0xf400d0,0x14c0128,0x1940170,0x1ec01c8,0x1840160,0x1dc01b8,0x2240200,0x27c0258,0x21401f0,0x26c0248,0x2b40290,0x30c02e8,0x21401f0,0x26c0248,0x2b40290,0x30c02e8,
0x21401f0,0x26c0248,0x2b40290,0x30c02e8,0x21401f0,0x26c0248,0x2b40290,0x30c02e8,0x21401f0,0x26c0248,0x2b40290,0x30c02e8,0x21401f0,0x26c0248,0x2b40290,0x30c02e8,
0x3f403c0,0x43c0418,0x4940460,0x4dc04b8,0x4840450,0x4cc04a8,1311984,6029368,263392,4980776,0xa40070,0xec00c8,9699424,0xdc00b8,0x1340100,0x17c0158,
9699424,0xdc00b8,0x1340100,0x17c0158,0x12400f0,0x16c0148,0x1c40190,0x20c01e8,0x1b40180,0x1fc01d8,0x2540220,0x29c0278,0x2440210,0x28c0268,0x2e402b0,0x32c0308,
0x2440210,0x28c0268,0x2e402b0,0x32c0308,0x2d402a0,0x31c02f8,0x3740340,0x3bc0398,0x3640330,0x3ac0388,0x40403d0,0x44c0428,0x3640330,0x3ac0388,0x40403d0,0x44c0428,
0x3640330,0x3ac0388,0x40403d0,0x44c0428,0x3640330,0x3ac0388,0x40403d0,0x44c0428,0x3640330,0x3ac0388,0x40403d0,0x44c0428,0x3640330,0x3ac0388,0x40403d0,0x44c0428,
0x3eb6e48b,0x3fd246dd,0x3f441b7d,0,0x3f7d3521,0x3f816570,0x3f7ffc1a,0,0x3ebf86f5,0x3fd01e43,0xbf47a682,0,0x3caf516f,0x3ffd42ba,0xbe52b743,0,
0x3de766dd,0x3ff18992,0x3eec6a50,0,0x3f1b757f,0x3fb24541,0x3f6b6e32,0,0x3f385d20,0x3fa3d170,0xbf75c5d4,0,0x3e32d4d8,0x3fe9a565,0xbf108a14,0,
0x3b79625f,0x3fff834f,0x3db27eb6,0,0x3e9208a2,0x3fdb7dd8,0x3f3308b4,0,0x3f64214d,0x3f8def5a,0x3f7e7a7a,0,0x3ee8356c,0x3fc5f2a5,0xbf56651d,0,
0x3d3d934b,0x3ffa1366,0xbe99f61c,0,0x3d93754e,0x3ff6c8ab,0x3ebec330,0,0x3f04ddec,0x3fbd910a,0x3f60712a,0,0x3f50cc75,0x3f9799c5,0xbf7b9c62,0,
0x3e6f920c,0x3fe20dbf,0xbf248dbb,0,0x38798a45,0x3ffffe0d,0xbc32b7da,0,0x3e6165f6,0x3fe3d341,0x3f203c85,0,0x3f4b522f,0x3f9a56e8,0x3f7a8575,0,
0x3f09cad8,0x3fbb1a94,0xbf631324,0,0x3da48e6d,0x3ff5b719,0xbec91503,0,0x3d23a2c4,0x3ffae2ea,0x3e8f45c0,0,0x3edeebd7,0x3fc8450a,0x3f534aca,0,
0x3f69b029,0x3f8b27eb,0xbf7f069e,0,0x3e99ee99,0x3fd9845a,0xbf36fbaf,0,0x3bc2c30c,0x3fff3d3d,0xbddef599,0,0x3e266b8d,0x3feb328e,0x3f0be54a,0,
0x3f3304f2,0x3fa67d87,0x3f7426cb,0,0x3f209e68,0x3fafb0cc,0xbf6d9156,0,0x3dfc76af,0x3ff03895,0xbef64429,0,0x3c8c73b0,0x3ffdce31,0x3e3cce2b,0,
0x3c78e8e7,0x3ffe0e2e,0x3e31d0d4,0,0x3eb2a24e,0x3fd3576c,0x3f424cfe,0,0x3f7a6a57,0x3f82cad5,0x3f7ff067,0,0x3ec3e6e0,0x3fcf0648,0xbf4962ed,0,
0x3cc22ee0,0x3ffcf744,0xbe5da259,0,0x3ddd31cd,0x3ff22ce3,0x3ee77284,0,0x3f18e598,0x3fb38d34,0x3f6a51da,0,0x3f3b0c88,0x3fa279bc,0xbf768a20,0,
0x3e393035,0x3fe8d9f9,0xbf12d5e8,0,0x3b3ef696,0x3fffa085,0x3d9c3ac6,0,0x3e8e2650,0x3fdc766c,0x3f310700,0,0x3f615b1a,0x3f8f5273,0x3f7e28c6,0,
0x3eece711,0x3fc4c63c,0xbf57e880,0,0x3d4b3e41,0x3ff9a60e,0xbe9f4757,0,0x3d8b3f75,0x3ff74c09,0x3eb9917b,0,0x3f026d0c,0x3fbec97a,0x3f5f15e7,0,
0x3f538bcb,0x3f963a1a,0xbf7c1c5c,0,0x3e76cc0a,0x3fe1267f,0xbf26aed9,0,0x3979885f,0x3ffff834,0xbcb2b521,0,0x3e5a744e,0x3fe4b176,0x3f1e0c90,0,
0x3f48976a,0x3f9bb44b,0x3f79ee89,0,0x3f0c46be,0x3fb9dca1,0xbf6459c6,0,0x3dad712d,0x3ff528ed,0xbece34d0,0,0x3d175dfc,0x3ffb4510,0x3e89e6f1,0,
0x3eda5430,0x3fc96af4,0x3f51b3f3,0,0x3f6c78a7,0x3f89c3ac,0xbf7f4109,0,0x3e9df200,0x3fd88380,0xbf38ecd8,0,0x3beb9ce3,0x3fff1463,0xbdf52731,0,
0x3e205dfe,0x3febf440,0x3f098c78,0,0x3f305c55,0x3fa7d1d6,0x3f734c1c,0,0x3f233743,0x3fae645f,0xbf6e9811,0,0x3e03a866,0x3fef8af3,0xbefb25e8,0,
0x3e0930a3,0x3feed9ec,0xbf000000,0,0x3c5ad5ed,0x3ffe4a54,0x3e26ce12,0,0x3eae6a3a,0x3fd46572,0x3f407893,0,0x3f779fb9,0x3f843024,0x3f7fdce9,0,
0x3ec8506c,0x3fcdebe5,0xbf4b1934,0,0x3cd5fff2,0x3ffca800,0xbe6886ae,0,0x3dd33462,0x3ff2ccba,0x3ee273aa,0,0x3f1658d5,0x3fb4d395,0x3f692e5f,0,
0x3f3dbe09,0x3fa120fc,0xbf7746ea,0,0x3e3fa51e,0x3fe80b5c,0xbf151d42,0,0x3b0c514f,0x3fffb9d7,0x3d85f213,0,0x3e8a4f42,0x3fdd6c30,0x3f2effe8,0,
0x3f5e95d6,0x3f90b515,0x3f7dcf55,0,0x3ef1a117,0x3fc397ba,0xbf596550,0,0x3d595fcb,0x3ff93502,0xbea493b7,0,0x3d8343c1,0x3ff7cbc4,0x3eb45a1e,0,
0x3f000000,0x3fc00000,0x3f5db3d7,0,0x3f564c7b,0x3f94d9c2,0xbf7c94a8,0,0x3e7e1db5,0x3fe03c49,0xbf28cae3,0,0x3a0c5aed,0x3fffee75,0xbd060472,0,
0x3e539b2f,0x3fe58c9a,0x3f1bd7ca,0,0x3f45de54,0x3f9d10d6,0x3f795000,0,0x3f0ec62b,0x3fb89cea,0xbf659974,0,0x3db68d07,0x3ff49730,0xbed34e56,0,
0x3d0b915c,0x3ffba375,0x3e8483ee,0,0x3ed5c57c,0x3fca8ea1,0x3f5016b8,0,0x3f6f41be,0x3f885f21,0xbf7f73af,0,0x3ea20031,0x3fd77ff4,0xbf3ad85f,0,
0x3c0c2ada,0x3ffee7aa,0xbe05a8a8,0,0x3e1a6abe,0x3fecb2a8,0x3f072f74,0,0x3f2db625,0x3fa924ee,0x3f726a03,0,0x3f25d2f1,0x3fad1688,0xbf6f9788,0,
0x3f28715e,0x3fabc751,0xbf708fb2,0,0x3e0ed3e4,0x3fee2584,0xbf026926,0,0x3c3eaf5d,0x3ffe82a1,0x3e1bc63c,0,0x3eaa3c6d,0x3fd570e5,0x3f3e9e4b,0,
0x3f74d55c,0x3f859552,0x3f7fc1a0,0,0x3eccc376,0x3fcccf22,0xbf4cc94c,0,0x3ceac409,0x3ffc54f0,0xbe7363ee,0,0x3dc96eeb,0x3ff36911,0x3edd6dea,0,
0x3f13cf4a,0x3fb6185b,0x3f6803ca,0,0x3f40718f,0x3f9fc739,0xbf77fc2c,0,0x3e463363,0x3fe73994,0xbf176012,0,0x3ac2e826,0x3fffcf46,0x3d5f4a99,0,
0x3e868396,0x3fde5f1b,0x3f2cf37b,0,0x3f5bd196,0x3f921735,0x3f7d6e28,0,0x3ef6635a,0x3fc2672a,0xbf5adb81,0,0x3d67f77b,0x3ff8c044,0xbea9db13,0,
0x3d7704df,0x3ff847d9,0x3eaf1d44,0,0x3efb2db4,0x3fc13493,0x3f5c4b07,0,0x3f590e71,0x3f9378c7,0xbf7d0543,0,0x3e82c36a,0x3fdf4f26,0xbf2ae1c9,0,
0x3a7980c5,0x3fffe0d0,0xbd32aa3e,0,0x3e4cdacf,0x3fe664a6,0x3f199e45,0,0x3f432704,0x3f9e6c7e,0x3f78a9e0,0,0x3f11490b,0x3fb75b7b,0xbf66d223,0,
0x3dbfe1b4,0x3ff401e5,0xbed8616c,0,0x3d003d42,0x3ffbfe16,0x3e7e39c4,0,0x3ed13fdd,0x3fcbb009,0x3f4e7327,0,0x3f720b56,0x3f86fa55,0xbf7f9e8c,0,
0x3ea6190a,0x3fd679bd,0xbf3cbe35,0,0x3c247613,0x3ffeb714,0xbe10b9a6,0,0x3e1491fc,0x3fed6dc1,0x3f04ce53,0,0x3f2b1276,0x3faa76c5,0x3f718088,0,
0x3f800000,0x3f800000,0x3f800000,0,0x3ebb30ce,0x3fd133cd,0xbf45e403,0,0x3c9d6830,0x3ffd8a5f,0xbe47c5c2,0,0x3df1d344,0x3ff0e2cc,0x3ef15aea,0,
0x3f1e0875,0x3fb0fbc5,0x3f6c835e,0,0x3f35afe7,0x3fa5280c,0xbf74fa0b,0,0x3e2c933b,0x3fea6d99,0xbf0e39da,0,0x3b9dc971,0x3fff6237,0x3dc8bd36,0,
0x3e95f61a,0x3fda827a,0x3f3504f3,0,0x3f66e859,0x3f8c8bd3,0x3f7ec46d,0,0x3ee38c4c,0x3fc71ced,0xbf54db31,0,0x3d305f55,0x3ffa7d05,0xbe94a031,0,
0x3d9be50c,0x3ff641af,0x3ec3ef15,0,0x3f07528b,0x3fbc56ba,0x3f61c598,0,0x3f4e0e90,0x3f98f8b8,0xbf7b14be,0,0x3e686ff3,0x3fe2f202,0xbf226799,0,
0,0x40000000,0x248d3132,0,0x3f800000,0x3f800000,0x3f800000,0,0x3f800000,0x3f800000,0x3f800000,0,0x3f800000,0x3f800000,0x3f800000,0,
};
#define ASM_FRONT \
  "s_cmp_gt_u32 s2, 110\n" \
  "s_cbranch_scc1 .Lcol_%=\n" \
  "s_load_dwordx2 s[36:37], s[0:1], 48\n" \
  "s_load_dwordx2 s[60:61], s[0:1], 32\n" \
  "s_load_dwordx2 s[64:65], s[0:1], 40\n" \
  "s_load_dwordx2 s[48:49], s[0:1], 0\n" \
  "v_lshrrev_b32 v255, 6, v0\n" \
  "s_getpc_b64 s[80:81]\n" \
  ".Lpcn_%=:\n" \
  "v_readfirstlane_b32 s20, v255\n" \
  "s_mov_b32 s82, .Ladm_b-.Lpcn_%=\n" \
  "s_mov_b32 s83, 131072\n" \
  "s_mov_b32 m0, 162176\n" \
  "s_lshr_b32 s98, s2, 3\n" \
  "s_mul_i32 s98, s98, 20\n" \
  "v_add_u32 v254, s98, v0\n" \
  "v_lshlrev_b32 v253, 7, v254\n" \
  "s_cmp_eq_u32 s20, 0\n" \
  "s_cselect_b64 exec, 1048575, 0\n" \
  "buffer_load_dword v253, s[80:83], 0 offen lds\n" \
  "s_mov_b64 exec, -1\n" \
  "s_mov_b32 s38, 134217728\n" \
  "s_mov_b32 s39, 131072\n" \
  "s_mov_b32 s62, 1152\n" \
  "s_mov_b32 s63, 131072\n" \
  "s_mov_b32 s66, 3981312\n" \
  "s_mov_b32 s67, 131072\n" \
  "s_mov_b32 s50, 2359296\n" \
  "s_mov_b32 s51, 131072\n" \
  "s_mov_b32 s78, 26944\n" \
  "s_mov_b32 s79, 131072\n" \
  "v_and_b32 v191, 63, v0\n" \
  "v_and_b32 v254, 31, v191\n" \
  "v_lshrrev_b32 v253, 5, v191\n" \
  "v_mul_u32_u24 v252, 1179648, v253\n" \
  "v_lshl_add_u32 v251, v254, 4, v252\n" \
  "v_lshl_add_u32 v250, v254, 2, v252\n" \
  "v_lshlrev_b32 v252, 4, v191\n" \
  "s_and_b32 s84, s2, 7\n" \
  "s_lshr_b32 s85, s2, 3\n" \
  "s_lshl_b32 s98, s84, 5\n" \
  "s_add_u32 s86, s98, s85\n" \
  "s_lshl_b32 s98, s20, 8\n" \
  "s_add_u32 s98, s98, s86\n" \
  "s_mul_i32 s87, s98, 1152\n" \
  "s_mul_i32 s98, s2, 57\n" \
  "s_lshr_b32 s22, s98, 11\n" \
  "s_mul_i32 s98, s22, 36\n" \
  "s_sub_u32 s23, s2, s98\n" \
  "v_lshlrev_b32 v249, 3, v0\n" \
  "v_lshrrev_b32 v248, 2, v0\n" \
  "v_and_b32 v247, 3, v0\n" \
  "v_mul_u32_u24 v246, 6912, v248\n" \
  "v_lshl_add_u32 v245, v247, 4, v246\n" \
  "s_mul_i32 s98, s22, 1327104\n" \
  "s_lshl_b32 s100, s23, 6\n" \
  "s_add_u32 s90, s98, s100\n" \
  "s_cmp_lt_u32 s2, 108\n" \
  "s_cselect_b32 s66, s66, 0\n" \
  "s_add_u32 s32, s90, 442368\n" \
  "s_add_u32 s33, s90, 884736\n" \
  "s_waitcnt lgkmcnt(0)\n" \
  "buffer_load_dwordx2 v[2:3], off, s[36:39], 0 sc1\n" \
  "buffer_load_dwordx2 v[4:5], v249, s[60:63], 0 offen offset:0\n" \
  "buffer_load_dwordx4 v[6:9], v245, s[64:67], s90 offen offset:0\n" \
  "buffer_load_dwordx4 v[10:13], v245, s[64:67], s32 offen offset:0\n" \
  "buffer_load_dwordx4 v[14:17], v245, s[64:67], s33 offen offset:0\n" \
  "buffer_load_dwordx4 v[18:21], v251, s[48:51], s87 offen offset:0\n" \
  "buffer_load_dwordx4 v[22:25], v251, s[48:51], s87 offen offset:512\n" \
  "buffer_load_dword v247, v250, s[48:51], s87 offen offset:1024\n" \
  "s_movk_i32 s32, 22528\n" \
  "buffer_load_dwordx4 v[26:29], v252, s[76:79], s32 offen offset:0\n" \
  "s_memrealtime s[16:17]\n" \
  "s_lshr_b32 s32, s20, 1\n" \
  "s_lshl3_add_u32 s32, s32, 8\n" \
  "s_load_dwordx2 s[52:53], s[0:1], s32\n" \
  "s_load_dwordx2 s[4:5], s[0:1], 24\n" \
  "s_load_dwordx2 s[68:69], s[0:1], 56\n" \
  "s_load_dwordx2 s[72:73], s[0:1], 64\n" \
  "s_load_dwordx2 s[40:41], s[0:1], 72\n" \
  "s_load_dwordx2 s[44:45], s[0:1], 80\n" \
  "s_mov_b32 s6, 18874368\n" \
  "s_mov_b32 s7, 131072\n" \
  "s_mov_b32 s70, 9216\n" \
  "s_mov_b32 s71, 131072\n" \
  "s_mov_b32 s74, 4096\n" \
  "s_mov_b32 s75, 131072\n" \
  "s_mov_b32 s42, 18874368\n" \
  "s_mov_b32 s43, 131072\n" \
  "s_mov_b32 s46, 2359296\n" \
  "s_mov_b32 s47, 131072\n" \
  "s_mov_b32 s54, 1183744\n" \
  "s_mov_b32 s55, 131072\n" \
  "v_and_b32 v246, 15, v191\n" \
  "v_lshrrev_b32 v244, 4, v191\n" \
  "v_lshlrev_b32 v243, 4, v0\n" \
  "v_lshrrev_b32 v242, 1, v246\n" \
  "v_and_b32 v241, 1, v244\n" \
  "v_lshl_add_u32 v240, v241, 9, s86\n" \
  "v_and_b32 v241, 1, v246\n" \
  "v_and_b32 v239, 3, v242\n" \
  "v_lshl_add_u32 v238, v241, 2, v239\n" \
  "v_lshl_add_u32 v239, v238, 8, s86\n" \
  "v_lshlrev_b32 v238, 1, v244\n" \
  "v_cvt_f32_u32 v237, v242\n" \
  "v_cvt_f32_u32 v242, v240\n" \
  "v_cmp_eq_u32 vcc, v241, v253\n" \
  "v_mul_f32 v237, 3120562176, v237\n" \
  "v_mov_b32 v240, 1048576000\n" \
  "v_cndmask_b32 v240, 0, v240, vcc\n" \
  "v_lshlrev_b32 v236, 31, v241\n" \
  "v_mul_f32 v242, v237, v242\n" \
  "v_xor_b32 v236, v240, v236\n" \
  "v_mul_f32 v237, 1132462080, v237\n" \
  "v_fma_f32 v235, v237, 0, v242\n" \
  "v_cos_f32 v234, v235\n" \
  "v_sin_f32 v233, v235\n" \
  "v_mul_f32 v232, v234, v240\n" \
  "v_mul_f32 v231, v233, v236\n" \
  "v_mul_f32 v230, v233, v240\n" \
  "v_mul_f32 v229, v234, v236\n" \
  "v_cvt_pk_f16_f32 v232, v232, -v231\n" \
  "v_cvt_pk_f16_f32 v230, v230, v229\n" \
  "v_accvgpr_write_b32 a160, v232\n" \
  "v_accvgpr_write_b32 a164, v230\n" \
  "v_fma_f32 v235, v237, 4.0, v242\n" \
  "v_cos_f32 v234, v235\n" \
  "v_sin_f32 v233, v235\n" \
  "v_mul_f32 v232, v234, v240\n" \
  "v_mul_f32 v231, v233, v236\n" \
  "v_mul_f32 v230, v233, v240\n" \
  "v_mul_f32 v229, v234, v236\n" \
  "v_cvt_pk_f16_f32 v232, v232, -v231\n" \
  "v_cvt_pk_f16_f32 v230, v230, v229\n" \
  "v_accvgpr_write_b32 a161, v232\n" \
  "v_accvgpr_write_b32 a165, v230\n" \
  "v_fma_f32 v235, v237, 1.0, v242\n" \
  "v_cos_f32 v234, v235\n" \
  "v_sin_f32 v233, v235\n" \
  "v_mul_f32 v232, v234, v240\n" \
  "v_mul_f32 v231, v233, v236\n" \
  "v_mul_f32 v230, v233, v240\n" \
  "v_mul_f32 v229, v234, v236\n" \
  "v_cvt_pk_f16_f32 v232, v232, -v231\n" \
  "v_cvt_pk_f16_f32 v230, v230, v229\n" \
  "v_accvgpr_write_b32 a162, v232\n" \
  "v_accvgpr_write_b32 a166, v230\n" \
  "v_fma_f32 v235, v237, 4.0, v242\n" \
  "v_add_f32 v235, v235, v237\n" \
  "v_cos_f32 v234, v235\n" \
  "v_sin_f32 v233, v235\n" \
  "v_mul_f32 v232, v234, v240\n" \
  "v_mul_f32 v231, v233, v236\n" \
  "v_mul_f32 v230, v233, v240\n" \
  "v_mul_f32 v229, v234, v236\n" \
  "v_cvt_pk_f16_f32 v232, v232, -v231\n" \
  "v_cvt_pk_f16_f32 v230, v230, v229\n" \
  "v_accvgpr_write_b32 a163, v232\n" \
  "v_accvgpr_write_b32 a167, v230\n" \
  "v_cvt_f32_u32 v237, v239\n" \
  "v_cvt_f32_u32 v242, v238\n" \
  "v_cmp_lt_u32 vcc, 7, v246\n" \
  "v_mul_f32 v237, 973078528, v237\n" \
  "v_mul_f32 v235, v237, v242\n" \
  "v_cos_f32 v234, v235\n" \
  "v_sin_f32 v233, v235\n" \
  "v_mul_f32 v234, 0.5, v234\n" \
  "v_mul_f32 v233, 0.5, v233\n" \
  "v_cvt_pk_f16_f32 v232, v234, -v233\n" \
  "v_cvt_pk_f16_f32 v231, v233, v234\n" \
  "v_cndmask_b32 v230, v232, 0, vcc\n" \
  "v_cndmask_b32 v232, 0, v232, vcc\n" \
  "v_cndmask_b32 v229, v231, 0, vcc\n" \
  "v_xor_b32 v231, 2147516416, v231\n" \
  "v_accvgpr_write_b32 a228, v230\n" \
  "v_cndmask_b32 v231, 0, v231, vcc\n" \
  "v_accvgpr_write_b32 a229, v232\n" \
  "v_accvgpr_write_b32 a232, v229\n" \
  "v_accvgpr_write_b32 a233, v231\n" \
  "v_add_f32 v235, v235, v237\n" \
  "v_cos_f32 v234, v235\n" \
  "v_sin_f32 v233, v235\n" \
  "v_mul_f32 v234, 0.5, v234\n" \
  "v_mul_f32 v233, 0.5, v233\n" \
  "v_cvt_pk_f16_f32 v232, v234, -v233\n" \
  "v_cvt_pk_f16_f32 v231, v233, v234\n" \
  "v_cndmask_b32 v230, v232, 0, vcc\n" \
  "v_cndmask_b32 v232, 0, v232, vcc\n" \
  "v_cndmask_b32 v229, v231, 0, vcc\n" \
  "v_xor_b32 v231, 2147516416, v231\n" \
  "v_accvgpr_write_b32 a230, v230\n" \
  "v_cndmask_b32 v231, 0, v231, vcc\n" \
  "v_accvgpr_write_b32 a231, v232\n" \
  "v_accvgpr_write_b32 a234, v229\n" \
  "v_accvgpr_write_b32 a235, v231\n" \
  "s_cmp_eq_u32 s20, 0\n" \
  "s_cselect_b64 s[58:59], -1, 0\n" \
  "s_movk_i32 s34, 144\n" \
  "v_cmp_gt_u32 s[94:95], s34, v0\n" \
  "v_mov_b32 v30, 0\n" \
  "v_mov_b32 v31, 0\n" \
  "v_mov_b32 v255, 161664\n" \
  "ds_write_b64 v255, v[30:31] offset:0\n" \
  "s_lshr_b32 s88, s2, 6\n" \
  "s_lshl_b32 s98, s84, 2\n" \
  "s_lshr_b32 s100, s85, 1\n" \
  "s_and_b32 s100, s100, 3\n" \
  "s_add_u32 s89, s98, s100\n" \
  "s_mul_i32 s98, s86, 9344\n" \
  "s_add_u32 s82, s98, 524288\n" \
  "s_lshl_b32 s98, s86, 8\n" \
  "s_add_u32 s11, s98, 9764864\n" \
  "s_lshl_b32 s98, s86, 4\n" \
  "s_add_u32 s28, s98, 65536\n" \
  "s_and_b32 s98, s84, 1\n" \
  "s_lshl_b32 s98, s98, 3\n" \
  "s_and_b32 s100, s85, 7\n" \
  "s_add_u32 s90, s98, s100\n" \
  "s_lshl_b32 s98, s88, 5\n" \
  "s_add_u32 s98, s98, s89\n" \
  "s_lshl_b32 s98, s98, 1\n" \
  "s_and_b32 s100, s85, 1\n" \
  "s_add_u32 s98, s98, s100\n" \
  "s_lshl_b32 s98, s98, 7\n" \
  "s_add_u32 s80, s98, 262144\n" \
  "s_mov_b32 s35, 987991609\n" \
  "v_mul_u32_u24 v242, 9, v244\n" \
  "v_add_u32 v240, 8, v242\n" \
  "v_lshlrev_b32 v242, 4, v246\n" \
  "v_lshl_add_u32 v239, v240, 16, v242\n" \
  "v_lshrrev_b32 v242, 3, v191\n" \
  "v_and_b32 v240, 7, v191\n" \
  "v_lshlrev_b32 v238, 4, v240\n" \
  "v_lshl_add_u32 v240, v242, 16, v238\n" \
  "v_add_u32 v242, s80, v240\n" \
  "s_add_u32 s83, s82, 4096\n" \
  "s_add_u32 s91, s82, 8192\n" \
  "v_mov_b32 v30, -1\n" \
  "v_mov_b32 v31, -1\n" \
  "v_mov_b32 v32, -1\n" \
  "v_mov_b32 v33, -1\n" \
  "v_cmp_eq_u32 vcc, 24, v191\n" \
  "v_mov_b32 v255, s28\n" \
  "s_cmp_eq_u32 s20, 2\n" \
  "v_cndmask_b32 v242, v242, v255, vcc\n" \
  "s_cselect_b64 vcc, -1, 0\n" \
  "s_cselect_b32 s91, s11, s91\n" \
  "v_cndmask_b32 v240, v243, v239, vcc\n" \
  "s_cmp_eq_u32 s20, 3\n" \
  "s_cselect_b64 vcc, -1, 0\n" \
  "s_cselect_b32 s91, 0, s91\n" \
  "s_cselect_b32 s32, 0, -1\n" \
  "v_cndmask_b32 v240, v240, v242, vcc\n" \
  "s_cselect_b32 s92, 33554431, -1\n" \
  "s_cselect_b32 s93, 0, -1\n" \
  "s_cmp_eq_u32 s20, 1\n" \
  "s_cselect_b64 s[92:93], 15, s[92:93]\n" \
  "v_mov_b32 v34, s32\n" \
  "v_mov_b32 v35, s32\n" \
  "v_mov_b32 v36, s32\n" \
  "v_mov_b32 v37, s32\n" \
  "s_movk_i32 s32, 768\n" \
  "v_lshlrev_b32 v242, 5, v191\n" \
  "v_bfe_u32 v239, v191, 5, 1\n" \
  "v_and_or_b32 v242, v242, s32, v239\n" \
  "v_lshrrev_b32 v239, 3, v191\n" \
  "v_mul_u32_u24 v242, 9216, v242\n" \
  "v_bitop3_b32 v239, v239, v191, 6 bitop3:0x6c\n" \
  "v_and_b32 v239, 7, v239\n" \
  "v_lshl_or_b32 v38, v239, 4, v242\n" \
  "v_add_u32 v39, 17408, v38\n" \
  "v_add_u32 v40, 34816, v38\n" \
  "v_add_u32 v41, 52224, v38\n" \
  "v_bfrev_b32 v242, 1\n" \
  "v_cndmask_b32 v242, v242, v243, s[58:59]\n" \
  "s_waitcnt lgkmcnt(0)\n" \
  "s_mov_b32 s17, s16\n" \
  "v_and_b32 v239, 127, v0\n" \
  "s_mul_i32 s98, s86, 4624\n" \
  "v_lshl_add_u32 v238, v239, 4, s98\n" \
  "v_add_u32 v237, 3440, v238\n" \
  "v_add_u32 v236, 2048, v238\n" \
  "v_add_u32 v235, 1392, v238\n" \
  "s_lshr_b32 s98, s20, 1\n" \
  "s_mul_i32 s98, s98, 6144\n" \
  "s_and_b32 s100, s20, 1\n" \
  "s_lshl_b32 s100, s100, 10\n" \
  "s_add_u32 s98, s98, s100\n" \
  "s_add_u32 s56, s98, 19584\n" \
  "v_cmp_gt_u32 vcc, 57, v239\n" \
  "v_subrev_u32 v255, 1183744, v237\n" \
  "s_mov_b32 m0, s56\n" \
  "v_cndmask_b32 v236, v235, v236, vcc\n" \
  "buffer_load_dwordx4 v238, s[52:55], 0 offen offset:0 lds\n" \
  "v_min_u32 v237, v237, v255\n" \
  "s_add_u32 m0, s56, 2048\n" \
  "s_bitcmp1_b32 s20, 0\n" \
  "buffer_load_dwordx4 v236, s[52:55], 0 offen offset:0 lds\n" \
  "s_add_u32 m0, s56, 4096\n" \
  "s_cselect_b32 exec_hi, 32767, -1\n" \
  "buffer_load_dwordx4 v237, s[52:55], 0 offen offset:0 lds\n" \
  "s_mov_b64 exec, -1\n" \
  "s_movk_i32 s32, 10240\n" \
  "buffer_load_dwordx4 v[42:45], v243, s[76:79], s32 offen offset:0\n" \
  "s_waitcnt vmcnt(12)\n" \
  "v_readfirstlane_b32 s18, v2\n" \
  "v_readfirstlane_b32 s19, v3\n" \
  "s_add_u32 s18, s18, 1\n" \
  "s_addc_u32 s19, s19, 0\n" \
  "s_waitcnt vmcnt(11)\n" \
  "v_lshlrev_b32 v2, 16, v4\n" \
  "v_and_b32 v3, 4294901760, v4\n" \
  "v_lshlrev_b32 v46, 16, v5\n" \
  "v_and_b32 v47, 4294901760, v5\n" \
  "v_mul_f32 v239, 3216550459, v2\n" \
  "v_mul_f32 v238, 3216550459, v3\n" \
  "v_mul_f32 v237, 3216550459, v46\n" \
  "v_mul_f32 v236, 3216550459, v47\n" \
  "v_exp_f32 v239, v239\n" \
  "v_exp_f32 v238, v238\n" \
  "v_exp_f32 v237, v237\n" \
  "v_exp_f32 v236, v236\n" \
  "v_add_f32 v239, 1.0, v239\n" \
  "v_add_f32 v238, 1.0, v238\n" \
  "v_add_f32 v237, 1.0, v237\n" \
  "v_add_f32 v236, 1.0, v236\n" \
  "v_rcp_f32 v239, v239\n" \
  "v_rcp_f32 v238, v238\n" \
  "v_rcp_f32 v237, v237\n" \
  "v_rcp_f32 v236, v236\n" \
  "v_mul_f32 v239, v239, v2\n" \
  "v_mul_f32 v238, v238, v3\n" \
  "v_mul_f32 v237, v237, v46\n" \
  "v_mul_f32 v236, v236, v47\n" \
  "v_cvt_pk_bf16_f32 v2, v239, v238\n" \
  "v_cvt_pk_bf16_f32 v3, v237, v236\n" \
  "v_add_u32 v239, 162432, v249\n" \
  "s_mul_i32 s98, s22, 384\n" \
  "s_add_u32 s98, s98, 162432\n" \
  "v_lshl_add_u32 v249, v248, 1, s98\n" \
  "v_and_b32 v248, 3, v191\n" \
  "v_lshlrev_b32 v238, 3, v244\n" \
  "v_lshl_add_u32 v237, v248, 5, v238\n" \
  "s_lshl_b32 s98, s20, 7\n" \
  "s_add_u32 s98, s98, 161152\n" \
  "v_add_u32 v248, s98, v237\n" \
  "v_lshlrev_b32 v238, 3, v246\n" \
  "v_add_u32 v237, 161152, v238\n" \
  "s_mul_i32 s98, s22, 9216\n" \
  "s_lshl_b32 s100, s23, 8\n" \
  "s_add_u32 s98, s98, s100\n" \
  "s_add_u32 s63, s98, 8192\n" \
  "s_mov_b64 exec, s[94:95]\n" \
  "ds_write_b64 v239, v[2:3] offset:0\n" \
  "s_mov_b64 exec, -1\n" \
  "s_cmp_lt_u32 s2, 108\n" \
  "s_cselect_b64 s[96:97], 65535, 0\n" \
  "s_and_b64 s[96:97], s[96:97], s[58:59]\n" \
  "s_waitcnt lgkmcnt(0)\n" \
  "s_barrier\n" \
  "ds_read_u16 v2, v249 offset:0\n" \
  "ds_read_u16 v4, v249 offset:128\n" \
  "ds_read_u16 v46, v249 offset:256\n" \
  "s_waitcnt lgkmcnt(2)\n" \
  "v_lshlrev_b32 v2, 16, v2\n" \
  "s_waitcnt lgkmcnt(1)\n" \
  "v_lshlrev_b32 v4, 16, v4\n" \
  "s_waitcnt lgkmcnt(0)\n" \
  "v_lshlrev_b32 v46, 16, v46\n" \
  "s_waitcnt vmcnt(10)\n" \
  "v_lshlrev_b32 v48, 16, v6\n" \
  "v_and_b32 v49, 4294901760, v6\n" \
  "v_pk_mul_f32 v[50:51], v[48:49], v[2:3] op_sel_hi:[1,0]\n" \
  "v_lshlrev_b32 v48, 16, v7\n" \
  "v_and_b32 v49, 4294901760, v7\n" \
  "v_pk_mul_f32 v[52:53], v[48:49], v[2:3] op_sel_hi:[1,0]\n" \
  "v_lshlrev_b32 v48, 16, v8\n" \
  "v_and_b32 v49, 4294901760, v8\n" \
  "v_pk_mul_f32 v[54:55], v[48:49], v[2:3] op_sel_hi:[1,0]\n" \
  "v_lshlrev_b32 v48, 16, v9\n" \
  "v_and_b32 v49, 4294901760, v9\n" \
  "v_pk_mul_f32 v[6:7], v[48:49], v[2:3] op_sel_hi:[1,0]\n" \
  "s_waitcnt vmcnt(9)\n" \
  "v_lshlrev_b32 v2, 16, v10\n" \
  "v_and_b32 v3, 4294901760, v10\n" \
  "v_pk_fma_f32 v[50:51], v[2:3], v[4:5], v[50:51] op_sel_hi:[1,0,1]\n" \
  "v_lshlrev_b32 v2, 16, v11\n" \
  "v_and_b32 v3, 4294901760, v11\n" \
  "v_pk_fma_f32 v[52:53], v[2:3], v[4:5], v[52:53] op_sel_hi:[1,0,1]\n" \
  "v_lshlrev_b32 v2, 16, v12\n" \
  "v_and_b32 v3, 4294901760, v12\n" \
  "v_pk_fma_f32 v[54:55], v[2:3], v[4:5], v[54:55] op_sel_hi:[1,0,1]\n" \
  "v_lshlrev_b32 v2, 16, v13\n" \
  "v_and_b32 v3, 4294901760, v13\n" \
  "v_pk_fma_f32 v[6:7], v[2:3], v[4:5], v[6:7] op_sel_hi:[1,0,1]\n" \
  "s_waitcnt vmcnt(8)\n" \
  "v_lshlrev_b32 v2, 16, v14\n" \
  "v_and_b32 v3, 4294901760, v14\n" \
  "v_pk_fma_f32 v[50:51], v[2:3], v[46:47], v[50:51] op_sel_hi:[1,0,1]\n" \
  "v_lshlrev_b32 v2, 16, v15\n" \
  "v_and_b32 v3, 4294901760, v15\n" \
  "v_pk_fma_f32 v[52:53], v[2:3], v[46:47], v[52:53] op_sel_hi:[1,0,1]\n" \
  "v_lshlrev_b32 v2, 16, v16\n" \
  "v_and_b32 v3, 4294901760, v16\n" \
  "v_pk_fma_f32 v[54:55], v[2:3], v[46:47], v[54:55] op_sel_hi:[1,0,1]\n" \
  "v_lshlrev_b32 v2, 16, v17\n" \
  "v_and_b32 v3, 4294901760, v17\n" \
  "v_pk_fma_f32 v[6:7], v[2:3], v[46:47], v[6:7] op_sel_hi:[1,0,1]\n" \
  "v_permlane32_swap_b32 v50, v54\n" \
  "v_permlane32_swap_b32 v51, v55\n" \
  "v_permlane32_swap_b32 v52, v6\n" \
  "v_permlane32_swap_b32 v53, v7\n" \
  "v_pk_add_f32 v[50:51], v[50:51], v[54:55]\n" \
  "v_pk_add_f32 v[52:53], v[52:53], v[6:7]\n" \
  "v_mov_b32 v2, s18\n" \
  "v_mov_b32 v3, s19\n" \
  "v_permlane16_swap_b32 v50, v52\n" \
  "v_permlane16_swap_b32 v51, v53\n" \
  "v_pk_add_f32 v[50:51], v[50:51], v[52:53]\n" \
  "s_nop 1\n" \
  "v_add_f32_dpp v50, v50, v50 row_ror:8 row_mask:0xf bank_mask:0xf\n" \
  "v_add_f32_dpp v51, v51, v51 row_ror:8 row_mask:0xf bank_mask:0xf\n" \
  "s_nop 0\n" \
  "v_add_f32_dpp v50, v50, v50 row_ror:4 row_mask:0xf bank_mask:0xf\n" \
  "v_add_f32_dpp v51, v51, v51 row_ror:4 row_mask:0xf bank_mask:0xf\n" \
  "ds_write_b64 v248, v[50:51] offset:0\n" \
  "s_waitcnt lgkmcnt(0)\n" \
  "s_barrier\n" \
  "ds_read_b64 v[50:51], v237 offset:0\n" \
  "ds_read_b64 v[52:53], v237 offset:128\n" \
  "ds_read_b64 v[54:55], v237 offset:256\n" \
  "ds_read_b64 v[6:7], v237 offset:384\n" \
  "s_waitcnt lgkmcnt(2)\n" \
  "v_pk_add_f32 v[50:51], v[50:51], v[52:53]\n" \
  "s_waitcnt lgkmcnt(0)\n" \
  "v_pk_add_f32 v[54:55], v[54:55], v[6:7]\n" \
  "v_pk_add_f32 v[4:5], v[50:51], v[54:55]\n" \
  "s_mov_b64 exec, s[96:97]\n" \
  "buffer_store_dwordx4 v[2:5], v252, s[36:39], s63 offen offset:0 sc1\n" \
  "s_mov_b64 exec, -1\n" \
  "buffer_store_dwordx4 v[30:33], v243, s[36:39], s82 offen offset:0 sc1\n" \
  "buffer_store_dwordx4 v[30:33], v243, s[36:39], s83 offen offset:0 sc1\n" \
  "s_mov_b64 exec, s[92:93]\n" \
  "buffer_store_dwordx4 v[34:37], v240, s[36:39], s91 offen offset:0 sc1\n" \
  "s_mov_b64 exec, -1\n" \
  "s_lshl_b32 s98, s85, 3\n" \
  "s_lshl_b32 s100, s20, 1\n" \
  "s_add_u32 s98, s98, s100\n" \
  "s_mul_i32 s98, s98, 9216\n" \
  "s_mul_i32 s100, s84, 1152\n" \
  "s_add_u32 s98, s98, s100\n" \
  "s_add_u32 s32, s98, 1024\n" \
  "s_lshl_b32 s98, s20, 10\n" \
  "s_add_u32 s33, s98, 155008\n" \
  "s_mov_b32 m0, s33\n" \
  "s_movk_i32 s31, 4096\n" \
  "s_cmp_gt_u32 s20, 1\n" \
  "buffer_load_dwordx4 v38, s[4:7], s32 offen offset:0 lds\n" \
  "s_cselect_b32 s31, -28672, s31\n" \
  "s_add_u32 m0, s33, s31\n" \
  "s_add_u32 s32, s32, 9437184\n" \
  "buffer_load_dwordx4 v38, s[4:7], s32 offen offset:0 lds\n" \
  "s_waitcnt vmcnt(6)\n" \
  "s_barrier\n" \
  "v_and_b32 v249, 65535, v42\n" \
  "ds_read_u16 v2, v249 offset:19584\n" \
  "ds_read_u16 v3, v249 offset:20162\n" \
  "ds_read_u16 v4, v249 offset:20740\n" \
  "ds_read_u16 v5, v249 offset:21318\n" \
  "ds_read_u16 v6, v249 offset:25728\n" \
  "ds_read_u16 v7, v249 offset:26306\n" \
  "ds_read_u16 v8, v249 offset:26884\n" \
  "ds_read_u16 v9, v249 offset:27462\n" \
  "s_add_u32 s32, s82, 2392064\n" \
  "s_cmp_eq_u32 s20, 1\n" \
  "s_cselect_b64 vcc, 255, s[58:59]\n" \
  "v_mov_b32 v249, 0\n" \
  "v_and_b32 v248, 65535, v43\n" \
  "ds_read_u16 v10, v248 offset:19584\n" \
  "ds_read_u16 v11, v248 offset:20162\n" \
  "ds_read_u16 v12, v248 offset:20740\n" \
  "ds_read_u16 v13, v248 offset:21318\n" \
  "ds_read_u16 v14, v248 offset:25728\n" \
  "ds_read_u16 v15, v248 offset:26306\n" \
  "ds_read_u16 v16, v248 offset:26884\n" \
  "ds_read_u16 v17, v248 offset:27462\n" \
  "v_lshrrev_b32 v248, 16, v42\n" \
  "s_waitcnt lgkmcnt(11)\n" \
  "v_lshl_or_b32 v2, v6, 16, v2\n" \
  "s_waitcnt lgkmcnt(10)\n" \
  "v_lshl_or_b32 v3, v7, 16, v3\n" \
  "s_waitcnt lgkmcnt(9)\n" \
  "v_lshl_or_b32 v4, v8, 16, v4\n" \
  "s_waitcnt lgkmcnt(8)\n" \
  "v_lshl_or_b32 v5, v9, 16, v5\n" \
  "buffer_store_dwordx4 v[2:5], v248, s[36:39], s32 offen offset:0 sc1\n" \
  "v_and_b32 v6, 2147450879, v2\n" \
  "v_pk_max_u16 v249, v249, v6\n" \
  "v_and_b32 v7, 2147450879, v3\n" \
  "v_pk_max_u16 v249, v249, v7\n" \
  "v_and_b32 v8, 2147450879, v4\n" \
  "v_pk_max_u16 v249, v249, v8\n" \
  "v_and_b32 v9, 2147450879, v5\n" \
  "v_pk_max_u16 v249, v249, v9\n" \
  "v_and_b32 v248, 65535, v44\n" \
  "ds_read_u16 v2, v248 offset:19584\n" \
  "ds_read_u16 v3, v248 offset:20162\n" \
  "ds_read_u16 v4, v248 offset:20740\n" \
  "ds_read_u16 v5, v248 offset:21318\n" \
  "ds_read_u16 v6, v248 offset:25728\n" \
  "ds_read_u16 v7, v248 offset:26306\n" \
  "ds_read_u16 v8, v248 offset:26884\n" \
  "ds_read_u16 v9, v248 offset:27462\n" \
  "v_lshrrev_b32 v248, 16, v43\n" \
  "s_waitcnt lgkmcnt(11)\n" \
  "v_lshl_or_b32 v10, v14, 16, v10\n" \
  "s_waitcnt lgkmcnt(10)\n" \
  "v_lshl_or_b32 v11, v15, 16, v11\n" \
  "s_waitcnt lgkmcnt(9)\n" \
  "v_lshl_or_b32 v12, v16, 16, v12\n" \
  "s_waitcnt lgkmcnt(8)\n" \
  "v_lshl_or_b32 v13, v17, 16, v13\n" \
  "buffer_store_dwordx4 v[10:13], v248, s[36:39], s32 offen offset:0 sc1\n" \
  "v_and_b32 v14, 2147450879, v10\n" \
  "v_pk_max_u16 v249, v249, v14\n" \
  "v_and_b32 v15, 2147450879, v11\n" \
  "v_pk_max_u16 v249, v249, v15\n" \
  "v_and_b32 v16, 2147450879, v12\n" \
  "v_pk_max_u16 v249, v249, v16\n" \
  "v_and_b32 v17, 2147450879, v13\n" \
  "v_pk_max_u16 v249, v249, v17\n" \
  "s_mov_b64 s[96:97], vcc\n" \
  "v_lshrrev_b32 v248, 16, v44\n" \
  "s_waitcnt lgkmcnt(3)\n" \
  "v_lshl_or_b32 v2, v6, 16, v2\n" \
  "s_waitcnt lgkmcnt(2)\n" \
  "v_lshl_or_b32 v3, v7, 16, v3\n" \
  "s_waitcnt lgkmcnt(1)\n" \
  "v_lshl_or_b32 v4, v8, 16, v4\n" \
  "s_waitcnt lgkmcnt(0)\n" \
  "v_lshl_or_b32 v5, v9, 16, v5\n" \
  "s_mov_b64 exec, s[96:97]\n" \
  "buffer_store_dwordx4 v[2:5], v248, s[36:39], s32 offen offset:0 sc1\n" \
  "s_mov_b64 exec, -1\n" \
  "v_and_b32 v6, 2147450879, v2\n" \
  "v_pk_max_u16 v249, v249, v6\n" \
  "v_and_b32 v7, 2147450879, v3\n" \
  "v_pk_max_u16 v249, v249, v7\n" \
  "v_and_b32 v8, 2147450879, v4\n" \
  "v_pk_max_u16 v249, v249, v8\n" \
  "v_and_b32 v9, 2147450879, v5\n" \
  "v_pk_max_u16 v249, v249, v9\n" \
  "v_lshlrev_b32 v255, 16, v249\n" \
  "v_max_u32 v249, v249, v255\n" \
  "v_mov_b32 v255, 161664\n" \
  "s_nop 0\n" \
  "v_max_u32_dpp v249, v249, v249 quad_perm:[1,0,3,2] row_mask:0xf bank_mask:0xf\n" \
  "s_nop 1\n" \
  "v_max_u32_dpp v249, v249, v249 quad_perm:[2,3,0,1] row_mask:0xf bank_mask:0xf\n" \
  "s_nop 1\n" \
  "v_max_u32_dpp v249, v249, v249 row_half_mirror row_mask:0xf bank_mask:0xf\n" \
  "s_nop 1\n" \
  "v_max_u32_dpp v249, v249, v249 row_mirror row_mask:0xf bank_mask:0xf\n" \
  "s_mov_b32 exec_lo, 65537\n" \
  "s_mov_b32 exec_hi, 65537\n" \
  "ds_max_u32 v255, v249 offset:4\n" \
  "s_mov_b64 exec, -1\n" \
  "v_mul_u32_u24 v249, 2448, v253\n" \
  "s_mul_i32 s98, s20, 4896\n" \
  "v_add_u32 v248, s98, v249\n" \
  "v_lshrrev_b32 v249, 3, v254\n" \
  "v_lshlrev_b32 v240, 4, v249\n" \
  "v_lshl_add_u32 v249, v254, 5, v240\n" \
  "v_add_u32 v240, v249, v248\n" \
  "v_lshl_add_u32 v249, v254, 3, v248\n" \
  "v_lshlrev_b32 v2, 16, v18\n" \
  "v_and_b32 v3, 4294901760, v18\n" \
  "v_lshlrev_b32 v4, 16, v19\n" \
  "v_and_b32 v5, 4294901760, v19\n" \
  "v_lshlrev_b32 v6, 16, v20\n" \
  "v_and_b32 v7, 4294901760, v20\n" \
  "v_lshlrev_b32 v8, 16, v21\n" \
  "v_and_b32 v9, 4294901760, v21\n" \
  "v_lshlrev_b32 v10, 16, v22\n" \
  "v_and_b32 v11, 4294901760, v22\n" \
  "v_lshlrev_b32 v12, 16, v23\n" \
  "v_and_b32 v13, 4294901760, v23\n" \
  "v_lshlrev_b32 v14, 16, v24\n" \
  "v_and_b32 v15, 4294901760, v24\n" \
  "v_lshlrev_b32 v16, 16, v25\n" \
  "v_and_b32 v17, 4294901760, v25\n" \
  "v_lshlrev_b32 v30, 16, v247\n" \
  "v_and_b32 v31, 4294901760, v247\n" \
  "v_mov_b32 v33, 925353388\n" \
  "v_mov_b32_dpp v34, v2 row_newbcast:0 row_mask:0xf bank_mask:0xf\n" \
  "v_mov_b32_dpp v248, v2 row_newbcast:0 row_mask:0xf bank_mask:0xf\n" \
  "s_nop 1\n" \
  "v_permlane16_swap_b32 v34, v248\n" \
  "v_pk_add_f32 v[2:3], v[2:3], v[34:35] op_sel_hi:[1,0] neg_lo:[0,1] neg_hi:[0,1]\n" \
  "v_pk_add_f32 v[4:5], v[4:5], v[34:35] op_sel_hi:[1,0] neg_lo:[0,1] neg_hi:[0,1]\n" \
  "v_pk_add_f32 v[6:7], v[6:7], v[34:35] op_sel_hi:[1,0] neg_lo:[0,1] neg_hi:[0,1]\n" \
  "v_pk_add_f32 v[8:9], v[8:9], v[34:35] op_sel_hi:[1,0] neg_lo:[0,1] neg_hi:[0,1]\n" \
  "v_pk_add_f32 v[10:11], v[10:11], v[34:35] op_sel_hi:[1,0] neg_lo:[0,1] neg_hi:[0,1]\n" \
  "v_pk_add_f32 v[12:13], v[12:13], v[34:35] op_sel_hi:[1,0] neg_lo:[0,1] neg_hi:[0,1]\n" \
  "v_pk_add_f32 v[14:15], v[14:15], v[34:35] op_sel_hi:[1,0] neg_lo:[0,1] neg_hi:[0,1]\n" \
  "v_pk_add_f32 v[16:17], v[16:17], v[34:35] op_sel_hi:[1,0] neg_lo:[0,1] neg_hi:[0,1]\n" \
  "v_pk_add_f32 v[30:31], v[30:31], v[34:35] op_sel_hi:[1,0] neg_lo:[0,1] neg_hi:[0,1]\n" \
  "v_pk_add_f32 v[34:35], v[2:3], v[4:5]\n" \
  "v_pk_mul_f32 v[36:37], v[2:3], v[2:3]\n" \
  "v_pk_fma_f32 v[36:37], v[4:5], v[4:5], v[36:37]\n" \
  "v_pk_add_f32 v[34:35], v[34:35], v[6:7]\n" \
  "v_pk_fma_f32 v[36:37], v[6:7], v[6:7], v[36:37]\n" \
  "v_pk_add_f32 v[34:35], v[34:35], v[8:9]\n" \
  "v_pk_fma_f32 v[36:37], v[8:9], v[8:9], v[36:37]\n" \
  "v_pk_add_f32 v[34:35], v[34:35], v[10:11]\n" \
  "v_pk_fma_f32 v[36:37], v[10:11], v[10:11], v[36:37]\n" \
  "v_pk_add_f32 v[34:35], v[34:35], v[12:13]\n" \
  "v_pk_fma_f32 v[36:37], v[12:13], v[12:13], v[36:37]\n" \
  "v_pk_add_f32 v[34:35], v[34:35], v[14:15]\n" \
  "v_pk_fma_f32 v[36:37], v[14:15], v[14:15], v[36:37]\n" \
  "v_pk_add_f32 v[34:35], v[34:35], v[16:17]\n" \
  "v_pk_fma_f32 v[36:37], v[16:17], v[16:17], v[36:37]\n" \
  "v_pk_add_f32 v[34:35], v[34:35], v[30:31]\n" \
  "v_pk_fma_f32 v[36:37], v[30:31], v[30:31], v[36:37]\n" \
  "v_add_f32 v248, v34, v35\n" \
  "v_add_f32 v239, v36, v37\n" \
  "s_nop 1\n" \
  "v_permlane16_swap_b32 v248, v239\n" \
  "v_add_f32 v248, v248, v239\n" \
  "s_nop 1\n" \
  "v_add_f32_dpp v248, v248, v248 quad_perm:[1,0,3,2] row_mask:0xf bank_mask:0xf\n" \
  "s_nop 1\n" \
  "v_add_f32_dpp v248, v248, v248 quad_perm:[2,3,0,1] row_mask:0xf bank_mask:0xf\n" \
  "s_nop 1\n" \
  "v_add_f32_dpp v248, v248, v248 row_half_mirror row_mask:0xf bank_mask:0xf\n" \
  "s_nop 1\n" \
  "v_add_f32_dpp v248, v248, v248 row_mirror row_mask:0xf bank_mask:0xf\n" \
  "v_mov_b32 v239, v248\n" \
  "s_nop 1\n" \
  "v_permlane16_swap_b32 v248, v239\n" \
  "v_mul_f32 v248, s35, v248\n" \
  "v_fma_f32 v239, v239, s35, v33\n" \
  "v_fma_f32 v239, -v248, v248, v239\n" \
  "v_max_f32 v239, v239, v33\n" \
  "v_rsq_f32 v32, v239\n" \
  "s_nop 0\n" \
  "v_mul_f32_e64 v33, -v248, v32\n" \
  "v_pk_fma_f32 v[34:35], v[2:3], v[32:33], v[32:33] op_sel:[0,0,1] op_sel_hi:[1,0,1]\n" \
  "v_pk_fma_f32 v[36:37], v[4:5], v[32:33], v[32:33] op_sel:[0,0,1] op_sel_hi:[1,0,1]\n" \
  "ds_write_b128 v240, v[34:37] offset:0\n" \
  "v_pk_fma_f32 v[2:3], v[6:7], v[32:33], v[32:33] op_sel:[0,0,1] op_sel_hi:[1,0,1]\n" \
  "v_pk_fma_f32 v[4:5], v[8:9], v[32:33], v[32:33] op_sel:[0,0,1] op_sel_hi:[1,0,1]\n" \
  "ds_write_b128 v240, v[2:5] offset:16\n" \
  "v_pk_fma_f32 v[2:3], v[10:11], v[32:33], v[32:33] op_sel:[0,0,1] op_sel_hi:[1,0,1]\n" \
  "v_pk_fma_f32 v[4:5], v[12:13], v[32:33], v[32:33] op_sel:[0,0,1] op_sel_hi:[1,0,1]\n" \
  "ds_write_b128 v240, v[2:5] offset:1088\n" \
  "v_pk_fma_f32 v[2:3], v[14:15], v[32:33], v[32:33] op_sel:[0,0,1] op_sel_hi:[1,0,1]\n" \
  "v_pk_fma_f32 v[4:5], v[16:17], v[32:33], v[32:33] op_sel:[0,0,1] op_sel_hi:[1,0,1]\n" \
  "ds_write_b128 v240, v[2:5] offset:1104\n" \
  "v_pk_fma_f32 v[30:31], v[30:31], v[32:33], v[32:33] op_sel:[0,0,1] op_sel_hi:[1,0,1]\n" \
  "ds_write_b64 v249, v[30:31] offset:2176\n" \
  "s_mul_i32 s33, s20, 4896\n" \
  "v_and_b32 v249, 65535, v26\n" \
  "v_add_u32 v248, s33, v249\n" \
  "v_and_b32 v240, 65535, v27\n" \
  "v_add_u32 v239, s33, v240\n" \
  "v_and_b32 v238, 65535, v28\n" \
  "v_add_u32 v237, s33, v238\n" \
  "v_and_b32 v236, 65535, v29\n" \
  "v_add_u32 v235, s33, v236\n" \
  "v_lshrrev_b32 v234, 16, v26\n" \
  "v_add_u32 v233, s33, v234\n" \
  "v_lshrrev_b32 v232, 16, v27\n" \
  "v_add_u32 v231, s33, v232\n" \
  "v_lshrrev_b32 v230, 16, v28\n" \
  "v_add_u32 v229, s33, v230\n" \
  "v_lshrrev_b32 v228, 16, v29\n" \
  "v_add_u32 v227, s33, v228\n" \
  "ds_read_b64 v[2:3], v248 offset:0\n" \
  "ds_read_b64 v[4:5], v239 offset:0\n" \
  "ds_read_b64 v[6:7], v237 offset:0\n" \
  "ds_read_b64 v[8:9], v235 offset:0\n" \
  "ds_read_b64 v[10:11], v233 offset:0\n" \
  "ds_read_b64 v[12:13], v231 offset:0\n" \
  "ds_read_b64 v[14:15], v229 offset:0\n" \
  "ds_read_b64 v[16:17], v227 offset:0\n" \
  "ds_read_b64 v[26:27], v248 offset:2448\n" \
  "ds_read_b64 v[28:29], v239 offset:2448\n" \
  "ds_read_b64 v[30:31], v237 offset:2448\n" \
  "ds_read_b64 v[32:33], v235 offset:2448\n" \
  "ds_read_b64 v[34:35], v233 offset:2448\n" \
  "ds_read_b64 v[36:37], v231 offset:2448\n" \
  "ds_read_b64 v[42:43], v229 offset:2448\n" \
  "ds_read_b64 v[44:45], v227 offset:2448\n" \
  "s_movk_i32 s32, 8192\n" \
  "buffer_load_dwordx4 v[46:49], v243, s[36:39], s32 offen offset:0 sc1\n" \
  "s_movk_i32 s32, 17408\n" \
  "buffer_load_dwordx4 v[50:53], v243, s[36:39], s32 offen offset:0 sc1\n" \
  "s_movk_i32 s32, 26624\n" \
  "buffer_load_dwordx4 v[54:57], v243, s[36:39], s32 offen offset:0 sc1\n" \
  "s_movk_i32 s32, 12288\n" \
  "buffer_load_dwordx4 v[58:61], v243, s[36:39], s32 offen offset:0 sc1\n" \
  "s_movk_i32 s32, 21504\n" \
  "buffer_load_dwordx4 v[62:65], v243, s[36:39], s32 offen offset:0 sc1\n" \
  "s_movk_i32 s32, 30720\n" \
  "buffer_load_dwordx4 v[66:69], v243, s[36:39], s32 offen offset:0 sc1\n" \
  "s_movk_i32 s32, 16384\n" \
  "buffer_load_dwordx4 v[70:73], v242, s[36:39], s32 offen offset:0 sc1\n" \
  "s_movk_i32 s32, 25600\n" \
  "buffer_load_dwordx4 v[74:77], v242, s[36:39], s32 offen offset:0 sc1\n" \
  "s_mov_b32 s32, 34816\n" \
  "buffer_load_dwordx4 v[78:81], v242, s[36:39], s32 offen offset:0 sc1\n" \
  "s_lshl_b32 s98, s2, 3\n" \
  "s_add_u32 s56, s98, 4096\n" \
  "s_lshl_b32 s98, s20, 10\n" \
  "s_add_u32 s33, s98, 62848\n" \
  "v_mov_b32 v82, s18\n" \
  "s_and_b32 s32, s19, 16777215\n" \
  "s_waitcnt vmcnt(9) lgkmcnt(0)\n" \
  "s_barrier\n" \
  "ds_read_b32 v83, v255 offset:4\n" \
  "s_cmp_eq_u32 s20, 3\n" \
  "s_waitcnt lgkmcnt(0)\n" \
  "v_lshrrev_b32 v83, 23, v83\n" \
  "v_lshl_or_b32 v83, v83, 24, s32\n" \
  "s_cselect_b64 exec, 1, 0\n" \
  "buffer_store_dwordx2 v[82:83], off, s[36:39], s56 sc1\n" \
  "s_mov_b64 exec, -1\n" \
  "s_add_u32 m0, s33, 0\n" \
  "s_movk_i32 s32, 0\n" \
  "buffer_load_dwordx4 v243, s[76:79], s32 offen offset:0 lds\n" \
  "s_add_u32 m0, s33, 30720\n" \
  "s_movk_i32 s32, 4096\n" \
  "buffer_load_dwordx4 v243, s[76:79], s32 offen offset:0 lds\n" \
  "s_add_u32 m0, s33, 61440\n" \
  "s_movk_i32 s32, 8192\n" \
  "buffer_load_dwordx4 v243, s[76:79], s32 offen offset:0 lds\n" \
  "s_movk_i32 s32, 14336\n" \
  "buffer_load_dwordx4 v[82:85], v243, s[76:79], s32 offen offset:0\n" \
  "s_movk_i32 s32, 23552\n" \
  "buffer_load_dwordx4 v[86:89], v252, s[76:79], s32 offen offset:0\n" \
  "s_mul_i32 s98, s20, 589824\n" \
  "s_mul_i32 s100, s84, 1152\n" \
  "s_add_u32 s98, s98, s100\n" \
  "s_and_b32 s100, s85, 7\n" \
  "s_lshl_b32 s100, s100, 7\n" \
  "s_add_u32 s12, s98, s100\n" \
  "s_mul_i32 s98, s20, 30720\n" \
  "s_add_u32 s57, s98, 38272\n" \
  "s_lshl_b32 s98, s20, 10\n" \
  "s_add_u32 s8, s98, s82\n" \
  "s_lshl_b32 s98, s84, 2\n" \
  "s_add_u32 s98, s98, s20\n" \
  "s_lshl_b32 s98, s98, 16\n" \
  "s_lshl_b32 s100, s85, 11\n" \
  "s_add_u32 s98, s98, s100\n" \
  "s_add_u32 s24, s98, 5308416\n" \
  "s_mul_i32 s81, s88, 294912\n" \
  "s_sub_u32 s13, s12, s81\n" \
  "s_add_u32 s12, s12, s81\n" \
  "v_lshrrev_b32 v248, 5, v0\n" \
  "v_lshlrev_b32 v239, 4, v248\n" \
  "v_lshl_add_u32 v248, v0, 3, v239\n" \
  "v_add_u32 v239, 35216, v248\n" \
  "v_add_u32 v237, 32496, v248\n" \
  "v_add_u32 v235, 37392, v248\n" \
  "v_add_u32 v233, 34672, v248\n" \
  "v_and_b32 v248, 48, v191\n" \
  "v_lshl_add_u32 v231, v246, 6, v248\n" \
  "v_cmp_gt_u32 vcc, 32, v0\n" \
  "v_mov_b32 v90, 1.0\n" \
  "s_nop 0\n" \
  "v_cndmask_b32 v237, v237, v235, vcc\n" \
  "v_cndmask_b32 v90, v90, 0, vcc\n" \
  "v_cndmask_b32 v248, v248, v231, s[58:59]\n" \
  "v_mov_b32 v95, 0\n" \
  "v_mov_b32 v99, 0\n" \
  "v_mov_b32 v103, 0\n" \
  "v_mov_b32 v107, 0\n" \
  "s_waitcnt vmcnt(6)\n" \
  ".Lanc_%=:\n" \
  "v_xor_b32 v235, s18, v46\n" \
  "v_bitop3_b32 v235, v47, s19, v235 bitop3:0xbe\n" \
  "v_bitop3_b32 v235, v50, s18, v235 bitop3:0xbe\n" \
  "v_bitop3_b32 v235, v51, s19, v235 bitop3:0xbe\n" \
  "v_bitop3_b32 v235, v54, s18, v235 bitop3:0xbe\n" \
  "v_bitop3_b32 v235, v55, s19, v235 bitop3:0xbe\n" \
  "v_bitop3_b32 v235, v58, s18, v235 bitop3:0xbe\n" \
  "v_bitop3_b32 v235, v59, s19, v235 bitop3:0xbe\n" \
  "v_bitop3_b32 v235, v62, s18, v235 bitop3:0xbe\n" \
  "v_bitop3_b32 v235, v63, s19, v235 bitop3:0xbe\n" \
  "v_bitop3_b32 v235, v66, s18, v235 bitop3:0xbe\n" \
  "v_bitop3_b32 v235, v67, s19, v235 bitop3:0xbe\n" \
  "v_xor_b32 v229, s18, v70\n" \
  "v_bitop3_b32 v229, v71, s19, v229 bitop3:0xbe\n" \
  "v_bitop3_b32 v229, v74, s18, v229 bitop3:0xbe\n" \
  "v_bitop3_b32 v229, v75, s19, v229 bitop3:0xbe\n" \
  "v_bitop3_b32 v229, v78, s18, v229 bitop3:0xbe\n" \
  "v_bitop3_b32 v229, v79, s19, v229 bitop3:0xbe\n" \
  "v_bitop3_b32 v235, v229, s58, v235 bitop3:0xea\n" \
  "v_cmp_ne_u32 vcc, 0, v235\n" \
  "s_cbranch_vccnz .Lans_%=\n" \
  ".Land_%=:\n" \
  "v_mov_b32 v242, 0\n" \
  "v_pk_add_f32 v[108:109], v[48:49], v[52:53]\n" \
  "v_pk_add_f32 v[108:109], v[108:109], v[56:57]\n" \
  "s_add_u32 m0, s57, 0\n" \
  "s_add_u32 s31, s12, 0\n" \
  "buffer_load_dwordx4 v38, s[4:7], s31 offen offset:0 lds\n" \
  "v_cvt_pk_bf16_f32 v235, v108, v109\n" \
  "v_lshlrev_b32 v108, 16, v235\n" \
  "v_and_b32 v109, 4294901760, v235\n" \
  "ds_write_b64 v239, v[108:109] offset:0\n" \
  "v_max3_f32 v242, |v108|, |v109|, v242\n" \
  "buffer_load_dwordx4 v39, s[4:7], s31 offen offset:1024 lds\n" \
  "v_pk_add_f32 v[46:47], v[60:61], v[64:65]\n" \
  "v_pk_add_f32 v[46:47], v[46:47], v[68:69]\n" \
  "buffer_load_dwordx4 v40, s[4:7], s31 offen offset:2048 lds\n" \
  "v_cvt_pk_bf16_f32 v239, v46, v47\n" \
  "v_lshlrev_b32 v46, 16, v239\n" \
  "v_and_b32 v47, 4294901760, v239\n" \
  "v_pk_add_f32 v[46:47], v[46:47], v[90:91] op_sel_hi:[1,0]\n" \
  "v_cvt_pk_bf16_f32 v239, v46, v47\n" \
  "v_lshlrev_b32 v46, 16, v239\n" \
  "v_and_b32 v47, 4294901760, v239\n" \
  "ds_write_b64 v237, v[46:47] offset:0\n" \
  "v_max3_f32 v242, |v46|, |v47|, v242\n" \
  "buffer_load_dwordx4 v41, s[4:7], s31 offen offset:3072 lds\n" \
  "s_mov_b64 exec, s[58:59]\n" \
  "v_pk_add_f32 v[46:47], v[72:73], v[76:77]\n" \
  "v_pk_add_f32 v[46:47], v[46:47], v[80:81]\n" \
  "v_cvt_pk_bf16_f32 v239, v46, v47\n" \
  "v_lshlrev_b32 v46, 16, v239\n" \
  "v_and_b32 v47, 4294901760, v239\n" \
  "v_add_f32 v46, 1.0, v46\n" \
  "v_add_f32 v47, 1.0, v47\n" \
  "v_cvt_pk_bf16_f32 v239, v46, v47\n" \
  "v_lshlrev_b32 v46, 16, v239\n" \
  "v_and_b32 v47, 4294901760, v239\n" \
  "ds_write_b64 v233, v[46:47] offset:0\n" \
  "v_max3_f32 v242, |v46|, |v47|, v242\n" \
  "s_mov_b64 exec, -1\n" \
  "s_add_u32 m0, s57, 4096\n" \
  "s_add_u32 s31, s12, 9437184\n" \
  "buffer_load_dwordx4 v38, s[4:7], s31 offen offset:0 lds\n" \
  "v_max_u32_dpp v242, v242, v242 quad_perm:[1,0,3,2] row_mask:0xf bank_mask:0xf\n" \
  "buffer_load_dwordx4 v39, s[4:7], s31 offen offset:1024 lds\n" \
  "s_nop 0\n" \
  "v_max_u32_dpp v242, v242, v242 quad_perm:[2,3,0,1] row_mask:0xf bank_mask:0xf\n" \
  "buffer_load_dwordx4 v40, s[4:7], s31 offen offset:2048 lds\n" \
  "s_nop 0\n" \
  "v_max_u32_dpp v242, v242, v242 row_half_mirror row_mask:0xf bank_mask:0xf\n" \
  "buffer_load_dwordx4 v41, s[4:7], s31 offen offset:3072 lds\n" \
  "s_nop 0\n" \
  "v_max_u32_dpp v242, v242, v242 row_mirror row_mask:0xf bank_mask:0xf\n" \
  "s_mov_b32 exec_lo, 65537\n" \
  "s_mov_b32 exec_hi, 65537\n" \
  "ds_max_u32 v255, v242\n" \
  "s_mov_b64 exec, -1\n" \
  "s_waitcnt vmcnt(8) lgkmcnt(0)\n" \
  "s_barrier\n" \
  "ds_read_b32 v46, v255 offset:0\n" \
  "ds_read_b64 v[48:49], v249 offset:32768\n" \
  "ds_read_b64 v[50:51], v249 offset:35216\n" \
  "ds_read_b64 v[52:53], v240 offset:32768\n" \
  "ds_read_b64 v[54:55], v240 offset:35216\n" \
  "ds_read_b64 v[56:57], v238 offset:32768\n" \
  "ds_read_b64 v[58:59], v238 offset:35216\n" \
  "ds_read_b64 v[60:61], v236 offset:32768\n" \
  "ds_read_b64 v[62:63], v236 offset:35216\n" \
  "ds_read_b64 v[64:65], v234 offset:32768\n" \
  "ds_read_b64 v[66:67], v234 offset:35216\n" \
  "ds_read_b64 v[68:69], v232 offset:32768\n" \
  "ds_read_b64 v[70:71], v232 offset:35216\n" \
  "ds_read_b64 v[72:73], v230 offset:32768\n" \
  "ds_read_b64 v[74:75], v230 offset:35216\n" \
  "ds_read_b64 v[76:77], v228 offset:32768\n" \
  "ds_read_b64 v[78:79], v228 offset:35216\n" \
  "v_add_u32 v255, 62848, v252\n" \
  "ds_read_b128 a[200:203], v255 offset:0\n" \
  "ds_read_b128 a[204:207], v255 offset:1024\n" \
  "ds_read_b128 a[168:171], v255 offset:2048\n" \
  "ds_read_b128 a[172:175], v255 offset:3072\n" \
  "s_waitcnt lgkmcnt(15)\n" \
  "v_lshrrev_b32 v46, 23, v46\n" \
  "v_max_u32 v46, 64, v46\n" \
  "s_nop 0\n" \
  "v_readfirstlane_b32 s26, v46\n" \
  "v_sub_u32 v46, 256, v46\n" \
  "v_lshlrev_b32 v46, 23, v46\n" \
  "v_pk_mul_f32 v[48:49], v[48:49], v[46:47] op_sel_hi:[1,0]\n" \
  "v_pk_mul_f32 v[50:51], v[50:51], v[46:47] op_sel_hi:[1,0]\n" \
  "v_pk_fma_f32 v[80:81], v[2:3], v[48:49], v[50:51]\n" \
  "v_pk_fma_f32 v[2:3], v[26:27], v[48:49], v[50:51]\n" \
  "v_pk_mul_f32 v[52:53], v[52:53], v[46:47] op_sel_hi:[1,0]\n" \
  "v_pk_mul_f32 v[54:55], v[54:55], v[46:47] op_sel_hi:[1,0]\n" \
  "v_pk_fma_f32 v[26:27], v[4:5], v[52:53], v[54:55]\n" \
  "v_pk_fma_f32 v[4:5], v[28:29], v[52:53], v[54:55]\n" \
  "v_pk_mul_f32 v[56:57], v[56:57], v[46:47] op_sel_hi:[1,0]\n" \
  "s_waitcnt lgkmcnt(14)\n" \
  "v_pk_mul_f32 v[58:59], v[58:59], v[46:47] op_sel_hi:[1,0]\n" \
  "v_pk_fma_f32 v[28:29], v[6:7], v[56:57], v[58:59]\n" \
  "v_pk_fma_f32 v[6:7], v[30:31], v[56:57], v[58:59]\n" \
  "s_waitcnt lgkmcnt(13)\n" \
  "v_pk_mul_f32 v[60:61], v[60:61], v[46:47] op_sel_hi:[1,0]\n" \
  "s_waitcnt lgkmcnt(12)\n" \
  "v_pk_mul_f32 v[62:63], v[62:63], v[46:47] op_sel_hi:[1,0]\n" \
  "v_pk_fma_f32 v[30:31], v[8:9], v[60:61], v[62:63]\n" \
  "v_pk_fma_f32 v[8:9], v[32:33], v[60:61], v[62:63]\n" \
  "s_waitcnt lgkmcnt(11)\n" \
  "v_pk_mul_f32 v[64:65], v[64:65], v[46:47] op_sel_hi:[1,0]\n" \
  "s_waitcnt lgkmcnt(10)\n" \
  "v_pk_mul_f32 v[66:67], v[66:67], v[46:47] op_sel_hi:[1,0]\n" \
  "v_pk_fma_f32 v[32:33], v[10:11], v[64:65], v[66:67]\n" \
  "v_pk_fma_f32 v[10:11], v[34:35], v[64:65], v[66:67]\n" \
  "s_waitcnt lgkmcnt(9)\n" \
  "v_pk_mul_f32 v[68:69], v[68:69], v[46:47] op_sel_hi:[1,0]\n" \
  "s_waitcnt lgkmcnt(8)\n" \
  "v_pk_mul_f32 v[70:71], v[70:71], v[46:47] op_sel_hi:[1,0]\n" \
  "v_pk_fma_f32 v[34:35], v[12:13], v[68:69], v[70:71]\n" \
  "v_pk_fma_f32 v[12:13], v[36:37], v[68:69], v[70:71]\n" \
  "s_waitcnt lgkmcnt(7)\n" \
  "v_pk_mul_f32 v[72:73], v[72:73], v[46:47] op_sel_hi:[1,0]\n" \
  "s_waitcnt lgkmcnt(6)\n" \
  "v_pk_mul_f32 v[74:75], v[74:75], v[46:47] op_sel_hi:[1,0]\n" \
  "v_pk_fma_f32 v[36:37], v[14:15], v[72:73], v[74:75]\n" \
  "v_pk_fma_f32 v[14:15], v[42:43], v[72:73], v[74:75]\n" \
  "s_waitcnt lgkmcnt(5)\n" \
  "v_pk_mul_f32 v[76:77], v[76:77], v[46:47] op_sel_hi:[1,0]\n" \
  "s_waitcnt lgkmcnt(4)\n" \
  "v_pk_mul_f32 v[78:79], v[78:79], v[46:47] op_sel_hi:[1,0]\n" \
  "v_pk_fma_f32 v[42:43], v[16:17], v[76:77], v[78:79]\n" \
  "v_pk_fma_f32 v[16:17], v[44:45], v[76:77], v[78:79]\n" \
  "v_cvt_pk_f16_f32 v44, v80, v81\n" \
  "v_cvt_pk_f16_f32 v45, v26, v27\n" \
  "v_cvt_pk_f16_f32 v46, v28, v29\n" \
  "v_cvt_pk_f16_f32 v47, v30, v31\n" \
  "v_cvt_pk_f16_f32 v26, v32, v33\n" \
  "v_cvt_pk_f16_f32 v27, v34, v35\n" \
  "s_waitcnt lgkmcnt(3)\n" \
  "v_mfma_f32_16x16x32_f16 v[30:33], v[44:47], a[200:203], 0\n" \
  "v_cvt_pk_f16_f32 v28, v36, v37\n" \
  "v_cvt_pk_f16_f32 v29, v42, v43\n" \
  "s_waitcnt lgkmcnt(2)\n" \
  "v_mfma_f32_16x16x32_f16 v[34:37], v[44:47], a[204:207], 0\n" \
  "v_cvt_pk_f16_f32 v42, v2, v3\n" \
  "v_cvt_pk_f16_f32 v43, v4, v5\n" \
  "s_waitcnt lgkmcnt(1)\n" \
  "v_mfma_f32_16x16x32_f16 v[2:5], v[26:29], a[168:171], 0\n" \
  "v_cvt_pk_f16_f32 v44, v6, v7\n" \
  "v_cvt_pk_f16_f32 v45, v8, v9\n" \
  "s_waitcnt lgkmcnt(0)\n" \
  "v_mfma_f32_16x16x32_f16 v[6:9], v[26:29], a[172:175], 0\n" \
  "v_cvt_pk_f16_f32 v26, v10, v11\n" \
  "v_cvt_pk_f16_f32 v27, v12, v13\n" \
  "v_mfma_f32_16x16x32_f16 v[10:13], v[42:45], a[200:203], 0\n" \
  "v_cvt_pk_f16_f32 v28, v14, v15\n" \
  "v_cvt_pk_f16_f32 v29, v16, v17\n" \
  "v_mfma_f32_16x16x32_f16 v[14:17], v[42:45], a[204:207], 0\n" \
  "ds_read_b128 a[208:211], v255 offset:30720\n" \
  "v_mfma_f32_16x16x32_f16 v[42:45], v[26:29], a[168:171], 0\n" \
  "ds_read_b128 a[212:215], v255 offset:31744\n" \
  "v_mfma_f32_16x16x32_f16 v[46:49], v[26:29], a[172:175], 0\n" \
  "ds_read_b128 a[220:223], v255 offset:32768\n" \
  "ds_read_b128 a[224:227], v255 offset:33792\n" \
  "v_pk_add_f32 v[26:27], v[30:31], v[2:3]\n" \
  "v_pk_add_f32 v[28:29], v[32:33], v[4:5]\n" \
  "v_pk_add_f32 v[50:51], v[34:35], v[6:7]\n" \
  "v_pk_add_f32 v[52:53], v[36:37], v[8:9]\n" \
  "v_pk_add_f32 v[54:55], v[30:31], v[2:3] neg_lo:[0,1] neg_hi:[0,1]\n" \
  "v_pk_add_f32 v[56:57], v[32:33], v[4:5] neg_lo:[0,1] neg_hi:[0,1]\n" \
  "v_pk_add_f32 v[2:3], v[34:35], v[6:7] neg_lo:[0,1] neg_hi:[0,1]\n" \
  "v_pk_add_f32 v[4:5], v[36:37], v[8:9] neg_lo:[0,1] neg_hi:[0,1]\n" \
  "v_pk_add_f32 v[6:7], v[10:11], v[42:43]\n" \
  "v_pk_add_f32 v[8:9], v[12:13], v[44:45]\n" \
  "v_pk_add_f32 v[30:31], v[14:15], v[46:47]\n" \
  "v_pk_add_f32 v[32:33], v[16:17], v[48:49]\n" \
  "v_pk_add_f32 v[34:35], v[10:11], v[42:43] neg_lo:[0,1] neg_hi:[0,1]\n" \
  "v_pk_add_f32 v[36:37], v[12:13], v[44:45] neg_lo:[0,1] neg_hi:[0,1]\n" \
  "v_pk_add_f32 v[10:11], v[14:15], v[46:47] neg_lo:[0,1] neg_hi:[0,1]\n" \
  "v_pk_add_f32 v[12:13], v[16:17], v[48:49] neg_lo:[0,1] neg_hi:[0,1]\n" \
  "v_cvt_pk_f16_f32 v92, v26, v50\n" \
  "v_cvt_pk_f16_f32 v93, v27, v51\n" \
  "v_cvt_pk_f16_f32 v94, v28, v52\n" \
  "v_cvt_pk_f16_f32 v96, v54, v2\n" \
  "v_cvt_pk_f16_f32 v97, v55, v3\n" \
  "s_waitcnt lgkmcnt(3)\n" \
  "v_mfma_f32_16x16x32_f16 v[14:17], a[208:211], v[92:95], 0\n" \
  "v_cvt_pk_f16_f32 v98, v56, v4\n" \
  "s_waitcnt lgkmcnt(2)\n" \
  "v_mfma_f32_16x16x32_f16 v[2:5], a[212:215], v[92:95], 0\n" \
  "v_cvt_pk_f16_f32 v100, v6, v30\n" \
  "v_cvt_pk_f16_f32 v101, v7, v31\n" \
  "v_mfma_f32_16x16x32_f16 v[26:29], a[208:211], v[96:99], 0\n" \
  "v_cvt_pk_f16_f32 v102, v8, v32\n" \
  "v_mfma_f32_16x16x32_f16 v[6:9], a[212:215], v[96:99], 0\n" \
  "v_cvt_pk_f16_f32 v104, v34, v10\n" \
  "v_cvt_pk_f16_f32 v105, v35, v11\n" \
  "v_mfma_f32_16x16x32_f16 v[30:33], a[208:211], v[100:103], 0\n" \
  "v_cvt_pk_f16_f32 v106, v36, v12\n" \
  "v_mfma_f32_16x16x32_f16 v[10:13], a[212:215], v[100:103], 0\n" \
  "s_nop 0\n" \
  "v_mfma_f32_16x16x32_f16 v[34:37], a[208:211], v[104:107], 0\n" \
  "v_mfma_f32_16x16x32_f16 v[42:45], a[212:215], v[104:107], 0\n" \
  "s_mov_b32 exec_hi, 65535\n" \
  "v_cvt_pk_f16_f32 v46, v14, v2\n" \
  "s_nop 2\n" \
  "v_cvt_pk_f16_f32 v47, v30, v10\n" \
  "ds_write_b64 v82, v[46:47] offset:0\n" \
  "v_cvt_pk_f16_f32 v46, v15, v3\n" \
  "v_cvt_pk_f16_f32 v47, v31, v11\n" \
  "ds_write_b64 v82, v[46:47] offset:512\n" \
  "v_cvt_pk_f16_f32 v46, v16, v4\n" \
  "v_cvt_pk_f16_f32 v47, v32, v12\n" \
  "ds_write_b64 v82, v[46:47] offset:1024\n" \
  "v_cvt_pk_f16_f32 v2, v26, v6\n" \
  "v_cvt_pk_f16_f32 v3, v34, v42\n" \
  "ds_write_b64 v82, v[2:3] offset:256\n" \
  "v_cvt_pk_f16_f32 v2, v27, v7\n" \
  "v_cvt_pk_f16_f32 v3, v35, v43\n" \
  "ds_write_b64 v82, v[2:3] offset:768\n" \
  "v_cvt_pk_f16_f32 v2, v28, v8\n" \
  "v_cvt_pk_f16_f32 v3, v36, v44\n" \
  "ds_write_b64 v82, v[2:3] offset:1280\n" \
  "s_mov_b64 exec, -1\n" \
  "s_waitcnt lgkmcnt(0)\n" \
  "s_barrier\n" \
  "ds_read_b128 v[2:5], v83 offset:0\n" \
  "ds_read_b128 v[6:9], v84 offset:0\n" \
  "ds_read_b128 v[10:13], v85 offset:0\n" \
  "v_lshrrev_b32 v255, 1, v191\n" \
  "v_and_b32 v249, 1, v191\n" \
  "s_lshl_b32 s98, s20, 1\n" \
  "v_add_u32 v242, s98, v249\n" \
  "v_and_b32 v249, 6, v255\n" \
  "v_xor_b32 v240, v242, v249\n" \
  "v_lshlrev_b32 v249, 4, v240\n" \
  "v_lshl_add_u32 v242, v255, 7, v249\n" \
  "v_add_u32 v255, 155008, v242\n" \
  "v_lshlrev_b32 v249, 15, v253\n" \
  "v_sub_u32 v242, v255, v249\n" \
  "s_cmp_eq_u32 s20, 1\n" \
  "s_cselect_b32 s92, 65537, s58\n" \
  "s_cselect_b32 s93, 65537, s59\n" \
  "s_add_u32 s32, s8, 4096\n" \
  "s_add_u32 s33, s8, 8192\n" \
  "s_waitcnt lgkmcnt(2)\n" \
  "v_mfma_f32_16x16x32_f16 v[14:17], a[160:163], v[2:5], 0\n" \
  "v_mfma_f32_16x16x32_f16 v[26:29], a[164:167], v[2:5], 0\n" \
  "s_waitcnt lgkmcnt(1)\n" \
  "v_mfma_f32_16x16x32_f16 v[2:5], a[160:163], v[6:9], 0\n" \
  "v_mfma_f32_16x16x32_f16 v[30:33], a[164:167], v[6:9], 0\n" \
  "s_waitcnt lgkmcnt(0)\n" \
  "v_mfma_f32_16x16x32_f16 v[6:9], a[160:163], v[10:13], 0\n" \
  "v_mfma_f32_16x16x32_f16 v[34:37], a[164:167], v[10:13], 0\n" \
  "ds_read_b128 v[10:13], v255 offset:0\n" \
  "ds_read_b128 v[42:45], v242 offset:4096\n" \
  "v_cvt_pk_f16_f32 v46, v14, v26\n" \
  "v_cvt_pk_f16_f32 v47, v15, v27\n" \
  "v_cvt_pk_f16_f32 v48, v16, v28\n" \
  "v_cvt_pk_f16_f32 v49, v17, v29\n" \
  "v_and_b32 v46, -2, v46\n" \
  "buffer_store_dwordx4 v[46:49], v231, s[36:39], s8 offen offset:0 sc1\n" \
  "v_cvt_pk_f16_f32 v14, v2, v30\n" \
  "v_cvt_pk_f16_f32 v15, v3, v31\n" \
  "v_cvt_pk_f16_f32 v16, v4, v32\n" \
  "v_cvt_pk_f16_f32 v17, v5, v33\n" \
  "v_and_b32 v14, -2, v14\n" \
  "buffer_store_dwordx4 v[14:17], v231, s[36:39], s32 offen offset:0 sc1\n" \
  "v_cvt_pk_f16_f32 v2, v6, v34\n" \
  "v_cvt_pk_f16_f32 v3, v7, v35\n" \
  "v_cvt_pk_f16_f32 v4, v8, v36\n" \
  "v_cvt_pk_f16_f32 v5, v9, v37\n" \
  "v_and_b32 v2, -2, v2\n" \
  "s_mov_b64 exec, s[92:93]\n" \
  "buffer_store_dwordx4 v[2:5], v231, s[36:39], s33 offen offset:0 sc1\n" \
  "s_mov_b64 exec, -1\n" \
  "s_waitcnt lgkmcnt(1)\n" \
  "buffer_store_dwordx4 v[10:13], v252, s[36:39], s24 offen offset:0 sc1\n" \
  "s_waitcnt lgkmcnt(0)\n" \
  "buffer_store_dwordx4 v[42:45], v252, s[36:39], s24 offen offset:1024 sc1\n" \
  "s_movk_i32 s32, 4096\n" \
  "buffer_load_dwordx4 v[2:5], v252, s[36:39], s32 offen offset:0 sc1\n" \
  "buffer_load_dwordx4 v[6:9], v252, s[36:39], s32 offen offset:1024 sc1\n" \
  "s_add_u32 s10, s87, 7405568\n" \
  "s_lshl_b32 s98, s89, 16\n" \
  "s_lshl_b32 s100, s20, 14\n" \
  "s_add_u32 s98, s98, s100\n" \
  "s_add_u32 s14, s98, 5308416\n" \
  "s_mul_i32 s98, s88, 4505600\n" \
  "s_lshl_b32 s100, s90, 2\n" \
  "s_lshr_b32 s101, s84, 1\n" \
  "s_add_u32 s100, s100, s101\n" \
  "s_lshl_b32 s100, s100, 12\n" \
  "s_add_u32 s98, s98, s100\n" \
  "s_add_u32 s16, s98, 13090816\n" \
  "s_mul_i32 s98, s20, 4896\n" \
  "s_add_u32 s21, s98, 9216\n" \
  "s_mul_i32 s27, s88, 9232\n" \
  "s_lshl_b32 s98, s20, 2\n" \
  "s_add_u32 s28, s98, s28\n" \
  "s_lshl_b32 s98, s84, 8\n" \
  "s_add_u32 s29, s98, 2048\n" \
  "s_lshl_b32 s98, s85, 3\n" \
  "s_add_u32 s30, s98, s29\n" \
  "s_cmp_lt_u32 s20, 2\n" \
  "s_cselect_b32 s9, 8192, 0\n" \
  "s_add_u32 s9, s9, s8\n" \
  "s_lshl_b32 s81, s88, 13\n" \
  "s_sub_u32 s15, s14, s81\n" \
  "s_add_u32 s14, s14, s81\n" \
  "s_mul_i32 s98, s84, 9\n" \
  "s_add_u32 s81, s98, s85\n" \
  "s_lshl_b32 s98, s81, 7\n" \
  "s_add_u32 s25, s98, 35840\n" \
  "s_cmp_lt_u32 s2, 72\n" \
  "s_cselect_b32 s66, 3981312, 0\n" \
  "s_lshl_b32 s33, s20, 10\n" \
  "s_lshl_b32 s98, s81, 6\n" \
  "s_add_u32 s63, s98, 2304\n" \
  "s_barrier\n" \
  "s_add_u32 m0, s33, 0\n" \
  "s_add_u32 s32, s63, 0\n" \
  "buffer_load_dwordx4 v245, s[64:67], s32 offen offset:0 lds\n" \
  "s_add_u32 m0, s33, 4096\n" \
  "s_add_u32 s32, s63, 442368\n" \
  "buffer_load_dwordx4 v245, s[64:67], s32 offen offset:0 lds\n" \
  "s_add_u32 m0, s33, 8192\n" \
  "s_add_u32 s32, s63, 884736\n" \
  "buffer_load_dwordx4 v245, s[64:67], s32 offen offset:0 lds\n" \
  "s_add_u32 m0, s33, 12288\n" \
  "s_add_u32 s32, s63, 1327104\n" \
  "buffer_load_dwordx4 v245, s[64:67], s32 offen offset:0 lds\n" \
  "s_add_u32 m0, s33, 16384\n" \
  "s_add_u32 s32, s63, 1769472\n" \
  "buffer_load_dwordx4 v245, s[64:67], s32 offen offset:0 lds\n" \
  "s_add_u32 m0, s33, 20480\n" \
  "s_add_u32 s32, s63, 2211840\n" \
  "buffer_load_dwordx4 v245, s[64:67], s32 offen offset:0 lds\n" \
  "s_add_u32 m0, s33, 24576\n" \
  "s_add_u32 s32, s63, 2654208\n" \
  "buffer_load_dwordx4 v245, s[64:67], s32 offen offset:0 lds\n" \
  "s_add_u32 m0, s33, 28672\n" \
  "s_add_u32 s32, s63, 3096576\n" \
  "buffer_load_dwordx4 v245, s[64:67], s32 offen offset:0 lds\n" \
  "s_add_u32 m0, s33, 32768\n" \
  "s_add_u32 s32, s63, 3538944\n" \
  "buffer_load_dwordx4 v245, s[64:67], s32 offen offset:0 lds\n" \
  "v_and_b32 v255, 1, v191\n" \
  "v_lshl_add_u32 v249, v253, 1, v255\n" \
  "v_lshlrev_b32 v255, 3, v249\n" \
  "v_and_b32 v249, 7, v191\n" \
  "v_lshlrev_b32 v241, 1, v249\n" \
  "v_and_b32 v249, 12, v191\n" \
  "v_add_u32 v245, v249, v244\n" \
  "s_lshl_b32 s98, s20, 4\n" \
  "v_add_u32 v249, s98, v245\n" \
  "v_lshlrev_b32 v245, 1, v249\n" \
  "v_bfe_u32 v249, v191, 1, 1\n" \
  "v_lshrrev_b32 v242, 2, v191\n" \
  "v_and_b32 v240, 12, v242\n" \
  "v_lshl_add_u32 v242, v249, 6, v240\n" \
  "s_lshr_b32 s98, s84, 1\n" \
  "s_lshl_b32 s98, s98, 4\n" \
  "s_add_u32 s98, s98, s20\n" \
  "v_add_u32 v249, s98, v242\n" \
  "v_lshlrev_b32 v242, 1, v249\n" \
  "s_mul_i32 s98, s84, 1152\n" \
  "s_and_b32 s100, s85, 7\n" \
  "s_lshl_b32 s100, s100, 7\n" \
  "s_add_u32 s98, s98, s100\n" \
  "s_lshl_b32 s100, s20, 5\n" \
  "s_add_u32 s60, s98, s100\n" \
  "s_mul_i32 s98, s84, 1152\n" \
  "s_and_b32 s100, s85, 6\n" \
  "s_lshl_b32 s100, s100, 4\n" \
  "s_add_u32 s98, s98, s100\n" \
  "s_lshr_b32 s100, s20, 1\n" \
  "s_lshl_b32 s100, s100, 4\n" \
  "s_add_u32 s98, s98, s100\n" \
  "s_add_u32 s61, s98, 1024\n" \
  "s_lshl_b32 s62, s90, 8\n" \
  "buffer_load_dwordx2 v[16:17], v255, s[68:71], s60 offen offset:0\n" \
  "buffer_load_ushort v255, v241, s[68:71], s61 offen offset:0\n" \
  "buffer_load_ushort v249, v245, s[72:75], s62 offen offset:0\n" \
  "buffer_load_ushort v241, v245, s[72:75], s62 offen offset:128\n" \
  "buffer_load_ushort v245, v242, s[72:75], s62 offen offset:0\n" \
  "s_add_u32 s98, s84, 1\n" \
  "s_lshr_b32 s98, s98, 3\n" \
  "s_sub_u32 s48, 14, s98\n" \
  "s_lshr_b32 s98, s4, 7\n" \
  "s_sub_u32 s98, 3, s98\n" \
  "s_sub_u32 s98, s98, s84\n" \
  "s_and_b32 s49, s98, 7\n" \
  "v_lshrrev_b32 v242, 5, v191\n" \
  "v_and_b32 v240, 31, v191\n" \
  "v_lshl_add_u32 v239, v242, 6, v240\n" \
  "v_mul_u32_u24 v242, 9216, v239\n" \
  "s_mul_i32 s98, s20, s48\n" \
  "s_add_u32 s50, s98, s85\n" \
  "s_mul_i32 s98, s50, 205\n" \
  "s_lshr_b32 s51, s98, 10\n" \
  "s_mul_i32 s98, s51, 5\n" \
  "s_sub_u32 s52, s50, s98\n" \
  "s_lshr_b32 s53, s50, 2\n" \
  "s_and_b32 s54, s50, 3\n" \
  "s_cmp_eq_u32 s84, 7\n" \
  "s_cselect_b32 s51, s51, s53\n" \
  "s_cselect_b32 s52, s52, s54\n" \
  "s_sub_u32 s53, 9, s52\n" \
  "s_cmp_lt_u32 s52, 2\n" \
  "s_cselect_b32 s53, s49, s53\n" \
  "s_cmp_eq_u32 s52, 0\n" \
  "s_cselect_b32 s54, 0, 294912\n" \
  "s_cmp_lt_u32 s51, 16\n" \
  "s_cselect_b64 s[68:69], -1, 0\n" \
  "s_mul_i32 s98, s51, 1179648\n" \
  "s_lshl_b32 s100, s53, 7\n" \
  "s_add_u32 s98, s98, s100\n" \
  "s_mul_i32 s100, s84, 1152\n" \
  "s_add_u32 s98, s98, s100\n" \
  "s_add_u32 s72, s98, s54\n" \
  "s_add_u32 s98, s20, 4\n" \
  "s_mul_i32 s98, s98, s48\n" \
  "s_add_u32 s50, s98, s85\n" \
  "s_mul_i32 s98, s50, 205\n" \
  "s_lshr_b32 s51, s98, 10\n" \
  "s_mul_i32 s98, s51, 5\n" \
  "s_sub_u32 s52, s50, s98\n" \
  "s_lshr_b32 s53, s50, 2\n" \
  "s_and_b32 s54, s50, 3\n" \
  "s_cmp_eq_u32 s84, 7\n" \
  "s_cselect_b32 s51, s51, s53\n" \
  "s_cselect_b32 s52, s52, s54\n" \
  "s_sub_u32 s53, 9, s52\n" \
  "s_cmp_lt_u32 s52, 2\n" \
  "s_cselect_b32 s53, s49, s53\n" \
  "s_cmp_eq_u32 s52, 0\n" \
  "s_cselect_b32 s54, 0, 294912\n" \
  "s_cmp_lt_u32 s51, 16\n" \
  "s_cselect_b64 s[70:71], -1, 0\n" \
  "s_mul_i32 s98, s51, 1179648\n" \
  "s_lshl_b32 s100, s53, 7\n" \
  "s_add_u32 s98, s98, s100\n" \
  "s_mul_i32 s100, s84, 1152\n" \
  "s_add_u32 s98, s98, s100\n" \
  "s_add_u32 s73, s98, s54\n" \
  "s_mov_b32 m0, 162176\n" \
  "s_mov_b64 exec, s[68:69]\n" \
  "buffer_load_dword v242, s[4:7], s72 offen lds\n" \
  "s_mov_b64 exec, s[70:71]\n" \
  "buffer_load_dword v242, s[4:7], s73 offen lds\n" \
  "s_mov_b64 exec, -1\n" \
  "s_add_u32 m0, s57, 8192\n" \
  "s_add_u32 s31, s12, 73728\n" \
  "buffer_load_dwordx4 v38, s[4:7], s31 offen offset:0 lds\n" \
  "buffer_load_dwordx4 v39, s[4:7], s31 offen offset:1024 lds\n" \
  "buffer_load_dwordx4 v40, s[4:7], s31 offen offset:2048 lds\n" \
  "buffer_load_dwordx4 v41, s[4:7], s31 offen offset:3072 lds\n" \
  "v_and_b32 v242, 3, v191\n" \
  "v_bfe_u32 v240, v191, 2, 2\n" \
  "v_lshl_add_u32 v239, v244, 3, v240\n" \
  "s_lshr_b32 s98, s20, 1\n" \
  "s_mul_i32 s98, s98, 18432\n" \
  "s_and_b32 s100, s20, 1\n" \
  "s_lshl_b32 s100, s100, 5\n" \
  "s_add_u32 s98, s98, s100\n" \
  "v_lshl_add_u32 v240, v239, 6, s98\n" \
  "v_lshl_add_u32 v239, v242, 3, v240\n" \
  "s_lshr_b32 s98, s20, 1\n" \
  "s_mul_i32 s98, s98, 576\n" \
  "s_add_u32 s98, s98, 162432\n" \
  "v_lshl_add_u32 v242, v244, 4, s98\n" \
  "s_lshl_b32 s98, s20, 6\n" \
  "s_add_u32 s98, s98, 161152\n" \
  "v_lshl_add_u32 v244, v246, 2, s98\n" \
  "v_and_b32 v246, 7, v191\n" \
  "v_lshlrev_b32 v240, 4, v246\n" \
  "v_add_u32 v246, 161152, v240\n" \
  "v_lshlrev_b32 v240, 5, v191\n" \
  "v_lshlrev_b32 v238, 2, v191\n" \
  "v_and_b32 v240, 1920, v240\n" \
  "v_and_b32 v238, 96, v238\n" \
  "v_add3_u32 v240, v240, v238, s57\n" \
  "v_and_b32 v238, 3, v191\n" \
  "v_lshl_add_u32 v26, v238, 3, v240\n" \
  "v_xor_b32 v27, 32, v26\n" \
  "v_xor_b32 v28, 64, v26\n" \
  "v_xor_b32 v29, 96, v26\n" \
  "ds_read_b128 v[10:13], v242 offset:0\n" \
  "ds_read_b128 v[30:33], v242 offset:64\n" \
  "ds_read_b128 v[34:37], v242 offset:128\n" \
  "ds_read_b128 v[42:45], v242 offset:192\n" \
  "ds_read_b128 v[46:49], v242 offset:256\n" \
  "ds_read_b128 v[50:53], v242 offset:320\n" \
  "ds_read_b128 v[54:57], v242 offset:384\n" \
  "ds_read_b128 v[58:61], v242 offset:448\n" \
  "ds_read_b128 v[62:65], v242 offset:512\n" \
  "s_waitcnt vmcnt(11)\n" \
  "s_barrier\n" \
  "ds_read_b64_tr_b16 v[66:67], v239 offset:0\n" \
  "ds_read_b64_tr_b16 v[68:69], v239 offset:256\n" \
  "ds_read_b64_tr_b16 v[70:71], v239 offset:2048\n" \
  "ds_read_b64_tr_b16 v[72:73], v239 offset:2304\n" \
  "ds_read_b64_tr_b16 v[74:75], v239 offset:4096\n" \
  "ds_read_b64_tr_b16 v[76:77], v239 offset:4352\n" \
  "ds_read_b64_tr_b16 v[78:79], v239 offset:6144\n" \
  "ds_read_b64_tr_b16 v[80:81], v239 offset:6400\n" \
  "ds_read_b64_tr_b16 v[108:109], v239 offset:8192\n" \
  "ds_read_b64_tr_b16 v[110:111], v239 offset:8448\n" \
  "ds_read_b64_tr_b16 v[112:113], v239 offset:10240\n" \
  "ds_read_b64_tr_b16 v[114:115], v239 offset:10496\n" \
  "ds_read_b64_tr_b16 v[116:117], v239 offset:12288\n" \
  "ds_read_b64_tr_b16 v[118:119], v239 offset:12544\n" \
  "ds_read_b64_tr_b16 v[120:121], v239 offset:14336\n" \
  "ds_read_b64_tr_b16 v[122:123], v239 offset:14592\n" \
  "ds_read_b64_tr_b16 v[124:125], v239 offset:16384\n" \
  "ds_read_b64_tr_b16 v[126:127], v239 offset:16640\n" \
  "s_waitcnt lgkmcnt(15)\n" \
  "v_mfma_f32_16x16x32_bf16 v[128:131], v[10:13], v[66:69], 0\n" \
  "s_waitcnt lgkmcnt(14)\n" \
  "v_mfma_f32_16x16x32_bf16 v[128:131], v[30:33], v[70:73], v[128:131]\n" \
  "s_waitcnt lgkmcnt(12)\n" \
  "v_mfma_f32_16x16x32_bf16 v[128:131], v[34:37], v[74:77], v[128:131]\n" \
  "s_waitcnt lgkmcnt(10)\n" \
  "v_mfma_f32_16x16x32_bf16 v[128:131], v[42:45], v[78:81], v[128:131]\n" \
  "s_waitcnt lgkmcnt(8)\n" \
  "v_mfma_f32_16x16x32_bf16 v[128:131], v[46:49], v[108:111], v[128:131]\n" \
  "s_waitcnt lgkmcnt(6)\n" \
  "v_mfma_f32_16x16x32_bf16 v[128:131], v[50:53], v[112:115], v[128:131]\n" \
  "s_waitcnt lgkmcnt(4)\n" \
  "v_mfma_f32_16x16x32_bf16 v[128:131], v[54:57], v[116:119], v[128:131]\n" \
  "s_waitcnt lgkmcnt(2)\n" \
  "v_mfma_f32_16x16x32_bf16 v[128:131], v[58:61], v[120:123], v[128:131]\n" \
  "s_waitcnt lgkmcnt(0)\n" \
  "v_mfma_f32_16x16x32_bf16 v[128:131], v[62:65], v[124:127], v[128:131]\n" \
  "v_mov_b32 v10, s18\n" \
  "v_mov_b32 v11, s19\n" \
  "s_lshr_b32 s32, s84, 1\n" \
  "s_cmp_eq_u32 s32, 2\n" \
  "s_cselect_b32 s32, 1065353216, 0\n" \
  "s_cmp_lt_u32 s2, 72\n" \
  "s_cselect_b64 s[96:97], 255, 0\n" \
  "s_and_b64 s[96:97], s[96:97], s[58:59]\n" \
  "ds_write_b32 v244, v128 offset:0\n" \
  "s_waitcnt lgkmcnt(0)\n" \
  "s_barrier\n" \
  "ds_read_b128 v[30:33], v246 offset:0\n" \
  "ds_read_b128 v[34:37], v246 offset:128\n" \
  "s_waitcnt lgkmcnt(0)\n" \
  "v_pk_add_f32 v[30:31], v[30:31], v[34:35]\n" \
  "v_pk_add_f32 v[32:33], v[32:33], v[36:37]\n" \
  "v_cvt_pk_bf16_f32 v12, v30, v31\n" \
  "v_cvt_pk_bf16_f32 v13, v32, v33\n" \
  "v_lshlrev_b32 v30, 16, v12\n" \
  "v_and_b32 v31, 4294901760, v12\n" \
  "v_lshlrev_b32 v32, 16, v13\n" \
  "v_and_b32 v33, 4294901760, v13\n" \
  "v_add_f32 v30, s32, v30\n" \
  "v_add_f32 v31, s32, v31\n" \
  "v_add_f32 v32, s32, v32\n" \
  "v_add_f32 v33, s32, v33\n" \
  "v_cvt_pk_bf16_f32 v12, v30, v31\n" \
  "v_cvt_pk_bf16_f32 v13, v32, v33\n" \
  "s_mov_b64 exec, s[96:97]\n" \
  "buffer_store_dwordx4 v[10:13], v252, s[36:39], s25 offen offset:0 sc1\n" \
  "s_cmp_eq_u32 s20, 3\n" \
  "s_cselect_b64 exec, 1, 0\n" \
  "buffer_store_dwordx2 v[10:11], off, s[36:39], s30 sc1\n" \
  "s_mov_b64 exec, -1\n" \
  "ds_read_b64_tr_b16 a[0:1], v26 offset:0\n" \
  "ds_read_b64_tr_b16 a[4:5], v27 offset:0\n" \
  "ds_read_b64_tr_b16 a[8:9], v28 offset:0\n" \
  "ds_read_b64_tr_b16 a[12:13], v29 offset:0\n" \
  "ds_read_b64_tr_b16 a[20:21], v26 offset:2048\n" \
  "ds_read_b64_tr_b16 a[24:25], v27 offset:2048\n" \
  "ds_read_b64_tr_b16 a[28:29], v28 offset:2048\n" \
  "ds_read_b64_tr_b16 a[32:33], v29 offset:2048\n" \
  "ds_read_b64_tr_b16 a[2:3], v26 offset:4096\n" \
  "ds_read_b64_tr_b16 a[6:7], v27 offset:4096\n" \
  "ds_read_b64_tr_b16 a[10:11], v28 offset:4096\n" \
  "ds_read_b64_tr_b16 a[14:15], v29 offset:4096\n" \
  "ds_read_b64_tr_b16 a[22:23], v26 offset:6144\n" \
  "ds_read_b64_tr_b16 a[26:27], v27 offset:6144\n" \
  "ds_read_b64_tr_b16 a[30:31], v28 offset:6144\n" \
  "ds_read_b64_tr_b16 a[34:35], v29 offset:6144\n" \
  "s_add_u32 m0, s57, 12288\n" \
  "s_add_u32 s31, s12, 9510912\n" \
  "buffer_load_dwordx4 v38, s[4:7], s31 offen offset:0 lds\n" \
  "buffer_load_dwordx4 v39, s[4:7], s31 offen offset:1024 lds\n" \
  "buffer_load_dwordx4 v40, s[4:7], s31 offen offset:2048 lds\n" \
  "buffer_load_dwordx4 v41, s[4:7], s31 offen offset:3072 lds\n" \
  "s_add_u32 m0, s57, 16384\n" \
  "s_add_u32 s31, s12, 147456\n" \
  "buffer_load_dwordx4 v38, s[4:7], s31 offen offset:0 lds\n" \
  "buffer_load_dwordx4 v39, s[4:7], s31 offen offset:1024 lds\n" \
  "buffer_load_dwordx4 v40, s[4:7], s31 offen offset:2048 lds\n" \
  "buffer_load_dwordx4 v41, s[4:7], s31 offen offset:3072 lds\n" \
  "s_add_u32 m0, s57, 20480\n" \
  "s_add_u32 s31, s12, 9584640\n" \
  "buffer_load_dwordx4 v38, s[4:7], s31 offen offset:0 lds\n" \
  "buffer_load_dwordx4 v39, s[4:7], s31 offen offset:1024 lds\n" \
  "buffer_load_dwordx4 v40, s[4:7], s31 offen offset:2048 lds\n" \
  "buffer_load_dwordx4 v41, s[4:7], s31 offen offset:3072 lds\n" \
  "s_add_u32 m0, s57, 0\n" \
  "s_add_u32 s31, s12, 221184\n" \
  "s_waitcnt lgkmcnt(0)\n" \
  "buffer_load_dwordx4 v38, s[4:7], s31 offen offset:0 lds\n" \
  "buffer_load_dwordx4 v39, s[4:7], s31 offen offset:1024 lds\n" \
  "buffer_load_dwordx4 v40, s[4:7], s31 offen offset:2048 lds\n" \
  "buffer_load_dwordx4 v41, s[4:7], s31 offen offset:3072 lds\n" \
  "s_add_u32 m0, s57, 4096\n" \
  "s_add_u32 s31, s12, 9658368\n" \
  "buffer_load_dwordx4 v38, s[4:7], s31 offen offset:0 lds\n" \
  "buffer_load_dwordx4 v39, s[4:7], s31 offen offset:1024 lds\n" \
  "buffer_load_dwordx4 v40, s[4:7], s31 offen offset:2048 lds\n" \
  "buffer_load_dwordx4 v41, s[4:7], s31 offen offset:3072 lds\n" \
  ".Lync_%=:\n" \
  "v_xor_b32 v246, s18, v2\n" \
  "v_bitop3_b32 v246, v4, s18, v246 bitop3:0xbe\n" \
  "v_bitop3_b32 v246, v6, s18, v246 bitop3:0xbe\n" \
  "v_bitop3_b32 v246, v8, s18, v246 bitop3:0xbe\n" \
  "v_xor_b32 v244, s19, v3\n" \
  "v_bitop3_b32 v244, v5, s19, v244 bitop3:0xbe\n" \
  "v_bitop3_b32 v244, v7, s19, v244 bitop3:0xbe\n" \
  "v_bitop3_b32 v244, v9, s19, v244 bitop3:0xbe\n" \
  "v_lshl_or_b32 v246, v244, 8, v246\n" \
  "v_cmp_ne_u32 vcc, 0, v246\n" \
  "s_cbranch_vccnz .Lyns_%=\n" \
  ".Lynd_%=:\n" \
  "s_or_b32 s32, s2, s20\n" \
  "s_cmp_eq_u32 s32, 0\n" \
  "s_cselect_b64 exec, 1, 0\n" \
  "buffer_store_dwordx2 v[10:11], off, s[36:39], 0 sc1\n" \
  "s_mov_b64 exec, -1\n" \
  "v_max3_u32 v246, v3, v5, v7\n" \
  "v_max_u32 v246, v246, v9\n" \
  "s_nop 1\n" \
  "v_max_u32_dpp v246, v246, v246 quad_perm:[1,0,3,2] row_mask:0xf bank_mask:0xf\n" \
  "s_nop 1\n" \
  "v_max_u32_dpp v246, v246, v246 quad_perm:[2,3,0,1] row_mask:0xf bank_mask:0xf\n" \
  "s_nop 1\n" \
  "v_max_u32_dpp v246, v246, v246 row_half_mirror row_mask:0xf bank_mask:0xf\n" \
  "s_nop 1\n" \
  "v_max_u32_dpp v246, v246, v246 row_mirror row_mask:0xf bank_mask:0xf\n" \
  "s_nop 0\n" \
  "v_readlane_b32 s48, v246, 0\n" \
  "v_readlane_b32 s49, v246, 16\n" \
  "v_readlane_b32 s50, v246, 32\n" \
  "v_readlane_b32 s51, v246, 48\n" \
  "s_max_u32 s48, s48, s49\n" \
  "s_max_u32 s50, s50, s51\n" \
  "s_max_u32 s48, s48, s50\n" \
  "s_lshr_b32 s48, s48, 24\n" \
  "s_max_u32 s48, s48, 64\n" \
  "s_min_u32 s48, s48, 250\n" \
  "s_add_u32 s23, s26, s48\n" \
  "s_sub_u32 s23, s23, 253\n" \
  "v_lshlrev_b32 v2, 16, v18\n" \
  "v_and_b32 v3, 4294901760, v18\n" \
  "v_lshlrev_b32 v4, 16, v19\n" \
  "v_and_b32 v5, 4294901760, v19\n" \
  "v_lshlrev_b32 v6, 16, v20\n" \
  "v_and_b32 v7, 4294901760, v20\n" \
  "v_lshlrev_b32 v8, 16, v21\n" \
  "v_and_b32 v9, 4294901760, v21\n" \
  "v_lshlrev_b32 v10, 16, v22\n" \
  "v_and_b32 v11, 4294901760, v22\n" \
  "v_lshlrev_b32 v12, 16, v23\n" \
  "v_and_b32 v13, 4294901760, v23\n" \
  "v_lshlrev_b32 v14, 16, v24\n" \
  "v_and_b32 v15, 4294901760, v24\n" \
  "v_lshlrev_b32 v18, 16, v25\n" \
  "v_and_b32 v19, 4294901760, v25\n" \
  "v_lshlrev_b32 v20, 16, v247\n" \
  "v_and_b32 v21, 4294901760, v247\n" \
  "v_and_b32 v30, 65535, v86\n" \
  "v_add_u32 v30, s21, v30\n" \
  "v_and_b32 v31, 65535, v87\n" \
  "v_add_u32 v31, s21, v31\n" \
  "v_and_b32 v32, 65535, v88\n" \
  "v_add_u32 v32, s21, v32\n" \
  "v_and_b32 v33, 65535, v89\n" \
  "v_add_u32 v33, s21, v33\n" \
  "v_lshrrev_b32 v34, 16, v86\n" \
  "v_add_u32 v34, s21, v34\n" \
  "v_lshrrev_b32 v35, 16, v87\n" \
  "v_add_u32 v35, s21, v35\n" \
  "v_lshrrev_b32 v36, 16, v88\n" \
  "v_add_u32 v36, s21, v36\n" \
  "v_lshrrev_b32 v37, 16, v89\n" \
  "v_add_u32 v37, s21, v37\n" \
  "v_mul_u32_u24 v247, 2448, v253\n" \
  "v_add_u32 v253, s21, v247\n" \
  "v_lshl_add_u32 v247, v254, 4, v253\n" \
  "v_lshl_add_u32 v246, v254, 2, v253\n" \
  "v_lshrrev_b32 v253, 3, v254\n" \
  "v_lshl_add_u32 v244, v253, 4, v247\n" \
  "v_lshrrev_b32 v253, 4, v191\n" \
  "v_lshl_add_u32 v242, v253, 2, s20\n" \
  "v_and_b32 v253, 3, v191\n" \
  "v_bfe_u32 v240, v191, 2, 2\n" \
  "v_mul_u32_u24 v239, 4896, v240\n" \
  "v_lshl_add_u32 v240, v253, 3, v239\n" \
  "v_lshl_add_u32 v253, v242, 5, v240\n" \
  "v_and_b32 v240, 15, v191\n" \
  "v_lshlrev_b32 v239, 4, v240\n" \
  "v_lshl_add_u32 v240, v242, 16, v239\n" \
  "v_lshlrev_b32 v242, 3, v254\n" \
  "s_lshr_b32 s98, s27, 8\n" \
  "s_and_b32 s98, s98, 255\n" \
  "v_subrev_u32 v239, s98, v0\n" \
  "v_bfrev_b32 v238, 1\n" \
  "v_cmp_gt_u32 s[96:97], 36, v239\n" \
  "v_cndmask_b32 v238, v238, v243, s[94:95]\n" \
  "s_cmp_eq_u32 s20, 1\n" \
  "s_cselect_b32 s92, 65537, s58\n" \
  "s_and_b32 s93, s59, 4294901758\n" \
  "s_waitcnt vmcnt(17)\n" \
  "ds_read_b64_tr_b16 a[40:41], v26 offset:8192\n" \
  "ds_read_b64_tr_b16 a[44:45], v27 offset:8192\n" \
  "ds_read_b64_tr_b16 a[48:49], v28 offset:8192\n" \
  "ds_read_b64_tr_b16 a[52:53], v29 offset:8192\n" \
  "ds_read_b64_tr_b16 a[60:61], v26 offset:10240\n" \
  "ds_read_b64_tr_b16 a[64:65], v27 offset:10240\n" \
  "ds_read_b64_tr_b16 a[68:69], v28 offset:10240\n" \
  "ds_read_b64_tr_b16 a[72:73], v29 offset:10240\n" \
  "ds_read_b64_tr_b16 a[42:43], v26 offset:12288\n" \
  "ds_read_b64_tr_b16 a[46:47], v27 offset:12288\n" \
  "ds_read_b64_tr_b16 a[50:51], v28 offset:12288\n" \
  "ds_read_b64_tr_b16 a[54:55], v29 offset:12288\n" \
  "ds_read_b64_tr_b16 a[62:63], v26 offset:14336\n" \
  "ds_read_b64_tr_b16 a[66:67], v27 offset:14336\n" \
  "ds_read_b64_tr_b16 a[70:71], v28 offset:14336\n" \
  "ds_read_b64_tr_b16 a[74:75], v29 offset:14336\n" \
  "s_add_u32 m0, s57, 8192\n" \
  "s_add_u32 s31, s13, 294912\n" \
  "s_waitcnt lgkmcnt(0)\n" \
  "buffer_load_dwordx4 v38, s[4:7], s31 offen offset:0 lds\n" \
  "buffer_load_dwordx4 v39, s[4:7], s31 offen offset:1024 lds\n" \
  "buffer_load_dwordx4 v40, s[4:7], s31 offen offset:2048 lds\n" \
  "buffer_load_dwordx4 v41, s[4:7], s31 offen offset:3072 lds\n" \
  "s_add_u32 m0, s57, 12288\n" \
  "s_add_u32 s31, s13, 9732096\n" \
  "buffer_load_dwordx4 v38, s[4:7], s31 offen offset:0 lds\n" \
  "buffer_load_dwordx4 v39, s[4:7], s31 offen offset:1024 lds\n" \
  "buffer_load_dwordx4 v40, s[4:7], s31 offen offset:2048 lds\n" \
  "buffer_load_dwordx4 v41, s[4:7], s31 offen offset:3072 lds\n" \
  "s_waitcnt vmcnt(17)\n" \
  "ds_read_b64_tr_b16 a[80:81], v26 offset:16384\n" \
  "ds_read_b64_tr_b16 a[84:85], v27 offset:16384\n" \
  "ds_read_b64_tr_b16 a[88:89], v28 offset:16384\n" \
  "ds_read_b64_tr_b16 a[92:93], v29 offset:16384\n" \
  "ds_read_b64_tr_b16 a[100:101], v26 offset:18432\n" \
  "ds_read_b64_tr_b16 a[104:105], v27 offset:18432\n" \
  "ds_read_b64_tr_b16 a[108:109], v28 offset:18432\n" \
  "ds_read_b64_tr_b16 a[112:113], v29 offset:18432\n" \
  "ds_read_b64_tr_b16 a[82:83], v26 offset:20480\n" \
  "ds_read_b64_tr_b16 a[86:87], v27 offset:20480\n" \
  "ds_read_b64_tr_b16 a[90:91], v28 offset:20480\n" \
  "ds_read_b64_tr_b16 a[94:95], v29 offset:20480\n" \
  "ds_read_b64_tr_b16 a[102:103], v26 offset:22528\n" \
  "ds_read_b64_tr_b16 a[106:107], v27 offset:22528\n" \
  "ds_read_b64_tr_b16 a[110:111], v28 offset:22528\n" \
  "ds_read_b64_tr_b16 a[114:115], v29 offset:22528\n" \
  "s_mov_b32 s32, 35840\n" \
  "buffer_load_dwordx4 v[22:25], v238, s[36:39], s32 offen offset:0 sc1\n" \
  "buffer_load_dwordx4 v[42:45], v238, s[36:39], s32 offen offset:2304 sc1\n" \
  "s_mov_b32 s32, 40448\n" \
  "buffer_load_dwordx4 v[46:49], v238, s[36:39], s32 offen offset:0 sc1\n" \
  "buffer_load_dwordx4 v[50:53], v238, s[36:39], s32 offen offset:2304 sc1\n" \
  "s_add_u32 s32, s8, 4096\n" \
  "buffer_load_dwordx4 v[54:57], v231, s[36:39], s8 offen offset:0 sc1\n" \
  "buffer_load_dwordx4 v[58:61], v231, s[36:39], s32 offen offset:0 sc1\n" \
  "buffer_load_dwordx4 v[62:65], v248, s[36:39], s9 offen offset:0 sc1\n" \
  "buffer_load_dwordx2 v[66:67], v242, s[36:39], s29 offen offset:0 sc1\n" \
  "s_waitcnt lgkmcnt(0)\n" \
  "s_barrier\n" \
  "s_waitcnt vmcnt(4)\n" \
  ".Lmnc_%=:\n" \
  "v_xor_b32 v239, s18, v22\n" \
  "v_bitop3_b32 v239, v23, s19, v239 bitop3:0xbe\n" \
  "v_bitop3_b32 v239, v42, s18, v239 bitop3:0xbe\n" \
  "v_bitop3_b32 v239, v43, s19, v239 bitop3:0xbe\n" \
  "v_bitop3_b32 v239, v46, s18, v239 bitop3:0xbe\n" \
  "v_bitop3_b32 v239, v47, s19, v239 bitop3:0xbe\n" \
  "v_bitop3_b32 v239, v50, s18, v239 bitop3:0xbe\n" \
  "v_bitop3_b32 v239, v51, s19, v239 bitop3:0xbe\n" \
  "v_cmp_ne_u32 vcc, 0, v239\n" \
  "s_and_b64 vcc, vcc, s[94:95]\n" \
  "s_cbranch_scc1 .Lmns_%=\n" \
  ".Lmnd_%=:\n" \
  "v_lshlrev_b32 v68, 16, v24\n" \
  "v_and_b32 v69, 4294901760, v24\n" \
  "v_lshlrev_b32 v70, 16, v25\n" \
  "v_and_b32 v71, 4294901760, v25\n" \
  "v_lshlrev_b32 v22, 16, v44\n" \
  "v_and_b32 v23, 4294901760, v44\n" \
  "v_lshlrev_b32 v24, 16, v45\n" \
  "v_and_b32 v25, 4294901760, v45\n" \
  "v_lshlrev_b32 v42, 16, v48\n" \
  "v_and_b32 v43, 4294901760, v48\n" \
  "v_lshlrev_b32 v44, 16, v49\n" \
  "v_and_b32 v45, 4294901760, v49\n" \
  "v_lshlrev_b32 v46, 16, v52\n" \
  "v_and_b32 v47, 4294901760, v52\n" \
  "v_lshlrev_b32 v48, 16, v53\n" \
  "v_and_b32 v49, 4294901760, v53\n" \
  "v_mul_f32 v68, 1055100473, v68\n" \
  "v_ldexp_f32 v68, v68, s23\n" \
  "v_mul_f32 v69, 1055100473, v69\n" \
  "v_ldexp_f32 v69, v69, s23\n" \
  "v_mul_f32 v70, 1055100473, v70\n" \
  "v_ldexp_f32 v70, v70, s23\n" \
  "v_mul_f32 v71, 1055100473, v71\n" \
  "v_ldexp_f32 v71, v71, s23\n" \
  "s_mov_b64 exec, s[94:95]\n" \
  "ds_write_b128 v243, v[22:25] offset:31104\n" \
  "ds_write_b128 v243, v[42:45] offset:33408\n" \
  "ds_write_b128 v243, v[68:71] offset:28800\n" \
  "s_bfe_u32 s32, s27, 524296\n" \
  "s_lshl_b32 s32, s32, 4\n" \
  "s_sub_u32 s32, 163200, s32\n" \
  "v_add_u32 v239, s32, v243\n" \
  "s_mov_b64 exec, s[96:97]\n" \
  "ds_write_b128 v239, v[46:49] offset:0\n" \
  "s_mov_b64 exec, -1\n" \
  "s_waitcnt vmcnt(1)\n" \
  ".Lrnc_%=:\n" \
  "v_bitop3_b32 v243, v54, v58, v62 bitop3:0x80\n" \
  "v_and_b32 v243, 1, v243\n" \
  "v_cmp_eq_u32 vcc, 0, v243\n" \
  "s_cbranch_vccnz .Lrns_%=\n" \
  ".Lrnd_%=:\n" \
  "v_mfma_f32_16x16x32_f16 v[22:25], a[228:231], v[54:57], 0\n" \
  "s_add_u32 m0, s57, 16384\n" \
  "s_add_u32 s31, s13, 368640\n" \
  "buffer_load_dwordx4 v38, s[4:7], s31 offen offset:0 lds\n" \
  "v_mfma_f32_16x16x32_f16 v[42:45], a[232:235], v[54:57], 0\n" \
  "buffer_load_dwordx4 v39, s[4:7], s31 offen offset:1024 lds\n" \
  "v_mfma_f32_16x16x32_f16 v[46:49], a[228:231], v[58:61], 0\n" \
  "buffer_load_dwordx4 v40, s[4:7], s31 offen offset:2048 lds\n" \
  "v_mfma_f32_16x16x32_f16 v[50:53], a[232:235], v[58:61], 0\n" \
  "buffer_load_dwordx4 v41, s[4:7], s31 offen offset:3072 lds\n" \
  "v_mfma_f32_16x16x32_f16 v[54:57], a[228:231], v[62:65], 0\n" \
  "s_add_u32 m0, s57, 20480\n" \
  "s_add_u32 s31, s13, 9805824\n" \
  "buffer_load_dwordx4 v38, s[4:7], s31 offen offset:0 lds\n" \
  "v_mfma_f32_16x16x32_f16 v[58:61], a[232:235], v[62:65], 0\n" \
  "buffer_load_dwordx4 v39, s[4:7], s31 offen offset:1024 lds\n" \
  "v_cvt_pk_f16_f32 v62, v42, v22\n" \
  "v_cvt_pk_f16_f32 v63, v43, v23\n" \
  "v_cvt_pk_f16_f32 v64, v44, v24\n" \
  "v_cvt_pk_f16_f32 v65, v45, v25\n" \
  "ds_write_b128 v83, v[62:65] offset:0\n" \
  "v_cvt_pk_f16_f32 v22, v50, v46\n" \
  "v_cvt_pk_f16_f32 v23, v51, v47\n" \
  "v_cvt_pk_f16_f32 v24, v52, v48\n" \
  "v_cvt_pk_f16_f32 v25, v53, v49\n" \
  "ds_write_b128 v84, v[22:25] offset:0\n" \
  "v_cvt_pk_f16_f32 v22, v58, v54\n" \
  "v_cvt_pk_f16_f32 v23, v59, v55\n" \
  "v_cvt_pk_f16_f32 v24, v60, v56\n" \
  "v_cvt_pk_f16_f32 v25, v61, v57\n" \
  "s_mov_b64 exec, s[92:93]\n" \
  "ds_write_b128 v85, v[22:25] offset:0\n" \
  "s_mov_b64 exec, -1\n" \
  "s_waitcnt lgkmcnt(0)\n" \
  "s_barrier\n" \
  "ds_read_b64 v[22:23], v82 offset:0\n" \
  "ds_read_b64 v[24:25], v82 offset:512\n" \
  "ds_read_b64 v[42:43], v82 offset:1024\n" \
  "ds_read_b64 v[44:45], v82 offset:256\n" \
  "ds_read_b64 v[46:47], v82 offset:768\n" \
  "ds_read_b64 v[48:49], v82 offset:1280\n" \
  "v_lshlrev_b32 v248, 5, v254\n" \
  "buffer_load_dwordx4 v40, s[4:7], s31 offen offset:2048 lds\n" \
  "buffer_load_dwordx4 v41, s[4:7], s31 offen offset:3072 lds\n" \
  "s_waitcnt lgkmcnt(5)\n" \
  "v_mov_b32 v92, v22\n" \
  "s_waitcnt lgkmcnt(4)\n" \
  "v_mov_b32 v93, v24\n" \
  "s_waitcnt lgkmcnt(3)\n" \
  "v_mov_b32 v94, v42\n" \
  "v_mov_b32 v100, v23\n" \
  "v_mov_b32 v101, v25\n" \
  "v_mfma_f32_16x16x32_f16 v[22:25], v[92:95], a[208:211], 0\n" \
  "v_mov_b32 v102, v43\n" \
  "s_waitcnt lgkmcnt(2)\n" \
  "v_mov_b32 v96, v44\n" \
  "v_mfma_f32_16x16x32_f16 v[50:53], v[92:95], a[212:215], 0\n" \
  "s_waitcnt lgkmcnt(1)\n" \
  "v_mov_b32 v97, v46\n" \
  "s_waitcnt lgkmcnt(0)\n" \
  "v_mov_b32 v98, v48\n" \
  "v_mfma_f32_16x16x32_f16 v[54:57], v[100:103], a[208:211], 0\n" \
  "v_mov_b32 v104, v45\n" \
  "v_mov_b32 v105, v47\n" \
  "v_mfma_f32_16x16x32_f16 v[42:45], v[100:103], a[212:215], 0\n" \
  "v_mov_b32 v106, v49\n" \
  "v_mfma_f32_16x16x32_f16 v[46:49], v[96:99], a[208:211], 0\n" \
  "ds_read_b128 v[58:61], v248 offset:31104\n" \
  "ds_read_b128 v[62:65], v248 offset:31120\n" \
  "v_mfma_f32_16x16x32_f16 v[68:71], v[96:99], a[212:215], 0\n" \
  "ds_read_b128 v[72:75], v248 offset:32128\n" \
  "ds_read_b128 v[76:79], v248 offset:32144\n" \
  "v_mfma_f32_16x16x32_f16 v[80:83], v[104:107], a[208:211], 0\n" \
  "ds_read_b64 v[84:85], v242 offset:33152\n" \
  "v_mfma_f32_16x16x32_f16 v[86:89], v[104:107], a[212:215], 0\n" \
  "ds_read_b128 v[90:93], v248 offset:28800\n" \
  "ds_read_b128 v[94:97], v248 offset:28816\n" \
  "ds_read_b128 v[98:101], v248 offset:29824\n" \
  "ds_read_b128 v[102:105], v248 offset:29840\n" \
  "ds_read_b64 v[106:107], v242 offset:30848\n" \
  "v_pk_add_f32 v[108:109], v[22:23], v[46:47]\n" \
  "v_pk_add_f32 v[110:111], v[24:25], v[48:49]\n" \
  "v_pk_add_f32 v[112:113], v[50:51], v[68:69]\n" \
  "v_pk_add_f32 v[114:115], v[52:53], v[70:71]\n" \
  "v_pk_add_f32 v[116:117], v[22:23], v[46:47] neg_lo:[0,1] neg_hi:[0,1]\n" \
  "v_pk_add_f32 v[118:119], v[24:25], v[48:49] neg_lo:[0,1] neg_hi:[0,1]\n" \
  "v_pk_add_f32 v[22:23], v[50:51], v[68:69] neg_lo:[0,1] neg_hi:[0,1]\n" \
  "v_pk_add_f32 v[24:25], v[52:53], v[70:71] neg_lo:[0,1] neg_hi:[0,1]\n" \
  "v_pk_add_f32 v[46:47], v[54:55], v[80:81]\n" \
  "v_pk_add_f32 v[48:49], v[56:57], v[82:83]\n" \
  "v_pk_add_f32 v[50:51], v[42:43], v[86:87]\n" \
  "v_pk_add_f32 v[52:53], v[44:45], v[88:89]\n" \
  "v_pk_add_f32 v[68:69], v[54:55], v[80:81] neg_lo:[0,1] neg_hi:[0,1]\n" \
  "v_pk_add_f32 v[70:71], v[56:57], v[82:83] neg_lo:[0,1] neg_hi:[0,1]\n" \
  "v_pk_add_f32 v[54:55], v[42:43], v[86:87] neg_lo:[0,1] neg_hi:[0,1]\n" \
  "v_pk_add_f32 v[56:57], v[44:45], v[88:89] neg_lo:[0,1] neg_hi:[0,1]\n" \
  "v_cvt_pk_f16_f32 v42, v108, v112\n" \
  "v_cvt_pk_f16_f32 v43, v109, v113\n" \
  "v_cvt_pk_f16_f32 v44, v110, v114\n" \
  "v_cvt_pk_f16_f32 v45, v111, v115\n" \
  "v_cvt_pk_f16_f32 v80, v116, v22\n" \
  "v_cvt_pk_f16_f32 v81, v117, v23\n" \
  "v_mfma_f32_16x16x32_f16 v[86:89], a[200:203], v[42:45], 0\n" \
  "v_cvt_pk_f16_f32 v82, v118, v24\n" \
  "v_cvt_pk_f16_f32 v83, v119, v25\n" \
  "v_mfma_f32_16x16x32_f16 v[22:25], a[204:207], v[42:45], 0\n" \
  "v_cvt_pk_f16_f32 v42, v46, v50\n" \
  "v_cvt_pk_f16_f32 v43, v47, v51\n" \
  "v_mfma_f32_16x16x32_f16 v[108:111], a[220:223], v[80:83], 0\n" \
  "v_cvt_pk_f16_f32 v44, v48, v52\n" \
  "v_cvt_pk_f16_f32 v45, v49, v53\n" \
  "v_mfma_f32_16x16x32_f16 v[46:49], a[224:227], v[80:83], 0\n" \
  "v_cvt_pk_f16_f32 v50, v68, v54\n" \
  "v_cvt_pk_f16_f32 v51, v69, v55\n" \
  "v_mfma_f32_16x16x32_f16 v[80:83], a[200:203], v[42:45], 0\n" \
  "v_cvt_pk_f16_f32 v52, v70, v56\n" \
  "v_cvt_pk_f16_f32 v53, v71, v57\n" \
  "v_mfma_f32_16x16x32_f16 v[54:57], a[204:207], v[42:45], 0\n" \
  "ds_read_b128 v[42:45], v248 offset:33408\n" \
  "v_mfma_f32_16x16x32_f16 v[68:71], a[220:223], v[50:53], 0\n" \
  "ds_read_b128 v[112:115], v248 offset:33424\n" \
  "v_mfma_f32_16x16x32_f16 v[116:119], a[224:227], v[50:53], 0\n" \
  "ds_read_b128 v[50:53], v248 offset:34432\n" \
  "ds_read_b128 v[120:123], v248 offset:34448\n" \
  "ds_read_b64 v[124:125], v242 offset:35456\n" \
  "s_waitcnt lgkmcnt(14)\n" \
  "v_readfirstlane_b32 s32, v58\n" \
  "s_mov_b32 exec_lo, 125241207\n" \
  "s_mov_b32 exec_hi, 125241207\n" \
  "v_cvt_pk_f16_f32 v254, v22, v86\n" \
  "ds_write_b32 v30, v254 offset:0\n" \
  "v_cvt_pk_f16_f32 v254, v23, v87\n" \
  "ds_write_b32 v31, v254 offset:0\n" \
  "v_cvt_pk_f16_f32 v254, v24, v88\n" \
  "ds_write_b32 v32, v254 offset:0\n" \
  "v_cvt_pk_f16_f32 v254, v25, v89\n" \
  "ds_write_b32 v33, v254 offset:0\n" \
  "v_cvt_pk_f16_f32 v254, v46, v108\n" \
  "ds_write_b32 v34, v254 offset:0\n" \
  "v_cvt_pk_f16_f32 v254, v47, v109\n" \
  "ds_write_b32 v35, v254 offset:0\n" \
  "v_cvt_pk_f16_f32 v254, v48, v110\n" \
  "ds_write_b32 v36, v254 offset:0\n" \
  "v_cvt_pk_f16_f32 v254, v49, v111\n" \
  "ds_write_b32 v37, v254 offset:0\n" \
  "v_cvt_pk_f16_f32 v254, v54, v80\n" \
  "ds_write_b32 v30, v254 offset:2448\n" \
  "v_cvt_pk_f16_f32 v254, v55, v81\n" \
  "ds_write_b32 v31, v254 offset:2448\n" \
  "v_cvt_pk_f16_f32 v254, v56, v82\n" \
  "ds_write_b32 v32, v254 offset:2448\n" \
  "v_cvt_pk_f16_f32 v254, v57, v83\n" \
  "ds_write_b32 v33, v254 offset:2448\n" \
  "v_cvt_pk_f16_f32 v254, v116, v68\n" \
  "ds_write_b32 v34, v254 offset:2448\n" \
  "v_cvt_pk_f16_f32 v254, v117, v69\n" \
  "ds_write_b32 v35, v254 offset:2448\n" \
  "v_cvt_pk_f16_f32 v254, v118, v70\n" \
  "ds_write_b32 v36, v254 offset:2448\n" \
  "v_cvt_pk_f16_f32 v254, v119, v71\n" \
  "ds_write_b32 v37, v254 offset:2448\n" \
  "s_mov_b64 exec, -1\n" \
  "ds_read_b128 v[22:25], v244 offset:0\n" \
  "ds_read_b128 v[30:33], v244 offset:576\n" \
  "ds_read_b32 v254, v246 offset:1152\n" \
  "v_mov_b32 v34, s32\n" \
  "v_pk_add_f32 v[58:59], v[58:59], v[34:35] op_sel_hi:[1,0] neg_lo:[0,1] neg_hi:[0,1]\n" \
  "v_pk_add_f32 v[60:61], v[60:61], v[34:35] op_sel_hi:[1,0] neg_lo:[0,1] neg_hi:[0,1]\n" \
  "s_waitcnt lgkmcnt(15)\n" \
  "v_pk_add_f32 v[62:63], v[62:63], v[34:35] op_sel_hi:[1,0] neg_lo:[0,1] neg_hi:[0,1]\n" \
  "v_pk_add_f32 v[64:65], v[64:65], v[34:35] op_sel_hi:[1,0] neg_lo:[0,1] neg_hi:[0,1]\n" \
  "v_pk_add_f32 v[72:73], v[72:73], v[34:35] op_sel_hi:[1,0] neg_lo:[0,1] neg_hi:[0,1]\n" \
  "v_pk_add_f32 v[74:75], v[74:75], v[34:35] op_sel_hi:[1,0] neg_lo:[0,1] neg_hi:[0,1]\n" \
  "v_pk_add_f32 v[76:77], v[76:77], v[34:35] op_sel_hi:[1,0] neg_lo:[0,1] neg_hi:[0,1]\n" \
  "v_pk_add_f32 v[78:79], v[78:79], v[34:35] op_sel_hi:[1,0] neg_lo:[0,1] neg_hi:[0,1]\n" \
  "v_pk_add_f32 v[84:85], v[84:85], v[34:35] op_sel_hi:[1,0] neg_lo:[0,1] neg_hi:[0,1]\n" \
  "s_waitcnt lgkmcnt(2)\n" \
  "v_fma_mix_f32 v2, v22, v90, v2 op_sel:[0,0,0] op_sel_hi:[1,0,0]\n" \
  "v_fma_mix_f32 v3, v22, v91, v3 op_sel:[1,0,0] op_sel_hi:[1,0,0]\n" \
  "v_fma_mix_f32 v4, v23, v92, v4 op_sel:[0,0,0] op_sel_hi:[1,0,0]\n" \
  "v_fma_mix_f32 v5, v23, v93, v5 op_sel:[1,0,0] op_sel_hi:[1,0,0]\n" \
  "v_fma_mix_f32 v6, v24, v94, v6 op_sel:[0,0,0] op_sel_hi:[1,0,0]\n" \
  "v_fma_mix_f32 v7, v24, v95, v7 op_sel:[1,0,0] op_sel_hi:[1,0,0]\n" \
  "v_fma_mix_f32 v8, v25, v96, v8 op_sel:[0,0,0] op_sel_hi:[1,0,0]\n" \
  "v_fma_mix_f32 v9, v25, v97, v9 op_sel:[1,0,0] op_sel_hi:[1,0,0]\n" \
  "s_waitcnt lgkmcnt(1)\n" \
  "v_fma_mix_f32 v10, v30, v98, v10 op_sel:[0,0,0] op_sel_hi:[1,0,0]\n" \
  "v_fma_mix_f32 v11, v30, v99, v11 op_sel:[1,0,0] op_sel_hi:[1,0,0]\n" \
  "v_fma_mix_f32 v12, v31, v100, v12 op_sel:[0,0,0] op_sel_hi:[1,0,0]\n" \
  "v_fma_mix_f32 v13, v31, v101, v13 op_sel:[1,0,0] op_sel_hi:[1,0,0]\n" \
  "v_fma_mix_f32 v14, v32, v102, v14 op_sel:[0,0,0] op_sel_hi:[1,0,0]\n" \
  "v_fma_mix_f32 v15, v32, v103, v15 op_sel:[1,0,0] op_sel_hi:[1,0,0]\n" \
  "v_fma_mix_f32 v18, v33, v104, v18 op_sel:[0,0,0] op_sel_hi:[1,0,0]\n" \
  "v_fma_mix_f32 v19, v33, v105, v19 op_sel:[1,0,0] op_sel_hi:[1,0,0]\n" \
  "s_waitcnt lgkmcnt(0)\n" \
  "v_fma_mix_f32 v20, v254, v106, v20 op_sel:[0,0,0] op_sel_hi:[1,0,0]\n" \
  "v_fma_mix_f32 v21, v254, v107, v21 op_sel:[1,0,0] op_sel_hi:[1,0,0]\n" \
  "v_mov_b32 v23, 925353388\n" \
  "v_mov_b32_dpp v24, v2 row_newbcast:0 row_mask:0xf bank_mask:0xf\n" \
  "v_mov_b32_dpp v254, v2 row_newbcast:0 row_mask:0xf bank_mask:0xf\n" \
  "v_cvt_pk_bf16_f32 v30, v2, v3\n" \
  "v_cvt_pk_bf16_f32 v31, v4, v5\n" \
  "v_cvt_pk_bf16_f32 v32, v6, v7\n" \
  "v_cvt_pk_bf16_f32 v33, v8, v9\n" \
  "v_cvt_pk_bf16_f32 v34, v10, v11\n" \
  "v_cvt_pk_bf16_f32 v35, v12, v13\n" \
  "v_cvt_pk_bf16_f32 v36, v14, v15\n" \
  "v_cvt_pk_bf16_f32 v37, v18, v19\n" \
  "v_cvt_pk_bf16_f32 v248, v20, v21\n" \
  "v_permlane16_swap_b32 v24, v254\n" \
  "v_pk_add_f32 v[2:3], v[2:3], v[24:25] op_sel_hi:[1,0] neg_lo:[0,1] neg_hi:[0,1]\n" \
  "v_pk_add_f32 v[4:5], v[4:5], v[24:25] op_sel_hi:[1,0] neg_lo:[0,1] neg_hi:[0,1]\n" \
  "v_pk_add_f32 v[6:7], v[6:7], v[24:25] op_sel_hi:[1,0] neg_lo:[0,1] neg_hi:[0,1]\n" \
  "v_pk_add_f32 v[8:9], v[8:9], v[24:25] op_sel_hi:[1,0] neg_lo:[0,1] neg_hi:[0,1]\n" \
  "v_pk_add_f32 v[10:11], v[10:11], v[24:25] op_sel_hi:[1,0] neg_lo:[0,1] neg_hi:[0,1]\n" \
  "v_pk_add_f32 v[12:13], v[12:13], v[24:25] op_sel_hi:[1,0] neg_lo:[0,1] neg_hi:[0,1]\n" \
  "v_pk_add_f32 v[14:15], v[14:15], v[24:25] op_sel_hi:[1,0] neg_lo:[0,1] neg_hi:[0,1]\n" \
  "v_pk_add_f32 v[18:19], v[18:19], v[24:25] op_sel_hi:[1,0] neg_lo:[0,1] neg_hi:[0,1]\n" \
  "v_pk_add_f32 v[20:21], v[20:21], v[24:25] op_sel_hi:[1,0] neg_lo:[0,1] neg_hi:[0,1]\n" \
  "v_pk_add_f32 v[24:25], v[2:3], v[4:5]\n" \
  "v_pk_mul_f32 v[46:47], v[2:3], v[2:3]\n" \
  "v_pk_fma_f32 v[46:47], v[4:5], v[4:5], v[46:47]\n" \
  "v_pk_add_f32 v[24:25], v[24:25], v[6:7]\n" \
  "v_pk_fma_f32 v[46:47], v[6:7], v[6:7], v[46:47]\n" \
  "v_pk_add_f32 v[24:25], v[24:25], v[8:9]\n" \
  "v_pk_fma_f32 v[46:47], v[8:9], v[8:9], v[46:47]\n" \
  "v_pk_add_f32 v[24:25], v[24:25], v[10:11]\n" \
  "v_pk_fma_f32 v[46:47], v[10:11], v[10:11], v[46:47]\n" \
  "v_pk_add_f32 v[24:25], v[24:25], v[12:13]\n" \
  "v_pk_fma_f32 v[46:47], v[12:13], v[12:13], v[46:47]\n" \
  "v_pk_add_f32 v[24:25], v[24:25], v[14:15]\n" \
  "v_pk_fma_f32 v[46:47], v[14:15], v[14:15], v[46:47]\n" \
  "v_pk_add_f32 v[24:25], v[24:25], v[18:19]\n" \
  "v_pk_fma_f32 v[46:47], v[18:19], v[18:19], v[46:47]\n" \
  "v_pk_add_f32 v[24:25], v[24:25], v[20:21]\n" \
  "v_pk_fma_f32 v[46:47], v[20:21], v[20:21], v[46:47]\n" \
  "v_add_f32 v254, v24, v25\n" \
  "v_add_f32 v244, v46, v47\n" \
  "ds_read_b64_tr_b16 a[120:121], v26 offset:0\n" \
  "ds_read_b64_tr_b16 a[124:125], v27 offset:0\n" \
  "ds_read_b64_tr_b16 a[128:129], v28 offset:0\n" \
  "ds_read_b64_tr_b16 a[132:133], v29 offset:0\n" \
  "v_permlane16_swap_b32 v254, v244\n" \
  "v_add_f32 v254, v254, v244\n" \
  "buffer_store_dwordx4 v[30:33], v251, s[36:39], s10 offen offset:0 sc1\n" \
  "ds_read_b64_tr_b16 a[140:141], v26 offset:2048\n" \
  "ds_read_b64_tr_b16 a[144:145], v27 offset:2048\n" \
  "ds_read_b64_tr_b16 a[148:149], v28 offset:2048\n" \
  "v_add_f32_dpp v254, v254, v254 quad_perm:[1,0,3,2] row_mask:0xf bank_mask:0xf\n" \
  "buffer_store_dwordx4 v[34:37], v251, s[36:39], s10 offen offset:512 sc1\n" \
  "ds_read_b64_tr_b16 a[152:153], v29 offset:2048\n" \
  "ds_read_b64_tr_b16 a[122:123], v26 offset:4096\n" \
  "ds_read_b64_tr_b16 a[126:127], v27 offset:4096\n" \
  "v_add_f32_dpp v254, v254, v254 quad_perm:[2,3,0,1] row_mask:0xf bank_mask:0xf\n" \
  "buffer_store_dword v248, v250, s[36:39], s10 offen offset:1024 sc1\n" \
  "ds_read_b64_tr_b16 a[130:131], v28 offset:4096\n" \
  "ds_read_b64_tr_b16 a[134:135], v29 offset:4096\n" \
  "ds_read_b64_tr_b16 a[142:143], v26 offset:6144\n" \
  "v_add_f32_dpp v254, v254, v254 row_half_mirror row_mask:0xf bank_mask:0xf\n" \
  "ds_read_b64_tr_b16 a[146:147], v27 offset:6144\n" \
  "ds_read_b64_tr_b16 a[150:151], v28 offset:6144\n" \
  "ds_read_b64_tr_b16 a[154:155], v29 offset:6144\n" \
  "v_add_f32_dpp v254, v254, v254 row_mirror row_mask:0xf bank_mask:0xf\n" \
  "v_mov_b32 v244, v254\n" \
  "ds_read_b64_tr_b16 a[160:161], v26 offset:8192\n" \
  "ds_read_b64_tr_b16 a[164:165], v27 offset:8192\n" \
  "ds_read_b64_tr_b16 a[168:169], v28 offset:8192\n" \
  "ds_read_b64_tr_b16 a[172:173], v29 offset:8192\n" \
  "v_permlane16_swap_b32 v254, v244\n" \
  "v_mul_f32 v254, s35, v254\n" \
  "v_fma_f32 v244, v244, s35, v23\n" \
  "v_fma_f32 v244, -v254, v254, v244\n" \
  "v_max_f32 v244, v244, v23\n" \
  "v_rsq_f32 v22, v244\n" \
  "ds_read_b64_tr_b16 a[180:181], v26 offset:10240\n" \
  "ds_read_b64_tr_b16 a[184:185], v27 offset:10240\n" \
  "ds_read_b64_tr_b16 a[188:189], v28 offset:10240\n" \
  "ds_read_b64_tr_b16 a[192:193], v29 offset:10240\n" \
  "v_mul_f32_e64 v23, -v254, v22\n" \
  "v_pk_fma_f32 v[2:3], v[2:3], v[22:23], v[22:23] op_sel:[0,0,1] op_sel_hi:[1,0,1]\n" \
  "v_pk_fma_f32 v[2:3], v[2:3], v[42:43], v[58:59]\n" \
  "v_pk_fma_f32 v[4:5], v[4:5], v[22:23], v[22:23] op_sel:[0,0,1] op_sel_hi:[1,0,1]\n" \
  "v_pk_fma_f32 v[4:5], v[4:5], v[44:45], v[60:61]\n" \
  "v_pk_fma_f32 v[6:7], v[6:7], v[22:23], v[22:23] op_sel:[0,0,1] op_sel_hi:[1,0,1]\n" \
  "v_pk_fma_f32 v[6:7], v[6:7], v[112:113], v[62:63]\n" \
  "ds_read_b64_tr_b16 a[162:163], v26 offset:12288\n" \
  "ds_read_b64_tr_b16 a[166:167], v27 offset:12288\n" \
  "s_add_u32 m0, s57, 0\n" \
  "s_add_u32 s31, s13, 442368\n" \
  "s_waitcnt lgkmcnt(10)\n" \
  "buffer_load_dwordx4 v38, s[4:7], s31 offen offset:0 lds\n" \
  "v_pk_fma_f32 v[8:9], v[8:9], v[22:23], v[22:23] op_sel:[0,0,1] op_sel_hi:[1,0,1]\n" \
  "v_pk_fma_f32 v[8:9], v[8:9], v[114:115], v[64:65]\n" \
  "v_pk_fma_f32 v[10:11], v[10:11], v[22:23], v[22:23] op_sel:[0,0,1] op_sel_hi:[1,0,1]\n" \
  "v_pk_fma_f32 v[10:11], v[10:11], v[50:51], v[72:73]\n" \
  "ds_read_b64_tr_b16 a[170:171], v28 offset:12288\n" \
  "ds_read_b64_tr_b16 a[174:175], v29 offset:12288\n" \
  "buffer_load_dwordx4 v39, s[4:7], s31 offen offset:1024 lds\n" \
  "v_pk_fma_f32 v[12:13], v[12:13], v[22:23], v[22:23] op_sel:[0,0,1] op_sel_hi:[1,0,1]\n" \
  "v_pk_fma_f32 v[12:13], v[12:13], v[52:53], v[74:75]\n" \
  "v_pk_fma_f32 v[14:15], v[14:15], v[22:23], v[22:23] op_sel:[0,0,1] op_sel_hi:[1,0,1]\n" \
  "v_pk_fma_f32 v[14:15], v[14:15], v[120:121], v[76:77]\n" \
  "ds_read_b64_tr_b16 a[182:183], v26 offset:14336\n" \
  "ds_read_b64_tr_b16 a[186:187], v27 offset:14336\n" \
  "buffer_load_dwordx4 v40, s[4:7], s31 offen offset:2048 lds\n" \
  "v_pk_fma_f32 v[18:19], v[18:19], v[22:23], v[22:23] op_sel:[0,0,1] op_sel_hi:[1,0,1]\n" \
  "v_pk_fma_f32 v[18:19], v[18:19], v[122:123], v[78:79]\n" \
  "v_pk_fma_f32 v[20:21], v[20:21], v[22:23], v[22:23] op_sel:[0,0,1] op_sel_hi:[1,0,1]\n" \
  "v_pk_fma_f32 v[20:21], v[20:21], v[124:125], v[84:85]\n" \
  "ds_read_b64_tr_b16 a[190:191], v28 offset:14336\n" \
  "ds_read_b64_tr_b16 a[194:195], v29 offset:14336\n" \
  "buffer_load_dwordx4 v41, s[4:7], s31 offen offset:3072 lds\n" \
  "v_mov_b32 v23, 925353388\n" \
  "v_pk_add_f32 v[24:25], v[2:3], v[4:5]\n" \
  "v_pk_mul_f32 v[30:31], v[2:3], v[2:3]\n" \
  "v_pk_fma_f32 v[30:31], v[4:5], v[4:5], v[30:31]\n" \
  "v_pk_add_f32 v[24:25], v[24:25], v[6:7]\n" \
  "v_pk_fma_f32 v[30:31], v[6:7], v[6:7], v[30:31]\n" \
  "v_pk_add_f32 v[24:25], v[24:25], v[8:9]\n" \
  "v_pk_fma_f32 v[30:31], v[8:9], v[8:9], v[30:31]\n" \
  "v_pk_add_f32 v[24:25], v[24:25], v[10:11]\n" \
  "v_pk_fma_f32 v[30:31], v[10:11], v[10:11], v[30:31]\n" \
  "v_pk_add_f32 v[24:25], v[24:25], v[12:13]\n" \
  "v_pk_fma_f32 v[30:31], v[12:13], v[12:13], v[30:31]\n" \
  "v_pk_add_f32 v[24:25], v[24:25], v[14:15]\n" \
  "v_pk_fma_f32 v[30:31], v[14:15], v[14:15], v[30:31]\n" \
  "v_pk_add_f32 v[24:25], v[24:25], v[18:19]\n" \
  "v_pk_fma_f32 v[30:31], v[18:19], v[18:19], v[30:31]\n" \
  "v_pk_add_f32 v[24:25], v[24:25], v[20:21]\n" \
  "v_pk_fma_f32 v[30:31], v[20:21], v[20:21], v[30:31]\n" \
  "v_add_f32 v254, v24, v25\n" \
  "v_add_f32 v251, v30, v31\n" \
  "s_waitcnt vmcnt(7)\n" \
  "ds_read_b64_tr_b16 a[200:201], v26 offset:16384\n" \
  "ds_read_b64_tr_b16 a[204:205], v27 offset:16384\n" \
  "ds_read_b64_tr_b16 a[208:209], v28 offset:16384\n" \
  "s_add_u32 m0, s57, 4096\n" \
  "s_add_u32 s31, s13, 9879552\n" \
  "buffer_load_dwordx4 v38, s[4:7], s31 offen offset:0 lds\n" \
  "v_permlane16_swap_b32 v254, v251\n" \
  "v_add_f32 v254, v254, v251\n" \
  "ds_read_b64_tr_b16 a[212:213], v29 offset:16384\n" \
  "ds_read_b64_tr_b16 a[220:221], v26 offset:18432\n" \
  "ds_read_b64_tr_b16 a[224:225], v27 offset:18432\n" \
  "buffer_load_dwordx4 v39, s[4:7], s31 offen offset:1024 lds\n" \
  "v_add_f32_dpp v254, v254, v254 quad_perm:[1,0,3,2] row_mask:0xf bank_mask:0xf\n" \
  "ds_read_b64_tr_b16 a[228:229], v28 offset:18432\n" \
  "ds_read_b64_tr_b16 a[232:233], v29 offset:18432\n" \
  "ds_read_b64_tr_b16 a[202:203], v26 offset:20480\n" \
  "buffer_load_dwordx4 v40, s[4:7], s31 offen offset:2048 lds\n" \
  "v_add_f32_dpp v254, v254, v254 quad_perm:[2,3,0,1] row_mask:0xf bank_mask:0xf\n" \
  "ds_read_b64_tr_b16 a[206:207], v27 offset:20480\n" \
  "ds_read_b64_tr_b16 a[210:211], v28 offset:20480\n" \
  "ds_read_b64_tr_b16 a[214:215], v29 offset:20480\n" \
  "buffer_load_dwordx4 v41, s[4:7], s31 offen offset:3072 lds\n" \
  "v_add_f32_dpp v254, v254, v254 row_half_mirror row_mask:0xf bank_mask:0xf\n" \
  "ds_read_b64_tr_b16 a[222:223], v26 offset:22528\n" \
  "ds_read_b64_tr_b16 a[226:227], v27 offset:22528\n" \
  "ds_read_b64_tr_b16 a[230:231], v28 offset:22528\n" \
  "ds_read_b64_tr_b16 a[234:235], v29 offset:22528\n" \
  "s_add_u32 m0, s57, 8192\n" \
  "s_add_u32 s31, s13, 516096\n" \
  "s_waitcnt lgkmcnt(15)\n" \
  "buffer_load_dwordx4 v38, s[4:7], s31 offen offset:0 lds\n" \
  "v_add_f32_dpp v254, v254, v254 row_mirror row_mask:0xf bank_mask:0xf\n" \
  "v_mov_b32 v251, v254\n" \
  "buffer_load_dwordx4 v39, s[4:7], s31 offen offset:1024 lds\n" \
  "buffer_load_dwordx4 v40, s[4:7], s31 offen offset:2048 lds\n" \
  "v_permlane16_swap_b32 v254, v251\n" \
  "v_mul_f32 v254, s35, v254\n" \
  "v_fma_f32 v251, v251, s35, v23\n" \
  "v_fma_f32 v251, -v254, v254, v251\n" \
  "v_max_f32 v251, v251, v23\n" \
  "v_rsq_f32 v22, v251\n" \
  "buffer_load_dwordx4 v41, s[4:7], s31 offen offset:3072 lds\n" \
  "v_mul_f32_e64 v23, -v254, v22\n" \
  "v_pk_fma_f32 v[2:3], v[2:3], v[22:23], v[22:23] op_sel:[0,0,1] op_sel_hi:[1,0,1]\n" \
  "v_pk_fma_f32 v[4:5], v[4:5], v[22:23], v[22:23] op_sel:[0,0,1] op_sel_hi:[1,0,1]\n" \
  "v_pk_fma_f32 v[6:7], v[6:7], v[22:23], v[22:23] op_sel:[0,0,1] op_sel_hi:[1,0,1]\n" \
  "v_pk_fma_f32 v[8:9], v[8:9], v[22:23], v[22:23] op_sel:[0,0,1] op_sel_hi:[1,0,1]\n" \
  "v_pk_fma_f32 v[10:11], v[10:11], v[22:23], v[22:23] op_sel:[0,0,1] op_sel_hi:[1,0,1]\n" \
  "v_pk_fma_f32 v[12:13], v[12:13], v[22:23], v[22:23] op_sel:[0,0,1] op_sel_hi:[1,0,1]\n" \
  "v_pk_fma_f32 v[14:15], v[14:15], v[22:23], v[22:23] op_sel:[0,0,1] op_sel_hi:[1,0,1]\n" \
  "v_pk_fma_f32 v[18:19], v[18:19], v[22:23], v[22:23] op_sel:[0,0,1] op_sel_hi:[1,0,1]\n" \
  "v_pk_fma_f32 v[20:21], v[20:21], v[22:23], v[22:23] op_sel:[0,0,1] op_sel_hi:[1,0,1]\n" \
  "v_cvt_pk_bf16_f32 v22, v2, v3\n" \
  "v_cvt_pk_bf16_f32 v23, v4, v5\n" \
  "v_cvt_pk_bf16_f32 v24, v6, v7\n" \
  "v_cvt_pk_bf16_f32 v25, v8, v9\n" \
  "ds_write_b128 v247, v[22:25] offset:0\n" \
  "v_cvt_pk_bf16_f32 v2, v10, v11\n" \
  "v_cvt_pk_bf16_f32 v3, v12, v13\n" \
  "v_cvt_pk_bf16_f32 v4, v14, v15\n" \
  "v_cvt_pk_bf16_f32 v5, v18, v19\n" \
  "ds_write_b128 v247, v[2:5] offset:512\n" \
  "v_cvt_pk_bf16_f32 v254, v20, v21\n" \
  "ds_write_b32 v246, v254 offset:1024\n" \
  "s_add_u32 m0, s57, 12288\n" \
  "s_add_u32 s31, s13, 9953280\n" \
  "buffer_load_dwordx4 v38, s[4:7], s31 offen offset:0 lds\n" \
  "buffer_load_dwordx4 v39, s[4:7], s31 offen offset:1024 lds\n" \
  "s_waitcnt lgkmcnt(0)\n" \
  "s_barrier\n" \
  "ds_read_b64_tr_b16 v[2:3], v253 offset:9216\n" \
  "ds_read_b64_tr_b16 v[4:5], v253 offset:11664\n" \
  "ds_read_b64_tr_b16 v[6:7], v253 offset:9728\n" \
  "ds_read_b64_tr_b16 v[8:9], v253 offset:12176\n" \
  "ds_read_b64_tr_b16 v[10:11], v253 offset:10240\n" \
  "ds_read_b64_tr_b16 v[12:13], v253 offset:12688\n" \
  "buffer_load_dwordx4 v40, s[4:7], s31 offen offset:2048 lds\n" \
  "buffer_load_dwordx4 v41, s[4:7], s31 offen offset:3072 lds\n" \
  "s_add_u32 s32, s11, 1048576\n" \
  "s_add_u32 s33, s11, 2097152\n" \
  "s_waitcnt lgkmcnt(4)\n" \
  "buffer_store_dwordx4 v[2:5], v240, s[36:39], s11 offen offset:0 sc1\n" \
  "s_waitcnt lgkmcnt(2)\n" \
  "buffer_store_dwordx4 v[6:9], v240, s[36:39], s32 offen offset:0 sc1\n" \
  "s_mov_b64 exec, 65535\n" \
  "s_waitcnt lgkmcnt(0)\n" \
  "buffer_store_dwordx4 v[10:13], v240, s[36:39], s33 offen offset:0 sc1\n" \
  "s_mov_b64 exec, -1\n" \
  ".Lsnc_%=:\n" \
  "v_xor_b32 v254, s18, v66\n" \
  "v_bitop3_b32 v254, v67, s19, v254 bitop3:0xbe\n" \
  "v_cmp_ne_u32 vcc, 0, v254\n" \
  "s_cbranch_vccnz .Lsns_%=\n" \
  ".Lsnd_%=:\n" \
  "s_add_u32 m0, s57, 16384\n" \
  "s_add_u32 s31, s14, 0\n" \
  "buffer_load_dwordx4 v252, s[36:39], s31 offen offset:0 sc1 lds\n" \
  "buffer_load_dwordx4 v252, s[36:39], s31 offen offset:1024 sc1 lds\n" \
  "buffer_load_dwordx4 v252, s[36:39], s31 offen offset:2048 sc1 lds\n" \
  "buffer_load_dwordx4 v252, s[36:39], s31 offen offset:3072 sc1 lds\n" \
  "s_add_u32 m0, s57, 20480\n" \
  "s_add_u32 s31, s14, 4096\n" \
  "buffer_load_dwordx4 v252, s[36:39], s31 offen offset:0 sc1 lds\n" \
  "buffer_load_dwordx4 v252, s[36:39], s31 offen offset:1024 sc1 lds\n" \
  "buffer_load_dwordx4 v252, s[36:39], s31 offen offset:2048 sc1 lds\n" \
  "buffer_load_dwordx4 v252, s[36:39], s31 offen offset:3072 sc1 lds\n" \
  "s_lshr_b32 s0, s2, 6\n" \
  "s_lshr_b32 s1, s2, 1\n" \
  "s_and_b32 s34, s1, 3\n" \
  "s_lshr_b32 s1, s2, 3\n" \
  "s_and_b32 s84, s1, 1\n" \
  "s_and_b32 s1, s2, 7\n" \
  "s_lshl_b32 s1, s1, 2\n" \
  "s_lshr_b32 s100, s2, 4\n" \
  "s_and_b32 s100, s100, 3\n" \
  "s_add_u32 s88, s1, s100\n" \
  "s_and_b32 s1, s2, 1\n" \
  "s_lshl_b32 s1, s1, 3\n" \
  "s_lshr_b32 s100, s2, 3\n" \
  "s_and_b32 s100, s100, 7\n" \
  "s_add_u32 s85, s1, s100\n" \
  "s_mul_i32 s1, s0, 4505600\n" \
  "s_add_u32 s52, s1, 12123648\n" \
  "s_add_u32 s53, s52, 3276800\n" \
  "s_add_u32 s83, s52, 2048000\n" \
  "s_lshl_b32 s1, s0, 5\n" \
  "s_add_u32 s1, s1, s88\n" \
  "s_lshl_b32 s1, s1, 1\n" \
  "s_add_u32 s1, s1, s84\n" \
  "s_lshl_b32 s1, s1, 7\n" \
  "s_lshl_b32 s100, s20, 5\n" \
  "s_add_u32 s1, s1, s100\n" \
  "s_add_u32 s54, s1, 262144\n" \
  "s_lshl_b32 s1, s20, 5\n" \
  "s_sub_u32 s1, s54, s1\n" \
  "s_lshl_b32 s100, s34, 11\n" \
  "s_sub_u32 s1, s1, s100\n" \
  "s_add_u32 s16, s1, 65536\n" \
  "s_mov_b32 s55, 3184710631\n" \
  "s_mul_i32 s56, s20, 9568\n" \
  "s_lshl_b32 s58, s20, 13\n" \
  "s_lshr_b32 s1, s88, 1\n" \
  "s_mul_i32 s90, s1, 18\n" \
  "s_and_b32 s1, s88, 1\n" \
  "s_lshl_b32 s1, s1, 3\n" \
  "s_add_u32 s1, s90, s1\n" \
  "s_lshl_b32 s100, s84, 2\n" \
  "s_add_u32 s1, s1, s100\n" \
  "s_add_u32 s1, s1, s20\n" \
  "s_add_u32 s1, s1, 1\n" \
  "s_lshl_b32 s63, s1, 9\n" \
  "s_and_b32 s1, s88, 1\n" \
  "s_add_u32 s1, s90, s1\n" \
  "s_add_u32 s1, s1, 17\n" \
  "s_lshl_b32 s1, s1, 9\n" \
  "s_lshl_b32 s100, s20, 7\n" \
  "s_add_u32 s64, s1, s100\n" \
  "s_lshl_b32 s1, s0, 5\n" \
  "s_lshl_b32 s100, s34, 3\n" \
  "s_add_u32 s1, s1, s100\n" \
  "s_lshl_b32 s100, s20, 1\n" \
  "s_add_u32 s1, s1, s100\n" \
  "s_lshl_b32 s1, s1, 8\n" \
  "s_add_u32 s1, s1, 262144\n" \
  "s_sub_u32 s59, s1, s63\n" \
  "s_add_u32 s75, s59, 196608\n" \
  "s_lshl_b32 s1, s85, 7\n" \
  "s_mul_i32 s1, s1, 576\n" \
  "s_mul_i32 s100, s0, 144\n" \
  "s_add_u32 s90, s1, s100\n" \
  "s_mul_i32 s1, s20, 9216\n" \
  "s_add_u32 s1, s90, s1\n" \
  "s_lshl_b32 s100, s34, 5\n" \
  "s_add_u32 s1, s1, s100\n" \
  "s_lshl_b32 s61, s1, 1\n" \
  "s_lshl_b32 s1, s34, 4\n" \
  "s_add_u32 s1, s1, s20\n" \
  "s_mul_i32 s1, s1, 576\n" \
  "s_add_u32 s1, s90, s1\n" \
  "s_add_u32 s1, s1, 128\n" \
  "s_lshl_b32 s62, s1, 1\n" \
  "v_and_b32 v254, 768, v252\n" \
  "v_lshlrev_b32 v254, 2, v254\n" \
  "v_and_b32 v253, 192, v252\n" \
  "v_lshl_or_b32 v254, v253, 1, v254\n" \
  "v_bfe_u32 v253, v191, 3, 2\n" \
  "v_lshl_or_b32 v254, v253, 5, v254\n" \
  "v_and_b32 v251, 3, v191\n" \
  "v_lshl_or_b32 v254, v251, 3, v254\n" \
  "v_add_u32 v8, s57, v254\n" \
  "v_mov_b32 v63, v8\n" \
  "v_lshl_or_b32 v9, v241, 16, v249\n" \
  "v_and_b32 v254, 12, v191\n" \
  "v_lshrrev_b32 v253, 4, v191\n" \
  "v_add_u32 v253, v253, v254\n" \
  "v_mul_u32_u24 v253, 1152, v253\n" \
  "v_lshl_add_u32 v253, v251, 3, v253\n" \
  "v_add_u32 v10, s61, v253\n" \
  "v_and_b32 v251, 1, v191\n" \
  "v_lshl_add_u32 v254, v251, 1, v254\n" \
  "v_bfe_u32 v253, v191, 1, 1\n" \
  "v_lshlrev_b32 v253, 6, v253\n" \
  "v_lshrrev_b32 v11, 2, v191\n" \
  "v_and_or_b32 v253, v11, 12, v253\n" \
  "v_mul_u32_u24 v253, 1152, v253\n" \
  "v_lshl_add_u32 v253, v254, 1, v253\n" \
  "v_add_u32 v11, s62, v253\n" \
  "v_lshrrev_b32 v254, 3, v252\n" \
  "v_bitop3_b32 v254, v254, 32, v252 bitop3:0x6a\n" \
  "v_lshl_or_b32 v254, v254, 5, v254\n" \
  "v_and_b32 v12, 28784, v254\n" \
  "v_xor_b32 v254, 64, v12\n" \
  "v_add_u32 v13, 31744, v254\n" \
  "v_lshlrev_b32 v14, 16, v245\n" \
  "v_add_u32 v254, s57, v252\n" \
  "v_add_u32 v22, 28672, v254\n" \
  "v_lshlrev_b32 v52, 16, v16\n" \
  "v_and_b32 v53, 4294901760, v16\n" \
  "v_lshlrev_b32 v54, 16, v17\n" \
  "v_and_b32 v55, 4294901760, v17\n" \
  "v_lshlrev_b32 v56, 16, v255\n" \
  "v_mov_b32 v57, 3222493025\n" \
  "s_mul_i32 s32, s20, 1056\n" \
  "v_bfe_u32 v254, v191, 1, 1\n" \
  "v_mul_u32_u24 v254, 544, v254\n" \
  "v_lshl_add_u32 v254, v251, 4, v254\n" \
  "v_bfe_u32 v253, v191, 2, 1\n" \
  "v_lshl_add_u32 v254, v253, 8, v254\n" \
  "v_bfe_u32 v253, v191, 3, 2\n" \
  "v_lshl_add_u32 v254, v253, 6, v254\n" \
  "v_and_b32 v253, 32, v191\n" \
  "v_add3_u32 v58, v254, v253, s32\n" \
  "v_add_u32 v59, 524288, v252\n" \
  "v_bfe_u32 v254, v191, 3, 2\n" \
  "v_mul_u32_u24 v254, 288, v254\n" \
  "v_lshl_add_u32 v254, v253, 1, v254\n" \
  "v_and_b32 v253, 7, v191\n" \
  "v_lshl_add_u32 v254, v253, 2, v254\n" \
  "s_and_b32 s32, s20, 1\n" \
  "s_lshl_b32 s32, s32, 7\n" \
  "s_lshr_b32 s33, s20, 1\n" \
  "s_lshl_b32 s33, s33, 5\n" \
  "s_add_u32 s32, s32, s33\n" \
  "s_add_u32 s32, s32, 4224\n" \
  "v_add_u32 v60, s32, v254\n" \
  "v_lshl_add_u32 v61, v191, 3, s63\n" \
  "v_lshl_add_u32 v62, v191, 1, s64\n" \
  "s_mul_i32 s1, s34, 1152\n" \
  "s_lshr_b32 s100, s20, 1\n" \
  "s_mul_i32 s100, s100, 576\n" \
  "s_add_u32 s1, s1, s100\n" \
  "s_lshl_b32 s1, s1, 12\n" \
  "s_lshl_b32 s100, s85, 8\n" \
  "s_add_u32 s65, s1, s100\n" \
  "s_and_b32 s1, s20, 1\n" \
  "s_lshl_b32 s1, s1, 20\n" \
  "s_add_u32 s1, s65, s1\n" \
  "s_add_u32 s60, s1, 655488\n" \
  "s_and_b32 s1, s20, 1\n" \
  "s_lshl_b32 s1, s1, 17\n" \
  "s_add_u32 s1, s65, s1\n" \
  "s_add_u32 s65, s1, 2097152\n" \
  "s_add_u32 s66, s65, 128\n" \
  "s_add_u32 s61, s60, 130944\n" \
  "s_add_u32 s62, s60, 131072\n" \
  "s_add_u32 s63, s60, 262016\n" \
  "s_add_u32 s64, s60, 262144\n" \
  "s_lshl_b32 s1, s34, 7\n" \
  "s_add_u32 s67, s1, 163200\n" \
  "s_lshl_b32 s1, s34, 2\n" \
  "s_add_u32 s98, s1, s20\n" \
  "s_mul_i32 s1, s98, 9216\n" \
  "s_add_u32 s1, s1, s52\n" \
  "s_add_u32 s68, s1, 819712\n" \
  "s_add_u32 s69, s68, 3072\n" \
  "s_add_u32 s70, s68, 6144\n" \
  "s_lshl_b32 s1, s85, 14\n" \
  "s_add_u32 s1, s1, s52\n" \
  "s_add_u32 s90, s1, 967168\n" \
  "s_lshl_b32 s1, s98, 8\n" \
  "s_add_u32 s79, s1, s90\n" \
  "s_add_u32 s80, s79, 4096\n" \
  "s_add_u32 s81, s79, 8192\n" \
  "s_add_u32 s82, s79, 12288\n" \
  "s_lshl_b32 s99, s20, 10\n" \
  "s_add_u32 s87, s99, 65536\n" \
  "s_add_u32 s90, s90, s99\n" \
  "s_lshl_b32 s1, s2, 2\n" \
  "s_add_u32 s1, s1, s20\n" \
  "s_lshl_b32 s1, s1, 8\n" \
  "s_add_u32 s91, s1, 69206016\n" \
  "s_mul_i32 s1, s0, 589824\n" \
  "s_lshl_b32 s100, s20, 14\n" \
  "s_add_u32 s1, s1, s100\n" \
  "s_add_u32 s48, s1, 9764864\n" \
  "s_sub_u32 s1, s34, 1\n" \
  "s_and_b32 s1, s1, 3\n" \
  "s_lshl_b32 s1, s1, 12\n" \
  "s_add_u32 s98, s1, s90\n" \
  "s_add_u32 s71, s98, 4292919296\n" \
  "s_add_u32 s76, s98, 4293328896\n" \
  "s_sub_u32 s1, s34, 2\n" \
  "s_and_b32 s1, s1, 3\n" \
  "s_lshl_b32 s1, s1, 12\n" \
  "s_add_u32 s98, s1, s90\n" \
  "s_add_u32 s72, s98, 4293328896\n" \
  "s_add_u32 s77, s98, 4293328896\n" \
  "s_sub_u32 s1, s34, 3\n" \
  "s_and_b32 s1, s1, 3\n" \
  "s_lshl_b32 s1, s1, 12\n" \
  "s_add_u32 s98, s1, s90\n" \
  "s_add_u32 s73, s98, 4293328896\n" \
  "s_add_u32 s78, s98, 4293738496\n" \
  "s_and_b32 s1, s34, 3\n" \
  "s_lshl_b32 s1, s1, 12\n" \
  "s_add_u32 s98, s1, s90\n" \
  "s_add_u32 s74, s98, 4294148096\n" \
  "s_and_b32 s1, s0, 1\n" \
  "s_lshl_b32 s98, s1, 13\n" \
  "s_sub_u32 s1, s48, s98\n" \
  "s_add_u32 s50, s1, 8192\n" \
  "s_add_u32 s51, s50, 4096\n" \
  "s_add_u32 s48, s48, s98\n" \
  "s_add_u32 s49, s48, 4096\n" \
  "s_lshr_b32 s1, s2, 3\n" \
  "s_lshl_b32 s1, s1, 2\n" \
  "s_add_u32 s98, s1, s20\n" \
  "s_and_b32 s1, s98, 31\n" \
  "s_lshl_b32 s1, s1, 2\n" \
  "s_add_u32 s90, s1, 448\n" \
  "s_and_b32 s1, s98, 8\n" \
  "s_lshl_b32 s1, s1, 5\n" \
  "s_and_b32 s100, s98, 7\n" \
  "s_lshl_b32 s100, s100, 2\n" \
  "s_add_u32 s1, s1, s100\n" \
  "s_add_u32 s89, s1, 160\n" \
  "s_and_b32 s1, s98, 15\n" \
  "s_lshl_b32 s1, s1, 2\n" \
  "s_add_u32 s86, s1, 192\n" \
  "s_bitcmp1_b32 s98, 4\n" \
  "s_cselect_b32 s89, s89, s86\n" \
  "s_bitcmp1_b32 s98, 5\n" \
  "s_cselect_b32 s89, s90, s89\n" \
  "s_lshr_b32 s1, s98, 6\n" \
  "s_mul_i32 s1, s1, 576\n" \
  "s_mul_i32 s100, s34, 1152\n" \
  "s_add_u32 s1, s1, s100\n" \
  "s_add_u32 s1, s1, s89\n" \
  "s_lshl_b32 s1, s1, 12\n" \
  "s_and_b32 s100, s2, 1\n" \
  "s_lshl_b32 s100, s100, 11\n" \
  "s_add_u32 s1, s1, s100\n" \
  "s_lshr_b32 s100, s2, 3\n" \
  "s_and_b32 s100, s100, 3\n" \
  "s_add_u32 s1, s1, s100\n" \
  "s_add_u32 s89, s1, 1\n" \
  "s_sub_u32 s98, s57, s99\n" \
  "s_lshl_b32 s1, s34, 17\n" \
  "s_add_u32 s85, s1, 65536\n" \
  "s_mov_b32 s90, 7405568\n" \
  "s_add_u32 s1, s0, 3\n" \
  "s_and_b32 s1, s1, 3\n" \
  "s_lshl_b32 s88, s1, 12\n" \
  "s_mov_b32 s30, s16\n" \
  "v_mov_b32 v2, v12\n" \
  "v_mov_b32 v3, v13\n" \
  "v_add_u32 v4, 63488, v12\n" \
  "v_add_u32 v5, 63488, v13\n" \
  "v_mov_b32 v190, 1\n" \
  "v_lshrrev_b32 v189, 1, v252\n" \
  "v_add_u32 v255, s57, v189\n" \
  "s_lshl_b32 s32, s20, 11\n" \
  "v_add_u32 v189, s32, v189\n" \
  "s_waitcnt vmcnt(19)\n" \
  "ds_read_b64_tr_b16 a[240:241], v26 offset:0\n" \
  "ds_read_b64_tr_b16 a[244:245], v27 offset:0\n" \
  "ds_read_b64_tr_b16 a[248:249], v28 offset:0\n" \
  "ds_read_b64_tr_b16 a[252:253], v29 offset:0\n" \
  "ds_read_b64_tr_b16 v[196:197], v26 offset:2048\n" \
  "ds_read_b64_tr_b16 v[200:201], v27 offset:2048\n" \
  "ds_read_b64_tr_b16 v[204:205], v28 offset:2048\n" \
  "ds_read_b64_tr_b16 v[208:209], v29 offset:2048\n" \
  "ds_read_b64_tr_b16 a[242:243], v26 offset:4096\n" \
  "ds_read_b64_tr_b16 a[246:247], v27 offset:4096\n" \
  "ds_read_b64_tr_b16 a[250:251], v28 offset:4096\n" \
  "ds_read_b64_tr_b16 a[254:255], v29 offset:4096\n" \
  "ds_read_b64_tr_b16 v[198:199], v26 offset:6144\n" \
  "ds_read_b64_tr_b16 v[202:203], v27 offset:6144\n" \
  "ds_read_b64_tr_b16 v[206:207], v28 offset:6144\n" \
  "ds_read_b64_tr_b16 v[210:211], v29 offset:6144\n" \
  "s_waitcnt vmcnt(11)\n" \
  "ds_read_b64_tr_b16 v[216:217], v26 offset:8192\n" \
  "ds_read_b64_tr_b16 v[220:221], v27 offset:8192\n" \
  "ds_read_b64_tr_b16 v[224:225], v28 offset:8192\n" \
  "ds_read_b64_tr_b16 v[228:229], v29 offset:8192\n" \
  "ds_read_b64_tr_b16 v[236:237], v26 offset:10240\n" \
  "ds_read_b64_tr_b16 v[240:241], v27 offset:10240\n" \
  "ds_read_b64_tr_b16 v[244:245], v28 offset:10240\n" \
  "ds_read_b64_tr_b16 v[248:249], v29 offset:10240\n" \
  "ds_read_b64_tr_b16 v[218:219], v26 offset:12288\n" \
  "ds_read_b64_tr_b16 v[222:223], v27 offset:12288\n" \
  "ds_read_b64_tr_b16 v[226:227], v28 offset:12288\n" \
  "ds_read_b64_tr_b16 v[230:231], v29 offset:12288\n" \
  "ds_read_b64_tr_b16 v[238:239], v26 offset:14336\n" \
  "ds_read_b64_tr_b16 v[242:243], v27 offset:14336\n" \
  "ds_read_b64_tr_b16 v[246:247], v28 offset:14336\n" \
  "ds_read_b64_tr_b16 v[250:251], v29 offset:14336\n" \
  "s_waitcnt vmcnt(8)\n" \
  "s_mov_b64 exec, 1\n" \
  "buffer_store_dword v190, off, s[36:39], s28 sc1\n" \
  "s_mov_b64 exec, -1\n" \
  "s_add_u32 m0, s57, 24576\n" \
  "s_add_u32 s31, s15, 8192\n" \
  "buffer_load_dwordx4 v252, s[36:39], s31 offen offset:0 sc1 lds\n" \
  "buffer_load_dwordx4 v252, s[36:39], s31 offen offset:1024 sc1 lds\n" \
  "buffer_load_dwordx4 v252, s[36:39], s31 offen offset:2048 sc1 lds\n" \
  "buffer_load_dwordx4 v252, s[36:39], s31 offen offset:3072 sc1 lds\n" \
  "s_add_u32 m0, s57, 28672\n" \
  "s_add_u32 s31, s15, 12288\n" \
  "buffer_load_dwordx4 v252, s[36:39], s31 offen offset:0 sc1 lds\n" \
  "buffer_load_dwordx4 v252, s[36:39], s31 offen offset:1024 sc1 lds\n" \
  "s_lshl_b32 m0, s20, 11\n" \
  "s_add_u32 s31, s31, 2048\n" \
  "buffer_load_dwordx4 v252, s[36:39], s31 offen offset:0 sc1 lds\n" \
  "buffer_load_dwordx4 v252, s[36:39], s31 offen offset:1024 sc1 lds\n" \
  "s_bfe_u32 s32, s27, 131076\n" \
  "s_sub_u32 s100, s60, 655488\n" \
  "s_sub_u32 s101, s60, 720896\n" \
  "s_lshl_b32 s32, s32, 12\n" \
  "s_add_u32 s32, s32, 0\n" \
  "s_cmp_ge_u32 s32, 16384\n" \
  "s_cselect_b32 s32, 0, s32\n" \
  "s_bitcmp1_b32 s32, 12\n" \
  "s_cselect_b32 s33, s101, s100\n" \
  "s_lshl4_add_u32 s31, s32, s33\n" \
  "s_add_u32 s33, s32, s57\n" \
  "s_add_u32 m0, s33, 0\n" \
  "s_waitcnt lgkmcnt(0)\n" \
  "buffer_load_dwordx4 v2, s[40:43], s31 offen offset:0 lds\n" \
  "buffer_load_dwordx4 v3, s[40:43], s31 offen offset:1024 lds\n" \
  "buffer_load_dwordx4 v4, s[40:43], s31 offen offset:2048 lds\n" \
  "buffer_load_dwordx4 v5, s[40:43], s31 offen offset:3072 lds\n" \
  "s_add_u32 s32, s32, 4096\n" \
  "s_cmp_ge_u32 s32, 16384\n" \
  "s_cselect_b32 s32, 0, s32\n" \
  "s_bitcmp1_b32 s32, 12\n" \
  "s_cselect_b32 s33, s101, s100\n" \
  "s_lshl4_add_u32 s31, s32, s33\n" \
  "s_add_u32 s33, s32, s57\n" \
  "s_add_u32 m0, s33, 0\n" \
  "s_nop 0\n" \
  "buffer_load_dwordx4 v2, s[40:43], s31 offen offset:0 lds\n" \
  "buffer_load_dwordx4 v3, s[40:43], s31 offen offset:1024 lds\n" \
  "buffer_load_dwordx4 v4, s[40:43], s31 offen offset:2048 lds\n" \
  "buffer_load_dwordx4 v5, s[40:43], s31 offen offset:3072 lds\n" \
  "s_add_u32 s32, s32, 4096\n" \
  "s_cmp_ge_u32 s32, 16384\n" \
  "s_cselect_b32 s32, 0, s32\n" \
  "s_bitcmp1_b32 s32, 12\n" \
  "s_cselect_b32 s33, s101, s100\n" \
  "s_lshl4_add_u32 s31, s32, s33\n" \
  "s_add_u32 s33, s32, s57\n" \
  "s_add_u32 m0, s33, 0\n" \
  "s_nop 0\n" \
  "buffer_load_dwordx4 v2, s[40:43], s31 offen offset:0 lds\n" \
  "buffer_load_dwordx4 v3, s[40:43], s31 offen offset:1024 lds\n" \
  "buffer_load_dwordx4 v4, s[40:43], s31 offen offset:2048 lds\n" \
  "buffer_load_dwordx4 v5, s[40:43], s31 offen offset:3072 lds\n" \
  "s_add_u32 s32, s32, 4096\n" \
  "s_cmp_ge_u32 s32, 16384\n" \
  "s_cselect_b32 s32, 0, s32\n" \
  "s_bitcmp1_b32 s32, 12\n" \
  "s_cselect_b32 s33, s101, s100\n" \
  "s_lshl4_add_u32 s31, s32, s33\n" \
  "s_add_u32 s33, s32, s57\n" \
  "s_add_u32 m0, s33, 0\n" \
  "s_nop 0\n" \
  "buffer_load_dwordx4 v2, s[40:43], s31 offen offset:0 lds\n" \
  "buffer_load_dwordx4 v3, s[40:43], s31 offen offset:1024 lds\n" \
  "buffer_load_dwordx4 v4, s[40:43], s31 offen offset:2048 lds\n" \
  "buffer_load_dwordx4 v5, s[40:43], s31 offen offset:3072 lds\n" \
  "s_waitcnt vmcnt(16)\n" \
  "ds_read_b64_tr_b16 a[16:17], v255 offset:16384\n" \
  "ds_read_b64_tr_b16 a[36:37], v255 offset:16896\n" \
  "ds_read_b64_tr_b16 a[18:19], v255 offset:17408\n" \
  "ds_read_b64_tr_b16 a[38:39], v255 offset:17920\n" \
  "ds_read_b64_tr_b16 a[56:57], v255 offset:18432\n" \
  "ds_read_b64_tr_b16 a[76:77], v255 offset:18944\n" \
  "ds_read_b64_tr_b16 a[58:59], v255 offset:19456\n" \
  "ds_read_b64_tr_b16 a[78:79], v255 offset:19968\n" \
  "ds_read_b64_tr_b16 a[96:97], v255 offset:20480\n" \
  "ds_read_b64_tr_b16 a[116:117], v255 offset:20992\n" \
  "ds_read_b64_tr_b16 a[98:99], v255 offset:21504\n" \
  "ds_read_b64_tr_b16 a[118:119], v255 offset:22016\n" \
  "ds_read_b64_tr_b16 a[136:137], v255 offset:22528\n" \
  "ds_read_b64_tr_b16 a[156:157], v255 offset:23040\n" \
  "ds_read_b64_tr_b16 a[138:139], v255 offset:23552\n" \
  "ds_read_b64_tr_b16 a[158:159], v255 offset:24064\n" \
  "ds_read_b64_tr_b16 a[176:177], v255 offset:24576\n" \
  "ds_read_b64_tr_b16 a[196:197], v255 offset:25088\n" \
  "ds_read_b64_tr_b16 a[178:179], v255 offset:25600\n" \
  "ds_read_b64_tr_b16 a[198:199], v255 offset:26112\n" \
  "ds_read_b64_tr_b16 a[216:217], v255 offset:26624\n" \
  "ds_read_b64_tr_b16 a[236:237], v255 offset:27136\n" \
  "ds_read_b64_tr_b16 a[218:219], v255 offset:27648\n" \
  "ds_read_b64_tr_b16 a[238:239], v255 offset:28160\n" \
  "ds_read_b64_tr_b16 v[192:193], v255 offset:28672\n" \
  "ds_read_b64_tr_b16 v[212:213], v255 offset:29184\n" \
  "ds_read_b64_tr_b16 v[194:195], v255 offset:29696\n" \
  "ds_read_b64_tr_b16 v[214:215], v255 offset:30208\n" \
  "ds_read_b64_tr_b16 v[232:233], v189 offset:0\n" \
  "ds_read_b64_tr_b16 v[252:253], v189 offset:512\n" \
  "ds_read_b64_tr_b16 v[234:235], v189 offset:1024\n" \
  "ds_read_b64_tr_b16 v[254:255], v189 offset:1536\n" \
  "v_and_b32 v189, 12, v191\n" \
  "v_and_b32 v190, 1, v191\n" \
  "v_lshl_add_u32 v189, v190, 1, v189\n" \
  "v_lshlrev_b32 v189, 2, v189\n" \
  "v_add_u32 v189, 163712, v189\n" \
  "ds_read_b64 v[6:7], v189 offset:0\n" \
  "s_bfe_u32 s32, s27, 131076\n" \
  "s_sub_u32 s100, s60, 131200\n" \
  "s_sub_u32 s101, s60, 196608\n" \
  "s_lshl_b32 s32, s32, 12\n" \
  "s_add_u32 s32, s32, 0\n" \
  "s_cmp_ge_u32 s32, 12288\n" \
  "s_cselect_b32 s32, 0, s32\n" \
  "s_bitcmp1_b32 s32, 12\n" \
  "s_cselect_b32 s33, s101, s100\n" \
  "s_lshl4_add_u32 s31, s32, s33\n" \
  "s_add_u32 s33, s32, s57\n" \
  "s_add_u32 m0, s33, 16384\n" \
  "s_waitcnt lgkmcnt(1)\n" \
  "buffer_load_dwordx4 v2, s[40:43], s31 offen offset:0 lds\n" \
  "buffer_load_dwordx4 v3, s[40:43], s31 offen offset:1024 lds\n" \
  "buffer_load_dwordx4 v4, s[40:43], s31 offen offset:2048 lds\n" \
  "buffer_load_dwordx4 v5, s[40:43], s31 offen offset:3072 lds\n" \
  "s_add_u32 s32, s32, 4096\n" \
  "s_cmp_ge_u32 s32, 12288\n" \
  "s_cselect_b32 s32, 0, s32\n" \
  "s_bitcmp1_b32 s32, 12\n" \
  "s_cselect_b32 s33, s101, s100\n" \
  "s_lshl4_add_u32 s31, s32, s33\n" \
  "s_add_u32 s33, s32, s57\n" \
  "s_add_u32 m0, s33, 16384\n" \
  "s_nop 0\n" \
  "buffer_load_dwordx4 v2, s[40:43], s31 offen offset:0 lds\n" \
  "buffer_load_dwordx4 v3, s[40:43], s31 offen offset:1024 lds\n" \
  "buffer_load_dwordx4 v4, s[40:43], s31 offen offset:2048 lds\n" \
  "buffer_load_dwordx4 v5, s[40:43], s31 offen offset:3072 lds\n" \
  "s_add_u32 s32, s32, 4096\n" \
  "s_cmp_ge_u32 s32, 12288\n" \
  "s_cselect_b32 s32, 0, s32\n" \
  "s_bitcmp1_b32 s32, 12\n" \
  "s_cselect_b32 s33, s101, s100\n" \
  "s_lshl4_add_u32 s31, s32, s33\n" \
  "s_add_u32 s33, s32, s57\n" \
  "s_add_u32 m0, s33, 16384\n" \
  "s_nop 0\n" \
  "buffer_load_dwordx4 v2, s[40:43], s31 offen offset:0 lds\n" \
  "buffer_load_dwordx4 v3, s[40:43], s31 offen offset:1024 lds\n" \
  "buffer_load_dwordx4 v4, s[40:43], s31 offen offset:2048 lds\n" \
  "buffer_load_dwordx4 v5, s[40:43], s31 offen offset:3072 lds\n" \
  "s_waitcnt lgkmcnt(0)\n" \
  "v_lshrrev_b32 v6, 16, v6\n" \
  "v_or_b32 v15, v6, v7\n" \
  "s_branch .Lgo_%=\n" \
  ".Lans_%=:\n" \
  "v_xor_b32 v235, s18, v46\n" \
  "v_bitop3_b32 v235, v47, s19, v235 bitop3:0xbe\n" \
  "s_mov_b32 s32, 8192\n" \
  "v_cmpx_ne_u32 vcc, 0, v235\n" \
  "buffer_load_dwordx4 v[46:49], v243, s[36:39], s32 offen sc1\n" \
  "s_mov_b64 exec, -1\n" \
  "v_xor_b32 v235, s18, v50\n" \
  "v_bitop3_b32 v235, v51, s19, v235 bitop3:0xbe\n" \
  "s_mov_b32 s32, 17408\n" \
  "v_cmpx_ne_u32 vcc, 0, v235\n" \
  "buffer_load_dwordx4 v[50:53], v243, s[36:39], s32 offen sc1\n" \
  "s_mov_b64 exec, -1\n" \
  "v_xor_b32 v235, s18, v54\n" \
  "v_bitop3_b32 v235, v55, s19, v235 bitop3:0xbe\n" \
  "s_mov_b32 s32, 26624\n" \
  "v_cmpx_ne_u32 vcc, 0, v235\n" \
  "buffer_load_dwordx4 v[54:57], v243, s[36:39], s32 offen sc1\n" \
  "s_mov_b64 exec, -1\n" \
  "v_xor_b32 v235, s18, v58\n" \
  "v_bitop3_b32 v235, v59, s19, v235 bitop3:0xbe\n" \
  "s_mov_b32 s32, 12288\n" \
  "v_cmpx_ne_u32 vcc, 0, v235\n" \
  "buffer_load_dwordx4 v[58:61], v243, s[36:39], s32 offen sc1\n" \
  "s_mov_b64 exec, -1\n" \
  "v_xor_b32 v235, s18, v62\n" \
  "v_bitop3_b32 v235, v63, s19, v235 bitop3:0xbe\n" \
  "s_mov_b32 s32, 21504\n" \
  "v_cmpx_ne_u32 vcc, 0, v235\n" \
  "buffer_load_dwordx4 v[62:65], v243, s[36:39], s32 offen sc1\n" \
  "s_mov_b64 exec, -1\n" \
  "v_xor_b32 v235, s18, v66\n" \
  "v_bitop3_b32 v235, v67, s19, v235 bitop3:0xbe\n" \
  "s_mov_b32 s32, 30720\n" \
  "v_cmpx_ne_u32 vcc, 0, v235\n" \
  "buffer_load_dwordx4 v[66:69], v243, s[36:39], s32 offen sc1\n" \
  "s_mov_b64 exec, -1\n" \
  "v_xor_b32 v235, s18, v70\n" \
  "v_bitop3_b32 v235, v71, s19, v235 bitop3:0xbe\n" \
  "s_mov_b32 s32, 16384\n" \
  "v_cmpx_ne_u32 vcc, 0, v235\n" \
  "buffer_load_dwordx4 v[70:73], v242, s[36:39], s32 offen sc1\n" \
  "s_mov_b64 exec, -1\n" \
  "v_xor_b32 v235, s18, v74\n" \
  "v_bitop3_b32 v235, v75, s19, v235 bitop3:0xbe\n" \
  "s_mov_b32 s32, 25600\n" \
  "v_cmpx_ne_u32 vcc, 0, v235\n" \
  "buffer_load_dwordx4 v[74:77], v242, s[36:39], s32 offen sc1\n" \
  "s_mov_b64 exec, -1\n" \
  "v_xor_b32 v235, s18, v78\n" \
  "v_bitop3_b32 v235, v79, s19, v235 bitop3:0xbe\n" \
  "s_mov_b32 s32, 34816\n" \
  "v_cmpx_ne_u32 vcc, 0, v235\n" \
  "buffer_load_dwordx4 v[78:81], v242, s[36:39], s32 offen sc1\n" \
  "s_mov_b64 exec, -1\n" \
  "s_memrealtime s[100:101]\n" \
  "s_waitcnt vmcnt(0) lgkmcnt(0)\n" \
  "s_sub_u32 s100, s100, s17\n" \
  "s_cmp_lt_u32 s100, 0x4c4b40\n" \
  "s_cbranch_scc1 .Lanc_%=\n" \
  "s_branch .Land_%=\n" \
  ".Lyns_%=:\n" \
  "s_sleep 2\n" \
  "s_movk_i32 s32, 4096\n" \
  "buffer_load_dwordx4 v[2:5], v252, s[36:39], s32 offen sc1\n" \
  "buffer_load_dwordx4 v[6:9], v252, s[36:39], s32 offen offset:1024 sc1\n" \
  "s_memrealtime s[100:101]\n" \
  "s_waitcnt vmcnt(0) lgkmcnt(0)\n" \
  "s_sub_u32 s100, s100, s17\n" \
  "s_cmp_lt_u32 s100, 0x4c4b40\n" \
  "s_cbranch_scc1 .Lync_%=\n" \
  "s_branch .Lynd_%=\n" \
  ".Lrns_%=:\n" \
  "s_add_u32 s32, s8, 4096\n" \
  "v_and_b32 v243, 1, v54\n" \
  "v_cmpx_eq_u32 vcc, 0, v243\n" \
  "buffer_load_dwordx4 v[54:57], v231, s[36:39], s8 offen sc1\n" \
  "s_mov_b64 exec, -1\n" \
  "v_and_b32 v243, 1, v58\n" \
  "v_cmpx_eq_u32 vcc, 0, v243\n" \
  "buffer_load_dwordx4 v[58:61], v231, s[36:39], s32 offen sc1\n" \
  "s_mov_b64 exec, -1\n" \
  "v_and_b32 v243, 1, v62\n" \
  "v_cmpx_eq_u32 vcc, 0, v243\n" \
  "buffer_load_dwordx4 v[62:65], v248, s[36:39], s9 offen sc1\n" \
  "s_mov_b64 exec, -1\n" \
  "s_memrealtime s[100:101]\n" \
  "s_waitcnt vmcnt(0) lgkmcnt(0)\n" \
  "s_sub_u32 s100, s100, s17\n" \
  "s_cmp_lt_u32 s100, 0x4c4b40\n" \
  "s_cbranch_scc1 .Lrnc_%=\n" \
  "s_branch .Lrnd_%=\n" \
  ".Lmns_%=:\n" \
  "s_sleep 2\n" \
  "s_mov_b32 s32, 35840\n" \
  "s_mov_b32 s33, 40448\n" \
  "buffer_load_dwordx4 v[22:25], v238, s[36:39], s32 offen offset:0 sc1\n" \
  "buffer_load_dwordx4 v[42:45], v238, s[36:39], s32 offen offset:2304 sc1\n" \
  "buffer_load_dwordx4 v[46:49], v238, s[36:39], s33 offen offset:0 sc1\n" \
  "buffer_load_dwordx4 v[50:53], v238, s[36:39], s33 offen offset:2304 sc1\n" \
  "s_memrealtime s[100:101]\n" \
  "s_waitcnt vmcnt(0) lgkmcnt(0)\n" \
  "s_sub_u32 s100, s100, s17\n" \
  "s_cmp_lt_u32 s100, 0x4c4b40\n" \
  "s_cbranch_scc1 .Lmnc_%=\n" \
  "s_branch .Lmnd_%=\n" \
  ".Lsns_%=:\n" \
  "buffer_load_dwordx2 v[66:67], v242, s[36:39], s29 offen sc1\n" \
  "s_memrealtime s[100:101]\n" \
  "s_waitcnt vmcnt(0) lgkmcnt(0)\n" \
  "s_sub_u32 s100, s100, s17\n" \
  "s_cmp_lt_u32 s100, 0x4c4b40\n" \
  "s_cbranch_scc1 .Lsnc_%=\n" \
  "s_branch .Lsnd_%=\n" \
  ".Lacs_%=:\n" \
  "v_xor_b32 v248, s18, v66\n" \
  "v_bitop3_b32 v248, v67, s19, v248 bitop3:0xbe\n" \
  "s_mov_b32 s32, 8192\n" \
  "v_cmpx_ne_u32 vcc, 0, v248\n" \
  "buffer_load_dwordx4 v[66:69], v246, s[36:39], s32 offen sc1\n" \
  "s_mov_b64 exec, -1\n" \
  "v_xor_b32 v248, s18, v70\n" \
  "v_bitop3_b32 v248, v71, s19, v248 bitop3:0xbe\n" \
  "s_mov_b32 s32, 17408\n" \
  "v_cmpx_ne_u32 vcc, 0, v248\n" \
  "buffer_load_dwordx4 v[70:73], v246, s[36:39], s32 offen sc1\n" \
  "s_mov_b64 exec, -1\n" \
  "v_xor_b32 v248, s18, v74\n" \
  "v_bitop3_b32 v248, v75, s19, v248 bitop3:0xbe\n" \
  "s_mov_b32 s32, 26624\n" \
  "v_cmpx_ne_u32 vcc, 0, v248\n" \
  "buffer_load_dwordx4 v[74:77], v246, s[36:39], s32 offen sc1\n" \
  "s_mov_b64 exec, -1\n" \
  "v_xor_b32 v248, s18, v78\n" \
  "v_bitop3_b32 v248, v79, s19, v248 bitop3:0xbe\n" \
  "s_mov_b32 s32, 12288\n" \
  "v_cmpx_ne_u32 vcc, 0, v248\n" \
  "buffer_load_dwordx4 v[78:81], v246, s[36:39], s32 offen sc1\n" \
  "s_mov_b64 exec, -1\n" \
  "v_xor_b32 v248, s18, v82\n" \
  "v_bitop3_b32 v248, v83, s19, v248 bitop3:0xbe\n" \
  "s_mov_b32 s32, 21504\n" \
  "v_cmpx_ne_u32 vcc, 0, v248\n" \
  "buffer_load_dwordx4 v[82:85], v246, s[36:39], s32 offen sc1\n" \
  "s_mov_b64 exec, -1\n" \
  "v_xor_b32 v248, s18, v86\n" \
  "v_bitop3_b32 v248, v87, s19, v248 bitop3:0xbe\n" \
  "s_mov_b32 s32, 30720\n" \
  "v_cmpx_ne_u32 vcc, 0, v248\n" \
  "buffer_load_dwordx4 v[86:89], v246, s[36:39], s32 offen sc1\n" \
  "s_mov_b64 exec, -1\n" \
  "v_xor_b32 v248, s18, v90\n" \
  "v_bitop3_b32 v248, v91, s19, v248 bitop3:0xbe\n" \
  "s_mov_b32 s32, 16384\n" \
  "v_cmpx_ne_u32 vcc, 0, v248\n" \
  "buffer_load_dwordx4 v[90:93], v245, s[36:39], s32 offen sc1\n" \
  "s_mov_b64 exec, -1\n" \
  "v_xor_b32 v248, s18, v94\n" \
  "v_bitop3_b32 v248, v95, s19, v248 bitop3:0xbe\n" \
  "s_mov_b32 s32, 25600\n" \
  "v_cmpx_ne_u32 vcc, 0, v248\n" \
  "buffer_load_dwordx4 v[94:97], v245, s[36:39], s32 offen sc1\n" \
  "s_mov_b64 exec, -1\n" \
  "v_xor_b32 v248, s18, v98\n" \
  "v_bitop3_b32 v248, v99, s19, v248 bitop3:0xbe\n" \
  "s_mov_b32 s32, 34816\n" \
  "v_cmpx_ne_u32 vcc, 0, v248\n" \
  "buffer_load_dwordx4 v[98:101], v245, s[36:39], s32 offen sc1\n" \
  "s_mov_b64 exec, -1\n" \
  "s_memrealtime s[100:101]\n" \
  "s_waitcnt vmcnt(0) lgkmcnt(0)\n" \
  "s_sub_u32 s100, s100, s17\n" \
  "s_cmp_lt_u32 s100, 0x4c4b40\n" \
  "s_cbranch_scc1 .Lacc_%=\n" \
  "s_branch .Lacd_%=\n" \
  ".Lycs_%=:\n" \
  "s_sleep 2\n" \
  "s_movk_i32 s32, 4096\n" \
  "buffer_load_dwordx4 v[42:45], v252, s[36:39], s32 offen sc1\n" \
  "buffer_load_dwordx4 v[46:49], v252, s[36:39], s32 offen offset:1024 sc1\n" \
  "s_memrealtime s[100:101]\n" \
  "s_waitcnt vmcnt(0) lgkmcnt(0)\n" \
  "s_sub_u32 s100, s100, s17\n" \
  "s_cmp_lt_u32 s100, 0x4c4b40\n" \
  "s_cbranch_scc1 .Lycc_%=\n" \
  "s_branch .Lycd_%=\n" \
  ".Lws_%=:\n" \
  "s_lshl_b32 s33, s20, 10\n" \
  "s_add_u32 m0, s33, 85376\n" \
  "s_mov_b32 s32, 2925632\n" \
  "buffer_load_dwordx4 v225, s[36:39], s32 offen sc1 lds\n" \
  "s_add_u32 m0, s33, 89472\n" \
  "s_mov_b32 s32, 3523648\n" \
  "buffer_load_dwordx4 v225, s[36:39], s32 offen sc1 lds\n" \
  "s_add_u32 m0, s33, 116096\n" \
  "s_mov_b32 s32, 4121664\n" \
  "buffer_load_dwordx4 v225, s[36:39], s32 offen sc1 lds\n" \
  "s_add_u32 m0, s33, 120192\n" \
  "s_mov_b32 s32, 4719680\n" \
  "buffer_load_dwordx4 v225, s[36:39], s32 offen sc1 lds\n" \
  "s_branch .Lwd_%=\n" \
  ".Lxs_%=:\n" \
  "v_bfe_u32 v245, v191, 5, 1\n" \
  "v_mul_u32_u24 v245, 22528, v245\n" \
  "v_add_u32 v255, v112, v245\n" \
  "v_sub_u32 v248, v113, v245\n" \
  "v_add_u32 v248, 55296, v248\n" \
  "v_add_u32 v255, 0x8000, v255\n" \
  "ds_read_b64 v[14:15], v248 offset:39296\n" \
  "ds_read_b64 v[26:27], v248 offset:38272\n" \
  "ds_read_b64 v[28:29], v248 offset:37248\n" \
  "ds_read_b64 v[30:31], v248 offset:36224\n" \
  "ds_read_b64 v[32:33], v255 offset:36224\n" \
  "ds_read_b64 v[34:35], v255 offset:37248\n" \
  "ds_read_b64 v[36:37], v255 offset:38272\n" \
  "ds_read_b64 v[38:39], v255 offset:39296\n" \
  "s_waitcnt lgkmcnt(0)\n" \
  "v_lshlrev_b32 v245, 16, v15\n" \
  "v_add_f32 v40, v40, v245\n" \
  "v_and_b32 v245, 0xffff0000, v15\n" \
  "v_sub_f32 v44, v44, v245\n" \
  "v_lshlrev_b32 v245, 16, v32\n" \
  "v_add_f32 v66, v66, v245\n" \
  "v_and_b32 v245, 0xffff0000, v32\n" \
  "v_sub_f32 v70, v70, v245\n" \
  "v_lshlrev_b32 v245, 16, v14\n" \
  "v_add_f32 v82, v82, v245\n" \
  "v_and_b32 v245, 0xffff0000, v14\n" \
  "v_sub_f32 v86, v86, v245\n" \
  "v_lshlrev_b32 v245, 16, v33\n" \
  "v_add_f32 v90, v90, v245\n" \
  "v_and_b32 v245, 0xffff0000, v33\n" \
  "v_sub_f32 v94, v94, v245\n" \
  "v_lshlrev_b32 v245, 16, v27\n" \
  "v_add_f32 v41, v41, v245\n" \
  "v_and_b32 v245, 0xffff0000, v27\n" \
  "v_sub_f32 v45, v45, v245\n" \
  "v_lshlrev_b32 v245, 16, v34\n" \
  "v_add_f32 v67, v67, v245\n" \
  "v_and_b32 v245, 0xffff0000, v34\n" \
  "v_sub_f32 v71, v71, v245\n" \
  "v_lshlrev_b32 v245, 16, v26\n" \
  "v_add_f32 v83, v83, v245\n" \
  "v_and_b32 v245, 0xffff0000, v26\n" \
  "v_sub_f32 v87, v87, v245\n" \
  "v_lshlrev_b32 v245, 16, v35\n" \
  "v_add_f32 v91, v91, v245\n" \
  "v_and_b32 v245, 0xffff0000, v35\n" \
  "v_sub_f32 v95, v95, v245\n" \
  "v_lshlrev_b32 v245, 16, v29\n" \
  "v_add_f32 v42, v42, v245\n" \
  "v_and_b32 v245, 0xffff0000, v29\n" \
  "v_sub_f32 v46, v46, v245\n" \
  "v_lshlrev_b32 v245, 16, v36\n" \
  "v_add_f32 v68, v68, v245\n" \
  "v_and_b32 v245, 0xffff0000, v36\n" \
  "v_sub_f32 v72, v72, v245\n" \
  "v_lshlrev_b32 v245, 16, v28\n" \
  "v_add_f32 v84, v84, v245\n" \
  "v_and_b32 v245, 0xffff0000, v28\n" \
  "v_sub_f32 v88, v88, v245\n" \
  "v_lshlrev_b32 v245, 16, v37\n" \
  "v_add_f32 v92, v92, v245\n" \
  "v_and_b32 v245, 0xffff0000, v37\n" \
  "v_sub_f32 v96, v96, v245\n" \
  "v_lshlrev_b32 v245, 16, v31\n" \
  "v_add_f32 v43, v43, v245\n" \
  "v_and_b32 v245, 0xffff0000, v31\n" \
  "v_sub_f32 v47, v47, v245\n" \
  "v_lshlrev_b32 v245, 16, v38\n" \
  "v_add_f32 v69, v69, v245\n" \
  "v_and_b32 v245, 0xffff0000, v38\n" \
  "v_sub_f32 v73, v73, v245\n" \
  "v_lshlrev_b32 v245, 16, v30\n" \
  "v_add_f32 v85, v85, v245\n" \
  "v_and_b32 v245, 0xffff0000, v30\n" \
  "v_sub_f32 v89, v89, v245\n" \
  "v_lshlrev_b32 v245, 16, v39\n" \
  "v_add_f32 v93, v93, v245\n" \
  "v_and_b32 v245, 0xffff0000, v39\n" \
  "v_sub_f32 v97, v97, v245\n" \
  "s_branch .Lxd_%=\n" \
  ".Lts_%=:\n" \
  "v_and_b32 v239, 15, v191\n" \
  "v_lshrrev_b32 v237, 4, v191\n" \
  "v_lshl_or_b32 v239, v237, 6, v239\n" \
  "v_mul_u32_u24 v239, 9344, v239\n" \
  "v_lshl_add_u32 v239, s20, 4, v239\n" \
  "s_add_u32 s32, s22, 0\n" \
  "v_and_b32 v237, 1, v106\n" \
  "v_cmpx_ne_u32 vcc, 0, v237\n" \
  "buffer_load_dwordx4 v[106:109], v239, s[36:39], s32 offen sc1\n" \
  "s_mov_b64 exec, -1\n" \
  "s_add_u32 s32, s22, 149504\n" \
  "v_and_b32 v237, 1, v124\n" \
  "v_cmpx_ne_u32 vcc, 0, v237\n" \
  "buffer_load_dwordx4 v[124:127], v239, s[36:39], s32 offen sc1\n" \
  "s_mov_b64 exec, -1\n" \
  "s_add_u32 s32, s22, 299008\n" \
  "v_and_b32 v237, 1, v128\n" \
  "v_cmpx_ne_u32 vcc, 0, v237\n" \
  "buffer_load_dwordx4 v[128:131], v239, s[36:39], s32 offen sc1\n" \
  "s_mov_b64 exec, -1\n" \
  "s_add_u32 s32, s22, 448512\n" \
  "v_and_b32 v237, 1, v132\n" \
  "v_cmpx_ne_u32 vcc, 0, v237\n" \
  "buffer_load_dwordx4 v[132:135], v239, s[36:39], s32 offen sc1\n" \
  "s_mov_b64 exec, -1\n" \
  "s_memrealtime s[100:101]\n" \
  "s_waitcnt vmcnt(0) lgkmcnt(0)\n" \
  "s_sub_u32 s100, s100, s17\n" \
  "s_cmp_lt_u32 s100, 0x4c4b40\n" \
  "s_cbranch_scc1 .Ltc_%=\n" \
  "s_branch .Ltd_%=\n" \
  ".Lrcs_%=:\n" \
  "s_add_u32 s32, s8, 4096\n" \
  "v_and_b32 v246, 1, v44\n" \
  "v_cmpx_eq_u32 vcc, 0, v246\n" \
  "buffer_load_dwordx4 v[44:47], v230, s[36:39], s8 offen sc1\n" \
  "s_mov_b64 exec, -1\n" \
  "v_and_b32 v246, 1, v66\n" \
  "v_cmpx_eq_u32 vcc, 0, v246\n" \
  "buffer_load_dwordx4 v[66:69], v230, s[36:39], s32 offen sc1\n" \
  "s_mov_b64 exec, -1\n" \
  "v_and_b32 v246, 1, v70\n" \
  "v_cmpx_eq_u32 vcc, 0, v246\n" \
  "buffer_load_dwordx4 v[70:73], v240, s[36:39], s9 offen sc1\n" \
  "s_mov_b64 exec, -1\n" \
  "s_memrealtime s[100:101]\n" \
  "s_waitcnt vmcnt(0) lgkmcnt(0)\n" \
  "s_sub_u32 s100, s100, s17\n" \
  "s_cmp_lt_u32 s100, 0x4c4b40\n" \
  "s_cbranch_scc1 .Lrcc_%=\n" \
  "s_branch .Lrcd_%=\n" \
  ".Lmcs_%=:\n" \
  "s_sleep 2\n" \
  "s_mov_b32 s32, 35840\n" \
  "s_mov_b32 s33, 40448\n" \
  "buffer_load_dwordx4 v[28:31], v238, s[36:39], s32 offen offset:0 sc1\n" \
  "buffer_load_dwordx4 v[32:35], v238, s[36:39], s32 offen offset:2304 sc1\n" \
  "buffer_load_dwordx4 v[36:39], v238, s[36:39], s33 offen offset:0 sc1\n" \
  "buffer_load_dwordx4 v[40:43], v238, s[36:39], s33 offen offset:2304 sc1\n" \
  "s_memrealtime s[100:101]\n" \
  "s_waitcnt vmcnt(0) lgkmcnt(0)\n" \
  "s_sub_u32 s100, s100, s17\n" \
  "s_cmp_lt_u32 s100, 0x4c4b40\n" \
  "s_cbranch_scc1 .Lmcc_%=\n" \
  "s_branch .Lmcd_%=\n" \
  ".Lscs_%=:\n" \
  "buffer_load_dwordx2 v[8:9], v245, s[36:39], s29 offen sc1\n" \
  "s_memrealtime s[100:101]\n" \
  "s_waitcnt vmcnt(0) lgkmcnt(0)\n" \
  "s_sub_u32 s100, s100, s17\n" \
  "s_cmp_lt_u32 s100, 0x4c4b40\n" \
  "s_cbranch_scc1 .Lscc_%=\n" \
  "s_branch .Lscd_%=\n" \
  ".Lcol_%=:\n" \
  "s_load_dwordx2 s[48:49], s[0:1], 0\n" \
  "v_lshrrev_b32 v255, 6, v0\n" \
  "s_getpc_b64 s[80:81]\n" \
  ".Lpcc_%=:\n" \
  "v_readfirstlane_b32 s20, v255\n" \
  "s_mov_b32 s82, .Ladm_b-.Lpcc_%=\n" \
  "s_mov_b32 s83, 131072\n" \
  "s_mov_b32 m0, 162176\n" \
  "s_lshr_b32 s98, s2, 3\n" \
  "s_mul_i32 s98, s98, 20\n" \
  "v_add_u32 v254, s98, v0\n" \
  "v_lshlrev_b32 v253, 7, v254\n" \
  "s_cmp_eq_u32 s20, 0\n" \
  "s_cselect_b64 exec, 1048575, 0\n" \
  "buffer_load_dword v253, s[80:83], 0 offen lds\n" \
  "s_mov_b64 exec, -1\n" \
  "s_mov_b32 s50, 2359296\n" \
  "s_mov_b32 s51, 131072\n" \
  "s_mov_b32 s78, 26944\n" \
  "s_mov_b32 s79, 131072\n" \
  "v_and_b32 v191, 63, v0\n" \
  "v_and_b32 v254, 31, v191\n" \
  "v_lshrrev_b32 v253, 5, v191\n" \
  "v_mul_u32_u24 v252, 1179648, v253\n" \
  "v_lshl_add_u32 v251, v254, 4, v252\n" \
  "v_lshl_add_u32 v250, v254, 2, v252\n" \
  "v_lshlrev_b32 v252, 4, v191\n" \
  "s_and_b32 s84, s2, 7\n" \
  "s_lshr_b32 s85, s2, 3\n" \
  "s_lshl_b32 s98, s84, 5\n" \
  "s_add_u32 s86, s98, s85\n" \
  "s_lshl_b32 s98, s20, 8\n" \
  "s_add_u32 s98, s98, s86\n" \
  "s_mul_i32 s87, s98, 1152\n" \
  "s_waitcnt lgkmcnt(0)\n" \
  "buffer_load_dwordx4 v[2:5], v251, s[48:51], s87 offen offset:0\n" \
  "buffer_load_dwordx4 v[6:9], v251, s[48:51], s87 offen offset:512\n" \
  "buffer_load_dword v249, v250, s[48:51], s87 offen offset:1024\n" \
  "s_movk_i32 s32, 22528\n" \
  "buffer_load_dwordx4 v[10:13], v252, s[76:79], s32 offen offset:0\n" \
  "s_memrealtime s[16:17]\n" \
  "s_lshr_b32 s32, s20, 1\n" \
  "s_lshl3_add_u32 s32, s32, 8\n" \
  "s_load_dwordx2 s[52:53], s[0:1], s32\n" \
  "s_load_dwordx2 s[4:5], s[0:1], 24\n" \
  "s_load_dwordx2 s[64:65], s[0:1], 40\n" \
  "s_load_dwordx2 s[36:37], s[0:1], 48\n" \
  "s_load_dwordx2 s[68:69], s[0:1], 56\n" \
  "s_load_dwordx2 s[72:73], s[0:1], 64\n" \
  "s_load_dwordx2 s[40:41], s[0:1], 72\n" \
  "s_load_dwordx2 s[44:45], s[0:1], 80\n" \
  "s_mov_b32 s6, 18874368\n" \
  "s_mov_b32 s7, 131072\n" \
  "s_mov_b32 s66, 3981312\n" \
  "s_mov_b32 s67, 131072\n" \
  "s_mov_b32 s38, 134217728\n" \
  "s_mov_b32 s39, 131072\n" \
  "s_mov_b32 s70, 9216\n" \
  "s_mov_b32 s71, 131072\n" \
  "s_mov_b32 s74, 4096\n" \
  "s_mov_b32 s75, 131072\n" \
  "s_mov_b32 s42, 18874368\n" \
  "s_mov_b32 s43, 131072\n" \
  "s_mov_b32 s46, 2359296\n" \
  "s_mov_b32 s47, 131072\n" \
  "s_mov_b32 s54, 1183744\n" \
  "s_mov_b32 s55, 131072\n" \
  "v_and_b32 v248, 15, v191\n" \
  "v_lshrrev_b32 v247, 4, v191\n" \
  "v_lshlrev_b32 v246, 4, v0\n" \
  "v_lshrrev_b32 v245, 1, v248\n" \
  "v_and_b32 v244, 1, v247\n" \
  "v_lshl_add_u32 v243, v244, 9, s86\n" \
  "v_and_b32 v244, 1, v248\n" \
  "v_and_b32 v242, 3, v245\n" \
  "v_lshl_add_u32 v241, v244, 2, v242\n" \
  "v_lshl_add_u32 v242, v241, 8, s86\n" \
  "v_lshlrev_b32 v241, 1, v247\n" \
  "v_cvt_f32_u32 v240, v245\n" \
  "v_cvt_f32_u32 v245, v243\n" \
  "v_cmp_eq_u32 vcc, v244, v253\n" \
  "v_mul_f32 v240, 3120562176, v240\n" \
  "v_mov_b32 v243, 1048576000\n" \
  "v_cndmask_b32 v243, 0, v243, vcc\n" \
  "v_lshlrev_b32 v239, 31, v244\n" \
  "v_mul_f32 v245, v240, v245\n" \
  "v_xor_b32 v239, v243, v239\n" \
  "v_mul_f32 v240, 1132462080, v240\n" \
  "v_fma_f32 v238, v240, 0, v245\n" \
  "v_cos_f32 v237, v238\n" \
  "v_sin_f32 v236, v238\n" \
  "v_mul_f32 v235, v237, v243\n" \
  "v_mul_f32 v234, v236, v239\n" \
  "v_mul_f32 v233, v236, v243\n" \
  "v_mul_f32 v232, v237, v239\n" \
  "v_cvt_pk_f16_f32 v235, v235, -v234\n" \
  "v_cvt_pk_f16_f32 v233, v233, v232\n" \
  "v_accvgpr_write_b32 a160, v235\n" \
  "v_accvgpr_write_b32 a164, v233\n" \
  "v_fma_f32 v238, v240, 4.0, v245\n" \
  "v_cos_f32 v237, v238\n" \
  "v_sin_f32 v236, v238\n" \
  "v_mul_f32 v235, v237, v243\n" \
  "v_mul_f32 v234, v236, v239\n" \
  "v_mul_f32 v233, v236, v243\n" \
  "v_mul_f32 v232, v237, v239\n" \
  "v_cvt_pk_f16_f32 v235, v235, -v234\n" \
  "v_cvt_pk_f16_f32 v233, v233, v232\n" \
  "v_accvgpr_write_b32 a161, v235\n" \
  "v_accvgpr_write_b32 a165, v233\n" \
  "v_fma_f32 v238, v240, 1.0, v245\n" \
  "v_cos_f32 v237, v238\n" \
  "v_sin_f32 v236, v238\n" \
  "v_mul_f32 v235, v237, v243\n" \
  "v_mul_f32 v234, v236, v239\n" \
  "v_mul_f32 v233, v236, v243\n" \
  "v_mul_f32 v232, v237, v239\n" \
  "v_cvt_pk_f16_f32 v235, v235, -v234\n" \
  "v_cvt_pk_f16_f32 v233, v233, v232\n" \
  "v_accvgpr_write_b32 a162, v235\n" \
  "v_accvgpr_write_b32 a166, v233\n" \
  "v_fma_f32 v238, v240, 4.0, v245\n" \
  "v_add_f32 v238, v238, v240\n" \
  "v_cos_f32 v237, v238\n" \
  "v_sin_f32 v236, v238\n" \
  "v_mul_f32 v235, v237, v243\n" \
  "v_mul_f32 v234, v236, v239\n" \
  "v_mul_f32 v233, v236, v243\n" \
  "v_mul_f32 v232, v237, v239\n" \
  "v_cvt_pk_f16_f32 v235, v235, -v234\n" \
  "v_cvt_pk_f16_f32 v233, v233, v232\n" \
  "v_accvgpr_write_b32 a163, v235\n" \
  "v_accvgpr_write_b32 a167, v233\n" \
  "v_cvt_f32_u32 v240, v242\n" \
  "v_cvt_f32_u32 v245, v241\n" \
  "v_cmp_lt_u32 vcc, 7, v248\n" \
  "v_mul_f32 v240, 973078528, v240\n" \
  "v_mul_f32 v238, v240, v245\n" \
  "v_cos_f32 v237, v238\n" \
  "v_sin_f32 v236, v238\n" \
  "v_mul_f32 v237, 0.5, v237\n" \
  "v_mul_f32 v236, 0.5, v236\n" \
  "v_cvt_pk_f16_f32 v235, v237, -v236\n" \
  "v_cvt_pk_f16_f32 v234, v236, v237\n" \
  "v_cndmask_b32 v233, v235, 0, vcc\n" \
  "v_cndmask_b32 v235, 0, v235, vcc\n" \
  "v_cndmask_b32 v232, v234, 0, vcc\n" \
  "v_xor_b32 v234, 2147516416, v234\n" \
  "v_accvgpr_write_b32 a228, v233\n" \
  "v_cndmask_b32 v234, 0, v234, vcc\n" \
  "v_accvgpr_write_b32 a229, v235\n" \
  "v_accvgpr_write_b32 a232, v232\n" \
  "v_accvgpr_write_b32 a233, v234\n" \
  "v_add_f32 v238, v238, v240\n" \
  "v_cos_f32 v237, v238\n" \
  "v_sin_f32 v236, v238\n" \
  "v_mul_f32 v237, 0.5, v237\n" \
  "v_mul_f32 v236, 0.5, v236\n" \
  "v_cvt_pk_f16_f32 v235, v237, -v236\n" \
  "v_cvt_pk_f16_f32 v234, v236, v237\n" \
  "v_cndmask_b32 v233, v235, 0, vcc\n" \
  "v_cndmask_b32 v235, 0, v235, vcc\n" \
  "v_cndmask_b32 v232, v234, 0, vcc\n" \
  "v_xor_b32 v234, 2147516416, v234\n" \
  "v_accvgpr_write_b32 a230, v233\n" \
  "v_cndmask_b32 v234, 0, v234, vcc\n" \
  "v_accvgpr_write_b32 a231, v235\n" \
  "v_accvgpr_write_b32 a234, v232\n" \
  "v_accvgpr_write_b32 a235, v234\n" \
  "s_lshr_b32 s88, s2, 6\n" \
  "s_lshl_b32 s98, s84, 2\n" \
  "s_lshr_b32 s100, s85, 1\n" \
  "s_and_b32 s100, s100, 3\n" \
  "s_add_u32 s89, s98, s100\n" \
  "s_mul_i32 s98, s86, 9344\n" \
  "s_add_u32 s82, s98, 524288\n" \
  "s_lshl_b32 s98, s86, 8\n" \
  "s_add_u32 s11, s98, 9764864\n" \
  "s_lshl_b32 s98, s86, 4\n" \
  "s_add_u32 s28, s98, 65536\n" \
  "s_and_b32 s98, s84, 1\n" \
  "s_lshl_b32 s98, s98, 3\n" \
  "s_and_b32 s100, s85, 7\n" \
  "s_add_u32 s90, s98, s100\n" \
  "s_lshl_b32 s98, s88, 5\n" \
  "s_add_u32 s98, s98, s89\n" \
  "s_lshl_b32 s98, s98, 1\n" \
  "s_and_b32 s100, s85, 1\n" \
  "s_add_u32 s98, s98, s100\n" \
  "s_lshl_b32 s98, s98, 7\n" \
  "s_add_u32 s80, s98, 262144\n" \
  "s_mov_b32 s35, 987991609\n" \
  "v_mul_u32_u24 v245, 9, v247\n" \
  "v_add_u32 v243, 8, v245\n" \
  "v_lshlrev_b32 v245, 4, v248\n" \
  "v_lshl_add_u32 v242, v243, 16, v245\n" \
  "v_lshrrev_b32 v245, 3, v191\n" \
  "v_and_b32 v243, 7, v191\n" \
  "v_lshlrev_b32 v241, 4, v243\n" \
  "v_lshl_add_u32 v243, v245, 16, v241\n" \
  "v_add_u32 v245, s80, v243\n" \
  "s_add_u32 s83, s82, 4096\n" \
  "s_add_u32 s91, s82, 8192\n" \
  "v_mov_b32 v14, -1\n" \
  "v_mov_b32 v15, -1\n" \
  "v_mov_b32 v16, -1\n" \
  "v_mov_b32 v17, -1\n" \
  "v_cmp_eq_u32 vcc, 24, v191\n" \
  "v_mov_b32 v255, s28\n" \
  "s_cmp_eq_u32 s20, 2\n" \
  "v_cndmask_b32 v245, v245, v255, vcc\n" \
  "s_cselect_b64 vcc, -1, 0\n" \
  "s_cselect_b32 s91, s11, s91\n" \
  "v_cndmask_b32 v243, v246, v242, vcc\n" \
  "s_cmp_eq_u32 s20, 3\n" \
  "s_cselect_b64 vcc, -1, 0\n" \
  "s_cselect_b32 s91, 0, s91\n" \
  "s_cselect_b32 s32, 0, -1\n" \
  "v_cndmask_b32 v243, v243, v245, vcc\n" \
  "s_cselect_b32 s92, 33554431, -1\n" \
  "s_cselect_b32 s93, 0, -1\n" \
  "s_cmp_eq_u32 s20, 1\n" \
  "s_cselect_b64 s[92:93], 15, s[92:93]\n" \
  "v_mov_b32 v18, s32\n" \
  "v_mov_b32 v19, s32\n" \
  "v_mov_b32 v20, s32\n" \
  "v_mov_b32 v21, s32\n" \
  "s_movk_i32 s32, 768\n" \
  "v_lshlrev_b32 v245, 5, v191\n" \
  "v_bfe_u32 v242, v191, 5, 1\n" \
  "v_and_or_b32 v245, v245, s32, v242\n" \
  "v_lshrrev_b32 v242, 3, v191\n" \
  "v_mul_u32_u24 v245, 9216, v245\n" \
  "v_bitop3_b32 v242, v242, v191, 6 bitop3:0x6c\n" \
  "v_and_b32 v242, 7, v242\n" \
  "v_lshl_or_b32 v22, v242, 4, v245\n" \
  "v_add_u32 v23, 17408, v22\n" \
  "v_add_u32 v24, 34816, v22\n" \
  "v_add_u32 v25, 52224, v22\n" \
  "s_cmp_eq_u32 s20, 0\n" \
  "s_cselect_b64 s[58:59], -1, 0\n" \
  "s_movk_i32 s34, 144\n" \
  "v_cmp_gt_u32 s[94:95], s34, v0\n" \
  "v_mov_b32 v26, 0\n" \
  "v_mov_b32 v27, 0\n" \
  "v_mov_b32 v255, 161664\n" \
  "ds_write_b64 v255, v[26:27] offset:0\n" \
  "v_bfrev_b32 v245, 1\n" \
  "v_cndmask_b32 v245, v245, v246, s[58:59]\n" \
  "v_and_b32 v242, 1, v191\n" \
  "v_lshl_add_u32 v241, v253, 1, v242\n" \
  "v_lshlrev_b32 v242, 3, v241\n" \
  "v_and_b32 v241, 7, v191\n" \
  "v_lshlrev_b32 v244, 1, v241\n" \
  "v_and_b32 v241, 12, v191\n" \
  "v_add_u32 v240, v241, v247\n" \
  "s_lshl_b32 s98, s20, 4\n" \
  "v_add_u32 v247, s98, v240\n" \
  "v_lshlrev_b32 v241, 1, v247\n" \
  "v_bfe_u32 v247, v191, 1, 1\n" \
  "v_lshrrev_b32 v240, 2, v191\n" \
  "v_and_b32 v239, 12, v240\n" \
  "v_lshl_add_u32 v240, v247, 6, v239\n" \
  "s_lshr_b32 s98, s84, 1\n" \
  "s_lshl_b32 s98, s98, 4\n" \
  "s_add_u32 s98, s98, s20\n" \
  "v_add_u32 v247, s98, v240\n" \
  "v_lshlrev_b32 v240, 1, v247\n" \
  "s_mul_i32 s98, s84, 1152\n" \
  "s_and_b32 s100, s85, 7\n" \
  "s_lshl_b32 s100, s100, 7\n" \
  "s_add_u32 s98, s98, s100\n" \
  "s_lshl_b32 s100, s20, 5\n" \
  "s_add_u32 s25, s98, s100\n" \
  "s_mul_i32 s98, s84, 1152\n" \
  "s_and_b32 s100, s85, 6\n" \
  "s_lshl_b32 s100, s100, 4\n" \
  "s_add_u32 s98, s98, s100\n" \
  "s_lshr_b32 s100, s20, 1\n" \
  "s_lshl_b32 s100, s100, 4\n" \
  "s_add_u32 s98, s98, s100\n" \
  "s_add_u32 s13, s98, 1024\n" \
  "s_lshl_b32 s15, s90, 8\n" \
  "s_waitcnt lgkmcnt(0)\n" \
  "s_mov_b32 s17, s16\n" \
  "v_and_b32 v247, 127, v0\n" \
  "s_mul_i32 s98, s86, 4624\n" \
  "v_lshl_add_u32 v239, v247, 4, s98\n" \
  "v_add_u32 v238, 3440, v239\n" \
  "v_add_u32 v237, 2048, v239\n" \
  "v_add_u32 v236, 1392, v239\n" \
  "s_lshr_b32 s98, s20, 1\n" \
  "s_mul_i32 s98, s98, 6144\n" \
  "s_and_b32 s100, s20, 1\n" \
  "s_lshl_b32 s100, s100, 10\n" \
  "s_add_u32 s98, s98, s100\n" \
  "s_add_u32 s56, s98, 19584\n" \
  "v_cmp_gt_u32 vcc, 57, v247\n" \
  "v_subrev_u32 v255, 1183744, v238\n" \
  "s_mov_b32 m0, s56\n" \
  "v_cndmask_b32 v237, v236, v237, vcc\n" \
  "buffer_load_dwordx4 v239, s[52:55], 0 offen offset:0 lds\n" \
  "v_min_u32 v238, v238, v255\n" \
  "s_add_u32 m0, s56, 2048\n" \
  "s_bitcmp1_b32 s20, 0\n" \
  "buffer_load_dwordx4 v237, s[52:55], 0 offen offset:0 lds\n" \
  "s_add_u32 m0, s56, 4096\n" \
  "s_cselect_b32 exec_hi, 32767, -1\n" \
  "buffer_load_dwordx4 v238, s[52:55], 0 offen offset:0 lds\n" \
  "s_mov_b64 exec, -1\n" \
  "s_movk_i32 s32, 10240\n" \
  "buffer_load_dwordx4 v[26:29], v246, s[76:79], s32 offen offset:0\n" \
  "buffer_load_dwordx2 v[30:31], off, s[36:39], 0 sc1\n" \
  "buffer_store_dwordx4 v[14:17], v246, s[36:39], s82 offen offset:0 sc1\n" \
  "buffer_store_dwordx4 v[14:17], v246, s[36:39], s83 offen offset:0 sc1\n" \
  "s_mov_b64 exec, s[92:93]\n" \
  "buffer_store_dwordx4 v[18:21], v243, s[36:39], s91 offen offset:0 sc1\n" \
  "s_mov_b64 exec, -1\n" \
  "s_lshl_b32 s98, s85, 3\n" \
  "s_lshl_b32 s100, s20, 1\n" \
  "s_add_u32 s98, s98, s100\n" \
  "s_mul_i32 s98, s98, 9216\n" \
  "s_mul_i32 s100, s84, 1152\n" \
  "s_add_u32 s98, s98, s100\n" \
  "s_add_u32 s32, s98, 1024\n" \
  "s_lshl_b32 s98, s20, 10\n" \
  "s_add_u32 s33, s98, 155008\n" \
  "s_mov_b32 m0, s33\n" \
  "s_movk_i32 s31, 4096\n" \
  "s_cmp_gt_u32 s20, 1\n" \
  "buffer_load_dwordx4 v22, s[4:7], s32 offen offset:0 lds\n" \
  "s_cselect_b32 s31, -28672, s31\n" \
  "s_add_u32 m0, s33, s31\n" \
  "s_add_u32 s32, s32, 9437184\n" \
  "buffer_load_dwordx4 v22, s[4:7], s32 offen offset:0 lds\n" \
  "buffer_load_dwordx2 v[16:17], v242, s[68:71], s25 offen offset:0\n" \
  "buffer_load_ushort v247, v244, s[68:71], s13 offen offset:0\n" \
  "buffer_load_ushort v244, v241, s[72:75], s15 offen offset:0\n" \
  "buffer_load_ushort v243, v241, s[72:75], s15 offen offset:128\n" \
  "buffer_load_ushort v242, v240, s[72:75], s15 offen offset:0\n" \
  "v_mul_u32_u24 v241, 2448, v253\n" \
  "s_mul_i32 s98, s20, 4896\n" \
  "v_add_u32 v240, s98, v241\n" \
  "v_lshrrev_b32 v241, 3, v254\n" \
  "v_lshlrev_b32 v239, 4, v241\n" \
  "v_lshl_add_u32 v241, v254, 5, v239\n" \
  "v_add_u32 v239, v241, v240\n" \
  "v_lshl_add_u32 v241, v254, 3, v240\n" \
  "s_waitcnt vmcnt(18)\n" \
  "v_lshlrev_b32 v14, 16, v2\n" \
  "v_and_b32 v15, 4294901760, v2\n" \
  "v_lshlrev_b32 v18, 16, v3\n" \
  "v_and_b32 v19, 4294901760, v3\n" \
  "v_lshlrev_b32 v20, 16, v4\n" \
  "v_and_b32 v21, 4294901760, v4\n" \
  "v_lshlrev_b32 v32, 16, v5\n" \
  "v_and_b32 v33, 4294901760, v5\n" \
  "s_waitcnt vmcnt(17)\n" \
  "v_lshlrev_b32 v34, 16, v6\n" \
  "v_and_b32 v35, 4294901760, v6\n" \
  "v_lshlrev_b32 v36, 16, v7\n" \
  "v_and_b32 v37, 4294901760, v7\n" \
  "v_lshlrev_b32 v38, 16, v8\n" \
  "v_and_b32 v39, 4294901760, v8\n" \
  "v_lshlrev_b32 v40, 16, v9\n" \
  "v_and_b32 v41, 4294901760, v9\n" \
  "s_waitcnt vmcnt(16)\n" \
  "v_lshlrev_b32 v42, 16, v249\n" \
  "v_and_b32 v43, 4294901760, v249\n" \
  "v_mov_b32 v45, 925353388\n" \
  "v_mov_b32_dpp v46, v14 row_newbcast:0 row_mask:0xf bank_mask:0xf\n" \
  "v_mov_b32_dpp v240, v14 row_newbcast:0 row_mask:0xf bank_mask:0xf\n" \
  "s_nop 1\n" \
  "v_permlane16_swap_b32 v46, v240\n" \
  "v_pk_add_f32 v[14:15], v[14:15], v[46:47] op_sel_hi:[1,0] neg_lo:[0,1] neg_hi:[0,1]\n" \
  "v_pk_add_f32 v[18:19], v[18:19], v[46:47] op_sel_hi:[1,0] neg_lo:[0,1] neg_hi:[0,1]\n" \
  "v_pk_add_f32 v[20:21], v[20:21], v[46:47] op_sel_hi:[1,0] neg_lo:[0,1] neg_hi:[0,1]\n" \
  "v_pk_add_f32 v[32:33], v[32:33], v[46:47] op_sel_hi:[1,0] neg_lo:[0,1] neg_hi:[0,1]\n" \
  "v_pk_add_f32 v[34:35], v[34:35], v[46:47] op_sel_hi:[1,0] neg_lo:[0,1] neg_hi:[0,1]\n" \
  "v_pk_add_f32 v[36:37], v[36:37], v[46:47] op_sel_hi:[1,0] neg_lo:[0,1] neg_hi:[0,1]\n" \
  "v_pk_add_f32 v[38:39], v[38:39], v[46:47] op_sel_hi:[1,0] neg_lo:[0,1] neg_hi:[0,1]\n" \
  "v_pk_add_f32 v[40:41], v[40:41], v[46:47] op_sel_hi:[1,0] neg_lo:[0,1] neg_hi:[0,1]\n" \
  "v_pk_add_f32 v[42:43], v[42:43], v[46:47] op_sel_hi:[1,0] neg_lo:[0,1] neg_hi:[0,1]\n" \
  "v_pk_add_f32 v[46:47], v[14:15], v[18:19]\n" \
  "v_pk_mul_f32 v[48:49], v[14:15], v[14:15]\n" \
  "v_pk_fma_f32 v[48:49], v[18:19], v[18:19], v[48:49]\n" \
  "v_pk_add_f32 v[46:47], v[46:47], v[20:21]\n" \
  "v_pk_fma_f32 v[48:49], v[20:21], v[20:21], v[48:49]\n" \
  "v_pk_add_f32 v[46:47], v[46:47], v[32:33]\n" \
  "v_pk_fma_f32 v[48:49], v[32:33], v[32:33], v[48:49]\n" \
  "v_pk_add_f32 v[46:47], v[46:47], v[34:35]\n" \
  "v_pk_fma_f32 v[48:49], v[34:35], v[34:35], v[48:49]\n" \
  "v_pk_add_f32 v[46:47], v[46:47], v[36:37]\n" \
  "v_pk_fma_f32 v[48:49], v[36:37], v[36:37], v[48:49]\n" \
  "v_pk_add_f32 v[46:47], v[46:47], v[38:39]\n" \
  "v_pk_fma_f32 v[48:49], v[38:39], v[38:39], v[48:49]\n" \
  "v_pk_add_f32 v[46:47], v[46:47], v[40:41]\n" \
  "v_pk_fma_f32 v[48:49], v[40:41], v[40:41], v[48:49]\n" \
  "v_pk_add_f32 v[46:47], v[46:47], v[42:43]\n" \
  "v_pk_fma_f32 v[48:49], v[42:43], v[42:43], v[48:49]\n" \
  "v_add_f32 v240, v46, v47\n" \
  "v_add_f32 v238, v48, v49\n" \
  "s_nop 1\n" \
  "v_permlane16_swap_b32 v240, v238\n" \
  "v_add_f32 v240, v240, v238\n" \
  "s_nop 1\n" \
  "v_add_f32_dpp v240, v240, v240 quad_perm:[1,0,3,2] row_mask:0xf bank_mask:0xf\n" \
  "s_nop 1\n" \
  "v_add_f32_dpp v240, v240, v240 quad_perm:[2,3,0,1] row_mask:0xf bank_mask:0xf\n" \
  "s_nop 1\n" \
  "v_add_f32_dpp v240, v240, v240 row_half_mirror row_mask:0xf bank_mask:0xf\n" \
  "s_nop 1\n" \
  "v_add_f32_dpp v240, v240, v240 row_mirror row_mask:0xf bank_mask:0xf\n" \
  "v_mov_b32 v238, v240\n" \
  "s_nop 1\n" \
  "v_permlane16_swap_b32 v240, v238\n" \
  "v_mul_f32 v240, s35, v240\n" \
  "v_fma_f32 v238, v238, s35, v45\n" \
  "v_fma_f32 v238, -v240, v240, v238\n" \
  "v_max_f32 v238, v238, v45\n" \
  "v_rsq_f32 v44, v238\n" \
  "s_nop 0\n" \
  "v_mul_f32_e64 v45, -v240, v44\n" \
  "v_pk_fma_f32 v[46:47], v[14:15], v[44:45], v[44:45] op_sel:[0,0,1] op_sel_hi:[1,0,1]\n" \
  "v_pk_fma_f32 v[48:49], v[18:19], v[44:45], v[44:45] op_sel:[0,0,1] op_sel_hi:[1,0,1]\n" \
  "ds_write_b128 v239, v[46:49] offset:0\n" \
  "v_pk_fma_f32 v[46:47], v[20:21], v[44:45], v[44:45] op_sel:[0,0,1] op_sel_hi:[1,0,1]\n" \
  "v_pk_fma_f32 v[48:49], v[32:33], v[44:45], v[44:45] op_sel:[0,0,1] op_sel_hi:[1,0,1]\n" \
  "ds_write_b128 v239, v[46:49] offset:16\n" \
  "v_pk_fma_f32 v[18:19], v[34:35], v[44:45], v[44:45] op_sel:[0,0,1] op_sel_hi:[1,0,1]\n" \
  "v_pk_fma_f32 v[20:21], v[36:37], v[44:45], v[44:45] op_sel:[0,0,1] op_sel_hi:[1,0,1]\n" \
  "ds_write_b128 v239, v[18:21] offset:1088\n" \
  "v_pk_fma_f32 v[18:19], v[38:39], v[44:45], v[44:45] op_sel:[0,0,1] op_sel_hi:[1,0,1]\n" \
  "v_pk_fma_f32 v[20:21], v[40:41], v[44:45], v[44:45] op_sel:[0,0,1] op_sel_hi:[1,0,1]\n" \
  "ds_write_b128 v239, v[18:21] offset:1104\n" \
  "v_pk_fma_f32 v[42:43], v[42:43], v[44:45], v[44:45] op_sel:[0,0,1] op_sel_hi:[1,0,1]\n" \
  "ds_write_b64 v241, v[42:43] offset:2176\n" \
  "s_waitcnt vmcnt(10)\n" \
  "v_readfirstlane_b32 s18, v30\n" \
  "v_readfirstlane_b32 s19, v31\n" \
  "s_add_u32 s18, s18, 1\n" \
  "s_addc_u32 s19, s19, 0\n" \
  "s_waitcnt lgkmcnt(0)\n" \
  "s_barrier\n" \
  "v_and_b32 v241, 65535, v26\n" \
  "ds_read_u16 v18, v241 offset:19584\n" \
  "ds_read_u16 v19, v241 offset:20162\n" \
  "ds_read_u16 v20, v241 offset:20740\n" \
  "ds_read_u16 v21, v241 offset:21318\n" \
  "ds_read_u16 v30, v241 offset:25728\n" \
  "ds_read_u16 v31, v241 offset:26306\n" \
  "ds_read_u16 v32, v241 offset:26884\n" \
  "ds_read_u16 v33, v241 offset:27462\n" \
  "s_add_u32 s32, s82, 2392064\n" \
  "s_cmp_eq_u32 s20, 1\n" \
  "s_cselect_b64 vcc, 255, s[58:59]\n" \
  "v_mov_b32 v241, 0\n" \
  "v_and_b32 v240, 65535, v27\n" \
  "ds_read_u16 v34, v240 offset:19584\n" \
  "ds_read_u16 v35, v240 offset:20162\n" \
  "ds_read_u16 v36, v240 offset:20740\n" \
  "ds_read_u16 v37, v240 offset:21318\n" \
  "ds_read_u16 v38, v240 offset:25728\n" \
  "ds_read_u16 v39, v240 offset:26306\n" \
  "ds_read_u16 v40, v240 offset:26884\n" \
  "ds_read_u16 v41, v240 offset:27462\n" \
  "v_lshrrev_b32 v240, 16, v26\n" \
  "s_waitcnt lgkmcnt(11)\n" \
  "v_lshl_or_b32 v18, v30, 16, v18\n" \
  "s_waitcnt lgkmcnt(10)\n" \
  "v_lshl_or_b32 v19, v31, 16, v19\n" \
  "s_waitcnt lgkmcnt(9)\n" \
  "v_lshl_or_b32 v20, v32, 16, v20\n" \
  "s_waitcnt lgkmcnt(8)\n" \
  "v_lshl_or_b32 v21, v33, 16, v21\n" \
  "buffer_store_dwordx4 v[18:21], v240, s[36:39], s32 offen offset:0 sc1\n" \
  "v_and_b32 v30, 2147450879, v18\n" \
  "v_pk_max_u16 v241, v241, v30\n" \
  "v_and_b32 v31, 2147450879, v19\n" \
  "v_pk_max_u16 v241, v241, v31\n" \
  "v_and_b32 v32, 2147450879, v20\n" \
  "v_pk_max_u16 v241, v241, v32\n" \
  "v_and_b32 v33, 2147450879, v21\n" \
  "v_pk_max_u16 v241, v241, v33\n" \
  "v_and_b32 v240, 65535, v28\n" \
  "ds_read_u16 v18, v240 offset:19584\n" \
  "ds_read_u16 v19, v240 offset:20162\n" \
  "ds_read_u16 v20, v240 offset:20740\n" \
  "ds_read_u16 v21, v240 offset:21318\n" \
  "ds_read_u16 v30, v240 offset:25728\n" \
  "ds_read_u16 v31, v240 offset:26306\n" \
  "ds_read_u16 v32, v240 offset:26884\n" \
  "ds_read_u16 v33, v240 offset:27462\n" \
  "v_lshrrev_b32 v240, 16, v27\n" \
  "s_waitcnt lgkmcnt(11)\n" \
  "v_lshl_or_b32 v34, v38, 16, v34\n" \
  "s_waitcnt lgkmcnt(10)\n" \
  "v_lshl_or_b32 v35, v39, 16, v35\n" \
  "s_waitcnt lgkmcnt(9)\n" \
  "v_lshl_or_b32 v36, v40, 16, v36\n" \
  "s_waitcnt lgkmcnt(8)\n" \
  "v_lshl_or_b32 v37, v41, 16, v37\n" \
  "buffer_store_dwordx4 v[34:37], v240, s[36:39], s32 offen offset:0 sc1\n" \
  "v_and_b32 v38, 2147450879, v34\n" \
  "v_pk_max_u16 v241, v241, v38\n" \
  "v_and_b32 v39, 2147450879, v35\n" \
  "v_pk_max_u16 v241, v241, v39\n" \
  "v_and_b32 v40, 2147450879, v36\n" \
  "v_pk_max_u16 v241, v241, v40\n" \
  "v_and_b32 v41, 2147450879, v37\n" \
  "v_pk_max_u16 v241, v241, v41\n" \
  "s_mov_b64 s[96:97], vcc\n" \
  "v_lshrrev_b32 v240, 16, v28\n" \
  "s_waitcnt lgkmcnt(3)\n" \
  "v_lshl_or_b32 v18, v30, 16, v18\n" \
  "s_waitcnt lgkmcnt(2)\n" \
  "v_lshl_or_b32 v19, v31, 16, v19\n" \
  "s_waitcnt lgkmcnt(1)\n" \
  "v_lshl_or_b32 v20, v32, 16, v20\n" \
  "s_waitcnt lgkmcnt(0)\n" \
  "v_lshl_or_b32 v21, v33, 16, v21\n" \
  "s_mov_b64 exec, s[96:97]\n" \
  "buffer_store_dwordx4 v[18:21], v240, s[36:39], s32 offen offset:0 sc1\n" \
  "s_mov_b64 exec, -1\n" \
  "v_and_b32 v30, 2147450879, v18\n" \
  "v_pk_max_u16 v241, v241, v30\n" \
  "v_and_b32 v31, 2147450879, v19\n" \
  "v_pk_max_u16 v241, v241, v31\n" \
  "v_and_b32 v32, 2147450879, v20\n" \
  "v_pk_max_u16 v241, v241, v32\n" \
  "v_and_b32 v33, 2147450879, v21\n" \
  "v_pk_max_u16 v241, v241, v33\n" \
  "v_lshlrev_b32 v255, 16, v241\n" \
  "v_max_u32 v241, v241, v255\n" \
  "v_mov_b32 v255, 161664\n" \
  "s_nop 0\n" \
  "v_max_u32_dpp v241, v241, v241 quad_perm:[1,0,3,2] row_mask:0xf bank_mask:0xf\n" \
  "s_nop 1\n" \
  "v_max_u32_dpp v241, v241, v241 quad_perm:[2,3,0,1] row_mask:0xf bank_mask:0xf\n" \
  "s_nop 1\n" \
  "v_max_u32_dpp v241, v241, v241 row_half_mirror row_mask:0xf bank_mask:0xf\n" \
  "s_nop 1\n" \
  "v_max_u32_dpp v241, v241, v241 row_mirror row_mask:0xf bank_mask:0xf\n" \
  "s_mov_b32 exec_lo, 65537\n" \
  "s_mov_b32 exec_hi, 65537\n" \
  "ds_max_u32 v255, v241 offset:4\n" \
  "s_mov_b64 exec, -1\n" \
  "s_mul_i32 s33, s20, 4896\n" \
  "v_and_b32 v241, 65535, v10\n" \
  "v_add_u32 v240, s33, v241\n" \
  "v_and_b32 v239, 65535, v11\n" \
  "v_add_u32 v238, s33, v239\n" \
  "v_and_b32 v237, 65535, v12\n" \
  "v_add_u32 v236, s33, v237\n" \
  "v_and_b32 v235, 65535, v13\n" \
  "v_add_u32 v234, s33, v235\n" \
  "v_lshrrev_b32 v233, 16, v10\n" \
  "v_add_u32 v232, s33, v233\n" \
  "v_lshrrev_b32 v231, 16, v11\n" \
  "v_add_u32 v230, s33, v231\n" \
  "v_lshrrev_b32 v229, 16, v12\n" \
  "v_add_u32 v228, s33, v229\n" \
  "v_lshrrev_b32 v227, 16, v13\n" \
  "v_add_u32 v226, s33, v227\n" \
  "ds_read_b64 v[10:11], v240 offset:0\n" \
  "ds_read_b64 v[12:13], v238 offset:0\n" \
  "ds_read_b64 v[14:15], v236 offset:0\n" \
  "ds_read_b64 v[18:19], v234 offset:0\n" \
  "ds_read_b64 v[20:21], v232 offset:0\n" \
  "ds_read_b64 v[26:27], v230 offset:0\n" \
  "ds_read_b64 v[28:29], v228 offset:0\n" \
  "ds_read_b64 v[30:31], v226 offset:0\n" \
  "ds_read_b64 v[32:33], v240 offset:2448\n" \
  "ds_read_b64 v[34:35], v238 offset:2448\n" \
  "ds_read_b64 v[36:37], v236 offset:2448\n" \
  "ds_read_b64 v[38:39], v234 offset:2448\n" \
  "ds_read_b64 v[40:41], v232 offset:2448\n" \
  "ds_read_b64 v[42:43], v230 offset:2448\n" \
  "ds_read_b64 v[44:45], v228 offset:2448\n" \
  "ds_read_b64 v[46:47], v226 offset:2448\n" \
  "s_mul_i32 s98, s20, 589824\n" \
  "s_mul_i32 s100, s84, 1152\n" \
  "s_add_u32 s98, s98, s100\n" \
  "s_and_b32 s100, s85, 7\n" \
  "s_lshl_b32 s100, s100, 7\n" \
  "s_add_u32 s12, s98, s100\n" \
  "s_mul_i32 s98, s20, 30720\n" \
  "s_add_u32 s57, s98, 38272\n" \
  "s_lshl_b32 s98, s20, 10\n" \
  "s_add_u32 s8, s98, s82\n" \
  "s_lshl_b32 s98, s84, 2\n" \
  "s_add_u32 s98, s98, s20\n" \
  "s_lshl_b32 s98, s98, 16\n" \
  "s_lshl_b32 s100, s85, 11\n" \
  "s_add_u32 s98, s98, s100\n" \
  "s_add_u32 s24, s98, 5308416\n" \
  "s_add_u32 s10, s87, 7405568\n" \
  "s_lshl_b32 s98, s89, 16\n" \
  "s_lshl_b32 s100, s20, 14\n" \
  "s_add_u32 s98, s98, s100\n" \
  "s_add_u32 s14, s98, 5308416\n" \
  "s_mul_i32 s98, s88, 4505600\n" \
  "s_lshl_b32 s100, s90, 2\n" \
  "s_lshr_b32 s101, s84, 1\n" \
  "s_add_u32 s100, s100, s101\n" \
  "s_lshl_b32 s100, s100, 12\n" \
  "s_add_u32 s98, s98, s100\n" \
  "s_add_u32 s16, s98, 13090816\n" \
  "s_mul_i32 s98, s20, 4896\n" \
  "s_add_u32 s21, s98, 9216\n" \
  "s_mul_i32 s27, s88, 9232\n" \
  "s_lshl_b32 s98, s20, 2\n" \
  "s_add_u32 s28, s98, s28\n" \
  "s_lshl_b32 s98, s84, 8\n" \
  "s_add_u32 s29, s98, 2048\n" \
  "s_lshl_b32 s98, s85, 3\n" \
  "s_add_u32 s30, s98, s29\n" \
  "s_cmp_lt_u32 s20, 2\n" \
  "s_cselect_b32 s9, 8192, 0\n" \
  "s_add_u32 s9, s9, s8\n" \
  "s_mul_i32 s98, s84, 18\n" \
  "s_add_u32 s98, s98, s85\n" \
  "s_sub_u32 s81, s98, 14\n" \
  "s_cmp_lt_u32 s2, 112\n" \
  "s_cselect_b32 s81, 144, s81\n" \
  "s_cmp_eq_u32 s81, 128\n" \
  "s_cselect_b32 s32, 2, 0\n" \
  "s_or_b32 s27, s27, s32\n" \
  "s_lshl_b32 s98, s81, 6\n" \
  "s_add_u32 s22, s98, 524288\n" \
  "s_lshl_b32 s98, s81, 4\n" \
  "s_add_u32 s32, s98, 24576\n" \
  "s_load_dwordx4 s[60:63], s[76:77], s32\n" \
  "v_lshrrev_b32 v240, 5, v0\n" \
  "v_lshlrev_b32 v238, 4, v240\n" \
  "v_lshl_add_u32 v240, v0, 3, v238\n" \
  "v_add_u32 v238, 35216, v240\n" \
  "v_add_u32 v236, 32496, v240\n" \
  "v_add_u32 v234, 37392, v240\n" \
  "v_add_u32 v232, 34672, v240\n" \
  "v_and_b32 v240, 48, v191\n" \
  "v_lshl_add_u32 v230, v248, 6, v240\n" \
  "v_cmp_gt_u32 vcc, 32, v0\n" \
  "v_mov_b32 v48, 1.0\n" \
  "s_nop 0\n" \
  "v_cndmask_b32 v236, v236, v234, vcc\n" \
  "v_cndmask_b32 v48, v48, 0, vcc\n" \
  "v_cndmask_b32 v240, v240, v230, s[58:59]\n" \
  "v_mov_b32 v53, 0\n" \
  "v_mov_b32 v57, 0\n" \
  "v_mov_b32 v61, 0\n" \
  "v_mov_b32 v65, 0\n" \
  "s_sub_u32 s98, s85, 14\n" \
  "s_mul_i32 s98, s98, 160\n" \
  "v_add_u32 v248, s98, v0\n" \
  "v_mul_u32_u24 v234, 52429, v248\n" \
  "v_lshrrev_b32 v228, 18, v234\n" \
  "v_mul_u32_u24 v234, 6272, v228\n" \
  "v_lshl_add_u32 v228, v248, 7, v234\n" \
  "s_mul_i32 s98, s84, 576\n" \
  "s_add_u32 s98, s98, 2304\n" \
  "s_and_b32 s25, s98, 16777088\n" \
  "v_lshrrev_b32 v248, 2, v0\n" \
  "v_mul_u32_u24 v234, 9344, v248\n" \
  "v_and_b32 v248, 3, v0\n" \
  "v_lshl_add_u32 v226, v248, 4, v234\n" \
  "v_lshrrev_b32 v248, 4, v0\n" \
  "v_xor_b32 v225, v0, v248\n" \
  "v_and_b32 v248, 3, v225\n" \
  "v_lshl_add_u32 v225, v248, 4, v234\n" \
  "s_movk_i32 s32, 8192\n" \
  "buffer_load_dwordx4 v[66:69], v246, s[36:39], s32 offen offset:0 sc1\n" \
  "s_movk_i32 s32, 17408\n" \
  "buffer_load_dwordx4 v[70:73], v246, s[36:39], s32 offen offset:0 sc1\n" \
  "s_movk_i32 s32, 26624\n" \
  "buffer_load_dwordx4 v[74:77], v246, s[36:39], s32 offen offset:0 sc1\n" \
  "s_movk_i32 s32, 12288\n" \
  "buffer_load_dwordx4 v[78:81], v246, s[36:39], s32 offen offset:0 sc1\n" \
  "s_movk_i32 s32, 21504\n" \
  "buffer_load_dwordx4 v[82:85], v246, s[36:39], s32 offen offset:0 sc1\n" \
  "s_movk_i32 s32, 30720\n" \
  "buffer_load_dwordx4 v[86:89], v246, s[36:39], s32 offen offset:0 sc1\n" \
  "s_movk_i32 s32, 16384\n" \
  "buffer_load_dwordx4 v[90:93], v245, s[36:39], s32 offen offset:0 sc1\n" \
  "s_movk_i32 s32, 25600\n" \
  "buffer_load_dwordx4 v[94:97], v245, s[36:39], s32 offen offset:0 sc1\n" \
  "s_mov_b32 s32, 34816\n" \
  "buffer_load_dwordx4 v[98:101], v245, s[36:39], s32 offen offset:0 sc1\n" \
  "s_lshl_b32 s98, s2, 3\n" \
  "s_add_u32 s56, s98, 4096\n" \
  "s_lshl_b32 s98, s20, 10\n" \
  "s_add_u32 s33, s98, 62848\n" \
  "v_mov_b32 v102, s18\n" \
  "s_and_b32 s32, s19, 16777215\n" \
  "s_waitcnt vmcnt(9) lgkmcnt(0)\n" \
  "s_barrier\n" \
  "ds_read_b32 v103, v255 offset:4\n" \
  "s_cmp_eq_u32 s20, 3\n" \
  "s_waitcnt lgkmcnt(0)\n" \
  "v_lshrrev_b32 v103, 23, v103\n" \
  "v_lshl_or_b32 v103, v103, 24, s32\n" \
  "s_cselect_b64 exec, 1, 0\n" \
  "buffer_store_dwordx2 v[102:103], off, s[36:39], s56 sc1\n" \
  "s_mov_b64 exec, -1\n" \
  "s_add_u32 m0, s33, 0\n" \
  "s_movk_i32 s32, 0\n" \
  "buffer_load_dwordx4 v246, s[76:79], s32 offen offset:0 lds\n" \
  "s_add_u32 m0, s33, 30720\n" \
  "s_movk_i32 s32, 4096\n" \
  "buffer_load_dwordx4 v246, s[76:79], s32 offen offset:0 lds\n" \
  "s_add_u32 m0, s33, 61440\n" \
  "s_movk_i32 s32, 8192\n" \
  "buffer_load_dwordx4 v246, s[76:79], s32 offen offset:0 lds\n" \
  "s_movk_i32 s32, 14336\n" \
  "buffer_load_dwordx4 v[102:105], v246, s[76:79], s32 offen offset:0\n" \
  "s_movk_i32 s32, 23552\n" \
  "buffer_load_dwordx4 v[106:109], v252, s[76:79], s32 offen offset:0\n" \
  "s_movk_i32 s32, 18432\n" \
  "buffer_load_dwordx4 v[110:113], v246, s[76:79], s32 offen offset:0\n" \
  "s_waitcnt vmcnt(7)\n" \
  ".Lacc_%=:\n" \
  "v_xor_b32 v248, s18, v66\n" \
  "v_bitop3_b32 v248, v67, s19, v248 bitop3:0xbe\n" \
  "v_bitop3_b32 v248, v70, s18, v248 bitop3:0xbe\n" \
  "v_bitop3_b32 v248, v71, s19, v248 bitop3:0xbe\n" \
  "v_bitop3_b32 v248, v74, s18, v248 bitop3:0xbe\n" \
  "v_bitop3_b32 v248, v75, s19, v248 bitop3:0xbe\n" \
  "v_bitop3_b32 v248, v78, s18, v248 bitop3:0xbe\n" \
  "v_bitop3_b32 v248, v79, s19, v248 bitop3:0xbe\n" \
  "v_bitop3_b32 v248, v82, s18, v248 bitop3:0xbe\n" \
  "v_bitop3_b32 v248, v83, s19, v248 bitop3:0xbe\n" \
  "v_bitop3_b32 v248, v86, s18, v248 bitop3:0xbe\n" \
  "v_bitop3_b32 v248, v87, s19, v248 bitop3:0xbe\n" \
  "v_xor_b32 v234, s18, v90\n" \
  "v_bitop3_b32 v234, v91, s19, v234 bitop3:0xbe\n" \
  "v_bitop3_b32 v234, v94, s18, v234 bitop3:0xbe\n" \
  "v_bitop3_b32 v234, v95, s19, v234 bitop3:0xbe\n" \
  "v_bitop3_b32 v234, v98, s18, v234 bitop3:0xbe\n" \
  "v_bitop3_b32 v234, v99, s19, v234 bitop3:0xbe\n" \
  "v_bitop3_b32 v248, v234, s58, v248 bitop3:0xea\n" \
  "v_cmp_ne_u32 vcc, 0, v248\n" \
  "s_cbranch_vccnz .Lacs_%=\n" \
  ".Lacd_%=:\n" \
  "s_mov_b32 m0, 162176\n" \
  "s_cmp_lt_u32 s20, 2\n" \
  "s_cselect_b64 exec, -1, 0\n" \
  "s_cmp_eq_u32 s20, 2\n" \
  "s_cselect_b32 exec_lo, -1, exec_lo\n" \
  "s_cmp_lt_u32 s2, 112\n" \
  "s_cselect_b64 exec, 0, exec\n" \
  "buffer_load_dword v228, s[64:67], s25 offen lds\n" \
  "s_mov_b64 exec, -1\n" \
  "v_mov_b32 v248, 0\n" \
  "v_pk_add_f32 v[114:115], v[68:69], v[72:73]\n" \
  "v_pk_add_f32 v[114:115], v[114:115], v[76:77]\n" \
  "v_cvt_pk_bf16_f32 v245, v114, v115\n" \
  "v_lshlrev_b32 v114, 16, v245\n" \
  "v_and_b32 v115, 4294901760, v245\n" \
  "ds_write_b64 v238, v[114:115] offset:0\n" \
  "v_max3_f32 v248, |v114|, |v115|, v248\n" \
  "v_pk_add_f32 v[66:67], v[80:81], v[84:85]\n" \
  "v_pk_add_f32 v[66:67], v[66:67], v[88:89]\n" \
  "v_cvt_pk_bf16_f32 v245, v66, v67\n" \
  "v_lshlrev_b32 v66, 16, v245\n" \
  "v_and_b32 v67, 4294901760, v245\n" \
  "v_pk_add_f32 v[66:67], v[66:67], v[48:49] op_sel_hi:[1,0]\n" \
  "v_cvt_pk_bf16_f32 v245, v66, v67\n" \
  "v_lshlrev_b32 v66, 16, v245\n" \
  "v_and_b32 v67, 4294901760, v245\n" \
  "ds_write_b64 v236, v[66:67] offset:0\n" \
  "v_max3_f32 v248, |v66|, |v67|, v248\n" \
  "s_mov_b64 exec, s[58:59]\n" \
  "v_pk_add_f32 v[48:49], v[92:93], v[96:97]\n" \
  "v_pk_add_f32 v[48:49], v[48:49], v[100:101]\n" \
  "v_cvt_pk_bf16_f32 v245, v48, v49\n" \
  "v_lshlrev_b32 v48, 16, v245\n" \
  "v_and_b32 v49, 4294901760, v245\n" \
  "v_add_f32 v48, 1.0, v48\n" \
  "v_add_f32 v49, 1.0, v49\n" \
  "v_cvt_pk_bf16_f32 v245, v48, v49\n" \
  "v_lshlrev_b32 v48, 16, v245\n" \
  "v_and_b32 v49, 4294901760, v245\n" \
  "ds_write_b64 v232, v[48:49] offset:0\n" \
  "v_max3_f32 v248, |v48|, |v49|, v248\n" \
  "s_mov_b64 exec, -1\n" \
  "s_nop 0\n" \
  "v_max_u32_dpp v248, v248, v248 quad_perm:[1,0,3,2] row_mask:0xf bank_mask:0xf\n" \
  "s_nop 1\n" \
  "v_max_u32_dpp v248, v248, v248 quad_perm:[2,3,0,1] row_mask:0xf bank_mask:0xf\n" \
  "s_nop 1\n" \
  "v_max_u32_dpp v248, v248, v248 row_half_mirror row_mask:0xf bank_mask:0xf\n" \
  "s_nop 1\n" \
  "v_max_u32_dpp v248, v248, v248 row_mirror row_mask:0xf bank_mask:0xf\n" \
  "s_mov_b32 exec_lo, 65537\n" \
  "s_mov_b32 exec_hi, 65537\n" \
  "ds_max_u32 v255, v248\n" \
  "s_mov_b64 exec, -1\n" \
  "s_waitcnt vmcnt(1) lgkmcnt(0)\n" \
  "s_barrier\n" \
  "ds_read_b32 v48, v255 offset:0\n" \
  "ds_read_b64 v[66:67], v241 offset:32768\n" \
  "ds_read_b64 v[68:69], v241 offset:35216\n" \
  "ds_read_b64 v[70:71], v239 offset:32768\n" \
  "ds_read_b64 v[72:73], v239 offset:35216\n" \
  "ds_read_b64 v[74:75], v237 offset:32768\n" \
  "ds_read_b64 v[76:77], v237 offset:35216\n" \
  "ds_read_b64 v[78:79], v235 offset:32768\n" \
  "ds_read_b64 v[80:81], v235 offset:35216\n" \
  "ds_read_b64 v[82:83], v233 offset:32768\n" \
  "ds_read_b64 v[84:85], v233 offset:35216\n" \
  "ds_read_b64 v[86:87], v231 offset:32768\n" \
  "ds_read_b64 v[88:89], v231 offset:35216\n" \
  "ds_read_b64 v[90:91], v229 offset:32768\n" \
  "ds_read_b64 v[92:93], v229 offset:35216\n" \
  "ds_read_b64 v[94:95], v227 offset:32768\n" \
  "ds_read_b64 v[96:97], v227 offset:35216\n" \
  "v_add_u32 v255, 62848, v252\n" \
  "ds_read_b128 a[200:203], v255 offset:0\n" \
  "ds_read_b128 a[204:207], v255 offset:1024\n" \
  "ds_read_b128 a[168:171], v255 offset:2048\n" \
  "ds_read_b128 a[172:175], v255 offset:3072\n" \
  "s_waitcnt lgkmcnt(15)\n" \
  "v_lshrrev_b32 v48, 23, v48\n" \
  "v_max_u32 v48, 64, v48\n" \
  "s_nop 0\n" \
  "v_readfirstlane_b32 s26, v48\n" \
  "v_sub_u32 v48, 256, v48\n" \
  "v_lshlrev_b32 v48, 23, v48\n" \
  "v_pk_mul_f32 v[66:67], v[66:67], v[48:49] op_sel_hi:[1,0]\n" \
  "v_pk_mul_f32 v[68:69], v[68:69], v[48:49] op_sel_hi:[1,0]\n" \
  "v_pk_fma_f32 v[98:99], v[10:11], v[66:67], v[68:69]\n" \
  "v_pk_fma_f32 v[10:11], v[32:33], v[66:67], v[68:69]\n" \
  "v_pk_mul_f32 v[70:71], v[70:71], v[48:49] op_sel_hi:[1,0]\n" \
  "v_pk_mul_f32 v[72:73], v[72:73], v[48:49] op_sel_hi:[1,0]\n" \
  "v_pk_fma_f32 v[32:33], v[12:13], v[70:71], v[72:73]\n" \
  "v_pk_fma_f32 v[12:13], v[34:35], v[70:71], v[72:73]\n" \
  "v_pk_mul_f32 v[74:75], v[74:75], v[48:49] op_sel_hi:[1,0]\n" \
  "s_waitcnt lgkmcnt(14)\n" \
  "v_pk_mul_f32 v[76:77], v[76:77], v[48:49] op_sel_hi:[1,0]\n" \
  "v_pk_fma_f32 v[34:35], v[14:15], v[74:75], v[76:77]\n" \
  "v_pk_fma_f32 v[14:15], v[36:37], v[74:75], v[76:77]\n" \
  "s_waitcnt lgkmcnt(13)\n" \
  "v_pk_mul_f32 v[78:79], v[78:79], v[48:49] op_sel_hi:[1,0]\n" \
  "s_waitcnt lgkmcnt(12)\n" \
  "v_pk_mul_f32 v[80:81], v[80:81], v[48:49] op_sel_hi:[1,0]\n" \
  "v_pk_fma_f32 v[36:37], v[18:19], v[78:79], v[80:81]\n" \
  "v_pk_fma_f32 v[18:19], v[38:39], v[78:79], v[80:81]\n" \
  "s_waitcnt lgkmcnt(11)\n" \
  "v_pk_mul_f32 v[82:83], v[82:83], v[48:49] op_sel_hi:[1,0]\n" \
  "s_waitcnt lgkmcnt(10)\n" \
  "v_pk_mul_f32 v[84:85], v[84:85], v[48:49] op_sel_hi:[1,0]\n" \
  "v_pk_fma_f32 v[38:39], v[20:21], v[82:83], v[84:85]\n" \
  "v_pk_fma_f32 v[20:21], v[40:41], v[82:83], v[84:85]\n" \
  "s_waitcnt lgkmcnt(9)\n" \
  "v_pk_mul_f32 v[86:87], v[86:87], v[48:49] op_sel_hi:[1,0]\n" \
  "s_waitcnt lgkmcnt(8)\n" \
  "v_pk_mul_f32 v[88:89], v[88:89], v[48:49] op_sel_hi:[1,0]\n" \
  "v_pk_fma_f32 v[40:41], v[26:27], v[86:87], v[88:89]\n" \
  "v_pk_fma_f32 v[26:27], v[42:43], v[86:87], v[88:89]\n" \
  "s_waitcnt lgkmcnt(7)\n" \
  "v_pk_mul_f32 v[90:91], v[90:91], v[48:49] op_sel_hi:[1,0]\n" \
  "s_waitcnt lgkmcnt(6)\n" \
  "v_pk_mul_f32 v[92:93], v[92:93], v[48:49] op_sel_hi:[1,0]\n" \
  "v_pk_fma_f32 v[42:43], v[28:29], v[90:91], v[92:93]\n" \
  "v_pk_fma_f32 v[28:29], v[44:45], v[90:91], v[92:93]\n" \
  "s_waitcnt lgkmcnt(5)\n" \
  "v_pk_mul_f32 v[94:95], v[94:95], v[48:49] op_sel_hi:[1,0]\n" \
  "s_waitcnt lgkmcnt(4)\n" \
  "v_pk_mul_f32 v[96:97], v[96:97], v[48:49] op_sel_hi:[1,0]\n" \
  "v_pk_fma_f32 v[44:45], v[30:31], v[94:95], v[96:97]\n" \
  "v_pk_fma_f32 v[30:31], v[46:47], v[94:95], v[96:97]\n" \
  "v_cvt_pk_f16_f32 v46, v98, v99\n" \
  "v_cvt_pk_f16_f32 v47, v32, v33\n" \
  "v_cvt_pk_f16_f32 v48, v34, v35\n" \
  "v_cvt_pk_f16_f32 v49, v36, v37\n" \
  "v_cvt_pk_f16_f32 v32, v38, v39\n" \
  "v_cvt_pk_f16_f32 v33, v40, v41\n" \
  "s_waitcnt lgkmcnt(3)\n" \
  "v_mfma_f32_16x16x32_f16 v[36:39], v[46:49], a[200:203], 0\n" \
  "v_cvt_pk_f16_f32 v34, v42, v43\n" \
  "v_cvt_pk_f16_f32 v35, v44, v45\n" \
  "s_waitcnt lgkmcnt(2)\n" \
  "v_mfma_f32_16x16x32_f16 v[40:43], v[46:49], a[204:207], 0\n" \
  "v_cvt_pk_f16_f32 v44, v10, v11\n" \
  "v_cvt_pk_f16_f32 v45, v12, v13\n" \
  "s_waitcnt lgkmcnt(1)\n" \
  "v_mfma_f32_16x16x32_f16 v[10:13], v[32:35], a[168:171], 0\n" \
  "v_cvt_pk_f16_f32 v46, v14, v15\n" \
  "v_cvt_pk_f16_f32 v47, v18, v19\n" \
  "s_waitcnt lgkmcnt(0)\n" \
  "v_mfma_f32_16x16x32_f16 v[66:69], v[32:35], a[172:175], 0\n" \
  "v_cvt_pk_f16_f32 v32, v20, v21\n" \
  "v_cvt_pk_f16_f32 v33, v26, v27\n" \
  "v_mfma_f32_16x16x32_f16 v[18:21], v[44:47], a[200:203], 0\n" \
  "v_cvt_pk_f16_f32 v34, v28, v29\n" \
  "v_cvt_pk_f16_f32 v35, v30, v31\n" \
  "v_mfma_f32_16x16x32_f16 v[26:29], v[44:47], a[204:207], 0\n" \
  "ds_read_b128 a[208:211], v255 offset:30720\n" \
  "v_mfma_f32_16x16x32_f16 v[44:47], v[32:35], a[168:171], 0\n" \
  "ds_read_b128 a[212:215], v255 offset:31744\n" \
  "v_mfma_f32_16x16x32_f16 v[70:73], v[32:35], a[172:175], 0\n" \
  "ds_read_b128 a[220:223], v255 offset:32768\n" \
  "ds_read_b128 a[224:227], v255 offset:33792\n" \
  "ds_read_b128 v[74:77], v255 offset:61440\n" \
  "ds_read_b128 v[78:81], v255 offset:62464\n" \
  "v_pk_add_f32 v[30:31], v[36:37], v[10:11]\n" \
  "v_pk_add_f32 v[32:33], v[38:39], v[12:13]\n" \
  "v_pk_add_f32 v[82:83], v[40:41], v[66:67]\n" \
  "v_pk_add_f32 v[84:85], v[42:43], v[68:69]\n" \
  "v_pk_add_f32 v[86:87], v[36:37], v[10:11] neg_lo:[0,1] neg_hi:[0,1]\n" \
  "v_pk_add_f32 v[88:89], v[38:39], v[12:13] neg_lo:[0,1] neg_hi:[0,1]\n" \
  "v_pk_add_f32 v[10:11], v[40:41], v[66:67] neg_lo:[0,1] neg_hi:[0,1]\n" \
  "v_pk_add_f32 v[12:13], v[42:43], v[68:69] neg_lo:[0,1] neg_hi:[0,1]\n" \
  "v_pk_add_f32 v[34:35], v[18:19], v[44:45]\n" \
  "v_pk_add_f32 v[36:37], v[20:21], v[46:47]\n" \
  "v_pk_add_f32 v[38:39], v[26:27], v[70:71]\n" \
  "v_pk_add_f32 v[40:41], v[28:29], v[72:73]\n" \
  "v_pk_add_f32 v[66:67], v[18:19], v[44:45] neg_lo:[0,1] neg_hi:[0,1]\n" \
  "v_pk_add_f32 v[68:69], v[20:21], v[46:47] neg_lo:[0,1] neg_hi:[0,1]\n" \
  "v_pk_add_f32 v[18:19], v[26:27], v[70:71] neg_lo:[0,1] neg_hi:[0,1]\n" \
  "v_pk_add_f32 v[20:21], v[28:29], v[72:73] neg_lo:[0,1] neg_hi:[0,1]\n" \
  "v_cvt_pk_f16_f32 v50, v30, v82\n" \
  "v_cvt_pk_f16_f32 v51, v31, v83\n" \
  "v_cvt_pk_f16_f32 v52, v32, v84\n" \
  "v_cvt_pk_f16_f32 v54, v86, v10\n" \
  "v_cvt_pk_f16_f32 v55, v87, v11\n" \
  "s_waitcnt lgkmcnt(5)\n" \
  "v_mfma_f32_16x16x32_f16 v[26:29], a[208:211], v[50:53], 0\n" \
  "v_cvt_pk_f16_f32 v56, v88, v12\n" \
  "s_waitcnt lgkmcnt(4)\n" \
  "v_mfma_f32_16x16x32_f16 v[10:13], a[212:215], v[50:53], 0\n" \
  "v_cvt_pk_f16_f32 v58, v34, v38\n" \
  "v_cvt_pk_f16_f32 v59, v35, v39\n" \
  "v_mfma_f32_16x16x32_f16 v[30:33], a[208:211], v[54:57], 0\n" \
  "v_cvt_pk_f16_f32 v60, v36, v40\n" \
  "v_mfma_f32_16x16x32_f16 v[34:37], a[212:215], v[54:57], 0\n" \
  "v_cvt_pk_f16_f32 v62, v66, v18\n" \
  "v_cvt_pk_f16_f32 v63, v67, v19\n" \
  "v_mfma_f32_16x16x32_f16 v[38:41], a[208:211], v[58:61], 0\n" \
  "v_cvt_pk_f16_f32 v64, v68, v20\n" \
  "v_mfma_f32_16x16x32_f16 v[18:21], a[212:215], v[58:61], 0\n" \
  "s_movk_i32 s32, 4096\n" \
  "buffer_load_dwordx4 v[42:45], v252, s[36:39], s32 offen offset:0 sc1\n" \
  "buffer_load_dwordx4 v[46:49], v252, s[36:39], s32 offen offset:1024 sc1\n" \
  "v_mfma_f32_16x16x32_f16 v[66:69], a[208:211], v[62:65], 0\n" \
  "v_mfma_f32_16x16x32_f16 v[70:73], a[212:215], v[62:65], 0\n" \
  "s_mov_b32 exec_hi, 65535\n" \
  "v_cvt_pk_f16_f32 v14, v26, v10\n" \
  "s_nop 0\n" \
  "v_cvt_pk_f16_f32 v15, v38, v18\n" \
  "ds_write_b64 v102, v[14:15] offset:0\n" \
  "v_cvt_pk_f16_f32 v14, v27, v11\n" \
  "v_cvt_pk_f16_f32 v15, v39, v19\n" \
  "ds_write_b64 v102, v[14:15] offset:512\n" \
  "v_cvt_pk_f16_f32 v14, v28, v12\n" \
  "v_cvt_pk_f16_f32 v15, v40, v20\n" \
  "ds_write_b64 v102, v[14:15] offset:1024\n" \
  "v_cvt_pk_f16_f32 v10, v30, v34\n" \
  "v_cvt_pk_f16_f32 v11, v66, v70\n" \
  "ds_write_b64 v102, v[10:11] offset:256\n" \
  "v_cvt_pk_f16_f32 v10, v31, v35\n" \
  "v_cvt_pk_f16_f32 v11, v67, v71\n" \
  "ds_write_b64 v102, v[10:11] offset:768\n" \
  "v_cvt_pk_f16_f32 v10, v32, v36\n" \
  "v_cvt_pk_f16_f32 v11, v68, v72\n" \
  "ds_write_b64 v102, v[10:11] offset:1280\n" \
  "s_mov_b64 exec, -1\n" \
  "s_waitcnt lgkmcnt(0)\n" \
  "s_barrier\n" \
  "ds_read_b128 v[10:13], v103 offset:0\n" \
  "ds_read_b128 v[18:21], v104 offset:0\n" \
  "ds_read_b128 v[26:29], v105 offset:0\n" \
  "v_lshrrev_b32 v255, 1, v191\n" \
  "v_and_b32 v248, 1, v191\n" \
  "s_lshl_b32 s98, s20, 1\n" \
  "v_add_u32 v245, s98, v248\n" \
  "v_and_b32 v248, 6, v255\n" \
  "v_xor_b32 v241, v245, v248\n" \
  "v_lshlrev_b32 v248, 4, v241\n" \
  "v_lshl_add_u32 v245, v255, 7, v248\n" \
  "v_add_u32 v255, 155008, v245\n" \
  "v_lshlrev_b32 v248, 15, v253\n" \
  "v_sub_u32 v245, v255, v248\n" \
  "s_cmp_eq_u32 s20, 1\n" \
  "s_cselect_b32 s92, 65537, s58\n" \
  "s_cselect_b32 s93, 65537, s59\n" \
  "s_add_u32 s32, s8, 4096\n" \
  "s_add_u32 s33, s8, 8192\n" \
  "s_waitcnt lgkmcnt(2)\n" \
  "v_mfma_f32_16x16x32_f16 v[30:33], a[160:163], v[10:13], 0\n" \
  "v_mfma_f32_16x16x32_f16 v[34:37], a[164:167], v[10:13], 0\n" \
  "s_waitcnt lgkmcnt(1)\n" \
  "v_mfma_f32_16x16x32_f16 v[10:13], a[160:163], v[18:21], 0\n" \
  "v_mfma_f32_16x16x32_f16 v[38:41], a[164:167], v[18:21], 0\n" \
  "s_waitcnt lgkmcnt(0)\n" \
  "v_mfma_f32_16x16x32_f16 v[18:21], a[160:163], v[26:29], 0\n" \
  "v_mfma_f32_16x16x32_f16 v[66:69], a[164:167], v[26:29], 0\n" \
  "ds_read_b128 v[26:29], v255 offset:0\n" \
  "ds_read_b128 v[70:73], v245 offset:4096\n" \
  "v_cvt_pk_f16_f32 v82, v30, v34\n" \
  "v_cvt_pk_f16_f32 v83, v31, v35\n" \
  "v_cvt_pk_f16_f32 v84, v32, v36\n" \
  "v_cvt_pk_f16_f32 v85, v33, v37\n" \
  "v_and_b32 v82, -2, v82\n" \
  "buffer_store_dwordx4 v[82:85], v230, s[36:39], s8 offen offset:0 sc1\n" \
  "v_cvt_pk_f16_f32 v30, v10, v38\n" \
  "v_cvt_pk_f16_f32 v31, v11, v39\n" \
  "v_cvt_pk_f16_f32 v32, v12, v40\n" \
  "v_cvt_pk_f16_f32 v33, v13, v41\n" \
  "v_and_b32 v30, -2, v30\n" \
  "buffer_store_dwordx4 v[30:33], v230, s[36:39], s32 offen offset:0 sc1\n" \
  "v_cvt_pk_f16_f32 v10, v18, v66\n" \
  "v_cvt_pk_f16_f32 v11, v19, v67\n" \
  "v_cvt_pk_f16_f32 v12, v20, v68\n" \
  "v_cvt_pk_f16_f32 v13, v21, v69\n" \
  "v_and_b32 v10, -2, v10\n" \
  "s_mov_b64 exec, s[92:93]\n" \
  "buffer_store_dwordx4 v[10:13], v230, s[36:39], s33 offen offset:0 sc1\n" \
  "s_mov_b64 exec, -1\n" \
  "s_waitcnt vmcnt(3)\n" \
  ".Lycc_%=:\n" \
  "v_xor_b32 v255, s18, v42\n" \
  "v_bitop3_b32 v255, v44, s18, v255 bitop3:0xbe\n" \
  "v_bitop3_b32 v255, v46, s18, v255 bitop3:0xbe\n" \
  "v_bitop3_b32 v255, v48, s18, v255 bitop3:0xbe\n" \
  "v_xor_b32 v248, s19, v43\n" \
  "v_bitop3_b32 v248, v45, s19, v248 bitop3:0xbe\n" \
  "v_bitop3_b32 v248, v47, s19, v248 bitop3:0xbe\n" \
  "v_bitop3_b32 v248, v49, s19, v248 bitop3:0xbe\n" \
  "v_lshl_or_b32 v255, v248, 8, v255\n" \
  "v_cmp_ne_u32 vcc, 0, v255\n" \
  "s_cbranch_vccnz .Lycs_%=\n" \
  ".Lycd_%=:\n" \
  "s_lshl_b32 s33, s20, 10\n" \
  "s_add_u32 m0, s33, 16384\n" \
  "s_add_u32 s32, s22, 2392064\n" \
  "buffer_load_dwordx4 v225, s[36:39], s32 offen offset:0 sc1 lds\n" \
  "s_add_u32 m0, s33, 20480\n" \
  "s_add_u32 s32, s22, 2990080\n" \
  "buffer_load_dwordx4 v225, s[36:39], s32 offen offset:0 sc1 lds\n" \
  "s_add_u32 m0, s33, 24576\n" \
  "s_add_u32 s32, s22, 3588096\n" \
  "buffer_load_dwordx4 v225, s[36:39], s32 offen offset:0 sc1 lds\n" \
  "s_add_u32 m0, s33, 28672\n" \
  "s_add_u32 s32, s22, 4186112\n" \
  "buffer_load_dwordx4 v225, s[36:39], s32 offen offset:0 sc1 lds\n" \
  "s_bitcmp1_b32 s27, 1\n" \
  "s_cbranch_scc1 .Lws_%=\n" \
  ".Lwd_%=:\n" \
  "s_waitcnt lgkmcnt(1)\n" \
  "buffer_store_dwordx4 v[26:29], v252, s[36:39], s24 offen offset:0 sc1\n" \
  "s_waitcnt lgkmcnt(0)\n" \
  "buffer_store_dwordx4 v[70:73], v252, s[36:39], s24 offen offset:1024 sc1\n" \
  "v_max3_u32 v255, v43, v45, v47\n" \
  "v_max_u32 v255, v255, v49\n" \
  "s_nop 1\n" \
  "v_max_u32_dpp v255, v255, v255 quad_perm:[1,0,3,2] row_mask:0xf bank_mask:0xf\n" \
  "s_nop 1\n" \
  "v_max_u32_dpp v255, v255, v255 quad_perm:[2,3,0,1] row_mask:0xf bank_mask:0xf\n" \
  "s_nop 1\n" \
  "v_max_u32_dpp v255, v255, v255 row_half_mirror row_mask:0xf bank_mask:0xf\n" \
  "s_nop 1\n" \
  "v_max_u32_dpp v255, v255, v255 row_mirror row_mask:0xf bank_mask:0xf\n" \
  "s_nop 0\n" \
  "v_readlane_b32 s48, v255, 0\n" \
  "v_readlane_b32 s49, v255, 16\n" \
  "v_readlane_b32 s50, v255, 32\n" \
  "v_readlane_b32 s51, v255, 48\n" \
  "s_max_u32 s48, s48, s49\n" \
  "s_max_u32 s50, s50, s51\n" \
  "s_max_u32 s48, s48, s50\n" \
  "s_lshr_b32 s48, s48, 24\n" \
  "s_max_u32 s48, s48, 64\n" \
  "s_min_u32 s48, s48, 250\n" \
  "s_add_u32 s23, s26, s48\n" \
  "s_sub_u32 s23, s23, 253\n" \
  "s_bfe_u32 s32, s27, 65537\n" \
  "s_add_u32 s32, s32, s48\n" \
  "s_sub_u32 s32, 251, s32\n" \
  "s_lshl_b32 s32, s32, 23\n" \
  "v_mov_b32 v255, s32\n" \
  "v_mul_f32 v10, s60, v255\n" \
  "v_mul_f32 v11, s61, v255\n" \
  "v_mul_f32 v12, s62, v255\n" \
  "v_lshlrev_b32 v255, 5, v191\n" \
  "v_lshlrev_b32 v248, 2, v191\n" \
  "v_and_b32 v255, 1920, v255\n" \
  "v_and_b32 v248, 96, v248\n" \
  "v_add3_u32 v255, v255, v248, s57\n" \
  "v_and_b32 v248, 3, v191\n" \
  "v_lshl_add_u32 v18, v248, 3, v255\n" \
  "v_xor_b32 v19, 32, v18\n" \
  "v_xor_b32 v20, 64, v18\n" \
  "v_xor_b32 v21, 96, v18\n" \
  "s_lshl_b32 s33, s20, 10\n" \
  "s_waitcnt vmcnt(2)\n" \
  "s_barrier\n" \
  "ds_read_b64 v[14:15], v112 offset:0\n" \
  "ds_read_b64 v[26:27], v112 offset:1024\n" \
  "ds_read_b64 v[28:29], v112 offset:2048\n" \
  "ds_read_b64 v[30:31], v112 offset:3072\n" \
  "ds_read_b64 v[32:33], v113 offset:3072\n" \
  "ds_read_b64 v[34:35], v113 offset:2048\n" \
  "ds_read_b64 v[36:37], v113 offset:1024\n" \
  "ds_read_b64 v[38:39], v113 offset:0\n" \
  "s_add_u32 m0, s33, 0\n" \
  "s_add_u32 s32, s22, 0\n" \
  "buffer_load_dwordx4 v225, s[36:39], s32 offen offset:0 sc1 lds\n" \
  "s_add_u32 m0, s33, 4096\n" \
  "s_add_u32 s32, s22, 598016\n" \
  "buffer_load_dwordx4 v225, s[36:39], s32 offen offset:0 sc1 lds\n" \
  "s_add_u32 m0, s33, 8192\n" \
  "s_add_u32 s32, s22, 1196032\n" \
  "buffer_load_dwordx4 v225, s[36:39], s32 offen offset:0 sc1 lds\n" \
  "s_add_u32 m0, s33, 12288\n" \
  "s_add_u32 s32, s22, 1794048\n" \
  "buffer_load_dwordx4 v225, s[36:39], s32 offen offset:0 sc1 lds\n" \
  "s_waitcnt lgkmcnt(7)\n" \
  "v_lshlrev_b32 v40, 16, v14\n" \
  "v_and_b32 v44, 4294901760, v14\n" \
  "s_waitcnt lgkmcnt(3)\n" \
  "v_lshlrev_b32 v66, 16, v33\n" \
  "v_and_b32 v70, 4294901760, v33\n" \
  "v_lshlrev_b32 v41, 16, v26\n" \
  "v_and_b32 v45, 4294901760, v26\n" \
  "s_waitcnt lgkmcnt(2)\n" \
  "v_lshlrev_b32 v67, 16, v35\n" \
  "v_and_b32 v71, 4294901760, v35\n" \
  "v_lshlrev_b32 v42, 16, v28\n" \
  "v_and_b32 v46, 4294901760, v28\n" \
  "s_waitcnt lgkmcnt(1)\n" \
  "v_lshlrev_b32 v68, 16, v37\n" \
  "s_add_u32 m0, s57, 0\n" \
  "s_add_u32 s31, s12, 0\n" \
  "buffer_load_dwordx4 v22, s[4:7], s31 offen offset:0 lds\n" \
  "v_and_b32 v72, 4294901760, v37\n" \
  "v_lshlrev_b32 v43, 16, v30\n" \
  "v_and_b32 v47, 4294901760, v30\n" \
  "s_waitcnt lgkmcnt(0)\n" \
  "v_lshlrev_b32 v69, 16, v39\n" \
  "v_and_b32 v73, 4294901760, v39\n" \
  "v_lshlrev_b32 v82, 16, v15\n" \
  "v_and_b32 v86, 4294901760, v15\n" \
  "v_lshlrev_b32 v90, 16, v32\n" \
  "v_and_b32 v94, 4294901760, v32\n" \
  "buffer_load_dwordx4 v23, s[4:7], s31 offen offset:1024 lds\n" \
  "v_lshlrev_b32 v83, 16, v27\n" \
  "v_and_b32 v87, 4294901760, v27\n" \
  "v_lshlrev_b32 v91, 16, v34\n" \
  "v_and_b32 v95, 4294901760, v34\n" \
  "v_lshlrev_b32 v84, 16, v29\n" \
  "v_and_b32 v88, 4294901760, v29\n" \
  "v_lshlrev_b32 v92, 16, v36\n" \
  "v_and_b32 v96, 4294901760, v36\n" \
  "v_lshlrev_b32 v85, 16, v31\n" \
  "buffer_load_dwordx4 v24, s[4:7], s31 offen offset:2048 lds\n" \
  "v_and_b32 v89, 4294901760, v31\n" \
  "v_lshlrev_b32 v93, 16, v38\n" \
  "v_and_b32 v97, 4294901760, v38\n" \
  "s_nop 1\n" \
  "s_bitcmp1_b32 s27, 1\n" \
  "s_cbranch_scc1 .Lxs_%=\n" \
  ".Lxd_%=:\n" \
  "v_pk_mul_f32 v[14:15], v[40:41], v[10:11] op_sel:[0,0] op_sel_hi:[1,0]\n" \
  "v_pk_mul_f32 v[26:27], v[44:45], v[10:11] op_sel:[0,0] op_sel_hi:[1,0]\n" \
  "v_pk_mul_f32 v[28:29], v[40:41], v[10:11] op_sel:[0,1] op_sel_hi:[1,1]\n" \
  "v_pk_mul_f32 v[32:33], v[44:45], v[10:11] op_sel:[0,1] op_sel_hi:[1,1]\n" \
  "v_pk_add_f32 v[36:37], v[44:45], v[70:71]\n" \
  "buffer_load_dwordx4 v25, s[4:7], s31 offen offset:3072 lds\n" \
  "v_pk_add_f32 v[38:39], v[40:41], v[66:67] neg_lo:[0,1] neg_hi:[0,1]\n" \
  "v_pk_fma_f32 v[40:41], v[66:67], v[10:11], v[14:15] op_sel:[0,1,0] op_sel_hi:[1,1,1]\n" \
  "v_pk_fma_f32 v[44:45], v[70:71], v[10:11], v[26:27] op_sel:[0,1,0] op_sel_hi:[1,1,1] neg_lo:[1,0,0] neg_hi:[1,0,0]\n" \
  "v_pk_fma_f32 v[28:29], v[66:67], v[10:11], v[28:29] op_sel:[0,0,0] op_sel_hi:[1,0,1]\n" \
  "v_pk_fma_f32 v[32:33], v[70:71], v[10:11], v[32:33] op_sel:[0,0,0] op_sel_hi:[1,0,1] neg_lo:[1,0,0] neg_hi:[1,0,0]\n" \
  "v_pk_mul_f32 v[66:67], v[36:37], v[12:13] op_sel:[0,0] op_sel_hi:[1,0] neg_lo:[1,0] neg_hi:[1,0]\n" \
  "v_pk_mul_f32 v[70:71], v[38:39], v[12:13] op_sel:[0,0] op_sel_hi:[1,0]\n" \
  "v_pk_mul_f32 v[14:15], v[42:43], v[10:11] op_sel:[0,0] op_sel_hi:[1,0]\n" \
  "v_pk_mul_f32 v[26:27], v[46:47], v[10:11] op_sel:[0,0] op_sel_hi:[1,0]\n" \
  "s_add_u32 m0, s57, 4096\n" \
  "s_add_u32 s31, s12, 9437184\n" \
  "buffer_load_dwordx4 v22, s[4:7], s31 offen offset:0 lds\n" \
  "v_pk_mul_f32 v[30:31], v[42:43], v[10:11] op_sel:[0,1] op_sel_hi:[1,1]\n" \
  "v_pk_mul_f32 v[34:35], v[46:47], v[10:11] op_sel:[0,1] op_sel_hi:[1,1]\n" \
  "v_pk_add_f32 v[36:37], v[46:47], v[72:73]\n" \
  "v_pk_add_f32 v[38:39], v[42:43], v[68:69] neg_lo:[0,1] neg_hi:[0,1]\n" \
  "v_pk_fma_f32 v[42:43], v[68:69], v[10:11], v[14:15] op_sel:[0,1,0] op_sel_hi:[1,1,1]\n" \
  "v_pk_fma_f32 v[46:47], v[72:73], v[10:11], v[26:27] op_sel:[0,1,0] op_sel_hi:[1,1,1] neg_lo:[1,0,0] neg_hi:[1,0,0]\n" \
  "v_pk_fma_f32 v[30:31], v[68:69], v[10:11], v[30:31] op_sel:[0,0,0] op_sel_hi:[1,0,1]\n" \
  "v_pk_fma_f32 v[34:35], v[72:73], v[10:11], v[34:35] op_sel:[0,0,0] op_sel_hi:[1,0,1] neg_lo:[1,0,0] neg_hi:[1,0,0]\n" \
  "v_pk_mul_f32 v[68:69], v[36:37], v[12:13] op_sel:[0,0] op_sel_hi:[1,0] neg_lo:[1,0] neg_hi:[1,0]\n" \
  "buffer_load_dwordx4 v23, s[4:7], s31 offen offset:1024 lds\n" \
  "v_pk_mul_f32 v[72:73], v[38:39], v[12:13] op_sel:[0,0] op_sel_hi:[1,0]\n" \
  "v_pk_mul_f32 v[14:15], v[82:83], v[10:11] op_sel:[0,0] op_sel_hi:[1,0]\n" \
  "v_pk_mul_f32 v[26:27], v[86:87], v[10:11] op_sel:[0,0] op_sel_hi:[1,0]\n" \
  "v_pk_mul_f32 v[36:37], v[82:83], v[10:11] op_sel:[0,1] op_sel_hi:[1,1]\n" \
  "v_pk_mul_f32 v[98:99], v[86:87], v[10:11] op_sel:[0,1] op_sel_hi:[1,1]\n" \
  "v_pk_add_f32 v[48:49], v[86:87], v[94:95]\n" \
  "v_pk_add_f32 v[114:115], v[82:83], v[90:91] neg_lo:[0,1] neg_hi:[0,1]\n" \
  "v_pk_fma_f32 v[82:83], v[90:91], v[10:11], v[14:15] op_sel:[0,1,0] op_sel_hi:[1,1,1]\n" \
  "v_pk_fma_f32 v[86:87], v[94:95], v[10:11], v[26:27] op_sel:[0,1,0] op_sel_hi:[1,1,1] neg_lo:[1,0,0] neg_hi:[1,0,0]\n" \
  "buffer_load_dwordx4 v24, s[4:7], s31 offen offset:2048 lds\n" \
  "v_pk_fma_f32 v[36:37], v[90:91], v[10:11], v[36:37] op_sel:[0,0,0] op_sel_hi:[1,0,1]\n" \
  "v_pk_fma_f32 v[98:99], v[94:95], v[10:11], v[98:99] op_sel:[0,0,0] op_sel_hi:[1,0,1] neg_lo:[1,0,0] neg_hi:[1,0,0]\n" \
  "v_pk_mul_f32 v[90:91], v[48:49], v[12:13] op_sel:[0,0] op_sel_hi:[1,0] neg_lo:[1,0] neg_hi:[1,0]\n" \
  "v_pk_mul_f32 v[94:95], v[114:115], v[12:13] op_sel:[0,0] op_sel_hi:[1,0]\n" \
  "v_pk_mul_f32 v[14:15], v[84:85], v[10:11] op_sel:[0,0] op_sel_hi:[1,0]\n" \
  "v_pk_mul_f32 v[26:27], v[88:89], v[10:11] op_sel:[0,0] op_sel_hi:[1,0]\n" \
  "v_pk_mul_f32 v[38:39], v[84:85], v[10:11] op_sel:[0,1] op_sel_hi:[1,1]\n" \
  "v_pk_mul_f32 v[100:101], v[88:89], v[10:11] op_sel:[0,1] op_sel_hi:[1,1]\n" \
  "v_pk_add_f32 v[48:49], v[88:89], v[96:97]\n" \
  "buffer_load_dwordx4 v25, s[4:7], s31 offen offset:3072 lds\n" \
  "v_pk_add_f32 v[114:115], v[84:85], v[92:93] neg_lo:[0,1] neg_hi:[0,1]\n" \
  "v_pk_fma_f32 v[84:85], v[92:93], v[10:11], v[14:15] op_sel:[0,1,0] op_sel_hi:[1,1,1]\n" \
  "v_pk_fma_f32 v[88:89], v[96:97], v[10:11], v[26:27] op_sel:[0,1,0] op_sel_hi:[1,1,1] neg_lo:[1,0,0] neg_hi:[1,0,0]\n" \
  "v_pk_fma_f32 v[38:39], v[92:93], v[10:11], v[38:39] op_sel:[0,0,0] op_sel_hi:[1,0,1]\n" \
  "v_pk_fma_f32 v[100:101], v[96:97], v[10:11], v[100:101] op_sel:[0,0,0] op_sel_hi:[1,0,1] neg_lo:[1,0,0] neg_hi:[1,0,0]\n" \
  "v_pk_mul_f32 v[92:93], v[48:49], v[12:13] op_sel:[0,0] op_sel_hi:[1,0] neg_lo:[1,0] neg_hi:[1,0]\n" \
  "v_pk_mul_f32 v[96:97], v[114:115], v[12:13] op_sel:[0,0] op_sel_hi:[1,0]\n" \
  "v_lshlrev_b32 v10, 16, v2\n" \
  "v_and_b32 v11, 4294901760, v2\n" \
  "v_lshlrev_b32 v12, 16, v3\n" \
  "v_and_b32 v13, 4294901760, v3\n" \
  "v_lshlrev_b32 v14, 16, v4\n" \
  "v_and_b32 v15, 4294901760, v4\n" \
  "v_lshlrev_b32 v26, 16, v5\n" \
  "v_and_b32 v27, 4294901760, v5\n" \
  "v_lshlrev_b32 v2, 16, v6\n" \
  "v_and_b32 v3, 4294901760, v6\n" \
  "v_lshlrev_b32 v4, 16, v7\n" \
  "v_and_b32 v5, 4294901760, v7\n" \
  "v_lshlrev_b32 v48, 16, v8\n" \
  "v_and_b32 v49, 4294901760, v8\n" \
  "v_lshlrev_b32 v114, 16, v9\n" \
  "v_and_b32 v115, 4294901760, v9\n" \
  "v_lshlrev_b32 v6, 16, v249\n" \
  "v_and_b32 v7, 4294901760, v249\n" \
  "v_and_b32 v116, 65535, v106\n" \
  "v_add_u32 v116, s21, v116\n" \
  "v_and_b32 v117, 65535, v107\n" \
  "v_add_u32 v117, s21, v117\n" \
  "v_and_b32 v118, 65535, v108\n" \
  "v_add_u32 v118, s21, v118\n" \
  "v_and_b32 v119, 65535, v109\n" \
  "v_add_u32 v119, s21, v119\n" \
  "v_lshrrev_b32 v120, 16, v106\n" \
  "v_add_u32 v120, s21, v120\n" \
  "v_lshrrev_b32 v121, 16, v107\n" \
  "v_add_u32 v121, s21, v121\n" \
  "v_lshrrev_b32 v122, 16, v108\n" \
  "v_add_u32 v122, s21, v122\n" \
  "v_lshrrev_b32 v123, 16, v109\n" \
  "v_add_u32 v123, s21, v123\n" \
  "v_mul_u32_u24 v255, 2448, v253\n" \
  "v_add_u32 v253, s21, v255\n" \
  "v_lshl_add_u32 v255, v254, 4, v253\n" \
  "v_lshl_add_u32 v249, v254, 2, v253\n" \
  "v_lshrrev_b32 v253, 3, v254\n" \
  "v_lshl_add_u32 v248, v253, 4, v255\n" \
  "v_lshrrev_b32 v253, 4, v191\n" \
  "v_lshl_add_u32 v245, v253, 2, s20\n" \
  "v_and_b32 v253, 3, v191\n" \
  "v_bfe_u32 v241, v191, 2, 2\n" \
  "v_mul_u32_u24 v239, 4896, v241\n" \
  "v_lshl_add_u32 v241, v253, 3, v239\n" \
  "v_lshl_add_u32 v253, v245, 5, v241\n" \
  "v_and_b32 v241, 15, v191\n" \
  "v_lshlrev_b32 v239, 4, v241\n" \
  "v_lshl_add_u32 v241, v245, 16, v239\n" \
  "v_lshlrev_b32 v245, 3, v254\n" \
  "s_lshr_b32 s98, s27, 8\n" \
  "s_and_b32 s98, s98, 255\n" \
  "v_subrev_u32 v239, s98, v0\n" \
  "v_bfrev_b32 v238, 1\n" \
  "v_cmp_gt_u32 s[96:97], 36, v239\n" \
  "v_cndmask_b32 v238, v238, v246, s[94:95]\n" \
  "s_cmp_eq_u32 s20, 1\n" \
  "s_cselect_b32 s92, 65537, s58\n" \
  "s_and_b32 s93, s59, 4294901758\n" \
  "v_mov_b32 v8, s18\n" \
  "v_mov_b32 v9, s19\n" \
  "s_waitcnt vmcnt(8)\n" \
  "s_barrier\n" \
  "ds_read_b128 v[106:109], v111 offset:0\n" \
  "ds_read_b128 v[124:127], v111 offset:1024\n" \
  "ds_read_b128 v[128:131], v111 offset:2048\n" \
  "ds_read_b128 v[132:135], v111 offset:3072\n" \
  "s_cmp_eq_u32 s20, 3\n" \
  "s_cselect_b64 exec, 1, 0\n" \
  "buffer_store_dwordx2 v[8:9], off, s[36:39], s30 sc1\n" \
  "s_mov_b64 exec, -1\n" \
  "s_waitcnt lgkmcnt(0)\n" \
  ".Ltc_%=:\n" \
  "v_or3_b32 v239, v106, v124, v128\n" \
  "v_bitop3_b32 v239, v239, 1, v132 bitop3:0xc8\n" \
  "v_cmp_ne_u32 vcc, 0, v239\n" \
  "s_cbranch_vccnz .Lts_%=\n" \
  ".Ltd_%=:\n" \
  "v_mov_b32 v136, v106\n" \
  "v_mov_b32 v137, v124\n" \
  "v_mov_b32 v138, v128\n" \
  "v_mov_b32 v139, v132\n" \
  "v_mov_b32 v140, v107\n" \
  "v_mov_b32 v141, v125\n" \
  "v_mfma_f32_16x16x32_f16 v[144:147], v[136:139], a[200:203], 0\n" \
  "v_mov_b32 v142, v129\n" \
  "v_mov_b32 v143, v133\n" \
  "v_mfma_f32_16x16x32_f16 v[148:151], v[136:139], a[204:207], 0\n" \
  "v_mov_b32 v136, v108\n" \
  "v_mov_b32 v137, v126\n" \
  "v_mfma_f32_16x16x32_f16 v[152:155], v[140:143], a[200:203], 0\n" \
  "v_mov_b32 v138, v130\n" \
  "v_mov_b32 v139, v134\n" \
  "v_mfma_f32_16x16x32_f16 v[156:159], v[140:143], a[204:207], 0\n" \
  "v_mov_b32 v140, v109\n" \
  "v_mov_b32 v141, v127\n" \
  "v_mfma_f32_16x16x32_f16 v[106:109], v[136:139], a[200:203], 0\n" \
  "v_mov_b32 v142, v131\n" \
  "v_mov_b32 v143, v135\n" \
  "v_mfma_f32_16x16x32_f16 v[124:127], v[136:139], a[204:207], 0\n" \
  "s_add_u32 m0, s57, 8192\n" \
  "s_add_u32 s31, s12, 294912\n" \
  "buffer_load_dwordx4 v22, s[4:7], s31 offen offset:0 lds\n" \
  "v_mfma_f32_16x16x32_f16 v[128:131], v[140:143], a[200:203], 0\n" \
  "buffer_load_dwordx4 v23, s[4:7], s31 offen offset:1024 lds\n" \
  "v_mfma_f32_16x16x32_f16 v[132:135], v[140:143], a[204:207], 0\n" \
  "buffer_load_dwordx4 v24, s[4:7], s31 offen offset:2048 lds\n" \
  "buffer_load_dwordx4 v25, s[4:7], s31 offen offset:3072 lds\n" \
  "s_add_u32 m0, s57, 12288\n" \
  "s_add_u32 s31, s12, 9732096\n" \
  "buffer_load_dwordx4 v22, s[4:7], s31 offen offset:0 lds\n" \
  "v_pk_mul_f32 v[136:137], v[144:145], v[74:75]\n" \
  "v_pk_mul_f32 v[140:141], v[144:145], v[78:79]\n" \
  "v_pk_mul_f32 v[138:139], v[146:147], v[76:77]\n" \
  "v_pk_mul_f32 v[142:143], v[146:147], v[80:81]\n" \
  "v_pk_fma_f32 v[136:137], v[148:149], v[78:79], v[136:137] neg_lo:[1,0,0] neg_hi:[1,0,0]\n" \
  "v_pk_fma_f32 v[140:141], v[148:149], v[74:75], v[140:141]\n" \
  "v_pk_fma_f32 v[138:139], v[150:151], v[80:81], v[138:139] neg_lo:[1,0,0] neg_hi:[1,0,0]\n" \
  "v_pk_fma_f32 v[142:143], v[150:151], v[76:77], v[142:143]\n" \
  "v_pk_mul_f32 v[144:145], v[152:153], v[74:75]\n" \
  "v_pk_mul_f32 v[148:149], v[152:153], v[78:79]\n" \
  "v_pk_mul_f32 v[146:147], v[154:155], v[76:77]\n" \
  "v_pk_mul_f32 v[150:151], v[154:155], v[80:81]\n" \
  "v_pk_fma_f32 v[144:145], v[156:157], v[78:79], v[144:145] neg_lo:[1,0,0] neg_hi:[1,0,0]\n" \
  "v_pk_fma_f32 v[148:149], v[156:157], v[74:75], v[148:149]\n" \
  "v_pk_fma_f32 v[146:147], v[158:159], v[80:81], v[146:147] neg_lo:[1,0,0] neg_hi:[1,0,0]\n" \
  "v_pk_fma_f32 v[150:151], v[158:159], v[76:77], v[150:151]\n" \
  "v_pk_mul_f32 v[152:153], v[106:107], v[74:75]\n" \
  "v_pk_mul_f32 v[156:157], v[106:107], v[78:79]\n" \
  "v_pk_mul_f32 v[154:155], v[108:109], v[76:77]\n" \
  "v_pk_mul_f32 v[158:159], v[108:109], v[80:81]\n" \
  "v_pk_fma_f32 v[152:153], v[124:125], v[78:79], v[152:153] neg_lo:[1,0,0] neg_hi:[1,0,0]\n" \
  "v_pk_fma_f32 v[156:157], v[124:125], v[74:75], v[156:157]\n" \
  "v_pk_fma_f32 v[154:155], v[126:127], v[80:81], v[154:155] neg_lo:[1,0,0] neg_hi:[1,0,0]\n" \
  "v_pk_fma_f32 v[158:159], v[126:127], v[76:77], v[158:159]\n" \
  "v_pk_mul_f32 v[106:107], v[128:129], v[74:75]\n" \
  "v_pk_mul_f32 v[124:125], v[128:129], v[78:79]\n" \
  "v_pk_mul_f32 v[108:109], v[130:131], v[76:77]\n" \
  "v_pk_mul_f32 v[126:127], v[130:131], v[80:81]\n" \
  "v_pk_fma_f32 v[106:107], v[132:133], v[78:79], v[106:107] neg_lo:[1,0,0] neg_hi:[1,0,0]\n" \
  "v_pk_fma_f32 v[124:125], v[132:133], v[74:75], v[124:125]\n" \
  "v_pk_fma_f32 v[108:109], v[134:135], v[80:81], v[108:109] neg_lo:[1,0,0] neg_hi:[1,0,0]\n" \
  "v_pk_fma_f32 v[126:127], v[134:135], v[76:77], v[126:127]\n" \
  "v_cvt_pk_f16_f32 v128, v136, v140\n" \
  "v_cvt_pk_f16_f32 v129, v137, v141\n" \
  "v_cvt_pk_f16_f32 v130, v138, v142\n" \
  "v_cvt_pk_f16_f32 v131, v139, v143\n" \
  "v_cvt_pk_f16_f32 v132, v144, v148\n" \
  "v_cvt_pk_f16_f32 v133, v145, v149\n" \
  "v_mfma_f32_16x16x32_f16 v[136:139], a[200:203], v[128:131], 0\n" \
  "v_cvt_pk_f16_f32 v134, v146, v150\n" \
  "v_cvt_pk_f16_f32 v135, v147, v151\n" \
  "v_mfma_f32_16x16x32_f16 v[140:143], a[204:207], v[128:131], 0\n" \
  "v_cvt_pk_f16_f32 v128, v152, v156\n" \
  "v_cvt_pk_f16_f32 v129, v153, v157\n" \
  "v_mfma_f32_16x16x32_f16 v[144:147], a[200:203], v[132:135], 0\n" \
  "v_cvt_pk_f16_f32 v130, v154, v158\n" \
  "v_cvt_pk_f16_f32 v131, v155, v159\n" \
  "v_mfma_f32_16x16x32_f16 v[148:151], a[204:207], v[132:135], 0\n" \
  "v_cvt_pk_f16_f32 v132, v106, v124\n" \
  "v_cvt_pk_f16_f32 v133, v107, v125\n" \
  "v_mfma_f32_16x16x32_f16 v[152:155], a[200:203], v[128:131], 0\n" \
  "v_cvt_pk_f16_f32 v134, v108, v126\n" \
  "v_cvt_pk_f16_f32 v135, v109, v127\n" \
  "v_mfma_f32_16x16x32_f16 v[106:109], a[204:207], v[128:131], 0\n" \
  "buffer_load_dwordx4 v23, s[4:7], s31 offen offset:1024 lds\n" \
  "v_mfma_f32_16x16x32_f16 v[124:127], a[200:203], v[132:135], 0\n" \
  "buffer_load_dwordx4 v24, s[4:7], s31 offen offset:2048 lds\n" \
  "v_mfma_f32_16x16x32_f16 v[128:131], a[204:207], v[132:135], 0\n" \
  "buffer_load_dwordx4 v25, s[4:7], s31 offen offset:3072 lds\n" \
  "s_add_u32 m0, s57, 16384\n" \
  "s_add_u32 s31, s12, 73728\n" \
  "buffer_load_dwordx4 v22, s[4:7], s31 offen offset:0 lds\n" \
  "buffer_load_dwordx4 v23, s[4:7], s31 offen offset:1024 lds\n" \
  "v_pk_mul_f32 v[132:133], v[40:41], v[140:141]\n" \
  "v_pk_mul_f32 v[156:157], v[40:41], v[136:137]\n" \
  "v_pk_mul_f32 v[160:161], v[28:29], v[148:149]\n" \
  "v_pk_mul_f32 v[164:165], v[28:29], v[144:145]\n" \
  "v_pk_fma_f32 v[132:133], v[44:45], v[136:137], v[132:133]\n" \
  "v_pk_fma_f32 v[156:157], v[44:45], v[140:141], v[156:157] neg_lo:[1,0,0] neg_hi:[1,0,0]\n" \
  "v_pk_fma_f32 v[160:161], v[32:33], v[144:145], v[160:161]\n" \
  "v_pk_fma_f32 v[164:165], v[32:33], v[148:149], v[164:165] neg_lo:[1,0,0] neg_hi:[1,0,0]\n" \
  "v_pk_fma_f32 v[132:133], v[66:67], v[148:149], v[132:133]\n" \
  "v_pk_fma_f32 v[156:157], v[66:67], v[144:145], v[156:157]\n" \
  "v_pk_fma_f32 v[160:161], v[66:67], v[140:141], v[160:161] neg_lo:[1,0,0] neg_hi:[1,0,0]\n" \
  "v_pk_fma_f32 v[164:165], v[66:67], v[136:137], v[164:165] neg_lo:[1,0,0] neg_hi:[1,0,0]\n" \
  "v_pk_fma_f32 v[132:133], v[70:71], v[144:145], v[132:133]\n" \
  "v_pk_fma_f32 v[156:157], v[70:71], v[148:149], v[156:157] neg_lo:[1,0,0] neg_hi:[1,0,0]\n" \
  "v_pk_fma_f32 v[160:161], v[70:71], v[136:137], v[160:161] neg_lo:[1,0,0] neg_hi:[1,0,0]\n" \
  "v_pk_fma_f32 v[164:165], v[70:71], v[140:141], v[164:165]\n" \
  "v_pk_mul_f32 v[134:135], v[42:43], v[142:143]\n" \
  "v_pk_mul_f32 v[158:159], v[42:43], v[138:139]\n" \
  "v_pk_mul_f32 v[162:163], v[30:31], v[150:151]\n" \
  "v_pk_mul_f32 v[166:167], v[30:31], v[146:147]\n" \
  "v_pk_fma_f32 v[134:135], v[46:47], v[138:139], v[134:135]\n" \
  "v_pk_fma_f32 v[158:159], v[46:47], v[142:143], v[158:159] neg_lo:[1,0,0] neg_hi:[1,0,0]\n" \
  "v_pk_fma_f32 v[162:163], v[34:35], v[146:147], v[162:163]\n" \
  "v_pk_fma_f32 v[166:167], v[34:35], v[150:151], v[166:167] neg_lo:[1,0,0] neg_hi:[1,0,0]\n" \
  "v_pk_fma_f32 v[134:135], v[68:69], v[150:151], v[134:135]\n" \
  "v_pk_fma_f32 v[158:159], v[68:69], v[146:147], v[158:159]\n" \
  "v_pk_fma_f32 v[162:163], v[68:69], v[142:143], v[162:163] neg_lo:[1,0,0] neg_hi:[1,0,0]\n" \
  "v_pk_fma_f32 v[166:167], v[68:69], v[138:139], v[166:167] neg_lo:[1,0,0] neg_hi:[1,0,0]\n" \
  "v_pk_fma_f32 v[134:135], v[72:73], v[146:147], v[134:135]\n" \
  "v_pk_fma_f32 v[158:159], v[72:73], v[150:151], v[158:159] neg_lo:[1,0,0] neg_hi:[1,0,0]\n" \
  "v_pk_fma_f32 v[162:163], v[72:73], v[138:139], v[162:163] neg_lo:[1,0,0] neg_hi:[1,0,0]\n" \
  "v_pk_fma_f32 v[166:167], v[72:73], v[142:143], v[166:167]\n" \
  "v_pk_mul_f32 v[28:29], v[82:83], v[106:107]\n" \
  "v_pk_mul_f32 v[32:33], v[82:83], v[152:153]\n" \
  "v_pk_mul_f32 v[40:41], v[36:37], v[128:129]\n" \
  "v_pk_mul_f32 v[44:45], v[36:37], v[124:125]\n" \
  "v_pk_fma_f32 v[28:29], v[86:87], v[152:153], v[28:29]\n" \
  "v_pk_fma_f32 v[32:33], v[86:87], v[106:107], v[32:33] neg_lo:[1,0,0] neg_hi:[1,0,0]\n" \
  "v_pk_fma_f32 v[40:41], v[98:99], v[124:125], v[40:41]\n" \
  "v_pk_fma_f32 v[44:45], v[98:99], v[128:129], v[44:45] neg_lo:[1,0,0] neg_hi:[1,0,0]\n" \
  "v_pk_fma_f32 v[28:29], v[90:91], v[128:129], v[28:29]\n" \
  "v_pk_fma_f32 v[32:33], v[90:91], v[124:125], v[32:33]\n" \
  "v_pk_fma_f32 v[40:41], v[90:91], v[106:107], v[40:41] neg_lo:[1,0,0] neg_hi:[1,0,0]\n" \
  "v_pk_fma_f32 v[44:45], v[90:91], v[152:153], v[44:45] neg_lo:[1,0,0] neg_hi:[1,0,0]\n" \
  "v_pk_fma_f32 v[28:29], v[94:95], v[124:125], v[28:29]\n" \
  "v_pk_fma_f32 v[32:33], v[94:95], v[128:129], v[32:33] neg_lo:[1,0,0] neg_hi:[1,0,0]\n" \
  "v_pk_fma_f32 v[40:41], v[94:95], v[152:153], v[40:41] neg_lo:[1,0,0] neg_hi:[1,0,0]\n" \
  "v_pk_fma_f32 v[44:45], v[94:95], v[106:107], v[44:45]\n" \
  "v_pk_mul_f32 v[30:31], v[84:85], v[108:109]\n" \
  "v_pk_mul_f32 v[34:35], v[84:85], v[154:155]\n" \
  "v_pk_mul_f32 v[42:43], v[38:39], v[130:131]\n" \
  "v_pk_mul_f32 v[46:47], v[38:39], v[126:127]\n" \
  "v_pk_fma_f32 v[30:31], v[88:89], v[154:155], v[30:31]\n" \
  "v_pk_fma_f32 v[34:35], v[88:89], v[108:109], v[34:35] neg_lo:[1,0,0] neg_hi:[1,0,0]\n" \
  "v_pk_fma_f32 v[42:43], v[100:101], v[126:127], v[42:43]\n" \
  "v_pk_fma_f32 v[46:47], v[100:101], v[130:131], v[46:47] neg_lo:[1,0,0] neg_hi:[1,0,0]\n" \
  "v_pk_fma_f32 v[30:31], v[92:93], v[130:131], v[30:31]\n" \
  "v_pk_fma_f32 v[34:35], v[92:93], v[126:127], v[34:35]\n" \
  "v_pk_fma_f32 v[42:43], v[92:93], v[108:109], v[42:43] neg_lo:[1,0,0] neg_hi:[1,0,0]\n" \
  "v_pk_fma_f32 v[46:47], v[92:93], v[154:155], v[46:47] neg_lo:[1,0,0] neg_hi:[1,0,0]\n" \
  "v_pk_fma_f32 v[30:31], v[96:97], v[126:127], v[30:31]\n" \
  "v_pk_fma_f32 v[34:35], v[96:97], v[130:131], v[34:35] neg_lo:[1,0,0] neg_hi:[1,0,0]\n" \
  "v_pk_fma_f32 v[42:43], v[96:97], v[154:155], v[42:43] neg_lo:[1,0,0] neg_hi:[1,0,0]\n" \
  "v_pk_fma_f32 v[46:47], v[96:97], v[108:109], v[46:47]\n" \
  "v_cvt_pk_f16_f32 v36, v132, v156\n" \
  "v_cvt_pk_f16_f32 v37, v133, v157\n" \
  "v_cvt_pk_f16_f32 v38, v134, v158\n" \
  "v_cvt_pk_f16_f32 v39, v135, v159\n" \
  "v_cvt_pk_f16_f32 v66, v160, v164\n" \
  "v_cvt_pk_f16_f32 v67, v161, v165\n" \
  "v_mfma_f32_16x16x32_f16 v[70:73], v[36:39], a[200:203], 0\n" \
  "v_cvt_pk_f16_f32 v68, v162, v166\n" \
  "v_cvt_pk_f16_f32 v69, v163, v167\n" \
  "v_mfma_f32_16x16x32_f16 v[82:85], v[36:39], a[204:207], 0\n" \
  "v_cvt_pk_f16_f32 v36, v28, v32\n" \
  "v_cvt_pk_f16_f32 v37, v29, v33\n" \
  "v_mfma_f32_16x16x32_f16 v[86:89], v[66:69], a[200:203], 0\n" \
  "v_cvt_pk_f16_f32 v38, v30, v34\n" \
  "v_cvt_pk_f16_f32 v39, v31, v35\n" \
  "v_mfma_f32_16x16x32_f16 v[28:31], v[66:69], a[204:207], 0\n" \
  "v_cvt_pk_f16_f32 v32, v40, v44\n" \
  "v_cvt_pk_f16_f32 v33, v41, v45\n" \
  "v_mfma_f32_16x16x32_f16 v[66:69], v[36:39], a[200:203], 0\n" \
  "v_cvt_pk_f16_f32 v34, v42, v46\n" \
  "v_cvt_pk_f16_f32 v35, v43, v47\n" \
  "v_mfma_f32_16x16x32_f16 v[40:43], v[36:39], a[204:207], 0\n" \
  "buffer_load_dwordx4 v24, s[4:7], s31 offen offset:2048 lds\n" \
  "v_mfma_f32_16x16x32_f16 v[36:39], v[32:35], a[200:203], 0\n" \
  "buffer_load_dwordx4 v25, s[4:7], s31 offen offset:3072 lds\n" \
  "v_mfma_f32_16x16x32_f16 v[44:47], v[32:35], a[204:207], 0\n" \
  "s_add_u32 m0, s57, 20480\n" \
  "s_add_u32 s31, s12, 9510912\n" \
  "buffer_load_dwordx4 v22, s[4:7], s31 offen offset:0 lds\n" \
  "buffer_load_dwordx4 v23, s[4:7], s31 offen offset:1024 lds\n" \
  "buffer_load_dwordx4 v24, s[4:7], s31 offen offset:2048 lds\n" \
  "v_pk_mul_f32 v[32:33], v[70:71], v[74:75]\n" \
  "v_pk_mul_f32 v[90:91], v[70:71], v[78:79]\n" \
  "v_pk_mul_f32 v[34:35], v[72:73], v[76:77]\n" \
  "v_pk_mul_f32 v[92:93], v[72:73], v[80:81]\n" \
  "v_pk_fma_f32 v[32:33], v[82:83], v[78:79], v[32:33] neg_lo:[1,0,0] neg_hi:[1,0,0]\n" \
  "v_pk_fma_f32 v[90:91], v[82:83], v[74:75], v[90:91]\n" \
  "v_pk_fma_f32 v[34:35], v[84:85], v[80:81], v[34:35] neg_lo:[1,0,0] neg_hi:[1,0,0]\n" \
  "v_pk_fma_f32 v[92:93], v[84:85], v[76:77], v[92:93]\n" \
  "v_pk_mul_f32 v[70:71], v[86:87], v[74:75]\n" \
  "v_pk_mul_f32 v[82:83], v[86:87], v[78:79]\n" \
  "v_pk_mul_f32 v[72:73], v[88:89], v[76:77]\n" \
  "v_pk_mul_f32 v[84:85], v[88:89], v[80:81]\n" \
  "v_pk_fma_f32 v[70:71], v[28:29], v[78:79], v[70:71] neg_lo:[1,0,0] neg_hi:[1,0,0]\n" \
  "v_pk_fma_f32 v[82:83], v[28:29], v[74:75], v[82:83]\n" \
  "v_pk_fma_f32 v[72:73], v[30:31], v[80:81], v[72:73] neg_lo:[1,0,0] neg_hi:[1,0,0]\n" \
  "v_pk_fma_f32 v[84:85], v[30:31], v[76:77], v[84:85]\n" \
  "v_pk_mul_f32 v[28:29], v[66:67], v[74:75]\n" \
  "v_pk_mul_f32 v[86:87], v[66:67], v[78:79]\n" \
  "v_pk_mul_f32 v[30:31], v[68:69], v[76:77]\n" \
  "v_pk_mul_f32 v[88:89], v[68:69], v[80:81]\n" \
  "v_pk_fma_f32 v[28:29], v[40:41], v[78:79], v[28:29] neg_lo:[1,0,0] neg_hi:[1,0,0]\n" \
  "v_pk_fma_f32 v[86:87], v[40:41], v[74:75], v[86:87]\n" \
  "v_pk_fma_f32 v[30:31], v[42:43], v[80:81], v[30:31] neg_lo:[1,0,0] neg_hi:[1,0,0]\n" \
  "v_pk_fma_f32 v[88:89], v[42:43], v[76:77], v[88:89]\n" \
  "v_pk_mul_f32 v[40:41], v[36:37], v[74:75]\n" \
  "v_pk_mul_f32 v[66:67], v[36:37], v[78:79]\n" \
  "v_pk_mul_f32 v[42:43], v[38:39], v[76:77]\n" \
  "v_pk_mul_f32 v[68:69], v[38:39], v[80:81]\n" \
  "v_pk_fma_f32 v[40:41], v[44:45], v[78:79], v[40:41] neg_lo:[1,0,0] neg_hi:[1,0,0]\n" \
  "v_pk_fma_f32 v[66:67], v[44:45], v[74:75], v[66:67]\n" \
  "v_pk_fma_f32 v[42:43], v[46:47], v[80:81], v[42:43] neg_lo:[1,0,0] neg_hi:[1,0,0]\n" \
  "v_pk_fma_f32 v[68:69], v[46:47], v[76:77], v[68:69]\n" \
  "v_cvt_pk_f16_f32 v36, v32, v90\n" \
  "v_cvt_pk_f16_f32 v37, v33, v91\n" \
  "v_cvt_pk_f16_f32 v38, v34, v92\n" \
  "v_cvt_pk_f16_f32 v39, v35, v93\n" \
  "v_cvt_pk_f16_f32 v32, v70, v82\n" \
  "v_cvt_pk_f16_f32 v33, v71, v83\n" \
  "v_mfma_f32_16x16x32_f16 v[44:47], a[200:203], v[36:39], 0\n" \
  "v_cvt_pk_f16_f32 v34, v72, v84\n" \
  "v_cvt_pk_f16_f32 v35, v73, v85\n" \
  "v_mfma_f32_16x16x32_f16 v[70:73], a[204:207], v[36:39], 0\n" \
  "v_cvt_pk_f16_f32 v36, v28, v86\n" \
  "v_cvt_pk_f16_f32 v37, v29, v87\n" \
  "v_mfma_f32_16x16x32_f16 v[74:77], a[200:203], v[32:35], 0\n" \
  "v_cvt_pk_f16_f32 v38, v30, v88\n" \
  "v_cvt_pk_f16_f32 v39, v31, v89\n" \
  "v_mfma_f32_16x16x32_f16 v[28:31], a[204:207], v[32:35], 0\n" \
  "v_cvt_pk_f16_f32 v32, v40, v66\n" \
  "v_cvt_pk_f16_f32 v33, v41, v67\n" \
  "v_mfma_f32_16x16x32_f16 v[78:81], a[200:203], v[36:39], 0\n" \
  "v_cvt_pk_f16_f32 v34, v42, v68\n" \
  "v_cvt_pk_f16_f32 v35, v43, v69\n" \
  "v_mfma_f32_16x16x32_f16 v[40:43], a[204:207], v[36:39], 0\n" \
  "buffer_load_dwordx4 v25, s[4:7], s31 offen offset:3072 lds\n" \
  "v_mfma_f32_16x16x32_f16 v[36:39], a[200:203], v[32:35], 0\n" \
  "v_mfma_f32_16x16x32_f16 v[66:69], a[204:207], v[32:35], 0\n" \
  "v_cvt_pk_f16_f32 v32, v70, v44\n" \
  "v_cvt_pk_f16_f32 v33, v28, v74\n" \
  "s_nop 2\n" \
  "v_cvt_pk_f16_f32 v34, v40, v78\n" \
  "s_nop 1\n" \
  "v_cvt_pk_f16_f32 v35, v66, v36\n" \
  "v_or_b32 v32, 1, v32\n" \
  "ds_write_b128 v111, v[32:35] offset:0\n" \
  "v_cvt_pk_f16_f32 v32, v71, v45\n" \
  "v_cvt_pk_f16_f32 v33, v29, v75\n" \
  "v_cvt_pk_f16_f32 v34, v41, v79\n" \
  "v_cvt_pk_f16_f32 v35, v67, v37\n" \
  "v_or_b32 v32, 1, v32\n" \
  "ds_write_b128 v111, v[32:35] offset:1024\n" \
  "v_cvt_pk_f16_f32 v32, v72, v46\n" \
  "v_cvt_pk_f16_f32 v33, v30, v76\n" \
  "v_cvt_pk_f16_f32 v34, v42, v80\n" \
  "v_cvt_pk_f16_f32 v35, v68, v38\n" \
  "v_or_b32 v32, 1, v32\n" \
  "ds_write_b128 v111, v[32:35] offset:2048\n" \
  "v_cvt_pk_f16_f32 v32, v73, v47\n" \
  "v_cvt_pk_f16_f32 v33, v31, v77\n" \
  "v_cvt_pk_f16_f32 v34, v43, v81\n" \
  "v_cvt_pk_f16_f32 v35, v69, v39\n" \
  "v_or_b32 v32, 1, v32\n" \
  "ds_write_b128 v111, v[32:35] offset:3072\n" \
  "s_waitcnt lgkmcnt(0)\n" \
  "s_barrier\n" \
  "ds_read_b128 v[28:31], v110 offset:0\n" \
  "ds_read_b128 v[32:35], v110 offset:4096\n" \
  "ds_read_b128 v[36:39], v110 offset:8192\n" \
  "ds_read_b128 v[40:43], v110 offset:12288\n" \
  "s_add_u32 s32, s22, 598016\n" \
  "s_waitcnt lgkmcnt(3)\n" \
  "buffer_store_dwordx4 v[28:31], v226, s[36:39], s22 offen offset:0 sc1\n" \
  "s_add_u32 s33, s22, 1196032\n" \
  "s_waitcnt lgkmcnt(2)\n" \
  "buffer_store_dwordx4 v[32:35], v226, s[36:39], s32 offen offset:0 sc1\n" \
  "s_add_u32 s32, s22, 1794048\n" \
  "s_waitcnt lgkmcnt(1)\n" \
  "buffer_store_dwordx4 v[36:39], v226, s[36:39], s33 offen offset:0 sc1\n" \
  "s_waitcnt lgkmcnt(0)\n" \
  "buffer_store_dwordx4 v[40:43], v226, s[36:39], s32 offen offset:0 sc1\n" \
  "s_mov_b32 s32, 35840\n" \
  "buffer_load_dwordx4 v[28:31], v238, s[36:39], s32 offen offset:0 sc1\n" \
  "buffer_load_dwordx4 v[32:35], v238, s[36:39], s32 offen offset:2304 sc1\n" \
  "s_mov_b32 s32, 40448\n" \
  "buffer_load_dwordx4 v[36:39], v238, s[36:39], s32 offen offset:0 sc1\n" \
  "buffer_load_dwordx4 v[40:43], v238, s[36:39], s32 offen offset:2304 sc1\n" \
  "s_add_u32 s32, s8, 4096\n" \
  "buffer_load_dwordx4 v[44:47], v230, s[36:39], s8 offen offset:0 sc1\n" \
  "buffer_load_dwordx4 v[66:69], v230, s[36:39], s32 offen offset:0 sc1\n" \
  "buffer_load_dwordx4 v[70:73], v240, s[36:39], s9 offen offset:0 sc1\n" \
  "buffer_load_dwordx2 v[8:9], v245, s[36:39], s29 offen offset:0 sc1\n" \
  "s_waitcnt vmcnt(29)\n" \
  "ds_read_b64_tr_b16 a[0:1], v18 offset:0\n" \
  "ds_read_b64_tr_b16 a[4:5], v19 offset:0\n" \
  "ds_read_b64_tr_b16 a[8:9], v20 offset:0\n" \
  "ds_read_b64_tr_b16 a[12:13], v21 offset:0\n" \
  "ds_read_b64_tr_b16 a[20:21], v18 offset:2048\n" \
  "ds_read_b64_tr_b16 a[24:25], v19 offset:2048\n" \
  "ds_read_b64_tr_b16 a[28:29], v20 offset:2048\n" \
  "ds_read_b64_tr_b16 a[32:33], v21 offset:2048\n" \
  "ds_read_b64_tr_b16 a[2:3], v18 offset:4096\n" \
  "ds_read_b64_tr_b16 a[6:7], v19 offset:4096\n" \
  "ds_read_b64_tr_b16 a[10:11], v20 offset:4096\n" \
  "ds_read_b64_tr_b16 a[14:15], v21 offset:4096\n" \
  "ds_read_b64_tr_b16 a[22:23], v18 offset:6144\n" \
  "ds_read_b64_tr_b16 a[26:27], v19 offset:6144\n" \
  "ds_read_b64_tr_b16 a[30:31], v20 offset:6144\n" \
  "ds_read_b64_tr_b16 a[34:35], v21 offset:6144\n" \
  "s_waitcnt vmcnt(20)\n" \
  "ds_read_b64_tr_b16 a[80:81], v18 offset:8192\n" \
  "ds_read_b64_tr_b16 a[84:85], v19 offset:8192\n" \
  "ds_read_b64_tr_b16 a[88:89], v20 offset:8192\n" \
  "ds_read_b64_tr_b16 a[92:93], v21 offset:8192\n" \
  "ds_read_b64_tr_b16 a[100:101], v18 offset:10240\n" \
  "ds_read_b64_tr_b16 a[104:105], v19 offset:10240\n" \
  "ds_read_b64_tr_b16 a[108:109], v20 offset:10240\n" \
  "ds_read_b64_tr_b16 a[112:113], v21 offset:10240\n" \
  "ds_read_b64_tr_b16 a[82:83], v18 offset:12288\n" \
  "ds_read_b64_tr_b16 a[86:87], v19 offset:12288\n" \
  "ds_read_b64_tr_b16 a[90:91], v20 offset:12288\n" \
  "ds_read_b64_tr_b16 a[94:95], v21 offset:12288\n" \
  "ds_read_b64_tr_b16 a[102:103], v18 offset:14336\n" \
  "ds_read_b64_tr_b16 a[106:107], v19 offset:14336\n" \
  "ds_read_b64_tr_b16 a[110:111], v20 offset:14336\n" \
  "ds_read_b64_tr_b16 a[114:115], v21 offset:14336\n" \
  "s_waitcnt vmcnt(12)\n" \
  "ds_read_b64_tr_b16 a[40:41], v18 offset:16384\n" \
  "ds_read_b64_tr_b16 a[44:45], v19 offset:16384\n" \
  "ds_read_b64_tr_b16 a[48:49], v20 offset:16384\n" \
  "ds_read_b64_tr_b16 a[52:53], v21 offset:16384\n" \
  "ds_read_b64_tr_b16 a[60:61], v18 offset:18432\n" \
  "ds_read_b64_tr_b16 a[64:65], v19 offset:18432\n" \
  "ds_read_b64_tr_b16 a[68:69], v20 offset:18432\n" \
  "ds_read_b64_tr_b16 a[72:73], v21 offset:18432\n" \
  "ds_read_b64_tr_b16 a[42:43], v18 offset:20480\n" \
  "ds_read_b64_tr_b16 a[46:47], v19 offset:20480\n" \
  "ds_read_b64_tr_b16 a[50:51], v20 offset:20480\n" \
  "ds_read_b64_tr_b16 a[54:55], v21 offset:20480\n" \
  "ds_read_b64_tr_b16 a[62:63], v18 offset:22528\n" \
  "ds_read_b64_tr_b16 a[66:67], v19 offset:22528\n" \
  "ds_read_b64_tr_b16 a[70:71], v20 offset:22528\n" \
  "ds_read_b64_tr_b16 a[74:75], v21 offset:22528\n" \
  "s_waitcnt lgkmcnt(0)\n" \
  "s_barrier\n" \
  "s_add_u32 m0, s57, 0\n" \
  "s_add_u32 s31, s12, 368640\n" \
  "buffer_load_dwordx4 v22, s[4:7], s31 offen offset:0 lds\n" \
  "buffer_load_dwordx4 v23, s[4:7], s31 offen offset:1024 lds\n" \
  "buffer_load_dwordx4 v24, s[4:7], s31 offen offset:2048 lds\n" \
  "buffer_load_dwordx4 v25, s[4:7], s31 offen offset:3072 lds\n" \
  "s_add_u32 m0, s57, 4096\n" \
  "s_add_u32 s31, s12, 9805824\n" \
  "buffer_load_dwordx4 v22, s[4:7], s31 offen offset:0 lds\n" \
  "buffer_load_dwordx4 v23, s[4:7], s31 offen offset:1024 lds\n" \
  "buffer_load_dwordx4 v24, s[4:7], s31 offen offset:2048 lds\n" \
  "buffer_load_dwordx4 v25, s[4:7], s31 offen offset:3072 lds\n" \
  "s_add_u32 m0, s57, 8192\n" \
  "s_add_u32 s31, s12, 147456\n" \
  "buffer_load_dwordx4 v22, s[4:7], s31 offen offset:0 lds\n" \
  "buffer_load_dwordx4 v23, s[4:7], s31 offen offset:1024 lds\n" \
  "buffer_load_dwordx4 v24, s[4:7], s31 offen offset:2048 lds\n" \
  "buffer_load_dwordx4 v25, s[4:7], s31 offen offset:3072 lds\n" \
  "s_waitcnt vmcnt(16)\n" \
  ".Lmcc_%=:\n" \
  "v_xor_b32 v239, s18, v28\n" \
  "v_bitop3_b32 v239, v29, s19, v239 bitop3:0xbe\n" \
  "v_bitop3_b32 v239, v32, s18, v239 bitop3:0xbe\n" \
  "v_bitop3_b32 v239, v33, s19, v239 bitop3:0xbe\n" \
  "v_bitop3_b32 v239, v36, s18, v239 bitop3:0xbe\n" \
  "v_bitop3_b32 v239, v37, s19, v239 bitop3:0xbe\n" \
  "v_bitop3_b32 v239, v40, s18, v239 bitop3:0xbe\n" \
  "v_bitop3_b32 v239, v41, s19, v239 bitop3:0xbe\n" \
  "v_cmp_ne_u32 vcc, 0, v239\n" \
  "s_and_b64 vcc, vcc, s[94:95]\n" \
  "s_cbranch_scc1 .Lmcs_%=\n" \
  ".Lmcd_%=:\n" \
  "v_lshlrev_b32 v74, 16, v30\n" \
  "v_and_b32 v75, 4294901760, v30\n" \
  "v_lshlrev_b32 v76, 16, v31\n" \
  "v_and_b32 v77, 4294901760, v31\n" \
  "v_lshlrev_b32 v28, 16, v34\n" \
  "v_and_b32 v29, 4294901760, v34\n" \
  "v_lshlrev_b32 v30, 16, v35\n" \
  "v_and_b32 v31, 4294901760, v35\n" \
  "v_lshlrev_b32 v32, 16, v38\n" \
  "v_and_b32 v33, 4294901760, v38\n" \
  "v_lshlrev_b32 v34, 16, v39\n" \
  "v_and_b32 v35, 4294901760, v39\n" \
  "v_lshlrev_b32 v36, 16, v42\n" \
  "v_and_b32 v37, 4294901760, v42\n" \
  "v_lshlrev_b32 v38, 16, v43\n" \
  "v_and_b32 v39, 4294901760, v43\n" \
  "v_mul_f32 v74, 1055100473, v74\n" \
  "v_ldexp_f32 v74, v74, s23\n" \
  "v_mul_f32 v75, 1055100473, v75\n" \
  "v_ldexp_f32 v75, v75, s23\n" \
  "v_mul_f32 v76, 1055100473, v76\n" \
  "v_ldexp_f32 v76, v76, s23\n" \
  "v_mul_f32 v77, 1055100473, v77\n" \
  "v_ldexp_f32 v77, v77, s23\n" \
  "s_mov_b64 exec, s[94:95]\n" \
  "ds_write_b128 v246, v[28:31] offset:31104\n" \
  "ds_write_b128 v246, v[32:35] offset:33408\n" \
  "ds_write_b128 v246, v[74:77] offset:28800\n" \
  "s_bfe_u32 s32, s27, 524296\n" \
  "s_lshl_b32 s32, s32, 4\n" \
  "s_sub_u32 s32, 163200, s32\n" \
  "v_add_u32 v239, s32, v246\n" \
  "s_mov_b64 exec, s[96:97]\n" \
  "ds_write_b128 v239, v[36:39] offset:0\n" \
  "s_mov_b64 exec, -1\n" \
  "s_waitcnt vmcnt(13)\n" \
  ".Lrcc_%=:\n" \
  "v_bitop3_b32 v246, v44, v66, v70 bitop3:0x80\n" \
  "v_and_b32 v246, 1, v246\n" \
  "v_cmp_eq_u32 vcc, 0, v246\n" \
  "s_cbranch_vccnz .Lrcs_%=\n" \
  ".Lrcd_%=:\n" \
  "v_mfma_f32_16x16x32_f16 v[28:31], a[228:231], v[44:47], 0\n" \
  "s_add_u32 m0, s57, 12288\n" \
  "s_add_u32 s31, s12, 9584640\n" \
  "buffer_load_dwordx4 v22, s[4:7], s31 offen offset:0 lds\n" \
  "v_mfma_f32_16x16x32_f16 v[32:35], a[232:235], v[44:47], 0\n" \
  "buffer_load_dwordx4 v23, s[4:7], s31 offen offset:1024 lds\n" \
  "v_mfma_f32_16x16x32_f16 v[36:39], a[228:231], v[66:69], 0\n" \
  "buffer_load_dwordx4 v24, s[4:7], s31 offen offset:2048 lds\n" \
  "v_mfma_f32_16x16x32_f16 v[40:43], a[232:235], v[66:69], 0\n" \
  "buffer_load_dwordx4 v25, s[4:7], s31 offen offset:3072 lds\n" \
  "v_mfma_f32_16x16x32_f16 v[44:47], a[228:231], v[70:73], 0\n" \
  "s_add_u32 m0, s57, 16384\n" \
  "s_add_u32 s31, s12, 442368\n" \
  "buffer_load_dwordx4 v22, s[4:7], s31 offen offset:0 lds\n" \
  "v_mfma_f32_16x16x32_f16 v[66:69], a[232:235], v[70:73], 0\n" \
  "buffer_load_dwordx4 v23, s[4:7], s31 offen offset:1024 lds\n" \
  "v_cvt_pk_f16_f32 v70, v32, v28\n" \
  "v_cvt_pk_f16_f32 v71, v33, v29\n" \
  "v_cvt_pk_f16_f32 v72, v34, v30\n" \
  "v_cvt_pk_f16_f32 v73, v35, v31\n" \
  "ds_write_b128 v103, v[70:73] offset:0\n" \
  "v_cvt_pk_f16_f32 v28, v40, v36\n" \
  "v_cvt_pk_f16_f32 v29, v41, v37\n" \
  "v_cvt_pk_f16_f32 v30, v42, v38\n" \
  "v_cvt_pk_f16_f32 v31, v43, v39\n" \
  "ds_write_b128 v104, v[28:31] offset:0\n" \
  "v_cvt_pk_f16_f32 v28, v66, v44\n" \
  "v_cvt_pk_f16_f32 v29, v67, v45\n" \
  "v_cvt_pk_f16_f32 v30, v68, v46\n" \
  "v_cvt_pk_f16_f32 v31, v69, v47\n" \
  "s_mov_b64 exec, s[92:93]\n" \
  "ds_write_b128 v105, v[28:31] offset:0\n" \
  "s_mov_b64 exec, -1\n" \
  "s_waitcnt lgkmcnt(0)\n" \
  "s_barrier\n" \
  "ds_read_b64 v[28:29], v102 offset:0\n" \
  "ds_read_b64 v[30:31], v102 offset:512\n" \
  "ds_read_b64 v[32:33], v102 offset:1024\n" \
  "ds_read_b64 v[34:35], v102 offset:256\n" \
  "ds_read_b64 v[36:37], v102 offset:768\n" \
  "ds_read_b64 v[38:39], v102 offset:1280\n" \
  "v_lshlrev_b32 v246, 5, v254\n" \
  "buffer_load_dwordx4 v24, s[4:7], s31 offen offset:2048 lds\n" \
  "buffer_load_dwordx4 v25, s[4:7], s31 offen offset:3072 lds\n" \
  "s_add_u32 m0, s57, 20480\n" \
  "s_add_u32 s31, s12, 9879552\n" \
  "buffer_load_dwordx4 v22, s[4:7], s31 offen offset:0 lds\n" \
  "s_waitcnt lgkmcnt(5)\n" \
  "v_mov_b32 v50, v28\n" \
  "s_waitcnt lgkmcnt(4)\n" \
  "v_mov_b32 v51, v30\n" \
  "s_waitcnt lgkmcnt(3)\n" \
  "v_mov_b32 v52, v32\n" \
  "v_mov_b32 v58, v29\n" \
  "v_mov_b32 v59, v31\n" \
  "v_mfma_f32_16x16x32_f16 v[28:31], v[50:53], a[208:211], 0\n" \
  "v_mov_b32 v60, v33\n" \
  "s_waitcnt lgkmcnt(2)\n" \
  "v_mov_b32 v54, v34\n" \
  "v_mfma_f32_16x16x32_f16 v[40:43], v[50:53], a[212:215], 0\n" \
  "s_waitcnt lgkmcnt(1)\n" \
  "v_mov_b32 v55, v36\n" \
  "s_waitcnt lgkmcnt(0)\n" \
  "v_mov_b32 v56, v38\n" \
  "v_mfma_f32_16x16x32_f16 v[44:47], v[58:61], a[208:211], 0\n" \
  "v_mov_b32 v62, v35\n" \
  "v_mov_b32 v63, v37\n" \
  "v_mfma_f32_16x16x32_f16 v[32:35], v[58:61], a[212:215], 0\n" \
  "v_mov_b32 v64, v39\n" \
  "buffer_load_dwordx4 v23, s[4:7], s31 offen offset:1024 lds\n" \
  "v_mfma_f32_16x16x32_f16 v[36:39], v[54:57], a[208:211], 0\n" \
  "ds_read_b128 v[50:53], v246 offset:31104\n" \
  "ds_read_b128 v[58:61], v246 offset:31120\n" \
  "v_mfma_f32_16x16x32_f16 v[66:69], v[54:57], a[212:215], 0\n" \
  "ds_read_b128 v[54:57], v246 offset:32128\n" \
  "ds_read_b128 v[70:73], v246 offset:32144\n" \
  "v_mfma_f32_16x16x32_f16 v[74:77], v[62:65], a[208:211], 0\n" \
  "ds_read_b64 v[78:79], v245 offset:33152\n" \
  "buffer_load_dwordx4 v24, s[4:7], s31 offen offset:2048 lds\n" \
  "v_mfma_f32_16x16x32_f16 v[80:83], v[62:65], a[212:215], 0\n" \
  "ds_read_b128 v[62:65], v246 offset:28800\n" \
  "ds_read_b128 v[84:87], v246 offset:28816\n" \
  "ds_read_b128 v[88:91], v246 offset:29824\n" \
  "ds_read_b128 v[92:95], v246 offset:29840\n" \
  "ds_read_b64 v[96:97], v245 offset:30848\n" \
  "buffer_load_dwordx4 v25, s[4:7], s31 offen offset:3072 lds\n" \
  "v_pk_add_f32 v[98:99], v[28:29], v[36:37]\n" \
  "v_pk_add_f32 v[100:101], v[30:31], v[38:39]\n" \
  "v_pk_add_f32 v[102:103], v[40:41], v[66:67]\n" \
  "v_pk_add_f32 v[104:105], v[42:43], v[68:69]\n" \
  "v_pk_add_f32 v[106:107], v[28:29], v[36:37] neg_lo:[0,1] neg_hi:[0,1]\n" \
  "v_pk_add_f32 v[108:109], v[30:31], v[38:39] neg_lo:[0,1] neg_hi:[0,1]\n" \
  "v_pk_add_f32 v[28:29], v[40:41], v[66:67] neg_lo:[0,1] neg_hi:[0,1]\n" \
  "v_pk_add_f32 v[30:31], v[42:43], v[68:69] neg_lo:[0,1] neg_hi:[0,1]\n" \
  "v_pk_add_f32 v[36:37], v[44:45], v[74:75]\n" \
  "v_pk_add_f32 v[38:39], v[46:47], v[76:77]\n" \
  "v_pk_add_f32 v[40:41], v[32:33], v[80:81]\n" \
  "v_pk_add_f32 v[42:43], v[34:35], v[82:83]\n" \
  "v_pk_add_f32 v[66:67], v[44:45], v[74:75] neg_lo:[0,1] neg_hi:[0,1]\n" \
  "v_pk_add_f32 v[68:69], v[46:47], v[76:77] neg_lo:[0,1] neg_hi:[0,1]\n" \
  "v_pk_add_f32 v[44:45], v[32:33], v[80:81] neg_lo:[0,1] neg_hi:[0,1]\n" \
  "v_pk_add_f32 v[46:47], v[34:35], v[82:83] neg_lo:[0,1] neg_hi:[0,1]\n" \
  "v_cvt_pk_f16_f32 v32, v98, v102\n" \
  "v_cvt_pk_f16_f32 v33, v99, v103\n" \
  "v_cvt_pk_f16_f32 v34, v100, v104\n" \
  "v_cvt_pk_f16_f32 v35, v101, v105\n" \
  "v_cvt_pk_f16_f32 v74, v106, v28\n" \
  "v_cvt_pk_f16_f32 v75, v107, v29\n" \
  "v_mfma_f32_16x16x32_f16 v[80:83], a[200:203], v[32:35], 0\n" \
  "v_cvt_pk_f16_f32 v76, v108, v30\n" \
  "v_cvt_pk_f16_f32 v77, v109, v31\n" \
  "v_mfma_f32_16x16x32_f16 v[28:31], a[204:207], v[32:35], 0\n" \
  "v_cvt_pk_f16_f32 v32, v36, v40\n" \
  "v_cvt_pk_f16_f32 v33, v37, v41\n" \
  "v_mfma_f32_16x16x32_f16 v[98:101], a[220:223], v[74:77], 0\n" \
  "v_cvt_pk_f16_f32 v34, v38, v42\n" \
  "v_cvt_pk_f16_f32 v35, v39, v43\n" \
  "v_mfma_f32_16x16x32_f16 v[36:39], a[224:227], v[74:77], 0\n" \
  "v_cvt_pk_f16_f32 v40, v66, v44\n" \
  "v_cvt_pk_f16_f32 v41, v67, v45\n" \
  "v_mfma_f32_16x16x32_f16 v[74:77], a[200:203], v[32:35], 0\n" \
  "v_cvt_pk_f16_f32 v42, v68, v46\n" \
  "v_cvt_pk_f16_f32 v43, v69, v47\n" \
  "v_mfma_f32_16x16x32_f16 v[44:47], a[204:207], v[32:35], 0\n" \
  "ds_read_b128 v[32:35], v246 offset:33408\n" \
  "v_mfma_f32_16x16x32_f16 v[66:69], a[220:223], v[40:43], 0\n" \
  "ds_read_b128 v[102:105], v246 offset:33424\n" \
  "v_mfma_f32_16x16x32_f16 v[106:109], a[224:227], v[40:43], 0\n" \
  "ds_read_b128 v[40:43], v246 offset:34432\n" \
  "ds_read_b128 v[110:113], v246 offset:34448\n" \
  "ds_read_b64 v[124:125], v245 offset:35456\n" \
  "s_waitcnt lgkmcnt(14)\n" \
  "v_readfirstlane_b32 s32, v50\n" \
  "s_mov_b32 exec_lo, 125241207\n" \
  "s_mov_b32 exec_hi, 125241207\n" \
  "v_cvt_pk_f16_f32 v254, v28, v80\n" \
  "ds_write_b32 v116, v254 offset:0\n" \
  "v_cvt_pk_f16_f32 v254, v29, v81\n" \
  "ds_write_b32 v117, v254 offset:0\n" \
  "v_cvt_pk_f16_f32 v254, v30, v82\n" \
  "ds_write_b32 v118, v254 offset:0\n" \
  "v_cvt_pk_f16_f32 v254, v31, v83\n" \
  "ds_write_b32 v119, v254 offset:0\n" \
  "v_cvt_pk_f16_f32 v254, v36, v98\n" \
  "ds_write_b32 v120, v254 offset:0\n" \
  "v_cvt_pk_f16_f32 v254, v37, v99\n" \
  "ds_write_b32 v121, v254 offset:0\n" \
  "v_cvt_pk_f16_f32 v254, v38, v100\n" \
  "ds_write_b32 v122, v254 offset:0\n" \
  "v_cvt_pk_f16_f32 v254, v39, v101\n" \
  "ds_write_b32 v123, v254 offset:0\n" \
  "v_cvt_pk_f16_f32 v254, v44, v74\n" \
  "ds_write_b32 v116, v254 offset:2448\n" \
  "v_cvt_pk_f16_f32 v254, v45, v75\n" \
  "ds_write_b32 v117, v254 offset:2448\n" \
  "v_cvt_pk_f16_f32 v254, v46, v76\n" \
  "ds_write_b32 v118, v254 offset:2448\n" \
  "v_cvt_pk_f16_f32 v254, v47, v77\n" \
  "ds_write_b32 v119, v254 offset:2448\n" \
  "v_cvt_pk_f16_f32 v254, v106, v66\n" \
  "ds_write_b32 v120, v254 offset:2448\n" \
  "v_cvt_pk_f16_f32 v254, v107, v67\n" \
  "ds_write_b32 v121, v254 offset:2448\n" \
  "v_cvt_pk_f16_f32 v254, v108, v68\n" \
  "ds_write_b32 v122, v254 offset:2448\n" \
  "v_cvt_pk_f16_f32 v254, v109, v69\n" \
  "ds_write_b32 v123, v254 offset:2448\n" \
  "s_mov_b64 exec, -1\n" \
  "ds_read_b128 v[28:31], v248 offset:0\n" \
  "ds_read_b128 v[36:39], v248 offset:576\n" \
  "ds_read_b32 v254, v249 offset:1152\n" \
  "v_mov_b32 v44, s32\n" \
  "v_pk_add_f32 v[50:51], v[50:51], v[44:45] op_sel_hi:[1,0] neg_lo:[0,1] neg_hi:[0,1]\n" \
  "v_pk_add_f32 v[52:53], v[52:53], v[44:45] op_sel_hi:[1,0] neg_lo:[0,1] neg_hi:[0,1]\n" \
  "s_waitcnt lgkmcnt(15)\n" \
  "v_pk_add_f32 v[58:59], v[58:59], v[44:45] op_sel_hi:[1,0] neg_lo:[0,1] neg_hi:[0,1]\n" \
  "v_pk_add_f32 v[60:61], v[60:61], v[44:45] op_sel_hi:[1,0] neg_lo:[0,1] neg_hi:[0,1]\n" \
  "v_pk_add_f32 v[54:55], v[54:55], v[44:45] op_sel_hi:[1,0] neg_lo:[0,1] neg_hi:[0,1]\n" \
  "v_pk_add_f32 v[56:57], v[56:57], v[44:45] op_sel_hi:[1,0] neg_lo:[0,1] neg_hi:[0,1]\n" \
  "v_pk_add_f32 v[70:71], v[70:71], v[44:45] op_sel_hi:[1,0] neg_lo:[0,1] neg_hi:[0,1]\n" \
  "v_pk_add_f32 v[72:73], v[72:73], v[44:45] op_sel_hi:[1,0] neg_lo:[0,1] neg_hi:[0,1]\n" \
  "v_pk_add_f32 v[78:79], v[78:79], v[44:45] op_sel_hi:[1,0] neg_lo:[0,1] neg_hi:[0,1]\n" \
  "s_waitcnt lgkmcnt(2)\n" \
  "v_fma_mix_f32 v10, v28, v62, v10 op_sel:[0,0,0] op_sel_hi:[1,0,0]\n" \
  "v_fma_mix_f32 v11, v28, v63, v11 op_sel:[1,0,0] op_sel_hi:[1,0,0]\n" \
  "v_fma_mix_f32 v12, v29, v64, v12 op_sel:[0,0,0] op_sel_hi:[1,0,0]\n" \
  "v_fma_mix_f32 v13, v29, v65, v13 op_sel:[1,0,0] op_sel_hi:[1,0,0]\n" \
  "v_fma_mix_f32 v14, v30, v84, v14 op_sel:[0,0,0] op_sel_hi:[1,0,0]\n" \
  "v_fma_mix_f32 v15, v30, v85, v15 op_sel:[1,0,0] op_sel_hi:[1,0,0]\n" \
  "v_fma_mix_f32 v26, v31, v86, v26 op_sel:[0,0,0] op_sel_hi:[1,0,0]\n" \
  "v_fma_mix_f32 v27, v31, v87, v27 op_sel:[1,0,0] op_sel_hi:[1,0,0]\n" \
  "s_waitcnt lgkmcnt(1)\n" \
  "v_fma_mix_f32 v2, v36, v88, v2 op_sel:[0,0,0] op_sel_hi:[1,0,0]\n" \
  "v_fma_mix_f32 v3, v36, v89, v3 op_sel:[1,0,0] op_sel_hi:[1,0,0]\n" \
  "v_fma_mix_f32 v4, v37, v90, v4 op_sel:[0,0,0] op_sel_hi:[1,0,0]\n" \
  "v_fma_mix_f32 v5, v37, v91, v5 op_sel:[1,0,0] op_sel_hi:[1,0,0]\n" \
  "v_fma_mix_f32 v48, v38, v92, v48 op_sel:[0,0,0] op_sel_hi:[1,0,0]\n" \
  "v_fma_mix_f32 v49, v38, v93, v49 op_sel:[1,0,0] op_sel_hi:[1,0,0]\n" \
  "v_fma_mix_f32 v114, v39, v94, v114 op_sel:[0,0,0] op_sel_hi:[1,0,0]\n" \
  "v_fma_mix_f32 v115, v39, v95, v115 op_sel:[1,0,0] op_sel_hi:[1,0,0]\n" \
  "s_waitcnt lgkmcnt(0)\n" \
  "v_fma_mix_f32 v6, v254, v96, v6 op_sel:[0,0,0] op_sel_hi:[1,0,0]\n" \
  "v_fma_mix_f32 v7, v254, v97, v7 op_sel:[1,0,0] op_sel_hi:[1,0,0]\n" \
  "v_mov_b32 v29, 925353388\n" \
  "v_mov_b32_dpp v30, v10 row_newbcast:0 row_mask:0xf bank_mask:0xf\n" \
  "v_mov_b32_dpp v254, v10 row_newbcast:0 row_mask:0xf bank_mask:0xf\n" \
  "v_cvt_pk_bf16_f32 v36, v10, v11\n" \
  "v_cvt_pk_bf16_f32 v37, v12, v13\n" \
  "v_cvt_pk_bf16_f32 v38, v14, v15\n" \
  "v_cvt_pk_bf16_f32 v39, v26, v27\n" \
  "v_cvt_pk_bf16_f32 v44, v2, v3\n" \
  "v_cvt_pk_bf16_f32 v45, v4, v5\n" \
  "v_cvt_pk_bf16_f32 v46, v48, v49\n" \
  "v_cvt_pk_bf16_f32 v47, v114, v115\n" \
  "v_cvt_pk_bf16_f32 v248, v6, v7\n" \
  "v_permlane16_swap_b32 v30, v254\n" \
  "v_pk_add_f32 v[10:11], v[10:11], v[30:31] op_sel_hi:[1,0] neg_lo:[0,1] neg_hi:[0,1]\n" \
  "v_pk_add_f32 v[12:13], v[12:13], v[30:31] op_sel_hi:[1,0] neg_lo:[0,1] neg_hi:[0,1]\n" \
  "v_pk_add_f32 v[14:15], v[14:15], v[30:31] op_sel_hi:[1,0] neg_lo:[0,1] neg_hi:[0,1]\n" \
  "v_pk_add_f32 v[26:27], v[26:27], v[30:31] op_sel_hi:[1,0] neg_lo:[0,1] neg_hi:[0,1]\n" \
  "v_pk_add_f32 v[2:3], v[2:3], v[30:31] op_sel_hi:[1,0] neg_lo:[0,1] neg_hi:[0,1]\n" \
  "v_pk_add_f32 v[4:5], v[4:5], v[30:31] op_sel_hi:[1,0] neg_lo:[0,1] neg_hi:[0,1]\n" \
  "v_pk_add_f32 v[48:49], v[48:49], v[30:31] op_sel_hi:[1,0] neg_lo:[0,1] neg_hi:[0,1]\n" \
  "v_pk_add_f32 v[114:115], v[114:115], v[30:31] op_sel_hi:[1,0] neg_lo:[0,1] neg_hi:[0,1]\n" \
  "v_pk_add_f32 v[6:7], v[6:7], v[30:31] op_sel_hi:[1,0] neg_lo:[0,1] neg_hi:[0,1]\n" \
  "v_pk_add_f32 v[30:31], v[10:11], v[12:13]\n" \
  "v_pk_mul_f32 v[62:63], v[10:11], v[10:11]\n" \
  "v_pk_fma_f32 v[62:63], v[12:13], v[12:13], v[62:63]\n" \
  "v_pk_add_f32 v[30:31], v[30:31], v[14:15]\n" \
  "v_pk_fma_f32 v[62:63], v[14:15], v[14:15], v[62:63]\n" \
  "v_pk_add_f32 v[30:31], v[30:31], v[26:27]\n" \
  "v_pk_fma_f32 v[62:63], v[26:27], v[26:27], v[62:63]\n" \
  "v_pk_add_f32 v[30:31], v[30:31], v[2:3]\n" \
  "v_pk_fma_f32 v[62:63], v[2:3], v[2:3], v[62:63]\n" \
  "v_pk_add_f32 v[30:31], v[30:31], v[4:5]\n" \
  "v_pk_fma_f32 v[62:63], v[4:5], v[4:5], v[62:63]\n" \
  "v_pk_add_f32 v[30:31], v[30:31], v[48:49]\n" \
  "v_pk_fma_f32 v[62:63], v[48:49], v[48:49], v[62:63]\n" \
  "v_pk_add_f32 v[30:31], v[30:31], v[114:115]\n" \
  "v_pk_fma_f32 v[62:63], v[114:115], v[114:115], v[62:63]\n" \
  "v_pk_add_f32 v[30:31], v[30:31], v[6:7]\n" \
  "v_pk_fma_f32 v[62:63], v[6:7], v[6:7], v[62:63]\n" \
  "v_add_f32 v254, v30, v31\n" \
  "v_add_f32 v246, v62, v63\n" \
  "s_waitcnt vmcnt(16)\n" \
  "ds_read_b64_tr_b16 a[120:121], v18 offset:0\n" \
  "ds_read_b64_tr_b16 a[124:125], v19 offset:0\n" \
  "ds_read_b64_tr_b16 a[128:129], v20 offset:0\n" \
  "ds_read_b64_tr_b16 a[132:133], v21 offset:0\n" \
  "v_permlane16_swap_b32 v254, v246\n" \
  "v_add_f32 v254, v254, v246\n" \
  "buffer_store_dwordx4 v[36:39], v251, s[36:39], s10 offen offset:0 sc1\n" \
  "ds_read_b64_tr_b16 a[140:141], v18 offset:2048\n" \
  "ds_read_b64_tr_b16 a[144:145], v19 offset:2048\n" \
  "ds_read_b64_tr_b16 a[148:149], v20 offset:2048\n" \
  "v_add_f32_dpp v254, v254, v254 quad_perm:[1,0,3,2] row_mask:0xf bank_mask:0xf\n" \
  "buffer_store_dwordx4 v[44:47], v251, s[36:39], s10 offen offset:512 sc1\n" \
  "ds_read_b64_tr_b16 a[152:153], v21 offset:2048\n" \
  "ds_read_b64_tr_b16 a[122:123], v18 offset:4096\n" \
  "ds_read_b64_tr_b16 a[126:127], v19 offset:4096\n" \
  "v_add_f32_dpp v254, v254, v254 quad_perm:[2,3,0,1] row_mask:0xf bank_mask:0xf\n" \
  "buffer_store_dword v248, v250, s[36:39], s10 offen offset:1024 sc1\n" \
  "ds_read_b64_tr_b16 a[130:131], v20 offset:4096\n" \
  "ds_read_b64_tr_b16 a[134:135], v21 offset:4096\n" \
  "ds_read_b64_tr_b16 a[142:143], v18 offset:6144\n" \
  "v_add_f32_dpp v254, v254, v254 row_half_mirror row_mask:0xf bank_mask:0xf\n" \
  "ds_read_b64_tr_b16 a[146:147], v19 offset:6144\n" \
  "ds_read_b64_tr_b16 a[150:151], v20 offset:6144\n" \
  "ds_read_b64_tr_b16 a[154:155], v21 offset:6144\n" \
  "v_add_f32_dpp v254, v254, v254 row_mirror row_mask:0xf bank_mask:0xf\n" \
  "v_mov_b32 v246, v254\n" \
  "s_waitcnt vmcnt(11)\n" \
  "ds_read_b64_tr_b16 a[160:161], v18 offset:8192\n" \
  "ds_read_b64_tr_b16 a[164:165], v19 offset:8192\n" \
  "ds_read_b64_tr_b16 a[168:169], v20 offset:8192\n" \
  "ds_read_b64_tr_b16 a[172:173], v21 offset:8192\n" \
  "v_permlane16_swap_b32 v254, v246\n" \
  "v_mul_f32 v254, s35, v254\n" \
  "v_fma_f32 v246, v246, s35, v29\n" \
  "v_fma_f32 v246, -v254, v254, v246\n" \
  "v_max_f32 v246, v246, v29\n" \
  "v_rsq_f32 v28, v246\n" \
  "ds_read_b64_tr_b16 a[180:181], v18 offset:10240\n" \
  "ds_read_b64_tr_b16 a[184:185], v19 offset:10240\n" \
  "ds_read_b64_tr_b16 a[188:189], v20 offset:10240\n" \
  "ds_read_b64_tr_b16 a[192:193], v21 offset:10240\n" \
  "v_mul_f32_e64 v29, -v254, v28\n" \
  "v_pk_fma_f32 v[10:11], v[10:11], v[28:29], v[28:29] op_sel:[0,0,1] op_sel_hi:[1,0,1]\n" \
  "v_pk_fma_f32 v[10:11], v[10:11], v[32:33], v[50:51]\n" \
  "v_pk_fma_f32 v[12:13], v[12:13], v[28:29], v[28:29] op_sel:[0,0,1] op_sel_hi:[1,0,1]\n" \
  "v_pk_fma_f32 v[12:13], v[12:13], v[34:35], v[52:53]\n" \
  "v_pk_fma_f32 v[14:15], v[14:15], v[28:29], v[28:29] op_sel:[0,0,1] op_sel_hi:[1,0,1]\n" \
  "v_pk_fma_f32 v[14:15], v[14:15], v[102:103], v[58:59]\n" \
  "ds_read_b64_tr_b16 a[162:163], v18 offset:12288\n" \
  "ds_read_b64_tr_b16 a[166:167], v19 offset:12288\n" \
  "s_add_u32 m0, s57, 0\n" \
  "s_add_u32 s31, s12, 221184\n" \
  "s_waitcnt lgkmcnt(10)\n" \
  "buffer_load_dwordx4 v22, s[4:7], s31 offen offset:0 lds\n" \
  "v_pk_fma_f32 v[26:27], v[26:27], v[28:29], v[28:29] op_sel:[0,0,1] op_sel_hi:[1,0,1]\n" \
  "v_pk_fma_f32 v[26:27], v[26:27], v[104:105], v[60:61]\n" \
  "v_pk_fma_f32 v[2:3], v[2:3], v[28:29], v[28:29] op_sel:[0,0,1] op_sel_hi:[1,0,1]\n" \
  "v_pk_fma_f32 v[2:3], v[2:3], v[40:41], v[54:55]\n" \
  "ds_read_b64_tr_b16 a[170:171], v20 offset:12288\n" \
  "ds_read_b64_tr_b16 a[174:175], v21 offset:12288\n" \
  "buffer_load_dwordx4 v23, s[4:7], s31 offen offset:1024 lds\n" \
  "v_pk_fma_f32 v[4:5], v[4:5], v[28:29], v[28:29] op_sel:[0,0,1] op_sel_hi:[1,0,1]\n" \
  "v_pk_fma_f32 v[4:5], v[4:5], v[42:43], v[56:57]\n" \
  "v_pk_fma_f32 v[48:49], v[48:49], v[28:29], v[28:29] op_sel:[0,0,1] op_sel_hi:[1,0,1]\n" \
  "v_pk_fma_f32 v[48:49], v[48:49], v[110:111], v[70:71]\n" \
  "ds_read_b64_tr_b16 a[182:183], v18 offset:14336\n" \
  "ds_read_b64_tr_b16 a[186:187], v19 offset:14336\n" \
  "buffer_load_dwordx4 v24, s[4:7], s31 offen offset:2048 lds\n" \
  "v_pk_fma_f32 v[114:115], v[114:115], v[28:29], v[28:29] op_sel:[0,0,1] op_sel_hi:[1,0,1]\n" \
  "v_pk_fma_f32 v[114:115], v[114:115], v[112:113], v[72:73]\n" \
  "v_pk_fma_f32 v[6:7], v[6:7], v[28:29], v[28:29] op_sel:[0,0,1] op_sel_hi:[1,0,1]\n" \
  "v_pk_fma_f32 v[6:7], v[6:7], v[124:125], v[78:79]\n" \
  "ds_read_b64_tr_b16 a[190:191], v20 offset:14336\n" \
  "ds_read_b64_tr_b16 a[194:195], v21 offset:14336\n" \
  "buffer_load_dwordx4 v25, s[4:7], s31 offen offset:3072 lds\n" \
  "v_mov_b32 v29, 925353388\n" \
  "v_pk_add_f32 v[30:31], v[10:11], v[12:13]\n" \
  "v_pk_mul_f32 v[32:33], v[10:11], v[10:11]\n" \
  "v_pk_fma_f32 v[32:33], v[12:13], v[12:13], v[32:33]\n" \
  "v_pk_add_f32 v[30:31], v[30:31], v[14:15]\n" \
  "v_pk_fma_f32 v[32:33], v[14:15], v[14:15], v[32:33]\n" \
  "v_pk_add_f32 v[30:31], v[30:31], v[26:27]\n" \
  "v_pk_fma_f32 v[32:33], v[26:27], v[26:27], v[32:33]\n" \
  "v_pk_add_f32 v[30:31], v[30:31], v[2:3]\n" \
  "v_pk_fma_f32 v[32:33], v[2:3], v[2:3], v[32:33]\n" \
  "v_pk_add_f32 v[30:31], v[30:31], v[4:5]\n" \
  "v_pk_fma_f32 v[32:33], v[4:5], v[4:5], v[32:33]\n" \
  "v_pk_add_f32 v[30:31], v[30:31], v[48:49]\n" \
  "v_pk_fma_f32 v[32:33], v[48:49], v[48:49], v[32:33]\n" \
  "v_pk_add_f32 v[30:31], v[30:31], v[114:115]\n" \
  "v_pk_fma_f32 v[32:33], v[114:115], v[114:115], v[32:33]\n" \
  "v_pk_add_f32 v[30:31], v[30:31], v[6:7]\n" \
  "v_pk_fma_f32 v[32:33], v[6:7], v[6:7], v[32:33]\n" \
  "v_add_f32 v254, v30, v31\n" \
  "v_add_f32 v251, v32, v33\n" \
  "s_waitcnt vmcnt(7)\n" \
  "ds_read_b64_tr_b16 a[240:241], v18 offset:16384\n" \
  "ds_read_b64_tr_b16 a[244:245], v19 offset:16384\n" \
  "ds_read_b64_tr_b16 a[248:249], v20 offset:16384\n" \
  "s_add_u32 m0, s57, 4096\n" \
  "s_add_u32 s31, s12, 9658368\n" \
  "buffer_load_dwordx4 v22, s[4:7], s31 offen offset:0 lds\n" \
  "v_permlane16_swap_b32 v254, v251\n" \
  "v_add_f32 v254, v254, v251\n" \
  "ds_read_b64_tr_b16 a[252:253], v21 offset:16384\n" \
  "ds_read_b64_tr_b16 v[196:197], v18 offset:18432\n" \
  "ds_read_b64_tr_b16 v[200:201], v19 offset:18432\n" \
  "buffer_load_dwordx4 v23, s[4:7], s31 offen offset:1024 lds\n" \
  "v_add_f32_dpp v254, v254, v254 quad_perm:[1,0,3,2] row_mask:0xf bank_mask:0xf\n" \
  "ds_read_b64_tr_b16 v[204:205], v20 offset:18432\n" \
  "ds_read_b64_tr_b16 v[208:209], v21 offset:18432\n" \
  "ds_read_b64_tr_b16 a[242:243], v18 offset:20480\n" \
  "buffer_load_dwordx4 v24, s[4:7], s31 offen offset:2048 lds\n" \
  "v_add_f32_dpp v254, v254, v254 quad_perm:[2,3,0,1] row_mask:0xf bank_mask:0xf\n" \
  "ds_read_b64_tr_b16 a[246:247], v19 offset:20480\n" \
  "ds_read_b64_tr_b16 a[250:251], v20 offset:20480\n" \
  "ds_read_b64_tr_b16 a[254:255], v21 offset:20480\n" \
  "buffer_load_dwordx4 v25, s[4:7], s31 offen offset:3072 lds\n" \
  "v_add_f32_dpp v254, v254, v254 row_half_mirror row_mask:0xf bank_mask:0xf\n" \
  "ds_read_b64_tr_b16 v[198:199], v18 offset:22528\n" \
  "ds_read_b64_tr_b16 v[202:203], v19 offset:22528\n" \
  "ds_read_b64_tr_b16 v[206:207], v20 offset:22528\n" \
  "ds_read_b64_tr_b16 v[210:211], v21 offset:22528\n" \
  "s_add_u32 m0, s57, 8192\n" \
  "s_add_u32 s31, s12, 516096\n" \
  "s_waitcnt lgkmcnt(15)\n" \
  "buffer_load_dwordx4 v22, s[4:7], s31 offen offset:0 lds\n" \
  "v_add_f32_dpp v254, v254, v254 row_mirror row_mask:0xf bank_mask:0xf\n" \
  "v_mov_b32 v251, v254\n" \
  "buffer_load_dwordx4 v23, s[4:7], s31 offen offset:1024 lds\n" \
  "buffer_load_dwordx4 v24, s[4:7], s31 offen offset:2048 lds\n" \
  "v_permlane16_swap_b32 v254, v251\n" \
  "v_mul_f32 v254, s35, v254\n" \
  "v_fma_f32 v251, v251, s35, v29\n" \
  "v_fma_f32 v251, -v254, v254, v251\n" \
  "v_max_f32 v251, v251, v29\n" \
  "v_rsq_f32 v28, v251\n" \
  "buffer_load_dwordx4 v25, s[4:7], s31 offen offset:3072 lds\n" \
  "v_mul_f32_e64 v29, -v254, v28\n" \
  "v_pk_fma_f32 v[10:11], v[10:11], v[28:29], v[28:29] op_sel:[0,0,1] op_sel_hi:[1,0,1]\n" \
  "v_pk_fma_f32 v[12:13], v[12:13], v[28:29], v[28:29] op_sel:[0,0,1] op_sel_hi:[1,0,1]\n" \
  "v_pk_fma_f32 v[14:15], v[14:15], v[28:29], v[28:29] op_sel:[0,0,1] op_sel_hi:[1,0,1]\n" \
  "v_pk_fma_f32 v[26:27], v[26:27], v[28:29], v[28:29] op_sel:[0,0,1] op_sel_hi:[1,0,1]\n" \
  "v_pk_fma_f32 v[2:3], v[2:3], v[28:29], v[28:29] op_sel:[0,0,1] op_sel_hi:[1,0,1]\n" \
  "v_pk_fma_f32 v[4:5], v[4:5], v[28:29], v[28:29] op_sel:[0,0,1] op_sel_hi:[1,0,1]\n" \
  "v_pk_fma_f32 v[48:49], v[48:49], v[28:29], v[28:29] op_sel:[0,0,1] op_sel_hi:[1,0,1]\n" \
  "v_pk_fma_f32 v[114:115], v[114:115], v[28:29], v[28:29] op_sel:[0,0,1] op_sel_hi:[1,0,1]\n" \
  "v_pk_fma_f32 v[6:7], v[6:7], v[28:29], v[28:29] op_sel:[0,0,1] op_sel_hi:[1,0,1]\n" \
  "v_cvt_pk_bf16_f32 v28, v10, v11\n" \
  "v_cvt_pk_bf16_f32 v29, v12, v13\n" \
  "v_cvt_pk_bf16_f32 v30, v14, v15\n" \
  "v_cvt_pk_bf16_f32 v31, v26, v27\n" \
  "ds_write_b128 v255, v[28:31] offset:0\n" \
  "v_cvt_pk_bf16_f32 v10, v2, v3\n" \
  "v_cvt_pk_bf16_f32 v11, v4, v5\n" \
  "v_cvt_pk_bf16_f32 v12, v48, v49\n" \
  "v_cvt_pk_bf16_f32 v13, v114, v115\n" \
  "ds_write_b128 v255, v[10:13] offset:512\n" \
  "v_cvt_pk_bf16_f32 v255, v6, v7\n" \
  "ds_write_b32 v249, v255 offset:1024\n" \
  "s_add_u32 m0, s57, 12288\n" \
  "s_add_u32 s31, s12, 9953280\n" \
  "buffer_load_dwordx4 v22, s[4:7], s31 offen offset:0 lds\n" \
  "buffer_load_dwordx4 v23, s[4:7], s31 offen offset:1024 lds\n" \
  "s_waitcnt lgkmcnt(0)\n" \
  "s_barrier\n" \
  "ds_read_b64_tr_b16 v[2:3], v253 offset:9216\n" \
  "ds_read_b64_tr_b16 v[4:5], v253 offset:11664\n" \
  "ds_read_b64_tr_b16 v[10:11], v253 offset:9728\n" \
  "ds_read_b64_tr_b16 v[12:13], v253 offset:12176\n" \
  "ds_read_b64_tr_b16 v[26:27], v253 offset:10240\n" \
  "ds_read_b64_tr_b16 v[28:29], v253 offset:12688\n" \
  "buffer_load_dwordx4 v24, s[4:7], s31 offen offset:2048 lds\n" \
  "buffer_load_dwordx4 v25, s[4:7], s31 offen offset:3072 lds\n" \
  "s_add_u32 s32, s11, 1048576\n" \
  "s_add_u32 s33, s11, 2097152\n" \
  "s_waitcnt lgkmcnt(4)\n" \
  "buffer_store_dwordx4 v[2:5], v241, s[36:39], s11 offen offset:0 sc1\n" \
  "s_waitcnt lgkmcnt(2)\n" \
  "buffer_store_dwordx4 v[10:13], v241, s[36:39], s32 offen offset:0 sc1\n" \
  "s_mov_b64 exec, 65535\n" \
  "s_waitcnt lgkmcnt(0)\n" \
  "buffer_store_dwordx4 v[26:29], v241, s[36:39], s33 offen offset:0 sc1\n" \
  "s_mov_b64 exec, -1\n" \
  ".Lscc_%=:\n" \
  "v_xor_b32 v255, s18, v8\n" \
  "v_bitop3_b32 v255, v9, s19, v255 bitop3:0xbe\n" \
  "v_cmp_ne_u32 vcc, 0, v255\n" \
  "s_cbranch_vccnz .Lscs_%=\n" \
  ".Lscd_%=:\n" \
  "s_add_u32 m0, s57, 16384\n" \
  "s_add_u32 s31, s14, 0\n" \
  "buffer_load_dwordx4 v252, s[36:39], s31 offen offset:0 sc1 lds\n" \
  "buffer_load_dwordx4 v252, s[36:39], s31 offen offset:1024 sc1 lds\n" \
  "buffer_load_dwordx4 v252, s[36:39], s31 offen offset:2048 sc1 lds\n" \
  "buffer_load_dwordx4 v252, s[36:39], s31 offen offset:3072 sc1 lds\n" \
  "s_add_u32 m0, s57, 20480\n" \
  "s_add_u32 s31, s14, 8192\n" \
  "buffer_load_dwordx4 v252, s[36:39], s31 offen offset:0 sc1 lds\n" \
  "buffer_load_dwordx4 v252, s[36:39], s31 offen offset:1024 sc1 lds\n" \
  "buffer_load_dwordx4 v252, s[36:39], s31 offen offset:2048 sc1 lds\n" \
  "buffer_load_dwordx4 v252, s[36:39], s31 offen offset:3072 sc1 lds\n" \
  "s_lshr_b32 s0, s2, 6\n" \
  "s_lshr_b32 s1, s2, 1\n" \
  "s_and_b32 s34, s1, 3\n" \
  "s_lshr_b32 s1, s2, 3\n" \
  "s_and_b32 s84, s1, 1\n" \
  "s_and_b32 s1, s2, 7\n" \
  "s_lshl_b32 s1, s1, 2\n" \
  "s_lshr_b32 s100, s2, 4\n" \
  "s_and_b32 s100, s100, 3\n" \
  "s_add_u32 s88, s1, s100\n" \
  "s_and_b32 s1, s2, 1\n" \
  "s_lshl_b32 s1, s1, 3\n" \
  "s_lshr_b32 s100, s2, 3\n" \
  "s_and_b32 s100, s100, 7\n" \
  "s_add_u32 s85, s1, s100\n" \
  "s_mul_i32 s1, s0, 4505600\n" \
  "s_add_u32 s52, s1, 12123648\n" \
  "s_add_u32 s53, s52, 3276800\n" \
  "s_add_u32 s83, s52, 2048000\n" \
  "s_lshl_b32 s1, s0, 5\n" \
  "s_add_u32 s1, s1, s88\n" \
  "s_lshl_b32 s1, s1, 1\n" \
  "s_add_u32 s1, s1, s84\n" \
  "s_lshl_b32 s1, s1, 7\n" \
  "s_lshl_b32 s100, s20, 5\n" \
  "s_add_u32 s1, s1, s100\n" \
  "s_add_u32 s54, s1, 262144\n" \
  "s_lshl_b32 s1, s20, 5\n" \
  "s_sub_u32 s1, s54, s1\n" \
  "s_lshl_b32 s100, s34, 11\n" \
  "s_sub_u32 s1, s1, s100\n" \
  "s_add_u32 s16, s1, 65536\n" \
  "s_mov_b32 s55, 3184710631\n" \
  "s_mul_i32 s56, s20, 9568\n" \
  "s_lshl_b32 s58, s20, 13\n" \
  "s_lshr_b32 s1, s88, 1\n" \
  "s_mul_i32 s90, s1, 18\n" \
  "s_and_b32 s1, s88, 1\n" \
  "s_lshl_b32 s1, s1, 3\n" \
  "s_add_u32 s1, s90, s1\n" \
  "s_lshl_b32 s100, s84, 2\n" \
  "s_add_u32 s1, s1, s100\n" \
  "s_add_u32 s1, s1, s20\n" \
  "s_add_u32 s1, s1, 1\n" \
  "s_lshl_b32 s63, s1, 9\n" \
  "s_and_b32 s1, s88, 1\n" \
  "s_add_u32 s1, s90, s1\n" \
  "s_add_u32 s1, s1, 17\n" \
  "s_lshl_b32 s1, s1, 9\n" \
  "s_lshl_b32 s100, s20, 7\n" \
  "s_add_u32 s64, s1, s100\n" \
  "s_lshl_b32 s1, s0, 5\n" \
  "s_lshl_b32 s100, s34, 3\n" \
  "s_add_u32 s1, s1, s100\n" \
  "s_lshl_b32 s100, s20, 1\n" \
  "s_add_u32 s1, s1, s100\n" \
  "s_lshl_b32 s1, s1, 8\n" \
  "s_add_u32 s1, s1, 262144\n" \
  "s_sub_u32 s59, s1, s63\n" \
  "s_add_u32 s75, s59, 196608\n" \
  "s_lshl_b32 s1, s85, 7\n" \
  "s_mul_i32 s1, s1, 576\n" \
  "s_mul_i32 s100, s0, 144\n" \
  "s_add_u32 s90, s1, s100\n" \
  "s_mul_i32 s1, s20, 9216\n" \
  "s_add_u32 s1, s90, s1\n" \
  "s_lshl_b32 s100, s34, 5\n" \
  "s_add_u32 s1, s1, s100\n" \
  "s_lshl_b32 s61, s1, 1\n" \
  "s_lshl_b32 s1, s34, 4\n" \
  "s_add_u32 s1, s1, s20\n" \
  "s_mul_i32 s1, s1, 576\n" \
  "s_add_u32 s1, s90, s1\n" \
  "s_add_u32 s1, s1, 128\n" \
  "s_lshl_b32 s62, s1, 1\n" \
  "v_and_b32 v255, 768, v252\n" \
  "v_lshlrev_b32 v255, 2, v255\n" \
  "v_and_b32 v254, 192, v252\n" \
  "v_lshl_or_b32 v255, v254, 1, v255\n" \
  "v_bfe_u32 v254, v191, 3, 2\n" \
  "v_lshl_or_b32 v255, v254, 5, v255\n" \
  "v_and_b32 v253, 3, v191\n" \
  "v_lshl_or_b32 v255, v253, 3, v255\n" \
  "v_add_u32 v8, s57, v255\n" \
  "v_mov_b32 v63, v8\n" \
  "v_lshl_or_b32 v9, v243, 16, v244\n" \
  "v_and_b32 v255, 12, v191\n" \
  "v_lshrrev_b32 v254, 4, v191\n" \
  "v_add_u32 v254, v254, v255\n" \
  "v_mul_u32_u24 v254, 1152, v254\n" \
  "v_lshl_add_u32 v254, v253, 3, v254\n" \
  "v_add_u32 v10, s61, v254\n" \
  "v_and_b32 v253, 1, v191\n" \
  "v_lshl_add_u32 v255, v253, 1, v255\n" \
  "v_bfe_u32 v254, v191, 1, 1\n" \
  "v_lshlrev_b32 v254, 6, v254\n" \
  "v_lshrrev_b32 v11, 2, v191\n" \
  "v_and_or_b32 v254, v11, 12, v254\n" \
  "v_mul_u32_u24 v254, 1152, v254\n" \
  "v_lshl_add_u32 v254, v255, 1, v254\n" \
  "v_add_u32 v11, s62, v254\n" \
  "v_lshrrev_b32 v255, 3, v252\n" \
  "v_bitop3_b32 v255, v255, 32, v252 bitop3:0x6a\n" \
  "v_lshl_or_b32 v255, v255, 5, v255\n" \
  "v_and_b32 v12, 28784, v255\n" \
  "v_xor_b32 v255, 64, v12\n" \
  "v_add_u32 v13, 31744, v255\n" \
  "v_lshlrev_b32 v14, 16, v242\n" \
  "v_add_u32 v255, s57, v252\n" \
  "v_add_u32 v22, 28672, v255\n" \
  "v_lshlrev_b32 v52, 16, v16\n" \
  "v_and_b32 v53, 4294901760, v16\n" \
  "v_lshlrev_b32 v54, 16, v17\n" \
  "v_and_b32 v55, 4294901760, v17\n" \
  "v_lshlrev_b32 v56, 16, v247\n" \
  "v_mov_b32 v57, 3222493025\n" \
  "s_mul_i32 s32, s20, 1056\n" \
  "v_bfe_u32 v255, v191, 1, 1\n" \
  "v_mul_u32_u24 v255, 544, v255\n" \
  "v_lshl_add_u32 v255, v253, 4, v255\n" \
  "v_bfe_u32 v254, v191, 2, 1\n" \
  "v_lshl_add_u32 v255, v254, 8, v255\n" \
  "v_bfe_u32 v254, v191, 3, 2\n" \
  "v_lshl_add_u32 v255, v254, 6, v255\n" \
  "v_and_b32 v254, 32, v191\n" \
  "v_add3_u32 v58, v255, v254, s32\n" \
  "v_add_u32 v59, 524288, v252\n" \
  "v_bfe_u32 v255, v191, 3, 2\n" \
  "v_mul_u32_u24 v255, 288, v255\n" \
  "v_lshl_add_u32 v255, v254, 1, v255\n" \
  "v_and_b32 v254, 7, v191\n" \
  "v_lshl_add_u32 v255, v254, 2, v255\n" \
  "s_and_b32 s32, s20, 1\n" \
  "s_lshl_b32 s32, s32, 7\n" \
  "s_lshr_b32 s33, s20, 1\n" \
  "s_lshl_b32 s33, s33, 5\n" \
  "s_add_u32 s32, s32, s33\n" \
  "s_add_u32 s32, s32, 4224\n" \
  "v_add_u32 v60, s32, v255\n" \
  "v_lshl_add_u32 v61, v191, 3, s63\n" \
  "v_lshl_add_u32 v62, v191, 1, s64\n" \
  "s_mul_i32 s1, s34, 1152\n" \
  "s_lshr_b32 s100, s20, 1\n" \
  "s_mul_i32 s100, s100, 576\n" \
  "s_add_u32 s1, s1, s100\n" \
  "s_lshl_b32 s1, s1, 12\n" \
  "s_lshl_b32 s100, s85, 8\n" \
  "s_add_u32 s65, s1, s100\n" \
  "s_and_b32 s1, s20, 1\n" \
  "s_lshl_b32 s1, s1, 20\n" \
  "s_add_u32 s1, s65, s1\n" \
  "s_add_u32 s60, s1, 655488\n" \
  "s_and_b32 s1, s20, 1\n" \
  "s_lshl_b32 s1, s1, 17\n" \
  "s_add_u32 s1, s65, s1\n" \
  "s_add_u32 s65, s1, 2097152\n" \
  "s_add_u32 s66, s65, 128\n" \
  "s_add_u32 s61, s60, 130944\n" \
  "s_add_u32 s62, s60, 131072\n" \
  "s_add_u32 s63, s60, 262016\n" \
  "s_add_u32 s64, s60, 262144\n" \
  "s_lshl_b32 s1, s34, 7\n" \
  "s_add_u32 s67, s1, 163200\n" \
  "s_lshl_b32 s1, s34, 2\n" \
  "s_add_u32 s98, s1, s20\n" \
  "s_mul_i32 s1, s98, 9216\n" \
  "s_add_u32 s1, s1, s52\n" \
  "s_add_u32 s68, s1, 819712\n" \
  "s_add_u32 s69, s68, 3072\n" \
  "s_add_u32 s70, s68, 6144\n" \
  "s_lshl_b32 s1, s85, 14\n" \
  "s_add_u32 s1, s1, s52\n" \
  "s_add_u32 s90, s1, 967168\n" \
  "s_lshl_b32 s1, s98, 8\n" \
  "s_add_u32 s79, s1, s90\n" \
  "s_add_u32 s80, s79, 4096\n" \
  "s_add_u32 s81, s79, 8192\n" \
  "s_add_u32 s82, s79, 12288\n" \
  "s_lshl_b32 s99, s20, 10\n" \
  "s_add_u32 s87, s99, 65536\n" \
  "s_add_u32 s90, s90, s99\n" \
  "s_lshl_b32 s1, s2, 2\n" \
  "s_add_u32 s1, s1, s20\n" \
  "s_lshl_b32 s1, s1, 8\n" \
  "s_add_u32 s91, s1, 69206016\n" \
  "s_mul_i32 s1, s0, 589824\n" \
  "s_lshl_b32 s100, s20, 14\n" \
  "s_add_u32 s1, s1, s100\n" \
  "s_add_u32 s48, s1, 9764864\n" \
  "s_sub_u32 s1, s34, 1\n" \
  "s_and_b32 s1, s1, 3\n" \
  "s_lshl_b32 s1, s1, 12\n" \
  "s_add_u32 s98, s1, s90\n" \
  "s_add_u32 s71, s98, 4292919296\n" \
  "s_add_u32 s76, s98, 4293328896\n" \
  "s_sub_u32 s1, s34, 2\n" \
  "s_and_b32 s1, s1, 3\n" \
  "s_lshl_b32 s1, s1, 12\n" \
  "s_add_u32 s98, s1, s90\n" \
  "s_add_u32 s72, s98, 4293328896\n" \
  "s_add_u32 s77, s98, 4293328896\n" \
  "s_sub_u32 s1, s34, 3\n" \
  "s_and_b32 s1, s1, 3\n" \
  "s_lshl_b32 s1, s1, 12\n" \
  "s_add_u32 s98, s1, s90\n" \
  "s_add_u32 s73, s98, 4293328896\n" \
  "s_add_u32 s78, s98, 4293738496\n" \
  "s_and_b32 s1, s34, 3\n" \
  "s_lshl_b32 s1, s1, 12\n" \
  "s_add_u32 s98, s1, s90\n" \
  "s_add_u32 s74, s98, 4294148096\n" \
  "s_add_u32 s49, s48, 8192\n" \
  "s_add_u32 s50, s48, 4096\n" \
  "s_add_u32 s51, s48, 12288\n" \
  "s_lshr_b32 s1, s2, 3\n" \
  "s_lshl_b32 s1, s1, 2\n" \
  "s_add_u32 s98, s1, s20\n" \
  "s_and_b32 s1, s98, 31\n" \
  "s_lshl_b32 s1, s1, 2\n" \
  "s_add_u32 s90, s1, 448\n" \
  "s_and_b32 s1, s98, 8\n" \
  "s_lshl_b32 s1, s1, 5\n" \
  "s_and_b32 s100, s98, 7\n" \
  "s_lshl_b32 s100, s100, 2\n" \
  "s_add_u32 s1, s1, s100\n" \
  "s_add_u32 s89, s1, 160\n" \
  "s_and_b32 s1, s98, 15\n" \
  "s_lshl_b32 s1, s1, 2\n" \
  "s_add_u32 s86, s1, 192\n" \
  "s_bitcmp1_b32 s98, 4\n" \
  "s_cselect_b32 s89, s89, s86\n" \
  "s_bitcmp1_b32 s98, 5\n" \
  "s_cselect_b32 s89, s90, s89\n" \
  "s_lshr_b32 s1, s98, 6\n" \
  "s_mul_i32 s1, s1, 576\n" \
  "s_mul_i32 s100, s34, 1152\n" \
  "s_add_u32 s1, s1, s100\n" \
  "s_add_u32 s1, s1, s89\n" \
  "s_lshl_b32 s1, s1, 12\n" \
  "s_and_b32 s100, s2, 1\n" \
  "s_lshl_b32 s100, s100, 11\n" \
  "s_add_u32 s1, s1, s100\n" \
  "s_lshr_b32 s100, s2, 3\n" \
  "s_and_b32 s100, s100, 3\n" \
  "s_add_u32 s1, s1, s100\n" \
  "s_add_u32 s89, s1, 1\n" \
  "s_sub_u32 s98, s57, s99\n" \
  "s_lshl_b32 s1, s34, 17\n" \
  "s_add_u32 s85, s1, 65536\n" \
  "s_mov_b32 s90, 7405568\n" \
  "s_add_u32 s1, s0, 3\n" \
  "s_and_b32 s1, s1, 3\n" \
  "s_lshl_b32 s88, s1, 12\n" \
  "s_mov_b32 s30, s16\n" \
  "v_mov_b32 v2, v12\n" \
  "v_mov_b32 v3, v13\n" \
  "v_add_u32 v4, 63488, v12\n" \
  "v_add_u32 v5, 63488, v13\n" \
  "v_mov_b32 v190, 1\n" \
  "v_lshrrev_b32 v189, 1, v252\n" \
  "v_add_u32 v255, s57, v189\n" \
  "s_lshl_b32 s32, s20, 11\n" \
  "v_add_u32 v189, s32, v189\n" \
  "s_waitcnt vmcnt(19)\n" \
  "ds_read_b64_tr_b16 a[200:201], v18 offset:0\n" \
  "ds_read_b64_tr_b16 a[204:205], v19 offset:0\n" \
  "ds_read_b64_tr_b16 a[208:209], v20 offset:0\n" \
  "ds_read_b64_tr_b16 a[212:213], v21 offset:0\n" \
  "ds_read_b64_tr_b16 a[220:221], v18 offset:2048\n" \
  "ds_read_b64_tr_b16 a[224:225], v19 offset:2048\n" \
  "ds_read_b64_tr_b16 a[228:229], v20 offset:2048\n" \
  "ds_read_b64_tr_b16 a[232:233], v21 offset:2048\n" \
  "ds_read_b64_tr_b16 a[202:203], v18 offset:4096\n" \
  "ds_read_b64_tr_b16 a[206:207], v19 offset:4096\n" \
  "ds_read_b64_tr_b16 a[210:211], v20 offset:4096\n" \
  "ds_read_b64_tr_b16 a[214:215], v21 offset:4096\n" \
  "ds_read_b64_tr_b16 a[222:223], v18 offset:6144\n" \
  "ds_read_b64_tr_b16 a[226:227], v19 offset:6144\n" \
  "ds_read_b64_tr_b16 a[230:231], v20 offset:6144\n" \
  "ds_read_b64_tr_b16 a[234:235], v21 offset:6144\n" \
  "s_waitcnt vmcnt(11)\n" \
  "ds_read_b64_tr_b16 v[216:217], v18 offset:8192\n" \
  "ds_read_b64_tr_b16 v[220:221], v19 offset:8192\n" \
  "ds_read_b64_tr_b16 v[224:225], v20 offset:8192\n" \
  "ds_read_b64_tr_b16 v[228:229], v21 offset:8192\n" \
  "ds_read_b64_tr_b16 v[236:237], v18 offset:10240\n" \
  "ds_read_b64_tr_b16 v[240:241], v19 offset:10240\n" \
  "ds_read_b64_tr_b16 v[244:245], v20 offset:10240\n" \
  "ds_read_b64_tr_b16 v[248:249], v21 offset:10240\n" \
  "ds_read_b64_tr_b16 v[218:219], v18 offset:12288\n" \
  "ds_read_b64_tr_b16 v[222:223], v19 offset:12288\n" \
  "ds_read_b64_tr_b16 v[226:227], v20 offset:12288\n" \
  "ds_read_b64_tr_b16 v[230:231], v21 offset:12288\n" \
  "ds_read_b64_tr_b16 v[238:239], v18 offset:14336\n" \
  "ds_read_b64_tr_b16 v[242:243], v19 offset:14336\n" \
  "ds_read_b64_tr_b16 v[246:247], v20 offset:14336\n" \
  "ds_read_b64_tr_b16 v[250:251], v21 offset:14336\n" \
  "s_waitcnt vmcnt(8)\n" \
  "s_mov_b64 exec, 1\n" \
  "buffer_store_dword v190, off, s[36:39], s28 sc1\n" \
  "s_mov_b64 exec, -1\n" \
  "s_add_u32 m0, s57, 24576\n" \
  "s_add_u32 s31, s14, 4096\n" \
  "buffer_load_dwordx4 v252, s[36:39], s31 offen offset:0 sc1 lds\n" \
  "buffer_load_dwordx4 v252, s[36:39], s31 offen offset:1024 sc1 lds\n" \
  "buffer_load_dwordx4 v252, s[36:39], s31 offen offset:2048 sc1 lds\n" \
  "buffer_load_dwordx4 v252, s[36:39], s31 offen offset:3072 sc1 lds\n" \
  "s_add_u32 m0, s57, 28672\n" \
  "s_add_u32 s31, s14, 12288\n" \
  "buffer_load_dwordx4 v252, s[36:39], s31 offen offset:0 sc1 lds\n" \
  "buffer_load_dwordx4 v252, s[36:39], s31 offen offset:1024 sc1 lds\n" \
  "s_lshl_b32 m0, s20, 11\n" \
  "s_add_u32 s31, s31, 2048\n" \
  "buffer_load_dwordx4 v252, s[36:39], s31 offen offset:0 sc1 lds\n" \
  "buffer_load_dwordx4 v252, s[36:39], s31 offen offset:1024 sc1 lds\n" \
  "s_bfe_u32 s32, s27, 131076\n" \
  "s_sub_u32 s100, s60, 655488\n" \
  "s_sub_u32 s101, s60, 720896\n" \
  "s_lshl_b32 s32, s32, 12\n" \
  "s_add_u32 s32, s32, 0\n" \
  "s_cmp_ge_u32 s32, 16384\n" \
  "s_cselect_b32 s32, 0, s32\n" \
  "s_bitcmp1_b32 s32, 12\n" \
  "s_cselect_b32 s33, s101, s100\n" \
  "s_lshl4_add_u32 s31, s32, s33\n" \
  "s_add_u32 s33, s32, s57\n" \
  "s_add_u32 m0, s33, 0\n" \
  "s_waitcnt lgkmcnt(0)\n" \
  "buffer_load_dwordx4 v2, s[40:43], s31 offen offset:0 lds\n" \
  "buffer_load_dwordx4 v3, s[40:43], s31 offen offset:1024 lds\n" \
  "buffer_load_dwordx4 v4, s[40:43], s31 offen offset:2048 lds\n" \
  "buffer_load_dwordx4 v5, s[40:43], s31 offen offset:3072 lds\n" \
  "s_add_u32 s32, s32, 4096\n" \
  "s_cmp_ge_u32 s32, 16384\n" \
  "s_cselect_b32 s32, 0, s32\n" \
  "s_bitcmp1_b32 s32, 12\n" \
  "s_cselect_b32 s33, s101, s100\n" \
  "s_lshl4_add_u32 s31, s32, s33\n" \
  "s_add_u32 s33, s32, s57\n" \
  "s_add_u32 m0, s33, 0\n" \
  "s_nop 0\n" \
  "buffer_load_dwordx4 v2, s[40:43], s31 offen offset:0 lds\n" \
  "buffer_load_dwordx4 v3, s[40:43], s31 offen offset:1024 lds\n" \
  "buffer_load_dwordx4 v4, s[40:43], s31 offen offset:2048 lds\n" \
  "buffer_load_dwordx4 v5, s[40:43], s31 offen offset:3072 lds\n" \
  "s_add_u32 s32, s32, 4096\n" \
  "s_cmp_ge_u32 s32, 16384\n" \
  "s_cselect_b32 s32, 0, s32\n" \
  "s_bitcmp1_b32 s32, 12\n" \
  "s_cselect_b32 s33, s101, s100\n" \
  "s_lshl4_add_u32 s31, s32, s33\n" \
  "s_add_u32 s33, s32, s57\n" \
  "s_add_u32 m0, s33, 0\n" \
  "s_nop 0\n" \
  "buffer_load_dwordx4 v2, s[40:43], s31 offen offset:0 lds\n" \
  "buffer_load_dwordx4 v3, s[40:43], s31 offen offset:1024 lds\n" \
  "buffer_load_dwordx4 v4, s[40:43], s31 offen offset:2048 lds\n" \
  "buffer_load_dwordx4 v5, s[40:43], s31 offen offset:3072 lds\n" \
  "s_add_u32 s32, s32, 4096\n" \
  "s_cmp_ge_u32 s32, 16384\n" \
  "s_cselect_b32 s32, 0, s32\n" \
  "s_bitcmp1_b32 s32, 12\n" \
  "s_cselect_b32 s33, s101, s100\n" \
  "s_lshl4_add_u32 s31, s32, s33\n" \
  "s_add_u32 s33, s32, s57\n" \
  "s_add_u32 m0, s33, 0\n" \
  "s_nop 0\n" \
  "buffer_load_dwordx4 v2, s[40:43], s31 offen offset:0 lds\n" \
  "buffer_load_dwordx4 v3, s[40:43], s31 offen offset:1024 lds\n" \
  "buffer_load_dwordx4 v4, s[40:43], s31 offen offset:2048 lds\n" \
  "buffer_load_dwordx4 v5, s[40:43], s31 offen offset:3072 lds\n" \
  "s_waitcnt vmcnt(16)\n" \
  "ds_read_b64_tr_b16 a[16:17], v255 offset:16384\n" \
  "ds_read_b64_tr_b16 a[36:37], v255 offset:16896\n" \
  "ds_read_b64_tr_b16 a[18:19], v255 offset:17408\n" \
  "ds_read_b64_tr_b16 a[38:39], v255 offset:17920\n" \
  "ds_read_b64_tr_b16 a[56:57], v255 offset:18432\n" \
  "ds_read_b64_tr_b16 a[76:77], v255 offset:18944\n" \
  "ds_read_b64_tr_b16 a[58:59], v255 offset:19456\n" \
  "ds_read_b64_tr_b16 a[78:79], v255 offset:19968\n" \
  "ds_read_b64_tr_b16 a[96:97], v255 offset:20480\n" \
  "ds_read_b64_tr_b16 a[116:117], v255 offset:20992\n" \
  "ds_read_b64_tr_b16 a[98:99], v255 offset:21504\n" \
  "ds_read_b64_tr_b16 a[118:119], v255 offset:22016\n" \
  "ds_read_b64_tr_b16 a[136:137], v255 offset:22528\n" \
  "ds_read_b64_tr_b16 a[156:157], v255 offset:23040\n" \
  "ds_read_b64_tr_b16 a[138:139], v255 offset:23552\n" \
  "ds_read_b64_tr_b16 a[158:159], v255 offset:24064\n" \
  "ds_read_b64_tr_b16 a[176:177], v255 offset:24576\n" \
  "ds_read_b64_tr_b16 a[196:197], v255 offset:25088\n" \
  "ds_read_b64_tr_b16 a[178:179], v255 offset:25600\n" \
  "ds_read_b64_tr_b16 a[198:199], v255 offset:26112\n" \
  "ds_read_b64_tr_b16 a[216:217], v255 offset:26624\n" \
  "ds_read_b64_tr_b16 a[236:237], v255 offset:27136\n" \
  "ds_read_b64_tr_b16 a[218:219], v255 offset:27648\n" \
  "ds_read_b64_tr_b16 a[238:239], v255 offset:28160\n" \
  "ds_read_b64_tr_b16 v[192:193], v255 offset:28672\n" \
  "ds_read_b64_tr_b16 v[212:213], v255 offset:29184\n" \
  "ds_read_b64_tr_b16 v[194:195], v255 offset:29696\n" \
  "ds_read_b64_tr_b16 v[214:215], v255 offset:30208\n" \
  "ds_read_b64_tr_b16 v[232:233], v189 offset:0\n" \
  "ds_read_b64_tr_b16 v[252:253], v189 offset:512\n" \
  "ds_read_b64_tr_b16 v[234:235], v189 offset:1024\n" \
  "ds_read_b64_tr_b16 v[254:255], v189 offset:1536\n" \
  "v_and_b32 v189, 12, v191\n" \
  "v_and_b32 v190, 1, v191\n" \
  "v_lshl_add_u32 v189, v190, 1, v189\n" \
  "v_lshlrev_b32 v189, 2, v189\n" \
  "v_add_u32 v189, 163712, v189\n" \
  "ds_read_b64 v[6:7], v189 offset:0\n" \
  "s_bfe_u32 s32, s27, 131076\n" \
  "s_sub_u32 s100, s60, 131200\n" \
  "s_sub_u32 s101, s60, 196608\n" \
  "s_lshl_b32 s32, s32, 12\n" \
  "s_add_u32 s32, s32, 0\n" \
  "s_cmp_ge_u32 s32, 12288\n" \
  "s_cselect_b32 s32, 0, s32\n" \
  "s_bitcmp1_b32 s32, 12\n" \
  "s_cselect_b32 s33, s101, s100\n" \
  "s_lshl4_add_u32 s31, s32, s33\n" \
  "s_add_u32 s33, s32, s57\n" \
  "s_add_u32 m0, s33, 16384\n" \
  "s_waitcnt lgkmcnt(1)\n" \
  "buffer_load_dwordx4 v2, s[40:43], s31 offen offset:0 lds\n" \
  "buffer_load_dwordx4 v3, s[40:43], s31 offen offset:1024 lds\n" \
  "buffer_load_dwordx4 v4, s[40:43], s31 offen offset:2048 lds\n" \
  "buffer_load_dwordx4 v5, s[40:43], s31 offen offset:3072 lds\n" \
  "s_add_u32 s32, s32, 4096\n" \
  "s_cmp_ge_u32 s32, 12288\n" \
  "s_cselect_b32 s32, 0, s32\n" \
  "s_bitcmp1_b32 s32, 12\n" \
  "s_cselect_b32 s33, s101, s100\n" \
  "s_lshl4_add_u32 s31, s32, s33\n" \
  "s_add_u32 s33, s32, s57\n" \
  "s_add_u32 m0, s33, 16384\n" \
  "s_nop 0\n" \
  "buffer_load_dwordx4 v2, s[40:43], s31 offen offset:0 lds\n" \
  "buffer_load_dwordx4 v3, s[40:43], s31 offen offset:1024 lds\n" \
  "buffer_load_dwordx4 v4, s[40:43], s31 offen offset:2048 lds\n" \
  "buffer_load_dwordx4 v5, s[40:43], s31 offen offset:3072 lds\n" \
  "s_add_u32 s32, s32, 4096\n" \
  "s_cmp_ge_u32 s32, 12288\n" \
  "s_cselect_b32 s32, 0, s32\n" \
  "s_bitcmp1_b32 s32, 12\n" \
  "s_cselect_b32 s33, s101, s100\n" \
  "s_lshl4_add_u32 s31, s32, s33\n" \
  "s_add_u32 s33, s32, s57\n" \
  "s_add_u32 m0, s33, 16384\n" \
  "s_nop 0\n" \
  "buffer_load_dwordx4 v2, s[40:43], s31 offen offset:0 lds\n" \
  "buffer_load_dwordx4 v3, s[40:43], s31 offen offset:1024 lds\n" \
  "buffer_load_dwordx4 v4, s[40:43], s31 offen offset:2048 lds\n" \
  "buffer_load_dwordx4 v5, s[40:43], s31 offen offset:3072 lds\n" \
  "s_waitcnt lgkmcnt(0)\n" \
  "v_lshrrev_b32 v6, 16, v6\n" \
  "v_or_b32 v15, v6, v7\n" \
  ".Lgo_%=:\n"
#define ASM_END \
  ".Ladm_b:\n"
#define ASM_CLOB "v1", "v2", "v3", "v4", "v5", "v6", "v7", "v8", "v9", "v10", "v11", "v12", "v13", "v14", "v15", "v16", "v17", "v18", "v19", "v20", "v21", "v22", "v23", "v24", "v25", "v26", "v27", "v28", "v29", "v30", "v31", "v32", "v33", "v34", "v35", "v36", "v37", "v38", "v39", "v40", "v41", "v42", "v43", "v44", "v45", "v46", "v47", "v48", "v49", "v50", "v51", "v52", "v53", "v54", "v55", "v56", "v57", "v58", "v59", "v60", "v61", "v62", "v63", "v64", "v65", "v66", "v67", "v68", "v69", "v70", "v71", "v72", "v73", "v74", "v75", "v76", "v77", "v78", "v79", "v80", "v81", "v82", "v83", "v84", "v85", "v86", "v87", "v88", "v89", "v90", "v91", "v92", "v93", "v94", "v95", "v96", "v97", "v98", "v99", "v100", "v101", "v102", "v103", "v104", "v105", "v106", "v107", "v108", "v109", "v110", "v111", "v112", "v113", "v114", "v115", "v116", "v117", "v118", "v119", "v120", "v121", "v122", "v123", "v124", "v125", "v126", "v127", "v128", "v129", "v130", "v131", "v132", "v133", "v134", "v135", "v136", "v137", "v138", "v139", "v140", "v141", "v142", "v143", "v144", "v145", "v146", "v147", "v148", "v149", "v150", "v151", "v152", "v153", "v154", "v155", "v156", "v157", "v158", "v159", "v160", "v161", "v162", "v163", "v164", "v165", "v166", "v167", "v168", "v169", "v170", "v171", "v172", "v173", "v174", "v175", "v176", "v177", "v178", "v179", "v180", "v181", "v182", "v183", "v184", "v185", "v186", "v187", "v188", "v189", "v190", "v191", "v192", "v193", "v194", "v195", "v196", "v197", "v198", "v199", "v200", "v201", "v202", "v203", "v204", "v205", "v206", "v207", "v208", "v209", "v210", "v211", "v212", "v213", "v214", "v215", "v216", "v217", "v218", "v219", "v220", "v221", "v222", "v223", "v224", "v225", "v226", "v227", "v228", "v229", "v230", "v231", "v232", "v233", "v234", "v235", "v236", "v237", "v238", "v239", "v240", "v241", "v242", "v243", "v244", "v245", "v246", "v247", "v248", "v249", "v250", "v251", "v252", "v253", "v254", "v255", "a0", "a1", "a2", "a3", "a4", "a5", "a6", "a7", "a8", "a9", "a10", "a11", "a12", "a13", "a14", "a15", "a16", "a17", "a18", "a19", "a20", "a21", "a22", "a23", "a24", "a25", "a26", "a27", "a28", "a29", "a30", "a31", "a32", "a33", "a34", "a35", "a36", "a37", "a38", "a39", "a40", "a41", "a42", "a43", "a44", "a45", "a46", "a47", "a48", "a49", "a50", "a51", "a52", "a53", "a54", "a55", "a56", "a57", "a58", "a59", "a60", "a61", "a62", "a63", "a64", "a65", "a66", "a67", "a68", "a69", "a70", "a71", "a72", "a73", "a74", "a75", "a76", "a77", "a78", "a79", "a80", "a81", "a82", "a83", "a84", "a85", "a86", "a87", "a88", "a89", "a90", "a91", "a92", "a93", "a94", "a95", "a96", "a97", "a98", "a99", "a100", "a101", "a102", "a103", "a104", "a105", "a106", "a107", "a108", "a109", "a110", "a111", "a112", "a113", "a114", "a115", "a116", "a117", "a118", "a119", "a120", "a121", "a122", "a123", "a124", "a125", "a126", "a127", "a128", "a129", "a130", "a131", "a132", "a133", "a134", "a135", "a136", "a137", "a138", "a139", "a140", "a141", "a142", "a143", "a144", "a145", "a146", "a147", "a148", "a149", "a150", "a151", "a152", "a153", "a154", "a155", "a156", "a157", "a158", "a159", "a160", "a161", "a162", "a163", "a164", "a165", "a166", "a167", "a168", "a169", "a170", "a171", "a172", "a173", "a174", "a175", "a176", "a177", "a178", "a179", "a180", "a181", "a182", "a183", "a184", "a185", "a186", "a187", "a188", "a189", "a190", "a191", "a192", "a193", "a194", "a195", "a196", "a197", "a198", "a199", "a200", "a201", "a202", "a203", "a204", "a205", "a206", "a207", "a208", "a209", "a210", "a211", "a212", "a213", "a214", "a215", "a216", "a217", "a218", "a219", "a220", "a221", "a222", "a223", "a224", "a225", "a226", "a227", "a228", "a229", "a230", "a231", "a232", "a233", "a234", "a235", "a236", "a237", "a238", "a239", "a240", "a241", "a242", "a243", "a244", "a245", "a246", "a247", "a248", "a249", "a250", "a251", "a252", "a253", "a254", "a255", "s3", "s4", "s5", "s6", "s7", "s8", "s9", "s10", "s11", "s12", "s13", "s14", "s15", "s16", "s17", "s18", "s19", "s20", "s21", "s22", "s23", "s24", "s25", "s26", "s27", "s28", "s29", "s30", "s31", "s32", "s33", "s34", "s35", "s36", "s37", "s38", "s39", "s40", "s41", "s42", "s43", "s44", "s45", "s46", "s47", "s48", "s49", "s50", "s51", "s52", "s53", "s54", "s55", "s56", "s57", "s58", "s59", "s60", "s61", "s62", "s63", "s64", "s65", "s66", "s67", "s68", "s69", "s70", "s71", "s72", "s73", "s74", "s75", "s78", "s79", "s80", "s81", "s82", "s83", "s84", "s85", "s86", "s87", "s88", "s89", "s90", "s91", "s92", "s93", "s94", "s95", "s96", "s97", "s98", "s99", "s100", "s101", "vcc", "scc", "memory"
#define ASM_GEMM \
  "s_memrealtime s[94:95]\n" \
  "s_waitcnt vmcnt(12)\n" \
  "v_xor_b32 v17, 32, v63\n" \
  "v_xor_b32 v18, 64, v63\n" \
  "v_xor_b32 v19, 96, v63\n" \
  "ds_read_b64_tr_b16 v[128:129], v63 offset:0\n" \
  "ds_read_b64_tr_b16 v[130:131], v63 offset:512\n" \
  "ds_read_b64_tr_b16 v[132:133], v17 offset:0\n" \
  "ds_read_b64_tr_b16 v[134:135], v17 offset:512\n" \
  "ds_read_b64_tr_b16 v[136:137], v18 offset:0\n" \
  "ds_read_b64_tr_b16 v[138:139], v18 offset:512\n" \
  "ds_read_b64_tr_b16 v[140:141], v19 offset:0\n" \
  "ds_read_b64_tr_b16 v[142:143], v19 offset:512\n" \
  "ds_read_b64_tr_b16 v[144:145], v63 offset:4096\n" \
  "ds_read_b64_tr_b16 v[146:147], v63 offset:4608\n" \
  "ds_read_b64_tr_b16 v[148:149], v17 offset:4096\n" \
  "ds_read_b64_tr_b16 v[150:151], v17 offset:4608\n" \
  "ds_read_b64_tr_b16 v[152:153], v18 offset:4096\n" \
  "ds_read_b64_tr_b16 v[154:155], v18 offset:4608\n" \
  "ds_read_b64_tr_b16 v[156:157], v19 offset:4096\n" \
  "ds_read_b64_tr_b16 v[158:159], v19 offset:4608\n" \
  "ds_read_b64_tr_b16 v[160:161], v63 offset:8192\n" \
  "ds_read_b64_tr_b16 v[162:163], v63 offset:8704\n" \
  "ds_read_b64_tr_b16 v[164:165], v17 offset:8192\n" \
  "ds_read_b64_tr_b16 v[166:167], v17 offset:8704\n" \
  "ds_read_b64_tr_b16 v[168:169], v18 offset:8192\n" \
  "ds_read_b64_tr_b16 v[170:171], v18 offset:8704\n" \
  "ds_read_b64_tr_b16 v[172:173], v19 offset:8192\n" \
  "ds_read_b64_tr_b16 v[174:175], v19 offset:8704\n" \
  "ds_read_b64_tr_b16 v[176:177], v63 offset:12288\n" \
  "ds_read_b64_tr_b16 v[178:179], v63 offset:12800\n" \
  "ds_read_b64_tr_b16 v[180:181], v17 offset:12288\n" \
  "ds_read_b64_tr_b16 v[182:183], v17 offset:12800\n" \
  "ds_read_b64_tr_b16 v[184:185], v18 offset:12288\n" \
  "ds_read_b64_tr_b16 v[186:187], v18 offset:12800\n" \
  "ds_read_b64_tr_b16 v[188:189], v19 offset:12288\n" \
  "ds_read_b64_tr_b16 v[190:191], v19 offset:12800\n" \
  "buffer_load_dwordx4 v[64:67], v59, s[36:39], s48 offen offset:0 sc1\n" \
  "buffer_load_dwordx4 v[68:71], v59, s[36:39], s48 offen offset:1024 sc1\n" \
  "buffer_load_dwordx4 v[72:75], v59, s[36:39], s48 offen offset:2048 sc1\n" \
  "buffer_load_dwordx4 v[76:79], v59, s[36:39], s48 offen offset:3072 sc1\n" \
  "buffer_load_dwordx4 v[80:83], v59, s[36:39], s49 offen offset:0 sc1\n" \
  "buffer_load_dwordx4 v[84:87], v59, s[36:39], s49 offen offset:1024 sc1\n" \
  "buffer_load_dwordx4 v[88:91], v59, s[36:39], s49 offen offset:2048 sc1\n" \
  "buffer_load_dwordx4 v[92:95], v59, s[36:39], s49 offen offset:3072 sc1\n" \
  "buffer_load_dwordx4 v[96:99], v59, s[36:39], s50 offen offset:0 sc1\n" \
  "buffer_load_dwordx4 v[100:103], v59, s[36:39], s50 offen offset:1024 sc1\n" \
  "buffer_load_dwordx4 v[104:107], v59, s[36:39], s50 offen offset:2048 sc1\n" \
  "buffer_load_dwordx4 v[108:111], v59, s[36:39], s50 offen offset:3072 sc1\n" \
  "buffer_load_dwordx4 v[112:115], v59, s[36:39], s51 offen offset:0 sc1\n" \
  "buffer_load_dwordx4 v[116:119], v59, s[36:39], s51 offen offset:1024 sc1\n" \
  "buffer_load_dwordx4 v[120:123], v59, s[36:39], s51 offen offset:2048 sc1\n" \
  "buffer_load_dwordx4 v[124:127], v59, s[36:39], s51 offen offset:3072 sc1\n" \
  "v_and_b32 v0, 0x3ff, v59\n" \
  "v_bfrev_b32 v1, 1\n" \
  "buffer_load_dwordx4 v[4:7], v0, s[36:39], s87 offen sc1\n" \
  "s_branch .Lz0_%=\n" \
  ".Lzr_%=:\n" \
  "s_waitcnt lgkmcnt(0)\n" \
  "ds_write_b128 v22, v[8:11]\n" \
  "ds_write_b128 v22, v[12:15] offset:1024\n" \
  "s_mov_b32 s101, s89\n" \
  "s_waitcnt lgkmcnt(0)\n" \
  "s_barrier\n" \
  "s_sub_u32 s31, s60, 0x60080\n" \
  "s_sub_u32 s32, s60, 0x70000\n" \
  "v_add_u32 v14, 0xf800, v12\n" \
  "v_add_u32 v15, 0xf800, v13\n" \
  "s_add_u32 s88, s88, 4096\n" \
  "s_cmp_eq_u32 s88, 16384\n" \
  "s_cselect_b32 s88, 0, s88\n" \
  "s_bitcmp1_b32 s88, 12\n" \
  "s_cselect_b32 s89, s32, s31\n" \
  "s_add_u32 m0, s88, s57\n" \
  "s_lshl4_add_u32 s33, s88, s89\n" \
  "buffer_load_dwordx4 v12, s[40:43], s33 offen offset:0 lds\n" \
  "buffer_load_dwordx4 v13, s[40:43], s33 offen offset:1024 lds\n" \
  "buffer_load_dwordx4 v14, s[40:43], s33 offen offset:2048 lds\n" \
  "buffer_load_dwordx4 v15, s[40:43], s33 offen offset:3072 lds\n" \
  "s_add_u32 s88, s88, 4096\n" \
  "s_cmp_eq_u32 s88, 16384\n" \
  "s_cselect_b32 s88, 0, s88\n" \
  "s_bitcmp1_b32 s88, 12\n" \
  "s_cselect_b32 s89, s32, s31\n" \
  "s_add_u32 m0, s88, s57\n" \
  "s_lshl4_add_u32 s33, s88, s89\n" \
  "buffer_load_dwordx4 v12, s[40:43], s33 offen offset:0 lds\n" \
  "buffer_load_dwordx4 v13, s[40:43], s33 offen offset:1024 lds\n" \
  "buffer_load_dwordx4 v14, s[40:43], s33 offen offset:2048 lds\n" \
  "buffer_load_dwordx4 v15, s[40:43], s33 offen offset:3072 lds\n" \
  "s_add_u32 s88, s88, 4096\n" \
  "s_cmp_eq_u32 s88, 16384\n" \
  "s_cselect_b32 s88, 0, s88\n" \
  "s_bitcmp1_b32 s88, 12\n" \
  "s_cselect_b32 s89, s32, s31\n" \
  "s_add_u32 m0, s88, s57\n" \
  "s_lshl4_add_u32 s33, s88, s89\n" \
  "buffer_load_dwordx4 v12, s[40:43], s33 offen offset:0 lds\n" \
  "buffer_load_dwordx4 v13, s[40:43], s33 offen offset:1024 lds\n" \
  "buffer_load_dwordx4 v14, s[40:43], s33 offen offset:2048 lds\n" \
  "buffer_load_dwordx4 v15, s[40:43], s33 offen offset:3072 lds\n" \
  "s_add_u32 s88, s88, 4096\n" \
  "s_cmp_eq_u32 s88, 16384\n" \
  "s_cselect_b32 s88, 0, s88\n" \
  "s_bitcmp1_b32 s88, 12\n" \
  "s_cselect_b32 s89, s32, s31\n" \
  "s_add_u32 m0, s88, s57\n" \
  "s_lshl4_add_u32 s33, s88, s89\n" \
  "buffer_load_dwordx4 v12, s[40:43], s33 offen offset:0 lds\n" \
  "buffer_load_dwordx4 v13, s[40:43], s33 offen offset:1024 lds\n" \
  "buffer_load_dwordx4 v14, s[40:43], s33 offen offset:2048 lds\n" \
  "buffer_load_dwordx4 v15, s[40:43], s33 offen offset:3072 lds\n" \
  "s_mov_b32 s89, s101\n" \
  "s_waitcnt vmcnt(16)\n" \
  "s_branch .Li_%=\n" \
  ".Lh_%=:\n" \
  "buffer_load_dwordx4 v[4:7], v0, s[36:39], s87 offen sc1\n" \
  "s_waitcnt vmcnt(0)\n" \
  ".Li_%=:\n" \
  "s_memrealtime s[96:97]\n" \
  "s_waitcnt lgkmcnt(0)\n" \
  "v_bitop3_b32 v4, v4, v5, v6 bitop3:0x80\n" \
  "v_and_b32 v4, v4, v7\n" \
  "v_cmp_ne_u32 vcc, 0, v4\n" \
  "v_max3_u32 v2, v64, v68, v72\n" \
  "v_max3_u32 v2, v2, v76, v80\n" \
  "v_max3_u32 v2, v2, v84, v88\n" \
  "v_max3_u32 v2, v2, v92, v96\n" \
  "v_max3_u32 v2, v2, v100, v104\n" \
  "v_max3_u32 v2, v2, v108, v112\n" \
  "v_max3_u32 v2, v2, v116, v120\n" \
  "v_max_u32 v2, v2, v124\n" \
  "v_cndmask_b32 v0, v0, v1, vcc\n" \
  "v_cmp_eq_u32 vcc, -1, v2\n" \
  "v_cmp_ne_u32 s[100:101], v0, v1\n" \
  "s_sub_u32 s96, s96, s94\n" \
  "s_cbranch_vccz .Lj_%=\n" \
  "v_cmpx_eq_u32 vcc, -1, v64\n" \
  "buffer_load_dwordx4 v[64:67], v59, s[36:39], s48 offen offset:0 sc1\n" \
  "s_mov_b64 exec, -1\n" \
  "v_cmpx_eq_u32 vcc, -1, v68\n" \
  "buffer_load_dwordx4 v[68:71], v59, s[36:39], s48 offen offset:1024 sc1\n" \
  "s_mov_b64 exec, -1\n" \
  "v_cmpx_eq_u32 vcc, -1, v72\n" \
  "buffer_load_dwordx4 v[72:75], v59, s[36:39], s48 offen offset:2048 sc1\n" \
  "s_mov_b64 exec, -1\n" \
  "v_cmpx_eq_u32 vcc, -1, v76\n" \
  "buffer_load_dwordx4 v[76:79], v59, s[36:39], s48 offen offset:3072 sc1\n" \
  "s_mov_b64 exec, -1\n" \
  "v_cmpx_eq_u32 vcc, -1, v80\n" \
  "buffer_load_dwordx4 v[80:83], v59, s[36:39], s49 offen offset:0 sc1\n" \
  "s_mov_b64 exec, -1\n" \
  "v_cmpx_eq_u32 vcc, -1, v84\n" \
  "buffer_load_dwordx4 v[84:87], v59, s[36:39], s49 offen offset:1024 sc1\n" \
  "s_mov_b64 exec, -1\n" \
  "v_cmpx_eq_u32 vcc, -1, v88\n" \
  "buffer_load_dwordx4 v[88:91], v59, s[36:39], s49 offen offset:2048 sc1\n" \
  "s_mov_b64 exec, -1\n" \
  "v_cmpx_eq_u32 vcc, -1, v92\n" \
  "buffer_load_dwordx4 v[92:95], v59, s[36:39], s49 offen offset:3072 sc1\n" \
  "s_mov_b64 exec, -1\n" \
  "v_cmpx_eq_u32 vcc, -1, v96\n" \
  "buffer_load_dwordx4 v[96:99], v59, s[36:39], s50 offen offset:0 sc1\n" \
  "s_mov_b64 exec, -1\n" \
  "v_cmpx_eq_u32 vcc, -1, v100\n" \
  "buffer_load_dwordx4 v[100:103], v59, s[36:39], s50 offen offset:1024 sc1\n" \
  "s_mov_b64 exec, -1\n" \
  "v_cmpx_eq_u32 vcc, -1, v104\n" \
  "buffer_load_dwordx4 v[104:107], v59, s[36:39], s50 offen offset:2048 sc1\n" \
  "s_mov_b64 exec, -1\n" \
  "v_cmpx_eq_u32 vcc, -1, v108\n" \
  "buffer_load_dwordx4 v[108:111], v59, s[36:39], s50 offen offset:3072 sc1\n" \
  "s_mov_b64 exec, -1\n" \
  "v_cmpx_eq_u32 vcc, -1, v112\n" \
  "buffer_load_dwordx4 v[112:115], v59, s[36:39], s51 offen offset:0 sc1\n" \
  "s_mov_b64 exec, -1\n" \
  "v_cmpx_eq_u32 vcc, -1, v116\n" \
  "buffer_load_dwordx4 v[116:119], v59, s[36:39], s51 offen offset:1024 sc1\n" \
  "s_mov_b64 exec, -1\n" \
  "v_cmpx_eq_u32 vcc, -1, v120\n" \
  "buffer_load_dwordx4 v[120:123], v59, s[36:39], s51 offen offset:2048 sc1\n" \
  "s_mov_b64 exec, -1\n" \
  "v_cmpx_eq_u32 vcc, -1, v124\n" \
  "buffer_load_dwordx4 v[124:127], v59, s[36:39], s51 offen offset:3072 sc1\n" \
  "s_mov_b64 exec, -1\n" \
  "s_or_b64 s[100:101], s[100:101], 1\n" \
  ".Lj_%=:\n" \
  "s_cmp_gt_u32 s96, 0x4c4b40\n" \
  "s_cbranch_scc1 .Lg_%=\n" \
  "s_cmp_lg_u64 s[100:101], 0\n" \
  "s_cbranch_scc1 .Lh_%=\n" \
  ".Lg_%=:\n" \
  "s_sub_u32 s31, s60, 0x60080\n" \
  "s_sub_u32 s32, s60, 0x70000\n" \
  "s_mov_b32 m0, s56\n" \
  "s_mov_b32 s94, s86\n" \
  "s_and_b32 s95, s89, 7\n" \
  "s_andn2_b32 s101, s89, 7\n" \
  "s_mul_i32 s95, s95, 409600\n" \
  "s_add_u32 s95, s95, s52\n" \
  "s_mov_b32 s86, 0x11111111\n" \
  "s_mov_b32 s87, 0x11111111\n" \
  "v_add_u32 v59, s85, v59\n" \
  "s_add_u32 s35, s52, 1638400\n" \
  "s_mov_b32 s34, s91\n" \
  "s_waitcnt lgkmcnt(0)\n" \
  "s_cmp_eq_u32 s84, 0\n" \
  "s_cbranch_scc0 .Ls_%=\n" \
  ".Lf_%=:\n" \
  "s_waitcnt vmcnt(16)\n" \
  "v_mfma_f32_16x16x32_bf16 v[0:3], v[64:67], a[0:3], 0\n" \
  "ds_read_b32 v44, v60 offset:0\n" \
  "v_add_f32 v42, 1.0, v42\n" \
  "v_mfma_f32_16x16x32_bf16 v[4:7], v[64:67], a[4:7], 0\n" \
  "ds_read_b32 v45, v60 offset:9568\n" \
  "v_add_f32 v43, 1.0, v43\n" \
  "v_mfma_f32_16x16x32_bf16 v[8:11], v[64:67], a[8:11], 0\n" \
  "ds_read_b32 v46, v60 offset:19136\n" \
  "v_add_u32 v59, 0x10000, v59\n" \
  "v_mfma_f32_16x16x32_bf16 v[12:15], v[64:67], a[12:15], 0\n" \
  "ds_read_b32 v47, v60 offset:28704\n" \
  "v_and_b32 v59, 0x7ffff, v59\n" \
  "v_mfma_f32_16x16x32_bf16 v[16:19], v[64:67], a[16:19], 0\n" \
  "buffer_load_dwordx4 v[64:67], v59, s[36:39], s48 offen offset:0 sc1\n" \
  "s_waitcnt vmcnt(16)\n" \
  "v_mfma_f32_16x16x32_bf16 v[0:3], v[68:71], a[20:23], v[0:3]\n" \
  "v_rcp_f32 v40, v40\n" \
  "v_mfma_f32_16x16x32_bf16 v[4:7], v[68:71], a[24:27], v[4:7]\n" \
  "v_rcp_f32 v41, v41\n" \
  "v_mfma_f32_16x16x32_bf16 v[8:11], v[68:71], a[28:31], v[8:11]\n" \
  "v_rcp_f32 v42, v42\n" \
  "v_mfma_f32_16x16x32_bf16 v[12:15], v[68:71], a[32:35], v[12:15]\n" \
  "v_rcp_f32 v43, v43\n" \
  "v_mfma_f32_16x16x32_bf16 v[16:19], v[68:71], a[36:39], v[16:19]\n" \
  "buffer_load_dwordx4 v[68:71], v59, s[36:39], s48 offen offset:1024 sc1\n" \
  "s_waitcnt vmcnt(15)\n" \
  "v_mfma_f32_16x16x32_bf16 v[0:3], v[72:75], a[40:43], v[0:3]\n" \
  "v_mul_f32 v36, v36, v40\n" \
  "v_mul_f32 v37, v37, v41\n" \
  "v_mfma_f32_16x16x32_bf16 v[4:7], v[72:75], a[44:47], v[4:7]\n" \
  "v_mul_f32 v38, v38, v42\n" \
  "v_mul_f32 v39, v39, v43\n" \
  "v_mfma_f32_16x16x32_bf16 v[8:11], v[72:75], a[48:51], v[8:11]\n" \
  "v_cvt_pk_bf16_f32 v40, v36, v37\n" \
  "v_cvt_pk_bf16_f32 v41, v38, v39\n" \
  "v_mfma_f32_16x16x32_bf16 v[12:15], v[72:75], a[52:55], v[12:15]\n" \
  "buffer_store_dwordx2 v[40:41], v61, s[36:39], s52 offen offset:0 sc1\n" \
  "ds_write_addtid_b32 v20 offset:5344\n" \
  "v_mfma_f32_16x16x32_bf16 v[16:19], v[72:75], a[56:59], v[16:19]\n" \
  "buffer_load_dwordx4 v[72:75], v59, s[36:39], s48 offen offset:2048 sc1\n" \
  "s_waitcnt vmcnt(16)\n" \
  "v_mfma_f32_16x16x32_bf16 v[0:3], v[76:79], a[60:63], v[0:3]\n" \
  "ds_write_addtid_b32 v21 offset:5888\n" \
  "s_waitcnt lgkmcnt(2)\n" \
  "v_mfma_f32_16x16x32_bf16 v[4:7], v[76:79], a[64:67], v[4:7]\n" \
  "ds_write_addtid_b32 v22 offset:5600\n" \
  "v_add_f32 v44, v44, v45\n" \
  "v_mfma_f32_16x16x32_bf16 v[8:11], v[76:79], a[68:71], v[8:11]\n" \
  "ds_write_addtid_b32 v23 offset:6144\n" \
  "v_add_f32 v46, v46, v47\n" \
  "v_mfma_f32_16x16x32_bf16 v[12:15], v[76:79], a[72:75], v[12:15]\n" \
  "ds_write_addtid_b32 v24 offset:6400\n" \
  "v_add_f32 v44, v44, v56\n" \
  "v_mfma_f32_16x16x32_bf16 v[16:19], v[76:79], a[76:79], v[16:19]\n" \
  "buffer_load_dwordx4 v[76:79], v59, s[36:39], s48 offen offset:3072 sc1\n" \
  "s_waitcnt vmcnt(16)\n" \
  "v_mfma_f32_16x16x32_bf16 v[0:3], v[80:83], a[80:83], v[0:3]\n" \
  "ds_write_addtid_b32 v25 offset:6944\n" \
  "v_add_f32 v44, v44, v46\n" \
  "v_mfma_f32_16x16x32_bf16 v[4:7], v[80:83], a[84:87], v[4:7]\n" \
  "ds_write_addtid_b32 v26 offset:6656\n" \
  "v_mul_f32 v45, v44, v44\n" \
  "v_mfma_f32_16x16x32_bf16 v[8:11], v[80:83], a[88:91], v[8:11]\n" \
  "ds_write_addtid_b32 v27 offset:7200\n" \
  "v_fma_f32 v45, v45, s55, v57\n" \
  "v_mfma_f32_16x16x32_bf16 v[12:15], v[80:83], a[92:95], v[12:15]\n" \
  "ds_write_addtid_b32 v28 offset:7456\n" \
  "v_mul_f32 v45, v44, v45\n" \
  "v_mfma_f32_16x16x32_bf16 v[16:19], v[80:83], a[96:99], v[16:19]\n" \
  "buffer_load_dwordx4 v[80:83], v59, s[36:39], s49 offen offset:0 sc1\n" \
  "s_waitcnt vmcnt(16)\n" \
  "v_mfma_f32_16x16x32_bf16 v[0:3], v[84:87], a[100:103], v[0:3]\n" \
  "ds_write_addtid_b32 v29 offset:8000\n" \
  "v_mfma_f32_16x16x32_bf16 v[4:7], v[84:87], a[104:107], v[4:7]\n" \
  "ds_write_addtid_b32 v30 offset:7712\n" \
  "v_mfma_f32_16x16x32_bf16 v[8:11], v[84:87], a[108:111], v[8:11]\n" \
  "ds_write_addtid_b32 v31 offset:8256\n" \
  "v_mfma_f32_16x16x32_bf16 v[12:15], v[84:87], a[112:115], v[12:15]\n" \
  "ds_write_addtid_b32 v32 offset:8512\n" \
  "v_mfma_f32_16x16x32_bf16 v[16:19], v[84:87], a[116:119], v[16:19]\n" \
  "buffer_load_dwordx4 v[84:87], v59, s[36:39], s49 offen offset:1024 sc1\n" \
  "s_waitcnt vmcnt(16)\n" \
  "v_mfma_f32_16x16x32_bf16 v[0:3], v[88:91], a[120:123], v[0:3]\n" \
  "ds_write_addtid_b32 v33 offset:9056\n" \
  "v_mfma_f32_16x16x32_bf16 v[4:7], v[88:91], a[124:127], v[4:7]\n" \
  "ds_write_addtid_b32 v34 offset:8768\n" \
  "v_mfma_f32_16x16x32_bf16 v[8:11], v[88:91], a[128:131], v[8:11]\n" \
  "ds_write_addtid_b32 v35 offset:9312\n" \
  "v_mfma_f32_16x16x32_bf16 v[12:15], v[88:91], a[132:135], v[12:15]\n" \
  "v_exp_f32 v45, v45\n" \
  "v_mfma_f32_16x16x32_bf16 v[16:19], v[88:91], a[136:139], v[16:19]\n" \
  "buffer_load_dwordx4 v[88:91], v59, s[36:39], s49 offen offset:2048 sc1\n" \
  "s_waitcnt vmcnt(16)\n" \
  "v_mfma_f32_16x16x32_bf16 v[0:3], v[92:95], a[140:143], v[0:3]\n" \
  "v_add_f32 v45, 1.0, v45\n" \
  "v_mfma_f32_16x16x32_bf16 v[4:7], v[92:95], a[144:147], v[4:7]\n" \
  "v_rcp_f32 v45, v45\n" \
  "v_mfma_f32_16x16x32_bf16 v[8:11], v[92:95], a[148:151], v[8:11]\n" \
  "v_mul_f32 v44, v44, v45\n" \
  "v_cvt_pk_bf16_f32 v45, v44, v44\n" \
  "v_mfma_f32_16x16x32_bf16 v[12:15], v[92:95], a[152:155], v[12:15]\n" \
  "s_waitcnt lgkmcnt(0)\n" \
  "s_barrier\n" \
  "v_mfma_f32_16x16x32_bf16 v[16:19], v[92:95], a[156:159], v[16:19]\n" \
  "buffer_load_dwordx4 v[92:95], v59, s[36:39], s49 offen offset:3072 sc1\n" \
  "s_waitcnt vmcnt(16)\n" \
  "v_mfma_f32_16x16x32_bf16 v[0:3], v[96:99], a[160:163], v[0:3]\n" \
  "buffer_store_short v45, v62, s[36:39], s52 offen offset:0 sc1\n" \
  "ds_read_b128 v[36:39], v58 offset:5344\n" \
  "v_mfma_f32_16x16x32_bf16 v[4:7], v[96:99], a[164:167], v[4:7]\n" \
  "ds_read_b128 v[40:43], v58 offset:14912\n" \
  "v_mfma_f32_16x16x32_bf16 v[8:11], v[96:99], a[168:171], v[8:11]\n" \
  "ds_read_b128 v[44:47], v58 offset:24480\n" \
  "v_mfma_f32_16x16x32_bf16 v[12:15], v[96:99], a[172:175], v[12:15]\n" \
  "ds_read_b128 v[48:51], v58 offset:34048\n" \
  "v_mfma_f32_16x16x32_bf16 v[16:19], v[96:99], a[176:179], v[16:19]\n" \
  "buffer_load_dwordx4 v[96:99], v59, s[36:39], s50 offen offset:0 sc1\n" \
  "s_waitcnt vmcnt(17)\n" \
  "v_mfma_f32_16x16x32_bf16 v[0:3], v[100:103], a[180:183], v[0:3]\n" \
  "s_cmp_eq_u32 s52, s83\n" \
  "s_cbranch_scc1 .Lxq5_%=\n" \
  ".Lyq5_%=:\n" \
  "v_mfma_f32_16x16x32_bf16 v[4:7], v[100:103], a[184:187], v[4:7]\n" \
  "v_mfma_f32_16x16x32_bf16 v[8:11], v[100:103], a[188:191], v[8:11]\n" \
  "v_mfma_f32_16x16x32_bf16 v[12:15], v[100:103], a[192:195], v[12:15]\n" \
  "v_mfma_f32_16x16x32_bf16 v[16:19], v[100:103], a[196:199], v[16:19]\n" \
  "buffer_load_dwordx4 v[100:103], v59, s[36:39], s50 offen offset:1024 sc1\n" \
  "s_waitcnt vmcnt(17)\n" \
  "v_mfma_f32_16x16x32_bf16 v[0:3], v[104:107], a[200:203], v[0:3]\n" \
  "s_waitcnt lgkmcnt(2)\n" \
  "v_add_f32 v36, v36, v40\n" \
  "v_mfma_f32_16x16x32_bf16 v[4:7], v[104:107], a[204:207], v[4:7]\n" \
  "v_add_f32 v37, v37, v41\n" \
  "v_add_f32 v38, v38, v42\n" \
  "v_mfma_f32_16x16x32_bf16 v[8:11], v[104:107], a[208:211], v[8:11]\n" \
  "v_add_f32 v39, v39, v43\n" \
  "v_add_f32 v36, v36, v52\n" \
  "v_mfma_f32_16x16x32_bf16 v[12:15], v[104:107], a[212:215], v[12:15]\n" \
  "v_add_f32 v37, v37, v53\n" \
  "v_add_f32 v38, v38, v54\n" \
  "v_mfma_f32_16x16x32_bf16 v[16:19], v[104:107], a[216:219], v[16:19]\n" \
  "buffer_load_dwordx4 v[104:107], v59, s[36:39], s50 offen offset:2048 sc1\n" \
  "s_waitcnt vmcnt(17)\n" \
  "v_mfma_f32_16x16x32_bf16 v[0:3], v[108:111], a[220:223], v[0:3]\n" \
  "v_add_f32 v39, v39, v55\n" \
  "s_waitcnt lgkmcnt(0)\n" \
  "v_mfma_f32_16x16x32_bf16 v[4:7], v[108:111], a[224:227], v[4:7]\n" \
  "v_add_f32 v44, v44, v48\n" \
  "v_add_f32 v45, v45, v49\n" \
  "v_mfma_f32_16x16x32_bf16 v[8:11], v[108:111], a[228:231], v[8:11]\n" \
  "v_add_f32 v46, v46, v50\n" \
  "v_add_f32 v47, v47, v51\n" \
  "v_mfma_f32_16x16x32_bf16 v[12:15], v[108:111], a[232:235], v[12:15]\n" \
  "v_add_f32 v36, v36, v44\n" \
  "v_add_f32 v37, v37, v45\n" \
  "v_mfma_f32_16x16x32_bf16 v[16:19], v[108:111], a[236:239], v[16:19]\n" \
  "buffer_load_dwordx4 v[108:111], v59, s[36:39], s50 offen offset:3072 sc1\n" \
  "s_waitcnt vmcnt(17)\n" \
  "v_mfma_f32_16x16x32_bf16 v[0:3], v[112:115], a[240:243], v[0:3]\n" \
  "v_add_f32 v38, v38, v46\n" \
  "v_add_f32 v39, v39, v47\n" \
  "v_mfma_f32_16x16x32_bf16 v[4:7], v[112:115], a[244:247], v[4:7]\n" \
  "v_mul_f32 v40, v36, v36\n" \
  "v_mul_f32 v41, v37, v37\n" \
  "v_mfma_f32_16x16x32_bf16 v[8:11], v[112:115], a[248:251], v[8:11]\n" \
  "v_mul_f32 v42, v38, v38\n" \
  "v_mul_f32 v43, v39, v39\n" \
  "v_mfma_f32_16x16x32_bf16 v[12:15], v[112:115], a[252:255], v[12:15]\n" \
  "v_fma_f32 v40, v40, s55, v57\n" \
  "v_fma_f32 v41, v41, s55, v57\n" \
  "v_mfma_f32_16x16x32_bf16 v[16:19], v[112:115], v[192:195], v[16:19]\n" \
  "buffer_load_dwordx4 v[112:115], v59, s[36:39], s51 offen offset:0 sc1\n" \
  "s_waitcnt vmcnt(17)\n" \
  "v_mfma_f32_16x16x32_bf16 v[0:3], v[116:119], v[196:199], v[0:3]\n" \
  "v_fma_f32 v42, v42, s55, v57\n" \
  "v_fma_f32 v43, v43, s55, v57\n" \
  "v_mfma_f32_16x16x32_bf16 v[4:7], v[116:119], v[200:203], v[4:7]\n" \
  "v_mul_f32 v40, v36, v40\n" \
  "v_mul_f32 v41, v37, v41\n" \
  "v_mfma_f32_16x16x32_bf16 v[8:11], v[116:119], v[204:207], v[8:11]\n" \
  "v_mul_f32 v42, v38, v42\n" \
  "v_mul_f32 v43, v39, v43\n" \
  "v_mfma_f32_16x16x32_bf16 v[12:15], v[116:119], v[208:211], v[12:15]\n" \
  "v_exp_f32 v40, v40\n" \
  "v_mfma_f32_16x16x32_bf16 v[16:19], v[116:119], v[212:215], v[16:19]\n" \
  "buffer_load_dwordx4 v[116:119], v59, s[36:39], s51 offen offset:1024 sc1\n" \
  "s_waitcnt vmcnt(17)\n" \
  "v_mfma_f32_16x16x32_bf16 v[0:3], v[120:123], v[216:219], v[0:3]\n" \
  "v_exp_f32 v41, v41\n" \
  "v_mfma_f32_16x16x32_bf16 v[4:7], v[120:123], v[220:223], v[4:7]\n" \
  "v_exp_f32 v42, v42\n" \
  "v_mfma_f32_16x16x32_bf16 v[8:11], v[120:123], v[224:227], v[8:11]\n" \
  "v_exp_f32 v43, v43\n" \
  "v_mfma_f32_16x16x32_bf16 v[12:15], v[120:123], v[228:231], v[12:15]\n" \
  "v_add_f32 v40, 1.0, v40\n" \
  "v_add_f32 v41, 1.0, v41\n" \
  "v_mfma_f32_16x16x32_bf16 v[16:19], v[120:123], v[232:235], v[16:19]\n" \
  "buffer_load_dwordx4 v[120:123], v59, s[36:39], s51 offen offset:2048 sc1\n" \
  "s_waitcnt vmcnt(17)\n" \
  "v_mfma_f32_16x16x32_bf16 v[0:3], v[124:127], v[236:239], v[0:3]\n" \
  "v_add_f32 v42, 1.0, v42\n" \
  "v_add_f32 v43, 1.0, v43\n" \
  "v_mfma_f32_16x16x32_bf16 v[4:7], v[124:127], v[240:243], v[4:7]\n" \
  "s_cmp_eq_u32 s52, s95\n" \
  "s_cbranch_scc1 .Lxt5_%=\n" \
  ".Lyt5_%=:\n" \
  "v_mfma_f32_16x16x32_bf16 v[8:11], v[124:127], v[244:247], v[8:11]\n" \
  "v_rcp_f32 v40, v40\n" \
  "v_mfma_f32_16x16x32_bf16 v[12:15], v[124:127], v[248:251], v[12:15]\n" \
  "v_rcp_f32 v41, v41\n" \
  "v_mfma_f32_16x16x32_bf16 v[16:19], v[124:127], v[252:255], v[16:19]\n" \
  "buffer_load_dwordx4 v[124:127], v59, s[36:39], s51 offen offset:3072 sc1\n" \
  "s_add_u32 s52, s52, 409600\n" \
  "s_cmp_eq_u32 s52, s53\n" \
  "s_cbranch_scc1 .Lls_%=\n" \
  ".Ls_%=:\n" \
  "s_waitcnt vmcnt(17)\n" \
  "v_mfma_f32_16x16x32_bf16 v[20:23], v[64:67], a[0:3], 0\n" \
  "v_rcp_f32 v42, v42\n" \
  "v_mfma_f32_16x16x32_bf16 v[24:27], v[64:67], a[4:7], 0\n" \
  "v_add_u32 v59, 0x10000, v59\n" \
  "ds_write_addtid_b32 v0 offset:0\n" \
  "v_mfma_f32_16x16x32_bf16 v[28:31], v[64:67], a[8:11], 0\n" \
  "v_and_b32 v59, 0x7ffff, v59\n" \
  "ds_write_addtid_b32 v1 offset:544\n" \
  "v_mfma_f32_16x16x32_bf16 v[32:35], v[64:67], a[12:15], 0\n" \
  "buffer_load_dwordx4 v[64:67], v59, s[36:39], s48 offen offset:0 sc1\n" \
  "s_waitcnt vmcnt(17)\n" \
  "v_mfma_f32_16x16x32_bf16 v[20:23], v[68:71], a[20:23], v[20:23]\n" \
  "v_rcp_f32 v43, v43\n" \
  "v_mfma_f32_16x16x32_bf16 v[24:27], v[68:71], a[24:27], v[24:27]\n" \
  "v_mul_f32 v36, v36, v40\n" \
  "v_mul_f32 v37, v37, v41\n" \
  "v_mfma_f32_16x16x32_bf16 v[28:31], v[68:71], a[28:31], v[28:31]\n" \
  "v_mul_f32 v38, v38, v42\n" \
  "v_mul_f32 v39, v39, v43\n" \
  "v_mfma_f32_16x16x32_bf16 v[32:35], v[68:71], a[32:35], v[32:35]\n" \
  "buffer_load_dwordx4 v[68:71], v59, s[36:39], s48 offen offset:1024 sc1\n" \
  "s_waitcnt vmcnt(16)\n" \
  "v_mfma_f32_16x16x32_bf16 v[20:23], v[72:75], a[40:43], v[20:23]\n" \
  "ds_write_addtid_b32 v2 offset:256\n" \
  "v_cvt_pk_bf16_f32 v40, v36, v37\n" \
  "v_mfma_f32_16x16x32_bf16 v[24:27], v[72:75], a[44:47], v[24:27]\n" \
  "ds_write_addtid_b32 v3 offset:800\n" \
  "v_cvt_pk_bf16_f32 v41, v38, v39\n" \
  "v_mfma_f32_16x16x32_bf16 v[28:31], v[72:75], a[48:51], v[28:31]\n" \
  "ds_write_addtid_b32 v4 offset:1056\n" \
  "buffer_store_dwordx2 v[40:41], v61, s[36:39], s52 offen offset:0 sc1\n" \
  "v_mfma_f32_16x16x32_bf16 v[32:35], v[72:75], a[52:55], v[32:35]\n" \
  "buffer_load_dwordx4 v[72:75], v59, s[36:39], s48 offen offset:2048 sc1\n" \
  "s_waitcnt vmcnt(17)\n" \
  "v_mfma_f32_16x16x32_bf16 v[20:23], v[76:79], a[60:63], v[20:23]\n" \
  "ds_write_addtid_b32 v5 offset:1600\n" \
  "v_mfma_f32_16x16x32_bf16 v[24:27], v[76:79], a[64:67], v[24:27]\n" \
  "ds_write_addtid_b32 v6 offset:1312\n" \
  "v_mfma_f32_16x16x32_bf16 v[28:31], v[76:79], a[68:71], v[28:31]\n" \
  "ds_write_addtid_b32 v7 offset:1856\n" \
  "v_mfma_f32_16x16x32_bf16 v[32:35], v[76:79], a[72:75], v[32:35]\n" \
  "buffer_load_dwordx4 v[76:79], v59, s[36:39], s48 offen offset:3072 sc1\n" \
  "s_waitcnt vmcnt(17)\n" \
  "v_mfma_f32_16x16x32_bf16 v[20:23], v[80:83], a[80:83], v[20:23]\n" \
  "ds_write_addtid_b32 v8 offset:2112\n" \
  "v_mfma_f32_16x16x32_bf16 v[24:27], v[80:83], a[84:87], v[24:27]\n" \
  "ds_write_addtid_b32 v9 offset:2656\n" \
  "v_mfma_f32_16x16x32_bf16 v[28:31], v[80:83], a[88:91], v[28:31]\n" \
  "ds_write_addtid_b32 v10 offset:2368\n" \
  "v_mfma_f32_16x16x32_bf16 v[32:35], v[80:83], a[92:95], v[32:35]\n" \
  "buffer_load_dwordx4 v[80:83], v59, s[36:39], s49 offen offset:0 sc1\n" \
  "s_waitcnt vmcnt(17)\n" \
  "v_mfma_f32_16x16x32_bf16 v[20:23], v[84:87], a[100:103], v[20:23]\n" \
  "ds_write_addtid_b32 v11 offset:2912\n" \
  "v_mfma_f32_16x16x32_bf16 v[24:27], v[84:87], a[104:107], v[24:27]\n" \
  "ds_write_addtid_b32 v12 offset:3168\n" \
  "v_mfma_f32_16x16x32_bf16 v[28:31], v[84:87], a[108:111], v[28:31]\n" \
  "ds_write_addtid_b32 v13 offset:3712\n" \
  "v_mfma_f32_16x16x32_bf16 v[32:35], v[84:87], a[112:115], v[32:35]\n" \
  "buffer_load_dwordx4 v[84:87], v59, s[36:39], s49 offen offset:1024 sc1\n" \
  "s_waitcnt vmcnt(17)\n" \
  "v_mfma_f32_16x16x32_bf16 v[20:23], v[88:91], a[120:123], v[20:23]\n" \
  "ds_write_addtid_b32 v14 offset:3424\n" \
  "v_mfma_f32_16x16x32_bf16 v[24:27], v[88:91], a[124:127], v[24:27]\n" \
  "ds_write_addtid_b32 v15 offset:3968\n" \
  "v_mfma_f32_16x16x32_bf16 v[28:31], v[88:91], a[128:131], v[28:31]\n" \
  "ds_write_addtid_b32 v16 offset:4224\n" \
  "v_mfma_f32_16x16x32_bf16 v[32:35], v[88:91], a[132:135], v[32:35]\n" \
  "buffer_load_dwordx4 v[88:91], v59, s[36:39], s49 offen offset:2048 sc1\n" \
  "s_waitcnt vmcnt(17)\n" \
  "v_mfma_f32_16x16x32_bf16 v[20:23], v[92:95], a[140:143], v[20:23]\n" \
  "ds_write_addtid_b32 v17 offset:4512\n" \
  "v_mfma_f32_16x16x32_bf16 v[24:27], v[92:95], a[144:147], v[24:27]\n" \
  "ds_write_addtid_b32 v18 offset:4800\n" \
  "v_mfma_f32_16x16x32_bf16 v[28:31], v[92:95], a[148:151], v[28:31]\n" \
  "ds_write_addtid_b32 v19 offset:5088\n" \
  "v_mfma_f32_16x16x32_bf16 v[32:35], v[92:95], a[152:155], v[32:35]\n" \
  "buffer_load_dwordx4 v[92:95], v59, s[36:39], s49 offen offset:3072 sc1\n" \
  "s_waitcnt vmcnt(16)\n" \
  "v_mfma_f32_16x16x32_bf16 v[20:23], v[96:99], a[160:163], v[20:23]\n" \
  "v_mfma_f32_16x16x32_bf16 v[24:27], v[96:99], a[164:167], v[24:27]\n" \
  "v_mfma_f32_16x16x32_bf16 v[28:31], v[96:99], a[168:171], v[28:31]\n" \
  "v_mfma_f32_16x16x32_bf16 v[32:35], v[96:99], a[172:175], v[32:35]\n" \
  "buffer_load_dwordx4 v[96:99], v59, s[36:39], s50 offen offset:0 sc1\n" \
  "s_waitcnt vmcnt(16)\n" \
  "v_mfma_f32_16x16x32_bf16 v[20:23], v[100:103], a[180:183], v[20:23]\n" \
  "s_waitcnt lgkmcnt(0)\n" \
  "s_barrier\n" \
  "v_mfma_f32_16x16x32_bf16 v[24:27], v[100:103], a[184:187], v[24:27]\n" \
  "ds_read_b128 v[36:39], v58 offset:0\n" \
  "v_mfma_f32_16x16x32_bf16 v[28:31], v[100:103], a[188:191], v[28:31]\n" \
  "ds_read_b128 v[40:43], v58 offset:9568\n" \
  "v_mfma_f32_16x16x32_bf16 v[32:35], v[100:103], a[192:195], v[32:35]\n" \
  "buffer_load_dwordx4 v[100:103], v59, s[36:39], s50 offen offset:1024 sc1\n" \
  "s_waitcnt vmcnt(16)\n" \
  "v_mfma_f32_16x16x32_bf16 v[20:23], v[104:107], a[200:203], v[20:23]\n" \
  "ds_read_b128 v[44:47], v58 offset:19136\n" \
  "v_mfma_f32_16x16x32_bf16 v[24:27], v[104:107], a[204:207], v[24:27]\n" \
  "ds_read_b128 v[48:51], v58 offset:28704\n" \
  "v_mfma_f32_16x16x32_bf16 v[28:31], v[104:107], a[208:211], v[28:31]\n" \
  "s_cmp_eq_u32 s52, s83\n" \
  "s_cbranch_scc1 .Lxq4_%=\n" \
  ".Lyq4_%=:\n" \
  "v_mfma_f32_16x16x32_bf16 v[32:35], v[104:107], a[212:215], v[32:35]\n" \
  "buffer_load_dwordx4 v[104:107], v59, s[36:39], s50 offen offset:2048 sc1\n" \
  "s_waitcnt vmcnt(16)\n" \
  "v_mfma_f32_16x16x32_bf16 v[20:23], v[108:111], a[220:223], v[20:23]\n" \
  "v_mfma_f32_16x16x32_bf16 v[24:27], v[108:111], a[224:227], v[24:27]\n" \
  "v_mfma_f32_16x16x32_bf16 v[28:31], v[108:111], a[228:231], v[28:31]\n" \
  "s_waitcnt lgkmcnt(2)\n" \
  "v_add_f32 v36, v36, v40\n" \
  "v_add_f32 v37, v37, v41\n" \
  "v_mfma_f32_16x16x32_bf16 v[32:35], v[108:111], a[232:235], v[32:35]\n" \
  "buffer_load_dwordx4 v[108:111], v59, s[36:39], s50 offen offset:3072 sc1\n" \
  "v_add_f32 v38, v38, v42\n" \
  "s_waitcnt vmcnt(16)\n" \
  "v_mfma_f32_16x16x32_bf16 v[20:23], v[112:115], a[240:243], v[20:23]\n" \
  "v_add_f32 v39, v39, v43\n" \
  "v_add_f32 v36, v36, v52\n" \
  "v_add_f32 v37, v37, v53\n" \
  "v_mfma_f32_16x16x32_bf16 v[24:27], v[112:115], a[244:247], v[24:27]\n" \
  "v_add_f32 v38, v38, v54\n" \
  "v_add_f32 v39, v39, v55\n" \
  "s_waitcnt lgkmcnt(0)\n" \
  "v_mfma_f32_16x16x32_bf16 v[28:31], v[112:115], a[248:251], v[28:31]\n" \
  "v_add_f32 v44, v44, v48\n" \
  "v_add_f32 v45, v45, v49\n" \
  "v_add_f32 v46, v46, v50\n" \
  "v_mfma_f32_16x16x32_bf16 v[32:35], v[112:115], a[252:255], v[32:35]\n" \
  "buffer_load_dwordx4 v[112:115], v59, s[36:39], s51 offen offset:0 sc1\n" \
  "v_add_f32 v47, v47, v51\n" \
  "s_waitcnt vmcnt(16)\n" \
  "v_mfma_f32_16x16x32_bf16 v[20:23], v[116:119], v[196:199], v[20:23]\n" \
  "v_add_f32 v36, v36, v44\n" \
  "v_add_f32 v37, v37, v45\n" \
  "v_add_f32 v38, v38, v46\n" \
  "v_mfma_f32_16x16x32_bf16 v[24:27], v[116:119], v[200:203], v[24:27]\n" \
  "v_add_f32 v39, v39, v47\n" \
  "v_mul_f32 v40, v36, v36\n" \
  "v_mul_f32 v41, v37, v37\n" \
  "v_mfma_f32_16x16x32_bf16 v[28:31], v[116:119], v[204:207], v[28:31]\n" \
  "v_mul_f32 v42, v38, v38\n" \
  "v_mul_f32 v43, v39, v39\n" \
  "v_fma_f32 v40, v40, s55, v57\n" \
  "v_mfma_f32_16x16x32_bf16 v[32:35], v[116:119], v[208:211], v[32:35]\n" \
  "buffer_load_dwordx4 v[116:119], v59, s[36:39], s51 offen offset:1024 sc1\n" \
  "v_fma_f32 v41, v41, s55, v57\n" \
  "s_waitcnt vmcnt(16)\n" \
  "v_mfma_f32_16x16x32_bf16 v[20:23], v[120:123], v[216:219], v[20:23]\n" \
  "v_fma_f32 v42, v42, s55, v57\n" \
  "v_fma_f32 v43, v43, s55, v57\n" \
  "v_mul_f32 v40, v36, v40\n" \
  "v_mfma_f32_16x16x32_bf16 v[24:27], v[120:123], v[220:223], v[24:27]\n" \
  "v_mul_f32 v41, v37, v41\n" \
  "v_mul_f32 v42, v38, v42\n" \
  "v_mfma_f32_16x16x32_bf16 v[28:31], v[120:123], v[224:227], v[28:31]\n" \
  "v_mul_f32 v43, v39, v43\n" \
  "v_mfma_f32_16x16x32_bf16 v[32:35], v[120:123], v[228:231], v[32:35]\n" \
  "buffer_load_dwordx4 v[120:123], v59, s[36:39], s51 offen offset:2048 sc1\n" \
  "s_waitcnt vmcnt(16)\n" \
  "v_mfma_f32_16x16x32_bf16 v[20:23], v[124:127], v[236:239], v[20:23]\n" \
  "s_cmp_eq_u32 s52, s95\n" \
  "s_cbranch_scc1 .Lxt4_%=\n" \
  ".Lyt4_%=:\n" \
  "v_mfma_f32_16x16x32_bf16 v[24:27], v[124:127], v[240:243], v[24:27]\n" \
  "v_exp_f32 v40, v40\n" \
  "v_mfma_f32_16x16x32_bf16 v[28:31], v[124:127], v[244:247], v[28:31]\n" \
  "v_exp_f32 v41, v41\n" \
  "v_mfma_f32_16x16x32_bf16 v[32:35], v[124:127], v[248:251], v[32:35]\n" \
  "buffer_load_dwordx4 v[124:127], v59, s[36:39], s51 offen offset:3072 sc1\n" \
  "v_exp_f32 v42, v42\n" \
  "v_exp_f32 v43, v43\n" \
  "v_add_f32 v40, 1.0, v40\n" \
  "s_add_u32 s52, s52, 409600\n" \
  "s_cmp_eq_u32 s52, s53\n" \
  "v_add_f32 v41, 1.0, v41\n" \
  "s_cbranch_scc0 .Lf_%=\n" \
  "v_mfma_f32_16x16x32_bf16 v[0:3], v[64:67], a[0:3], 0\n" \
  "ds_read_b32 v44, v60 offset:0\n" \
  "v_and_b32 v59, 0x3ff, v59\n" \
  "s_add_u32 s48, s68, 409600\n" \
  "v_mfma_f32_16x16x32_bf16 v[4:7], v[64:67], a[4:7], 0\n" \
  "ds_read_b32 v45, v60 offset:9568\n" \
  "s_add_u32 s49, s69, 409600\n" \
  "s_add_u32 s50, s70, 409600\n" \
  "v_mfma_f32_16x16x32_bf16 v[8:11], v[64:67], a[8:11], 0\n" \
  "ds_read_b32 v46, v60 offset:19136\n" \
  "s_add_u32 s100, s90, 73728\n" \
  "s_mov_b32 s35, 73728\n" \
  "v_mfma_f32_16x16x32_bf16 v[12:15], v[64:67], a[12:15], 0\n" \
  "ds_read_b32 v47, v60 offset:28704\n" \
  "ds_write_addtid_b32 v20 offset:5344\n" \
  "v_add_f32 v42, 1.0, v42\n" \
  "v_mfma_f32_16x16x32_bf16 v[16:19], v[64:67], a[16:19], 0\n" \
  "ds_write_addtid_b32 v21 offset:5888\n" \
  "ds_write_addtid_b32 v22 offset:5600\n" \
  "v_mfma_f32_16x16x32_bf16 v[0:3], v[68:71], a[20:23], v[0:3]\n" \
  "ds_write_addtid_b32 v23 offset:6144\n" \
  "ds_write_addtid_b32 v24 offset:6400\n" \
  "v_add_f32 v43, 1.0, v43\n" \
  "v_mfma_f32_16x16x32_bf16 v[4:7], v[68:71], a[24:27], v[4:7]\n" \
  "ds_write_addtid_b32 v25 offset:6944\n" \
  "ds_write_addtid_b32 v26 offset:6656\n" \
  "v_mfma_f32_16x16x32_bf16 v[8:11], v[68:71], a[28:31], v[8:11]\n" \
  "ds_write_addtid_b32 v27 offset:7200\n" \
  "ds_write_addtid_b32 v28 offset:7456\n" \
  "v_mfma_f32_16x16x32_bf16 v[12:15], v[68:71], a[32:35], v[12:15]\n" \
  "ds_write_addtid_b32 v29 offset:8000\n" \
  "ds_write_addtid_b32 v30 offset:7712\n" \
  "v_mfma_f32_16x16x32_bf16 v[16:19], v[68:71], a[36:39], v[16:19]\n" \
  "ds_write_addtid_b32 v31 offset:8256\n" \
  "ds_write_addtid_b32 v32 offset:8512\n" \
  "s_waitcnt vmcnt(15)\n" \
  "v_mfma_f32_16x16x32_bf16 v[0:3], v[72:75], a[40:43], v[0:3]\n" \
  "s_waitcnt vmcnt(6)\n" \
  "v_cmp_eq_u32 vcc, 0, v63\n" \
  "v_add_u32 v71, s57, v59\n" \
  "v_mfma_f32_16x16x32_bf16 v[4:7], v[72:75], a[44:47], v[4:7]\n" \
  "ds_read_b32 v64, v71 offset:28672\n" \
  "ds_read_b64 v[68:69], v71 offset:29696\n" \
  "s_waitcnt lgkmcnt(15)\n" \
  "v_mfma_f32_16x16x32_bf16 v[8:11], v[72:75], a[48:51], v[8:11]\n" \
  "s_and_b64 vcc, vcc, s[86:87]\n" \
  "s_cbranch_scc1 .LsaF_%=\n" \
  ".LbaF_%=:\n" \
  "ds_write_addtid_b32 v33 offset:9056\n" \
  "v_mfma_f32_16x16x32_bf16 v[12:15], v[72:75], a[52:55], v[12:15]\n" \
  "ds_write_addtid_b32 v34 offset:8768\n" \
  "ds_write_addtid_b32 v35 offset:9312\n" \
  "v_add_f32 v44, v44, v45\n" \
  "v_mfma_f32_16x16x32_bf16 v[16:19], v[72:75], a[56:59], v[16:19]\n" \
  "v_rcp_f32 v40, v40\n" \
  "s_waitcnt vmcnt(14)\n" \
  "v_mfma_f32_16x16x32_bf16 v[0:3], v[76:79], a[60:63], v[0:3]\n" \
  "buffer_load_dwordx4 v[72:75], v59, s[36:39], s68 offen offset:0 sc1\n" \
  "s_waitcnt lgkmcnt(3)\n" \
  "v_xor_b32 v65, 32, v64\n" \
  "v_mfma_f32_16x16x32_bf16 v[4:7], v[76:79], a[64:67], v[4:7]\n" \
  "v_xor_b32 v66, 64, v64\n" \
  "v_xor_b32 v67, 96, v64\n" \
  "v_add_u32 v70, 0xf800, v68\n" \
  "v_mfma_f32_16x16x32_bf16 v[8:11], v[76:79], a[68:71], v[8:11]\n" \
  "v_add_u32 v71, 0xf800, v69\n" \
  "ds_read_b64_tr_b16 a[0:1], v64 offset:0\n" \
  "ds_read_b64_tr_b16 a[2:3], v64 offset:512\n" \
  "v_mfma_f32_16x16x32_bf16 v[12:15], v[76:79], a[72:75], v[12:15]\n" \
  "ds_read_b64_tr_b16 a[4:5], v65 offset:0\n" \
  "ds_read_b64_tr_b16 a[6:7], v65 offset:512\n" \
  "v_add_f32 v46, v46, v47\n" \
  "v_mfma_f32_16x16x32_bf16 v[16:19], v[76:79], a[76:79], v[16:19]\n" \
  "s_waitcnt lgkmcnt(0)\n" \
  "s_barrier\n" \
  "v_mfma_f32_16x16x32_bf16 v[0:3], v[80:83], a[80:83], v[0:3]\n" \
  "buffer_load_dwordx4 v[76:79], v59, s[36:39], s68 offen offset:1024 sc1\n" \
  "ds_read_b64_tr_b16 a[8:9], v66 offset:0\n" \
  "ds_read_b64_tr_b16 a[10:11], v66 offset:512\n" \
  "v_mfma_f32_16x16x32_bf16 v[4:7], v[80:83], a[84:87], v[4:7]\n" \
  "ds_read_b64_tr_b16 a[12:13], v67 offset:0\n" \
  "ds_read_b64_tr_b16 a[14:15], v67 offset:512\n" \
  "v_add_f32 v44, v44, v56\n" \
  "v_mfma_f32_16x16x32_bf16 v[8:11], v[80:83], a[88:91], v[8:11]\n" \
  "v_rcp_f32 v41, v41\n" \
  "v_add_f32 v44, v44, v46\n" \
  "v_mfma_f32_16x16x32_bf16 v[12:15], v[80:83], a[92:95], v[12:15]\n" \
  "v_rcp_f32 v42, v42\n" \
  "v_mul_f32 v45, v44, v44\n" \
  "v_mfma_f32_16x16x32_bf16 v[16:19], v[80:83], a[96:99], v[16:19]\n" \
  "ds_read_b64_tr_b16 a[16:17], v64 offset:4096\n" \
  "ds_read_b64_tr_b16 a[18:19], v64 offset:4608\n" \
  "v_mfma_f32_16x16x32_bf16 v[0:3], v[84:87], a[100:103], v[0:3]\n" \
  "buffer_load_dwordx4 v[80:83], v59, s[36:39], s68 offen offset:2048 sc1\n" \
  "s_waitcnt lgkmcnt(2)\n" \
  "s_add_u32 m0, s57, 0\n" \
  "v_mfma_f32_16x16x32_bf16 v[4:7], v[84:87], a[104:107], v[4:7]\n" \
  "ds_read_b64_tr_b16 a[20:21], v65 offset:4096\n" \
  "ds_read_b64_tr_b16 a[22:23], v65 offset:4608\n" \
  "v_fma_f32 v45, v45, s55, v57\n" \
  "v_mfma_f32_16x16x32_bf16 v[8:11], v[84:87], a[108:111], v[8:11]\n" \
  "ds_read_b64_tr_b16 a[24:25], v66 offset:4096\n" \
  "ds_read_b64_tr_b16 a[26:27], v66 offset:4608\n" \
  "v_mul_f32 v45, v44, v45\n" \
  "v_mfma_f32_16x16x32_bf16 v[12:15], v[84:87], a[112:115], v[12:15]\n" \
  "buffer_load_dwordx4 v68, s[40:43], s60 offen offset:0 lds\n" \
  "ds_read_b64_tr_b16 a[28:29], v67 offset:4096\n" \
  "ds_read_b64_tr_b16 a[30:31], v67 offset:4608\n" \
  "v_mfma_f32_16x16x32_bf16 v[16:19], v[84:87], a[116:119], v[16:19]\n" \
  "v_rcp_f32 v43, v43\n" \
  "v_mfma_f32_16x16x32_bf16 v[0:3], v[88:91], a[120:123], v[0:3]\n" \
  "v_exp_f32 v45, v45\n" \
  "v_mul_f32 v36, v36, v40\n" \
  "v_mfma_f32_16x16x32_bf16 v[4:7], v[88:91], a[124:127], v[4:7]\n" \
  "buffer_load_dwordx4 v69, s[40:43], s60 offen offset:1024 lds\n" \
  "v_mul_f32 v37, v37, v41\n" \
  "v_add_f32 v45, 1.0, v45\n" \
  "v_mfma_f32_16x16x32_bf16 v[8:11], v[88:91], a[128:131], v[8:11]\n" \
  "v_mul_f32 v38, v38, v42\n" \
  "v_rcp_f32 v45, v45\n" \
  "v_mfma_f32_16x16x32_bf16 v[12:15], v[88:91], a[132:135], v[12:15]\n" \
  "v_mul_f32 v39, v39, v43\n" \
  "v_cvt_pk_bf16_f32 v40, v36, v37\n" \
  "v_mul_f32 v44, v44, v45\n" \
  "v_mfma_f32_16x16x32_bf16 v[16:19], v[88:91], a[136:139], v[16:19]\n" \
  "buffer_load_dwordx4 v70, s[40:43], s60 offen offset:2048 lds\n" \
  "ds_read_b64_tr_b16 a[32:33], v64 offset:8192\n" \
  "v_mfma_f32_16x16x32_bf16 v[0:3], v[92:95], a[140:143], v[0:3]\n" \
  "ds_read_b64_tr_b16 a[34:35], v64 offset:8704\n" \
  "ds_read_b64_tr_b16 a[36:37], v65 offset:8192\n" \
  "v_cvt_pk_bf16_f32 v41, v38, v39\n" \
  "v_mfma_f32_16x16x32_bf16 v[4:7], v[92:95], a[144:147], v[4:7]\n" \
  "ds_read_b64_tr_b16 a[38:39], v65 offset:8704\n" \
  "ds_read_b64_tr_b16 a[40:41], v66 offset:8192\n" \
  "v_cvt_pk_bf16_f32 v45, v44, v44\n" \
  "v_mfma_f32_16x16x32_bf16 v[8:11], v[92:95], a[148:151], v[8:11]\n" \
  "buffer_load_dwordx4 v71, s[40:43], s60 offen offset:3072 lds\n" \
  "ds_read_b64_tr_b16 a[42:43], v66 offset:8704\n" \
  "ds_read_b64_tr_b16 a[44:45], v67 offset:8192\n" \
  "v_mfma_f32_16x16x32_bf16 v[12:15], v[92:95], a[152:155], v[12:15]\n" \
  "ds_read_b64_tr_b16 a[46:47], v67 offset:8704\n" \
  "buffer_store_dwordx2 v[40:41], v61, s[36:39], s52 offen offset:0 sc1\n" \
  "buffer_store_short v45, v62, s[36:39], s52 offen offset:0 sc1\n" \
  "v_mfma_f32_16x16x32_bf16 v[16:19], v[92:95], a[156:159], v[16:19]\n" \
  "ds_read_b128 v[36:39], v58 offset:5344\n" \
  "ds_read_b128 v[40:43], v58 offset:14912\n" \
  "s_waitcnt vmcnt(18)\n" \
  "v_mfma_f32_16x16x32_bf16 v[0:3], v[96:99], a[160:163], v[0:3]\n" \
  "buffer_load_dwordx4 v[84:87], v59, s[36:39], s69 offen offset:0 sc1\n" \
  "ds_read_b128 v[44:47], v58 offset:24480\n" \
  "ds_read_b128 v[48:51], v58 offset:34048\n" \
  "v_mfma_f32_16x16x32_bf16 v[4:7], v[96:99], a[164:167], v[4:7]\n" \
  "s_add_u32 s52, s52, 409600\n" \
  "v_mfma_f32_16x16x32_bf16 v[8:11], v[96:99], a[168:171], v[8:11]\n" \
  "v_mfma_f32_16x16x32_bf16 v[12:15], v[96:99], a[172:175], v[12:15]\n" \
  "buffer_load_dwordx4 v[88:91], v59, s[36:39], s69 offen offset:1024 sc1\n" \
  "v_mfma_f32_16x16x32_bf16 v[16:19], v[96:99], a[176:179], v[16:19]\n" \
  "ds_read_b64_tr_b16 a[48:49], v64 offset:12288\n" \
  "ds_read_b64_tr_b16 a[50:51], v64 offset:12800\n" \
  "s_waitcnt vmcnt(19)\n" \
  "v_mfma_f32_16x16x32_bf16 v[0:3], v[100:103], a[180:183], v[0:3]\n" \
  "ds_read_b64_tr_b16 a[52:53], v65 offset:12288\n" \
  "ds_read_b64_tr_b16 a[54:55], v65 offset:12800\n" \
  "v_mfma_f32_16x16x32_bf16 v[4:7], v[100:103], a[184:187], v[4:7]\n" \
  "buffer_load_dwordx4 v[92:95], v59, s[36:39], s69 offen offset:2048 sc1\n" \
  "s_waitcnt lgkmcnt(15)\n" \
  "s_add_u32 m0, s57, 4096\n" \
  "v_mfma_f32_16x16x32_bf16 v[8:11], v[100:103], a[188:191], v[8:11]\n" \
  "ds_read_b64_tr_b16 a[56:57], v66 offset:12288\n" \
  "ds_read_b64_tr_b16 a[58:59], v66 offset:12800\n" \
  "s_waitcnt lgkmcnt(8)\n" \
  "v_mfma_f32_16x16x32_bf16 v[12:15], v[100:103], a[192:195], v[12:15]\n" \
  "ds_read_b64_tr_b16 a[60:61], v67 offset:12288\n" \
  "ds_read_b64_tr_b16 a[62:63], v67 offset:12800\n" \
  "v_add_f32 v36, v36, v40\n" \
  "v_mfma_f32_16x16x32_bf16 v[16:19], v[100:103], a[196:199], v[16:19]\n" \
  "buffer_load_dwordx4 v68, s[40:43], s61 offen offset:0 lds\n" \
  "v_add_f32 v37, v37, v41\n" \
  "s_waitcnt vmcnt(18)\n" \
  "v_mfma_f32_16x16x32_bf16 v[0:3], v[104:107], a[200:203], v[0:3]\n" \
  "v_add_f32 v38, v38, v42\n" \
  "v_add_f32 v39, v39, v43\n" \
  "v_add_f32 v36, v36, v52\n" \
  "v_mfma_f32_16x16x32_bf16 v[4:7], v[104:107], a[204:207], v[4:7]\n" \
  "v_add_f32 v37, v37, v53\n" \
  "v_add_f32 v38, v38, v54\n" \
  "v_add_f32 v39, v39, v55\n" \
  "v_mfma_f32_16x16x32_bf16 v[8:11], v[104:107], a[208:211], v[8:11]\n" \
  "buffer_load_dwordx4 v69, s[40:43], s61 offen offset:1024 lds\n" \
  "s_waitcnt lgkmcnt(8)\n" \
  "v_add_f32 v44, v44, v48\n" \
  "v_mfma_f32_16x16x32_bf16 v[12:15], v[104:107], a[212:215], v[12:15]\n" \
  "v_add_f32 v45, v45, v49\n" \
  "v_add_f32 v46, v46, v50\n" \
  "v_add_f32 v47, v47, v51\n" \
  "v_mfma_f32_16x16x32_bf16 v[16:19], v[104:107], a[216:219], v[16:19]\n" \
  "ds_read_b64_tr_b16 a[64:65], v64 offset:16384\n" \
  "ds_read_b64_tr_b16 a[66:67], v64 offset:16896\n" \
  "s_waitcnt vmcnt(18)\n" \
  "v_mfma_f32_16x16x32_bf16 v[0:3], v[108:111], a[220:223], v[0:3]\n" \
  "buffer_load_dwordx4 v70, s[40:43], s61 offen offset:2048 lds\n" \
  "ds_read_b64_tr_b16 a[68:69], v65 offset:16384\n" \
  "ds_read_b64_tr_b16 a[70:71], v65 offset:16896\n" \
  "v_mfma_f32_16x16x32_bf16 v[4:7], v[108:111], a[224:227], v[4:7]\n" \
  "ds_read_b64_tr_b16 a[72:73], v66 offset:16384\n" \
  "ds_read_b64_tr_b16 a[74:75], v66 offset:16896\n" \
  "v_add_f32 v36, v36, v44\n" \
  "v_mfma_f32_16x16x32_bf16 v[8:11], v[108:111], a[228:231], v[8:11]\n" \
  "ds_read_b64_tr_b16 a[76:77], v67 offset:16384\n" \
  "ds_read_b64_tr_b16 a[78:79], v67 offset:16896\n" \
  "v_add_f32 v37, v37, v45\n" \
  "v_mfma_f32_16x16x32_bf16 v[12:15], v[108:111], a[232:235], v[12:15]\n" \
  "buffer_load_dwordx4 v71, s[40:43], s61 offen offset:3072 lds\n" \
  "v_add_f32 v38, v38, v46\n" \
  "v_add_f32 v39, v39, v47\n" \
  "v_mfma_f32_16x16x32_bf16 v[16:19], v[108:111], a[236:239], v[16:19]\n" \
  "v_mul_f32 v40, v36, v36\n" \
  "v_mul_f32 v41, v37, v37\n" \
  "s_waitcnt vmcnt(19)\n" \
  "v_mfma_f32_16x16x32_bf16 v[0:3], v[112:115], a[240:243], v[0:3]\n" \
  "v_mul_f32 v42, v38, v38\n" \
  "v_mul_f32 v43, v39, v39\n" \
  "v_fma_f32 v40, v40, s55, v57\n" \
  "v_mfma_f32_16x16x32_bf16 v[4:7], v[112:115], a[244:247], v[4:7]\n" \
  "buffer_load_dwordx4 v[108:111], v59, s[36:39], s48 offen offset:0 sc1\n" \
  "s_add_u32 m0, s57, 8192\n" \
  "v_mfma_f32_16x16x32_bf16 v[8:11], v[112:115], a[248:251], v[8:11]\n" \
  "v_fma_f32 v41, v41, s55, v57\n" \
  "v_fma_f32 v42, v42, s55, v57\n" \
  "v_fma_f32 v43, v43, s55, v57\n" \
  "v_mfma_f32_16x16x32_bf16 v[12:15], v[112:115], a[252:255], v[12:15]\n" \
  "v_mul_f32 v40, v36, v40\n" \
  "v_mul_f32 v41, v37, v41\n" \
  "v_mul_f32 v42, v38, v42\n" \
  "v_mfma_f32_16x16x32_bf16 v[16:19], v[112:115], v[192:195], v[16:19]\n" \
  "buffer_load_dwordx4 v68, s[40:43], s62 offen offset:0 lds\n" \
  "ds_read_b64_tr_b16 a[80:81], v64 offset:20480\n" \
  "s_waitcnt vmcnt(20)\n" \
  "v_mfma_f32_16x16x32_bf16 v[0:3], v[116:119], v[196:199], v[0:3]\n" \
  "ds_read_b64_tr_b16 a[82:83], v64 offset:20992\n" \
  "ds_read_b64_tr_b16 a[84:85], v65 offset:20480\n" \
  "v_mul_f32 v43, v39, v43\n" \
  "v_mfma_f32_16x16x32_bf16 v[4:7], v[116:119], v[200:203], v[4:7]\n" \
  "ds_read_b64_tr_b16 a[86:87], v65 offset:20992\n" \
  "ds_read_b64_tr_b16 a[88:89], v66 offset:20480\n" \
  "v_mfma_f32_16x16x32_bf16 v[8:11], v[116:119], v[204:207], v[8:11]\n" \
  "buffer_load_dwordx4 v69, s[40:43], s62 offen offset:1024 lds\n" \
  "ds_read_b64_tr_b16 a[90:91], v66 offset:20992\n" \
  "ds_read_b64_tr_b16 a[92:93], v67 offset:20480\n" \
  "v_mfma_f32_16x16x32_bf16 v[12:15], v[116:119], v[208:211], v[12:15]\n" \
  "ds_read_b64_tr_b16 a[94:95], v67 offset:20992\n" \
  "v_exp_f32 v40, v40\n" \
  "v_mfma_f32_16x16x32_bf16 v[16:19], v[116:119], v[212:215], v[16:19]\n" \
  "v_exp_f32 v41, v41\n" \
  "s_waitcnt vmcnt(20)\n" \
  "v_mfma_f32_16x16x32_bf16 v[0:3], v[120:123], v[216:219], v[0:3]\n" \
  "buffer_load_dwordx4 v70, s[40:43], s62 offen offset:2048 lds\n" \
  "v_exp_f32 v42, v42\n" \
  "v_mfma_f32_16x16x32_bf16 v[4:7], v[120:123], v[220:223], v[4:7]\n" \
  "v_exp_f32 v43, v43\n" \
  "v_add_f32 v40, 1.0, v40\n" \
  "v_mfma_f32_16x16x32_bf16 v[8:11], v[120:123], v[224:227], v[8:11]\n" \
  "v_add_f32 v41, 1.0, v41\n" \
  "v_add_f32 v42, 1.0, v42\n" \
  "v_add_f32 v43, 1.0, v43\n" \
  "v_mfma_f32_16x16x32_bf16 v[12:15], v[120:123], v[228:231], v[12:15]\n" \
  "buffer_load_dwordx4 v71, s[40:43], s62 offen offset:3072 lds\n" \
  "v_rcp_f32 v40, v40\n" \
  "v_mfma_f32_16x16x32_bf16 v[16:19], v[120:123], v[232:235], v[16:19]\n" \
  "ds_read_b64_tr_b16 a[96:97], v64 offset:24576\n" \
  "ds_read_b64_tr_b16 a[98:99], v64 offset:25088\n" \
  "s_waitcnt vmcnt(21)\n" \
  "v_mfma_f32_16x16x32_bf16 v[0:3], v[124:127], v[236:239], v[0:3]\n" \
  "ds_read_b64_tr_b16 a[100:101], v65 offset:24576\n" \
  "ds_read_b64_tr_b16 a[102:103], v65 offset:25088\n" \
  "v_mfma_f32_16x16x32_bf16 v[4:7], v[124:127], v[240:243], v[4:7]\n" \
  "buffer_load_dwordx4 v[112:115], v59, s[36:39], s48 offen offset:1024 sc1\n" \
  "ds_read_b64_tr_b16 a[104:105], v66 offset:24576\n" \
  "ds_read_b64_tr_b16 a[106:107], v66 offset:25088\n" \
  "v_mfma_f32_16x16x32_bf16 v[8:11], v[124:127], v[244:247], v[8:11]\n" \
  "ds_read_b64_tr_b16 a[108:109], v67 offset:24576\n" \
  "ds_read_b64_tr_b16 a[110:111], v67 offset:25088\n" \
  "v_mfma_f32_16x16x32_bf16 v[12:15], v[124:127], v[248:251], v[12:15]\n" \
  "v_rcp_f32 v41, v41\n" \
  "v_mfma_f32_16x16x32_bf16 v[16:19], v[124:127], v[252:255], v[16:19]\n" \
  "buffer_load_dwordx4 v[116:119], v59, s[36:39], s48 offen offset:2048 sc1\n" \
  "s_mov_b32 m0, s56\n" \
  "s_waitcnt vmcnt(22)\n" \
  "v_mfma_f32_16x16x32_bf16 v[208:211], v[128:131], v[72:75], 0\n" \
  "v_rcp_f32 v42, v42\n" \
  "v_mfma_f32_16x16x32_bf16 v[212:215], v[132:135], v[72:75], 0\n" \
  "ds_write_addtid_b32 v0 offset:0\n" \
  "ds_write_addtid_b32 v1 offset:544\n" \
  "v_mfma_f32_16x16x32_bf16 v[216:219], v[136:139], v[72:75], 0\n" \
  "buffer_load_dwordx4 v[120:123], v59, s[36:39], s49 offen offset:0 sc1\n" \
  "ds_write_addtid_b32 v2 offset:256\n" \
  "ds_write_addtid_b32 v3 offset:800\n" \
  "v_mfma_f32_16x16x32_bf16 v[220:223], v[140:143], v[72:75], 0\n" \
  "ds_write_addtid_b32 v4 offset:1056\n" \
  "ds_write_addtid_b32 v5 offset:1600\n" \
  "v_mfma_f32_16x16x32_bf16 v[224:227], v[144:147], v[72:75], 0\n" \
  "ds_write_addtid_b32 v6 offset:1312\n" \
  "ds_write_addtid_b32 v7 offset:1856\n" \
  "v_mfma_f32_16x16x32_bf16 v[228:231], v[148:151], v[72:75], 0\n" \
  "buffer_load_dwordx4 v[124:127], v59, s[36:39], s49 offen offset:1024 sc1\n" \
  "ds_write_addtid_b32 v8 offset:2112\n" \
  "ds_write_addtid_b32 v9 offset:2656\n" \
  "v_mfma_f32_16x16x32_bf16 v[232:235], v[152:155], v[72:75], 0\n" \
  "ds_write_addtid_b32 v10 offset:2368\n" \
  "ds_write_addtid_b32 v11 offset:2912\n" \
  "v_rcp_f32 v43, v43\n" \
  "v_mfma_f32_16x16x32_bf16 v[236:239], v[156:159], v[72:75], 0\n" \
  "ds_write_addtid_b32 v12 offset:3168\n" \
  "ds_write_addtid_b32 v13 offset:3712\n" \
  "v_mul_f32 v36, v36, v40\n" \
  "s_waitcnt vmcnt(23)\n" \
  "v_mfma_f32_16x16x32_bf16 v[208:211], v[160:163], v[76:79], v[208:211]\n" \
  "v_mul_f32 v37, v37, v41\n" \
  "v_mul_f32 v38, v38, v42\n" \
  "v_mul_f32 v39, v39, v43\n" \
  "v_cvt_pk_bf16_f32 v40, v36, v37\n" \
  "v_cvt_pk_bf16_f32 v41, v38, v39\n" \
  "buffer_store_dwordx2 v[40:41], v61, s[36:39], s52 offen offset:0 sc1\n" \
  "v_mfma_f32_16x16x32_bf16 v[212:215], v[164:167], v[76:79], v[212:215]\n" \
  "buffer_load_dwordx4 v[192:195], v59, s[36:39], s49 offen offset:2048 sc1\n" \
  "ds_write_addtid_b32 v14 offset:3424\n" \
  "ds_write_addtid_b32 v15 offset:3968\n" \
  "v_mfma_f32_16x16x32_bf16 v[216:219], v[168:171], v[76:79], v[216:219]\n" \
  "ds_write_addtid_b32 v16 offset:4224\n" \
  "ds_write_addtid_b32 v17 offset:4512\n" \
  "s_add_u32 s52, s52, 409600\n" \
  "v_mfma_f32_16x16x32_bf16 v[220:223], v[172:175], v[76:79], v[220:223]\n" \
  "ds_write_addtid_b32 v18 offset:4800\n" \
  "ds_write_addtid_b32 v19 offset:5088\n" \
  "s_waitcnt vmcnt(20)\n" \
  "v_mfma_f32_16x16x32_bf16 v[224:227], v[176:179], v[76:79], v[224:227]\n" \
  "buffer_load_dwordx4 v[96:99], v59, s[36:39], s70 offen offset:0 sc1\n" \
  "s_add_u32 m0, s57, 12288\n" \
  "v_mfma_f32_16x16x32_bf16 v[228:231], v[180:183], v[76:79], v[228:231]\n" \
  "ds_read_b64_tr_b16 a[112:113], v64 offset:0\n" \
  "ds_read_b64_tr_b16 a[114:115], v64 offset:512\n" \
  "v_mfma_f32_16x16x32_bf16 v[232:235], v[184:187], v[76:79], v[232:235]\n" \
  "ds_read_b64_tr_b16 a[116:117], v65 offset:0\n" \
  "ds_read_b64_tr_b16 a[118:119], v65 offset:512\n" \
  "v_mfma_f32_16x16x32_bf16 v[236:239], v[188:191], v[76:79], v[236:239]\n" \
  "buffer_load_dwordx4 v68, s[40:43], s63 offen offset:0 lds\n" \
  "s_waitcnt lgkmcnt(15)\n" \
  "v_mfma_f32_16x16x32_bf16 v[208:211], a[0:3], v[80:83], v[208:211]\n" \
  "ds_read_b64_tr_b16 a[120:121], v66 offset:0\n" \
  "ds_read_b64_tr_b16 a[122:123], v66 offset:512\n" \
  "v_mfma_f32_16x16x32_bf16 v[212:215], a[4:7], v[80:83], v[212:215]\n" \
  "s_waitcnt lgkmcnt(0)\n" \
  "s_barrier\n" \
  "ds_read_b128 v[36:39], v58 offset:0\n" \
  "v_mfma_f32_16x16x32_bf16 v[216:219], a[8:11], v[80:83], v[216:219]\n" \
  "buffer_load_dwordx4 v69, s[40:43], s63 offen offset:1024 lds\n" \
  "ds_read_b128 v[40:43], v58 offset:9568\n" \
  "ds_read_b128 v[44:47], v58 offset:19136\n" \
  "v_mfma_f32_16x16x32_bf16 v[220:223], a[12:15], v[80:83], v[220:223]\n" \
  "ds_read_b128 v[48:51], v58 offset:28704\n" \
  "ds_read_b64_tr_b16 a[124:125], v67 offset:0\n" \
  "v_mfma_f32_16x16x32_bf16 v[224:227], a[16:19], v[80:83], v[224:227]\n" \
  "ds_read_b64_tr_b16 a[126:127], v67 offset:512\n" \
  "ds_read_b32 v252, v60 offset:0\n" \
  "v_mfma_f32_16x16x32_bf16 v[228:231], a[20:23], v[80:83], v[228:231]\n" \
  "buffer_load_dwordx4 v70, s[40:43], s63 offen offset:2048 lds\n" \
  "ds_read_b32 v253, v60 offset:9568\n" \
  "ds_read_b32 v254, v60 offset:19136\n" \
  "v_mfma_f32_16x16x32_bf16 v[232:235], a[24:27], v[80:83], v[232:235]\n" \
  "ds_read_b32 v255, v60 offset:28704\n" \
  "v_mfma_f32_16x16x32_bf16 v[236:239], a[28:31], v[80:83], v[236:239]\n" \
  "s_waitcnt vmcnt(21)\n" \
  "v_mfma_f32_16x16x32_bf16 v[208:211], a[32:35], v[84:87], v[208:211]\n" \
  "buffer_load_dwordx4 v71, s[40:43], s63 offen offset:3072 lds\n" \
  "s_add_u32 m0, s57, 16384\n" \
  "v_mfma_f32_16x16x32_bf16 v[212:215], a[36:39], v[84:87], v[212:215]\n" \
  "v_mfma_f32_16x16x32_bf16 v[216:219], a[40:43], v[84:87], v[216:219]\n" \
  "s_waitcnt lgkmcnt(8)\n" \
  "v_add_f32 v36, v36, v40\n" \
  "v_add_f32 v37, v37, v41\n" \
  "v_mfma_f32_16x16x32_bf16 v[220:223], a[44:47], v[84:87], v[220:223]\n" \
  "buffer_load_dwordx4 v68, s[40:43], s64 offen offset:0 lds\n" \
  "v_add_f32 v38, v38, v42\n" \
  "v_mfma_f32_16x16x32_bf16 v[224:227], a[48:51], v[84:87], v[224:227]\n" \
  "s_waitcnt vmcnt(17)\n" \
  "ds_read_b64_tr_b16 a[128:129], v64 offset:4096\n" \
  "ds_read_b64_tr_b16 a[130:131], v64 offset:4608\n" \
  "v_mfma_f32_16x16x32_bf16 v[228:231], a[52:55], v[84:87], v[228:231]\n" \
  "ds_read_b64_tr_b16 a[132:133], v65 offset:4096\n" \
  "ds_read_b64_tr_b16 a[134:135], v65 offset:4608\n" \
  "v_add_f32 v39, v39, v43\n" \
  "v_mfma_f32_16x16x32_bf16 v[232:235], a[56:59], v[84:87], v[232:235]\n" \
  "buffer_load_dwordx4 v69, s[40:43], s64 offen offset:1024 lds\n" \
  "ds_read_b64_tr_b16 a[136:137], v66 offset:4096\n" \
  "ds_read_b64_tr_b16 a[138:139], v66 offset:4608\n" \
  "v_mfma_f32_16x16x32_bf16 v[236:239], a[60:63], v[84:87], v[236:239]\n" \
  "ds_read_b64_tr_b16 a[140:141], v67 offset:4096\n" \
  "v_mfma_f32_16x16x32_bf16 v[208:211], a[64:67], v[88:91], v[208:211]\n" \
  "ds_read_b64_tr_b16 a[142:143], v67 offset:4608\n" \
  "v_add_f32 v36, v36, v52\n" \
  "v_add_f32 v37, v37, v53\n" \
  "v_mfma_f32_16x16x32_bf16 v[212:215], a[68:71], v[88:91], v[212:215]\n" \
  "buffer_load_dwordx4 v70, s[40:43], s64 offen offset:2048 lds\n" \
  "v_add_f32 v38, v38, v54\n" \
  "v_add_f32 v39, v39, v55\n" \
  "v_mfma_f32_16x16x32_bf16 v[216:219], a[72:75], v[88:91], v[216:219]\n" \
  "s_waitcnt lgkmcnt(14)\n" \
  "v_add_f32 v44, v44, v48\n" \
  "v_add_f32 v45, v45, v49\n" \
  "v_mfma_f32_16x16x32_bf16 v[220:223], a[76:79], v[88:91], v[220:223]\n" \
  "v_add_f32 v46, v46, v50\n" \
  "v_add_f32 v47, v47, v51\n" \
  "v_mfma_f32_16x16x32_bf16 v[224:227], a[80:83], v[88:91], v[224:227]\n" \
  "buffer_load_dwordx4 v71, s[40:43], s64 offen offset:3072 lds\n" \
  "v_add_f32 v36, v36, v44\n" \
  "v_add_f32 v37, v37, v45\n" \
  "v_mfma_f32_16x16x32_bf16 v[228:231], a[84:87], v[88:91], v[228:231]\n" \
  "v_add_f32 v38, v38, v46\n" \
  "v_add_f32 v39, v39, v47\n" \
  "v_mul_f32 v40, v36, v36\n" \
  "v_mfma_f32_16x16x32_bf16 v[232:235], a[88:91], v[88:91], v[232:235]\n" \
  "v_mul_f32 v41, v37, v37\n" \
  "v_mul_f32 v42, v38, v38\n" \
  "v_mul_f32 v43, v39, v39\n" \
  "v_mfma_f32_16x16x32_bf16 v[236:239], a[92:95], v[88:91], v[236:239]\n" \
  "buffer_load_dwordx4 v[100:103], v59, s[36:39], s70 offen offset:1024 sc1\n" \
  "v_mfma_f32_16x16x32_bf16 v[208:211], a[96:99], v[92:95], v[208:211]\n" \
  "s_add_u32 m0, s57, 20480\n" \
  "v_fma_f32 v40, v40, s55, v57\n" \
  "v_mfma_f32_16x16x32_bf16 v[212:215], a[100:103], v[92:95], v[212:215]\n" \
  "v_fma_f32 v41, v41, s55, v57\n" \
  "v_fma_f32 v42, v42, s55, v57\n" \
  "v_fma_f32 v43, v43, s55, v57\n" \
  "v_mfma_f32_16x16x32_bf16 v[216:219], a[104:107], v[92:95], v[216:219]\n" \
  "buffer_load_dwordx4 v68, s[40:43], s65 offen offset:0 lds\n" \
  "v_mul_f32 v40, v36, v40\n" \
  "v_mul_f32 v41, v37, v41\n" \
  "v_mfma_f32_16x16x32_bf16 v[220:223], a[108:111], v[92:95], v[220:223]\n" \
  "s_waitcnt vmcnt(17)\n" \
  "ds_read_b64_tr_b16 a[144:145], v64 offset:8192\n" \
  "v_mfma_f32_16x16x32_bf16 v[0:3], v[128:131], v[108:111], 0\n" \
  "ds_read_b64_tr_b16 a[146:147], v64 offset:8704\n" \
  "ds_read_b64_tr_b16 a[148:149], v65 offset:8192\n" \
  "v_mul_f32 v42, v38, v42\n" \
  "v_mfma_f32_16x16x32_bf16 v[4:7], v[132:135], v[108:111], 0\n" \
  "buffer_load_dwordx4 v69, s[40:43], s65 offen offset:1024 lds\n" \
  "ds_read_b64_tr_b16 a[150:151], v65 offset:8704\n" \
  "ds_read_b64_tr_b16 a[152:153], v66 offset:8192\n" \
  "v_mfma_f32_16x16x32_bf16 v[8:11], v[136:139], v[108:111], 0\n" \
  "ds_read_b64_tr_b16 a[154:155], v66 offset:8704\n" \
  "ds_read_b64_tr_b16 a[156:157], v67 offset:8192\n" \
  "v_mul_f32 v43, v39, v43\n" \
  "v_mfma_f32_16x16x32_bf16 v[12:15], v[140:143], v[108:111], 0\n" \
  "ds_read_b64_tr_b16 a[158:159], v67 offset:8704\n" \
  "v_exp_f32 v40, v40\n" \
  "v_mfma_f32_16x16x32_bf16 v[16:19], v[144:147], v[108:111], 0\n" \
  "buffer_load_dwordx4 v70, s[40:43], s65 offen offset:2048 lds\n" \
  "v_exp_f32 v41, v41\n" \
  "v_mfma_f32_16x16x32_bf16 v[20:23], v[148:151], v[108:111], 0\n" \
  "v_exp_f32 v42, v42\n" \
  "s_waitcnt lgkmcnt(15)\n" \
  "v_mfma_f32_16x16x32_bf16 v[24:27], v[152:155], v[108:111], 0\n" \
  "v_exp_f32 v43, v43\n" \
  "v_add_f32 v40, 1.0, v40\n" \
  "v_mfma_f32_16x16x32_bf16 v[28:31], v[156:159], v[108:111], 0\n" \
  "buffer_load_dwordx4 v71, s[40:43], s65 offen offset:3072 lds\n" \
  "v_add_f32 v41, 1.0, v41\n" \
  "s_waitcnt vmcnt(19)\n" \
  "v_mfma_f32_16x16x32_bf16 v[0:3], v[160:163], v[112:115], v[0:3]\n" \
  "v_add_f32 v42, 1.0, v42\n" \
  "v_add_f32 v43, 1.0, v43\n" \
  "v_add_f32 v252, v252, v253\n" \
  "v_mfma_f32_16x16x32_bf16 v[4:7], v[164:167], v[112:115], v[4:7]\n" \
  "v_rcp_f32 v40, v40\n" \
  "v_add_f32 v254, v254, v255\n" \
  "v_mfma_f32_16x16x32_bf16 v[8:11], v[168:171], v[112:115], v[8:11]\n" \
  "buffer_load_dwordx4 v[104:107], v59, s[36:39], s70 offen offset:2048 sc1\n" \
  "s_add_u32 m0, s57, 24576\n" \
  "v_mfma_f32_16x16x32_bf16 v[12:15], v[172:175], v[112:115], v[12:15]\n" \
  "v_rcp_f32 v41, v41\n" \
  "v_add_f32 v252, v252, v56\n" \
  "v_mfma_f32_16x16x32_bf16 v[16:19], v[176:179], v[112:115], v[16:19]\n" \
  "v_rcp_f32 v42, v42\n" \
  "v_add_f32 v252, v252, v254\n" \
  "v_mfma_f32_16x16x32_bf16 v[20:23], v[180:183], v[112:115], v[20:23]\n" \
  "buffer_load_dwordx4 v68, s[40:43], s66 offen offset:0 lds\n" \
  "v_rcp_f32 v43, v43\n" \
  "v_mfma_f32_16x16x32_bf16 v[24:27], v[184:187], v[112:115], v[24:27]\n" \
  "v_mul_f32 v36, v36, v40\n" \
  "v_mul_f32 v37, v37, v41\n" \
  "v_mul_f32 v38, v38, v42\n" \
  "v_mfma_f32_16x16x32_bf16 v[28:31], v[188:191], v[112:115], v[28:31]\n" \
  "v_mul_f32 v39, v39, v43\n" \
  "v_cvt_pk_bf16_f32 v40, v36, v37\n" \
  "s_waitcnt vmcnt(20)\n" \
  "v_mfma_f32_16x16x32_bf16 v[0:3], a[0:3], v[116:119], v[0:3]\n" \
  "buffer_load_dwordx4 v69, s[40:43], s66 offen offset:1024 lds\n" \
  "v_cvt_pk_bf16_f32 v41, v38, v39\n" \
  "v_mul_f32 v253, v252, v252\n" \
  "v_mfma_f32_16x16x32_bf16 v[4:7], a[4:7], v[116:119], v[4:7]\n" \
  "v_fma_f32 v253, v253, s55, v57\n" \
  "v_mul_f32 v253, v252, v253\n" \
  "v_mfma_f32_16x16x32_bf16 v[8:11], a[8:11], v[116:119], v[8:11]\n" \
  "v_exp_f32 v253, v253\n" \
  "v_mfma_f32_16x16x32_bf16 v[12:15], a[12:15], v[116:119], v[12:15]\n" \
  "buffer_load_dwordx4 v70, s[40:43], s66 offen offset:2048 lds\n" \
  "v_add_f32 v253, 1.0, v253\n" \
  "v_mfma_f32_16x16x32_bf16 v[16:19], a[16:19], v[116:119], v[16:19]\n" \
  "v_rcp_f32 v253, v253\n" \
  "v_mfma_f32_16x16x32_bf16 v[20:23], a[20:23], v[116:119], v[20:23]\n" \
  "v_mul_f32 v252, v252, v253\n" \
  "v_cvt_pk_bf16_f32 v253, v252, v252\n" \
  "v_mfma_f32_16x16x32_bf16 v[24:27], a[24:27], v[116:119], v[24:27]\n" \
  "buffer_load_dwordx4 v71, s[40:43], s66 offen offset:3072 lds\n" \
  "v_mfma_f32_16x16x32_bf16 v[28:31], a[28:31], v[116:119], v[28:31]\n" \
  "s_waitcnt vmcnt(22)\n" \
  "v_mfma_f32_16x16x32_bf16 v[0:3], a[32:35], v[120:123], v[0:3]\n" \
  "v_mfma_f32_16x16x32_bf16 v[4:7], a[36:39], v[120:123], v[4:7]\n" \
  "buffer_load_dwordx4 v[196:199], v59, s[36:39], s50 offen offset:0 sc1\n" \
  "v_mfma_f32_16x16x32_bf16 v[8:11], a[40:43], v[120:123], v[8:11]\n" \
  "v_mfma_f32_16x16x32_bf16 v[12:15], a[44:47], v[120:123], v[12:15]\n" \
  "v_mfma_f32_16x16x32_bf16 v[16:19], a[48:51], v[120:123], v[16:19]\n" \
  "buffer_load_dwordx4 v[200:203], v59, s[36:39], s50 offen offset:1024 sc1\n" \
  "v_mfma_f32_16x16x32_bf16 v[20:23], a[52:55], v[120:123], v[20:23]\n" \
  "v_mfma_f32_16x16x32_bf16 v[24:27], a[56:59], v[120:123], v[24:27]\n" \
  "s_waitcnt vmcnt(16)\n" \
  "ds_read_b64_tr_b16 a[160:161], v64 offset:12288\n" \
  "ds_read_b64_tr_b16 a[162:163], v64 offset:12800\n" \
  "v_mfma_f32_16x16x32_bf16 v[28:31], a[60:63], v[120:123], v[28:31]\n" \
  "buffer_load_dwordx4 v[204:207], v59, s[36:39], s50 offen offset:2048 sc1\n" \
  "ds_read_b64_tr_b16 a[164:165], v65 offset:12288\n" \
  "s_waitcnt vmcnt(24)\n" \
  "v_mfma_f32_16x16x32_bf16 v[0:3], a[64:67], v[124:127], v[0:3]\n" \
  "s_add_u32 s59, s59, 0x10000\n" \
  "buffer_load_dword v63, v61, s[36:39], s59 offen offset:0 sc1\n" \
  "v_add_u32 v59, 819200, v59\n" \
  "v_mfma_f32_16x16x32_bf16 v[4:7], a[68:71], v[124:127], v[4:7]\n" \
  "ds_read_b64_tr_b16 a[166:167], v65 offset:12800\n" \
  "ds_read_b64_tr_b16 a[168:169], v66 offset:12288\n" \
  "buffer_store_short v253, v62, s[36:39], s52 offen offset:0 sc1\n" \
  "v_mfma_f32_16x16x32_bf16 v[8:11], a[72:75], v[124:127], v[8:11]\n" \
  "v_and_b32 v58, 0x3ff, v59\n" \
  "v_add_u32 v58, s99, v58\n" \
  "v_add_u32 v46, s98, v58\n" \
  "v_mfma_f32_16x16x32_bf16 v[12:15], a[76:79], v[124:127], v[12:15]\n" \
  "ds_read_b64 v[56:57], v46 offset:28680\n" \
  "ds_read_b64_tr_b16 a[170:171], v66 offset:12800\n" \
  "buffer_store_dwordx2 v[40:41], v61, s[36:39], s52 offen offset:0 sc1\n" \
  "v_mfma_f32_16x16x32_bf16 v[16:19], a[80:83], v[124:127], v[16:19]\n" \
  "buffer_load_dwordx4 v[72:75], v59, s[36:39], s68 offen offset:0 sc1\n" \
  "ds_read_b64_tr_b16 a[172:173], v67 offset:12288\n" \
  "ds_read_b64_tr_b16 a[174:175], v67 offset:12800\n" \
  "v_mfma_f32_16x16x32_bf16 v[20:23], a[84:87], v[124:127], v[20:23]\n" \
  "v_mfma_f32_16x16x32_bf16 v[24:27], a[88:91], v[124:127], v[24:27]\n" \
  "v_mfma_f32_16x16x32_bf16 v[28:31], a[92:95], v[124:127], v[28:31]\n" \
  "buffer_load_dwordx4 v[76:79], v59, s[36:39], s68 offen offset:1024 sc1\n" \
  "s_waitcnt vmcnt(27)\n" \
  "v_mfma_f32_16x16x32_bf16 v[0:3], a[96:99], v[192:195], v[0:3]\n" \
  "s_waitcnt lgkmcnt(0)\n" \
  "s_barrier\n" \
  "s_mov_b32 m0, s58\n" \
  "v_mfma_f32_16x16x32_bf16 v[4:7], a[100:103], v[192:195], v[4:7]\n" \
  "v_mfma_f32_16x16x32_bf16 v[8:11], a[104:107], v[192:195], v[8:11]\n" \
  "buffer_load_dwordx4 v[80:83], v59, s[36:39], s68 offen offset:2048 sc1\n" \
  "s_waitcnt vmcnt(19)\n" \
  "ds_read_b64_tr_b16 a[176:177], v64 offset:16384\n" \
  "v_mfma_f32_16x16x32_bf16 v[12:15], a[108:111], v[192:195], v[12:15]\n" \
  "ds_read_b64_tr_b16 a[178:179], v64 offset:16896\n" \
  "ds_read_b64_tr_b16 a[180:181], v65 offset:16384\n" \
  "v_mfma_f32_16x16x32_bf16 v[224:227], a[112:115], v[92:95], v[224:227]\n" \
  "ds_read_b64_tr_b16 a[182:183], v65 offset:16896\n" \
  "ds_read_b64_tr_b16 a[184:185], v66 offset:16384\n" \
  "v_mfma_f32_16x16x32_bf16 v[228:231], a[116:119], v[92:95], v[228:231]\n" \
  "buffer_load_dwordx4 v[84:87], v59, s[36:39], s69 offen offset:0 sc1\n" \
  "ds_read_b64_tr_b16 a[186:187], v66 offset:16896\n" \
  "ds_read_b64_tr_b16 a[188:189], v67 offset:16384\n" \
  "v_mfma_f32_16x16x32_bf16 v[232:235], a[120:123], v[92:95], v[232:235]\n" \
  "ds_read_b64_tr_b16 a[190:191], v67 offset:16896\n" \
  "v_mfma_f32_16x16x32_bf16 v[236:239], a[124:127], v[92:95], v[236:239]\n" \
  "v_mfma_f32_16x16x32_bf16 v[208:211], a[128:131], v[96:99], v[208:211]\n" \
  "buffer_load_dwordx4 v[88:91], v59, s[36:39], s69 offen offset:1024 sc1\n" \
  "v_mfma_f32_16x16x32_bf16 v[212:215], a[132:135], v[96:99], v[212:215]\n" \
  "v_mfma_f32_16x16x32_bf16 v[216:219], a[136:139], v[96:99], v[216:219]\n" \
  "v_mfma_f32_16x16x32_bf16 v[220:223], a[140:143], v[96:99], v[220:223]\n" \
  "buffer_load_dwordx4 v[92:95], v59, s[36:39], s69 offen offset:2048 sc1\n" \
  "v_mfma_f32_16x16x32_bf16 v[224:227], a[144:147], v[96:99], v[224:227]\n" \
  "v_mfma_f32_16x16x32_bf16 v[228:231], a[148:151], v[96:99], v[228:231]\n" \
  "v_mfma_f32_16x16x32_bf16 v[232:235], a[152:155], v[96:99], v[232:235]\n" \
  "v_mfma_f32_16x16x32_bf16 v[236:239], a[156:159], v[96:99], v[236:239]\n" \
  "buffer_load_dwordx4 v[96:99], v59, s[36:39], s70 offen offset:0 sc1\n" \
  "s_waitcnt vmcnt(22)\n" \
  "v_mfma_f32_16x16x32_bf16 v[208:211], a[160:163], v[100:103], v[208:211]\n" \
  "s_waitcnt vmcnt(18)\n" \
  "ds_read_b64_tr_b16 a[192:193], v64 offset:20480\n" \
  "ds_read_b64_tr_b16 a[194:195], v64 offset:20992\n" \
  "v_mfma_f32_16x16x32_bf16 v[212:215], a[164:167], v[100:103], v[212:215]\n" \
  "ds_read_b64_tr_b16 a[196:197], v65 offset:20480\n" \
  "ds_read_b64_tr_b16 a[198:199], v65 offset:20992\n" \
  "v_mfma_f32_16x16x32_bf16 v[216:219], a[168:171], v[100:103], v[216:219]\n" \
  "ds_read_b64_tr_b16 a[200:201], v66 offset:20480\n" \
  "ds_read_b64_tr_b16 a[202:203], v66 offset:20992\n" \
  "v_mfma_f32_16x16x32_bf16 v[220:223], a[172:175], v[100:103], v[220:223]\n" \
  "ds_read_b64_tr_b16 a[204:205], v67 offset:20480\n" \
  "ds_read_b64_tr_b16 a[206:207], v67 offset:20992\n" \
  "s_waitcnt lgkmcnt(8)\n" \
  "v_mfma_f32_16x16x32_bf16 v[224:227], a[176:179], v[100:103], v[224:227]\n" \
  "s_waitcnt vmcnt(13)\n" \
  "ds_read_b64_tr_b16 a[208:209], v64 offset:24576\n" \
  "ds_read_b64_tr_b16 a[210:211], v64 offset:25088\n" \
  "v_mfma_f32_16x16x32_bf16 v[228:231], a[180:183], v[100:103], v[228:231]\n" \
  "ds_read_b64_tr_b16 a[212:213], v65 offset:24576\n" \
  "ds_read_b64_tr_b16 a[214:215], v65 offset:25088\n" \
  "v_mfma_f32_16x16x32_bf16 v[232:235], a[184:187], v[100:103], v[232:235]\n" \
  "ds_read_b64_tr_b16 a[216:217], v66 offset:24576\n" \
  "ds_read_b64_tr_b16 a[218:219], v66 offset:25088\n" \
  "v_mfma_f32_16x16x32_bf16 v[236:239], a[188:191], v[100:103], v[236:239]\n" \
  "ds_read_b64_tr_b16 a[220:221], v67 offset:24576\n" \
  "ds_read_b64_tr_b16 a[222:223], v67 offset:25088\n" \
  "s_waitcnt lgkmcnt(8)\n" \
  "v_mfma_f32_16x16x32_bf16 v[208:211], a[192:195], v[104:107], v[208:211]\n" \
  "buffer_load_dwordx4 v[100:103], v59, s[36:39], s70 offen offset:1024 sc1\n" \
  "v_mfma_f32_16x16x32_bf16 v[212:215], a[196:199], v[104:107], v[212:215]\n" \
  "v_mfma_f32_16x16x32_bf16 v[216:219], a[200:203], v[104:107], v[216:219]\n" \
  "v_mfma_f32_16x16x32_bf16 v[220:223], a[204:207], v[104:107], v[220:223]\n" \
  "s_waitcnt lgkmcnt(0)\n" \
  "v_mfma_f32_16x16x32_bf16 v[224:227], a[208:211], v[104:107], v[224:227]\n" \
  "v_mfma_f32_16x16x32_bf16 v[228:231], a[212:215], v[104:107], v[228:231]\n" \
  "v_mfma_f32_16x16x32_bf16 v[232:235], a[216:219], v[104:107], v[232:235]\n" \
  "v_mfma_f32_16x16x32_bf16 v[236:239], a[220:223], v[104:107], v[236:239]\n" \
  "buffer_load_dwordx4 v[104:107], v59, s[36:39], s70 offen offset:2048 sc1\n" \
  "v_mfma_f32_16x16x32_bf16 v[16:19], a[112:115], v[192:195], v[16:19]\n" \
  "v_mfma_f32_16x16x32_bf16 v[20:23], a[116:119], v[192:195], v[20:23]\n" \
  "ds_write_addtid_b32 v208 offset:0\n" \
  "ds_write_addtid_b32 v209 offset:256\n" \
  "v_mfma_f32_16x16x32_bf16 v[24:27], a[120:123], v[192:195], v[24:27]\n" \
  "ds_write_addtid_b32 v210 offset:512\n" \
  "ds_write_addtid_b32 v211 offset:768\n" \
  "v_mfma_f32_16x16x32_bf16 v[28:31], a[124:127], v[192:195], v[28:31]\n" \
  "ds_write_addtid_b32 v212 offset:1024\n" \
  "ds_write_addtid_b32 v213 offset:1280\n" \
  "s_waitcnt vmcnt(14)\n" \
  "v_mfma_f32_16x16x32_bf16 v[0:3], a[128:131], v[196:199], v[0:3]\n" \
  "ds_write_addtid_b32 v214 offset:1536\n" \
  "ds_write_addtid_b32 v215 offset:1792\n" \
  "v_mfma_f32_16x16x32_bf16 v[4:7], a[132:135], v[196:199], v[4:7]\n" \
  "ds_write_addtid_b32 v216 offset:2048\n" \
  "ds_write_addtid_b32 v217 offset:2304\n" \
  "v_mfma_f32_16x16x32_bf16 v[8:11], a[136:139], v[196:199], v[8:11]\n" \
  "ds_write_addtid_b32 v218 offset:2560\n" \
  "ds_write_addtid_b32 v219 offset:2816\n" \
  "v_mfma_f32_16x16x32_bf16 v[12:15], a[140:143], v[196:199], v[12:15]\n" \
  "ds_write_addtid_b32 v220 offset:3072\n" \
  "ds_write_addtid_b32 v221 offset:3328\n" \
  "v_mfma_f32_16x16x32_bf16 v[16:19], a[144:147], v[196:199], v[16:19]\n" \
  "ds_write_addtid_b32 v222 offset:3584\n" \
  "ds_write_addtid_b32 v223 offset:3840\n" \
  "v_mfma_f32_16x16x32_bf16 v[20:23], a[148:151], v[196:199], v[20:23]\n" \
  "ds_write_addtid_b32 v224 offset:4096\n" \
  "ds_write_addtid_b32 v225 offset:4352\n" \
  "v_mfma_f32_16x16x32_bf16 v[24:27], a[152:155], v[196:199], v[24:27]\n" \
  "ds_write_addtid_b32 v226 offset:4608\n" \
  "ds_write_addtid_b32 v227 offset:4864\n" \
  "v_mfma_f32_16x16x32_bf16 v[28:31], a[156:159], v[196:199], v[28:31]\n" \
  "ds_write_addtid_b32 v228 offset:5120\n" \
  "ds_write_addtid_b32 v229 offset:5376\n" \
  "s_waitcnt vmcnt(13)\n" \
  "v_mfma_f32_16x16x32_bf16 v[0:3], a[160:163], v[200:203], v[0:3]\n" \
  "ds_write_addtid_b32 v230 offset:5632\n" \
  "ds_write_addtid_b32 v231 offset:5888\n" \
  "v_mfma_f32_16x16x32_bf16 v[4:7], a[164:167], v[200:203], v[4:7]\n" \
  "ds_write_addtid_b32 v232 offset:6144\n" \
  "ds_write_addtid_b32 v233 offset:6400\n" \
  "v_mfma_f32_16x16x32_bf16 v[8:11], a[168:171], v[200:203], v[8:11]\n" \
  "ds_write_addtid_b32 v234 offset:6656\n" \
  "ds_write_addtid_b32 v235 offset:6912\n" \
  "v_mfma_f32_16x16x32_bf16 v[12:15], a[172:175], v[200:203], v[12:15]\n" \
  "ds_write_addtid_b32 v236 offset:7168\n" \
  "ds_write_addtid_b32 v237 offset:7424\n" \
  "v_mfma_f32_16x16x32_bf16 v[16:19], a[176:179], v[200:203], v[16:19]\n" \
  "ds_write_addtid_b32 v238 offset:7680\n" \
  "ds_write_addtid_b32 v239 offset:7936\n" \
  "v_mfma_f32_16x16x32_bf16 v[20:23], a[180:183], v[200:203], v[20:23]\n" \
  "v_mfma_f32_16x16x32_bf16 v[24:27], a[184:187], v[200:203], v[24:27]\n" \
  "v_mfma_f32_16x16x32_bf16 v[28:31], a[188:191], v[200:203], v[28:31]\n" \
  "s_waitcnt vmcnt(12)\n" \
  "v_mfma_f32_16x16x32_bf16 v[0:3], a[192:195], v[204:207], v[0:3]\n" \
  "v_mfma_f32_16x16x32_bf16 v[4:7], a[196:199], v[204:207], v[4:7]\n" \
  "v_mfma_f32_16x16x32_bf16 v[8:11], a[200:203], v[204:207], v[8:11]\n" \
  "s_waitcnt lgkmcnt(0)\n" \
  "s_barrier\n" \
  "ds_read_b128 v[32:35], v58 offset:0\n" \
  "v_mfma_f32_16x16x32_bf16 v[12:15], a[204:207], v[204:207], v[12:15]\n" \
  "ds_read_b128 v[36:39], v58 offset:8192\n" \
  "ds_read_b128 v[40:43], v58 offset:16384\n" \
  "v_mfma_f32_16x16x32_bf16 v[16:19], a[208:211], v[204:207], v[16:19]\n" \
  "ds_read_b128 v[44:47], v58 offset:24576\n" \
  "ds_read_b128 v[48:51], v58 offset:4096\n" \
  "v_mfma_f32_16x16x32_bf16 v[20:23], a[212:215], v[204:207], v[20:23]\n" \
  "ds_read_b128 v[240:243], v58 offset:12288\n" \
  "ds_read_b128 v[244:247], v58 offset:20480\n" \
  "v_mfma_f32_16x16x32_bf16 v[24:27], a[216:219], v[204:207], v[24:27]\n" \
  "ds_read_b128 v[248:251], v58 offset:28672\n" \
  "v_mfma_f32_16x16x32_bf16 v[28:31], a[220:223], v[204:207], v[28:31]\n" \
  "s_waitcnt vmcnt(7)\n" \
  "v_mfma_f32_16x16x32_bf16 v[208:211], v[128:131], v[72:75], 0\n" \
  "s_mov_b64 exec, 1\n" \
  "buffer_store_dword v61, off, s[36:39], s54 offset:0 sc1\n" \
  "s_mov_b64 exec, -1\n" \
  "v_mfma_f32_16x16x32_bf16 v[212:215], v[132:135], v[72:75], 0\n" \
  "v_cmp_eq_u32 vcc, 0, v63\n" \
  "ds_write_addtid_b32 v0 offset:32768\n" \
  "v_mfma_f32_16x16x32_bf16 v[216:219], v[136:139], v[72:75], 0\n" \
  "ds_write_addtid_b32 v1 offset:33024\n" \
  "ds_write_addtid_b32 v2 offset:33280\n" \
  "v_lshlrev_b32 v52, 5, v59\n" \
  "v_mfma_f32_16x16x32_bf16 v[220:223], v[140:143], v[72:75], 0\n" \
  "s_and_b64 vcc, vcc, s[86:87]\n" \
  "s_cbranch_scc1 .LsbF_%=\n" \
  ".LbbF_%=:\n" \
  "buffer_load_dwordx4 v[108:111], v59, s[36:39], s48 offen offset:0 sc1\n" \
  "v_mfma_f32_16x16x32_bf16 v[224:227], v[144:147], v[72:75], 0\n" \
  "ds_write_addtid_b32 v3 offset:33536\n" \
  "ds_write_addtid_b32 v4 offset:33792\n" \
  "s_waitcnt lgkmcnt(9)\n" \
  "v_mfma_f32_16x16x32_bf16 v[228:231], v[148:151], v[72:75], 0\n" \
  "ds_write_addtid_b32 v5 offset:34048\n" \
  "ds_write_addtid_b32 v6 offset:34304\n" \
  "v_add_f32 v32, v32, v36\n" \
  "v_mfma_f32_16x16x32_bf16 v[232:235], v[152:155], v[72:75], 0\n" \
  "buffer_load_dwordx4 v[112:115], v59, s[36:39], s48 offen offset:1024 sc1\n" \
  "ds_write_addtid_b32 v7 offset:34560\n" \
  "ds_write_addtid_b32 v8 offset:34816\n" \
  "v_mfma_f32_16x16x32_bf16 v[236:239], v[156:159], v[72:75], 0\n" \
  "ds_write_addtid_b32 v9 offset:35072\n" \
  "ds_write_addtid_b32 v10 offset:35328\n" \
  "v_add_f32 v33, v33, v37\n" \
  "v_mfma_f32_16x16x32_bf16 v[208:211], v[160:163], v[76:79], v[208:211]\n" \
  "ds_write_addtid_b32 v11 offset:35584\n" \
  "ds_write_addtid_b32 v12 offset:35840\n" \
  "v_add_f32 v34, v34, v38\n" \
  "v_mfma_f32_16x16x32_bf16 v[212:215], v[164:167], v[76:79], v[212:215]\n" \
  "buffer_load_dwordx4 v[116:119], v59, s[36:39], s48 offen offset:2048 sc1\n" \
  "ds_write_addtid_b32 v13 offset:36096\n" \
  "ds_write_addtid_b32 v14 offset:36352\n" \
  "v_mfma_f32_16x16x32_bf16 v[216:219], v[168:171], v[76:79], v[216:219]\n" \
  "ds_write_addtid_b32 v15 offset:36608\n" \
  "ds_write_addtid_b32 v16 offset:36864\n" \
  "v_add_f32 v35, v35, v39\n" \
  "v_mfma_f32_16x16x32_bf16 v[220:223], v[172:175], v[76:79], v[220:223]\n" \
  "ds_write_addtid_b32 v17 offset:37120\n" \
  "ds_write_addtid_b32 v18 offset:37376\n" \
  "v_add_f32 v40, v40, v44\n" \
  "v_mfma_f32_16x16x32_bf16 v[224:227], v[176:179], v[76:79], v[224:227]\n" \
  "buffer_load_dwordx4 v[120:123], v59, s[36:39], s49 offen offset:0 sc1\n" \
  "ds_write_addtid_b32 v19 offset:37632\n" \
  "ds_write_addtid_b32 v20 offset:37888\n" \
  "v_mfma_f32_16x16x32_bf16 v[228:231], v[180:183], v[76:79], v[228:231]\n" \
  "ds_write_addtid_b32 v21 offset:38144\n" \
  "ds_write_addtid_b32 v22 offset:38400\n" \
  "v_add_f32 v41, v41, v45\n" \
  "v_mfma_f32_16x16x32_bf16 v[232:235], v[184:187], v[76:79], v[232:235]\n" \
  "ds_write_addtid_b32 v23 offset:38656\n" \
  "ds_write_addtid_b32 v24 offset:38912\n" \
  "v_add_f32 v42, v42, v46\n" \
  "v_mfma_f32_16x16x32_bf16 v[236:239], v[188:191], v[76:79], v[236:239]\n" \
  "buffer_load_dwordx4 v[124:127], v59, s[36:39], s49 offen offset:1024 sc1\n" \
  "ds_write_addtid_b32 v25 offset:39168\n" \
  "s_waitcnt vmcnt(11)\n" \
  "v_mfma_f32_16x16x32_bf16 v[208:211], a[0:3], v[80:83], v[208:211]\n" \
  "ds_write_addtid_b32 v26 offset:39424\n" \
  "ds_write_addtid_b32 v27 offset:39680\n" \
  "v_add_f32 v43, v43, v47\n" \
  "v_mfma_f32_16x16x32_bf16 v[212:215], a[4:7], v[80:83], v[212:215]\n" \
  "ds_write_addtid_b32 v28 offset:39936\n" \
  "ds_write_addtid_b32 v29 offset:40192\n" \
  "v_add_f32 v32, v32, v40\n" \
  "v_mfma_f32_16x16x32_bf16 v[216:219], a[8:11], v[80:83], v[216:219]\n" \
  "buffer_load_dwordx4 v[192:195], v59, s[36:39], s49 offen offset:2048 sc1\n" \
  "ds_write_addtid_b32 v30 offset:40448\n" \
  "ds_write_addtid_b32 v31 offset:40704\n" \
  "v_mfma_f32_16x16x32_bf16 v[220:223], a[12:15], v[80:83], v[220:223]\n" \
  "v_add_f32 v33, v33, v41\n" \
  "v_add_f32 v34, v34, v42\n" \
  "v_add_f32 v35, v35, v43\n" \
  "v_mfma_f32_16x16x32_bf16 v[224:227], a[16:19], v[80:83], v[224:227]\n" \
  "v_cvt_pk_bf16_f32 v36, v32, v33\n" \
  "v_cvt_pk_bf16_f32 v37, v34, v35\n" \
  "s_waitcnt lgkmcnt(15)\n" \
  "v_mfma_f32_16x16x32_bf16 v[228:231], a[20:23], v[80:83], v[228:231]\n" \
  "buffer_load_dwordx4 v[196:199], v59, s[36:39], s50 offen offset:0 sc1\n" \
  "v_add_f32 v48, v48, v240\n" \
  "v_add_f32 v49, v49, v241\n" \
  "v_mfma_f32_16x16x32_bf16 v[232:235], a[24:27], v[80:83], v[232:235]\n" \
  "v_add_f32 v50, v50, v242\n" \
  "v_add_f32 v51, v51, v243\n" \
  "v_add_f32 v244, v244, v248\n" \
  "v_mfma_f32_16x16x32_bf16 v[236:239], a[28:31], v[80:83], v[236:239]\n" \
  "v_add_f32 v245, v245, v249\n" \
  "v_add_f32 v246, v246, v250\n" \
  "v_add_f32 v247, v247, v251\n" \
  "v_mfma_f32_16x16x32_bf16 v[208:211], a[32:35], v[84:87], v[208:211]\n" \
  "buffer_load_dwordx4 v[200:203], v59, s[36:39], s50 offen offset:1024 sc1\n" \
  "s_waitcnt lgkmcnt(0)\n" \
  "s_barrier\n" \
  "v_mfma_f32_16x16x32_bf16 v[212:215], a[36:39], v[84:87], v[212:215]\n" \
  "v_add_f32 v48, v48, v244\n" \
  "v_add_f32 v49, v49, v245\n" \
  "v_add_f32 v50, v50, v246\n" \
  "v_mfma_f32_16x16x32_bf16 v[216:219], a[40:43], v[84:87], v[216:219]\n" \
  "v_and_b32 v52, 0x1800, v52\n" \
  "v_add_f32 v51, v51, v247\n" \
  "v_and_b32 v53, 0x30, v59\n" \
  "v_mfma_f32_16x16x32_bf16 v[220:223], a[44:47], v[84:87], v[220:223]\n" \
  "buffer_load_dwordx4 v[204:207], v59, s[36:39], s50 offen offset:2048 sc1\n" \
  "v_cvt_pk_bf16_f32 v38, v48, v49\n" \
  "v_lshl_or_b32 v52, v53, 1, v52\n" \
  "v_mfma_f32_16x16x32_bf16 v[224:227], a[48:51], v[84:87], v[224:227]\n" \
  "v_cvt_pk_bf16_f32 v39, v50, v51\n" \
  "buffer_load_dword v53, v52, s[36:39], s30 offen offset:0 sc1\n" \
  "v_mfma_f32_16x16x32_bf16 v[228:231], a[52:55], v[84:87], v[228:231]\n" \
  "v_mfma_f32_16x16x32_bf16 v[232:235], a[56:59], v[84:87], v[232:235]\n" \
  "buffer_store_dwordx4 v[36:39], v59, s[36:39], s74 offen offset:0 sc1\n" \
  "ds_read_b128 v[32:35], v58 offset:32768\n" \
  "s_nop 0\n" \
  "ds_read_b128 v[36:39], v58 offset:40960\n" \
  "v_mfma_f32_16x16x32_bf16 v[236:239], a[60:63], v[84:87], v[236:239]\n" \
  "ds_read_b128 v[40:43], v58 offset:49152\n" \
  "ds_read_b128 v[44:47], v58 offset:57344\n" \
  "s_waitcnt vmcnt(15)\n" \
  "v_mfma_f32_16x16x32_bf16 v[208:211], a[64:67], v[88:91], v[208:211]\n" \
  "ds_read_b128 v[48:51], v58 offset:36864\n" \
  "ds_read_b128 v[240:243], v58 offset:45056\n" \
  "v_mfma_f32_16x16x32_bf16 v[212:215], a[68:71], v[88:91], v[212:215]\n" \
  "ds_read_b128 v[244:247], v58 offset:53248\n" \
  "ds_read_b128 v[248:251], v58 offset:61440\n" \
  "v_mfma_f32_16x16x32_bf16 v[216:219], a[72:75], v[88:91], v[216:219]\n" \
  "v_mfma_f32_16x16x32_bf16 v[220:223], a[76:79], v[88:91], v[220:223]\n" \
  "v_mfma_f32_16x16x32_bf16 v[224:227], a[80:83], v[88:91], v[224:227]\n" \
  "v_mfma_f32_16x16x32_bf16 v[228:231], a[84:87], v[88:91], v[228:231]\n" \
  "v_mfma_f32_16x16x32_bf16 v[232:235], a[88:91], v[88:91], v[232:235]\n" \
  "v_mfma_f32_16x16x32_bf16 v[236:239], a[92:95], v[88:91], v[236:239]\n" \
  "s_waitcnt lgkmcnt(4)\n" \
  "v_add_f32 v32, v32, v36\n" \
  "v_add_f32 v33, v33, v37\n" \
  "v_mfma_f32_16x16x32_bf16 v[208:211], a[96:99], v[92:95], v[208:211]\n" \
  "v_add_f32 v34, v34, v38\n" \
  "v_add_f32 v35, v35, v39\n" \
  "v_add_f32 v40, v40, v44\n" \
  "v_mfma_f32_16x16x32_bf16 v[212:215], a[100:103], v[92:95], v[212:215]\n" \
  "v_add_f32 v41, v41, v45\n" \
  "v_add_f32 v42, v42, v46\n" \
  "v_add_f32 v43, v43, v47\n" \
  "v_mfma_f32_16x16x32_bf16 v[216:219], a[104:107], v[92:95], v[216:219]\n" \
  "v_add_f32 v32, v32, v40\n" \
  "v_add_f32 v33, v33, v41\n" \
  "v_add_f32 v34, v34, v42\n" \
  "v_mfma_f32_16x16x32_bf16 v[220:223], a[108:111], v[92:95], v[220:223]\n" \
  "v_add_f32 v35, v35, v43\n" \
  "v_cvt_pk_bf16_f32 v36, v32, v33\n" \
  "v_cvt_pk_bf16_f32 v37, v34, v35\n" \
  "v_mfma_f32_16x16x32_bf16 v[224:227], a[112:115], v[92:95], v[224:227]\n" \
  "s_waitcnt lgkmcnt(0)\n" \
  "v_add_f32 v48, v48, v240\n" \
  "v_add_f32 v49, v49, v241\n" \
  "v_mfma_f32_16x16x32_bf16 v[228:231], a[116:119], v[92:95], v[228:231]\n" \
  "v_add_f32 v50, v50, v242\n" \
  "v_add_f32 v51, v51, v243\n" \
  "v_add_f32 v244, v244, v248\n" \
  "v_mfma_f32_16x16x32_bf16 v[232:235], a[120:123], v[92:95], v[232:235]\n" \
  "v_add_f32 v245, v245, v249\n" \
  "v_add_f32 v246, v246, v250\n" \
  "v_add_f32 v247, v247, v251\n" \
  "v_mfma_f32_16x16x32_bf16 v[236:239], a[124:127], v[92:95], v[236:239]\n" \
  "v_add_f32 v48, v48, v244\n" \
  "v_add_f32 v49, v49, v245\n" \
  "s_waitcnt vmcnt(13)\n" \
  "v_mfma_f32_16x16x32_bf16 v[208:211], a[128:131], v[96:99], v[208:211]\n" \
  "v_add_f32 v50, v50, v246\n" \
  "v_add_f32 v51, v51, v247\n" \
  "v_cvt_pk_bf16_f32 v38, v48, v49\n" \
  "v_mfma_f32_16x16x32_bf16 v[212:215], a[132:135], v[96:99], v[212:215]\n" \
  "v_cvt_pk_bf16_f32 v39, v50, v51\n" \
  "v_add_u32 v59, 409600, v59\n" \
  "buffer_store_dwordx4 v[36:39], v59, s[36:39], s74 offen offset:0 sc1\n" \
  "v_mfma_f32_16x16x32_bf16 v[216:219], a[136:139], v[96:99], v[216:219]\n" \
  "v_mfma_f32_16x16x32_bf16 v[220:223], a[140:143], v[96:99], v[220:223]\n" \
  "v_mfma_f32_16x16x32_bf16 v[224:227], a[144:147], v[96:99], v[224:227]\n" \
  "v_mfma_f32_16x16x32_bf16 v[228:231], a[148:151], v[96:99], v[228:231]\n" \
  "v_mfma_f32_16x16x32_bf16 v[232:235], a[152:155], v[96:99], v[232:235]\n" \
  "v_mfma_f32_16x16x32_bf16 v[236:239], a[156:159], v[96:99], v[236:239]\n" \
  "v_mfma_f32_16x16x32_bf16 v[208:211], a[160:163], v[100:103], v[208:211]\n" \
  "v_mfma_f32_16x16x32_bf16 v[212:215], a[164:167], v[100:103], v[212:215]\n" \
  "v_mfma_f32_16x16x32_bf16 v[216:219], a[168:171], v[100:103], v[216:219]\n" \
  "v_mfma_f32_16x16x32_bf16 v[220:223], a[172:175], v[100:103], v[220:223]\n" \
  "v_mfma_f32_16x16x32_bf16 v[224:227], a[176:179], v[100:103], v[224:227]\n" \
  "v_mfma_f32_16x16x32_bf16 v[228:231], a[180:183], v[100:103], v[228:231]\n" \
  "v_mfma_f32_16x16x32_bf16 v[232:235], a[184:187], v[100:103], v[232:235]\n" \
  "v_mfma_f32_16x16x32_bf16 v[236:239], a[188:191], v[100:103], v[236:239]\n" \
  "s_waitcnt vmcnt(13)\n" \
  "v_mfma_f32_16x16x32_bf16 v[208:211], a[192:195], v[104:107], v[208:211]\n" \
  "v_mfma_f32_16x16x32_bf16 v[212:215], a[196:199], v[104:107], v[212:215]\n" \
  "v_mfma_f32_16x16x32_bf16 v[216:219], a[200:203], v[104:107], v[216:219]\n" \
  "v_mfma_f32_16x16x32_bf16 v[220:223], a[204:207], v[104:107], v[220:223]\n" \
  "v_mfma_f32_16x16x32_bf16 v[224:227], a[208:211], v[104:107], v[224:227]\n" \
  "v_mfma_f32_16x16x32_bf16 v[228:231], a[212:215], v[104:107], v[228:231]\n" \
  "v_mfma_f32_16x16x32_bf16 v[232:235], a[216:219], v[104:107], v[232:235]\n" \
  "v_mfma_f32_16x16x32_bf16 v[236:239], a[220:223], v[104:107], v[236:239]\n" \
  "s_branch .Le_%=\n" \
  ".Lzl_%=:\n" \
  "s_branch .Lzr_%=\n" \
  ".Lls_%=:\n" \
  "v_mfma_f32_16x16x32_bf16 v[20:23], v[64:67], a[0:3], 0\n" \
  "v_and_b32 v59, 0x3ff, v59\n" \
  "s_add_u32 s48, s68, 409600\n" \
  "s_add_u32 s49, s69, 409600\n" \
  "v_mfma_f32_16x16x32_bf16 v[24:27], v[64:67], a[4:7], 0\n" \
  "s_add_u32 s50, s70, 409600\n" \
  "s_add_u32 s100, s90, 73728\n" \
  "s_mov_b32 s35, 73728\n" \
  "v_mfma_f32_16x16x32_bf16 v[28:31], v[64:67], a[8:11], 0\n" \
  "ds_write_addtid_b32 v0 offset:0\n" \
  "ds_write_addtid_b32 v1 offset:544\n" \
  "v_mfma_f32_16x16x32_bf16 v[32:35], v[64:67], a[12:15], 0\n" \
  "ds_write_addtid_b32 v2 offset:256\n" \
  "ds_write_addtid_b32 v3 offset:800\n" \
  "v_mfma_f32_16x16x32_bf16 v[20:23], v[68:71], a[20:23], v[20:23]\n" \
  "ds_write_addtid_b32 v4 offset:1056\n" \
  "ds_write_addtid_b32 v5 offset:1600\n" \
  "v_mfma_f32_16x16x32_bf16 v[24:27], v[68:71], a[24:27], v[24:27]\n" \
  "ds_write_addtid_b32 v6 offset:1312\n" \
  "ds_write_addtid_b32 v7 offset:1856\n" \
  "v_mfma_f32_16x16x32_bf16 v[28:31], v[68:71], a[28:31], v[28:31]\n" \
  "ds_write_addtid_b32 v8 offset:2112\n" \
  "ds_write_addtid_b32 v9 offset:2656\n" \
  "v_mfma_f32_16x16x32_bf16 v[32:35], v[68:71], a[32:35], v[32:35]\n" \
  "ds_write_addtid_b32 v10 offset:2368\n" \
  "ds_write_addtid_b32 v11 offset:2912\n" \
  "s_waitcnt vmcnt(16)\n" \
  "v_mfma_f32_16x16x32_bf16 v[20:23], v[72:75], a[40:43], v[20:23]\n" \
  "s_waitcnt vmcnt(7)\n" \
  "v_cmp_eq_u32 vcc, 0, v63\n" \
  "v_add_u32 v71, s57, v59\n" \
  "v_mfma_f32_16x16x32_bf16 v[24:27], v[72:75], a[44:47], v[24:27]\n" \
  "ds_read_b32 v64, v71 offset:28672\n" \
  "ds_read_b64 v[68:69], v71 offset:29696\n" \
  "v_mfma_f32_16x16x32_bf16 v[28:31], v[72:75], a[48:51], v[28:31]\n" \
  "s_and_b64 vcc, vcc, s[86:87]\n" \
  "s_cbranch_scc1 .LsaS_%=\n" \
  ".LbaS_%=:\n" \
  "ds_write_addtid_b32 v12 offset:3168\n" \
  "v_mfma_f32_16x16x32_bf16 v[32:35], v[72:75], a[52:55], v[32:35]\n" \
  "ds_write_addtid_b32 v13 offset:3712\n" \
  "ds_write_addtid_b32 v14 offset:3424\n" \
  "s_waitcnt vmcnt(15)\n" \
  "v_mfma_f32_16x16x32_bf16 v[20:23], v[76:79], a[60:63], v[20:23]\n" \
  "buffer_load_dwordx4 v[72:75], v59, s[36:39], s68 offen offset:0 sc1\n" \
  "ds_write_addtid_b32 v15 offset:3968\n" \
  "ds_write_addtid_b32 v16 offset:4224\n" \
  "v_mfma_f32_16x16x32_bf16 v[24:27], v[76:79], a[64:67], v[24:27]\n" \
  "s_waitcnt lgkmcnt(5)\n" \
  "v_xor_b32 v65, 32, v64\n" \
  "v_xor_b32 v66, 64, v64\n" \
  "v_mfma_f32_16x16x32_bf16 v[28:31], v[76:79], a[68:71], v[28:31]\n" \
  "v_xor_b32 v67, 96, v64\n" \
  "v_add_u32 v70, 0xf800, v68\n" \
  "v_add_u32 v71, 0xf800, v69\n" \
  "v_mfma_f32_16x16x32_bf16 v[32:35], v[76:79], a[72:75], v[32:35]\n" \
  "ds_write_addtid_b32 v17 offset:4512\n" \
  "ds_write_addtid_b32 v18 offset:4800\n" \
  "v_mfma_f32_16x16x32_bf16 v[20:23], v[80:83], a[80:83], v[20:23]\n" \
  "buffer_load_dwordx4 v[76:79], v59, s[36:39], s68 offen offset:1024 sc1\n" \
  "ds_write_addtid_b32 v19 offset:5088\n" \
  "ds_read_b64_tr_b16 a[0:1], v64 offset:0\n" \
  "v_mfma_f32_16x16x32_bf16 v[24:27], v[80:83], a[84:87], v[24:27]\n" \
  "ds_read_b64_tr_b16 a[2:3], v64 offset:512\n" \
  "ds_read_b64_tr_b16 a[4:5], v65 offset:0\n" \
  "v_mfma_f32_16x16x32_bf16 v[28:31], v[80:83], a[88:91], v[28:31]\n" \
  "ds_read_b64_tr_b16 a[6:7], v65 offset:512\n" \
  "ds_read_b64_tr_b16 a[8:9], v66 offset:0\n" \
  "v_mfma_f32_16x16x32_bf16 v[32:35], v[80:83], a[92:95], v[32:35]\n" \
  "ds_read_b64_tr_b16 a[10:11], v66 offset:512\n" \
  "ds_read_b64_tr_b16 a[12:13], v67 offset:0\n" \
  "v_mfma_f32_16x16x32_bf16 v[20:23], v[84:87], a[100:103], v[20:23]\n" \
  "buffer_load_dwordx4 v[80:83], v59, s[36:39], s68 offen offset:2048 sc1\n" \
  "ds_read_b64_tr_b16 a[14:15], v67 offset:512\n" \
  "v_mfma_f32_16x16x32_bf16 v[24:27], v[84:87], a[104:107], v[24:27]\n" \
  "v_rcp_f32 v42, v42\n" \
  "v_mfma_f32_16x16x32_bf16 v[28:31], v[84:87], a[108:111], v[28:31]\n" \
  "s_waitcnt lgkmcnt(0)\n" \
  "s_barrier\n" \
  "v_mfma_f32_16x16x32_bf16 v[32:35], v[84:87], a[112:115], v[32:35]\n" \
  "v_rcp_f32 v43, v43\n" \
  "v_mfma_f32_16x16x32_bf16 v[20:23], v[88:91], a[120:123], v[20:23]\n" \
  "buffer_load_dwordx4 v[84:87], v59, s[36:39], s69 offen offset:0 sc1\n" \
  "s_add_u32 m0, s57, 0\n" \
  "v_mfma_f32_16x16x32_bf16 v[24:27], v[88:91], a[124:127], v[24:27]\n" \
  "ds_read_b64_tr_b16 a[16:17], v64 offset:4096\n" \
  "ds_read_b64_tr_b16 a[18:19], v64 offset:4608\n" \
  "v_mul_f32 v36, v36, v40\n" \
  "v_mfma_f32_16x16x32_bf16 v[28:31], v[88:91], a[128:131], v[28:31]\n" \
  "ds_read_b64_tr_b16 a[20:21], v65 offset:4096\n" \
  "ds_read_b64_tr_b16 a[22:23], v65 offset:4608\n" \
  "v_mul_f32 v37, v37, v41\n" \
  "v_mfma_f32_16x16x32_bf16 v[32:35], v[88:91], a[132:135], v[32:35]\n" \
  "buffer_load_dwordx4 v68, s[40:43], s60 offen offset:0 lds\n" \
  "ds_read_b64_tr_b16 a[24:25], v66 offset:4096\n" \
  "v_mfma_f32_16x16x32_bf16 v[20:23], v[92:95], a[140:143], v[20:23]\n" \
  "ds_read_b64_tr_b16 a[26:27], v66 offset:4608\n" \
  "ds_read_b64_tr_b16 a[28:29], v67 offset:4096\n" \
  "v_mul_f32 v38, v38, v42\n" \
  "v_mfma_f32_16x16x32_bf16 v[24:27], v[92:95], a[144:147], v[24:27]\n" \
  "ds_read_b64_tr_b16 a[30:31], v67 offset:4608\n" \
  "v_mul_f32 v39, v39, v43\n" \
  "v_cvt_pk_bf16_f32 v40, v36, v37\n" \
  "v_mfma_f32_16x16x32_bf16 v[28:31], v[92:95], a[148:151], v[28:31]\n" \
  "buffer_load_dwordx4 v69, s[40:43], s60 offen offset:1024 lds\n" \
  "v_cvt_pk_bf16_f32 v41, v38, v39\n" \
  "buffer_store_dwordx2 v[40:41], v61, s[36:39], s52 offen offset:0 sc1\n" \
  "v_mfma_f32_16x16x32_bf16 v[32:35], v[92:95], a[152:155], v[32:35]\n" \
  "ds_read_b128 v[36:39], v58 offset:0\n" \
  "ds_read_b128 v[40:43], v58 offset:9568\n" \
  "v_mfma_f32_16x16x32_bf16 v[20:23], v[96:99], a[160:163], v[20:23]\n" \
  "ds_read_b128 v[44:47], v58 offset:19136\n" \
  "ds_read_b128 v[48:51], v58 offset:28704\n" \
  "s_add_u32 s52, s52, 409600\n" \
  "v_mfma_f32_16x16x32_bf16 v[24:27], v[96:99], a[164:167], v[24:27]\n" \
  "buffer_load_dwordx4 v70, s[40:43], s60 offen offset:2048 lds\n" \
  "v_mfma_f32_16x16x32_bf16 v[28:31], v[96:99], a[168:171], v[28:31]\n" \
  "ds_read_b64_tr_b16 a[32:33], v64 offset:8192\n" \
  "ds_read_b64_tr_b16 a[34:35], v64 offset:8704\n" \
  "v_mfma_f32_16x16x32_bf16 v[32:35], v[96:99], a[172:175], v[32:35]\n" \
  "ds_read_b64_tr_b16 a[36:37], v65 offset:8192\n" \
  "ds_read_b64_tr_b16 a[38:39], v65 offset:8704\n" \
  "s_waitcnt vmcnt(14)\n" \
  "v_mfma_f32_16x16x32_bf16 v[20:23], v[100:103], a[180:183], v[20:23]\n" \
  "buffer_load_dwordx4 v71, s[40:43], s60 offen offset:3072 lds\n" \
  "ds_read_b64_tr_b16 a[40:41], v66 offset:8192\n" \
  "ds_read_b64_tr_b16 a[42:43], v66 offset:8704\n" \
  "v_mfma_f32_16x16x32_bf16 v[24:27], v[100:103], a[184:187], v[24:27]\n" \
  "ds_read_b64_tr_b16 a[44:45], v67 offset:8192\n" \
  "ds_read_b64_tr_b16 a[46:47], v67 offset:8704\n" \
  "v_mfma_f32_16x16x32_bf16 v[28:31], v[100:103], a[188:191], v[28:31]\n" \
  "v_mfma_f32_16x16x32_bf16 v[32:35], v[100:103], a[192:195], v[32:35]\n" \
  "buffer_load_dwordx4 v[88:91], v59, s[36:39], s69 offen offset:1024 sc1\n" \
  "s_waitcnt lgkmcnt(10)\n" \
  "s_waitcnt vmcnt(15)\n" \
  "v_mfma_f32_16x16x32_bf16 v[20:23], v[104:107], a[200:203], v[20:23]\n" \
  "v_add_f32 v36, v36, v40\n" \
  "v_add_f32 v37, v37, v41\n" \
  "v_add_f32 v38, v38, v42\n" \
  "v_mfma_f32_16x16x32_bf16 v[24:27], v[104:107], a[204:207], v[24:27]\n" \
  "v_add_f32 v39, v39, v43\n" \
  "v_add_f32 v36, v36, v52\n" \
  "v_add_f32 v37, v37, v53\n" \
  "v_mfma_f32_16x16x32_bf16 v[28:31], v[104:107], a[208:211], v[28:31]\n" \
  "buffer_load_dwordx4 v[92:95], v59, s[36:39], s69 offen offset:2048 sc1\n" \
  "s_add_u32 m0, s57, 4096\n" \
  "v_mfma_f32_16x16x32_bf16 v[32:35], v[104:107], a[212:215], v[32:35]\n" \
  "v_add_f32 v38, v38, v54\n" \
  "v_add_f32 v39, v39, v55\n" \
  "s_waitcnt vmcnt(15)\n" \
  "v_mfma_f32_16x16x32_bf16 v[20:23], v[108:111], a[220:223], v[20:23]\n" \
  "ds_read_b64_tr_b16 a[48:49], v64 offset:12288\n" \
  "ds_read_b64_tr_b16 a[50:51], v64 offset:12800\n" \
  "s_waitcnt lgkmcnt(10)\n" \
  "v_mfma_f32_16x16x32_bf16 v[24:27], v[108:111], a[224:227], v[24:27]\n" \
  "buffer_load_dwordx4 v68, s[40:43], s61 offen offset:0 lds\n" \
  "ds_read_b64_tr_b16 a[52:53], v65 offset:12288\n" \
  "ds_read_b64_tr_b16 a[54:55], v65 offset:12800\n" \
  "v_mfma_f32_16x16x32_bf16 v[28:31], v[108:111], a[228:231], v[28:31]\n" \
  "ds_read_b64_tr_b16 a[56:57], v66 offset:12288\n" \
  "ds_read_b64_tr_b16 a[58:59], v66 offset:12800\n" \
  "v_add_f32 v44, v44, v48\n" \
  "v_mfma_f32_16x16x32_bf16 v[32:35], v[108:111], a[232:235], v[32:35]\n" \
  "ds_read_b64_tr_b16 a[60:61], v67 offset:12288\n" \
  "ds_read_b64_tr_b16 a[62:63], v67 offset:12800\n" \
  "s_waitcnt vmcnt(15)\n" \
  "v_mfma_f32_16x16x32_bf16 v[20:23], v[112:115], a[240:243], v[20:23]\n" \
  "buffer_load_dwordx4 v69, s[40:43], s61 offen offset:1024 lds\n" \
  "v_add_f32 v45, v45, v49\n" \
  "v_add_f32 v46, v46, v50\n" \
  "v_mfma_f32_16x16x32_bf16 v[24:27], v[112:115], a[244:247], v[24:27]\n" \
  "v_add_f32 v47, v47, v51\n" \
  "v_add_f32 v36, v36, v44\n" \
  "v_add_f32 v37, v37, v45\n" \
  "v_mfma_f32_16x16x32_bf16 v[28:31], v[112:115], a[248:251], v[28:31]\n" \
  "v_add_f32 v38, v38, v46\n" \
  "v_add_f32 v39, v39, v47\n" \
  "v_mul_f32 v40, v36, v36\n" \
  "v_mfma_f32_16x16x32_bf16 v[32:35], v[112:115], a[252:255], v[32:35]\n" \
  "buffer_load_dwordx4 v70, s[40:43], s61 offen offset:2048 lds\n" \
  "v_mul_f32 v41, v37, v37\n" \
  "s_waitcnt vmcnt(16)\n" \
  "v_mfma_f32_16x16x32_bf16 v[20:23], v[116:119], v[196:199], v[20:23]\n" \
  "v_mul_f32 v42, v38, v38\n" \
  "v_mul_f32 v43, v39, v39\n" \
  "v_fma_f32 v40, v40, s55, v57\n" \
  "v_mfma_f32_16x16x32_bf16 v[24:27], v[116:119], v[200:203], v[24:27]\n" \
  "v_fma_f32 v41, v41, s55, v57\n" \
  "v_fma_f32 v42, v42, s55, v57\n" \
  "v_fma_f32 v43, v43, s55, v57\n" \
  "v_mfma_f32_16x16x32_bf16 v[28:31], v[116:119], v[204:207], v[28:31]\n" \
  "buffer_load_dwordx4 v71, s[40:43], s61 offen offset:3072 lds\n" \
  "ds_read_b64_tr_b16 a[64:65], v64 offset:16384\n" \
  "ds_read_b64_tr_b16 a[66:67], v64 offset:16896\n" \
  "v_mfma_f32_16x16x32_bf16 v[32:35], v[116:119], v[208:211], v[32:35]\n" \
  "ds_read_b64_tr_b16 a[68:69], v65 offset:16384\n" \
  "ds_read_b64_tr_b16 a[70:71], v65 offset:16896\n" \
  "s_waitcnt vmcnt(16)\n" \
  "v_mfma_f32_16x16x32_bf16 v[20:23], v[120:123], v[216:219], v[20:23]\n" \
  "ds_read_b64_tr_b16 a[72:73], v66 offset:16384\n" \
  "ds_read_b64_tr_b16 a[74:75], v66 offset:16896\n" \
  "v_mul_f32 v40, v36, v40\n" \
  "v_mfma_f32_16x16x32_bf16 v[24:27], v[120:123], v[220:223], v[24:27]\n" \
  "buffer_load_dwordx4 v[108:111], v59, s[36:39], s48 offen offset:0 sc1\n" \
  "ds_read_b64_tr_b16 a[76:77], v67 offset:16384\n" \
  "ds_read_b64_tr_b16 a[78:79], v67 offset:16896\n" \
  "v_mfma_f32_16x16x32_bf16 v[28:31], v[120:123], v[224:227], v[28:31]\n" \
  "v_mul_f32 v41, v37, v41\n" \
  "v_mul_f32 v42, v38, v42\n" \
  "v_mul_f32 v43, v39, v43\n" \
  "v_mfma_f32_16x16x32_bf16 v[32:35], v[120:123], v[228:231], v[32:35]\n" \
  "v_exp_f32 v40, v40\n" \
  "s_waitcnt vmcnt(16)\n" \
  "v_mfma_f32_16x16x32_bf16 v[20:23], v[124:127], v[236:239], v[20:23]\n" \
  "buffer_load_dwordx4 v[112:115], v59, s[36:39], s48 offen offset:1024 sc1\n" \
  "v_exp_f32 v41, v41\n" \
  "v_mfma_f32_16x16x32_bf16 v[24:27], v[124:127], v[240:243], v[24:27]\n" \
  "v_exp_f32 v42, v42\n" \
  "ds_read_b32 v48, v60 offset:0\n" \
  "v_mfma_f32_16x16x32_bf16 v[28:31], v[124:127], v[244:247], v[28:31]\n" \
  "v_exp_f32 v43, v43\n" \
  "v_add_f32 v40, 1.0, v40\n" \
  "v_mfma_f32_16x16x32_bf16 v[32:35], v[124:127], v[248:251], v[32:35]\n" \
  "buffer_load_dwordx4 v[116:119], v59, s[36:39], s48 offen offset:2048 sc1\n" \
  "s_mov_b32 m0, s56\n" \
  "s_waitcnt vmcnt(17)\n" \
  "v_mfma_f32_16x16x32_bf16 v[208:211], v[128:131], v[72:75], 0\n" \
  "ds_read_b64_tr_b16 a[80:81], v64 offset:20480\n" \
  "ds_read_b64_tr_b16 a[82:83], v64 offset:20992\n" \
  "v_add_f32 v41, 1.0, v41\n" \
  "v_mfma_f32_16x16x32_bf16 v[212:215], v[132:135], v[72:75], 0\n" \
  "ds_write_addtid_b32 v20 offset:5344\n" \
  "ds_write_addtid_b32 v21 offset:5888\n" \
  "v_add_f32 v42, 1.0, v42\n" \
  "v_mfma_f32_16x16x32_bf16 v[216:219], v[136:139], v[72:75], 0\n" \
  "buffer_load_dwordx4 v[120:123], v59, s[36:39], s49 offen offset:0 sc1\n" \
  "ds_write_addtid_b32 v22 offset:5600\n" \
  "ds_write_addtid_b32 v23 offset:6144\n" \
  "v_mfma_f32_16x16x32_bf16 v[220:223], v[140:143], v[72:75], 0\n" \
  "ds_write_addtid_b32 v24 offset:6400\n" \
  "ds_write_addtid_b32 v25 offset:6944\n" \
  "v_add_f32 v43, 1.0, v43\n" \
  "v_mfma_f32_16x16x32_bf16 v[224:227], v[144:147], v[72:75], 0\n" \
  "ds_write_addtid_b32 v26 offset:6656\n" \
  "ds_write_addtid_b32 v27 offset:7200\n" \
  "v_rcp_f32 v40, v40\n" \
  "v_mfma_f32_16x16x32_bf16 v[228:231], v[148:151], v[72:75], 0\n" \
  "buffer_load_dwordx4 v[124:127], v59, s[36:39], s49 offen offset:1024 sc1\n" \
  "ds_write_addtid_b32 v28 offset:7456\n" \
  "ds_write_addtid_b32 v29 offset:8000\n" \
  "v_mfma_f32_16x16x32_bf16 v[232:235], v[152:155], v[72:75], 0\n" \
  "ds_write_addtid_b32 v30 offset:7712\n" \
  "ds_write_addtid_b32 v31 offset:8256\n" \
  "v_rcp_f32 v41, v41\n" \
  "v_mfma_f32_16x16x32_bf16 v[236:239], v[156:159], v[72:75], 0\n" \
  "ds_write_addtid_b32 v32 offset:8512\n" \
  "ds_write_addtid_b32 v33 offset:9056\n" \
  "s_waitcnt vmcnt(18)\n" \
  "v_mfma_f32_16x16x32_bf16 v[208:211], v[160:163], v[76:79], v[208:211]\n" \
  "v_rcp_f32 v42, v42\n" \
  "v_rcp_f32 v43, v43\n" \
  "v_mul_f32 v36, v36, v40\n" \
  "v_mul_f32 v37, v37, v41\n" \
  "v_mul_f32 v38, v38, v42\n" \
  "v_mul_f32 v39, v39, v43\n" \
  "v_cvt_pk_bf16_f32 v40, v36, v37\n" \
  "v_cvt_pk_bf16_f32 v41, v38, v39\n" \
  "buffer_store_dwordx2 v[40:41], v61, s[36:39], s52 offen offset:0 sc1\n" \
  "v_mfma_f32_16x16x32_bf16 v[212:215], v[164:167], v[76:79], v[212:215]\n" \
  "buffer_load_dwordx4 v[192:195], v59, s[36:39], s49 offen offset:2048 sc1\n" \
  "ds_write_addtid_b32 v34 offset:8768\n" \
  "ds_write_addtid_b32 v35 offset:9312\n" \
  "v_mfma_f32_16x16x32_bf16 v[216:219], v[168:171], v[76:79], v[216:219]\n" \
  "ds_read_b64_tr_b16 a[84:85], v65 offset:20480\n" \
  "ds_read_b64_tr_b16 a[86:87], v65 offset:20992\n" \
  "v_mfma_f32_16x16x32_bf16 v[220:223], v[172:175], v[76:79], v[220:223]\n" \
  "ds_read_b64_tr_b16 a[88:89], v66 offset:20480\n" \
  "ds_read_b64_tr_b16 a[90:91], v66 offset:20992\n" \
  "v_mfma_f32_16x16x32_bf16 v[224:227], v[176:179], v[76:79], v[224:227]\n" \
  "buffer_load_dwordx4 v[96:99], v59, s[36:39], s70 offen offset:0 sc1\n" \
  "s_waitcnt lgkmcnt(15)\n" \
  "s_add_u32 m0, s57, 8192\n" \
  "v_mfma_f32_16x16x32_bf16 v[228:231], v[180:183], v[76:79], v[228:231]\n" \
  "ds_read_b64_tr_b16 a[92:93], v67 offset:20480\n" \
  "ds_read_b64_tr_b16 a[94:95], v67 offset:20992\n" \
  "v_mfma_f32_16x16x32_bf16 v[232:235], v[184:187], v[76:79], v[232:235]\n" \
  "ds_read_b64_tr_b16 a[96:97], v64 offset:24576\n" \
  "ds_read_b64_tr_b16 a[98:99], v64 offset:25088\n" \
  "v_mfma_f32_16x16x32_bf16 v[236:239], v[188:191], v[76:79], v[236:239]\n" \
  "buffer_load_dwordx4 v68, s[40:43], s62 offen offset:0 lds\n" \
  "s_waitcnt lgkmcnt(15)\n" \
  "s_waitcnt vmcnt(21)\n" \
  "v_mfma_f32_16x16x32_bf16 v[208:211], a[0:3], v[80:83], v[208:211]\n" \
  "s_waitcnt lgkmcnt(0)\n" \
  "s_barrier\n" \
  "ds_read_b64_tr_b16 a[100:101], v65 offset:24576\n" \
  "v_mfma_f32_16x16x32_bf16 v[212:215], a[4:7], v[80:83], v[212:215]\n" \
  "ds_read_b64_tr_b16 a[102:103], v65 offset:25088\n" \
  "ds_read_b64_tr_b16 a[104:105], v66 offset:24576\n" \
  "v_mfma_f32_16x16x32_bf16 v[216:219], a[8:11], v[80:83], v[216:219]\n" \
  "buffer_load_dwordx4 v69, s[40:43], s62 offen offset:1024 lds\n" \
  "ds_read_b64_tr_b16 a[106:107], v66 offset:25088\n" \
  "ds_read_b64_tr_b16 a[108:109], v67 offset:24576\n" \
  "v_mfma_f32_16x16x32_bf16 v[220:223], a[12:15], v[80:83], v[220:223]\n" \
  "ds_read_b64_tr_b16 a[110:111], v67 offset:25088\n" \
  "ds_read_b32 v49, v60 offset:9568\n" \
  "v_mfma_f32_16x16x32_bf16 v[224:227], a[16:19], v[80:83], v[224:227]\n" \
  "ds_read_b32 v50, v60 offset:19136\n" \
  "ds_read_b32 v51, v60 offset:28704\n" \
  "v_mfma_f32_16x16x32_bf16 v[228:231], a[20:23], v[80:83], v[228:231]\n" \
  "buffer_load_dwordx4 v70, s[40:43], s62 offen offset:2048 lds\n" \
  "v_mfma_f32_16x16x32_bf16 v[232:235], a[24:27], v[80:83], v[232:235]\n" \
  "s_waitcnt vmcnt(17)\n" \
  "ds_read_b64_tr_b16 a[112:113], v64 offset:0\n" \
  "ds_read_b64_tr_b16 a[114:115], v64 offset:512\n" \
  "v_mfma_f32_16x16x32_bf16 v[236:239], a[28:31], v[80:83], v[236:239]\n" \
  "ds_read_b64_tr_b16 a[116:117], v65 offset:0\n" \
  "v_mfma_f32_16x16x32_bf16 v[208:211], a[32:35], v[84:87], v[208:211]\n" \
  "buffer_load_dwordx4 v71, s[40:43], s62 offen offset:3072 lds\n" \
  "s_add_u32 m0, s57, 12288\n" \
  "v_mfma_f32_16x16x32_bf16 v[212:215], a[36:39], v[84:87], v[212:215]\n" \
  "ds_read_b64_tr_b16 a[118:119], v65 offset:512\n" \
  "ds_read_b64_tr_b16 a[120:121], v66 offset:0\n" \
  "v_mfma_f32_16x16x32_bf16 v[216:219], a[40:43], v[84:87], v[216:219]\n" \
  "ds_read_b64_tr_b16 a[122:123], v66 offset:512\n" \
  "ds_read_b64_tr_b16 a[124:125], v67 offset:0\n" \
  "s_waitcnt lgkmcnt(7)\n" \
  "v_mfma_f32_16x16x32_bf16 v[220:223], a[44:47], v[84:87], v[220:223]\n" \
  "buffer_load_dwordx4 v68, s[40:43], s63 offen offset:0 lds\n" \
  "ds_read_b64_tr_b16 a[126:127], v67 offset:512\n" \
  "v_mfma_f32_16x16x32_bf16 v[224:227], a[48:51], v[84:87], v[224:227]\n" \
  "v_add_f32 v48, v48, v49\n" \
  "v_add_f32 v50, v50, v51\n" \
  "v_add_f32 v48, v48, v56\n" \
  "v_mfma_f32_16x16x32_bf16 v[228:231], a[52:55], v[84:87], v[228:231]\n" \
  "v_add_f32 v48, v48, v50\n" \
  "v_mul_f32 v49, v48, v48\n" \
  "v_fma_f32 v49, v49, s55, v57\n" \
  "v_mfma_f32_16x16x32_bf16 v[232:235], a[56:59], v[84:87], v[232:235]\n" \
  "buffer_load_dwordx4 v69, s[40:43], s63 offen offset:1024 lds\n" \
  "v_mul_f32 v49, v48, v49\n" \
  "v_mfma_f32_16x16x32_bf16 v[236:239], a[60:63], v[84:87], v[236:239]\n" \
  "s_waitcnt vmcnt(19)\n" \
  "v_mfma_f32_16x16x32_bf16 v[208:211], a[64:67], v[88:91], v[208:211]\n" \
  "v_exp_f32 v49, v49\n" \
  "v_mfma_f32_16x16x32_bf16 v[212:215], a[68:71], v[88:91], v[212:215]\n" \
  "buffer_load_dwordx4 v70, s[40:43], s63 offen offset:2048 lds\n" \
  "v_add_f32 v49, 1.0, v49\n" \
  "v_mfma_f32_16x16x32_bf16 v[216:219], a[72:75], v[88:91], v[216:219]\n" \
  "v_rcp_f32 v49, v49\n" \
  "v_mfma_f32_16x16x32_bf16 v[220:223], a[76:79], v[88:91], v[220:223]\n" \
  "v_mul_f32 v48, v48, v49\n" \
  "v_cvt_pk_bf16_f32 v49, v48, v48\n" \
  "v_mfma_f32_16x16x32_bf16 v[224:227], a[80:83], v[88:91], v[224:227]\n" \
  "buffer_load_dwordx4 v71, s[40:43], s63 offen offset:3072 lds\n" \
  "buffer_store_short v49, v62, s[36:39], s52 offen offset:0 sc1\n" \
  "ds_read_b128 v[36:39], v58 offset:5344\n" \
  "v_mfma_f32_16x16x32_bf16 v[228:231], a[84:87], v[88:91], v[228:231]\n" \
  "ds_read_b128 v[40:43], v58 offset:14912\n" \
  "ds_read_b128 v[44:47], v58 offset:24480\n" \
  "s_add_u32 s52, s52, 409600\n" \
  "v_mfma_f32_16x16x32_bf16 v[232:235], a[88:91], v[88:91], v[232:235]\n" \
  "ds_read_b128 v[48:51], v58 offset:34048\n" \
  "v_mfma_f32_16x16x32_bf16 v[236:239], a[92:95], v[88:91], v[236:239]\n" \
  "buffer_load_dwordx4 v[100:103], v59, s[36:39], s70 offen offset:1024 sc1\n" \
  "s_waitcnt vmcnt(22)\n" \
  "v_mfma_f32_16x16x32_bf16 v[208:211], a[96:99], v[92:95], v[208:211]\n" \
  "s_add_u32 m0, s57, 16384\n" \
  "s_waitcnt vmcnt(18)\n" \
  "v_mfma_f32_16x16x32_bf16 v[212:215], a[100:103], v[92:95], v[212:215]\n" \
  "ds_read_b64_tr_b16 a[128:129], v64 offset:4096\n" \
  "ds_read_b64_tr_b16 a[130:131], v64 offset:4608\n" \
  "v_mfma_f32_16x16x32_bf16 v[216:219], a[104:107], v[92:95], v[216:219]\n" \
  "buffer_load_dwordx4 v68, s[40:43], s64 offen offset:0 lds\n" \
  "ds_read_b64_tr_b16 a[132:133], v65 offset:4096\n" \
  "ds_read_b64_tr_b16 a[134:135], v65 offset:4608\n" \
  "v_mfma_f32_16x16x32_bf16 v[220:223], a[108:111], v[92:95], v[220:223]\n" \
  "ds_read_b64_tr_b16 a[136:137], v66 offset:4096\n" \
  "ds_read_b64_tr_b16 a[138:139], v66 offset:4608\n" \
  "s_waitcnt vmcnt(18)\n" \
  "v_mfma_f32_16x16x32_bf16 v[0:3], v[128:131], v[108:111], 0\n" \
  "ds_read_b64_tr_b16 a[140:141], v67 offset:4096\n" \
  "ds_read_b64_tr_b16 a[142:143], v67 offset:4608\n" \
  "v_mfma_f32_16x16x32_bf16 v[4:7], v[132:135], v[108:111], 0\n" \
  "buffer_load_dwordx4 v69, s[40:43], s64 offen offset:1024 lds\n" \
  "s_waitcnt lgkmcnt(10)\n" \
  "v_add_f32 v36, v36, v40\n" \
  "v_mfma_f32_16x16x32_bf16 v[8:11], v[136:139], v[108:111], 0\n" \
  "v_add_f32 v37, v37, v41\n" \
  "v_add_f32 v38, v38, v42\n" \
  "v_add_f32 v39, v39, v43\n" \
  "v_mfma_f32_16x16x32_bf16 v[12:15], v[140:143], v[108:111], 0\n" \
  "v_add_f32 v36, v36, v52\n" \
  "v_add_f32 v37, v37, v53\n" \
  "v_add_f32 v38, v38, v54\n" \
  "v_mfma_f32_16x16x32_bf16 v[16:19], v[144:147], v[108:111], 0\n" \
  "buffer_load_dwordx4 v70, s[40:43], s64 offen offset:2048 lds\n" \
  "v_add_f32 v39, v39, v55\n" \
  "s_waitcnt lgkmcnt(8)\n" \
  "v_mfma_f32_16x16x32_bf16 v[20:23], v[148:151], v[108:111], 0\n" \
  "v_add_f32 v44, v44, v48\n" \
  "v_add_f32 v45, v45, v49\n" \
  "v_add_f32 v46, v46, v50\n" \
  "v_mfma_f32_16x16x32_bf16 v[24:27], v[152:155], v[108:111], 0\n" \
  "v_add_f32 v47, v47, v51\n" \
  "v_add_f32 v36, v36, v44\n" \
  "v_add_f32 v37, v37, v45\n" \
  "v_mfma_f32_16x16x32_bf16 v[28:31], v[156:159], v[108:111], 0\n" \
  "buffer_load_dwordx4 v71, s[40:43], s64 offen offset:3072 lds\n" \
  "v_add_f32 v38, v38, v46\n" \
  "s_waitcnt vmcnt(20)\n" \
  "v_mfma_f32_16x16x32_bf16 v[0:3], v[160:163], v[112:115], v[0:3]\n" \
  "s_add_u32 m0, s57, 20480\n" \
  "v_add_f32 v39, v39, v47\n" \
  "v_mfma_f32_16x16x32_bf16 v[4:7], v[164:167], v[112:115], v[4:7]\n" \
  "v_mul_f32 v40, v36, v36\n" \
  "v_mul_f32 v41, v37, v37\n" \
  "v_mul_f32 v42, v38, v38\n" \
  "v_mfma_f32_16x16x32_bf16 v[8:11], v[168:171], v[112:115], v[8:11]\n" \
  "buffer_load_dwordx4 v68, s[40:43], s65 offen offset:0 lds\n" \
  "v_mul_f32 v43, v39, v39\n" \
  "v_fma_f32 v40, v40, s55, v57\n" \
  "v_mfma_f32_16x16x32_bf16 v[12:15], v[172:175], v[112:115], v[12:15]\n" \
  "v_fma_f32 v41, v41, s55, v57\n" \
  "v_fma_f32 v42, v42, s55, v57\n" \
  "v_fma_f32 v43, v43, s55, v57\n" \
  "v_mfma_f32_16x16x32_bf16 v[16:19], v[176:179], v[112:115], v[16:19]\n" \
  "v_mul_f32 v40, v36, v40\n" \
  "v_mul_f32 v41, v37, v41\n" \
  "v_mul_f32 v42, v38, v42\n" \
  "v_mfma_f32_16x16x32_bf16 v[20:23], v[180:183], v[112:115], v[20:23]\n" \
  "buffer_load_dwordx4 v69, s[40:43], s65 offen offset:1024 lds\n" \
  "v_mul_f32 v43, v39, v43\n" \
  "v_mfma_f32_16x16x32_bf16 v[24:27], v[184:187], v[112:115], v[24:27]\n" \
  "v_exp_f32 v40, v40\n" \
  "v_mfma_f32_16x16x32_bf16 v[28:31], v[188:191], v[112:115], v[28:31]\n" \
  "v_exp_f32 v41, v41\n" \
  "s_waitcnt vmcnt(21)\n" \
  "v_mfma_f32_16x16x32_bf16 v[0:3], a[0:3], v[116:119], v[0:3]\n" \
  "buffer_load_dwordx4 v70, s[40:43], s65 offen offset:2048 lds\n" \
  "v_exp_f32 v42, v42\n" \
  "v_mfma_f32_16x16x32_bf16 v[4:7], a[4:7], v[116:119], v[4:7]\n" \
  "v_exp_f32 v43, v43\n" \
  "v_add_f32 v40, 1.0, v40\n" \
  "v_add_f32 v41, 1.0, v41\n" \
  "v_mfma_f32_16x16x32_bf16 v[8:11], a[8:11], v[116:119], v[8:11]\n" \
  "v_add_f32 v42, 1.0, v42\n" \
  "v_add_f32 v43, 1.0, v43\n" \
  "v_rcp_f32 v40, v40\n" \
  "v_mfma_f32_16x16x32_bf16 v[12:15], a[12:15], v[116:119], v[12:15]\n" \
  "buffer_load_dwordx4 v71, s[40:43], s65 offen offset:3072 lds\n" \
  "v_rcp_f32 v41, v41\n" \
  "v_mfma_f32_16x16x32_bf16 v[16:19], a[16:19], v[116:119], v[16:19]\n" \
  "v_rcp_f32 v42, v42\n" \
  "v_mfma_f32_16x16x32_bf16 v[20:23], a[20:23], v[116:119], v[20:23]\n" \
  "v_rcp_f32 v43, v43\n" \
  "v_mul_f32 v36, v36, v40\n" \
  "v_mfma_f32_16x16x32_bf16 v[24:27], a[24:27], v[116:119], v[24:27]\n" \
  "v_mul_f32 v37, v37, v41\n" \
  "v_mul_f32 v38, v38, v42\n" \
  "v_mul_f32 v39, v39, v43\n" \
  "v_cvt_pk_bf16_f32 v40, v36, v37\n" \
  "v_cvt_pk_bf16_f32 v41, v38, v39\n" \
  "v_mfma_f32_16x16x32_bf16 v[28:31], a[28:31], v[116:119], v[28:31]\n" \
  "buffer_load_dwordx4 v[104:107], v59, s[36:39], s70 offen offset:2048 sc1\n" \
  "v_and_b32 v58, 0x3ff, v59\n" \
  "s_waitcnt vmcnt(23)\n" \
  "v_mfma_f32_16x16x32_bf16 v[0:3], a[32:35], v[120:123], v[0:3]\n" \
  "s_add_u32 m0, s57, 24576\n" \
  "v_add_u32 v58, s99, v58\n" \
  "v_mfma_f32_16x16x32_bf16 v[4:7], a[36:39], v[120:123], v[4:7]\n" \
  "v_add_u32 v46, s98, v58\n" \
  "ds_read_b64 v[56:57], v46 offset:28680\n" \
  "v_mfma_f32_16x16x32_bf16 v[8:11], a[40:43], v[120:123], v[8:11]\n" \
  "buffer_load_dwordx4 v68, s[40:43], s66 offen offset:0 lds\n" \
  "v_mfma_f32_16x16x32_bf16 v[12:15], a[44:47], v[120:123], v[12:15]\n" \
  "v_mfma_f32_16x16x32_bf16 v[16:19], a[48:51], v[120:123], v[16:19]\n" \
  "v_mfma_f32_16x16x32_bf16 v[20:23], a[52:55], v[120:123], v[20:23]\n" \
  "buffer_load_dwordx4 v69, s[40:43], s66 offen offset:1024 lds\n" \
  "v_mfma_f32_16x16x32_bf16 v[24:27], a[56:59], v[120:123], v[24:27]\n" \
  "s_waitcnt vmcnt(17)\n" \
  "ds_read_b64_tr_b16 a[144:145], v64 offset:8192\n" \
  "ds_read_b64_tr_b16 a[146:147], v64 offset:8704\n" \
  "v_mfma_f32_16x16x32_bf16 v[28:31], a[60:63], v[120:123], v[28:31]\n" \
  "ds_read_b64_tr_b16 a[148:149], v65 offset:8192\n" \
  "ds_read_b64_tr_b16 a[150:151], v65 offset:8704\n" \
  "v_mfma_f32_16x16x32_bf16 v[0:3], a[64:67], v[124:127], v[0:3]\n" \
  "buffer_load_dwordx4 v70, s[40:43], s66 offen offset:2048 lds\n" \
  "ds_read_b64_tr_b16 a[152:153], v66 offset:8192\n" \
  "ds_read_b64_tr_b16 a[154:155], v66 offset:8704\n" \
  "v_mfma_f32_16x16x32_bf16 v[4:7], a[68:71], v[124:127], v[4:7]\n" \
  "ds_read_b64_tr_b16 a[156:157], v67 offset:8192\n" \
  "ds_read_b64_tr_b16 a[158:159], v67 offset:8704\n" \
  "v_mfma_f32_16x16x32_bf16 v[8:11], a[72:75], v[124:127], v[8:11]\n" \
  "v_mfma_f32_16x16x32_bf16 v[12:15], a[76:79], v[124:127], v[12:15]\n" \
  "buffer_load_dwordx4 v71, s[40:43], s66 offen offset:3072 lds\n" \
  "v_mfma_f32_16x16x32_bf16 v[16:19], a[80:83], v[124:127], v[16:19]\n" \
  "s_waitcnt lgkmcnt(0)\n" \
  "s_barrier\n" \
  "s_mov_b32 m0, s58\n" \
  "v_mfma_f32_16x16x32_bf16 v[20:23], a[84:87], v[124:127], v[20:23]\n" \
  "v_mfma_f32_16x16x32_bf16 v[24:27], a[88:91], v[124:127], v[24:27]\n" \
  "buffer_load_dwordx4 v[196:199], v59, s[36:39], s50 offen offset:0 sc1\n" \
  "v_mfma_f32_16x16x32_bf16 v[28:31], a[92:95], v[124:127], v[28:31]\n" \
  "v_mfma_f32_16x16x32_bf16 v[0:3], a[96:99], v[192:195], v[0:3]\n" \
  "v_mfma_f32_16x16x32_bf16 v[4:7], a[100:103], v[192:195], v[4:7]\n" \
  "buffer_load_dwordx4 v[200:203], v59, s[36:39], s50 offen offset:1024 sc1\n" \
  "v_mfma_f32_16x16x32_bf16 v[8:11], a[104:107], v[192:195], v[8:11]\n" \
  "s_waitcnt vmcnt(17)\n" \
  "ds_read_b64_tr_b16 a[160:161], v64 offset:12288\n" \
  "ds_read_b64_tr_b16 a[162:163], v64 offset:12800\n" \
  "v_mfma_f32_16x16x32_bf16 v[12:15], a[108:111], v[192:195], v[12:15]\n" \
  "ds_read_b64_tr_b16 a[164:165], v65 offset:12288\n" \
  "ds_read_b64_tr_b16 a[166:167], v65 offset:12800\n" \
  "v_mfma_f32_16x16x32_bf16 v[224:227], a[112:115], v[92:95], v[224:227]\n" \
  "buffer_load_dwordx4 v[204:207], v59, s[36:39], s50 offen offset:2048 sc1\n" \
  "s_add_u32 s59, s59, 0x10000\n" \
  "buffer_load_dword v63, v61, s[36:39], s59 offen offset:0 sc1\n" \
  "v_mfma_f32_16x16x32_bf16 v[228:231], a[116:119], v[92:95], v[228:231]\n" \
  "v_add_u32 v59, 819200, v59\n" \
  "ds_read_b64_tr_b16 a[168:169], v66 offset:12288\n" \
  "ds_read_b64_tr_b16 a[170:171], v66 offset:12800\n" \
  "v_mfma_f32_16x16x32_bf16 v[232:235], a[120:123], v[92:95], v[232:235]\n" \
  "ds_read_b64_tr_b16 a[172:173], v67 offset:12288\n" \
  "ds_read_b64_tr_b16 a[174:175], v67 offset:12800\n" \
  "buffer_store_dwordx2 v[40:41], v61, s[36:39], s52 offen offset:0 sc1\n" \
  "v_mfma_f32_16x16x32_bf16 v[236:239], a[124:127], v[92:95], v[236:239]\n" \
  "buffer_load_dwordx4 v[72:75], v59, s[36:39], s68 offen offset:0 sc1\n" \
  "v_mfma_f32_16x16x32_bf16 v[208:211], a[128:131], v[96:99], v[208:211]\n" \
  "v_mfma_f32_16x16x32_bf16 v[212:215], a[132:135], v[96:99], v[212:215]\n" \
  "v_mfma_f32_16x16x32_bf16 v[216:219], a[136:139], v[96:99], v[216:219]\n" \
  "buffer_load_dwordx4 v[76:79], v59, s[36:39], s68 offen offset:1024 sc1\n" \
  "v_mfma_f32_16x16x32_bf16 v[220:223], a[140:143], v[96:99], v[220:223]\n" \
  "v_mfma_f32_16x16x32_bf16 v[224:227], a[144:147], v[96:99], v[224:227]\n" \
  "s_waitcnt vmcnt(16)\n" \
  "ds_read_b64_tr_b16 a[176:177], v64 offset:16384\n" \
  "ds_read_b64_tr_b16 a[178:179], v64 offset:16896\n" \
  "v_mfma_f32_16x16x32_bf16 v[228:231], a[148:151], v[96:99], v[228:231]\n" \
  "ds_read_b64_tr_b16 a[180:181], v65 offset:16384\n" \
  "ds_read_b64_tr_b16 a[182:183], v65 offset:16896\n" \
  "buffer_load_dwordx4 v[80:83], v59, s[36:39], s68 offen offset:2048 sc1\n" \
  "v_mfma_f32_16x16x32_bf16 v[232:235], a[152:155], v[96:99], v[232:235]\n" \
  "ds_read_b64_tr_b16 a[184:185], v66 offset:16384\n" \
  "ds_read_b64_tr_b16 a[186:187], v66 offset:16896\n" \
  "v_mfma_f32_16x16x32_bf16 v[236:239], a[156:159], v[96:99], v[236:239]\n" \
  "ds_read_b64_tr_b16 a[188:189], v67 offset:16384\n" \
  "ds_read_b64_tr_b16 a[190:191], v67 offset:16896\n" \
  "s_waitcnt lgkmcnt(8)\n" \
  "v_mfma_f32_16x16x32_bf16 v[208:211], a[160:163], v[100:103], v[208:211]\n" \
  "s_waitcnt vmcnt(13)\n" \
  "ds_read_b64_tr_b16 a[192:193], v64 offset:20480\n" \
  "ds_read_b64_tr_b16 a[194:195], v64 offset:20992\n" \
  "v_mfma_f32_16x16x32_bf16 v[212:215], a[164:167], v[100:103], v[212:215]\n" \
  "ds_read_b64_tr_b16 a[196:197], v65 offset:20480\n" \
  "ds_read_b64_tr_b16 a[198:199], v65 offset:20992\n" \
  "buffer_load_dwordx4 v[84:87], v59, s[36:39], s69 offen offset:0 sc1\n" \
  "v_mfma_f32_16x16x32_bf16 v[216:219], a[168:171], v[100:103], v[216:219]\n" \
  "ds_read_b64_tr_b16 a[200:201], v66 offset:20480\n" \
  "ds_read_b64_tr_b16 a[202:203], v66 offset:20992\n" \
  "v_mfma_f32_16x16x32_bf16 v[220:223], a[172:175], v[100:103], v[220:223]\n" \
  "ds_read_b64_tr_b16 a[204:205], v67 offset:20480\n" \
  "ds_read_b64_tr_b16 a[206:207], v67 offset:20992\n" \
  "s_waitcnt lgkmcnt(8)\n" \
  "v_mfma_f32_16x16x32_bf16 v[224:227], a[176:179], v[100:103], v[224:227]\n" \
  "s_waitcnt vmcnt(9)\n" \
  "ds_read_b64_tr_b16 a[208:209], v64 offset:24576\n" \
  "ds_read_b64_tr_b16 a[210:211], v64 offset:25088\n" \
  "v_mfma_f32_16x16x32_bf16 v[228:231], a[180:183], v[100:103], v[228:231]\n" \
  "ds_read_b64_tr_b16 a[212:213], v65 offset:24576\n" \
  "ds_read_b64_tr_b16 a[214:215], v65 offset:25088\n" \
  "buffer_load_dwordx4 v[88:91], v59, s[36:39], s69 offen offset:1024 sc1\n" \
  "v_mfma_f32_16x16x32_bf16 v[232:235], a[184:187], v[100:103], v[232:235]\n" \
  "ds_read_b64_tr_b16 a[216:217], v66 offset:24576\n" \
  "ds_read_b64_tr_b16 a[218:219], v66 offset:25088\n" \
  "v_mfma_f32_16x16x32_bf16 v[236:239], a[188:191], v[100:103], v[236:239]\n" \
  "ds_read_b64_tr_b16 a[220:221], v67 offset:24576\n" \
  "ds_read_b64_tr_b16 a[222:223], v67 offset:25088\n" \
  "s_waitcnt lgkmcnt(8)\n" \
  "v_mfma_f32_16x16x32_bf16 v[208:211], a[192:195], v[104:107], v[208:211]\n" \
  "buffer_load_dwordx4 v[92:95], v59, s[36:39], s69 offen offset:2048 sc1\n" \
  "v_mfma_f32_16x16x32_bf16 v[212:215], a[196:199], v[104:107], v[212:215]\n" \
  "v_mfma_f32_16x16x32_bf16 v[216:219], a[200:203], v[104:107], v[216:219]\n" \
  "v_mfma_f32_16x16x32_bf16 v[220:223], a[204:207], v[104:107], v[220:223]\n" \
  "buffer_load_dwordx4 v[96:99], v59, s[36:39], s70 offen offset:0 sc1\n" \
  "s_waitcnt lgkmcnt(0)\n" \
  "v_mfma_f32_16x16x32_bf16 v[224:227], a[208:211], v[104:107], v[224:227]\n" \
  "v_mfma_f32_16x16x32_bf16 v[228:231], a[212:215], v[104:107], v[228:231]\n" \
  "v_mfma_f32_16x16x32_bf16 v[232:235], a[216:219], v[104:107], v[232:235]\n" \
  "buffer_load_dwordx4 v[100:103], v59, s[36:39], s70 offen offset:1024 sc1\n" \
  "v_mfma_f32_16x16x32_bf16 v[236:239], a[220:223], v[104:107], v[236:239]\n" \
  "v_mfma_f32_16x16x32_bf16 v[16:19], a[112:115], v[192:195], v[16:19]\n" \
  "v_mfma_f32_16x16x32_bf16 v[20:23], a[116:119], v[192:195], v[20:23]\n" \
  "buffer_load_dwordx4 v[104:107], v59, s[36:39], s70 offen offset:2048 sc1\n" \
  "ds_write_addtid_b32 v208 offset:0\n" \
  "ds_write_addtid_b32 v209 offset:256\n" \
  "v_mfma_f32_16x16x32_bf16 v[24:27], a[120:123], v[192:195], v[24:27]\n" \
  "ds_write_addtid_b32 v210 offset:512\n" \
  "ds_write_addtid_b32 v211 offset:768\n" \
  "v_mfma_f32_16x16x32_bf16 v[28:31], a[124:127], v[192:195], v[28:31]\n" \
  "ds_write_addtid_b32 v212 offset:1024\n" \
  "ds_write_addtid_b32 v213 offset:1280\n" \
  "s_waitcnt vmcnt(13)\n" \
  "v_mfma_f32_16x16x32_bf16 v[0:3], a[128:131], v[196:199], v[0:3]\n" \
  "ds_write_addtid_b32 v214 offset:1536\n" \
  "ds_write_addtid_b32 v215 offset:1792\n" \
  "v_mfma_f32_16x16x32_bf16 v[4:7], a[132:135], v[196:199], v[4:7]\n" \
  "ds_write_addtid_b32 v216 offset:2048\n" \
  "ds_write_addtid_b32 v217 offset:2304\n" \
  "v_mfma_f32_16x16x32_bf16 v[8:11], a[136:139], v[196:199], v[8:11]\n" \
  "ds_write_addtid_b32 v218 offset:2560\n" \
  "ds_write_addtid_b32 v219 offset:2816\n" \
  "v_mfma_f32_16x16x32_bf16 v[12:15], a[140:143], v[196:199], v[12:15]\n" \
  "ds_write_addtid_b32 v220 offset:3072\n" \
  "ds_write_addtid_b32 v221 offset:3328\n" \
  "v_mfma_f32_16x16x32_bf16 v[16:19], a[144:147], v[196:199], v[16:19]\n" \
  "ds_write_addtid_b32 v222 offset:3584\n" \
  "ds_write_addtid_b32 v223 offset:3840\n" \
  "v_mfma_f32_16x16x32_bf16 v[20:23], a[148:151], v[196:199], v[20:23]\n" \
  "ds_write_addtid_b32 v224 offset:4096\n" \
  "ds_write_addtid_b32 v225 offset:4352\n" \
  "v_mfma_f32_16x16x32_bf16 v[24:27], a[152:155], v[196:199], v[24:27]\n" \
  "ds_write_addtid_b32 v226 offset:4608\n" \
  "ds_write_addtid_b32 v227 offset:4864\n" \
  "v_mfma_f32_16x16x32_bf16 v[28:31], a[156:159], v[196:199], v[28:31]\n" \
  "ds_write_addtid_b32 v228 offset:5120\n" \
  "ds_write_addtid_b32 v229 offset:5376\n" \
  "s_waitcnt vmcnt(12)\n" \
  "v_mfma_f32_16x16x32_bf16 v[0:3], a[160:163], v[200:203], v[0:3]\n" \
  "ds_write_addtid_b32 v230 offset:5632\n" \
  "ds_write_addtid_b32 v231 offset:5888\n" \
  "v_mfma_f32_16x16x32_bf16 v[4:7], a[164:167], v[200:203], v[4:7]\n" \
  "ds_write_addtid_b32 v232 offset:6144\n" \
  "ds_write_addtid_b32 v233 offset:6400\n" \
  "v_mfma_f32_16x16x32_bf16 v[8:11], a[168:171], v[200:203], v[8:11]\n" \
  "ds_write_addtid_b32 v234 offset:6656\n" \
  "ds_write_addtid_b32 v235 offset:6912\n" \
  "v_mfma_f32_16x16x32_bf16 v[12:15], a[172:175], v[200:203], v[12:15]\n" \
  "ds_write_addtid_b32 v236 offset:7168\n" \
  "ds_write_addtid_b32 v237 offset:7424\n" \
  "v_mfma_f32_16x16x32_bf16 v[16:19], a[176:179], v[200:203], v[16:19]\n" \
  "ds_write_addtid_b32 v238 offset:7680\n" \
  "ds_write_addtid_b32 v239 offset:7936\n" \
  "v_mfma_f32_16x16x32_bf16 v[20:23], a[180:183], v[200:203], v[20:23]\n" \
  "v_mfma_f32_16x16x32_bf16 v[24:27], a[184:187], v[200:203], v[24:27]\n" \
  "v_mfma_f32_16x16x32_bf16 v[28:31], a[188:191], v[200:203], v[28:31]\n" \
  "s_waitcnt vmcnt(11)\n" \
  "v_mfma_f32_16x16x32_bf16 v[0:3], a[192:195], v[204:207], v[0:3]\n" \
  "v_mfma_f32_16x16x32_bf16 v[4:7], a[196:199], v[204:207], v[4:7]\n" \
  "v_mfma_f32_16x16x32_bf16 v[8:11], a[200:203], v[204:207], v[8:11]\n" \
  "s_waitcnt lgkmcnt(0)\n" \
  "s_barrier\n" \
  "ds_read_b128 v[32:35], v58 offset:0\n" \
  "v_mfma_f32_16x16x32_bf16 v[12:15], a[204:207], v[204:207], v[12:15]\n" \
  "ds_read_b128 v[36:39], v58 offset:8192\n" \
  "ds_read_b128 v[40:43], v58 offset:16384\n" \
  "v_mfma_f32_16x16x32_bf16 v[16:19], a[208:211], v[204:207], v[16:19]\n" \
  "ds_read_b128 v[44:47], v58 offset:24576\n" \
  "ds_read_b128 v[48:51], v58 offset:4096\n" \
  "v_mfma_f32_16x16x32_bf16 v[20:23], a[212:215], v[204:207], v[20:23]\n" \
  "ds_read_b128 v[240:243], v58 offset:12288\n" \
  "ds_read_b128 v[244:247], v58 offset:20480\n" \
  "v_mfma_f32_16x16x32_bf16 v[24:27], a[216:219], v[204:207], v[24:27]\n" \
  "ds_read_b128 v[248:251], v58 offset:28672\n" \
  "v_mfma_f32_16x16x32_bf16 v[28:31], a[220:223], v[204:207], v[28:31]\n" \
  "s_waitcnt vmcnt(7)\n" \
  "v_mfma_f32_16x16x32_bf16 v[208:211], v[128:131], v[72:75], 0\n" \
  "s_mov_b64 exec, 1\n" \
  "buffer_store_dword v61, off, s[36:39], s54 offset:0 sc1\n" \
  "s_mov_b64 exec, -1\n" \
  "v_mfma_f32_16x16x32_bf16 v[212:215], v[132:135], v[72:75], 0\n" \
  "v_cmp_eq_u32 vcc, 0, v63\n" \
  "ds_write_addtid_b32 v0 offset:32768\n" \
  "v_mfma_f32_16x16x32_bf16 v[216:219], v[136:139], v[72:75], 0\n" \
  "ds_write_addtid_b32 v1 offset:33024\n" \
  "ds_write_addtid_b32 v2 offset:33280\n" \
  "v_lshlrev_b32 v52, 5, v59\n" \
  "v_mfma_f32_16x16x32_bf16 v[220:223], v[140:143], v[72:75], 0\n" \
  "s_and_b64 vcc, vcc, s[86:87]\n" \
  "s_cbranch_scc1 .LsbS_%=\n" \
  ".LbbS_%=:\n" \
  "buffer_load_dwordx4 v[108:111], v59, s[36:39], s48 offen offset:0 sc1\n" \
  "v_mfma_f32_16x16x32_bf16 v[224:227], v[144:147], v[72:75], 0\n" \
  "ds_write_addtid_b32 v3 offset:33536\n" \
  "ds_write_addtid_b32 v4 offset:33792\n" \
  "s_waitcnt lgkmcnt(9)\n" \
  "v_mfma_f32_16x16x32_bf16 v[228:231], v[148:151], v[72:75], 0\n" \
  "ds_write_addtid_b32 v5 offset:34048\n" \
  "ds_write_addtid_b32 v6 offset:34304\n" \
  "v_add_f32 v32, v32, v36\n" \
  "v_mfma_f32_16x16x32_bf16 v[232:235], v[152:155], v[72:75], 0\n" \
  "buffer_load_dwordx4 v[112:115], v59, s[36:39], s48 offen offset:1024 sc1\n" \
  "ds_write_addtid_b32 v7 offset:34560\n" \
  "ds_write_addtid_b32 v8 offset:34816\n" \
  "v_mfma_f32_16x16x32_bf16 v[236:239], v[156:159], v[72:75], 0\n" \
  "ds_write_addtid_b32 v9 offset:35072\n" \
  "ds_write_addtid_b32 v10 offset:35328\n" \
  "v_add_f32 v33, v33, v37\n" \
  "v_mfma_f32_16x16x32_bf16 v[208:211], v[160:163], v[76:79], v[208:211]\n" \
  "ds_write_addtid_b32 v11 offset:35584\n" \
  "ds_write_addtid_b32 v12 offset:35840\n" \
  "v_add_f32 v34, v34, v38\n" \
  "v_mfma_f32_16x16x32_bf16 v[212:215], v[164:167], v[76:79], v[212:215]\n" \
  "buffer_load_dwordx4 v[116:119], v59, s[36:39], s48 offen offset:2048 sc1\n" \
  "ds_write_addtid_b32 v13 offset:36096\n" \
  "ds_write_addtid_b32 v14 offset:36352\n" \
  "v_mfma_f32_16x16x32_bf16 v[216:219], v[168:171], v[76:79], v[216:219]\n" \
  "ds_write_addtid_b32 v15 offset:36608\n" \
  "ds_write_addtid_b32 v16 offset:36864\n" \
  "v_add_f32 v35, v35, v39\n" \
  "v_mfma_f32_16x16x32_bf16 v[220:223], v[172:175], v[76:79], v[220:223]\n" \
  "ds_write_addtid_b32 v17 offset:37120\n" \
  "ds_write_addtid_b32 v18 offset:37376\n" \
  "v_add_f32 v40, v40, v44\n" \
  "v_mfma_f32_16x16x32_bf16 v[224:227], v[176:179], v[76:79], v[224:227]\n" \
  "buffer_load_dwordx4 v[120:123], v59, s[36:39], s49 offen offset:0 sc1\n" \
  "ds_write_addtid_b32 v19 offset:37632\n" \
  "ds_write_addtid_b32 v20 offset:37888\n" \
  "v_mfma_f32_16x16x32_bf16 v[228:231], v[180:183], v[76:79], v[228:231]\n" \
  "ds_write_addtid_b32 v21 offset:38144\n" \
  "ds_write_addtid_b32 v22 offset:38400\n" \
  "v_add_f32 v41, v41, v45\n" \
  "v_mfma_f32_16x16x32_bf16 v[232:235], v[184:187], v[76:79], v[232:235]\n" \
  "ds_write_addtid_b32 v23 offset:38656\n" \
  "ds_write_addtid_b32 v24 offset:38912\n" \
  "v_add_f32 v42, v42, v46\n" \
  "v_mfma_f32_16x16x32_bf16 v[236:239], v[188:191], v[76:79], v[236:239]\n" \
  "buffer_load_dwordx4 v[124:127], v59, s[36:39], s49 offen offset:1024 sc1\n" \
  "ds_write_addtid_b32 v25 offset:39168\n" \
  "s_waitcnt vmcnt(11)\n" \
  "v_mfma_f32_16x16x32_bf16 v[208:211], a[0:3], v[80:83], v[208:211]\n" \
  "ds_write_addtid_b32 v26 offset:39424\n" \
  "ds_write_addtid_b32 v27 offset:39680\n" \
  "v_add_f32 v43, v43, v47\n" \
  "v_mfma_f32_16x16x32_bf16 v[212:215], a[4:7], v[80:83], v[212:215]\n" \
  "ds_write_addtid_b32 v28 offset:39936\n" \
  "ds_write_addtid_b32 v29 offset:40192\n" \
  "v_add_f32 v32, v32, v40\n" \
  "v_mfma_f32_16x16x32_bf16 v[216:219], a[8:11], v[80:83], v[216:219]\n" \
  "buffer_load_dwordx4 v[192:195], v59, s[36:39], s49 offen offset:2048 sc1\n" \
  "ds_write_addtid_b32 v30 offset:40448\n" \
  "ds_write_addtid_b32 v31 offset:40704\n" \
  "v_mfma_f32_16x16x32_bf16 v[220:223], a[12:15], v[80:83], v[220:223]\n" \
  "v_add_f32 v33, v33, v41\n" \
  "v_add_f32 v34, v34, v42\n" \
  "v_add_f32 v35, v35, v43\n" \
  "v_mfma_f32_16x16x32_bf16 v[224:227], a[16:19], v[80:83], v[224:227]\n" \
  "v_cvt_pk_bf16_f32 v36, v32, v33\n" \
  "v_cvt_pk_bf16_f32 v37, v34, v35\n" \
  "s_waitcnt lgkmcnt(15)\n" \
  "v_mfma_f32_16x16x32_bf16 v[228:231], a[20:23], v[80:83], v[228:231]\n" \
  "buffer_load_dwordx4 v[196:199], v59, s[36:39], s50 offen offset:0 sc1\n" \
  "v_add_f32 v48, v48, v240\n" \
  "v_add_f32 v49, v49, v241\n" \
  "v_mfma_f32_16x16x32_bf16 v[232:235], a[24:27], v[80:83], v[232:235]\n" \
  "v_add_f32 v50, v50, v242\n" \
  "v_add_f32 v51, v51, v243\n" \
  "v_add_f32 v244, v244, v248\n" \
  "v_mfma_f32_16x16x32_bf16 v[236:239], a[28:31], v[80:83], v[236:239]\n" \
  "v_add_f32 v245, v245, v249\n" \
  "v_add_f32 v246, v246, v250\n" \
  "v_add_f32 v247, v247, v251\n" \
  "v_mfma_f32_16x16x32_bf16 v[208:211], a[32:35], v[84:87], v[208:211]\n" \
  "buffer_load_dwordx4 v[200:203], v59, s[36:39], s50 offen offset:1024 sc1\n" \
  "s_waitcnt lgkmcnt(0)\n" \
  "s_barrier\n" \
  "v_mfma_f32_16x16x32_bf16 v[212:215], a[36:39], v[84:87], v[212:215]\n" \
  "v_add_f32 v48, v48, v244\n" \
  "v_add_f32 v49, v49, v245\n" \
  "v_add_f32 v50, v50, v246\n" \
  "v_mfma_f32_16x16x32_bf16 v[216:219], a[40:43], v[84:87], v[216:219]\n" \
  "v_and_b32 v52, 0x1800, v52\n" \
  "v_add_f32 v51, v51, v247\n" \
  "v_and_b32 v53, 0x30, v59\n" \
  "v_mfma_f32_16x16x32_bf16 v[220:223], a[44:47], v[84:87], v[220:223]\n" \
  "buffer_load_dwordx4 v[204:207], v59, s[36:39], s50 offen offset:2048 sc1\n" \
  "v_cvt_pk_bf16_f32 v38, v48, v49\n" \
  "v_lshl_or_b32 v52, v53, 1, v52\n" \
  "v_mfma_f32_16x16x32_bf16 v[224:227], a[48:51], v[84:87], v[224:227]\n" \
  "v_cvt_pk_bf16_f32 v39, v50, v51\n" \
  "buffer_load_dword v53, v52, s[36:39], s30 offen offset:0 sc1\n" \
  "v_mfma_f32_16x16x32_bf16 v[228:231], a[52:55], v[84:87], v[228:231]\n" \
  "v_mfma_f32_16x16x32_bf16 v[232:235], a[56:59], v[84:87], v[232:235]\n" \
  "buffer_store_dwordx4 v[36:39], v59, s[36:39], s74 offen offset:0 sc1\n" \
  "ds_read_b128 v[32:35], v58 offset:32768\n" \
  "s_nop 0\n" \
  "ds_read_b128 v[36:39], v58 offset:40960\n" \
  "v_mfma_f32_16x16x32_bf16 v[236:239], a[60:63], v[84:87], v[236:239]\n" \
  "ds_read_b128 v[40:43], v58 offset:49152\n" \
  "ds_read_b128 v[44:47], v58 offset:57344\n" \
  "s_waitcnt vmcnt(15)\n" \
  "v_mfma_f32_16x16x32_bf16 v[208:211], a[64:67], v[88:91], v[208:211]\n" \
  "ds_read_b128 v[48:51], v58 offset:36864\n" \
  "ds_read_b128 v[240:243], v58 offset:45056\n" \
  "v_mfma_f32_16x16x32_bf16 v[212:215], a[68:71], v[88:91], v[212:215]\n" \
  "ds_read_b128 v[244:247], v58 offset:53248\n" \
  "ds_read_b128 v[248:251], v58 offset:61440\n" \
  "v_mfma_f32_16x16x32_bf16 v[216:219], a[72:75], v[88:91], v[216:219]\n" \
  "v_mfma_f32_16x16x32_bf16 v[220:223], a[76:79], v[88:91], v[220:223]\n" \
  "v_mfma_f32_16x16x32_bf16 v[224:227], a[80:83], v[88:91], v[224:227]\n" \
  "v_mfma_f32_16x16x32_bf16 v[228:231], a[84:87], v[88:91], v[228:231]\n" \
  "v_mfma_f32_16x16x32_bf16 v[232:235], a[88:91], v[88:91], v[232:235]\n" \
  "v_mfma_f32_16x16x32_bf16 v[236:239], a[92:95], v[88:91], v[236:239]\n" \
  "s_waitcnt lgkmcnt(4)\n" \
  "v_add_f32 v32, v32, v36\n" \
  "v_add_f32 v33, v33, v37\n" \
  "v_mfma_f32_16x16x32_bf16 v[208:211], a[96:99], v[92:95], v[208:211]\n" \
  "v_add_f32 v34, v34, v38\n" \
  "v_add_f32 v35, v35, v39\n" \
  "v_add_f32 v40, v40, v44\n" \
  "v_mfma_f32_16x16x32_bf16 v[212:215], a[100:103], v[92:95], v[212:215]\n" \
  "v_add_f32 v41, v41, v45\n" \
  "v_add_f32 v42, v42, v46\n" \
  "v_add_f32 v43, v43, v47\n" \
  "v_mfma_f32_16x16x32_bf16 v[216:219], a[104:107], v[92:95], v[216:219]\n" \
  "v_add_f32 v32, v32, v40\n" \
  "v_add_f32 v33, v33, v41\n" \
  "v_add_f32 v34, v34, v42\n" \
  "v_mfma_f32_16x16x32_bf16 v[220:223], a[108:111], v[92:95], v[220:223]\n" \
  "v_add_f32 v35, v35, v43\n" \
  "v_cvt_pk_bf16_f32 v36, v32, v33\n" \
  "v_cvt_pk_bf16_f32 v37, v34, v35\n" \
  "v_mfma_f32_16x16x32_bf16 v[224:227], a[112:115], v[92:95], v[224:227]\n" \
  "s_waitcnt lgkmcnt(0)\n" \
  "v_add_f32 v48, v48, v240\n" \
  "v_add_f32 v49, v49, v241\n" \
  "v_mfma_f32_16x16x32_bf16 v[228:231], a[116:119], v[92:95], v[228:231]\n" \
  "v_add_f32 v50, v50, v242\n" \
  "v_add_f32 v51, v51, v243\n" \
  "v_add_f32 v244, v244, v248\n" \
  "v_mfma_f32_16x16x32_bf16 v[232:235], a[120:123], v[92:95], v[232:235]\n" \
  "v_add_f32 v245, v245, v249\n" \
  "v_add_f32 v246, v246, v250\n" \
  "v_add_f32 v247, v247, v251\n" \
  "v_mfma_f32_16x16x32_bf16 v[236:239], a[124:127], v[92:95], v[236:239]\n" \
  "v_add_f32 v48, v48, v244\n" \
  "v_add_f32 v49, v49, v245\n" \
  "s_waitcnt vmcnt(13)\n" \
  "v_mfma_f32_16x16x32_bf16 v[208:211], a[128:131], v[96:99], v[208:211]\n" \
  "v_add_f32 v50, v50, v246\n" \
  "v_add_f32 v51, v51, v247\n" \
  "v_cvt_pk_bf16_f32 v38, v48, v49\n" \
  "v_mfma_f32_16x16x32_bf16 v[212:215], a[132:135], v[96:99], v[212:215]\n" \
  "v_cvt_pk_bf16_f32 v39, v50, v51\n" \
  "v_add_u32 v59, 409600, v59\n" \
  "buffer_store_dwordx4 v[36:39], v59, s[36:39], s74 offen offset:0 sc1\n" \
  "v_mfma_f32_16x16x32_bf16 v[216:219], a[136:139], v[96:99], v[216:219]\n" \
  "v_mfma_f32_16x16x32_bf16 v[220:223], a[140:143], v[96:99], v[220:223]\n" \
  "v_mfma_f32_16x16x32_bf16 v[224:227], a[144:147], v[96:99], v[224:227]\n" \
  "v_mfma_f32_16x16x32_bf16 v[228:231], a[148:151], v[96:99], v[228:231]\n" \
  "v_mfma_f32_16x16x32_bf16 v[232:235], a[152:155], v[96:99], v[232:235]\n" \
  "v_mfma_f32_16x16x32_bf16 v[236:239], a[156:159], v[96:99], v[236:239]\n" \
  "v_mfma_f32_16x16x32_bf16 v[208:211], a[160:163], v[100:103], v[208:211]\n" \
  "v_mfma_f32_16x16x32_bf16 v[212:215], a[164:167], v[100:103], v[212:215]\n" \
  "v_mfma_f32_16x16x32_bf16 v[216:219], a[168:171], v[100:103], v[216:219]\n" \
  "v_mfma_f32_16x16x32_bf16 v[220:223], a[172:175], v[100:103], v[220:223]\n" \
  "v_mfma_f32_16x16x32_bf16 v[224:227], a[176:179], v[100:103], v[224:227]\n" \
  "v_mfma_f32_16x16x32_bf16 v[228:231], a[180:183], v[100:103], v[228:231]\n" \
  "v_mfma_f32_16x16x32_bf16 v[232:235], a[184:187], v[100:103], v[232:235]\n" \
  "v_mfma_f32_16x16x32_bf16 v[236:239], a[188:191], v[100:103], v[236:239]\n" \
  "s_waitcnt vmcnt(13)\n" \
  "v_mfma_f32_16x16x32_bf16 v[208:211], a[192:195], v[104:107], v[208:211]\n" \
  "v_mfma_f32_16x16x32_bf16 v[212:215], a[196:199], v[104:107], v[212:215]\n" \
  "v_mfma_f32_16x16x32_bf16 v[216:219], a[200:203], v[104:107], v[216:219]\n" \
  "v_mfma_f32_16x16x32_bf16 v[220:223], a[204:207], v[104:107], v[220:223]\n" \
  "v_mfma_f32_16x16x32_bf16 v[224:227], a[208:211], v[104:107], v[224:227]\n" \
  "v_mfma_f32_16x16x32_bf16 v[228:231], a[212:215], v[104:107], v[228:231]\n" \
  "v_mfma_f32_16x16x32_bf16 v[232:235], a[216:219], v[104:107], v[232:235]\n" \
  "v_mfma_f32_16x16x32_bf16 v[236:239], a[220:223], v[104:107], v[236:239]\n" \
  ".Le_%=:\n" \
  "s_waitcnt vmcnt(10)\n" \
  "v_mfma_f32_16x16x32_bf16 v[0:3], v[128:131], v[108:111], 0\n" \
  "v_add_u32 v59, 409600, v59\n" \
  "v_mfma_f32_16x16x32_bf16 v[4:7], v[132:135], v[108:111], 0\n" \
  "ds_write_addtid_b32 v208 offset:0\n" \
  "v_mfma_f32_16x16x32_bf16 v[8:11], v[136:139], v[108:111], 0\n" \
  "buffer_load_dwordx4 v[72:75], v59, s[36:39], s68 offen offset:0 sc1\n" \
  "ds_write_addtid_b32 v209 offset:256\n" \
  "v_mfma_f32_16x16x32_bf16 v[12:15], v[140:143], v[108:111], 0\n" \
  "ds_write_addtid_b32 v210 offset:512\n" \
  "v_mfma_f32_16x16x32_bf16 v[16:19], v[144:147], v[108:111], 0\n" \
  "ds_write_addtid_b32 v211 offset:768\n" \
  "v_mfma_f32_16x16x32_bf16 v[20:23], v[148:151], v[108:111], 0\n" \
  "ds_write_addtid_b32 v212 offset:1024\n" \
  "v_mfma_f32_16x16x32_bf16 v[24:27], v[152:155], v[108:111], 0\n" \
  "ds_write_addtid_b32 v213 offset:1280\n" \
  "v_mfma_f32_16x16x32_bf16 v[28:31], v[156:159], v[108:111], 0\n" \
  "ds_write_addtid_b32 v214 offset:1536\n" \
  "v_mfma_f32_16x16x32_bf16 v[0:3], v[160:163], v[112:115], v[0:3]\n" \
  "buffer_load_dwordx4 v[76:79], v59, s[36:39], s68 offen offset:1024 sc1\n" \
  "ds_write_addtid_b32 v215 offset:1792\n" \
  "v_mfma_f32_16x16x32_bf16 v[4:7], v[164:167], v[112:115], v[4:7]\n" \
  "ds_write_addtid_b32 v216 offset:2048\n" \
  "v_mfma_f32_16x16x32_bf16 v[8:11], v[168:171], v[112:115], v[8:11]\n" \
  "ds_write_addtid_b32 v217 offset:2304\n" \
  "v_mfma_f32_16x16x32_bf16 v[12:15], v[172:175], v[112:115], v[12:15]\n" \
  "ds_write_addtid_b32 v218 offset:2560\n" \
  "v_mfma_f32_16x16x32_bf16 v[16:19], v[176:179], v[112:115], v[16:19]\n" \
  "ds_write_addtid_b32 v219 offset:2816\n" \
  "v_mfma_f32_16x16x32_bf16 v[20:23], v[180:183], v[112:115], v[20:23]\n" \
  "ds_write_addtid_b32 v220 offset:3072\n" \
  "v_mfma_f32_16x16x32_bf16 v[24:27], v[184:187], v[112:115], v[24:27]\n" \
  "buffer_load_dwordx4 v[80:83], v59, s[36:39], s68 offen offset:2048 sc1\n" \
  "ds_write_addtid_b32 v221 offset:3328\n" \
  "v_mfma_f32_16x16x32_bf16 v[28:31], v[188:191], v[112:115], v[28:31]\n" \
  "ds_write_addtid_b32 v222 offset:3584\n" \
  "s_waitcnt vmcnt(11)\n" \
  "v_mfma_f32_16x16x32_bf16 v[0:3], a[0:3], v[116:119], v[0:3]\n" \
  "ds_write_addtid_b32 v223 offset:3840\n" \
  "v_mfma_f32_16x16x32_bf16 v[4:7], a[4:7], v[116:119], v[4:7]\n" \
  "ds_write_addtid_b32 v224 offset:4096\n" \
  "v_mfma_f32_16x16x32_bf16 v[8:11], a[8:11], v[116:119], v[8:11]\n" \
  "ds_write_addtid_b32 v225 offset:4352\n" \
  "v_mfma_f32_16x16x32_bf16 v[12:15], a[12:15], v[116:119], v[12:15]\n" \
  "ds_write_addtid_b32 v226 offset:4608\n" \
  "v_mfma_f32_16x16x32_bf16 v[16:19], a[16:19], v[116:119], v[16:19]\n" \
  "buffer_load_dwordx4 v[84:87], v59, s[36:39], s69 offen offset:0 sc1\n" \
  "ds_write_addtid_b32 v227 offset:4864\n" \
  "v_mfma_f32_16x16x32_bf16 v[20:23], a[20:23], v[116:119], v[20:23]\n" \
  "ds_write_addtid_b32 v228 offset:5120\n" \
  "v_mfma_f32_16x16x32_bf16 v[24:27], a[24:27], v[116:119], v[24:27]\n" \
  "ds_write_addtid_b32 v229 offset:5376\n" \
  "v_mfma_f32_16x16x32_bf16 v[28:31], a[28:31], v[116:119], v[28:31]\n" \
  "ds_write_addtid_b32 v230 offset:5632\n" \
  "v_mfma_f32_16x16x32_bf16 v[0:3], a[32:35], v[120:123], v[0:3]\n" \
  "s_add_u32 s59, s59, 0x10000\n" \
  "buffer_load_dword v63, v61, s[36:39], s59 offen offset:0 sc1\n" \
  "v_mfma_f32_16x16x32_bf16 v[4:7], a[36:39], v[120:123], v[4:7]\n" \
  "ds_write_addtid_b32 v231 offset:5888\n" \
  "v_mfma_f32_16x16x32_bf16 v[8:11], a[40:43], v[120:123], v[8:11]\n" \
  "buffer_load_dwordx4 v[88:91], v59, s[36:39], s69 offen offset:1024 sc1\n" \
  "ds_write_addtid_b32 v232 offset:6144\n" \
  "v_mfma_f32_16x16x32_bf16 v[12:15], a[44:47], v[120:123], v[12:15]\n" \
  "ds_write_addtid_b32 v233 offset:6400\n" \
  "v_mfma_f32_16x16x32_bf16 v[16:19], a[48:51], v[120:123], v[16:19]\n" \
  "ds_write_addtid_b32 v234 offset:6656\n" \
  "v_mfma_f32_16x16x32_bf16 v[20:23], a[52:55], v[120:123], v[20:23]\n" \
  "ds_write_addtid_b32 v235 offset:6912\n" \
  "v_mfma_f32_16x16x32_bf16 v[24:27], a[56:59], v[120:123], v[24:27]\n" \
  "ds_write_addtid_b32 v236 offset:7168\n" \
  "v_mfma_f32_16x16x32_bf16 v[28:31], a[60:63], v[120:123], v[28:31]\n" \
  "ds_write_addtid_b32 v237 offset:7424\n" \
  "s_waitcnt vmcnt(12)\n" \
  "v_mfma_f32_16x16x32_bf16 v[0:3], a[64:67], v[124:127], v[0:3]\n" \
  "buffer_load_dwordx4 v[92:95], v59, s[36:39], s69 offen offset:2048 sc1\n" \
  "ds_write_addtid_b32 v238 offset:7680\n" \
  "v_mfma_f32_16x16x32_bf16 v[4:7], a[68:71], v[124:127], v[4:7]\n" \
  "ds_write_addtid_b32 v239 offset:7936\n" \
  "v_mfma_f32_16x16x32_bf16 v[8:11], a[72:75], v[124:127], v[8:11]\n" \
  "s_waitcnt vmcnt(9)\n" \
  "v_cmp_eq_u32 vcc, 0, v53\n" \
  "v_mfma_f32_16x16x32_bf16 v[12:15], a[76:79], v[124:127], v[12:15]\n" \
  "v_mfma_f32_16x16x32_bf16 v[16:19], a[80:83], v[124:127], v[16:19]\n" \
  "s_cbranch_vccnz .Lspf_%=\n" \
  ".Lbpf_%=:\n" \
  "v_bfe_u32 v69, v59, 2, 8\n" \
  "v_mfma_f32_16x16x32_bf16 v[20:23], a[84:87], v[124:127], v[20:23]\n" \
  "buffer_load_dword v64, v69, s[36:39], s79 offen offset:0 sc1\n" \
  "buffer_load_dword v65, v69, s[36:39], s80 offen offset:0 sc1\n" \
  "v_mfma_f32_16x16x32_bf16 v[24:27], a[88:91], v[124:127], v[24:27]\n" \
  "buffer_load_dwordx4 v[96:99], v59, s[36:39], s70 offen offset:0 sc1\n" \
  "buffer_load_dword v66, v69, s[36:39], s81 offen offset:0 sc1\n" \
  "v_mfma_f32_16x16x32_bf16 v[28:31], a[92:95], v[124:127], v[28:31]\n" \
  "s_waitcnt lgkmcnt(0)\n" \
  "s_barrier\n" \
  "v_mfma_f32_16x16x32_bf16 v[0:3], a[96:99], v[192:195], v[0:3]\n" \
  "ds_read_b128 v[32:35], v58 offset:0\n" \
  "buffer_load_dword v67, v69, s[36:39], s82 offen offset:0 sc1\n" \
  "v_mfma_f32_16x16x32_bf16 v[4:7], a[100:103], v[192:195], v[4:7]\n" \
  "ds_read_b128 v[36:39], v58 offset:8192\n" \
  "buffer_load_dword v68, v57, s[36:39], s90 offen offset:0 sc1\n" \
  "v_mfma_f32_16x16x32_bf16 v[8:11], a[104:107], v[192:195], v[8:11]\n" \
  "ds_read_b128 v[40:43], v58 offset:16384\n" \
  "v_add_u32 v70, s98, v58\n" \
  "v_mfma_f32_16x16x32_bf16 v[12:15], a[108:111], v[192:195], v[12:15]\n" \
  "ds_read_b128 v[44:47], v58 offset:24576\n" \
  "v_mfma_f32_16x16x32_bf16 v[16:19], a[112:115], v[192:195], v[16:19]\n" \
  "buffer_load_dwordx4 v[100:103], v59, s[36:39], s70 offen offset:1024 sc1\n" \
  "ds_read_b128 v[48:51], v58 offset:4096\n" \
  "v_mfma_f32_16x16x32_bf16 v[20:23], a[116:119], v[192:195], v[20:23]\n" \
  "ds_read_b128 v[240:243], v58 offset:12288\n" \
  "v_mfma_f32_16x16x32_bf16 v[24:27], a[120:123], v[192:195], v[24:27]\n" \
  "ds_read_b128 v[244:247], v58 offset:20480\n" \
  "v_mfma_f32_16x16x32_bf16 v[28:31], a[124:127], v[192:195], v[28:31]\n" \
  "ds_read_b128 v[248:251], v58 offset:28672\n" \
  "v_mfma_f32_16x16x32_bf16 v[0:3], a[128:131], v[196:199], v[0:3]\n" \
  "ds_read_b64 v[70:71], v70 offset:29704\n" \
  "v_mfma_f32_16x16x32_bf16 v[4:7], a[132:135], v[196:199], v[4:7]\n" \
  "v_mfma_f32_16x16x32_bf16 v[8:11], a[136:139], v[196:199], v[8:11]\n" \
  "buffer_load_dwordx4 v[104:107], v59, s[36:39], s70 offen offset:2048 sc1\n" \
  "v_mfma_f32_16x16x32_bf16 v[12:15], a[140:143], v[196:199], v[12:15]\n" \
  "s_waitcnt lgkmcnt(5)\n" \
  "v_add_f32 v32, v32, v36\n" \
  "v_mfma_f32_16x16x32_bf16 v[16:19], a[144:147], v[196:199], v[16:19]\n" \
  "v_add_f32 v33, v33, v37\n" \
  "v_add_f32 v34, v34, v38\n" \
  "v_mfma_f32_16x16x32_bf16 v[20:23], a[148:151], v[196:199], v[20:23]\n" \
  "v_add_f32 v35, v35, v39\n" \
  "v_add_f32 v40, v40, v44\n" \
  "v_mfma_f32_16x16x32_bf16 v[24:27], a[152:155], v[196:199], v[24:27]\n" \
  "v_add_f32 v41, v41, v45\n" \
  "v_add_f32 v42, v42, v46\n" \
  "v_mfma_f32_16x16x32_bf16 v[28:31], a[156:159], v[196:199], v[28:31]\n" \
  "v_add_f32 v43, v43, v47\n" \
  "v_add_f32 v32, v32, v40\n" \
  "v_mfma_f32_16x16x32_bf16 v[0:3], a[160:163], v[200:203], v[0:3]\n" \
  "v_add_f32 v33, v33, v41\n" \
  "v_add_f32 v34, v34, v42\n" \
  "v_mfma_f32_16x16x32_bf16 v[4:7], a[164:167], v[200:203], v[4:7]\n" \
  "v_add_f32 v35, v35, v43\n" \
  "v_cvt_pk_bf16_f32 v36, v32, v33\n" \
  "v_mfma_f32_16x16x32_bf16 v[8:11], a[168:171], v[200:203], v[8:11]\n" \
  "v_cvt_pk_bf16_f32 v37, v34, v35\n" \
  "s_waitcnt lgkmcnt(1)\n" \
  "v_mfma_f32_16x16x32_bf16 v[12:15], a[172:175], v[200:203], v[12:15]\n" \
  "v_add_f32 v48, v48, v240\n" \
  "v_add_f32 v49, v49, v241\n" \
  "v_mfma_f32_16x16x32_bf16 v[16:19], a[176:179], v[200:203], v[16:19]\n" \
  "v_add_f32 v50, v50, v242\n" \
  "v_add_f32 v51, v51, v243\n" \
  "v_mfma_f32_16x16x32_bf16 v[20:23], a[180:183], v[200:203], v[20:23]\n" \
  "v_add_f32 v244, v244, v248\n" \
  "v_add_f32 v245, v245, v249\n" \
  "v_mfma_f32_16x16x32_bf16 v[24:27], a[184:187], v[200:203], v[24:27]\n" \
  "v_add_f32 v246, v246, v250\n" \
  "v_add_f32 v247, v247, v251\n" \
  "v_mfma_f32_16x16x32_bf16 v[28:31], a[188:191], v[200:203], v[28:31]\n" \
  "v_add_f32 v48, v48, v244\n" \
  "v_mfma_f32_16x16x32_bf16 v[0:3], a[192:195], v[204:207], v[0:3]\n" \
  "v_add_f32 v49, v49, v245\n" \
  "v_add_f32 v50, v50, v246\n" \
  "v_mfma_f32_16x16x32_bf16 v[4:7], a[196:199], v[204:207], v[4:7]\n" \
  "v_add_f32 v51, v51, v247\n" \
  "v_cvt_pk_bf16_f32 v38, v48, v49\n" \
  "v_mfma_f32_16x16x32_bf16 v[8:11], a[200:203], v[204:207], v[8:11]\n" \
  "v_cvt_pk_bf16_f32 v39, v50, v51\n" \
  "buffer_store_dwordx4 v[36:39], v59, s[36:39], s74 offen offset:0 sc1\n" \
  "v_mfma_f32_16x16x32_bf16 v[12:15], a[204:207], v[204:207], v[12:15]\n" \
  "v_mfma_f32_16x16x32_bf16 v[16:19], a[208:211], v[204:207], v[16:19]\n" \
  "v_mfma_f32_16x16x32_bf16 v[20:23], a[212:215], v[204:207], v[20:23]\n" \
  "v_mfma_f32_16x16x32_bf16 v[24:27], a[216:219], v[204:207], v[24:27]\n" \
  "v_mfma_f32_16x16x32_bf16 v[28:31], a[220:223], v[204:207], v[28:31]\n" \
  "s_waitcnt vmcnt(14)\n" \
  "v_mfma_f32_16x16x32_bf16 v[208:211], v[128:131], v[72:75], 0\n" \
  "v_add_u32 v59, 409600, v59\n" \
  "v_mfma_f32_16x16x32_bf16 v[212:215], v[132:135], v[72:75], 0\n" \
  "s_waitcnt vmcnt(11)\n" \
  "v_cmp_eq_u32 vcc, 0, v63\n" \
  "v_mfma_f32_16x16x32_bf16 v[216:219], v[136:139], v[72:75], 0\n" \
  "ds_write_addtid_b32 v0 offset:32768\n" \
  "v_mfma_f32_16x16x32_bf16 v[220:223], v[140:143], v[72:75], 0\n" \
  "s_and_b64 vcc, vcc, s[86:87]\n" \
  "s_cbranch_scc1 .Lsg4_%=\n" \
  ".Lbg4_%=:\n" \
  "v_mfma_f32_16x16x32_bf16 v[224:227], v[144:147], v[72:75], 0\n" \
  "buffer_load_dwordx4 v[108:111], v59, s[36:39], s68 offen offset:0 sc1\n" \
  "ds_write_addtid_b32 v1 offset:33024\n" \
  "v_mfma_f32_16x16x32_bf16 v[228:231], v[148:151], v[72:75], 0\n" \
  "ds_write_addtid_b32 v2 offset:33280\n" \
  "s_waitcnt vmcnt(4)\n" \
  "v_mfma_f32_16x16x32_bf16 v[232:235], v[152:155], v[72:75], 0\n" \
  "ds_write_addtid_b32 v3 offset:33536\n" \
  "v_max3_u32 v52, v64, v65, v66\n" \
  "v_mfma_f32_16x16x32_bf16 v[236:239], v[156:159], v[72:75], 0\n" \
  "ds_write_addtid_b32 v4 offset:33792\n" \
  "v_max_u32 v52, v52, v67\n" \
  "v_mfma_f32_16x16x32_bf16 v[208:211], v[160:163], v[76:79], v[208:211]\n" \
  "buffer_load_dwordx4 v[112:115], v59, s[36:39], s68 offen offset:1024 sc1\n" \
  "ds_write_addtid_b32 v5 offset:34048\n" \
  "v_mfma_f32_16x16x32_bf16 v[212:215], v[164:167], v[76:79], v[212:215]\n" \
  "ds_write_addtid_b32 v6 offset:34304\n" \
  "v_cmp_eq_u32 vcc, -1, v52\n" \
  "v_mfma_f32_16x16x32_bf16 v[216:219], v[168:171], v[76:79], v[216:219]\n" \
  "ds_write_addtid_b32 v7 offset:34560\n" \
  "v_mfma_f32_16x16x32_bf16 v[220:223], v[172:175], v[76:79], v[220:223]\n" \
  "ds_write_addtid_b32 v8 offset:34816\n" \
  "s_cbranch_vccnz .Lse_%=\n" \
  ".Lbe_%=:\n" \
  "v_mfma_f32_16x16x32_bf16 v[224:227], v[176:179], v[76:79], v[224:227]\n" \
  "ds_write_addtid_b32 v9 offset:35072\n" \
  "v_lshlrev_b32 v52, 16, v64\n" \
  "v_mfma_f32_16x16x32_bf16 v[228:231], v[180:183], v[76:79], v[228:231]\n" \
  "ds_write_addtid_b32 v10 offset:35328\n" \
  "v_and_b32 v53, 0xffff0000, v64\n" \
  "v_mfma_f32_16x16x32_bf16 v[232:235], v[184:187], v[76:79], v[232:235]\n" \
  "buffer_load_dwordx4 v[116:119], v59, s[36:39], s68 offen offset:2048 sc1\n" \
  "ds_write_addtid_b32 v11 offset:35584\n" \
  "v_mfma_f32_16x16x32_bf16 v[236:239], v[188:191], v[76:79], v[236:239]\n" \
  "ds_write_addtid_b32 v12 offset:35840\n" \
  "v_mfma_f32_16x16x32_bf16 v[208:211], a[0:3], v[80:83], v[208:211]\n" \
  "ds_write_addtid_b32 v13 offset:36096\n" \
  "v_lshlrev_b32 v54, 16, v65\n" \
  "v_mfma_f32_16x16x32_bf16 v[212:215], a[4:7], v[80:83], v[212:215]\n" \
  "ds_write_addtid_b32 v14 offset:36352\n" \
  "v_and_b32 v55, 0xffff0000, v65\n" \
  "v_mfma_f32_16x16x32_bf16 v[216:219], a[8:11], v[80:83], v[216:219]\n" \
  "ds_write_addtid_b32 v15 offset:36608\n" \
  "v_lshlrev_b32 v252, 16, v66\n" \
  "v_mfma_f32_16x16x32_bf16 v[220:223], a[12:15], v[80:83], v[220:223]\n" \
  "ds_write_addtid_b32 v16 offset:36864\n" \
  "v_and_b32 v253, 0xffff0000, v66\n" \
  "v_mfma_f32_16x16x32_bf16 v[224:227], a[16:19], v[80:83], v[224:227]\n" \
  "buffer_load_dwordx4 v[120:123], v59, s[36:39], s69 offen offset:0 sc1\n" \
  "ds_write_addtid_b32 v17 offset:37120\n" \
  "v_mfma_f32_16x16x32_bf16 v[228:231], a[20:23], v[80:83], v[228:231]\n" \
  "ds_write_addtid_b32 v18 offset:37376\n" \
  "v_lshlrev_b32 v254, 16, v67\n" \
  "v_mfma_f32_16x16x32_bf16 v[232:235], a[24:27], v[80:83], v[232:235]\n" \
  "ds_write_addtid_b32 v19 offset:37632\n" \
  "v_and_b32 v255, 0xffff0000, v67\n" \
  "v_mfma_f32_16x16x32_bf16 v[236:239], a[28:31], v[80:83], v[236:239]\n" \
  "ds_write_addtid_b32 v20 offset:37888\n" \
  "v_add_f32 v52, v52, v54\n" \
  "v_mfma_f32_16x16x32_bf16 v[208:211], a[32:35], v[84:87], v[208:211]\n" \
  "ds_write_addtid_b32 v21 offset:38144\n" \
  "v_add_f32 v53, v53, v55\n" \
  "v_mfma_f32_16x16x32_bf16 v[212:215], a[36:39], v[84:87], v[212:215]\n" \
  "ds_write_addtid_b32 v22 offset:38400\n" \
  "v_add_f32 v252, v252, v254\n" \
  "v_mfma_f32_16x16x32_bf16 v[216:219], a[40:43], v[84:87], v[216:219]\n" \
  "buffer_load_dwordx4 v[124:127], v59, s[36:39], s69 offen offset:1024 sc1\n" \
  "ds_write_addtid_b32 v23 offset:38656\n" \
  "v_mfma_f32_16x16x32_bf16 v[220:223], a[44:47], v[84:87], v[220:223]\n" \
  "ds_write_addtid_b32 v24 offset:38912\n" \
  "v_add_f32 v253, v253, v255\n" \
  "v_mfma_f32_16x16x32_bf16 v[224:227], a[48:51], v[84:87], v[224:227]\n" \
  "ds_write_addtid_b32 v25 offset:39168\n" \
  "s_waitcnt lgkmcnt(15)\n" \
  "v_mfma_f32_16x16x32_bf16 v[228:231], a[52:55], v[84:87], v[228:231]\n" \
  "ds_write_addtid_b32 v26 offset:39424\n" \
  "v_add_f32 v52, v52, v70\n" \
  "v_mfma_f32_16x16x32_bf16 v[232:235], a[56:59], v[84:87], v[232:235]\n" \
  "ds_write_addtid_b32 v27 offset:39680\n" \
  "v_add_f32 v53, v53, v70\n" \
  "v_mfma_f32_16x16x32_bf16 v[236:239], a[60:63], v[84:87], v[236:239]\n" \
  "ds_write_addtid_b32 v28 offset:39936\n" \
  "s_waitcnt vmcnt(14)\n" \
  "v_mfma_f32_16x16x32_bf16 v[208:211], a[64:67], v[88:91], v[208:211]\n" \
  "buffer_load_dwordx4 v[192:195], v59, s[36:39], s69 offen offset:2048 sc1\n" \
  "ds_write_addtid_b32 v29 offset:40192\n" \
  "v_mfma_f32_16x16x32_bf16 v[212:215], a[68:71], v[88:91], v[212:215]\n" \
  "ds_write_addtid_b32 v30 offset:40448\n" \
  "v_lshlrev_b32 v54, 16, v71\n" \
  "v_mfma_f32_16x16x32_bf16 v[216:219], a[72:75], v[88:91], v[216:219]\n" \
  "ds_write_addtid_b32 v31 offset:40704\n" \
  "v_and_b32 v55, 0xffff0000, v71\n" \
  "v_mfma_f32_16x16x32_bf16 v[220:223], a[76:79], v[88:91], v[220:223]\n" \
  "v_lshlrev_b32 v254, 16, v68\n" \
  "v_and_b32 v255, 0xffff0000, v68\n" \
  "v_mfma_f32_16x16x32_bf16 v[224:227], a[80:83], v[88:91], v[224:227]\n" \
  "v_add_f32 v52, v52, v252\n" \
  "v_add_f32 v53, v53, v253\n" \
  "v_mfma_f32_16x16x32_bf16 v[228:231], a[84:87], v[88:91], v[228:231]\n" \
  "v_fma_f32 v52, v54, v52, v254\n" \
  "v_fma_f32 v53, v55, v53, v255\n" \
  "v_mfma_f32_16x16x32_bf16 v[232:235], a[88:91], v[88:91], v[232:235]\n" \
  "buffer_load_dwordx4 v[196:199], v59, s[36:39], s70 offen offset:0 sc1\n" \
  "v_cvt_pk_bf16_f32 v52, v52, v53\n" \
  "v_mfma_f32_16x16x32_bf16 v[236:239], a[92:95], v[88:91], v[236:239]\n" \
  "buffer_store_dword v52, v57, s[44:47], 0 offen offset:0 sc1\n" \
  "v_mfma_f32_16x16x32_bf16 v[208:211], a[96:99], v[92:95], v[208:211]\n" \
  "s_waitcnt lgkmcnt(0)\n" \
  "s_barrier\n" \
  "v_mfma_f32_16x16x32_bf16 v[212:215], a[100:103], v[92:95], v[212:215]\n" \
  "ds_read_b128 v[32:35], v58 offset:32768\n" \
  "v_mfma_f32_16x16x32_bf16 v[216:219], a[104:107], v[92:95], v[216:219]\n" \
  "ds_read_b128 v[36:39], v58 offset:40960\n" \
  "v_mfma_f32_16x16x32_bf16 v[220:223], a[108:111], v[92:95], v[220:223]\n" \
  "ds_read_b128 v[40:43], v58 offset:49152\n" \
  "v_mfma_f32_16x16x32_bf16 v[224:227], a[112:115], v[92:95], v[224:227]\n" \
  "buffer_load_dwordx4 v[200:203], v59, s[36:39], s70 offen offset:1024 sc1\n" \
  "ds_read_b128 v[44:47], v58 offset:57344\n" \
  "v_mfma_f32_16x16x32_bf16 v[228:231], a[116:119], v[92:95], v[228:231]\n" \
  "ds_read_b128 v[48:51], v58 offset:36864\n" \
  "v_mfma_f32_16x16x32_bf16 v[232:235], a[120:123], v[92:95], v[232:235]\n" \
  "ds_read_b128 v[240:243], v58 offset:45056\n" \
  "v_mfma_f32_16x16x32_bf16 v[236:239], a[124:127], v[92:95], v[236:239]\n" \
  "ds_read_b128 v[244:247], v58 offset:53248\n" \
  "s_waitcnt vmcnt(11)\n" \
  "v_mfma_f32_16x16x32_bf16 v[208:211], a[128:131], v[96:99], v[208:211]\n" \
  "ds_read_b128 v[248:251], v58 offset:61440\n" \
  "v_mfma_f32_16x16x32_bf16 v[212:215], a[132:135], v[96:99], v[212:215]\n" \
  "v_mfma_f32_16x16x32_bf16 v[216:219], a[136:139], v[96:99], v[216:219]\n" \
  "buffer_load_dwordx4 v[204:207], v59, s[36:39], s70 offen offset:2048 sc1\n" \
  "v_mfma_f32_16x16x32_bf16 v[220:223], a[140:143], v[96:99], v[220:223]\n" \
  "v_mfma_f32_16x16x32_bf16 v[224:227], a[144:147], v[96:99], v[224:227]\n" \
  "s_waitcnt lgkmcnt(4)\n" \
  "v_add_f32 v32, v32, v36\n" \
  "v_mfma_f32_16x16x32_bf16 v[228:231], a[148:151], v[96:99], v[228:231]\n" \
  "v_add_f32 v33, v33, v37\n" \
  "v_add_f32 v34, v34, v38\n" \
  "v_mfma_f32_16x16x32_bf16 v[232:235], a[152:155], v[96:99], v[232:235]\n" \
  "v_add_f32 v35, v35, v39\n" \
  "v_add_f32 v40, v40, v44\n" \
  "v_mfma_f32_16x16x32_bf16 v[236:239], a[156:159], v[96:99], v[236:239]\n" \
  "v_add_f32 v41, v41, v45\n" \
  "v_add_f32 v42, v42, v46\n" \
  "v_mfma_f32_16x16x32_bf16 v[208:211], a[160:163], v[100:103], v[208:211]\n" \
  "v_add_f32 v43, v43, v47\n" \
  "v_add_f32 v32, v32, v40\n" \
  "v_mfma_f32_16x16x32_bf16 v[212:215], a[164:167], v[100:103], v[212:215]\n" \
  "v_add_f32 v33, v33, v41\n" \
  "v_add_f32 v34, v34, v42\n" \
  "v_mfma_f32_16x16x32_bf16 v[216:219], a[168:171], v[100:103], v[216:219]\n" \
  "v_add_f32 v35, v35, v43\n" \
  "v_cvt_pk_bf16_f32 v36, v32, v33\n" \
  "v_mfma_f32_16x16x32_bf16 v[220:223], a[172:175], v[100:103], v[220:223]\n" \
  "v_cvt_pk_bf16_f32 v37, v34, v35\n" \
  "s_waitcnt lgkmcnt(0)\n" \
  "v_mfma_f32_16x16x32_bf16 v[224:227], a[176:179], v[100:103], v[224:227]\n" \
  "v_add_f32 v48, v48, v240\n" \
  "v_add_f32 v49, v49, v241\n" \
  "v_mfma_f32_16x16x32_bf16 v[228:231], a[180:183], v[100:103], v[228:231]\n" \
  "v_add_f32 v50, v50, v242\n" \
  "v_add_f32 v51, v51, v243\n" \
  "v_mfma_f32_16x16x32_bf16 v[232:235], a[184:187], v[100:103], v[232:235]\n" \
  "v_add_f32 v244, v244, v248\n" \
  "v_add_f32 v245, v245, v249\n" \
  "v_mfma_f32_16x16x32_bf16 v[236:239], a[188:191], v[100:103], v[236:239]\n" \
  "v_add_f32 v246, v246, v250\n" \
  "s_waitcnt vmcnt(11)\n" \
  "v_mfma_f32_16x16x32_bf16 v[208:211], a[192:195], v[104:107], v[208:211]\n" \
  "v_add_f32 v247, v247, v251\n" \
  "v_add_f32 v48, v48, v244\n" \
  "v_mfma_f32_16x16x32_bf16 v[212:215], a[196:199], v[104:107], v[212:215]\n" \
  "v_add_f32 v49, v49, v245\n" \
  "v_add_f32 v50, v50, v246\n" \
  "v_mfma_f32_16x16x32_bf16 v[216:219], a[200:203], v[104:107], v[216:219]\n" \
  "v_add_f32 v51, v51, v247\n" \
  "v_cvt_pk_bf16_f32 v38, v48, v49\n" \
  "v_mfma_f32_16x16x32_bf16 v[220:223], a[204:207], v[104:107], v[220:223]\n" \
  "v_cvt_pk_bf16_f32 v39, v50, v51\n" \
  "buffer_store_dwordx4 v[36:39], v59, s[36:39], s74 offen offset:0 sc1\n" \
  "v_mfma_f32_16x16x32_bf16 v[224:227], a[208:211], v[104:107], v[224:227]\n" \
  "v_mfma_f32_16x16x32_bf16 v[228:231], a[212:215], v[104:107], v[228:231]\n" \
  "v_mfma_f32_16x16x32_bf16 v[232:235], a[216:219], v[104:107], v[232:235]\n" \
  "v_mfma_f32_16x16x32_bf16 v[236:239], a[220:223], v[104:107], v[236:239]\n" \
  "s_waitcnt vmcnt(9)\n" \
  "v_mfma_f32_16x16x32_bf16 v[0:3], v[128:131], v[108:111], 0\n" \
  "v_add_u32 v59, 409600, v59\n" \
  "v_add_u32 v60, s98, v58\n" \
  "v_mfma_f32_16x16x32_bf16 v[4:7], v[132:135], v[108:111], 0\n" \
  "ds_write_addtid_b32 v208 offset:0\n" \
  "v_mfma_f32_16x16x32_bf16 v[8:11], v[136:139], v[108:111], 0\n" \
  "buffer_load_dwordx4 v[72:75], v59, s[36:39], s68 offen offset:0 sc1\n" \
  "ds_write_addtid_b32 v209 offset:256\n" \
  "v_mfma_f32_16x16x32_bf16 v[12:15], v[140:143], v[108:111], 0\n" \
  "ds_write_addtid_b32 v210 offset:512\n" \
  "v_mfma_f32_16x16x32_bf16 v[16:19], v[144:147], v[108:111], 0\n" \
  "ds_write_addtid_b32 v211 offset:768\n" \
  "v_mfma_f32_16x16x32_bf16 v[20:23], v[148:151], v[108:111], 0\n" \
  "buffer_load_dwordx4 v[64:67], v59, s[36:39], s71 offen offset:0 sc1\n" \
  "ds_write_addtid_b32 v212 offset:1024\n" \
  "v_mfma_f32_16x16x32_bf16 v[24:27], v[152:155], v[108:111], 0\n" \
  "ds_write_addtid_b32 v213 offset:1280\n" \
  "v_mfma_f32_16x16x32_bf16 v[28:31], v[156:159], v[108:111], 0\n" \
  "ds_write_addtid_b32 v214 offset:1536\n" \
  "v_mfma_f32_16x16x32_bf16 v[0:3], v[160:163], v[112:115], v[0:3]\n" \
  "buffer_load_dwordx4 v[76:79], v59, s[36:39], s68 offen offset:1024 sc1\n" \
  "ds_write_addtid_b32 v215 offset:1792\n" \
  "v_mfma_f32_16x16x32_bf16 v[4:7], v[164:167], v[112:115], v[4:7]\n" \
  "ds_write_addtid_b32 v216 offset:2048\n" \
  "v_mfma_f32_16x16x32_bf16 v[8:11], v[168:171], v[112:115], v[8:11]\n" \
  "ds_write_addtid_b32 v217 offset:2304\n" \
  "v_mfma_f32_16x16x32_bf16 v[12:15], v[172:175], v[112:115], v[12:15]\n" \
  "ds_write_addtid_b32 v218 offset:2560\n" \
  "v_mfma_f32_16x16x32_bf16 v[16:19], v[176:179], v[112:115], v[16:19]\n" \
  "ds_write_addtid_b32 v219 offset:2816\n" \
  "v_mfma_f32_16x16x32_bf16 v[20:23], v[180:183], v[112:115], v[20:23]\n" \
  "ds_write_addtid_b32 v220 offset:3072\n" \
  "v_mfma_f32_16x16x32_bf16 v[24:27], v[184:187], v[112:115], v[24:27]\n" \
  "buffer_load_dwordx4 v[80:83], v59, s[36:39], s68 offen offset:2048 sc1\n" \
  "ds_write_addtid_b32 v221 offset:3328\n" \
  "v_mfma_f32_16x16x32_bf16 v[28:31], v[188:191], v[112:115], v[28:31]\n" \
  "ds_write_addtid_b32 v222 offset:3584\n" \
  "s_waitcnt vmcnt(11)\n" \
  "v_mfma_f32_16x16x32_bf16 v[0:3], a[0:3], v[116:119], v[0:3]\n" \
  "ds_write_addtid_b32 v223 offset:3840\n" \
  "v_mfma_f32_16x16x32_bf16 v[4:7], a[4:7], v[116:119], v[4:7]\n" \
  "buffer_load_dwordx4 v[52:55], v59, s[36:39], s76 offen offset:0 sc1\n" \
  "ds_write_addtid_b32 v224 offset:4096\n" \
  "v_mfma_f32_16x16x32_bf16 v[8:11], a[8:11], v[116:119], v[8:11]\n" \
  "ds_write_addtid_b32 v225 offset:4352\n" \
  "v_mfma_f32_16x16x32_bf16 v[12:15], a[12:15], v[116:119], v[12:15]\n" \
  "ds_write_addtid_b32 v226 offset:4608\n" \
  "v_mfma_f32_16x16x32_bf16 v[16:19], a[16:19], v[116:119], v[16:19]\n" \
  "buffer_load_dwordx4 v[84:87], v59, s[36:39], s69 offen offset:0 sc1\n" \
  "ds_write_addtid_b32 v227 offset:4864\n" \
  "v_mfma_f32_16x16x32_bf16 v[20:23], a[20:23], v[116:119], v[20:23]\n" \
  "ds_write_addtid_b32 v228 offset:5120\n" \
  "v_mfma_f32_16x16x32_bf16 v[24:27], a[24:27], v[116:119], v[24:27]\n" \
  "ds_write_addtid_b32 v229 offset:5376\n" \
  "v_mfma_f32_16x16x32_bf16 v[28:31], a[28:31], v[116:119], v[28:31]\n" \
  "ds_write_addtid_b32 v230 offset:5632\n" \
  "v_mfma_f32_16x16x32_bf16 v[0:3], a[32:35], v[120:123], v[0:3]\n" \
  "ds_write_addtid_b32 v231 offset:5888\n" \
  "v_mfma_f32_16x16x32_bf16 v[4:7], a[36:39], v[120:123], v[4:7]\n" \
  "ds_write_addtid_b32 v232 offset:6144\n" \
  "v_mfma_f32_16x16x32_bf16 v[8:11], a[40:43], v[120:123], v[8:11]\n" \
  "buffer_load_dwordx4 v[88:91], v59, s[36:39], s69 offen offset:1024 sc1\n" \
  "ds_write_addtid_b32 v233 offset:6400\n" \
  "v_mfma_f32_16x16x32_bf16 v[12:15], a[44:47], v[120:123], v[12:15]\n" \
  "ds_write_addtid_b32 v234 offset:6656\n" \
  "v_mfma_f32_16x16x32_bf16 v[16:19], a[48:51], v[120:123], v[16:19]\n" \
  "ds_write_addtid_b32 v235 offset:6912\n" \
  "v_mfma_f32_16x16x32_bf16 v[20:23], a[52:55], v[120:123], v[20:23]\n" \
  "ds_write_addtid_b32 v236 offset:7168\n" \
  "v_mfma_f32_16x16x32_bf16 v[24:27], a[56:59], v[120:123], v[24:27]\n" \
  "ds_write_addtid_b32 v237 offset:7424\n" \
  "v_mfma_f32_16x16x32_bf16 v[28:31], a[60:63], v[120:123], v[28:31]\n" \
  "ds_write_addtid_b32 v238 offset:7680\n" \
  "s_waitcnt vmcnt(12)\n" \
  "v_mfma_f32_16x16x32_bf16 v[0:3], a[64:67], v[124:127], v[0:3]\n" \
  "buffer_load_dwordx4 v[92:95], v59, s[36:39], s69 offen offset:2048 sc1\n" \
  "ds_write_addtid_b32 v239 offset:7936\n" \
  "v_mfma_f32_16x16x32_bf16 v[4:7], a[68:71], v[124:127], v[4:7]\n" \
  "ds_read_b32 v61, v60 offset:28676\n" \
  "v_mfma_f32_16x16x32_bf16 v[8:11], a[72:75], v[124:127], v[8:11]\n" \
  "v_mfma_f32_16x16x32_bf16 v[12:15], a[76:79], v[124:127], v[12:15]\n" \
  "v_mfma_f32_16x16x32_bf16 v[16:19], a[80:83], v[124:127], v[16:19]\n" \
  "v_mfma_f32_16x16x32_bf16 v[20:23], a[84:87], v[124:127], v[20:23]\n" \
  "v_mfma_f32_16x16x32_bf16 v[24:27], a[88:91], v[124:127], v[24:27]\n" \
  "buffer_load_dwordx4 v[96:99], v59, s[36:39], s70 offen offset:0 sc1\n" \
  "v_mfma_f32_16x16x32_bf16 v[28:31], a[92:95], v[124:127], v[28:31]\n" \
  "s_waitcnt lgkmcnt(0)\n" \
  "s_barrier\n" \
  "v_mfma_f32_16x16x32_bf16 v[0:3], a[96:99], v[192:195], v[0:3]\n" \
  "ds_read_b128 v[32:35], v58 offset:0\n" \
  "v_mfma_f32_16x16x32_bf16 v[4:7], a[100:103], v[192:195], v[4:7]\n" \
  "ds_read_b128 v[36:39], v58 offset:8192\n" \
  "v_mfma_f32_16x16x32_bf16 v[8:11], a[104:107], v[192:195], v[8:11]\n" \
  "ds_read_b128 v[40:43], v58 offset:16384\n" \
  "v_lshlrev_b32 v60, 16, v61\n" \
  "v_mfma_f32_16x16x32_bf16 v[12:15], a[108:111], v[192:195], v[12:15]\n" \
  "ds_read_b128 v[44:47], v58 offset:24576\n" \
  "v_and_b32 v61, 0xffff0000, v61\n" \
  "v_mfma_f32_16x16x32_bf16 v[16:19], a[112:115], v[192:195], v[16:19]\n" \
  "buffer_load_dwordx4 v[100:103], v59, s[36:39], s70 offen offset:1024 sc1\n" \
  "ds_read_b128 v[48:51], v58 offset:4096\n" \
  "v_mfma_f32_16x16x32_bf16 v[20:23], a[116:119], v[192:195], v[20:23]\n" \
  "ds_read_b128 v[240:243], v58 offset:12288\n" \
  "v_mfma_f32_16x16x32_bf16 v[24:27], a[120:123], v[192:195], v[24:27]\n" \
  "ds_read_b128 v[244:247], v58 offset:20480\n" \
  "v_mfma_f32_16x16x32_bf16 v[28:31], a[124:127], v[192:195], v[28:31]\n" \
  "ds_read_b128 v[248:251], v58 offset:28672\n" \
  "s_waitcnt vmcnt(12)\n" \
  "v_mfma_f32_16x16x32_bf16 v[0:3], a[128:131], v[196:199], v[0:3]\n" \
  "v_mfma_f32_16x16x32_bf16 v[4:7], a[132:135], v[196:199], v[4:7]\n" \
  "v_mfma_f32_16x16x32_bf16 v[8:11], a[136:139], v[196:199], v[8:11]\n" \
  "buffer_load_dwordx4 v[104:107], v59, s[36:39], s70 offen offset:2048 sc1\n" \
  "v_mfma_f32_16x16x32_bf16 v[12:15], a[140:143], v[196:199], v[12:15]\n" \
  "s_waitcnt lgkmcnt(4)\n" \
  "v_add_f32 v32, v32, v36\n" \
  "v_mfma_f32_16x16x32_bf16 v[16:19], a[144:147], v[196:199], v[16:19]\n" \
  "v_add_f32 v33, v33, v37\n" \
  "v_add_f32 v34, v34, v38\n" \
  "v_mfma_f32_16x16x32_bf16 v[20:23], a[148:151], v[196:199], v[20:23]\n" \
  "v_add_f32 v35, v35, v39\n" \
  "v_add_f32 v40, v40, v44\n" \
  "v_mfma_f32_16x16x32_bf16 v[24:27], a[152:155], v[196:199], v[24:27]\n" \
  "v_add_f32 v41, v41, v45\n" \
  "v_add_f32 v42, v42, v46\n" \
  "v_mfma_f32_16x16x32_bf16 v[28:31], a[156:159], v[196:199], v[28:31]\n" \
  "v_add_f32 v43, v43, v47\n" \
  "v_add_f32 v32, v32, v40\n" \
  "v_mfma_f32_16x16x32_bf16 v[0:3], a[160:163], v[200:203], v[0:3]\n" \
  "v_add_f32 v33, v33, v41\n" \
  "v_add_f32 v34, v34, v42\n" \
  "v_mfma_f32_16x16x32_bf16 v[4:7], a[164:167], v[200:203], v[4:7]\n" \
  "v_add_f32 v35, v35, v43\n" \
  "v_cvt_pk_bf16_f32 v36, v32, v33\n" \
  "v_mfma_f32_16x16x32_bf16 v[8:11], a[168:171], v[200:203], v[8:11]\n" \
  "v_cvt_pk_bf16_f32 v37, v34, v35\n" \
  "s_waitcnt lgkmcnt(0)\n" \
  "v_mfma_f32_16x16x32_bf16 v[12:15], a[172:175], v[200:203], v[12:15]\n" \
  "v_add_f32 v48, v48, v240\n" \
  "v_add_f32 v49, v49, v241\n" \
  "v_mfma_f32_16x16x32_bf16 v[16:19], a[176:179], v[200:203], v[16:19]\n" \
  "v_add_f32 v50, v50, v242\n" \
  "v_add_f32 v51, v51, v243\n" \
  "v_mfma_f32_16x16x32_bf16 v[20:23], a[180:183], v[200:203], v[20:23]\n" \
  "v_add_f32 v244, v244, v248\n" \
  "v_add_f32 v245, v245, v249\n" \
  "v_mfma_f32_16x16x32_bf16 v[24:27], a[184:187], v[200:203], v[24:27]\n" \
  "v_add_f32 v246, v246, v250\n" \
  "v_add_f32 v247, v247, v251\n" \
  "v_mfma_f32_16x16x32_bf16 v[28:31], a[188:191], v[200:203], v[28:31]\n" \
  "v_add_f32 v48, v48, v244\n" \
  "s_waitcnt vmcnt(12)\n" \
  "v_mfma_f32_16x16x32_bf16 v[0:3], a[192:195], v[204:207], v[0:3]\n" \
  "v_add_f32 v49, v49, v245\n" \
  "v_add_f32 v50, v50, v246\n" \
  "v_mfma_f32_16x16x32_bf16 v[4:7], a[196:199], v[204:207], v[4:7]\n" \
  "v_add_f32 v51, v51, v247\n" \
  "v_cvt_pk_bf16_f32 v38, v48, v49\n" \
  "v_mfma_f32_16x16x32_bf16 v[8:11], a[200:203], v[204:207], v[8:11]\n" \
  "v_cvt_pk_bf16_f32 v39, v50, v51\n" \
  "buffer_store_dwordx4 v[36:39], v59, s[36:39], s74 offen offset:0 sc1\n" \
  "v_mfma_f32_16x16x32_bf16 v[12:15], a[204:207], v[204:207], v[12:15]\n" \
  "v_mfma_f32_16x16x32_bf16 v[16:19], a[208:211], v[204:207], v[16:19]\n" \
  "v_mfma_f32_16x16x32_bf16 v[20:23], a[212:215], v[204:207], v[20:23]\n" \
  "v_mfma_f32_16x16x32_bf16 v[24:27], a[216:219], v[204:207], v[24:27]\n" \
  "v_mfma_f32_16x16x32_bf16 v[28:31], a[220:223], v[204:207], v[28:31]\n" \
  "s_waitcnt vmcnt(9)\n" \
  "v_mfma_f32_16x16x32_bf16 v[208:211], v[128:131], v[72:75], 0\n" \
  "v_add_u32 v59, 409600, v59\n" \
  "s_waitcnt vmcnt(7)\n" \
  "v_mfma_f32_16x16x32_bf16 v[212:215], v[132:135], v[72:75], 0\n" \
  "ds_write_addtid_b32 v0 offset:32768\n" \
  "v_max_u32 v62, v64, v52\n" \
  "v_mfma_f32_16x16x32_bf16 v[216:219], v[136:139], v[72:75], 0\n" \
  "buffer_load_dwordx4 v[108:111], v59, s[36:39], s68 offen offset:0 sc1\n" \
  "ds_write_addtid_b32 v1 offset:33024\n" \
  "v_mfma_f32_16x16x32_bf16 v[220:223], v[140:143], v[72:75], 0\n" \
  "ds_write_addtid_b32 v2 offset:33280\n" \
  "v_cmp_eq_u32 vcc, -1, v62\n" \
  "v_mfma_f32_16x16x32_bf16 v[224:227], v[144:147], v[72:75], 0\n" \
  "ds_write_addtid_b32 v3 offset:33536\n" \
  "v_mfma_f32_16x16x32_bf16 v[228:231], v[148:151], v[72:75], 0\n" \
  "ds_write_addtid_b32 v4 offset:33792\n" \
  "s_cbranch_vccnz .Lsp1_%=\n" \
  ".Lbp1_%=:\n" \
  "v_mfma_f32_16x16x32_bf16 v[232:235], v[152:155], v[72:75], 0\n" \
  "ds_write_addtid_b32 v5 offset:34048\n" \
  "v_and_b32 v71, 0xffff0000, v67\n" \
  "v_mfma_f32_16x16x32_bf16 v[236:239], v[156:159], v[72:75], 0\n" \
  "ds_write_addtid_b32 v6 offset:34304\n" \
  "v_lshlrev_b32 v70, 16, v67\n" \
  "v_mfma_f32_16x16x32_bf16 v[208:211], v[160:163], v[76:79], v[208:211]\n" \
  "buffer_load_dwordx4 v[112:115], v59, s[36:39], s68 offen offset:1024 sc1\n" \
  "ds_write_addtid_b32 v7 offset:34560\n" \
  "v_mfma_f32_16x16x32_bf16 v[212:215], v[164:167], v[76:79], v[212:215]\n" \
  "ds_write_addtid_b32 v8 offset:34816\n" \
  "v_and_b32 v69, 0xffff0000, v66\n" \
  "v_mfma_f32_16x16x32_bf16 v[216:219], v[168:171], v[76:79], v[216:219]\n" \
  "ds_write_addtid_b32 v9 offset:35072\n" \
  "v_lshlrev_b32 v68, 16, v66\n" \
  "v_mfma_f32_16x16x32_bf16 v[220:223], v[172:175], v[76:79], v[220:223]\n" \
  "ds_write_addtid_b32 v10 offset:35328\n" \
  "v_and_b32 v67, 0xffff0000, v65\n" \
  "v_mfma_f32_16x16x32_bf16 v[224:227], v[176:179], v[76:79], v[224:227]\n" \
  "ds_write_addtid_b32 v11 offset:35584\n" \
  "v_lshlrev_b32 v66, 16, v65\n" \
  "v_mfma_f32_16x16x32_bf16 v[228:231], v[180:183], v[76:79], v[228:231]\n" \
  "ds_write_addtid_b32 v12 offset:35840\n" \
  "v_and_b32 v65, 0xffff0000, v64\n" \
  "v_mfma_f32_16x16x32_bf16 v[232:235], v[184:187], v[76:79], v[232:235]\n" \
  "buffer_load_dwordx4 v[116:119], v59, s[36:39], s68 offen offset:2048 sc1\n" \
  "ds_write_addtid_b32 v13 offset:36096\n" \
  "v_mfma_f32_16x16x32_bf16 v[236:239], v[188:191], v[76:79], v[236:239]\n" \
  "ds_write_addtid_b32 v14 offset:36352\n" \
  "s_waitcnt vmcnt(9)\n" \
  "v_mfma_f32_16x16x32_bf16 v[208:211], a[0:3], v[80:83], v[208:211]\n" \
  "ds_write_addtid_b32 v15 offset:36608\n" \
  "v_lshlrev_b32 v64, 16, v64\n" \
  "v_mfma_f32_16x16x32_bf16 v[212:215], a[4:7], v[80:83], v[212:215]\n" \
  "ds_write_addtid_b32 v16 offset:36864\n" \
  "v_add_f32 v64, v64, v60\n" \
  "v_mfma_f32_16x16x32_bf16 v[216:219], a[8:11], v[80:83], v[216:219]\n" \
  "ds_write_addtid_b32 v17 offset:37120\n" \
  "v_add_f32 v65, v65, v60\n" \
  "v_mfma_f32_16x16x32_bf16 v[220:223], a[12:15], v[80:83], v[220:223]\n" \
  "ds_write_addtid_b32 v18 offset:37376\n" \
  "v_add_f32 v66, v66, v60\n" \
  "v_mfma_f32_16x16x32_bf16 v[224:227], a[16:19], v[80:83], v[224:227]\n" \
  "buffer_load_dwordx4 v[120:123], v59, s[36:39], s69 offen offset:0 sc1\n" \
  "ds_write_addtid_b32 v19 offset:37632\n" \
  "v_mfma_f32_16x16x32_bf16 v[228:231], a[20:23], v[80:83], v[228:231]\n" \
  "ds_write_addtid_b32 v20 offset:37888\n" \
  "v_add_f32 v67, v67, v60\n" \
  "v_mfma_f32_16x16x32_bf16 v[232:235], a[24:27], v[80:83], v[232:235]\n" \
  "ds_write_addtid_b32 v21 offset:38144\n" \
  "v_add_f32 v68, v68, v61\n" \
  "v_mfma_f32_16x16x32_bf16 v[236:239], a[28:31], v[80:83], v[236:239]\n" \
  "ds_write_addtid_b32 v22 offset:38400\n" \
  "v_add_f32 v69, v69, v61\n" \
  "v_mfma_f32_16x16x32_bf16 v[208:211], a[32:35], v[84:87], v[208:211]\n" \
  "ds_write_addtid_b32 v23 offset:38656\n" \
  "v_add_f32 v70, v70, v61\n" \
  "v_mfma_f32_16x16x32_bf16 v[212:215], a[36:39], v[84:87], v[212:215]\n" \
  "ds_write_addtid_b32 v24 offset:38912\n" \
  "v_add_f32 v71, v71, v61\n" \
  "v_mfma_f32_16x16x32_bf16 v[216:219], a[40:43], v[84:87], v[216:219]\n" \
  "buffer_load_dwordx4 v[124:127], v59, s[36:39], s69 offen offset:1024 sc1\n" \
  "ds_write_addtid_b32 v25 offset:39168\n" \
  "v_mfma_f32_16x16x32_bf16 v[220:223], a[44:47], v[84:87], v[220:223]\n" \
  "ds_write_addtid_b32 v26 offset:39424\n" \
  "v_and_b32 v255, 0xffff0000, v55\n" \
  "v_mfma_f32_16x16x32_bf16 v[224:227], a[48:51], v[84:87], v[224:227]\n" \
  "ds_write_addtid_b32 v27 offset:39680\n" \
  "v_lshlrev_b32 v254, 16, v55\n" \
  "v_mfma_f32_16x16x32_bf16 v[228:231], a[52:55], v[84:87], v[228:231]\n" \
  "ds_write_addtid_b32 v28 offset:39936\n" \
  "v_and_b32 v253, 0xffff0000, v54\n" \
  "v_mfma_f32_16x16x32_bf16 v[232:235], a[56:59], v[84:87], v[232:235]\n" \
  "ds_write_addtid_b32 v29 offset:40192\n" \
  "v_lshlrev_b32 v252, 16, v54\n" \
  "v_mfma_f32_16x16x32_bf16 v[236:239], a[60:63], v[84:87], v[236:239]\n" \
  "ds_write_addtid_b32 v30 offset:40448\n" \
  "s_waitcnt vmcnt(9)\n" \
  "v_mfma_f32_16x16x32_bf16 v[208:211], a[64:67], v[88:91], v[208:211]\n" \
  "buffer_load_dwordx4 v[192:195], v59, s[36:39], s69 offen offset:2048 sc1\n" \
  "ds_write_addtid_b32 v31 offset:40704\n" \
  "v_mfma_f32_16x16x32_bf16 v[212:215], a[68:71], v[88:91], v[212:215]\n" \
  "v_and_b32 v55, 0xffff0000, v53\n" \
  "v_lshlrev_b32 v54, 16, v53\n" \
  "v_mfma_f32_16x16x32_bf16 v[216:219], a[72:75], v[88:91], v[216:219]\n" \
  "v_and_b32 v53, 0xffff0000, v52\n" \
  "v_lshlrev_b32 v52, 16, v52\n" \
  "v_mfma_f32_16x16x32_bf16 v[220:223], a[76:79], v[88:91], v[220:223]\n" \
  "v_add_f32 v52, v52, v60\n" \
  "v_add_f32 v53, v53, v60\n" \
  "v_mfma_f32_16x16x32_bf16 v[224:227], a[80:83], v[88:91], v[224:227]\n" \
  "v_add_f32 v54, v54, v60\n" \
  "v_add_f32 v55, v55, v60\n" \
  "v_mfma_f32_16x16x32_bf16 v[228:231], a[84:87], v[88:91], v[228:231]\n" \
  "v_add_f32 v252, v252, v61\n" \
  "v_add_f32 v253, v253, v61\n" \
  "v_mfma_f32_16x16x32_bf16 v[232:235], a[88:91], v[88:91], v[232:235]\n" \
  "buffer_load_dwordx4 v[196:199], v59, s[36:39], s70 offen offset:0 sc1\n" \
  "v_add_f32 v254, v254, v61\n" \
  "v_mfma_f32_16x16x32_bf16 v[236:239], a[92:95], v[88:91], v[236:239]\n" \
  "s_waitcnt lgkmcnt(0)\n" \
  "s_barrier\n" \
  "v_mfma_f32_16x16x32_bf16 v[208:211], a[96:99], v[92:95], v[208:211]\n" \
  "ds_read_b128 v[32:35], v58 offset:32768\n" \
  "v_add_f32 v255, v255, v61\n" \
  "v_mfma_f32_16x16x32_bf16 v[212:215], a[100:103], v[92:95], v[212:215]\n" \
  "ds_read_b128 v[36:39], v58 offset:40960\n" \
  "v_mfma_f32_16x16x32_bf16 v[216:219], a[104:107], v[92:95], v[216:219]\n" \
  "ds_read_b128 v[40:43], v58 offset:49152\n" \
  "v_mfma_f32_16x16x32_bf16 v[220:223], a[108:111], v[92:95], v[220:223]\n" \
  "ds_read_b128 v[44:47], v58 offset:57344\n" \
  "v_mfma_f32_16x16x32_bf16 v[224:227], a[112:115], v[92:95], v[224:227]\n" \
  "buffer_load_dwordx4 v[200:203], v59, s[36:39], s70 offen offset:1024 sc1\n" \
  "ds_read_b128 v[48:51], v58 offset:36864\n" \
  "v_mfma_f32_16x16x32_bf16 v[228:231], a[116:119], v[92:95], v[228:231]\n" \
  "ds_read_b128 v[240:243], v58 offset:45056\n" \
  "v_mfma_f32_16x16x32_bf16 v[232:235], a[120:123], v[92:95], v[232:235]\n" \
  "ds_read_b128 v[244:247], v58 offset:53248\n" \
  "v_mfma_f32_16x16x32_bf16 v[236:239], a[124:127], v[92:95], v[236:239]\n" \
  "buffer_load_dwordx4 v[60:63], v59, s[36:39], s72 offen offset:0 sc1\n" \
  "s_waitcnt vmcnt(11)\n" \
  "v_mfma_f32_16x16x32_bf16 v[208:211], a[128:131], v[96:99], v[208:211]\n" \
  "ds_read_b128 v[248:251], v58 offset:61440\n" \
  "v_mfma_f32_16x16x32_bf16 v[212:215], a[132:135], v[96:99], v[212:215]\n" \
  "v_mfma_f32_16x16x32_bf16 v[216:219], a[136:139], v[96:99], v[216:219]\n" \
  "buffer_load_dwordx4 v[204:207], v59, s[36:39], s70 offen offset:2048 sc1\n" \
  "v_mfma_f32_16x16x32_bf16 v[220:223], a[140:143], v[96:99], v[220:223]\n" \
  "s_waitcnt lgkmcnt(4)\n" \
  "v_add_f32 v32, v32, v36\n" \
  "v_mfma_f32_16x16x32_bf16 v[224:227], a[144:147], v[96:99], v[224:227]\n" \
  "v_add_f32 v33, v33, v37\n" \
  "v_add_f32 v34, v34, v38\n" \
  "v_mfma_f32_16x16x32_bf16 v[228:231], a[148:151], v[96:99], v[228:231]\n" \
  "v_add_f32 v35, v35, v39\n" \
  "v_add_f32 v40, v40, v44\n" \
  "v_mfma_f32_16x16x32_bf16 v[232:235], a[152:155], v[96:99], v[232:235]\n" \
  "v_add_f32 v41, v41, v45\n" \
  "v_add_f32 v42, v42, v46\n" \
  "v_mfma_f32_16x16x32_bf16 v[236:239], a[156:159], v[96:99], v[236:239]\n" \
  "v_add_f32 v43, v43, v47\n" \
  "v_add_f32 v32, v32, v40\n" \
  "v_mfma_f32_16x16x32_bf16 v[208:211], a[160:163], v[100:103], v[208:211]\n" \
  "v_add_f32 v33, v33, v41\n" \
  "v_add_f32 v34, v34, v42\n" \
  "v_mfma_f32_16x16x32_bf16 v[212:215], a[164:167], v[100:103], v[212:215]\n" \
  "v_add_f32 v35, v35, v43\n" \
  "v_cvt_pk_bf16_f32 v36, v32, v33\n" \
  "v_mfma_f32_16x16x32_bf16 v[216:219], a[168:171], v[100:103], v[216:219]\n" \
  "v_cvt_pk_bf16_f32 v37, v34, v35\n" \
  "s_waitcnt lgkmcnt(0)\n" \
  "v_mfma_f32_16x16x32_bf16 v[220:223], a[172:175], v[100:103], v[220:223]\n" \
  "v_add_f32 v48, v48, v240\n" \
  "v_add_f32 v49, v49, v241\n" \
  "v_mfma_f32_16x16x32_bf16 v[224:227], a[176:179], v[100:103], v[224:227]\n" \
  "v_add_f32 v50, v50, v242\n" \
  "v_add_f32 v51, v51, v243\n" \
  "v_mfma_f32_16x16x32_bf16 v[228:231], a[180:183], v[100:103], v[228:231]\n" \
  "v_add_f32 v244, v244, v248\n" \
  "v_add_f32 v245, v245, v249\n" \
  "v_mfma_f32_16x16x32_bf16 v[232:235], a[184:187], v[100:103], v[232:235]\n" \
  "v_add_f32 v246, v246, v250\n" \
  "v_add_f32 v247, v247, v251\n" \
  "v_mfma_f32_16x16x32_bf16 v[236:239], a[188:191], v[100:103], v[236:239]\n" \
  "v_add_f32 v48, v48, v244\n" \
  "s_waitcnt vmcnt(11)\n" \
  "v_mfma_f32_16x16x32_bf16 v[208:211], a[192:195], v[104:107], v[208:211]\n" \
  "v_add_f32 v49, v49, v245\n" \
  "v_add_f32 v50, v50, v246\n" \
  "v_mfma_f32_16x16x32_bf16 v[212:215], a[196:199], v[104:107], v[212:215]\n" \
  "v_add_f32 v51, v51, v247\n" \
  "v_cvt_pk_bf16_f32 v38, v48, v49\n" \
  "v_mfma_f32_16x16x32_bf16 v[216:219], a[200:203], v[104:107], v[216:219]\n" \
  "v_cvt_pk_bf16_f32 v39, v50, v51\n" \
  "buffer_store_dwordx4 v[36:39], v59, s[36:39], s74 offen offset:0 sc1\n" \
  "v_mfma_f32_16x16x32_bf16 v[220:223], a[204:207], v[104:107], v[220:223]\n" \
  "v_mfma_f32_16x16x32_bf16 v[224:227], a[208:211], v[104:107], v[224:227]\n" \
  "v_mfma_f32_16x16x32_bf16 v[228:231], a[212:215], v[104:107], v[228:231]\n" \
  "v_mfma_f32_16x16x32_bf16 v[232:235], a[216:219], v[104:107], v[232:235]\n" \
  "v_mfma_f32_16x16x32_bf16 v[236:239], a[220:223], v[104:107], v[236:239]\n" \
  "s_waitcnt vmcnt(9)\n" \
  "v_mfma_f32_16x16x32_bf16 v[0:3], v[128:131], v[108:111], 0\n" \
  "v_add_u32 v59, 409600, v59\n" \
  "v_mfma_f32_16x16x32_bf16 v[4:7], v[132:135], v[108:111], 0\n" \
  "ds_write_addtid_b32 v208 offset:0\n" \
  "ds_write_addtid_b32 v209 offset:256\n" \
  "v_mfma_f32_16x16x32_bf16 v[8:11], v[136:139], v[108:111], 0\n" \
  "buffer_load_dwordx4 v[72:75], v59, s[36:39], s68 offen offset:0 sc1\n" \
  "ds_write_addtid_b32 v210 offset:512\n" \
  "v_mfma_f32_16x16x32_bf16 v[12:15], v[140:143], v[108:111], 0\n" \
  "ds_write_addtid_b32 v211 offset:768\n" \
  "ds_write_addtid_b32 v212 offset:1024\n" \
  "v_mfma_f32_16x16x32_bf16 v[16:19], v[144:147], v[108:111], 0\n" \
  "ds_write_addtid_b32 v213 offset:1280\n" \
  "ds_write_addtid_b32 v214 offset:1536\n" \
  "v_mfma_f32_16x16x32_bf16 v[20:23], v[148:151], v[108:111], 0\n" \
  "ds_write_addtid_b32 v215 offset:1792\n" \
  "ds_write_addtid_b32 v216 offset:2048\n" \
  "v_mfma_f32_16x16x32_bf16 v[24:27], v[152:155], v[108:111], 0\n" \
  "ds_write_addtid_b32 v217 offset:2304\n" \
  "ds_write_addtid_b32 v218 offset:2560\n" \
  "v_mfma_f32_16x16x32_bf16 v[28:31], v[156:159], v[108:111], 0\n" \
  "ds_write_addtid_b32 v219 offset:2816\n" \
  "ds_write_addtid_b32 v220 offset:3072\n" \
  "v_mfma_f32_16x16x32_bf16 v[0:3], v[160:163], v[112:115], v[0:3]\n" \
  "buffer_load_dwordx4 v[76:79], v59, s[36:39], s68 offen offset:1024 sc1\n" \
  "ds_write_addtid_b32 v221 offset:3328\n" \
  "v_mfma_f32_16x16x32_bf16 v[4:7], v[164:167], v[112:115], v[4:7]\n" \
  "ds_write_addtid_b32 v222 offset:3584\n" \
  "ds_write_addtid_b32 v223 offset:3840\n" \
  "v_mfma_f32_16x16x32_bf16 v[8:11], v[168:171], v[112:115], v[8:11]\n" \
  "ds_write_addtid_b32 v224 offset:4096\n" \
  "ds_write_addtid_b32 v225 offset:4352\n" \
  "v_mfma_f32_16x16x32_bf16 v[12:15], v[172:175], v[112:115], v[12:15]\n" \
  "buffer_load_dwordx4 v[108:111], v59, s[36:39], s77 offen offset:0 sc1\n" \
  "ds_write_addtid_b32 v226 offset:4608\n" \
  "v_mfma_f32_16x16x32_bf16 v[16:19], v[176:179], v[112:115], v[16:19]\n" \
  "ds_write_addtid_b32 v227 offset:4864\n" \
  "ds_write_addtid_b32 v228 offset:5120\n" \
  "v_mfma_f32_16x16x32_bf16 v[20:23], v[180:183], v[112:115], v[20:23]\n" \
  "ds_write_addtid_b32 v229 offset:5376\n" \
  "ds_write_addtid_b32 v230 offset:5632\n" \
  "v_mfma_f32_16x16x32_bf16 v[24:27], v[184:187], v[112:115], v[24:27]\n" \
  "buffer_load_dwordx4 v[80:83], v59, s[36:39], s68 offen offset:2048 sc1\n" \
  "ds_write_addtid_b32 v231 offset:5888\n" \
  "v_mfma_f32_16x16x32_bf16 v[28:31], v[188:191], v[112:115], v[28:31]\n" \
  "ds_write_addtid_b32 v232 offset:6144\n" \
  "s_waitcnt vmcnt(11)\n" \
  "v_mfma_f32_16x16x32_bf16 v[0:3], a[0:3], v[116:119], v[0:3]\n" \
  "ds_write_addtid_b32 v233 offset:6400\n" \
  "ds_write_addtid_b32 v234 offset:6656\n" \
  "v_mfma_f32_16x16x32_bf16 v[4:7], a[4:7], v[116:119], v[4:7]\n" \
  "ds_write_addtid_b32 v235 offset:6912\n" \
  "ds_write_addtid_b32 v236 offset:7168\n" \
  "v_mfma_f32_16x16x32_bf16 v[8:11], a[8:11], v[116:119], v[8:11]\n" \
  "ds_write_addtid_b32 v237 offset:7424\n" \
  "ds_write_addtid_b32 v238 offset:7680\n" \
  "v_mfma_f32_16x16x32_bf16 v[12:15], a[12:15], v[116:119], v[12:15]\n" \
  "ds_write_addtid_b32 v239 offset:7936\n" \
  "v_mfma_f32_16x16x32_bf16 v[16:19], a[16:19], v[116:119], v[16:19]\n" \
  "buffer_load_dwordx4 v[84:87], v59, s[36:39], s69 offen offset:0 sc1\n" \
  "v_mfma_f32_16x16x32_bf16 v[20:23], a[20:23], v[116:119], v[20:23]\n" \
  "v_mfma_f32_16x16x32_bf16 v[24:27], a[24:27], v[116:119], v[24:27]\n" \
  "v_mfma_f32_16x16x32_bf16 v[28:31], a[28:31], v[116:119], v[28:31]\n" \
  "v_mfma_f32_16x16x32_bf16 v[0:3], a[32:35], v[120:123], v[0:3]\n" \
  "v_mfma_f32_16x16x32_bf16 v[4:7], a[36:39], v[120:123], v[4:7]\n" \
  "s_waitcnt lgkmcnt(0)\n" \
  "s_barrier\n" \
  "v_mfma_f32_16x16x32_bf16 v[8:11], a[40:43], v[120:123], v[8:11]\n" \
  "buffer_load_dwordx4 v[88:91], v59, s[36:39], s69 offen offset:1024 sc1\n" \
  "ds_read_b128 v[32:35], v58 offset:0\n" \
  "v_mfma_f32_16x16x32_bf16 v[12:15], a[44:47], v[120:123], v[12:15]\n" \
  "ds_read_b128 v[36:39], v58 offset:8192\n" \
  "ds_read_b128 v[40:43], v58 offset:16384\n" \
  "v_mfma_f32_16x16x32_bf16 v[16:19], a[48:51], v[120:123], v[16:19]\n" \
  "ds_read_b128 v[44:47], v58 offset:24576\n" \
  "ds_read_b128 v[48:51], v58 offset:4096\n" \
  "v_mfma_f32_16x16x32_bf16 v[20:23], a[52:55], v[120:123], v[20:23]\n" \
  "ds_read_b128 v[240:243], v58 offset:12288\n" \
  "ds_read_b128 v[244:247], v58 offset:20480\n" \
  "v_mfma_f32_16x16x32_bf16 v[24:27], a[56:59], v[120:123], v[24:27]\n" \
  "ds_read_b128 v[248:251], v58 offset:28672\n" \
  "s_waitcnt vmcnt(8)\n" \
  "v_mfma_f32_16x16x32_bf16 v[28:31], a[60:63], v[120:123], v[28:31]\n" \
  "v_cmp_eq_u32 vcc, -1, v60\n" \
  "v_mfma_f32_16x16x32_bf16 v[0:3], a[64:67], v[124:127], v[0:3]\n" \
  "buffer_load_dwordx4 v[92:95], v59, s[36:39], s69 offen offset:2048 sc1\n" \
  "v_mfma_f32_16x16x32_bf16 v[4:7], a[68:71], v[124:127], v[4:7]\n" \
  "buffer_load_dwordx2 v[116:117], v56, s[36:39], s90 offen offset:0 sc1\n" \
  "s_cbranch_vccnz .Lsp2A_%=\n" \
  ".Lbp2A_%=:\n" \
  "v_mfma_f32_16x16x32_bf16 v[8:11], a[72:75], v[124:127], v[8:11]\n" \
  "buffer_load_dwordx2 v[118:119], v56, s[36:39], s90 offen offset:32 sc1\n" \
  "v_lshlrev_b32 v112, 16, v60\n" \
  "v_mfma_f32_16x16x32_bf16 v[12:15], a[76:79], v[124:127], v[12:15]\n" \
  "v_and_b32 v113, 0xffff0000, v60\n" \
  "v_add_f32 v64, v64, v112\n" \
  "v_mfma_f32_16x16x32_bf16 v[16:19], a[80:83], v[124:127], v[16:19]\n" \
  "s_waitcnt lgkmcnt(4)\n" \
  "v_add_f32 v32, v32, v36\n" \
  "v_mfma_f32_16x16x32_bf16 v[20:23], a[84:87], v[124:127], v[20:23]\n" \
  "v_add_f32 v33, v33, v37\n" \
  "v_add_f32 v34, v34, v38\n" \
  "v_mfma_f32_16x16x32_bf16 v[24:27], a[88:91], v[124:127], v[24:27]\n" \
  "buffer_load_dwordx4 v[96:99], v59, s[36:39], s70 offen offset:0 sc1\n" \
  "v_add_f32 v35, v35, v39\n" \
  "v_mfma_f32_16x16x32_bf16 v[28:31], a[92:95], v[124:127], v[28:31]\n" \
  "buffer_load_dwordx2 v[120:121], v56, s[36:39], s100 offen offset:0 sc1\n" \
  "v_add_f32 v40, v40, v44\n" \
  "v_mfma_f32_16x16x32_bf16 v[0:3], a[96:99], v[192:195], v[0:3]\n" \
  "buffer_load_dwordx2 v[122:123], v56, s[36:39], s100 offen offset:32 sc1\n" \
  "v_add_f32 v41, v41, v45\n" \
  "v_mfma_f32_16x16x32_bf16 v[4:7], a[100:103], v[192:195], v[4:7]\n" \
  "v_add_f32 v42, v42, v46\n" \
  "v_add_f32 v43, v43, v47\n" \
  "v_mfma_f32_16x16x32_bf16 v[8:11], a[104:107], v[192:195], v[8:11]\n" \
  "v_add_f32 v32, v32, v40\n" \
  "v_add_f32 v33, v33, v41\n" \
  "v_mfma_f32_16x16x32_bf16 v[12:15], a[108:111], v[192:195], v[12:15]\n" \
  "v_add_f32 v34, v34, v42\n" \
  "v_add_f32 v35, v35, v43\n" \
  "v_mfma_f32_16x16x32_bf16 v[16:19], a[112:115], v[192:195], v[16:19]\n" \
  "buffer_load_dwordx4 v[100:103], v59, s[36:39], s70 offen offset:1024 sc1\n" \
  "v_cvt_pk_bf16_f32 v36, v32, v33\n" \
  "v_mfma_f32_16x16x32_bf16 v[20:23], a[116:119], v[192:195], v[20:23]\n" \
  "v_cvt_pk_bf16_f32 v37, v34, v35\n" \
  "s_waitcnt lgkmcnt(0)\n" \
  "v_mfma_f32_16x16x32_bf16 v[24:27], a[120:123], v[192:195], v[24:27]\n" \
  "v_add_f32 v48, v48, v240\n" \
  "v_add_f32 v49, v49, v241\n" \
  "v_mfma_f32_16x16x32_bf16 v[28:31], a[124:127], v[192:195], v[28:31]\n" \
  "v_add_f32 v50, v50, v242\n" \
  "v_mfma_f32_16x16x32_bf16 v[0:3], a[128:131], v[196:199], v[0:3]\n" \
  "v_add_f32 v65, v65, v113\n" \
  "v_add_f32 v51, v51, v243\n" \
  "v_mfma_f32_16x16x32_bf16 v[4:7], a[132:135], v[196:199], v[4:7]\n" \
  "v_lshlrev_b32 v112, 16, v61\n" \
  "v_add_f32 v244, v244, v248\n" \
  "v_mfma_f32_16x16x32_bf16 v[8:11], a[136:139], v[196:199], v[8:11]\n" \
  "buffer_load_dwordx4 v[104:107], v59, s[36:39], s70 offen offset:2048 sc1\n" \
  "v_and_b32 v113, 0xffff0000, v61\n" \
  "v_mfma_f32_16x16x32_bf16 v[12:15], a[140:143], v[196:199], v[12:15]\n" \
  "v_add_f32 v245, v245, v249\n" \
  "v_add_f32 v66, v66, v112\n" \
  "v_mfma_f32_16x16x32_bf16 v[16:19], a[144:147], v[196:199], v[16:19]\n" \
  "v_add_f32 v246, v246, v250\n" \
  "v_add_f32 v67, v67, v113\n" \
  "v_mfma_f32_16x16x32_bf16 v[20:23], a[148:151], v[196:199], v[20:23]\n" \
  "v_add_f32 v247, v247, v251\n" \
  "v_lshlrev_b32 v112, 16, v62\n" \
  "v_mfma_f32_16x16x32_bf16 v[24:27], a[152:155], v[196:199], v[24:27]\n" \
  "v_add_f32 v48, v48, v244\n" \
  "v_and_b32 v113, 0xffff0000, v62\n" \
  "v_mfma_f32_16x16x32_bf16 v[28:31], a[156:159], v[196:199], v[28:31]\n" \
  "v_add_f32 v49, v49, v245\n" \
  "v_add_f32 v68, v68, v112\n" \
  "v_mfma_f32_16x16x32_bf16 v[0:3], a[160:163], v[200:203], v[0:3]\n" \
  "v_add_f32 v50, v50, v246\n" \
  "v_add_f32 v69, v69, v113\n" \
  "v_mfma_f32_16x16x32_bf16 v[4:7], a[164:167], v[200:203], v[4:7]\n" \
  "v_add_f32 v51, v51, v247\n" \
  "v_lshlrev_b32 v112, 16, v63\n" \
  "v_mfma_f32_16x16x32_bf16 v[8:11], a[168:171], v[200:203], v[8:11]\n" \
  "v_cvt_pk_bf16_f32 v38, v48, v49\n" \
  "v_and_b32 v113, 0xffff0000, v63\n" \
  "v_mfma_f32_16x16x32_bf16 v[12:15], a[172:175], v[200:203], v[12:15]\n" \
  "v_cvt_pk_bf16_f32 v39, v50, v51\n" \
  "v_add_f32 v70, v70, v112\n" \
  "v_mfma_f32_16x16x32_bf16 v[16:19], a[176:179], v[200:203], v[16:19]\n" \
  "buffer_store_dwordx4 v[36:39], v59, s[36:39], s74 offen offset:0 sc1\n" \
  "v_add_f32 v71, v71, v113\n" \
  "v_mfma_f32_16x16x32_bf16 v[20:23], a[180:183], v[200:203], v[20:23]\n" \
  "v_mfma_f32_16x16x32_bf16 v[24:27], a[184:187], v[200:203], v[24:27]\n" \
  "v_mfma_f32_16x16x32_bf16 v[28:31], a[188:191], v[200:203], v[28:31]\n" \
  "s_waitcnt vmcnt(16)\n" \
  "v_mfma_f32_16x16x32_bf16 v[0:3], a[192:195], v[204:207], v[0:3]\n" \
  "v_mfma_f32_16x16x32_bf16 v[4:7], a[196:199], v[204:207], v[4:7]\n" \
  "v_mfma_f32_16x16x32_bf16 v[8:11], a[200:203], v[204:207], v[8:11]\n" \
  "v_mfma_f32_16x16x32_bf16 v[12:15], a[204:207], v[204:207], v[12:15]\n" \
  "v_mfma_f32_16x16x32_bf16 v[16:19], a[208:211], v[204:207], v[16:19]\n" \
  "v_mfma_f32_16x16x32_bf16 v[20:23], a[212:215], v[204:207], v[20:23]\n" \
  "v_mfma_f32_16x16x32_bf16 v[24:27], a[216:219], v[204:207], v[24:27]\n" \
  "v_mfma_f32_16x16x32_bf16 v[28:31], a[220:223], v[204:207], v[28:31]\n" \
  "s_waitcnt vmcnt(13)\n" \
  "v_mfma_f32_16x16x32_bf16 v[208:211], v[128:131], v[72:75], 0\n" \
  "v_add_u32 v59, 409600, v59\n" \
  "s_waitcnt vmcnt(12)\n" \
  "v_mfma_f32_16x16x32_bf16 v[212:215], v[132:135], v[72:75], 0\n" \
  "ds_write_addtid_b32 v0 offset:32768\n" \
  "ds_write_addtid_b32 v1 offset:33024\n" \
  "v_mfma_f32_16x16x32_bf16 v[216:219], v[136:139], v[72:75], 0\n" \
  "ds_write_addtid_b32 v2 offset:33280\n" \
  "ds_write_addtid_b32 v3 offset:33536\n" \
  "v_mfma_f32_16x16x32_bf16 v[220:223], v[140:143], v[72:75], 0\n" \
  "buffer_load_dwordx4 v[112:115], v59, s[36:39], s73 offen offset:0 sc1\n" \
  "ds_write_addtid_b32 v4 offset:33792\n" \
  "v_mfma_f32_16x16x32_bf16 v[208:211], v[160:163], v[76:79], v[208:211]\n" \
  "ds_write_addtid_b32 v5 offset:34048\n" \
  "ds_write_addtid_b32 v6 offset:34304\n" \
  "v_mfma_f32_16x16x32_bf16 v[212:215], v[164:167], v[76:79], v[212:215]\n" \
  "ds_write_addtid_b32 v7 offset:34560\n" \
  "ds_write_addtid_b32 v8 offset:34816\n" \
  "v_mfma_f32_16x16x32_bf16 v[216:219], v[168:171], v[76:79], v[216:219]\n" \
  "ds_write_addtid_b32 v9 offset:35072\n" \
  "ds_write_addtid_b32 v10 offset:35328\n" \
  "v_mfma_f32_16x16x32_bf16 v[220:223], v[172:175], v[76:79], v[220:223]\n" \
  "ds_write_addtid_b32 v11 offset:35584\n" \
  "s_waitcnt vmcnt(11)\n" \
  "v_mfma_f32_16x16x32_bf16 v[208:211], a[0:3], v[80:83], v[208:211]\n" \
  "ds_write_addtid_b32 v12 offset:35840\n" \
  "ds_write_addtid_b32 v13 offset:36096\n" \
  "v_mfma_f32_16x16x32_bf16 v[212:215], a[4:7], v[80:83], v[212:215]\n" \
  "ds_write_addtid_b32 v14 offset:36352\n" \
  "ds_write_addtid_b32 v15 offset:36608\n" \
  "v_mfma_f32_16x16x32_bf16 v[216:219], a[8:11], v[80:83], v[216:219]\n" \
  "ds_write_addtid_b32 v16 offset:36864\n" \
  "ds_write_addtid_b32 v17 offset:37120\n" \
  "v_mfma_f32_16x16x32_bf16 v[220:223], a[12:15], v[80:83], v[220:223]\n" \
  "ds_write_addtid_b32 v18 offset:37376\n" \
  "ds_write_addtid_b32 v19 offset:37632\n" \
  "v_mfma_f32_16x16x32_bf16 v[208:211], a[32:35], v[84:87], v[208:211]\n" \
  "ds_write_addtid_b32 v20 offset:37888\n" \
  "ds_write_addtid_b32 v21 offset:38144\n" \
  "v_mfma_f32_16x16x32_bf16 v[212:215], a[36:39], v[84:87], v[212:215]\n" \
  "ds_write_addtid_b32 v22 offset:38400\n" \
  "ds_write_addtid_b32 v23 offset:38656\n" \
  "v_mfma_f32_16x16x32_bf16 v[216:219], a[40:43], v[84:87], v[216:219]\n" \
  "ds_write_addtid_b32 v24 offset:38912\n" \
  "ds_write_addtid_b32 v25 offset:39168\n" \
  "v_mfma_f32_16x16x32_bf16 v[220:223], a[44:47], v[84:87], v[220:223]\n" \
  "ds_write_addtid_b32 v26 offset:39424\n" \
  "s_waitcnt vmcnt(9)\n" \
  "v_mfma_f32_16x16x32_bf16 v[208:211], a[64:67], v[88:91], v[208:211]\n" \
  "ds_write_addtid_b32 v27 offset:39680\n" \
  "ds_write_addtid_b32 v28 offset:39936\n" \
  "v_mfma_f32_16x16x32_bf16 v[212:215], a[68:71], v[88:91], v[212:215]\n" \
  "ds_write_addtid_b32 v29 offset:40192\n" \
  "ds_write_addtid_b32 v30 offset:40448\n" \
  "v_mfma_f32_16x16x32_bf16 v[216:219], a[72:75], v[88:91], v[216:219]\n" \
  "ds_write_addtid_b32 v31 offset:40704\n" \
  "v_cmp_eq_u32 vcc, -1, v108\n" \
  "v_mfma_f32_16x16x32_bf16 v[220:223], a[76:79], v[88:91], v[220:223]\n" \
  "v_and_b32 v62, 0x30, v59\n" \
  "v_add_u32 v62, s67, v62\n" \
  "v_mfma_f32_16x16x32_bf16 v[208:211], a[96:99], v[92:95], v[208:211]\n" \
  "s_cbranch_vccnz .Lsp2B_%=\n" \
  ".Lbp2B_%=:\n" \
  "v_lshlrev_b32 v60, 16, v108\n" \
  "v_mfma_f32_16x16x32_bf16 v[212:215], a[100:103], v[92:95], v[212:215]\n" \
  "v_and_b32 v61, 0xffff0000, v108\n" \
  "v_add_f32 v52, v52, v60\n" \
  "v_mfma_f32_16x16x32_bf16 v[216:219], a[104:107], v[92:95], v[216:219]\n" \
  "v_add_f32 v53, v53, v61\n" \
  "v_lshlrev_b32 v60, 16, v109\n" \
  "v_mfma_f32_16x16x32_bf16 v[220:223], a[108:111], v[92:95], v[220:223]\n" \
  "v_and_b32 v61, 0xffff0000, v109\n" \
  "s_waitcnt vmcnt(3)\n" \
  "v_mfma_f32_16x16x32_bf16 v[208:211], a[128:131], v[96:99], v[208:211]\n" \
  "s_waitcnt lgkmcnt(0)\n" \
  "s_barrier\n" \
  "v_mfma_f32_16x16x32_bf16 v[212:215], a[132:135], v[96:99], v[212:215]\n" \
  "ds_read_b128 v[32:35], v58 offset:32768\n" \
  "ds_read_b128 v[36:39], v58 offset:40960\n" \
  "v_mfma_f32_16x16x32_bf16 v[216:219], a[136:139], v[96:99], v[216:219]\n" \
  "ds_read_b128 v[40:43], v58 offset:49152\n" \
  "ds_read_b128 v[44:47], v58 offset:57344\n" \
  "v_mfma_f32_16x16x32_bf16 v[220:223], a[140:143], v[96:99], v[220:223]\n" \
  "ds_read_b128 v[48:51], v58 offset:36864\n" \
  "ds_read_b128 v[240:243], v58 offset:45056\n" \
  "v_mfma_f32_16x16x32_bf16 v[208:211], a[160:163], v[100:103], v[208:211]\n" \
  "ds_read_b128 v[244:247], v58 offset:53248\n" \
  "ds_read_b128 v[248:251], v58 offset:61440\n" \
  "v_mfma_f32_16x16x32_bf16 v[212:215], a[164:167], v[100:103], v[212:215]\n" \
  "v_add_f32 v54, v54, v60\n" \
  "v_add_f32 v55, v55, v61\n" \
  "v_mfma_f32_16x16x32_bf16 v[216:219], a[168:171], v[100:103], v[216:219]\n" \
  "v_lshlrev_b32 v60, 16, v110\n" \
  "v_and_b32 v61, 0xffff0000, v110\n" \
  "v_mfma_f32_16x16x32_bf16 v[220:223], a[172:175], v[100:103], v[220:223]\n" \
  "v_add_f32 v252, v252, v60\n" \
  "s_waitcnt vmcnt(2)\n" \
  "v_mfma_f32_16x16x32_bf16 v[208:211], a[192:195], v[104:107], v[208:211]\n" \
  "v_add_f32 v253, v253, v61\n" \
  "v_lshlrev_b32 v60, 16, v111\n" \
  "v_mfma_f32_16x16x32_bf16 v[212:215], a[196:199], v[104:107], v[212:215]\n" \
  "v_and_b32 v61, 0xffff0000, v111\n" \
  "v_add_f32 v254, v254, v60\n" \
  "v_mfma_f32_16x16x32_bf16 v[216:219], a[200:203], v[104:107], v[216:219]\n" \
  "s_waitcnt lgkmcnt(4)\n" \
  "v_add_f32 v32, v32, v36\n" \
  "v_mfma_f32_16x16x32_bf16 v[220:223], a[204:207], v[104:107], v[220:223]\n" \
  "v_add_f32 v33, v33, v37\n" \
  "v_add_f32 v34, v34, v38\n" \
  "v_mfma_f32_16x16x32_bf16 v[224:227], v[144:147], v[72:75], 0\n" \
  "v_add_f32 v35, v35, v39\n" \
  "v_add_f32 v40, v40, v44\n" \
  "v_mfma_f32_16x16x32_bf16 v[228:231], v[148:151], v[72:75], 0\n" \
  "v_add_f32 v41, v41, v45\n" \
  "v_add_f32 v42, v42, v46\n" \
  "v_mfma_f32_16x16x32_bf16 v[232:235], v[152:155], v[72:75], 0\n" \
  "ds_write_addtid_b32 v208 offset:0\n" \
  "ds_write_addtid_b32 v209 offset:256\n" \
  "v_mfma_f32_16x16x32_bf16 v[236:239], v[156:159], v[72:75], 0\n" \
  "ds_write_addtid_b32 v210 offset:512\n" \
  "ds_write_addtid_b32 v211 offset:768\n" \
  "v_mfma_f32_16x16x32_bf16 v[224:227], v[176:179], v[76:79], v[224:227]\n" \
  "ds_write_addtid_b32 v212 offset:1024\n" \
  "ds_write_addtid_b32 v213 offset:1280\n" \
  "v_mfma_f32_16x16x32_bf16 v[228:231], v[180:183], v[76:79], v[228:231]\n" \
  "ds_write_addtid_b32 v214 offset:1536\n" \
  "ds_write_addtid_b32 v215 offset:1792\n" \
  "v_mfma_f32_16x16x32_bf16 v[232:235], v[184:187], v[76:79], v[232:235]\n" \
  "ds_write_addtid_b32 v216 offset:2048\n" \
  "ds_write_addtid_b32 v217 offset:2304\n" \
  "v_mfma_f32_16x16x32_bf16 v[236:239], v[188:191], v[76:79], v[236:239]\n" \
  "ds_write_addtid_b32 v218 offset:2560\n" \
  "ds_write_addtid_b32 v219 offset:2816\n" \
  "v_mfma_f32_16x16x32_bf16 v[224:227], a[16:19], v[80:83], v[224:227]\n" \
  "ds_write_addtid_b32 v220 offset:3072\n" \
  "ds_write_addtid_b32 v221 offset:3328\n" \
  "v_mfma_f32_16x16x32_bf16 v[228:231], a[20:23], v[80:83], v[228:231]\n" \
  "ds_write_addtid_b32 v222 offset:3584\n" \
  "ds_write_addtid_b32 v223 offset:3840\n" \
  "v_mfma_f32_16x16x32_bf16 v[232:235], a[24:27], v[80:83], v[232:235]\n" \
  "v_add_f32 v43, v43, v47\n" \
  "v_add_f32 v32, v32, v40\n" \
  "v_mfma_f32_16x16x32_bf16 v[236:239], a[28:31], v[80:83], v[236:239]\n" \
  "v_add_f32 v33, v33, v41\n" \
  "v_add_f32 v34, v34, v42\n" \
  "v_mfma_f32_16x16x32_bf16 v[224:227], a[48:51], v[84:87], v[224:227]\n" \
  "v_add_f32 v255, v255, v61\n" \
  "v_add_f32 v35, v35, v43\n" \
  "v_mfma_f32_16x16x32_bf16 v[228:231], a[52:55], v[84:87], v[228:231]\n" \
  "s_waitcnt vmcnt(0)\n" \
  "v_mfma_f32_16x16x32_bf16 v[232:235], a[56:59], v[84:87], v[232:235]\n" \
  "buffer_load_dwordx4 v[124:127], v59, s[36:39], s78 offen offset:0 sc1\n" \
  "v_cmp_eq_u32 vcc, -1, v112\n" \
  "v_mfma_f32_16x16x32_bf16 v[236:239], a[60:63], v[84:87], v[236:239]\n" \
  "s_waitcnt lgkmcnt(0)\n" \
  "s_barrier\n" \
  "v_mfma_f32_16x16x32_bf16 v[224:227], a[80:83], v[88:91], v[224:227]\n" \
  "ds_read_b128 v[16:19], v58 offset:0\n" \
  "ds_read_b128 v[20:23], v58 offset:8192\n" \
  "v_mfma_f32_16x16x32_bf16 v[228:231], a[84:87], v[88:91], v[228:231]\n" \
  "ds_read_b128 v[24:27], v58 offset:16384\n" \
  "ds_read_b128 v[28:31], v58 offset:24576\n" \
  "v_mfma_f32_16x16x32_bf16 v[232:235], a[88:91], v[88:91], v[232:235]\n" \
  "v_add_f32 v48, v48, v240\n" \
  "s_cbranch_vccnz .Lsp3A_%=\n" \
  ".Lbp3A_%=:\n" \
  "v_mfma_f32_16x16x32_bf16 v[236:239], a[92:95], v[88:91], v[236:239]\n" \
  "v_add_f32 v49, v49, v241\n" \
  "v_lshlrev_b32 v60, 16, v112\n" \
  "v_mfma_f32_16x16x32_bf16 v[224:227], a[112:115], v[92:95], v[224:227]\n" \
  "v_add_f32 v50, v50, v242\n" \
  "v_and_b32 v61, 0xffff0000, v112\n" \
  "v_mfma_f32_16x16x32_bf16 v[228:231], a[116:119], v[92:95], v[228:231]\n" \
  "v_add_f32 v51, v51, v243\n" \
  "v_add_f32 v64, v64, v60\n" \
  "v_mfma_f32_16x16x32_bf16 v[232:235], a[120:123], v[92:95], v[232:235]\n" \
  "v_add_f32 v244, v244, v248\n" \
  "v_add_f32 v65, v65, v61\n" \
  "v_mfma_f32_16x16x32_bf16 v[236:239], a[124:127], v[92:95], v[236:239]\n" \
  "v_add_f32 v245, v245, v249\n" \
  "v_lshlrev_b32 v60, 16, v113\n" \
  "v_mfma_f32_16x16x32_bf16 v[224:227], a[144:147], v[96:99], v[224:227]\n" \
  "v_add_f32 v246, v246, v250\n" \
  "v_and_b32 v61, 0xffff0000, v113\n" \
  "v_mfma_f32_16x16x32_bf16 v[228:231], a[148:151], v[96:99], v[228:231]\n" \
  "v_add_f32 v247, v247, v251\n" \
  "v_add_f32 v66, v66, v60\n" \
  "v_mfma_f32_16x16x32_bf16 v[232:235], a[152:155], v[96:99], v[232:235]\n" \
  "v_add_f32 v48, v48, v244\n" \
  "v_add_f32 v67, v67, v61\n" \
  "v_mfma_f32_16x16x32_bf16 v[236:239], a[156:159], v[96:99], v[236:239]\n" \
  "v_add_f32 v49, v49, v245\n" \
  "v_lshlrev_b32 v60, 16, v114\n" \
  "v_add_f32 v50, v50, v246\n" \
  "v_mfma_f32_16x16x32_bf16 v[224:227], a[176:179], v[100:103], v[224:227]\n" \
  "v_and_b32 v61, 0xffff0000, v114\n" \
  "v_add_f32 v51, v51, v247\n" \
  "v_add_f32 v68, v68, v60\n" \
  "v_mfma_f32_16x16x32_bf16 v[228:231], a[180:183], v[100:103], v[228:231]\n" \
  "v_add_f32 v69, v69, v61\n" \
  "v_lshlrev_b32 v60, 16, v115\n" \
  "v_and_b32 v61, 0xffff0000, v115\n" \
  "v_mfma_f32_16x16x32_bf16 v[232:235], a[184:187], v[100:103], v[232:235]\n" \
  "v_add_f32 v70, v70, v60\n" \
  "ds_read_b128 v[8:11], v62 offset:0\n" \
  "v_mfma_f32_16x16x32_bf16 v[236:239], a[188:191], v[100:103], v[236:239]\n" \
  "v_add_f32 v71, v71, v61\n" \
  "ds_read_b128 v[12:15], v62 offset:64\n" \
  "v_mfma_f32_16x16x32_bf16 v[224:227], a[208:211], v[104:107], v[224:227]\n" \
  "v_lshlrev_b32 v0, 16, v116\n" \
  "v_and_b32 v1, 0xffff0000, v116\n" \
  "v_mfma_f32_16x16x32_bf16 v[228:231], a[212:215], v[104:107], v[228:231]\n" \
  "v_lshlrev_b32 v2, 16, v117\n" \
  "v_and_b32 v3, 0xffff0000, v117\n" \
  "v_mfma_f32_16x16x32_bf16 v[232:235], a[216:219], v[104:107], v[232:235]\n" \
  "v_lshlrev_b32 v4, 16, v120\n" \
  "v_and_b32 v5, 0xffff0000, v120\n" \
  "v_mfma_f32_16x16x32_bf16 v[236:239], a[220:223], v[104:107], v[236:239]\n" \
  "v_lshlrev_b32 v6, 16, v121\n" \
  "v_and_b32 v7, 0xffff0000, v121\n" \
  "s_waitcnt lgkmcnt(0)\n" \
  "v_fma_f32 v0, v8, v64, v0\n" \
  "v_fma_f32 v1, v9, v65, v1\n" \
  "v_fma_f32 v2, v10, v66, v2\n" \
  "v_fma_f32 v3, v11, v67, v3\n" \
  "v_fma_f32 v4, v8, v68, v4\n" \
  "v_fma_f32 v5, v9, v69, v5\n" \
  "v_fma_f32 v6, v10, v70, v6\n" \
  "v_fma_f32 v7, v11, v71, v7\n" \
  "v_fma_f32 v0, v8, v32, v0\n" \
  "v_fma_f32 v1, v9, v33, v1\n" \
  "v_fma_f32 v2, v10, v34, v2\n" \
  "v_fma_f32 v3, v11, v35, v3\n" \
  "v_cvt_pk_bf16_f32 v0, v0, v1\n" \
  "v_cvt_pk_bf16_f32 v1, v2, v3\n" \
  "buffer_store_dwordx2 v[0:1], v56, s[44:47], 0 offen offset:0 sc1\n" \
  "v_fma_f32 v4, v8, v48, v4\n" \
  "v_fma_f32 v5, v9, v49, v5\n" \
  "v_fma_f32 v6, v10, v50, v6\n" \
  "v_fma_f32 v7, v11, v51, v7\n" \
  "v_cvt_pk_bf16_f32 v4, v4, v5\n" \
  "v_cvt_pk_bf16_f32 v5, v6, v7\n" \
  "buffer_store_dwordx2 v[4:5], v56, s[44:47], s35 offen offset:0 sc1\n" \
  "v_lshlrev_b32 v64, 16, v118\n" \
  "v_and_b32 v65, 0xffff0000, v118\n" \
  "v_lshlrev_b32 v66, 16, v119\n" \
  "v_and_b32 v67, 0xffff0000, v119\n" \
  "ds_write_addtid_b32 v224 offset:4096\n" \
  "ds_write_addtid_b32 v225 offset:4352\n" \
  "ds_write_addtid_b32 v226 offset:4608\n" \
  "ds_write_addtid_b32 v227 offset:4864\n" \
  "ds_write_addtid_b32 v228 offset:5120\n" \
  "ds_write_addtid_b32 v229 offset:5376\n" \
  "ds_write_addtid_b32 v230 offset:5632\n" \
  "ds_write_addtid_b32 v231 offset:5888\n" \
  "ds_write_addtid_b32 v232 offset:6144\n" \
  "ds_write_addtid_b32 v233 offset:6400\n" \
  "ds_write_addtid_b32 v234 offset:6656\n" \
  "ds_write_addtid_b32 v235 offset:6912\n" \
  "ds_write_addtid_b32 v236 offset:7168\n" \
  "ds_write_addtid_b32 v237 offset:7424\n" \
  "ds_write_addtid_b32 v238 offset:7680\n" \
  "ds_write_addtid_b32 v239 offset:7936\n" \
  "v_lshlrev_b32 v68, 16, v122\n" \
  "v_and_b32 v69, 0xffff0000, v122\n" \
  "v_lshlrev_b32 v70, 16, v123\n" \
  "v_and_b32 v71, 0xffff0000, v123\n" \
  "s_waitcnt lgkmcnt(15)\n" \
  "v_pk_add_f32 v[16:17], v[16:17], v[20:21]\n" \
  "v_pk_add_f32 v[18:19], v[18:19], v[22:23]\n" \
  "v_pk_add_f32 v[24:25], v[24:25], v[28:29]\n" \
  "v_pk_add_f32 v[26:27], v[26:27], v[30:31]\n" \
  "v_pk_add_f32 v[16:17], v[16:17], v[24:25]\n" \
  "v_pk_add_f32 v[18:19], v[18:19], v[26:27]\n" \
  "s_waitcnt lgkmcnt(0)\n" \
  "s_barrier\n" \
  "ds_read_b128 v[32:35], v58 offset:4096\n" \
  "ds_read_b128 v[36:39], v58 offset:12288\n" \
  "ds_read_b128 v[40:43], v58 offset:20480\n" \
  "ds_read_b128 v[44:47], v58 offset:28672\n" \
  "s_waitcnt vmcnt(2)\n" \
  "v_cmp_eq_u32 vcc, -1, v124\n" \
  "s_cbranch_vccnz .Lsp3B_%=\n" \
  ".Lbp3B_%=:\n" \
  "v_lshlrev_b32 v20, 16, v124\n" \
  "v_and_b32 v21, 0xffff0000, v124\n" \
  "v_lshlrev_b32 v22, 16, v125\n" \
  "v_and_b32 v23, 0xffff0000, v125\n" \
  "v_lshlrev_b32 v24, 16, v126\n" \
  "v_and_b32 v25, 0xffff0000, v126\n" \
  "v_lshlrev_b32 v26, 16, v127\n" \
  "v_and_b32 v27, 0xffff0000, v127\n" \
  "v_pk_add_f32 v[52:53], v[52:53], v[20:21]\n" \
  "v_pk_add_f32 v[54:55], v[54:55], v[22:23]\n" \
  "v_pk_add_f32 v[252:253], v[252:253], v[24:25]\n" \
  "v_pk_add_f32 v[254:255], v[254:255], v[26:27]\n" \
  "v_pk_fma_f32 v[64:65], v[12:13], v[52:53], v[64:65]\n" \
  "v_pk_fma_f32 v[66:67], v[14:15], v[54:55], v[66:67]\n" \
  "v_pk_fma_f32 v[68:69], v[12:13], v[252:253], v[68:69]\n" \
  "v_pk_fma_f32 v[70:71], v[14:15], v[254:255], v[70:71]\n" \
  "v_pk_fma_f32 v[64:65], v[12:13], v[16:17], v[64:65]\n" \
  "v_pk_fma_f32 v[66:67], v[14:15], v[18:19], v[66:67]\n" \
  "v_cvt_pk_bf16_f32 v64, v64, v65\n" \
  "v_cvt_pk_bf16_f32 v65, v66, v67\n" \
  "buffer_store_dwordx2 v[64:65], v56, s[44:47], 0 offen offset:32 sc1\n" \
  "s_waitcnt lgkmcnt(0)\n" \
  "v_pk_add_f32 v[32:33], v[32:33], v[36:37]\n" \
  "v_pk_add_f32 v[34:35], v[34:35], v[38:39]\n" \
  "v_pk_add_f32 v[40:41], v[40:41], v[44:45]\n" \
  "v_pk_add_f32 v[42:43], v[42:43], v[46:47]\n" \
  "v_pk_add_f32 v[32:33], v[32:33], v[40:41]\n" \
  "v_pk_add_f32 v[34:35], v[34:35], v[42:43]\n" \
  "v_pk_fma_f32 v[68:69], v[12:13], v[32:33], v[68:69]\n" \
  "v_pk_fma_f32 v[70:71], v[14:15], v[34:35], v[70:71]\n" \
  "v_cvt_pk_bf16_f32 v68, v68, v69\n" \
  "v_cvt_pk_bf16_f32 v69, v70, v71\n" \
  "buffer_store_dwordx2 v[68:69], v56, s[44:47], s35 offen offset:32 sc1\n" \
  "s_branch .Lend_%=\n" \
  ".LsaF_%=:\n" \
  "s_memrealtime s[94:95]\n" \
  "s_waitcnt lgkmcnt(0)\n" \
  ".LpaF_%=:\n" \
  "s_sleep 2\n" \
  "buffer_load_dword v63, v61, s[36:39], s59 offen offset:0 sc1\n" \
  "s_memrealtime s[96:97]\n" \
  "s_waitcnt vmcnt(0) lgkmcnt(0)\n" \
  "s_sub_u32 s96, s96, s94\n" \
  "s_cmp_gt_u32 s96, 0x4c4b40\n" \
  "s_cbranch_scc1 .LbaF_%=\n" \
  "v_cmp_eq_u32 vcc, 0, v63\n" \
  "s_and_b64 vcc, vcc, s[86:87]\n" \
  "s_cbranch_scc1 .LpaF_%=\n" \
  "s_branch .LbaF_%=\n" \
  ".LsbF_%=:\n" \
  "s_memrealtime s[94:95]\n" \
  "s_waitcnt lgkmcnt(0)\n" \
  ".LpbF_%=:\n" \
  "s_sleep 2\n" \
  "buffer_load_dword v63, v61, s[36:39], s59 offen offset:0 sc1\n" \
  "s_memrealtime s[96:97]\n" \
  "s_waitcnt vmcnt(0) lgkmcnt(0)\n" \
  "s_sub_u32 s96, s96, s94\n" \
  "s_cmp_gt_u32 s96, 0x4c4b40\n" \
  "s_cbranch_scc1 .LbbF_%=\n" \
  "v_cmp_eq_u32 vcc, 0, v63\n" \
  "s_and_b64 vcc, vcc, s[86:87]\n" \
  "s_cbranch_scc1 .LpbF_%=\n" \
  "s_branch .LbbF_%=\n" \
  ".LsaS_%=:\n" \
  "s_memrealtime s[94:95]\n" \
  "s_waitcnt lgkmcnt(0)\n" \
  ".LpaS_%=:\n" \
  "s_sleep 2\n" \
  "buffer_load_dword v63, v61, s[36:39], s59 offen offset:0 sc1\n" \
  "s_memrealtime s[96:97]\n" \
  "s_waitcnt vmcnt(0) lgkmcnt(0)\n" \
  "s_sub_u32 s96, s96, s94\n" \
  "s_cmp_gt_u32 s96, 0x4c4b40\n" \
  "s_cbranch_scc1 .LbaS_%=\n" \
  "v_cmp_eq_u32 vcc, 0, v63\n" \
  "s_and_b64 vcc, vcc, s[86:87]\n" \
  "s_cbranch_scc1 .LpaS_%=\n" \
  "s_branch .LbaS_%=\n" \
  ".LsbS_%=:\n" \
  "s_memrealtime s[94:95]\n" \
  "s_waitcnt lgkmcnt(0)\n" \
  ".LpbS_%=:\n" \
  "s_sleep 2\n" \
  "buffer_load_dword v63, v61, s[36:39], s59 offen offset:0 sc1\n" \
  "s_memrealtime s[96:97]\n" \
  "s_waitcnt vmcnt(0) lgkmcnt(0)\n" \
  "s_sub_u32 s96, s96, s94\n" \
  "s_cmp_gt_u32 s96, 0x4c4b40\n" \
  "s_cbranch_scc1 .LbbS_%=\n" \
  "v_cmp_eq_u32 vcc, 0, v63\n" \
  "s_and_b64 vcc, vcc, s[86:87]\n" \
  "s_cbranch_scc1 .LpbS_%=\n" \
  "s_branch .LbbS_%=\n" \
  ".Lsp3B_%=:\n" \
  "s_memrealtime s[94:95]\n" \
  "s_waitcnt lgkmcnt(0)\n" \
  ".Lpp3B_%=:\n" \
  "s_sub_u32 s101, s78, 0\n" \
  "buffer_load_dwordx4 v[124:127], v59, s[36:39], s101 offen offset:0 sc1\n" \
  "s_memrealtime s[96:97]\n" \
  "s_waitcnt vmcnt(0) lgkmcnt(0)\n" \
  "s_sub_u32 s96, s96, s94\n" \
  "s_cmp_gt_u32 s96, 0x4c4b40\n" \
  "s_cbranch_scc1 .Lbp3B_%=\n" \
  "v_cmp_eq_u32 vcc, -1, v124\n" \
  "s_cbranch_vccnz .Lpp3B_%=\n" \
  "s_branch .Lbp3B_%=\n" \
  ".Lspf_%=:\n" \
  "s_memrealtime s[94:95]\n" \
  "s_waitcnt lgkmcnt(0)\n" \
  ".Lppf_%=:\n" \
  "s_sleep 2\n" \
  "buffer_load_dword v53, v52, s[36:39], s30 offen offset:0 sc1\n" \
  "s_memrealtime s[96:97]\n" \
  "s_waitcnt vmcnt(0) lgkmcnt(0)\n" \
  "s_sub_u32 s96, s96, s94\n" \
  "s_cmp_gt_u32 s96, 0x4c4b40\n" \
  "s_cbranch_scc1 .Lbpf_%=\n" \
  "v_cmp_eq_u32 vcc, 0, v53\n" \
  "s_cbranch_vccnz .Lppf_%=\n" \
  "s_branch .Lbpf_%=\n" \
  ".Lsg4_%=:\n" \
  "s_memrealtime s[94:95]\n" \
  "s_waitcnt lgkmcnt(0)\n" \
  ".Lpg4_%=:\n" \
  "s_sleep 2\n" \
  "buffer_load_dword v63, v61, s[36:39], s59 offen offset:0 sc1\n" \
  "s_memrealtime s[96:97]\n" \
  "s_waitcnt vmcnt(0) lgkmcnt(0)\n" \
  "s_sub_u32 s96, s96, s94\n" \
  "s_cmp_gt_u32 s96, 0x4c4b40\n" \
  "s_cbranch_scc1 .Lbg4_%=\n" \
  "v_cmp_eq_u32 vcc, 0, v63\n" \
  "s_and_b64 vcc, vcc, s[86:87]\n" \
  "s_cbranch_scc1 .Lpg4_%=\n" \
  "s_branch .Lbg4_%=\n" \
  ".Lse_%=:\n" \
  "s_memrealtime s[94:95]\n" \
  "s_waitcnt lgkmcnt(0)\n" \
  ".Lpe_%=:\n" \
  "buffer_load_dword v64, v69, s[36:39], s79 offen offset:0 sc1\n" \
  "buffer_load_dword v65, v69, s[36:39], s80 offen offset:0 sc1\n" \
  "buffer_load_dword v66, v69, s[36:39], s81 offen offset:0 sc1\n" \
  "buffer_load_dword v67, v69, s[36:39], s82 offen offset:0 sc1\n" \
  "s_memrealtime s[96:97]\n" \
  "s_waitcnt vmcnt(0) lgkmcnt(0)\n" \
  "s_sub_u32 s96, s96, s94\n" \
  "s_cmp_gt_u32 s96, 0x4c4b40\n" \
  "s_cbranch_scc1 .Lbe_%=\n" \
  "v_max3_u32 v52, v64, v65, v66\n" \
  "v_max_u32 v52, v52, v67\n" \
  "v_cmp_eq_u32 vcc, -1, v52\n" \
  "s_cbranch_vccnz .Lpe_%=\n" \
  "s_branch .Lbe_%=\n" \
  ".Lsp1_%=:\n" \
  "s_memrealtime s[94:95]\n" \
  "s_waitcnt lgkmcnt(0)\n" \
  ".Lpp1_%=:\n" \
  "s_sub_u32 s101, s71, 409600\n" \
  "buffer_load_dwordx4 v[64:67], v59, s[36:39], s101 offen offset:0 sc1\n" \
  "s_sub_u32 s101, s76, 409600\n" \
  "buffer_load_dwordx4 v[52:55], v59, s[36:39], s101 offen offset:0 sc1\n" \
  "s_memrealtime s[96:97]\n" \
  "s_waitcnt vmcnt(0) lgkmcnt(0)\n" \
  "s_sub_u32 s96, s96, s94\n" \
  "s_cmp_gt_u32 s96, 0x4c4b40\n" \
  "s_cbranch_scc1 .Lbp1_%=\n" \
  "v_max_u32 v62, v64, v52\n" \
  "v_cmp_eq_u32 vcc, -1, v62\n" \
  "s_cbranch_vccnz .Lpp1_%=\n" \
  "s_branch .Lbp1_%=\n" \
  ".Lsp2A_%=:\n" \
  "s_memrealtime s[94:95]\n" \
  "s_waitcnt lgkmcnt(0)\n" \
  ".Lpp2A_%=:\n" \
  "s_sub_u32 s101, s72, 409600\n" \
  "buffer_load_dwordx4 v[60:63], v59, s[36:39], s101 offen offset:0 sc1\n" \
  "s_memrealtime s[96:97]\n" \
  "s_waitcnt vmcnt(0) lgkmcnt(0)\n" \
  "s_sub_u32 s96, s96, s94\n" \
  "s_cmp_gt_u32 s96, 0x4c4b40\n" \
  "s_cbranch_scc1 .Lbp2A_%=\n" \
  "v_cmp_eq_u32 vcc, -1, v60\n" \
  "s_cbranch_vccnz .Lpp2A_%=\n" \
  "s_branch .Lbp2A_%=\n" \
  ".Lsp2B_%=:\n" \
  "s_memrealtime s[94:95]\n" \
  "s_waitcnt lgkmcnt(0)\n" \
  ".Lpp2B_%=:\n" \
  "s_sub_u32 s101, s77, 409600\n" \
  "buffer_load_dwordx4 v[108:111], v59, s[36:39], s101 offen offset:0 sc1\n" \
  "s_memrealtime s[96:97]\n" \
  "s_waitcnt vmcnt(0) lgkmcnt(0)\n" \
  "s_sub_u32 s96, s96, s94\n" \
  "s_cmp_gt_u32 s96, 0x4c4b40\n" \
  "s_cbranch_scc1 .Lbp2B_%=\n" \
  "v_cmp_eq_u32 vcc, -1, v108\n" \
  "s_cbranch_vccnz .Lpp2B_%=\n" \
  "s_branch .Lbp2B_%=\n" \
  ".Lsp3A_%=:\n" \
  "s_memrealtime s[94:95]\n" \
  "s_waitcnt lgkmcnt(0)\n" \
  ".Lpp3A_%=:\n" \
  "s_sub_u32 s101, s73, 0\n" \
  "buffer_load_dwordx4 v[112:115], v59, s[36:39], s101 offen offset:0 sc1\n" \
  "s_memrealtime s[96:97]\n" \
  "s_waitcnt vmcnt(0) lgkmcnt(0)\n" \
  "s_sub_u32 s96, s96, s94\n" \
  "s_cmp_gt_u32 s96, 0x4c4b40\n" \
  "s_cbranch_scc1 .Lbp3A_%=\n" \
  "v_cmp_eq_u32 vcc, -1, v112\n" \
  "s_cbranch_vccnz .Lpp3A_%=\n" \
  "s_branch .Lbp3A_%=\n" \
  ".Lz0_%=:\n" \
  "s_branch .Lz1_%=\n" \
  ".Lxq5_%=:\n" \
  "s_mov_b64 exec, 1\n" \
  "buffer_store_dword v61, off, s[36:39], s54 offset:0 sc1\n" \
  "s_mov_b64 exec, -1\n" \
  "buffer_load_dword v63, v61, s[36:39], s75 offen offset:0 sc1\n" \
  "s_mov_b32 s75, s59\n" \
  "s_add_u32 s54, s54, 0x10000\n" \
  "s_add_u32 s83, s83, 819200\n" \
  "s_branch .Lyq5_%=\n" \
  ".Lz1_%=:\n" \
  "s_branch .Lz2_%=\n" \
  ".Lxt5_%=:\n" \
  "v_and_b32 v20, 0x300, v59\n" \
  "v_and_b32 v21, 0xf0, v59\n" \
  "v_lshlrev_b32 v20, 4, v20\n" \
  "s_mov_b32 m0, 162176\n" \
  "v_lshl_or_b32 v20, v21, 3, v20\n" \
  "buffer_load_dword v20, s[40:43], s101 offen lds\n" \
  "s_mov_b32 m0, s56\n" \
  "v_and_b32 v26, 0x3ff, v59\n" \
  "v_mov_b32 v22, -1\n" \
  "v_mov_b32 v23, -1\n" \
  "v_mov_b32 v24, -1\n" \
  "v_mov_b32 v25, -1\n" \
  "s_add_u32 s96, s74, 819200\n" \
  "buffer_store_dwordx4 v[22:25], v26, s[36:39], s96 offen offset:0 sc1\n" \
  "s_add_u32 s96, s74, 1228800\n" \
  "buffer_store_dwordx4 v[22:25], v26, s[36:39], s96 offen offset:0 sc1\n" \
  "s_add_u32 s96, s74, 1638400\n" \
  "buffer_store_dwordx4 v[22:25], v26, s[36:39], s96 offen offset:0 sc1\n" \
  "s_add_u32 s96, s74, 2048000\n" \
  "buffer_store_dwordx4 v[22:25], v26, s[36:39], s96 offen offset:0 sc1\n" \
  "s_add_u32 s96, s74, 2457600\n" \
  "buffer_store_dwordx4 v[22:25], v26, s[36:39], s96 offen offset:0 sc1\n" \
  "s_add_u32 s96, s74, 2867200\n" \
  "buffer_store_dwordx4 v[22:25], v26, s[36:39], s96 offen offset:0 sc1\n" \
  "s_add_u32 s96, s74, 3276800\n" \
  "buffer_store_dwordx4 v[22:25], v26, s[36:39], s96 offen offset:0 sc1\n" \
  "s_nop 0\n" \
  "s_branch .Lyt5_%=\n" \
  ".Lz2_%=:\n" \
  "s_branch .Lz3_%=\n" \
  ".Lxq4_%=:\n" \
  "s_mov_b64 exec, 1\n" \
  "buffer_store_dword v61, off, s[36:39], s54 offset:0 sc1\n" \
  "s_mov_b64 exec, -1\n" \
  "buffer_load_dword v63, v61, s[36:39], s75 offen offset:0 sc1\n" \
  "s_mov_b32 s75, s59\n" \
  "s_add_u32 s54, s54, 0x10000\n" \
  "s_add_u32 s83, s83, 819200\n" \
  "s_branch .Lyq4_%=\n" \
  ".Lz3_%=:\n" \
  "s_branch .Lz4_%=\n" \
  ".Lxt4_%=:\n" \
  "v_and_b32 v0, 0x300, v59\n" \
  "v_and_b32 v1, 0xf0, v59\n" \
  "v_lshlrev_b32 v0, 4, v0\n" \
  "s_mov_b32 m0, 162176\n" \
  "v_lshl_or_b32 v0, v1, 3, v0\n" \
  "buffer_load_dword v0, s[40:43], s101 offen lds\n" \
  "s_mov_b32 m0, s56\n" \
  "v_and_b32 v6, 0x3ff, v59\n" \
  "v_mov_b32 v2, -1\n" \
  "v_mov_b32 v3, -1\n" \
  "v_mov_b32 v4, -1\n" \
  "v_mov_b32 v5, -1\n" \
  "s_add_u32 s96, s74, 819200\n" \
  "buffer_store_dwordx4 v[2:5], v6, s[36:39], s96 offen offset:0 sc1\n" \
  "s_add_u32 s96, s74, 1228800\n" \
  "buffer_store_dwordx4 v[2:5], v6, s[36:39], s96 offen offset:0 sc1\n" \
  "s_add_u32 s96, s74, 1638400\n" \
  "buffer_store_dwordx4 v[2:5], v6, s[36:39], s96 offen offset:0 sc1\n" \
  "s_add_u32 s96, s74, 2048000\n" \
  "buffer_store_dwordx4 v[2:5], v6, s[36:39], s96 offen offset:0 sc1\n" \
  "s_add_u32 s96, s74, 2457600\n" \
  "buffer_store_dwordx4 v[2:5], v6, s[36:39], s96 offen offset:0 sc1\n" \
  "s_add_u32 s96, s74, 2867200\n" \
  "buffer_store_dwordx4 v[2:5], v6, s[36:39], s96 offen offset:0 sc1\n" \
  "s_add_u32 s96, s74, 3276800\n" \
  "buffer_store_dwordx4 v[2:5], v6, s[36:39], s96 offen offset:0 sc1\n" \
  "s_nop 0\n" \
  "s_branch .Lyt4_%=\n" \
  ".Lz4_%=:\n" \
  "s_branch .Lzl_%=\n" \
  ".Lend_%=:\n"
extern "C" __global__ __launch_bounds__(256, 1) void adm(
    const u16* x, const u16* wr, const u16* wi, const u16* w1, const u16* cvec, const u16* wmod, char* wsb, const u16* b1, const u16* b2, const u16* w2, u16* outp) {
  u64 ka = (u64)__builtin_amdgcn_kernarg_segment_ptr(), tab = (u64)TAB;
  unsigned bid = blockIdx.x, tid = threadIdx.x;
  asm volatile(ASM_FRONT ASM_GEMM ASM_END : "+{s[0:1]}"(ka), "+{s2}"(bid), "+{s[76:77]}"(tab), "+{v0}"(tid)::ASM_CLOB);
}

extern "C" void kernel_launch(void* const* d_in, const int* in_sizes, int n_in, void* d_out, int out_size, void* d_ws,
                              size_t ws_size, hipStream_t stream) {
  (void)hipFuncSetAttribute((const void*)adm, hipFuncAttributeMaxDynamicSharedMemorySize, 163840);
  adm<<<256, 256, 163840, stream>>>((const u16*)d_in[0], (const u16*)d_in[1], (const u16*)d_in[2], (const u16*)d_in[4], (const u16*)d_in[8], (const u16*)d_in[9],
                                    (char*)d_ws, (const u16*)d_in[5], (const u16*)d_in[7], (const u16*)d_in[6], (u16*)d_out);
}
```

```cpp
#include <hip/hip_runtime.h>
typedef unsigned short u16;
typedef unsigned long long u64;
static __device__ const unsigned __attribute__((aligned(128))) TAB[6736] = {
13312,13312,13312,13312,13312,0x2e1f3364,0x31a831a8,0x33642e1f,13312,0x31a831a8,0x34000000,0x31a8b1a8,13312,0x33642e1f,0x31a8b1a8,0xae1fb364,
13312,0x34000000,46080,0xb4000000,13312,0x3364ae1f,0xb1a8b1a8,0xae1f3364,13312,0x31a8b1a8,0xb4000000,0x31a831a8,13312,0x2e1fb364,0xb1a831a8,0x3364ae1f,
13312,46080,13312,46080,13312,0xae1fb364,0x31a831a8,0xb364ae1f,13312,0xb1a8b1a8,0x34000000,0xb1a831a8,13312,0xb364ae1f,0x31a8b1a8,0x2e1f3364,
13312,0xb4000000,46080,0x34000000,13312,0xb3642e1f,0xb1a8b1a8,0x2e1fb364,13312,0xb1a831a8,0xb4000000,0xb1a8b1a8,13312,0xae1f3364,0xb1a831a8,0xb3642e1f,
13312,13312,13312,13312,0x34000000,0x3364ae1f,0x31a8b1a8,0x2e1fb364,46080,0xb1a8b1a8,0xb4000000,0xb1a831a8,0xb4000000,0xae1f3364,0x31a831a8,0x3364ae1f,
13312,0x34000000,46080,0xb4000000,0x34000000,0xae1fb364,0xb1a831a8,0x33642e1f,46080,0xb1a831a8,0x34000000,0xb1a8b1a8,0xb4000000,0x33642e1f,0xb1a8b1a8,0x2e1f3364,
13312,46080,13312,46080,0x34000000,0xb3642e1f,0x31a8b1a8,0xae1f3364,46080,0x31a831a8,0xb4000000,0x31a8b1a8,0xb4000000,0x2e1fb364,0x31a831a8,0xb3642e1f,
13312,0xb4000000,46080,0x34000000,0x34000000,0x2e1f3364,0xb1a831a8,0xb364ae1f,46080,0x31a8b1a8,0x34000000,0x31a831a8,0xb4000000,0xb364ae1f,0xb1a8b1a8,0xae1fb364,
13312,13312,13312,13312,46080,0xae1fb364,0xb1a8b1a8,0xb364ae1f,13312,0x31a831a8,0x34000000,0x31a8b1a8,46080,0xb364ae1f,0xb1a831a8,0x2e1f3364,
13312,0x34000000,46080,0xb4000000,46080,0xb3642e1f,0x31a831a8,0x2e1fb364,13312,0x31a8b1a8,0xb4000000,0x31a831a8,46080,0xae1f3364,0x31a8b1a8,0xb3642e1f,
13312,46080,13312,46080,46080,0x2e1f3364,0xb1a8b1a8,0x33642e1f,13312,0xb1a8b1a8,0x34000000,0xb1a831a8,46080,0x33642e1f,0xb1a831a8,0xae1fb364,
13312,0xb4000000,46080,0x34000000,46080,0x3364ae1f,0x31a831a8,0xae1f3364,13312,0xb1a831a8,0xb4000000,0xb1a8b1a8,46080,0x2e1fb364,0x31a8b1a8,0x3364ae1f,
13312,13312,13312,13312,0xb4000000,0xb3642e1f,0xb1a831a8,0xae1f3364,46080,0xb1a8b1a8,0xb4000000,0xb1a831a8,0x34000000,0x2e1fb364,0xb1a8b1a8,0xb3642e1f,
13312,0x34000000,46080,0xb4000000,0xb4000000,0x2e1f3364,0x31a8b1a8,0xb364ae1f,46080,0xb1a831a8,0x34000000,0xb1a8b1a8,0x34000000,0xb364ae1f,0x31a831a8,0xae1fb364,
13312,46080,13312,46080,0xb4000000,0x3364ae1f,0xb1a831a8,0x2e1fb364,46080,0x31a831a8,0xb4000000,0x31a8b1a8,0x34000000,0xae1f3364,0xb1a8b1a8,0x3364ae1f,
13312,0xb4000000,46080,0x34000000,0xb4000000,0xae1fb364,0x31a8b1a8,0x33642e1f,46080,0x31a8b1a8,0x34000000,0x31a831a8,0x34000000,0x33642e1f,0x31a831a8,0x2e1f3364,
0x34000000,0x34000000,0x34000000,0x34000000,0x34000000,0x3364ae1f,0x31a8b1a8,0x2e1fb364,0x34000000,0x31a8b1a8,46080,0xb1a8b1a8,0x34000000,0x2e1fb364,0xb1a8b1a8,0xb3642e1f,
0x34000000,46080,0xb4000000,13312,0x34000000,0xae1fb364,0xb1a831a8,0x33642e1f,0x34000000,0xb1a8b1a8,13312,0x31a8b1a8,0x34000000,0xb364ae1f,0x31a831a8,0xae1fb364,
0x34000000,0xb4000000,0x34000000,0xb4000000,0x34000000,0xb3642e1f,0x31a8b1a8,0xae1f3364,0x34000000,0xb1a831a8,46080,0x31a831a8,0x34000000,0xae1f3364,0xb1a8b1a8,0x3364ae1f,
0x34000000,13312,0xb4000000,46080,0x34000000,0x2e1f3364,0xb1a831a8,0xb364ae1f,0x34000000,0x31a831a8,13312,0xb1a831a8,0x34000000,0x33642e1f,0x31a831a8,0x2e1f3364,
0x34000000,0x34000000,0x34000000,0x34000000,46080,0xae1fb364,0xb1a8b1a8,0xb364ae1f,0xb4000000,0xb1a831a8,13312,0x31a831a8,13312,0x33642e1f,0x31a8b1a8,0xae1fb364,
0x34000000,46080,0xb4000000,13312,46080,0xb3642e1f,0x31a831a8,0x2e1fb364,0xb4000000,0x31a831a8,46080,0xb1a831a8,13312,0x2e1fb364,0xb1a831a8,0x3364ae1f,
0x34000000,0xb4000000,0x34000000,0xb4000000,46080,0x2e1f3364,0xb1a8b1a8,0x33642e1f,0xb4000000,0x31a8b1a8,13312,0xb1a8b1a8,13312,0xb364ae1f,0x31a8b1a8,0x2e1f3364,
0x34000000,13312,0xb4000000,46080,46080,0x3364ae1f,0x31a831a8,0xae1f3364,0xb4000000,0xb1a8b1a8,46080,0x31a8b1a8,13312,0xae1f3364,0xb1a831a8,0xb3642e1f,
0x34000000,0x34000000,0x34000000,0x34000000,0xb4000000,0xb3642e1f,0xb1a831a8,0xae1f3364,0x34000000,0x31a8b1a8,46080,0xb1a8b1a8,0xb4000000,0xae1f3364,0x31a831a8,0x3364ae1f,
0x34000000,46080,0xb4000000,13312,0xb4000000,0x2e1f3364,0x31a8b1a8,0xb364ae1f,0x34000000,0xb1a8b1a8,13312,0x31a8b1a8,0xb4000000,0x33642e1f,0xb1a8b1a8,0x2e1f3364,
0x34000000,0xb4000000,0x34000000,0xb4000000,0xb4000000,0x3364ae1f,0xb1a831a8,0x2e1fb364,0x34000000,0xb1a831a8,46080,0x31a831a8,0xb4000000,0x2e1fb364,0x31a831a8,0xb3642e1f,
0x34000000,13312,0xb4000000,46080,0xb4000000,0xae1fb364,0x31a8b1a8,0x33642e1f,0x34000000,0x31a831a8,13312,0xb1a831a8,0xb4000000,0xb364ae1f,0xb1a8b1a8,0xae1fb364,
0x34000000,0x34000000,0x34000000,0x34000000,13312,0x2e1f3364,0x31a831a8,0x33642e1f,0xb4000000,0xb1a831a8,13312,0x31a831a8,46080,0xb364ae1f,0xb1a831a8,0x2e1f3364,
0x34000000,46080,0xb4000000,13312,13312,0x3364ae1f,0xb1a8b1a8,0xae1f3364,0xb4000000,0x31a831a8,46080,0xb1a831a8,46080,0xae1f3364,0x31a8b1a8,0xb3642e1f,
0x34000000,0xb4000000,0x34000000,0xb4000000,13312,0xae1fb364,0x31a831a8,0xb364ae1f,0xb4000000,0x31a8b1a8,13312,0xb1a8b1a8,46080,0x33642e1f,0xb1a831a8,0xae1fb364,
0x34000000,13312,0xb4000000,46080,13312,0xb3642e1f,0xb1a8b1a8,0x2e1fb364,0xb4000000,0xb1a8b1a8,46080,0x31a8b1a8,46080,0x2e1fb364,0x31a8b1a8,0x3364ae1f,
13312,13312,13312,13312,0x2a3e33d9,0x307232a7,0x32a73072,0x33d92a3e,0x2e1f3364,0x33642e1f,0x3364ae1f,0x2e1fb364,0x307232a7,0x33d9aa3e,0x2a3eb3d9,0xb2a7b072,
0x31a831a8,0x31a8b1a8,0xb1a8b1a8,0xb1a831a8,0x32a73072,0x2a3eb3d9,0xb3d92a3e,0x307232a7,0x33642e1f,0xae1fb364,0xae1f3364,0x3364ae1f,0x33d92a3e,0xb2a7b072,0x307232a7,0xaa3eb3d9,
0x34000000,0xb4000000,0x34000000,0xb4000000,0x33d9aa3e,0xb2a73072,0x3072b2a7,0xaa3e33d9,0x3364ae1f,0xae1f3364,0xae1fb364,0x33642e1f,0x32a7b072,0x2a3e33d9,0xb3d9aa3e,0x3072b2a7,
0x31a8b1a8,0x31a831a8,0xb1a831a8,0xb1a8b1a8,0x3072b2a7,0x33d92a3e,0x2a3e33d9,0xb2a73072,0x2e1fb364,0x3364ae1f,0x33642e1f,0x2e1f3364,0x2a3eb3d9,0x3072b2a7,0x32a7b072,0x33d9aa3e,
13312,13312,13312,13312,0x33d9aa3e,0x32a7b072,0x3072b2a7,0x2a3eb3d9,0xae1fb364,0xb364ae1f,0xb3642e1f,0xae1f3364,0xb2a73072,0x2a3e33d9,0x33d92a3e,0x3072b2a7,
0x31a831a8,0x31a8b1a8,0xb1a8b1a8,0xb1a831a8,0x3072b2a7,0xb3d9aa3e,0x2a3e33d9,0x32a7b072,0xb364ae1f,0x2e1f3364,0x2e1fb364,0xb3642e1f,0xaa3e33d9,0x3072b2a7,0xb2a73072,0x33d9aa3e,
0x34000000,0xb4000000,0x34000000,0xb4000000,0xaa3eb3d9,0x307232a7,0xb2a7b072,0x33d92a3e,0xb3642e1f,0x2e1fb364,0x2e1f3364,0xb364ae1f,0x307232a7,0xb3d92a3e,0x2a3eb3d9,0x32a73072,
0x31a8b1a8,0x31a831a8,0xb1a831a8,0xb1a8b1a8,0xb2a7b072,0x2a3eb3d9,0x33d9aa3e,0x307232a7,0xae1f3364,0xb3642e1f,0xb364ae1f,0xae1fb364,0x33d92a3e,0x32a73072,0x307232a7,0x2a3e33d9,
13312,13312,13312,13312,0xaa3eb3d9,0xb072b2a7,0xb2a7b072,0xb3d9aa3e,0x2e1f3364,0x33642e1f,0x3364ae1f,0x2e1fb364,0xb072b2a7,0xb3d92a3e,0xaa3e33d9,0x32a73072,
0x31a831a8,0x31a8b1a8,0xb1a8b1a8,0xb1a831a8,0xb2a7b072,0xaa3e33d9,0x33d9aa3e,0xb072b2a7,0x33642e1f,0xae1fb364,0xae1f3364,0x3364ae1f,0xb3d9aa3e,0x32a73072,0xb072b2a7,0x2a3e33d9,
0x34000000,0xb4000000,0x34000000,0xb4000000,0xb3d92a3e,0x32a7b072,0xb07232a7,0x2a3eb3d9,0x3364ae1f,0xae1f3364,0xae1fb364,0x33642e1f,0xb2a73072,0xaa3eb3d9,0x33d92a3e,0xb07232a7,
0x31a8b1a8,0x31a831a8,0xb1a831a8,0xb1a8b1a8,0xb07232a7,0xb3d9aa3e,0xaa3eb3d9,0x32a7b072,0x2e1fb364,0x3364ae1f,0x33642e1f,0x2e1f3364,0xaa3e33d9,0xb07232a7,0xb2a73072,0xb3d92a3e,
13312,13312,13312,13312,0xb3d92a3e,0xb2a73072,0xb07232a7,0xaa3e33d9,0xae1fb364,0xb364ae1f,0xb3642e1f,0xae1f3364,0x32a7b072,0xaa3eb3d9,0xb3d9aa3e,0xb07232a7,
0x31a831a8,0x31a8b1a8,0xb1a8b1a8,0xb1a831a8,0xb07232a7,0x33d92a3e,0xaa3eb3d9,0xb2a73072,0xb364ae1f,0x2e1f3364,0x2e1fb364,0xb3642e1f,0x2a3eb3d9,0xb07232a7,0x32a7b072,0xb3d92a3e,
0x34000000,0xb4000000,0x34000000,0xb4000000,0x2a3e33d9,0xb072b2a7,0x32a73072,0xb3d9aa3e,0xb3642e1f,0x2e1fb364,0x2e1f3364,0xb364ae1f,0xb072b2a7,0x33d9aa3e,0xaa3e33d9,0xb2a7b072,
0x31a8b1a8,0x31a831a8,0xb1a831a8,0xb1a8b1a8,0x32a73072,0xaa3e33d9,0xb3d92a3e,0xb072b2a7,0xae1f3364,0xb3642e1f,0xb364ae1f,0xae1fb364,0xb3d9aa3e,0xb2a7b072,0xb072b2a7,0xaa3eb3d9,
0x34000000,0x34000000,0x34000000,0x34000000,0x33d9aa3e,0x32a7b072,0x3072b2a7,0x2a3eb3d9,0x3364ae1f,0x2e1fb364,0xae1fb364,0xb364ae1f,0x32a7b072,0xaa3eb3d9,0xb3d9aa3e,0xb07232a7,
0x31a8b1a8,0xb1a8b1a8,0xb1a831a8,0x31a831a8,0x3072b2a7,0xb3d9aa3e,0x2a3e33d9,0x32a7b072,0x2e1fb364,0xb3642e1f,0x33642e1f,0xae1fb364,0x2a3eb3d9,0xb07232a7,0x32a7b072,0xb3d92a3e,
46080,13312,46080,13312,0xaa3eb3d9,0x307232a7,0xb2a7b072,0x33d92a3e,0xae1fb364,0x33642e1f,0xb3642e1f,0x2e1fb364,0xb072b2a7,0x33d9aa3e,0xaa3e33d9,0xb2a7b072,
0xb1a8b1a8,0x31a8b1a8,0x31a831a8,0xb1a831a8,0xb2a7b072,0x2a3eb3d9,0x33d9aa3e,0x307232a7,0xb364ae1f,0xae1fb364,0x2e1fb364,0x3364ae1f,0xb3d9aa3e,0xb2a7b072,0xb072b2a7,0xaa3eb3d9,
0x34000000,0x34000000,0x34000000,0x34000000,0xaa3eb3d9,0xb072b2a7,0xb2a7b072,0xb3d9aa3e,0xb3642e1f,0xae1f3364,0x2e1f3364,0x33642e1f,0x307232a7,0x33d9aa3e,0x2a3eb3d9,0xb2a7b072,
0x31a8b1a8,0xb1a8b1a8,0xb1a831a8,0x31a831a8,0xb2a7b072,0xaa3e33d9,0x33d9aa3e,0xb072b2a7,0xae1f3364,0x3364ae1f,0xb364ae1f,0x2e1f3364,0x33d92a3e,0xb2a7b072,0x307232a7,0xaa3eb3d9,
46080,13312,46080,13312,0xb3d92a3e,0x32a7b072,0xb07232a7,0x2a3eb3d9,0x2e1f3364,0xb364ae1f,0x3364ae1f,0xae1f3364,0x32a7b072,0x2a3e33d9,0xb3d9aa3e,0x3072b2a7,
0xb1a8b1a8,0x31a8b1a8,0x31a831a8,0xb1a831a8,0xb07232a7,0xb3d9aa3e,0xaa3eb3d9,0x32a7b072,0x33642e1f,0x2e1f3364,0xae1f3364,0xb3642e1f,0x2a3eb3d9,0x3072b2a7,0x32a7b072,0x33d9aa3e,
0x34000000,0x34000000,0x34000000,0x34000000,0xb3d92a3e,0xb2a73072,0xb07232a7,0xaa3e33d9,0x3364ae1f,0x2e1fb364,0xae1fb364,0xb364ae1f,0xb2a73072,0x2a3e33d9,0x33d92a3e,0x3072b2a7,
0x31a8b1a8,0xb1a8b1a8,0xb1a831a8,0x31a831a8,0xb07232a7,0x33d92a3e,0xaa3eb3d9,0xb2a73072,0x2e1fb364,0xb3642e1f,0x33642e1f,0xae1fb364,0xaa3e33d9,0x3072b2a7,0xb2a73072,0x33d9aa3e,
46080,13312,46080,13312,0x2a3e33d9,0xb072b2a7,0x32a73072,0xb3d9aa3e,0xae1fb364,0x33642e1f,0xb3642e1f,0x2e1fb364,0x307232a7,0xb3d92a3e,0x2a3eb3d9,0x32a73072,
0xb1a8b1a8,0x31a8b1a8,0x31a831a8,0xb1a831a8,0x32a73072,0xaa3e33d9,0xb3d92a3e,0xb072b2a7,0xb364ae1f,0xae1fb364,0x2e1fb364,0x3364ae1f,0x33d92a3e,0x32a73072,0x307232a7,0x2a3e33d9,
0x34000000,0x34000000,0x34000000,0x34000000,0x2a3e33d9,0x307232a7,0x32a73072,0x33d92a3e,0xb3642e1f,0xae1f3364,0x2e1f3364,0x33642e1f,0xb072b2a7,0xb3d92a3e,0xaa3e33d9,0x32a73072,
0x31a8b1a8,0xb1a8b1a8,0xb1a831a8,0x31a831a8,0x32a73072,0x2a3eb3d9,0xb3d92a3e,0x307232a7,0xae1f3364,0x3364ae1f,0xb364ae1f,0x2e1f3364,0xb3d9aa3e,0x32a73072,0xb072b2a7,0x2a3e33d9,
46080,13312,46080,13312,0x33d9aa3e,0xb2a73072,0x3072b2a7,0xaa3e33d9,0x2e1f3364,0xb364ae1f,0x3364ae1f,0xae1f3364,0xb2a73072,0xaa3eb3d9,0x33d92a3e,0xb07232a7,
0xb1a8b1a8,0x31a8b1a8,0x31a831a8,0xb1a831a8,0x3072b2a7,0x33d92a3e,0x2a3e33d9,0xb2a73072,0x33642e1f,0x2e1f3364,0xae1f3364,0xb3642e1f,0xaa3e33d9,0xb07232a7,0xb2a73072,0xb3d92a3e,
13312,13312,13312,0,13312,0x31243221,0x33e1298f,0,13312,0x33e1298f,0x2d79b384,0,0,0,0,0,
13312,0x32eeb000,0xb2eeb000,0,13312,0x2d79b384,0xb1243221,0,13312,0xad79b384,0x31243221,0,0,0,0,0,
13312,0xb2eeb000,0x32eeb000,0,13312,0xb3e1298f,0xad79b384,0,13312,0xb1243221,0xb3e1298f,0,0,0,0,0,
0,0,0,0,0,0,0,0,0,0,0,0,0,0,0,0,
13312,13312,13312,0,0x32eeb000,0x2d79b384,0xad79b384,0,0xb2eeb000,0xb1243221,0x31243221,0,0,0,0,0,
13312,0x32eeb000,0xb2eeb000,0,0x32eeb000,0xb3e1298f,0x33e1298f,0,0xb2eeb000,0x33e1298f,0xb3e1298f,0,0,0,0,0,
13312,0xb2eeb000,0x32eeb000,0,0x32eeb000,0x31243221,0xb1243221,0,0xb2eeb000,0xad79b384,0x2d79b384,0,0,0,0,0,
0,0,0,0,0,0,0,0,0,0,0,0,0,0,0,0,
13312,13312,13312,0,0xb2eeb000,0xb3e1298f,0xb1243221,0,0x32eeb000,0xad79b384,0xb3e1298f,0,0,0,0,0,
13312,0x32eeb000,0xb2eeb000,0,0xb2eeb000,0x31243221,0xad79b384,0,0x32eeb000,0xb1243221,0x2d79b384,0,0,0,0,0,
13312,0xb2eeb000,0x32eeb000,0,0xb2eeb000,0x2d79b384,0x33e1298f,0,0x32eeb000,0x33e1298f,0x31243221,0,0,0,0,0,
0,0,0,0,0,0,0,0,0,0,0,0,0,0,0,0,
0,0,0,0,0,0,0,0,0,0,0,0,0,0,0,0,
0,0,0,0,0,0,0,0,0,0,0,0,0,0,0,0,
0,0,0,0,0,0,0,0,0,0,0,0,0,0,0,0,
0,0,0,0,0,0,0,0,0,0,0,0,0,0,0,0,
0x34000000,0x34000000,0x34000000,0,0x34000000,0x3221b124,0x298fb3e1,0,0x34000000,0x298fb3e1,0xb384ad79,0,0,0,0,0,
0x34000000,0xb000b2ee,0xb00032ee,0,0x34000000,0xb384ad79,0x32213124,0,0x34000000,0xb3842d79,0x3221b124,0,0,0,0,0,
0x34000000,0xb00032ee,0xb000b2ee,0,0x34000000,0x298f33e1,0xb3842d79,0,0x34000000,0x32213124,0x298f33e1,0,0,0,0,0,
0,0,0,0,0,0,0,0,0,0,0,0,0,0,0,0,
0x34000000,0x34000000,0x34000000,0,0xb000b2ee,0xb384ad79,0xb3842d79,0,0xb00032ee,0x32213124,0x3221b124,0,0,0,0,0,
0x34000000,0xb000b2ee,0xb00032ee,0,0xb000b2ee,0x298f33e1,0x298fb3e1,0,0xb00032ee,0x298fb3e1,0x298f33e1,0,0,0,0,0,
0x34000000,0xb00032ee,0xb000b2ee,0,0xb000b2ee,0x3221b124,0x32213124,0,0xb00032ee,0xb3842d79,0xb384ad79,0,0,0,0,0,
0,0,0,0,0,0,0,0,0,0,0,0,0,0,0,0,
0x34000000,0x34000000,0x34000000,0,0xb00032ee,0x298f33e1,0x32213124,0,0xb000b2ee,0xb3842d79,0x298f33e1,0,0,0,0,0,
0x34000000,0xb000b2ee,0xb00032ee,0,0xb00032ee,0x3221b124,0xb3842d79,0,0xb000b2ee,0x32213124,0xb384ad79,0,0,0,0,0,
0x34000000,0xb00032ee,0xb000b2ee,0,0xb00032ee,0xb384ad79,0x298fb3e1,0,0xb000b2ee,0x298fb3e1,0x3221b124,0,0,0,0,0,
0,0,0,0,0,0,0,0,0,0,0,0,0,0,0,0,
0,0,0,0,0,0,0,0,0,0,0,0,0,0,0,0,
0,0,0,0,0,0,0,0,0,0,0,0,0,0,0,0,
0,0,0,0,0,0,0,0,0,0,0,0,0,0,0,0,
0,0,0,0,0,0,0,0,0,0,0,0,0,0,0,0,
13312,0x2a3e33d9,0x2e1f3364,0x307232a7,13312,0x307232a7,0x33642e1f,0x33d9aa3e,13312,0x32a73072,0x3364ae1f,0x2a3eb3d9,13312,0x33d92a3e,0x2e1fb364,0xb2a7b072,
13312,0x33d9aa3e,0xae1fb364,0xb2a73072,13312,0x32a7b072,0xb364ae1f,0x2a3e33d9,13312,0x3072b2a7,0xb3642e1f,0x33d92a3e,13312,0x2a3eb3d9,0xae1f3364,0x3072b2a7,
13312,0xaa3eb3d9,0x2e1f3364,0xb072b2a7,13312,0xb072b2a7,0x33642e1f,0xb3d92a3e,13312,0xb2a7b072,0x3364ae1f,0xaa3e33d9,13312,0xb3d9aa3e,0x2e1fb364,0x32a73072,
13312,0xb3d92a3e,0xae1fb364,0x32a7b072,13312,0xb2a73072,0xb364ae1f,0xaa3eb3d9,13312,0xb07232a7,0xb3642e1f,0xb3d9aa3e,13312,0xaa3e33d9,0xae1f3364,0xb07232a7,
0x31a831a8,0x32a73072,0x33642e1f,0x33d92a3e,0x31a8b1a8,0x2a3eb3d9,0xae1fb364,0xb2a7b072,0xb1a8b1a8,0xb3d92a3e,0xae1f3364,0x307232a7,0xb1a831a8,0x307232a7,0x3364ae1f,0xaa3eb3d9,
0x31a831a8,0x3072b2a7,0xb364ae1f,0xaa3e33d9,0x31a8b1a8,0xb3d9aa3e,0x2e1f3364,0x3072b2a7,0xb1a8b1a8,0x2a3e33d9,0x2e1fb364,0xb2a73072,0xb1a831a8,0x32a7b072,0xb3642e1f,0x33d9aa3e,
0x31a831a8,0xb2a7b072,0x33642e1f,0xb3d9aa3e,0x31a8b1a8,0xaa3e33d9,0xae1fb364,0x32a73072,0xb1a8b1a8,0x33d9aa3e,0xae1f3364,0xb072b2a7,0xb1a831a8,0xb072b2a7,0x3364ae1f,0x2a3e33d9,
0x31a831a8,0xb07232a7,0xb364ae1f,0x2a3eb3d9,0x31a8b1a8,0x33d92a3e,0x2e1f3364,0xb07232a7,0xb1a8b1a8,0xaa3eb3d9,0x2e1fb364,0x32a7b072,0xb1a831a8,0xb2a73072,0xb3642e1f,0xb3d92a3e,
0x34000000,0x33d9aa3e,0x3364ae1f,0x32a7b072,0xb4000000,0xb2a73072,0xae1f3364,0x2a3e33d9,0x34000000,0x3072b2a7,0xae1fb364,0xb3d9aa3e,0xb4000000,0xaa3e33d9,0x33642e1f,0x3072b2a7,
0x34000000,0xaa3eb3d9,0xb3642e1f,0x307232a7,0xb4000000,0x307232a7,0x2e1fb364,0xb3d92a3e,0x34000000,0xb2a7b072,0x2e1f3364,0x2a3eb3d9,0xb4000000,0x33d92a3e,0xb364ae1f,0x32a73072,
0x34000000,0xb3d92a3e,0x3364ae1f,0xb2a73072,0xb4000000,0x32a7b072,0xae1f3364,0xaa3eb3d9,0x34000000,0xb07232a7,0xae1fb364,0x33d92a3e,0xb4000000,0x2a3eb3d9,0x33642e1f,0xb07232a7,
0x34000000,0x2a3e33d9,0xb3642e1f,0xb072b2a7,0xb4000000,0xb072b2a7,0x2e1fb364,0x33d9aa3e,0x34000000,0x32a73072,0x2e1f3364,0xaa3e33d9,0xb4000000,0xb3d9aa3e,0xb364ae1f,0xb2a7b072,
0x31a8b1a8,0x3072b2a7,0x2e1fb364,0x2a3eb3d9,0x31a831a8,0x33d92a3e,0x3364ae1f,0x3072b2a7,0xb1a831a8,0x2a3e33d9,0x33642e1f,0x32a7b072,0xb1a8b1a8,0xb2a73072,0x2e1f3364,0x33d9aa3e,
0x31a8b1a8,0xb2a7b072,0xae1f3364,0x33d92a3e,0x31a831a8,0x2a3eb3d9,0xb3642e1f,0x32a73072,0xb1a831a8,0x33d9aa3e,0xb364ae1f,0x307232a7,0xb1a8b1a8,0x307232a7,0xae1fb364,0x2a3e33d9,
0x31a8b1a8,0xb07232a7,0x2e1fb364,0xaa3e33d9,0x31a831a8,0xb3d9aa3e,0x3364ae1f,0xb07232a7,0xb1a831a8,0xaa3eb3d9,0x33642e1f,0xb2a73072,0xb1a8b1a8,0x32a7b072,0x2e1f3364,0xb3d92a3e,
0x31a8b1a8,0x32a73072,0xae1f3364,0xb3d9aa3e,0x31a831a8,0xaa3e33d9,0xb3642e1f,0xb2a7b072,0xb1a831a8,0xb3d92a3e,0xb364ae1f,0xb072b2a7,0xb1a8b1a8,0xb072b2a7,0xae1fb364,0xaa3eb3d9,
0x34000000,0x33d9aa3e,0x3364ae1f,0x32a7b072,0x34000000,0x32a7b072,0x2e1fb364,0xaa3eb3d9,0x34000000,0x3072b2a7,0xae1fb364,0xb3d9aa3e,0x34000000,0x2a3eb3d9,0xb364ae1f,0xb07232a7,
0x34000000,0xaa3eb3d9,0xb3642e1f,0x307232a7,0x34000000,0xb072b2a7,0xae1f3364,0x33d9aa3e,0x34000000,0xb2a7b072,0x2e1f3364,0x2a3eb3d9,0x34000000,0xb3d9aa3e,0x33642e1f,0xb2a7b072,
0x34000000,0xb3d92a3e,0x3364ae1f,0xb2a73072,0x34000000,0xb2a73072,0x2e1fb364,0x2a3e33d9,0x34000000,0xb07232a7,0xae1fb364,0x33d92a3e,0x34000000,0xaa3e33d9,0xb364ae1f,0x3072b2a7,
0x34000000,0x2a3e33d9,0xb3642e1f,0xb072b2a7,0x34000000,0x307232a7,0xae1f3364,0xb3d92a3e,0x34000000,0x32a73072,0x2e1f3364,0xaa3e33d9,0x34000000,0x33d92a3e,0x33642e1f,0x32a73072,
0x31a8b1a8,0x3072b2a7,0x2e1fb364,0x2a3eb3d9,0xb1a8b1a8,0xb3d9aa3e,0xb3642e1f,0xb07232a7,0xb1a831a8,0x2a3e33d9,0x33642e1f,0x32a7b072,0x31a831a8,0x32a7b072,0xae1fb364,0xb3d92a3e,
0x31a8b1a8,0xb2a7b072,0xae1f3364,0x33d92a3e,0xb1a8b1a8,0xaa3e33d9,0x3364ae1f,0xb2a7b072,0xb1a831a8,0x33d9aa3e,0xb364ae1f,0x307232a7,0x31a831a8,0xb072b2a7,0x2e1f3364,0xaa3eb3d9,
0x31a8b1a8,0xb07232a7,0x2e1fb364,0xaa3e33d9,0xb1a8b1a8,0x33d92a3e,0xb3642e1f,0x3072b2a7,0xb1a831a8,0xaa3eb3d9,0x33642e1f,0xb2a73072,0x31a831a8,0xb2a73072,0xae1fb364,0x33d9aa3e,
0x31a8b1a8,0x32a73072,0xae1f3364,0xb3d9aa3e,0xb1a8b1a8,0x2a3eb3d9,0x3364ae1f,0x32a73072,0xb1a831a8,0xb3d92a3e,0xb364ae1f,0xb072b2a7,0x31a831a8,0x307232a7,0x2e1f3364,0x2a3e33d9,
46080,0xaa3eb3d9,0xae1fb364,0xb072b2a7,13312,0x307232a7,0x33642e1f,0x33d9aa3e,46080,0xb2a7b072,0xb3642e1f,0xaa3e33d9,13312,0x33d92a3e,0x2e1fb364,0xb2a7b072,
46080,0xb3d92a3e,0x2e1f3364,0x32a7b072,13312,0x32a7b072,0xb364ae1f,0x2a3e33d9,46080,0xb07232a7,0x3364ae1f,0xb3d9aa3e,13312,0x2a3eb3d9,0xae1f3364,0x3072b2a7,
46080,0x2a3e33d9,0xae1fb364,0x307232a7,13312,0xb072b2a7,0x33642e1f,0xb3d92a3e,46080,0x32a73072,0xb3642e1f,0x2a3eb3d9,13312,0xb3d9aa3e,0x2e1fb364,0x32a73072,
46080,0x33d9aa3e,0x2e1f3364,0xb2a73072,13312,0xb2a73072,0xb364ae1f,0xaa3eb3d9,46080,0x3072b2a7,0x3364ae1f,0x33d92a3e,13312,0xaa3e33d9,0xae1f3364,0xb07232a7,
0xb1a8b1a8,0xb2a7b072,0xb364ae1f,0xb3d9aa3e,0x31a8b1a8,0x2a3eb3d9,0xae1fb364,0xb2a7b072,0x31a831a8,0x33d9aa3e,0x2e1fb364,0xb072b2a7,0xb1a831a8,0x307232a7,0x3364ae1f,0xaa3eb3d9,
0xb1a8b1a8,0xb07232a7,0x33642e1f,0x2a3eb3d9,0x31a8b1a8,0xb3d9aa3e,0x2e1f3364,0x3072b2a7,0x31a831a8,0xaa3eb3d9,0xae1f3364,0x32a7b072,0xb1a831a8,0x32a7b072,0xb3642e1f,0x33d9aa3e,
0xb1a8b1a8,0x32a73072,0xb364ae1f,0x33d92a3e,0x31a8b1a8,0xaa3e33d9,0xae1fb364,0x32a73072,0x31a831a8,0xb3d92a3e,0x2e1fb364,0x307232a7,0xb1a831a8,0xb072b2a7,0x3364ae1f,0x2a3e33d9,
0xb1a8b1a8,0x3072b2a7,0x33642e1f,0xaa3e33d9,0x31a8b1a8,0x33d92a3e,0x2e1f3364,0xb07232a7,0x31a831a8,0x2a3e33d9,0xae1f3364,0xb2a73072,0xb1a831a8,0xb2a73072,0xb3642e1f,0xb3d92a3e,
0x3f800000,0x3f800000,0x3f800000,0x3f800000,0x3f800000,0x3f7fec43,0x3f7fb10f,0x3f7f4e6d,0x3f800000,0x3f7fb10f,0x3f7ec46d,0x3f7d3aac,0x3f800000,0x3f7f4e6d,0x3f7d3aac,0x3f79c79d,
0x3f800000,0x3f7ec46d,0x3f7b14be,0x3f74fa0b,0x3f800000,0x3f7e1324,0x3f7853f8,0x3f6ed89e,0x3f800000,0x3f7d3aac,0x3f74fa0b,0x3f676bd8,0x3f800000,0x3f7c3b28,0x3f710908,0x3f5ebe05,
0x3f800000,0x3f7b14be,0x3f6c835e,0x3f54db31,0x3f800000,0x3f79c79d,0x3f676bd8,0x3f49d112,0x3f800000,0x3f7853f8,0x3f61c598,0x3f3daef9,0x3f800000,0x3f76ba07,0x3f5b941a,0x3f3085bb,
0x3f800000,0x3f74fa0b,0x3f54db31,0x3f226799,0x3f800000,0x3f731447,0x3f4d9f02,0x3f13682a,0x3f800000,0x3f710908,0x3f45e403,0x3f039c3d,0x3f800000,0x3f6ed89e,0x3f3daef9,0x3ee63375,
0x3f800000,0x3f800000,0x3f800000,0x3f800000,0x3f7ec46d,0x3f7e1324,0x3f7d3aac,0x3f7c3b28,0x3f7b14be,0x3f7853f8,0x3f74fa0b,0x3f710908,0x3f74fa0b,0x3f6ed89e,0x3f676bd8,0x3f5ebe05,
0x3f6c835e,0x3f61c598,0x3f54db31,0x3f45e403,0x3f61c598,0x3f514d3d,0x3f3daef9,0x3f273656,0x3f54db31,0x3f3daef9,0x3f226799,0x3f039c3d,0x3f45e403,0x3f273656,0x3f039c3d,0x3eb8442a,
0x3f3504f3,0x3f0e39da,0x3ec3ef15,0x3e47c5c2,0x3f226799,0x3ee63375,0x3e78cfcc,0x3cc90ab0,0x3f0e39da,0x3eac7cd4,0x3dc8bd36,0xbe164083,0x3ef15aea,0x3e605c13,0xbd48fb30,0xbea09ae5,
0x3ec3ef15,0x3dc8bd36,0xbe47c5c2,0xbef15aea,0x3e94a031,0xbcc90ab0,0xbeac7cd4,0xbf1d7fd1,0x3e47c5c2,0xbe164083,0xbef15aea,0xbf3daef9,0x3dc8bd36,0xbe888e93,0xbf187fc0,0xbf584853,
0x3f800000,0x3f800000,0x3f800000,0x3f800000,0x3f7b14be,0x3f79c79d,0x3f7853f8,0x3f76ba07,0x3f6c835e,0x3f676bd8,0x3f61c598,0x3f5b941a,0x3f54db31,0x3f49d112,0x3f3daef9,0x3f3085bb,
0x3f3504f3,0x3f226799,0x3f0e39da,0x3ef15aea,0x3f0e39da,0x3ee63375,0x3eac7cd4,0x3e605c13,0x3ec3ef15,0x3e78cfcc,0x3dc8bd36,0xbd48fb30,0x3e47c5c2,0x3cc90ab0,0xbe164083,0xbea09ae5,
0x248d3132,0xbe47c5c2,0xbec3ef15,0xbf0e39da,0xbe47c5c2,0xbecf7bca,0xbf187fc0,0xbf41d870,0xbec3ef15,0xbf187fc0,0xbf45e403,0xbf676bd8,0xbf0e39da,0xbf41d870,0xbf676bd8,0xbf7c3b28,
0xbf3504f3,0xbf61c598,0xbf7b14be,0xbf7ec46d,0xbf54db31,0xbf76ba07,0xbf7fb10f,0xbf6ed89e,0xbf6c835e,0xbf7fb10f,0xbf74fa0b,0xbf4d9f02,0xbf7b14be,0xbf7c3b28,0xbf5b941a,0xbf1d7fd1,
0x3f800000,0x3f800000,0x3f800000,0x3f800000,0x3f74fa0b,0x3f731447,0x3f710908,0x3f6ed89e,0x3f54db31,0x3f4d9f02,0x3f45e403,0x3f3daef9,0x3f226799,0x3f13682a,0x3f039c3d,0x3ee63375,
0x3ec3ef15,0x3e94a031,0x3e47c5c2,0x3dc8bd36,0x3dc8bd36,0xbcc90ab0,0xbe164083,0xbe888e93,0xbe47c5c2,0xbeac7cd4,0xbef15aea,0xbf187fc0,0xbef15aea,0xbf1d7fd1,0xbf3daef9,0xbf584853,
0xbf3504f3,0xbf54db31,0xbf6c835e,0xbf7b14be,0xbf61c598,0xbf76ba07,0xbf7fb10f,0xbf7c3b28,0xbf7b14be,0xbf7fb10f,0xbf74fa0b,0xbf5b941a,0xbf7ec46d,0xbf6ed89e,0xbf4d9f02,0xbf1d7fd1,
0xbf6c835e,0xbf45e403,0xbf0e39da,0xbe94a031,0xbf45e403,0xbf08f59b,0xbe78cfcc,0x3d96a905,0xbf0e39da,0xbe78cfcc,0x3dc8bd36,0x3edae880,0xbe94a031,0x3d96a905,0x3edae880,0x3f396842,
0,0,0,0,0,0xbcc90ab0,0xbd48fb30,0xbd96a905,0,0xbd48fb30,0xbdc8bd36,0xbe164083,0,0xbd96a905,0xbe164083,0xbe605c13,
0,0xbdc8bd36,0xbe47c5c2,0xbe94a031,0,0xbdfab273,0xbe78cfcc,0xbeb8442a,0,0xbe164083,0xbe94a031,0xbedae880,0,0xbe2f10a2,0xbeac7cd4,0xbefc5d27,
0,0xbe47c5c2,0xbec3ef15,0xbf0e39da,0,0xbe605c13,0xbedae880,0xbf1d7fd1,0,0xbe78cfcc,0xbef15aea,0xbf2beb4a,0,0xbe888e93,0xbf039c3d,0xbf396842,
0,0xbe94a031,0xbf0e39da,0xbf45e403,0,0xbea09ae5,0xbf187fc0,0xbf514d3d,0,0xbeac7cd4,0xbf226799,0xbf5b941a,0,0xbeb8442a,0xbf2beb4a,0xbf64aa59,
0,0,0,0,0xbdc8bd36,0xbdfab273,0xbe164083,0xbe2f10a2,0xbe47c5c2,0xbe78cfcc,0xbe94a031,0xbeac7cd4,0xbe94a031,0xbeb8442a,0xbedae880,0xbefc5d27,
0xbec3ef15,0xbef15aea,0xbf0e39da,0xbf226799,0xbef15aea,0xbf13682a,0xbf2beb4a,0xbf41d870,0xbf0e39da,0xbf2beb4a,0xbf45e403,0xbf5b941a,0xbf226799,0xbf41d870,0xbf5b941a,0xbf6ed89e,
0xbf3504f3,0xbf54db31,0xbf6c835e,0xbf7b14be,0xbf45e403,0xbf64aa59,0xbf7853f8,0xbf7fec43,0xbf54db31,0xbf710908,0xbf7ec46d,0xbf7d3aac,0xbf61c598,0xbf79c79d,0xbf7fb10f,0xbf731447,
0xbf6c835e,0xbf7ec46d,0xbf7b14be,0xbf61c598,0xbf74fa0b,0xbf7fec43,0xbf710908,0xbf49d112,0xbf7b14be,0xbf7d3aac,0xbf61c598,0xbf2beb4a,0xbf7ec46d,0xbf76ba07,0xbf4d9f02,0xbf08f59b,
0,0,0,0,0xbe47c5c2,0xbe605c13,0xbe78cfcc,0xbe888e93,0xbec3ef15,0xbedae880,0xbef15aea,0xbf039c3d,0xbf0e39da,0xbf1d7fd1,0xbf2beb4a,0xbf396842,
0xbf3504f3,0xbf45e403,0xbf54db31,0xbf61c598,0xbf54db31,0xbf64aa59,0xbf710908,0xbf79c79d,0xbf6c835e,0xbf7853f8,0xbf7ec46d,0xbf7fb10f,0xbf7b14be,0xbf7fec43,0xbf7d3aac,0xbf731447,
0xbf800000,0xbf7b14be,0xbf6c835e,0xbf54db31,0xbf7b14be,0xbf6a09a7,0xbf4d9f02,0xbf273656,0xbf6c835e,0xbf4d9f02,0xbf226799,0xbedae880,0xbf54db31,0xbf273656,0xbedae880,0xbe2f10a2,
0xbf3504f3,0xbef15aea,0xbe47c5c2,0x3dc8bd36,0xbf0e39da,0xbe888e93,0x3d48fb30,0x3eb8442a,0xbec3ef15,0xbd48fb30,0x3e94a031,0x3f187fc0,0xbe47c5c2,0x3e2f10a2,0x3f039c3d,0x3f49d112,
0,0,0,0,0xbe94a031,0xbea09ae5,0xbeac7cd4,0xbeb8442a,0xbf0e39da,0xbf187fc0,0xbf226799,0xbf2beb4a,0xbf45e403,0xbf514d3d,0xbf5b941a,0xbf64aa59,
0xbf6c835e,0xbf74fa0b,0xbf7b14be,0xbf7ec46d,0xbf7ec46d,0xbf7fec43,0xbf7d3aac,0xbf76ba07,0xbf7b14be,0xbf710908,0xbf61c598,0xbf4d9f02,0xbf61c598,0xbf49d112,0xbf2beb4a,0xbf08f59b,
0xbf3504f3,0xbf0e39da,0xbec3ef15,0xbe47c5c2,0xbef15aea,0xbe888e93,0xbd48fb30,0x3e2f10a2,0xbe47c5c2,0x3d48fb30,0x3e94a031,0x3f039c3d,0x3dc8bd36,0x3eb8442a,0x3f187fc0,0x3f49d112,
0x3ec3ef15,0x3f226799,0x3f54db31,0x3f74fa0b,0x3f226799,0x3f584853,0x3f7853f8,0x3f7f4e6d,0x3f54db31,0x3f7853f8,0x3f7ec46d,0x3f676bd8,0x3f74fa0b,0x3f7f4e6d,0x3f676bd8,0x3f3085bb,
0x20000000,0x204001c2,0x7c000fe,0,0x20100b98,0x20500d5a,0x7d00c96,0,0x20200482,0x206002c0,0x7e00384,0,0x2030101a,0x20700e58,0x7f00f1c,0,
0x24400240,128,0x8000100,0,0x24500dd8,1051672,0x8100c98,0,0x24600242,2098178,0x8200382,0,0x24700dda,3149722,0x8300f1a,0,
4194306,0x8400082,0x10400102,0,5245850,0x8500c1a,0x10500c9a,0,6292608,0x8600400,0x10600380,0,7344152,0x8700f98,0x10700f18,0,
0x8800004,0x10800084,0x18800104,0,0x8900b9c,0x10900c1c,0x18900c9c,0,0x8a0047e,0x10a003fe,0x18a0037e,0,0x8b01016,0x10b00f96,0x18b00f16,0,
0x10c00006,0x18c00086,0x1f40013a,0,0x10d00b9e,0x18d00c1e,0x1f500cd2,0,0x10e0047c,0x18e003fc,0x1f600348,0,0x10f01014,0x18f00f94,0x1f700ee0,0,
0x19000008,0x1f0001b8,0x17000138,0,0x19100ba0,0x1f100d50,0x17100cd0,0,0x1920047a,0x1f2002ca,0x1720034a,0,0x19301012,0x1f300e62,0x17300ee2,0,
0x1ec00236,0x16c001b6,0xec00136,0,0x1ed00dce,0x16d00d4e,0xed00cce,0,0x1ee0024c,0x16e002cc,0xee0034c,0,0x1ef00de4,0x16f00e64,0xef00ee4,0,
0x16800234,0xe8001b4,0x6800134,0,0x16900dcc,0xe900d4c,0x6900ccc,0,0x16a0024e,0xea002ce,0x6a0034e,0,0x16b00de6,0xeb00e66,0x6b00ee6,0,
0xe400232,0x64001b2,0x21c0010e,0,0xe500dca,0x6500d4a,0x21d00ca6,0,0xe600250,0x66002d0,0x21e00374,0,0xe700de8,0x6700e68,0x21f00f0c,0,
0x6000230,0x22000090,0x2000110,0,0x6100dc8,0x22100c28,0x2100ca8,0,0x6200252,0x222003f2,0x2200372,0,0x6300dea,0x22300f8a,0x2300f0a,0,
0x22400012,0x2400092,0xa400112,0,0x22500baa,0x2500c2a,0xa500caa,0,0x22600470,0x26003f0,0xa600370,0,0x22701008,0x2700f88,0xa700f08,0,
0x2800014,0xa800094,0x12800114,0,0x2900bac,0xa900c2c,0x12900cac,0,0x2a0046e,0xaa003ee,0x12a0036e,0,0x2b01006,0xab00f86,0x12b00f06,0,
0xac00016,0x12c00096,0x1ac00116,0,0xad00bae,0x12d00c2e,0x1ad00cae,0,0xae0046c,0x12e003ec,0x1ae0036c,0,0xaf01004,0x12f00f84,0x1af00f04,0,
0x13000018,0x1b000098,0x1d000128,0,0x13100bb0,0x1b100c30,0x1d100cc0,0,0x1320046a,0x1b2003ea,0x1d20035a,0,0x13301002,0x1b300f82,0x1d300ef2,0,
0x1b40001a,0x1cc001a6,0x14c00126,0,0x1b500bb2,0x1cd00d3e,0x14d00cbe,0,0x1b600468,0x1ce002dc,0x14e0035c,0,0x1b701000,0x1cf00e74,0x14f00ef4,0,
0x1c800224,0x148001a4,0xc800124,0,0x1c900dbc,0x14900d3c,0xc900cbc,0,0x1ca0025e,0x14a002de,0xca0035e,0,0x1cb00df6,0x14b00e76,0xcb00ef6,0,
0x14400222,0xc4001a2,0x4400122,0,0x14500dba,0xc500d3a,0x4500cba,0,0x14600260,0xc6002e0,0x4600360,0,0x14700df8,0xc700e78,0x4700ef8,0,
0xc000220,0x40001a0,0x24000120,0,0xc100db8,0x4100d38,0x24100cb8,0,0xc200262,0x42002e2,0x24200362,0,0xc300dfa,0x4300e7a,0x24300efa,0,
0x3c0021e,0x23c0019e,0x24000120,0,0x3d00db6,0x23d00d36,0x24100cb8,0,0x3e00264,0x23e002e4,0x24200362,0,0x3f00dfc,0x23f00e7c,0x24300efa,0,
0x2380021c,0x48000a4,0x24000120,0,0x23900db4,0x4900c3c,0x24100cb8,0,0x23a00266,0x4a003de,0x24200362,0,0x23b00dfe,0x4b00f76,0x24300efa,0,
0x4c00026,0xcc000a6,0x24000120,0,0x4d00bbe,0xcd00c3e,0x24100cb8,0,0x4e0045c,0xce003dc,0x24200362,0,0x4f00ff4,0xcf00f74,0x24300efa,0,
0xd000028,0x150000a8,0x24000120,0,0xd100bc0,0x15100c40,0x24100cb8,0,0xd20045a,0x152003da,0x24200362,0,0xd300ff2,0x15300f72,0x24300efa,0,
0x1540002a,0x1d4000aa,0x24000120,0,0x15500bc2,0x1d500c42,0x24100cb8,0,0x15600458,0x1d6003d8,0x24200362,0,0x15700ff0,0x1d700f70,0x24300efa,0,
0x1d80002c,0x1a800194,0x24000120,0,0x1d900bc4,0x1a900d2c,0x24100cb8,0,0x1da00456,0x1aa002ee,0x24200362,0,0x1db00fee,0x1ab00e86,0x24300efa,0,
0x1a400212,0x12400192,0x24000120,0,0x1a500daa,0x12500d2a,0x24100cb8,0,0x1a600270,0x126002f0,0x24200362,0,0x1a700e08,0x12700e88,0x24300efa,0,
0x12000210,0xa000190,0x24000120,0,0x12100da8,0xa100d28,0x24100cb8,0,0x12200272,0xa2002f2,0x24200362,0,0x12300e0a,0xa300e8a,0x24300efa,0,
0x9c0020e,0x1c0018e,0x24000120,0,0x9d00da6,0x1d00d26,0x24100cb8,0,0x9e00274,0x1e002f4,0x24200362,0,0x9f00e0c,0x1f00e8c,0x24300efa,0,
0x180020c,0x2180018c,0x24000120,0,0x1900da4,0x21900d24,0x24100cb8,0,0x1a00276,0x21a002f6,0x24200362,0,0x1b00e0e,0x21b00e8e,0x24300efa,0,
0x2140020a,0x6c000b6,0x24000120,0,0x21500da2,0x6d00c4e,0x24100cb8,0,0x21600278,0x6e003cc,0x24200362,0,0x21700e10,0x6f00f64,0x24300efa,0,
0x7000038,0xf0000b8,0x24000120,0,0x7100bd0,0xf100c50,0x24100cb8,0,0x720044a,0xf2003ca,0x24200362,0,0x7300fe2,0xf300f62,0x24300efa,0,
0xf40003a,0x174000ba,0x24000120,0,0xf500bd2,0x17500c52,0x24100cb8,0,0xf600448,0x176003c8,0x24200362,0,0xf700fe0,0x17700f60,0x24300efa,0,
0x1780003c,0x1f8000bc,0x24000120,0,0x17900bd4,0x1f900c54,0x24100cb8,0,0x17a00446,0x1fa003c6,0x24200362,0,0x17b00fde,0x1fb00f5e,0x24300efa,0,
0x1fc0003e,0x18400182,0x24000120,0,0x1fd00bd6,0x18500d1a,0x24100cb8,0,0x1fe00444,0x18600300,0x24200362,0,0x1ff00fdc,0x18700e98,0x24300efa,0,
0x18000200,0x10000180,0x24000120,0,0x18100d98,0x10100d18,0x24100cb8,0,0x18200282,0x10200302,0x24200362,0,0x18300e1a,0x10300e9a,0x24300efa,0,
0x17c001fe,0xfc0017e,0x24000120,0,0x17d00d96,0xfd00d16,0x24100cb8,0,0x17e00284,0xfe00304,0x24200362,0,0x17f00e1c,0xff00e9c,0x24300efa,0,
0xf8001fc,0x780017c,0x24000120,0,0xf900d94,0x7900d14,0x24100cb8,0,0xfa00286,0x7a00306,0x24200362,0,0xfb00e1e,0x7b00e9e,0x24300efa,0,
0x74001fa,0x20c000c6,0x24000120,0,0x7500d92,0x20d00c5e,0x24100cb8,0,0x7600288,0x20e003bc,0x24200362,0,0x7700e20,0x20f00f54,0x24300efa,0,
0x21000048,0x10000c8,0x24000120,0,0x21100be0,0x1100c60,0x24100cb8,0,0x2120043a,0x12003ba,0x24200362,0,0x21300fd2,0x1300f52,0x24300efa,0,
0x140004a,0x94000ca,0x24000120,0,0x1500be2,0x9500c62,0x24100cb8,0,0x1600438,0x96003b8,0x24200362,0,0x1700fd0,0x9700f50,0x24300efa,0,
0x980004c,0x118000cc,0x24000120,0,0x9900be4,0x11900c64,0x24100cb8,0,0x9a00436,0x11a003b6,0x24200362,0,0x9b00fce,0x11b00f4e,0x24300efa,0,
0x11c0004e,0x19c000ce,0x24000120,0,0x11d00be6,0x19d00c66,0x24100cb8,0,0x11e00434,0x19e003b4,0x24200362,0,0x11f00fcc,0x19f00f4c,0x24300efa,0,
0x1a000050,0x1e000170,0x24000120,0,0x1a100be8,0x1e100d08,0x24100cb8,0,0x1a200432,0x1e200312,0x24200362,0,0x1a300fca,0x1e300eaa,0x24300efa,0,
0x1dc001ee,0x15c0016e,0x24000120,0,0x1dd00d86,0x15d00d06,0x24100cb8,0,0x1de00294,0x15e00314,0x24200362,0,0x1df00e2c,0x15f00eac,0x24300efa,0,
0x158001ec,0xd80016c,0x24000120,0,0x15900d84,0xd900d04,0x24100cb8,0,0x15a00296,0xda00316,0x24200362,0,0x15b00e2e,0xdb00eae,0x24300efa,0,
0xd4001ea,0x540016a,0x24000120,0,0xd500d82,0x5500d02,0x24100cb8,0,0xd600298,0x5600318,0x24200362,0,0xd700e30,0x5700eb0,0x24300efa,0,
0x50001e8,0x230000d8,0x24000120,0,0x5100d80,0x23100c70,0x24100cb8,0,0x520029a,0x232003aa,0x24200362,0,0x5300e32,0x23300f42,0x24300efa,0,
0x2340005a,0x34000da,0x24000120,0,0x23500bf2,0x3500c72,0x24100cb8,0,0x23600428,0x36003a8,0x24200362,0,0x23700fc0,0x3700f40,0x24300efa,0,
0x380005c,0xb8000dc,0x24000120,0,0x3900bf4,0xb900c74,0x24100cb8,0,0x3a00426,0xba003a6,0x24200362,0,0x3b00fbe,0xbb00f3e,0x24300efa,0,
0xbc0005e,0x13c000de,0x24000120,0,0xbd00bf6,0x13d00c76,0x24100cb8,0,0xbe00424,0x13e003a4,0x24200362,0,0xbf00fbc,0x13f00f3c,0x24300efa,0,
0x14000060,0x1c0000e0,0x24000120,0,0x14100bf8,0x1c100c78,0x24100cb8,0,0x14200422,0x1c2003a2,0x24200362,0,0x14300fba,0x1c300f3a,0x24300efa,0,
0x1c400062,0x1bc0015e,0x24000120,0,0x1c500bfa,0x1bd00cf6,0x24100cb8,0,0x1c600420,0x1be00324,0x24200362,0,0x1c700fb8,0x1bf00ebc,0x24300efa,0,
0x1b8001dc,0x1380015c,0x24000120,0,0x1b900d74,0x13900cf4,0x24100cb8,0,0x1ba002a6,0x13a00326,0x24200362,0,0x1bb00e3e,0x13b00ebe,0x24300efa,0,
0x134001da,0xb40015a,0x24000120,0,0x13500d72,0xb500cf2,0x24100cb8,0,0x136002a8,0xb600328,0x24200362,0,0x13700e40,0xb700ec0,0x24300efa,0,
0xb0001d8,0x3000158,0x24000120,0,0xb100d70,0x3100cf0,0x24100cb8,0,0xb2002aa,0x320032a,0x24200362,0,0xb300e42,0x3300ec2,0x24300efa,0,
0x2c001d6,0x22c00156,0x24000120,0,0x2d00d6e,0x22d00cee,0x24100cb8,0,0x2e002ac,0x22e0032c,0x24200362,0,0x2f00e44,0x22f00ec4,0x24300efa,0,
0x228001d4,0x58000ec,0x24000120,0,0x22900d6c,0x5900c84,0x24100cb8,0,0x22a002ae,0x5a00396,0x24200362,0,0x22b00e46,0x5b00f2e,0x24300efa,0,
0x5c0006e,0xdc000ee,0x24000120,0,0x5d00c06,0xdd00c86,0x24100cb8,0,0x5e00414,0xde00394,0x24200362,0,0x5f00fac,0xdf00f2c,0x24300efa,0,
0xe000070,0x160000f0,0x24000120,0,0xe100c08,0x16100c88,0x24100cb8,0,0xe200412,0x16200392,0x24200362,0,0xe300faa,0x16300f2a,0x24300efa,0,
0x16400072,0x1e4000f2,0x24000120,0,0x16500c0a,0x1e500c8a,0x24100cb8,0,0x16600410,0x1e600390,0x24200362,0,0x16700fa8,0x1e700f28,0x24300efa,0,
0x1e800074,0x1980014c,0x24000120,0,0x1e900c0c,0x19900ce4,0x24100cb8,0,0x1ea0040e,0x19a00336,0x24200362,0,0x1eb00fa6,0x19b00ece,0x24300efa,0,
0x194001ca,0x1140014a,0x24000120,0,0x19500d62,0x11500ce2,0x24100cb8,0,0x196002b8,0x11600338,0x24200362,0,0x19700e50,0x11700ed0,0x24300efa,0,
0x110001c8,0x9000148,0x24000120,0,0x11100d60,0x9100ce0,0x24100cb8,0,0x112002ba,0x920033a,0x24200362,0,0x11300e52,0x9300ed2,0x24300efa,0,
0x8c001c6,0xc00146,0x24000120,0,0x8d00d5e,0xd00cde,0x24100cb8,0,0x8e002bc,0xe0033c,0x24200362,0,0x8f00e54,0xf00ed4,0x24300efa,0,
8389060,0x20800144,0x24000120,0,9440604,0x20900cdc,0x24100cb8,0,0xa002be,0x20a0033e,0x24200362,0,0xb00e56,0x20b00ed6,0x24300efa,0,
0,512,1536,0,16,528,1552,16,32,544,1568,32,48,560,1584,48,
64,576,1600,64,80,592,1616,80,96,608,1632,96,112,624,1648,112,
128,640,1664,128,144,656,1680,144,160,672,1696,160,176,688,1712,176,
192,704,1728,192,208,720,1744,208,224,736,1760,224,240,752,1776,240,
1536,5120,6144,4608,1552,5136,6160,4624,1568,5152,6176,4640,1584,5168,6192,4656,
1600,5184,6208,4672,1616,5200,6224,4688,1632,5216,6240,4704,1648,5232,6256,4720,
1664,5248,6272,4736,1680,5264,6288,4752,1696,5280,6304,4768,1712,5296,6320,4784,
1728,5312,6336,4800,1744,5328,6352,4816,1760,5344,6368,4832,1776,5360,6384,4848,
3072,4096,3072,0,3088,4592,3568,496,3104,4576,3552,480,3120,4560,3536,464,
3136,4544,3520,448,3152,4528,3504,432,3168,4512,3488,416,3184,4496,3472,400,
3200,4480,3456,384,3216,4464,3440,368,3232,4448,3424,352,3248,4432,3408,336,
3264,4416,3392,320,3280,4400,3376,304,3296,4384,3360,288,3312,4368,3344,272,
3072,8704,7680,4608,3088,9200,8176,5104,3104,9184,8160,5088,3120,9168,8144,5072,
3136,9152,8128,5056,3152,9136,8112,5040,3168,9120,8096,5024,3184,9104,8080,5008,
3200,9088,8064,4992,3216,9072,8048,4976,3232,9056,8032,4960,3248,9040,8016,4944,
3264,9024,8000,4928,3280,9008,7984,4912,3296,8992,7968,4896,3312,8976,7952,4880,
8,768,1792,256,24,784,1808,272,40,800,1824,288,56,816,1840,304,
72,832,1856,320,88,848,1872,336,104,864,1888,352,120,880,1904,368,
136,896,1920,384,152,912,1936,400,168,928,1952,416,184,944,1968,432,
200,960,1984,448,216,976,2000,464,232,992,2016,480,248,1008,2032,496,
1544,5376,6400,4864,1560,5392,6416,4880,1576,5408,6432,4896,1592,5424,6448,4912,
1608,5440,6464,4928,1624,5456,6480,4944,1640,5472,6496,4960,1656,5488,6512,4976,
1672,5504,6528,4992,1688,5520,6544,5008,1704,5536,6560,5024,1720,5552,6576,5040,
1736,5568,6592,5056,1752,5584,6608,5072,1768,5600,6624,5088,1784,5616,6640,5104,
3080,4352,3328,256,3096,4336,3312,240,3112,4320,3296,224,3128,4304,3280,208,
3144,4288,3264,192,3160,4272,3248,176,3176,4256,3232,160,3192,4240,3216,144,
3208,4224,3200,128,3224,4208,3184,112,3240,4192,3168,96,3256,4176,3152,80,
3272,4160,3136,64,3288,4144,3120,48,3304,4128,3104,32,3320,4112,3088,16,
3080,8960,7936,4864,3096,8944,7920,4848,3112,8928,7904,4832,3128,8912,7888,4816,
3144,8896,7872,4800,3160,8880,7856,4784,3176,8864,7840,4768,3192,8848,7824,4752,
3208,8832,7808,4736,3224,8816,7792,4720,3240,8800,7776,4704,3256,8784,7760,4688,
3272,8768,7744,4672,3288,8752,7728,4656,3304,8736,7712,4640,3320,8720,7696,4624,
4608,1024,2048,0,4624,1040,2064,16,4640,1056,2080,32,4656,1072,2096,48,
4672,1088,2112,64,4688,1104,2128,80,4704,1120,2144,96,4720,1136,2160,112,
4736,1152,2176,128,4752,1168,2192,144,4768,1184,2208,160,4784,1200,2224,176,
4800,1216,2240,192,4816,1232,2256,208,4832,1248,2272,224,4848,1264,2288,240,
6144,5632,6656,4608,6160,5648,6672,4624,6176,5664,6688,4640,6192,5680,6704,4656,
6208,5696,6720,4672,6224,5712,6736,4688,6240,5728,6752,4704,6256,5744,6768,4720,
6272,5760,6784,4736,6288,5776,6800,4752,6304,5792,6816,4768,6320,5808,6832,4784,
6336,5824,6848,4800,6352,5840,6864,4816,6368,5856,6880,4832,6384,5872,6896,4848,
7680,3584,2560,0,7696,4080,3056,496,7712,4064,3040,480,7728,4048,3024,464,
7744,4032,3008,448,7760,4016,2992,432,7776,4000,2976,416,7792,3984,2960,400,
7808,3968,2944,384,7824,3952,2928,368,7840,3936,2912,352,7856,3920,2896,336,
7872,3904,2880,320,7888,3888,2864,304,7904,3872,2848,288,7920,3856,2832,272,
7680,8192,7168,4608,7696,8688,7664,5104,7712,8672,7648,5088,7728,8656,7632,5072,
7744,8640,7616,5056,7760,8624,7600,5040,7776,8608,7584,5024,7792,8592,7568,5008,
7808,8576,7552,4992,7824,8560,7536,4976,7840,8544,7520,4960,7856,8528,7504,4944,
7872,8512,7488,4928,7888,8496,7472,4912,7904,8480,7456,4896,7920,8464,7440,4880,
4616,1280,2304,256,4632,1296,2320,272,4648,1312,2336,288,4664,1328,2352,304,
4680,1344,2368,320,4696,1360,2384,336,4712,1376,2400,352,4728,1392,2416,368,
4744,1408,2432,384,4760,1424,2448,400,4776,1440,2464,416,4792,1456,2480,432,
4808,1472,2496,448,4824,1488,2512,464,4840,1504,2528,480,4856,1520,2544,496,
6152,5888,6912,4864,6168,5904,6928,4880,6184,5920,6944,4896,6200,5936,6960,4912,
6216,5952,6976,4928,6232,5968,6992,4944,6248,5984,7008,4960,6264,6000,7024,4976,
6280,6016,7040,4992,6296,6032,7056,5008,6312,6048,7072,5024,6328,6064,7088,5040,
6344,6080,7104,5056,6360,6096,7120,5072,6376,6112,7136,5088,6392,6128,7152,5104,
7688,3840,2816,256,7704,3824,2800,240,7720,3808,2784,224,7736,3792,2768,208,
7752,3776,2752,192,7768,3760,2736,176,7784,3744,2720,160,7800,3728,2704,144,
7816,3712,2688,128,7832,3696,2672,112,7848,3680,2656,96,7864,3664,2640,80,
7880,3648,2624,64,7896,3632,2608,48,7912,3616,2592,32,7928,3600,2576,16,
7688,8448,7424,4864,7704,8432,7408,4848,7720,8416,7392,4832,7736,8400,7376,4816,
7752,8384,7360,4800,7768,8368,7344,4784,7784,8352,7328,4768,7800,8336,7312,4752,
7816,8320,7296,4736,7832,8304,7280,4720,7848,8288,7264,4704,7864,8272,7248,4688,
7880,8256,7232,4672,7896,8240,7216,4656,7912,8224,7200,4640,7928,8208,7184,4624,
0,0,16384,29640,16,64,16448,29576,32,128,16512,29512,48,192,16576,29448,
64,272,16656,29400,80,336,16720,29336,96,400,16784,29272,112,464,16848,29208,
128,544,16928,29160,144,608,16992,29096,160,672,17056,29032,176,736,17120,28968,
192,816,17200,28920,208,880,17264,28856,224,944,17328,28792,240,1008,17392,28728,
272,4096,20480,25544,256,4160,20544,25480,304,4224,20608,25416,288,4288,20672,25352,
336,4368,20752,25304,320,4432,20816,25240,368,4496,20880,25176,352,4560,20944,25112,
400,4640,21024,25064,384,4704,21088,25000,432,4768,21152,24936,416,4832,21216,24872,
464,4912,21296,24824,448,4976,21360,24760,496,5040,21424,24696,480,5104,21488,24632,
544,8192,24576,21448,560,8256,24640,21384,512,8320,24704,21320,528,8384,24768,21256,
608,8464,24848,21208,624,8528,24912,21144,576,8592,24976,21080,592,8656,25040,21016,
672,8736,25120,20968,688,8800,25184,20904,640,8864,25248,20840,656,8928,25312,20776,
736,9008,25392,20728,752,9072,25456,20664,704,9136,25520,20600,720,9200,25584,20536,
816,12288,28672,17352,800,12352,28736,17288,784,12416,28800,17224,768,12480,28864,17160,
880,12560,28944,17112,864,12624,29008,17048,848,12688,29072,16984,832,12752,29136,16920,
944,12832,29216,16872,928,12896,29280,16808,912,12960,29344,16744,896,13024,29408,16680,
1008,13104,29488,16632,992,13168,29552,16568,976,13232,29616,16504,960,13296,29680,16440,
1024,16,16392,29632,1040,80,16456,29568,1056,144,16520,29504,1072,208,16584,29440,
1088,256,16664,29392,1104,320,16728,29328,1120,384,16792,29264,1136,448,16856,29200,
1152,560,16936,29152,1168,624,17000,29088,1184,688,17064,29024,1200,752,17128,28960,
1216,800,17208,28912,1232,864,17272,28848,1248,928,17336,28784,1264,992,17400,28720,
1296,4112,20488,25536,1280,4176,20552,25472,1328,4240,20616,25408,1312,4304,20680,25344,
1360,4352,20760,25296,1344,4416,20824,25232,1392,4480,20888,25168,1376,4544,20952,25104,
1424,4656,21032,25056,1408,4720,21096,24992,1456,4784,21160,24928,1440,4848,21224,24864,
1488,4896,21304,24816,1472,4960,21368,24752,1520,5024,21432,24688,1504,5088,21496,24624,
1568,8208,24584,21440,1584,8272,24648,21376,1536,8336,24712,21312,1552,8400,24776,21248,
1632,8448,24856,21200,1648,8512,24920,21136,1600,8576,24984,21072,1616,8640,25048,21008,
1696,8752,25128,20960,1712,8816,25192,20896,1664,8880,25256,20832,1680,8944,25320,20768,
1760,8992,25400,20720,1776,9056,25464,20656,1728,9120,25528,20592,1744,9184,25592,20528,
1840,12304,28680,17344,1824,12368,28744,17280,1808,12432,28808,17216,1792,12496,28872,17152,
1904,12544,28952,17104,1888,12608,29016,17040,1872,12672,29080,16976,1856,12736,29144,16912,
1968,12848,29224,16864,1952,12912,29288,16800,1936,12976,29352,16736,1920,13040,29416,16672,
2032,13088,29496,16624,2016,13152,29560,16560,2000,13216,29624,16496,1984,13280,29688,16432,
2048,32,16400,29656,2064,96,16464,29592,2080,160,16528,29528,2096,224,16592,29464,
2112,304,16640,29384,2128,368,16704,29320,2144,432,16768,29256,2160,496,16832,29192,
2176,512,16944,29176,2192,576,17008,29112,2208,640,17072,29048,2224,704,17136,28984,
2240,784,17184,28904,2256,848,17248,28840,2272,912,17312,28776,2288,976,17376,28712,
2320,4128,20496,25560,2304,4192,20560,25496,2352,4256,20624,25432,2336,4320,20688,25368,
2384,4400,20736,25288,2368,4464,20800,25224,2416,4528,20864,25160,2400,4592,20928,25096,
2448,4608,21040,25080,2432,4672,21104,25016,2480,4736,21168,24952,2464,4800,21232,24888,
2512,4880,21280,24808,2496,4944,21344,24744,2544,5008,21408,24680,2528,5072,21472,24616,
2592,8224,24592,21464,2608,8288,24656,21400,2560,8352,24720,21336,2576,8416,24784,21272,
2656,8496,24832,21192,2672,8560,24896,21128,2624,8624,24960,21064,2640,8688,25024,21000,
2720,8704,25136,20984,2736,8768,25200,20920,2688,8832,25264,20856,2704,8896,25328,20792,
2784,8976,25376,20712,2800,9040,25440,20648,2752,9104,25504,20584,2768,9168,25568,20520,
2864,12320,28688,17368,2848,12384,28752,17304,2832,12448,28816,17240,2816,12512,28880,17176,
2928,12592,28928,17096,2912,12656,28992,17032,2896,12720,29056,16968,2880,12784,29120,16904,
2992,12800,29232,16888,2976,12864,29296,16824,2960,12928,29360,16760,2944,12992,29424,16696,
3056,13072,29472,16616,3040,13136,29536,16552,3024,13200,29600,16488,3008,13264,29664,16424,
3072,48,16408,29648,3088,112,16472,29584,3104,176,16536,29520,3120,240,16600,29456,
3136,288,16648,29376,3152,352,16712,29312,3168,416,16776,29248,3184,480,16840,29184,
3200,528,16952,29168,3216,592,17016,29104,3232,656,17080,29040,3248,720,17144,28976,
3264,768,17192,28896,3280,832,17256,28832,3296,896,17320,28768,3312,960,17384,28704,
3344,4144,20504,25552,3328,4208,20568,25488,3376,4272,20632,25424,3360,4336,20696,25360,
3408,4384,20744,25280,3392,4448,20808,25216,3440,4512,20872,25152,3424,4576,20936,25088,
3472,4624,21048,25072,3456,4688,21112,25008,3504,4752,21176,24944,3488,4816,21240,24880,
3536,4864,21288,24800,3520,4928,21352,24736,3568,4992,21416,24672,3552,5056,21480,24608,
3616,8240,24600,21456,3632,8304,24664,21392,3584,8368,24728,21328,3600,8432,24792,21264,
3680,8480,24840,21184,3696,8544,24904,21120,3648,8608,24968,21056,3664,8672,25032,20992,
3744,8720,25144,20976,3760,8784,25208,20912,3712,8848,25272,20848,3728,8912,25336,20784,
3808,8960,25384,20704,3824,9024,25448,20640,3776,9088,25512,20576,3792,9152,25576,20512,
3888,12336,28696,17360,3872,12400,28760,17296,3856,12464,28824,17232,3840,12528,28888,17168,
3952,12576,28936,17088,3936,12640,29000,17024,3920,12704,29064,16960,3904,12768,29128,16896,
4016,12816,29240,16880,4000,12880,29304,16816,3984,12944,29368,16752,3968,13008,29432,16688,
4080,13056,29480,16608,4064,13120,29544,16544,4048,13184,29608,16480,4032,13248,29672,16416,
4718592,0xd80090,0x1780130,0x20801c0,0x1580110,0x1e801a0,0x2880240,0x31802d0,0x2680220,0x2f802b0,0x3980350,0x42803e0,0x3780330,0x40803c0,0x4a80460,0x53804f0,
0x3780330,0x40803c0,0x4a80460,0x53804f0,0x4880440,0x51804d0,0x5b80570,0x6480600,0x5980550,0x62805e0,0x6c80680,0x7580710,0x6a80660,0x73806f0,0x7d80790,0x8680820,
0x6a80660,0x73806f0,0x7d80790,0x8680820,0x7b80770,0x8480800,0x8e808a0,0x9780930,0x8c80880,0x9580910,6815776,0xf800b0,0x8c80880,0x9580910,6815776,0xf800b0,
0x8c80880,0x9580910,6815776,0xf800b0,0x8c80880,0x9580910,6815776,0xf800b0,0x8c80880,0x9580910,6815776,0xf800b0,0x8c80880,0x9580910,6815776,0xf800b0,
0x2a80260,0x34802f0,0x3d80390,0x4780420,0x3b80370,0x4580400,0x4e804a0,0x5880530,0x4c80480,0x5680510,0x5f805b0,0x6980640,0x5d80590,0x6780620,0x70806c0,0x7a80750,
0x5d80590,0x6780620,0x70806c0,0x7a80750,0x6e806a0,0x7880730,0x81807d0,0x8b80860,0x7f807b0,0x8980840,0x92808e0,3672432,0x90808c0,1575248,0xa80060,0x14800f0,
0x90808c0,1575248,0xa80060,0x14800f0,8912960,0x12800d0,0x1b80170,0x2580200,0x1980150,0x23801e0,0x2c80280,0x3680310,0x1980150,0x23801e0,0x2c80280,0x3680310,
0x1980150,0x23801e0,0x2c80280,0x3680310,0x1980150,0x23801e0,0x2c80280,0x3680310,0x1980150,0x23801e0,0x2c80280,0x3680310,0x1980150,0x23801e0,0x2c80280,0x3680310,
0x50804c0,0x5a80560,0x63805f0,0x6d80690,0x61805d0,0x6b80670,0x7480700,0x7e807a0,0x72806e0,0x7c80780,0x8580810,0x8f808b0,0x83807f0,0x8d80890,0x9680920,7864368,
0x83807f0,0x8d80890,0x9680920,7864368,0x9480900,5767184,0xe800a0,0x1880140,0xc80080,0x1680120,0x1f801b0,0x2980250,0x1d80190,0x2780230,0x30802c0,0x3a80360,
0x1d80190,0x2780230,0x30802c0,0x3a80360,0x2e802a0,0x3880340,0x41803d0,0x4b80470,0x3f803b0,0x4980450,0x52804e0,0x5c80580,0x3f803b0,0x4980450,0x52804e0,0x5c80580,
0x3f803b0,0x4980450,0x52804e0,0x5c80580,0x3f803b0,0x4980450,0x52804e0,0x5c80580,0x3f803b0,0x4980450,0x52804e0,0x5c80580,0x3f803b0,0x4980450,0x52804e0,0x5c80580,
0x7780720,0x80807c0,0x8a80850,0x93808f0,0x8880830,0x91808d0,2623840,0xb80070,526656,9961552,0x13800e0,0x1c80180,0x11800c0,0x1a80160,0x24801f0,0x2d80290,
0x11800c0,0x1a80160,0x24801f0,0x2d80290,0x22801d0,0x2b80270,0x3580300,0x3e803a0,0x33802e0,0x3c80380,0x4680410,0x4f804b0,0x44803f0,0x4d80490,0x5780520,0x60805c0,
0x44803f0,0x4d80490,0x5780520,0x60805c0,0x5580500,0x5e805a0,0x6880630,0x71806d0,0x6680610,0x6f806b0,0x7980740,0x82807e0,0x6680610,0x6f806b0,0x7980740,0x82807e0,
0x6680610,0x6f806b0,0x7980740,0x82807e0,0x6680610,0x6f806b0,0x7980740,0x82807e0,0x6680610,0x6f806b0,0x7980740,0x82807e0,0x6680610,0x6f806b0,0x7980740,0x82807e0,
2359296,7077960,0xc400a0,0x10c00e8,0xb40090,0xfc00d8,0x1540130,0x19c0178,0x1440120,0x18c0168,0x1e401c0,0x22c0208,0x1d401b0,0x21c01f8,0x2740250,0x2bc0298,
0x1d401b0,0x21c01f8,0x2740250,0x2bc0298,0x2640240,0x2ac0288,0x30402e0,0x34c0328,0x2f402d0,0x33c0318,0x3940370,0x3dc03b8,0x3840360,0x3cc03a8,0x4240400,0x46c0448,
0x3840360,0x3cc03a8,0x4240400,0x46c0448,0x41403f0,0x45c0438,0x4b40490,0x4fc04d8,0x4a40480,0x4ec04c8,3407888,8126552,0x4a40480,0x4ec04c8,3407888,8126552,
0x4a40480,0x4ec04c8,3407888,8126552,0x4a40480,0x4ec04c8,3407888,8126552,0x4a40480,0x4ec04c8,3407888,8126552,0x4a40480,0x4ec04c8,3407888,8126552,
0x1640140,0x1bc0188,0x20401e0,0x25c0228,0x1f401d0,0x24c0218,0x2940270,0x2ec02b8,0x2840260,0x2dc02a8,0x3240300,0x37c0348,0x31402f0,0x36c0338,0x3b40390,0x40c03d8,
0x31402f0,0x36c0338,0x3b40390,0x40c03d8,0x3a40380,0x3fc03c8,0x4440420,0x49c0468,0x4340410,0x48c0458,0x4d404b0,1836280,0x4c404a0,787688,5505072,0xac0078,
0x4c404a0,787688,5505072,0xac0078,4456480,0x9c0068,0xe400c0,0x13c0108,0xd400b0,0x12c00f8,0x1740150,0x1cc0198,0xd400b0,0x12c00f8,0x1740150,0x1cc0198,
0xd400b0,0x12c00f8,0x1740150,0x1cc0198,0xd400b0,0x12c00f8,0x1740150,0x1cc0198,0xd400b0,0x12c00f8,0x1740150,0x1cc0198,0xd400b0,0x12c00f8,0x1740150,0x1cc0198,
0x2a40280,0x2fc02d8,0x3440320,0x39c0378,0x3340310,0x38c0368,0x3d403b0,0x42c0408,0x3c403a0,0x41c03f8,0x4640440,0x4bc0498,0x4540430,0x4ac0488,0x4f404d0,3932184,
0x4540430,0x4ac0488,0x4f404d0,3932184,0x4e404c0,2883592,7602256,0xcc00a8,6553664,0xbc0098,0x10400e0,0x15c0138,0xf400d0,0x14c0128,0x1940170,0x1ec01c8,
0xf400d0,0x14c0128,0x1940170,0x1ec01c8,0x1840160,0x1dc01b8,0x2240200,0x27c0258,0x21401f0,0x26c0248,0x2b40290,0x30c02e8,0x21401f0,0x26c0248,0x2b40290,0x30c02e8,
0x21401f0,0x26c0248,0x2b40290,0x30c02e8,0x21401f0,0x26c0248,0x2b40290,0x30c02e8,0x21401f0,0x26c0248,0x2b40290,0x30c02e8,0x21401f0,0x26c0248,0x2b40290,0x30c02e8,
0x3f403c0,0x43c0418,0x4940460,0x4dc04b8,0x4840450,0x4cc04a8,1311984,6029368,263392,4980776,0xa40070,0xec00c8,9699424,0xdc00b8,0x1340100,0x17c0158,
9699424,0xdc00b8,0x1340100,0x17c0158,0x12400f0,0x16c0148,0x1c40190,0x20c01e8,0x1b40180,0x1fc01d8,0x2540220,0x29c0278,0x2440210,0x28c0268,0x2e402b0,0x32c0308,
0x2440210,0x28c0268,0x2e402b0,0x32c0308,0x2d402a0,0x31c02f8,0x3740340,0x3bc0398,0x3640330,0x3ac0388,0x40403d0,0x44c0428,0x3640330,0x3ac0388,0x40403d0,0x44c0428,
0x3640330,0x3ac0388,0x40403d0,0x44c0428,0x3640330,0x3ac0388,0x40403d0,0x44c0428,0x3640330,0x3ac0388,0x40403d0,0x44c0428,0x3640330,0x3ac0388,0x40403d0,0x44c0428,
0x3eb6e48b,0x3fd246dd,0x3f441b7d,0,0x3f7d3521,0x3f816570,0x3f7ffc1a,0,0x3ebf86f5,0x3fd01e43,0xbf47a682,0,0x3caf516f,0x3ffd42ba,0xbe52b743,0,
0x3de766dd,0x3ff18992,0x3eec6a50,0,0x3f1b757f,0x3fb24541,0x3f6b6e32,0,0x3f385d20,0x3fa3d170,0xbf75c5d4,0,0x3e32d4d8,0x3fe9a565,0xbf108a14,0,
0x3b79625f,0x3fff834f,0x3db27eb6,0,0x3e9208a2,0x3fdb7dd8,0x3f3308b4,0,0x3f64214d,0x3f8def5a,0x3f7e7a7a,0,0x3ee8356c,0x3fc5f2a5,0xbf56651d,0,
0x3d3d934b,0x3ffa1366,0xbe99f61c,0,0x3d93754e,0x3ff6c8ab,0x3ebec330,0,0x3f04ddec,0x3fbd910a,0x3f60712a,0,0x3f50cc75,0x3f9799c5,0xbf7b9c62,0,
0x3e6f920c,0x3fe20dbf,0xbf248dbb,0,0x38798a45,0x3ffffe0d,0xbc32b7da,0,0x3e6165f6,0x3fe3d341,0x3f203c85,0,0x3f4b522f,0x3f9a56e8,0x3f7a8575,0,
0x3f09cad8,0x3fbb1a94,0xbf631324,0,0x3da48e6d,0x3ff5b719,0xbec91503,0,0x3d23a2c4,0x3ffae2ea,0x3e8f45c0,0,0x3edeebd7,0x3fc8450a,0x3f534aca,0,
0x3f69b029,0x3f8b27eb,0xbf7f069e,0,0x3e99ee99,0x3fd9845a,0xbf36fbaf,0,0x3bc2c30c,0x3fff3d3d,0xbddef599,0,0x3e266b8d,0x3feb328e,0x3f0be54a,0,
0x3f3304f2,0x3fa67d87,0x3f7426cb,0,0x3f209e68,0x3fafb0cc,0xbf6d9156,0,0x3dfc76af,0x3ff03895,0xbef64429,0,0x3c8c73b0,0x3ffdce31,0x3e3cce2b,0,
0x3c78e8e7,0x3ffe0e2e,0x3e31d0d4,0,0x3eb2a24e,0x3fd3576c,0x3f424cfe,0,0x3f7a6a57,0x3f82cad5,0x3f7ff067,0,0x3ec3e6e0,0x3fcf0648,0xbf4962ed,0,
0x3cc22ee0,0x3ffcf744,0xbe5da259,0,0x3ddd31cd,0x3ff22ce3,0x3ee77284,0,0x3f18e598,0x3fb38d34,0x3f6a51da,0,0x3f3b0c88,0x3fa279bc,0xbf768a20,0,
0x3e393035,0x3fe8d9f9,0xbf12d5e8,0,0x3b3ef696,0x3fffa085,0x3d9c3ac6,0,0x3e8e2650,0x3fdc766c,0x3f310700,0,0x3f615b1a,0x3f8f5273,0x3f7e28c6,0,
0x3eece711,0x3fc4c63c,0xbf57e880,0,0x3d4b3e41,0x3ff9a60e,0xbe9f4757,0,0x3d8b3f75,0x3ff74c09,0x3eb9917b,0,0x3f026d0c,0x3fbec97a,0x3f5f15e7,0,
0x3f538bcb,0x3f963a1a,0xbf7c1c5c,0,0x3e76cc0a,0x3fe1267f,0xbf26aed9,0,0x3979885f,0x3ffff834,0xbcb2b521,0,0x3e5a744e,0x3fe4b176,0x3f1e0c90,0,
0x3f48976a,0x3f9bb44b,0x3f79ee89,0,0x3f0c46be,0x3fb9dca1,0xbf6459c6,0,0x3dad712d,0x3ff528ed,0xbece34d0,0,0x3d175dfc,0x3ffb4510,0x3e89e6f1,0,
0x3eda5430,0x3fc96af4,0x3f51b3f3,0,0x3f6c78a7,0x3f89c3ac,0xbf7f4109,0,0x3e9df200,0x3fd88380,0xbf38ecd8,0,0x3beb9ce3,0x3fff1463,0xbdf52731,0,
0x3e205dfe,0x3febf440,0x3f098c78,0,0x3f305c55,0x3fa7d1d6,0x3f734c1c,0,0x3f233743,0x3fae645f,0xbf6e9811,0,0x3e03a866,0x3fef8af3,0xbefb25e8,0,
0x3e0930a3,0x3feed9ec,0xbf000000,0,0x3c5ad5ed,0x3ffe4a54,0x3e26ce12,0,0x3eae6a3a,0x3fd46572,0x3f407893,0,0x3f779fb9,0x3f843024,0x3f7fdce9,0,
0x3ec8506c,0x3fcdebe5,0xbf4b1934,0,0x3cd5fff2,0x3ffca800,0xbe6886ae,0,0x3dd33462,0x3ff2ccba,0x3ee273aa,0,0x3f1658d5,0x3fb4d395,0x3f692e5f,0,
0x3f3dbe09,0x3fa120fc,0xbf7746ea,0,0x3e3fa51e,0x3fe80b5c,0xbf151d42,0,0x3b0c514f,0x3fffb9d7,0x3d85f213,0,0x3e8a4f42,0x3fdd6c30,0x3f2effe8,0,
0x3f5e95d6,0x3f90b515,0x3f7dcf55,0,0x3ef1a117,0x3fc397ba,0xbf596550,0,0x3d595fcb,0x3ff93502,0xbea493b7,0,0x3d8343c1,0x3ff7cbc4,0x3eb45a1e,0,
0x3f000000,0x3fc00000,0x3f5db3d7,0,0x3f564c7b,0x3f94d9c2,0xbf7c94a8,0,0x3e7e1db5,0x3fe03c49,0xbf28cae3,0,0x3a0c5aed,0x3fffee75,0xbd060472,0,
0x3e539b2f,0x3fe58c9a,0x3f1bd7ca,0,0x3f45de54,0x3f9d10d6,0x3f795000,0,0x3f0ec62b,0x3fb89cea,0xbf659974,0,0x3db68d07,0x3ff49730,0xbed34e56,0,
0x3d0b915c,0x3ffba375,0x3e8483ee,0,0x3ed5c57c,0x3fca8ea1,0x3f5016b8,0,0x3f6f41be,0x3f885f21,0xbf7f73af,0,0x3ea20031,0x3fd77ff4,0xbf3ad85f,0,
0x3c0c2ada,0x3ffee7aa,0xbe05a8a8,0,0x3e1a6abe,0x3fecb2a8,0x3f072f74,0,0x3f2db625,0x3fa924ee,0x3f726a03,0,0x3f25d2f1,0x3fad1688,0xbf6f9788,0,
0x3f28715e,0x3fabc751,0xbf708fb2,0,0x3e0ed3e4,0x3fee2584,0xbf026926,0,0x3c3eaf5d,0x3ffe82a1,0x3e1bc63c,0,0x3eaa3c6d,0x3fd570e5,0x3f3e9e4b,0,
0x3f74d55c,0x3f859552,0x3f7fc1a0,0,0x3eccc376,0x3fcccf22,0xbf4cc94c,0,0x3ceac409,0x3ffc54f0,0xbe7363ee,0,0x3dc96eeb,0x3ff36911,0x3edd6dea,0,
0x3f13cf4a,0x3fb6185b,0x3f6803ca,0,0x3f40718f,0x3f9fc739,0xbf77fc2c,0,0x3e463363,0x3fe73994,0xbf176012,0,0x3ac2e826,0x3fffcf46,0x3d5f4a99,0,
0x3e868396,0x3fde5f1b,0x3f2cf37b,0,0x3f5bd196,0x3f921735,0x3f7d6e28,0,0x3ef6635a,0x3fc2672a,0xbf5adb81,0,0x3d67f77b,0x3ff8c044,0xbea9db13,0,
0x3d7704df,0x3ff847d9,0x3eaf1d44,0,0x3efb2db4,0x3fc13493,0x3f5c4b07,0,0x3f590e71,0x3f9378c7,0xbf7d0543,0,0x3e82c36a,0x3fdf4f26,0xbf2ae1c9,0,
0x3a7980c5,0x3fffe0d0,0xbd32aa3e,0,0x3e4cdacf,0x3fe664a6,0x3f199e45,0,0x3f432704,0x3f9e6c7e,0x3f78a9e0,0,0x3f11490b,0x3fb75b7b,0xbf66d223,0,
0x3dbfe1b4,0x3ff401e5,0xbed8616c,0,0x3d003d42,0x3ffbfe16,0x3e7e39c4,0,0x3ed13fdd,0x3fcbb009,0x3f4e7327,0,0x3f720b56,0x3f86fa55,0xbf7f9e8c,0,
0x3ea6190a,0x3fd679bd,0xbf3cbe35,0,0x3c247613,0x3ffeb714,0xbe10b9a6,0,0x3e1491fc,0x3fed6dc1,0x3f04ce53,0,0x3f2b1276,0x3faa76c5,0x3f718088,0,
0x3f800000,0x3f800000,0x3f800000,0,0x3ebb30ce,0x3fd133cd,0xbf45e403,0,0x3c9d6830,0x3ffd8a5f,0xbe47c5c2,0,0x3df1d344,0x3ff0e2cc,0x3ef15aea,0,
0x3f1e0875,0x3fb0fbc5,0x3f6c835e,0,0x3f35afe7,0x3fa5280c,0xbf74fa0b,0,0x3e2c933b,0x3fea6d99,0xbf0e39da,0,0x3b9dc971,0x3fff6237,0x3dc8bd36,0,
0x3e95f61a,0x3fda827a,0x3f3504f3,0,0x3f66e859,0x3f8c8bd3,0x3f7ec46d,0,0x3ee38c4c,0x3fc71ced,0xbf54db31,0,0x3d305f55,0x3ffa7d05,0xbe94a031,0,
0x3d9be50c,0x3ff641af,0x3ec3ef15,0,0x3f07528b,0x3fbc56ba,0x3f61c598,0,0x3f4e0e90,0x3f98f8b8,0xbf7b14be,0,0x3e686ff3,0x3fe2f202,0xbf226799,0,
0,0x40000000,0x248d3132,0,0x3f800000,0x3f800000,0x3f800000,0,0x3f800000,0x3f800000,0x3f800000,0,0x3f800000,0x3f800000,0x3f800000,0,
};
#define ASM_FRONT \
  "s_cmp_gt_u32 s2, 110\n" \
  "s_cbranch_scc1 .Lcol_%=\n" \
  "s_load_dwordx2 s[36:37], s[0:1], 48\n" \
  "s_load_dwordx2 s[60:61], s[0:1], 32\n" \
  "s_load_dwordx2 s[64:65], s[0:1], 40\n" \
  "s_load_dwordx2 s[48:49], s[0:1], 0\n" \
  "v_lshrrev_b32 v255, 6, v0\n" \
  "s_getpc_b64 s[80:81]\n" \
  ".Lpcn_%=:\n" \
  "v_readfirstlane_b32 s20, v255\n" \
  "s_mov_b32 s82, .Ladm_b-.Lpcn_%=\n" \
  "s_mov_b32 s83, 131072\n" \
  "s_mov_b32 m0, 162176\n" \
  "s_lshr_b32 s98, s2, 3\n" \
  "s_mul_i32 s98, s98, 20\n" \
  "v_add_u32 v254, s98, v0\n" \
  "v_lshlrev_b32 v253, 7, v254\n" \
  "s_cmp_eq_u32 s20, 0\n" \
  "s_cselect_b64 exec, 1048575, 0\n" \
  "buffer_load_dword v253, s[80:83], 0 offen lds\n" \
  "s_mov_b64 exec, -1\n" \
  "s_mov_b32 s38, 134217728\n" \
  "s_mov_b32 s39, 131072\n" \
  "s_mov_b32 s62, 1152\n" \
  "s_mov_b32 s63, 131072\n" \
  "s_mov_b32 s66, 3981312\n" \
  "s_mov_b32 s67, 131072\n" \
  "s_mov_b32 s50, 2359296\n" \
  "s_mov_b32 s51, 131072\n" \
  "s_mov_b32 s78, 26944\n" \
  "s_mov_b32 s79, 131072\n" \
  "v_and_b32 v191, 63, v0\n" \
  "v_and_b32 v254, 31, v191\n" \
  "v_lshrrev_b32 v253, 5, v191\n" \
  "v_mul_u32_u24 v252, 1179648, v253\n" \
  "v_lshl_add_u32 v251, v254, 4, v252\n" \
  "v_lshl_add_u32 v250, v254, 2, v252\n" \
  "v_lshlrev_b32 v252, 4, v191\n" \
  "s_and_b32 s84, s2, 7\n" \
  "s_lshr_b32 s85, s2, 3\n" \
  "s_lshl_b32 s98, s84, 5\n" \
  "s_add_u32 s86, s98, s85\n" \
  "s_lshl_b32 s98, s20, 8\n" \
  "s_add_u32 s98, s98, s86\n" \
  "s_mul_i32 s87, s98, 1152\n" \
  "s_mul_i32 s98, s2, 57\n" \
  "s_lshr_b32 s22, s98, 11\n" \
  "s_mul_i32 s98, s22, 36\n" \
  "s_sub_u32 s23, s2, s98\n" \
  "v_lshlrev_b32 v249, 3, v0\n" \
  "v_lshrrev_b32 v248, 2, v0\n" \
  "v_and_b32 v247, 3, v0\n" \
  "v_mul_u32_u24 v246, 6912, v248\n" \
  "v_lshl_add_u32 v245, v247, 4, v246\n" \
  "s_mul_i32 s98, s22, 1327104\n" \
  "s_lshl_b32 s100, s23, 6\n" \
  "s_add_u32 s90, s98, s100\n" \
  "s_cmp_lt_u32 s2, 108\n" \
  "s_cselect_b32 s66, s66, 0\n" \
  "s_add_u32 s32, s90, 442368\n" \
  "s_add_u32 s33, s90, 884736\n" \
  "s_waitcnt lgkmcnt(0)\n" \
  "buffer_load_dwordx2 v[2:3], off, s[36:39], 0 sc1\n" \
  "buffer_load_dwordx2 v[4:5], v249, s[60:63], 0 offen offset:0\n" \
  "buffer_load_dwordx4 v[6:9], v245, s[64:67], s90 offen offset:0\n" \
  "buffer_load_dwordx4 v[10:13], v245, s[64:67], s32 offen offset:0\n" \
  "buffer_load_dwordx4 v[14:17], v245, s[64:67], s33 offen offset:0\n" \
  "buffer_load_dwordx4 v[18:21], v251, s[48:51], s87 offen offset:0\n" \
  "buffer_load_dwordx4 v[22:25], v251, s[48:51], s87 offen offset:512\n" \
  "buffer_load_dword v247, v250, s[48:51], s87 offen offset:1024\n" \
  "s_movk_i32 s32, 22528\n" \
  "buffer_load_dwordx4 v[26:29], v252, s[76:79], s32 offen offset:0\n" \
  "s_memrealtime s[16:17]\n" \
  "s_lshr_b32 s32, s20, 1\n" \
  "s_lshl3_add_u32 s32, s32, 8\n" \
  "s_load_dwordx2 s[52:53], s[0:1], s32\n" \
  "s_load_dwordx2 s[4:5], s[0:1], 24\n" \
  "s_load_dwordx2 s[68:69], s[0:1], 56\n" \
  "s_load_dwordx2 s[72:73], s[0:1], 64\n" \
  "s_load_dwordx2 s[40:41], s[0:1], 72\n" \
  "s_load_dwordx2 s[44:45], s[0:1], 80\n" \
  "s_mov_b32 s6, 18874368\n" \
  "s_mov_b32 s7, 131072\n" \
  "s_mov_b32 s70, 9216\n" \
  "s_mov_b32 s71, 131072\n" \
  "s_mov_b32 s74, 4096\n" \
  "s_mov_b32 s75, 131072\n" \
  "s_mov_b32 s42, 18874368\n" \
  "s_mov_b32 s43, 131072\n" \
  "s_mov_b32 s46, 2359296\n" \
  "s_mov_b32 s47, 131072\n" \
  "s_mov_b32 s54, 1183744\n" \
  "s_mov_b32 s55, 131072\n" \
  "v_and_b32 v246, 15, v191\n" \
  "v_lshrrev_b32 v244, 4, v191\n" \
  "v_lshlrev_b32 v243, 4, v0\n" \
  "v_lshrrev_b32 v242, 1, v246\n" \
  "v_and_b32 v241, 1, v244\n" \
  "v_lshl_add_u32 v240, v241, 9, s86\n" \
  "v_and_b32 v241, 1, v246\n" \
  "v_and_b32 v239, 3, v242\n" \
  "v_lshl_add_u32 v238, v241, 2, v239\n" \
  "v_lshl_add_u32 v239, v238, 8, s86\n" \
  "v_lshlrev_b32 v238, 1, v244\n" \
  "v_cvt_f32_u32 v237, v242\n" \
  "v_cvt_f32_u32 v242, v240\n" \
  "v_cmp_eq_u32 vcc, v241, v253\n" \
  "v_mul_f32 v237, 3120562176, v237\n" \
  "v_mov_b32 v240, 1048576000\n" \
  "v_cndmask_b32 v240, 0, v240, vcc\n" \
  "v_lshlrev_b32 v236, 31, v241\n" \
  "v_mul_f32 v242, v237, v242\n" \
  "v_xor_b32 v236, v240, v236\n" \
  "v_mul_f32 v237, 1132462080, v237\n" \
  "v_fma_f32 v235, v237, 0, v242\n" \
  "v_cos_f32 v234, v235\n" \
  "v_sin_f32 v233, v235\n" \
  "v_mul_f32 v232, v234, v240\n" \
  "v_mul_f32 v231, v233, v236\n" \
  "v_mul_f32 v230, v233, v240\n" \
  "v_mul_f32 v229, v234, v236\n" \
  "v_cvt_pk_f16_f32 v232, v232, -v231\n" \
  "v_cvt_pk_f16_f32 v230, v230, v229\n" \
  "v_accvgpr_write_b32 a160, v232\n" \
  "v_accvgpr_write_b32 a164, v230\n" \
  "v_fma_f32 v235, v237, 4.0, v242\n" \
  "v_cos_f32 v234, v235\n" \
  "v_sin_f32 v233, v235\n" \
  "v_mul_f32 v232, v234, v240\n" \
  "v_mul_f32 v231, v233, v236\n" \
  "v_mul_f32 v230, v233, v240\n" \
  "v_mul_f32 v229, v234, v236\n" \
  "v_cvt_pk_f16_f32 v232, v232, -v231\n" \
  "v_cvt_pk_f16_f32 v230, v230, v229\n" \
  "v_accvgpr_write_b32 a161, v232\n" \
  "v_accvgpr_write_b32 a165, v230\n" \
  "v_fma_f32 v235, v237, 1.0, v242\n" \
  "v_cos_f32 v234, v235\n" \
  "v_sin_f32 v233, v235\n" \
  "v_mul_f32 v232, v234, v240\n" \
  "v_mul_f32 v231, v233, v236\n" \
  "v_mul_f32 v230, v233, v240\n" \
  "v_mul_f32 v229, v234, v236\n" \
  "v_cvt_pk_f16_f32 v232, v232, -v231\n" \
  "v_cvt_pk_f16_f32 v230, v230, v229\n" \
  "v_accvgpr_write_b32 a162, v232\n" \
  "v_accvgpr_write_b32 a166, v230\n" \
  "v_fma_f32 v235, v237, 4.0, v242\n" \
  "v_add_f32 v235, v235, v237\n" \
  "v_cos_f32 v234, v235\n" \
  "v_sin_f32 v233, v235\n" \
  "v_mul_f32 v232, v234, v240\n" \
  "v_mul_f32 v231, v233, v236\n" \
  "v_mul_f32 v230, v233, v240\n" \
  "v_mul_f32 v229, v234, v236\n" \
  "v_cvt_pk_f16_f32 v232, v232, -v231\n" \
  "v_cvt_pk_f16_f32 v230, v230, v229\n" \
  "v_accvgpr_write_b32 a163, v232\n" \
  "v_accvgpr_write_b32 a167, v230\n" \
  "v_cvt_f32_u32 v237, v239\n" \
  "v_cvt_f32_u32 v242, v238\n" \
  "v_cmp_lt_u32 vcc, 7, v246\n" \
  "v_mul_f32 v237, 973078528, v237\n" \
  "v_mul_f32 v235, v237, v242\n" \
  "v_cos_f32 v234, v235\n" \
  "v_sin_f32 v233, v235\n" \
  "v_mul_f32 v234, 0.5, v234\n" \
  "v_mul_f32 v233, 0.5, v233\n" \
  "v_cvt_pk_f16_f32 v232, v234, -v233\n" \
  "v_cvt_pk_f16_f32 v231, v233, v234\n" \
  "v_cndmask_b32 v230, v232, 0, vcc\n" \
  "v_cndmask_b32 v232, 0, v232, vcc\n" \
  "v_cndmask_b32 v229, v231, 0, vcc\n" \
  "v_xor_b32 v231, 2147516416, v231\n" \
  "v_accvgpr_write_b32 a228, v230\n" \
  "v_cndmask_b32 v231, 0, v231, vcc\n" \
  "v_accvgpr_write_b32 a229, v232\n" \
  "v_accvgpr_write_b32 a232, v229\n" \
  "v_accvgpr_write_b32 a233, v231\n" \
  "v_add_f32 v235, v235, v237\n" \
  "v_cos_f32 v234, v235\n" \
  "v_sin_f32 v233, v235\n" \
  "v_mul_f32 v234, 0.5, v234\n" \
  "v_mul_f32 v233, 0.5, v233\n" \
  "v_cvt_pk_f16_f32 v232, v234, -v233\n" \
  "v_cvt_pk_f16_f32 v231, v233, v234\n" \
  "v_cndmask_b32 v230, v232, 0, vcc\n" \
  "v_cndmask_b32 v232, 0, v232, vcc\n" \
  "v_cndmask_b32 v229, v231, 0, vcc\n" \
  "v_xor_b32 v231, 2147516416, v231\n" \
  "v_accvgpr_write_b32 a230, v230\n" \
  "v_cndmask_b32 v231, 0, v231, vcc\n" \
  "v_accvgpr_write_b32 a231, v232\n" \
  "v_accvgpr_write_b32 a234, v229\n" \
  "v_accvgpr_write_b32 a235, v231\n" \
  "s_cmp_eq_u32 s20, 0\n" \
  "s_cselect_b64 s[58:59], -1, 0\n" \
  "s_movk_i32 s34, 144\n" \
  "v_cmp_gt_u32 s[94:95], s34, v0\n" \
  "v_mov_b32 v30, 0\n" \
  "v_mov_b32 v31, 0\n" \
  "v_mov_b32 v255, 161664\n" \
  "ds_write_b64 v255, v[30:31] offset:0\n" \
  "s_lshr_b32 s88, s2, 6\n" \
  "s_lshl_b32 s98, s84, 2\n" \
  "s_lshr_b32 s100, s85, 1\n" \
  "s_and_b32 s100, s100, 3\n" \
  "s_add_u32 s89, s98, s100\n" \
  "s_mul_i32 s98, s86, 9344\n" \
  "s_add_u32 s82, s98, 524288\n" \
  "s_lshl_b32 s98, s86, 8\n" \
  "s_add_u32 s11, s98, 9764864\n" \
  "s_lshl_b32 s98, s86, 4\n" \
  "s_add_u32 s28, s98, 65536\n" \
  "s_and_b32 s98, s84, 1\n" \
  "s_lshl_b32 s98, s98, 3\n" \
  "s_and_b32 s100, s85, 7\n" \
  "s_add_u32 s90, s98, s100\n" \
  "s_lshl_b32 s98, s88, 5\n" \
  "s_add_u32 s98, s98, s89\n" \
  "s_lshl_b32 s98, s98, 1\n" \
  "s_and_b32 s100, s85, 1\n" \
  "s_add_u32 s98, s98, s100\n" \
  "s_lshl_b32 s98, s98, 7\n" \
  "s_add_u32 s80, s98, 262144\n" \
  "s_mov_b32 s35, 987991609\n" \
  "v_mul_u32_u24 v242, 9, v244\n" \
  "v_add_u32 v240, 8, v242\n" \
  "v_lshlrev_b32 v242, 4, v246\n" \
  "v_lshl_add_u32 v239, v240, 16, v242\n" \
  "v_lshrrev_b32 v242, 3, v191\n" \
  "v_and_b32 v240, 7, v191\n" \
  "v_lshlrev_b32 v238, 4, v240\n" \
  "v_lshl_add_u32 v240, v242, 16, v238\n" \
  "v_add_u32 v242, s80, v240\n" \
  "s_add_u32 s83, s82, 4096\n" \
  "s_add_u32 s91, s82, 8192\n" \
  "v_mov_b32 v30, -1\n" \
  "v_mov_b32 v31, -1\n" \
  "v_mov_b32 v32, -1\n" \
  "v_mov_b32 v33, -1\n" \
  "v_cmp_eq_u32 vcc, 24, v191\n" \
  "v_mov_b32 v255, s28\n" \
  "s_cmp_eq_u32 s20, 2\n" \
  "v_cndmask_b32 v242, v242, v255, vcc\n" \
  "s_cselect_b64 vcc, -1, 0\n" \
  "s_cselect_b32 s91, s11, s91\n" \
  "v_cndmask_b32 v240, v243, v239, vcc\n" \
  "s_cmp_eq_u32 s20, 3\n" \
  "s_cselect_b64 vcc, -1, 0\n" \
  "s_cselect_b32 s91, 0, s91\n" \
  "s_cselect_b32 s32, 0, -1\n" \
  "v_cndmask_b32 v240, v240, v242, vcc\n" \
  "s_cselect_b32 s92, 33554431, -1\n" \
  "s_cselect_b32 s93, 0, -1\n" \
  "s_cmp_eq_u32 s20, 1\n" \
  "s_cselect_b64 s[92:93], 15, s[92:93]\n" \
  "v_mov_b32 v34, s32\n" \
  "v_mov_b32 v35, s32\n" \
  "v_mov_b32 v36, s32\n" \
  "v_mov_b32 v37, s32\n" \
  "s_movk_i32 s32, 768\n" \
  "v_lshlrev_b32 v242, 5, v191\n" \
  "v_bfe_u32 v239, v191, 5, 1\n" \
  "v_and_or_b32 v242, v242, s32, v239\n" \
  "v_lshrrev_b32 v239, 3, v191\n" \
  "v_mul_u32_u24 v242, 9216, v242\n" \
  "v_bitop3_b32 v239, v239, v191, 6 bitop3:0x6c\n" \
  "v_and_b32 v239, 7, v239\n" \
  "v_lshl_or_b32 v38, v239, 4, v242\n" \
  "v_add_u32 v39, 17408, v38\n" \
  "v_add_u32 v40, 34816, v38\n" \
  "v_add_u32 v41, 52224, v38\n" \
  "v_bfrev_b32 v242, 1\n" \
  "v_cndmask_b32 v242, v242, v243, s[58:59]\n" \
  "s_waitcnt lgkmcnt(0)\n" \
  "s_mov_b32 s17, s16\n" \
  "v_and_b32 v239, 127, v0\n" \
  "s_mul_i32 s98, s86, 4624\n" \
  "v_lshl_add_u32 v238, v239, 4, s98\n" \
  "v_add_u32 v237, 3440, v238\n" \
  "v_add_u32 v236, 2048, v238\n" \
  "v_add_u32 v235, 1392, v238\n" \
  "s_lshr_b32 s98, s20, 1\n" \
  "s_mul_i32 s98, s98, 6144\n" \
  "s_and_b32 s100, s20, 1\n" \
  "s_lshl_b32 s100, s100, 10\n" \
  "s_add_u32 s98, s98, s100\n" \
  "s_add_u32 s56, s98, 19584\n" \
  "v_cmp_gt_u32 vcc, 57, v239\n" \
  "v_subrev_u32 v255, 1183744, v237\n" \
  "s_mov_b32 m0, s56\n" \
  "v_cndmask_b32 v236, v235, v236, vcc\n" \
  "buffer_load_dwordx4 v238, s[52:55], 0 offen offset:0 lds\n" \
  "v_min_u32 v237, v237, v255\n" \
  "s_add_u32 m0, s56, 2048\n" \
  "s_bitcmp1_b32 s20, 0\n" \
  "buffer_load_dwordx4 v236, s[52:55], 0 offen offset:0 lds\n" \
  "s_add_u32 m0, s56, 4096\n" \
  "s_cselect_b32 exec_hi, 32767, -1\n" \
  "buffer_load_dwordx4 v237, s[52:55], 0 offen offset:0 lds\n" \
  "s_mov_b64 exec, -1\n" \
  "s_movk_i32 s32, 10240\n" \
  "buffer_load_dwordx4 v[42:45], v243, s[76:79], s32 offen offset:0\n" \
  "s_waitcnt vmcnt(12)\n" \
  "v_readfirstlane_b32 s18, v2\n" \
  "v_readfirstlane_b32 s19, v3\n" \
  "s_add_u32 s18, s18, 1\n" \
  "s_addc_u32 s19, s19, 0\n" \
  "s_waitcnt vmcnt(11)\n" \
  "v_lshlrev_b32 v2, 16, v4\n" \
  "v_and_b32 v3, 4294901760, v4\n" \
  "v_lshlrev_b32 v46, 16, v5\n" \
  "v_and_b32 v47, 4294901760, v5\n" \
  "v_mul_f32 v239, 3216550459, v2\n" \
  "v_mul_f32 v238, 3216550459, v3\n" \
  "v_mul_f32 v237, 3216550459, v46\n" \
  "v_mul_f32 v236, 3216550459, v47\n" \
  "v_exp_f32 v239, v239\n" \
  "v_exp_f32 v238, v238\n" \
  "v_exp_f32 v237, v237\n" \
  "v_exp_f32 v236, v236\n" \
  "v_add_f32 v239, 1.0, v239\n" \
  "v_add_f32 v238, 1.0, v238\n" \
  "v_add_f32 v237, 1.0, v237\n" \
  "v_add_f32 v236, 1.0, v236\n" \
  "v_rcp_f32 v239, v239\n" \
  "v_rcp_f32 v238, v238\n" \
  "v_rcp_f32 v237, v237\n" \
  "v_rcp_f32 v236, v236\n" \
  "v_mul_f32 v239, v239, v2\n" \
  "v_mul_f32 v238, v238, v3\n" \
  "v_mul_f32 v237, v237, v46\n" \
  "v_mul_f32 v236, v236, v47\n" \
  "v_cvt_pk_bf16_f32 v2, v239, v238\n" \
  "v_cvt_pk_bf16_f32 v3, v237, v236\n" \
  "v_add_u32 v239, 162432, v249\n" \
  "s_mul_i32 s98, s22, 384\n" \
  "s_add_u32 s98, s98, 162432\n" \
  "v_lshl_add_u32 v249, v248, 1, s98\n" \
  "v_and_b32 v248, 3, v191\n" \
  "v_lshlrev_b32 v238, 3, v244\n" \
  "v_lshl_add_u32 v237, v248, 5, v238\n" \
  "s_lshl_b32 s98, s20, 7\n" \
  "s_add_u32 s98, s98, 161152\n" \
  "v_add_u32 v248, s98, v237\n" \
  "v_lshlrev_b32 v238, 3, v246\n" \
  "v_add_u32 v237, 161152, v238\n" \
  "s_mul_i32 s98, s22, 9216\n" \
  "s_lshl_b32 s100, s23, 8\n" \
  "s_add_u32 s98, s98, s100\n" \
  "s_add_u32 s63, s98, 8192\n" \
  "s_mov_b64 exec, s[94:95]\n" \
  "ds_write_b64 v239, v[2:3] offset:0\n" \
  "s_mov_b64 exec, -1\n" \
  "s_cmp_lt_u32 s2, 108\n" \
  "s_cselect_b64 s[96:97], 65535, 0\n" \
  "s_and_b64 s[96:97], s[96:97], s[58:59]\n" \
  "s_waitcnt lgkmcnt(0)\n" \
  "s_barrier\n" \
  "ds_read_u16 v2, v249 offset:0\n" \
  "ds_read_u16 v4, v249 offset:128\n" \
  "ds_read_u16 v46, v249 offset:256\n" \
  "s_waitcnt lgkmcnt(2)\n" \
  "v_lshlrev_b32 v2, 16, v2\n" \
  "s_waitcnt lgkmcnt(1)\n" \
  "v_lshlrev_b32 v4, 16, v4\n" \
  "s_waitcnt lgkmcnt(0)\n" \
  "v_lshlrev_b32 v46, 16, v46\n" \
  "s_waitcnt vmcnt(10)\n" \
  "v_lshlrev_b32 v48, 16, v6\n" \
  "v_and_b32 v49, 4294901760, v6\n" \
  "v_pk_mul_f32 v[50:51], v[48:49], v[2:3] op_sel_hi:[1,0]\n" \
  "v_lshlrev_b32 v48, 16, v7\n" \
  "v_and_b32 v49, 4294901760, v7\n" \
  "v_pk_mul_f32 v[52:53], v[48:49], v[2:3] op_sel_hi:[1,0]\n" \
  "v_lshlrev_b32 v48, 16, v8\n" \
  "v_and_b32 v49, 4294901760, v8\n" \
  "v_pk_mul_f32 v[54:55], v[48:49], v[2:3] op_sel_hi:[1,0]\n" \
  "v_lshlrev_b32 v48, 16, v9\n" \
  "v_and_b32 v49, 4294901760, v9\n" \
  "v_pk_mul_f32 v[6:7], v[48:49], v[2:3] op_sel_hi:[1,0]\n" \
  "s_waitcnt vmcnt(9)\n" \
  "v_lshlrev_b32 v2, 16, v10\n" \
  "v_and_b32 v3, 4294901760, v10\n" \
  "v_pk_fma_f32 v[50:51], v[2:3], v[4:5], v[50:51] op_sel_hi:[1,0,1]\n" \
  "v_lshlrev_b32 v2, 16, v11\n" \
  "v_and_b32 v3, 4294901760, v11\n" \
  "v_pk_fma_f32 v[52:53], v[2:3], v[4:5], v[52:53] op_sel_hi:[1,0,1]\n" \
  "v_lshlrev_b32 v2, 16, v12\n" \
  "v_and_b32 v3, 4294901760, v12\n" \
  "v_pk_fma_f32 v[54:55], v[2:3], v[4:5], v[54:55] op_sel_hi:[1,0,1]\n" \
  "v_lshlrev_b32 v2, 16, v13\n" \
  "v_and_b32 v3, 4294901760, v13\n" \
  "v_pk_fma_f32 v[6:7], v[2:3], v[4:5], v[6:7] op_sel_hi:[1,0,1]\n" \
  "s_waitcnt vmcnt(8)\n" \
  "v_lshlrev_b32 v2, 16, v14\n" \
  "v_and_b32 v3, 4294901760, v14\n" \
  "v_pk_fma_f32 v[50:51], v[2:3], v[46:47], v[50:51] op_sel_hi:[1,0,1]\n" \
  "v_lshlrev_b32 v2, 16, v15\n" \
  "v_and_b32 v3, 4294901760, v15\n" \
  "v_pk_fma_f32 v[52:53], v[2:3], v[46:47], v[52:53] op_sel_hi:[1,0,1]\n" \
  "v_lshlrev_b32 v2, 16, v16\n" \
  "v_and_b32 v3, 4294901760, v16\n" \
  "v_pk_fma_f32 v[54:55], v[2:3], v[46:47], v[54:55] op_sel_hi:[1,0,1]\n" \
  "v_lshlrev_b32 v2, 16, v17\n" \
  "v_and_b32 v3, 4294901760, v17\n" \
  "v_pk_fma_f32 v[6:7], v[2:3], v[46:47], v[6:7] op_sel_hi:[1,0,1]\n" \
  "v_permlane32_swap_b32 v50, v54\n" \
  "v_permlane32_swap_b32 v51, v55\n" \
  "v_permlane32_swap_b32 v52, v6\n" \
  "v_permlane32_swap_b32 v53, v7\n" \
  "v_pk_add_f32 v[50:51], v[50:51], v[54:55]\n" \
  "v_pk_add_f32 v[52:53], v[52:53], v[6:7]\n" \
  "v_mov_b32 v2, s18\n" \
  "v_mov_b32 v3, s19\n" \
  "v_permlane16_swap_b32 v50, v52\n" \
  "v_permlane16_swap_b32 v51, v53\n" \
  "v_pk_add_f32 v[50:51], v[50:51], v[52:53]\n" \
  "s_nop 1\n" \
  "v_add_f32_dpp v50, v50, v50 row_ror:8 row_mask:0xf bank_mask:0xf\n" \
  "v_add_f32_dpp v51, v51, v51 row_ror:8 row_mask:0xf bank_mask:0xf\n" \
  "s_nop 0\n" \
  "v_add_f32_dpp v50, v50, v50 row_ror:4 row_mask:0xf bank_mask:0xf\n" \
  "v_add_f32_dpp v51, v51, v51 row_ror:4 row_mask:0xf bank_mask:0xf\n" \
  "ds_write_b64 v248, v[50:51] offset:0\n" \
  "s_waitcnt lgkmcnt(0)\n" \
  "s_barrier\n" \
  "ds_read_b64 v[50:51], v237 offset:0\n" \
  "ds_read_b64 v[52:53], v237 offset:128\n" \
  "ds_read_b64 v[54:55], v237 offset:256\n" \
  "ds_read_b64 v[6:7], v237 offset:384\n" \
  "s_waitcnt lgkmcnt(2)\n" \
  "v_pk_add_f32 v[50:51], v[50:51], v[52:53]\n" \
  "s_waitcnt lgkmcnt(0)\n" \
  "v_pk_add_f32 v[54:55], v[54:55], v[6:7]\n" \
  "v_pk_add_f32 v[4:5], v[50:51], v[54:55]\n" \
  "s_mov_b64 exec, s[96:97]\n" \
  "buffer_store_dwordx4 v[2:5], v252, s[36:39], s63 offen offset:0 sc1\n" \
  "s_mov_b64 exec, -1\n" \
  "buffer_store_dwordx4 v[30:33], v243, s[36:39], s82 offen offset:0 sc1\n" \
  "buffer_store_dwordx4 v[30:33], v243, s[36:39], s83 offen offset:0 sc1\n" \
  "s_mov_b64 exec, s[92:93]\n" \
  "buffer_store_dwordx4 v[34:37], v240, s[36:39], s91 offen offset:0 sc1\n" \
  "s_mov_b64 exec, -1\n" \
  "s_lshl_b32 s98, s85, 3\n" \
  "s_lshl_b32 s100, s20, 1\n" \
  "s_add_u32 s98, s98, s100\n" \
  "s_mul_i32 s98, s98, 9216\n" \
  "s_mul_i32 s100, s84, 1152\n" \
  "s_add_u32 s98, s98, s100\n" \
  "s_add_u32 s32, s98, 1024\n" \
  "s_lshl_b32 s98, s20, 10\n" \
  "s_add_u32 s33, s98, 155008\n" \
  "s_mov_b32 m0, s33\n" \
  "s_movk_i32 s31, 4096\n" \
  "s_cmp_gt_u32 s20, 1\n" \
  "buffer_load_dwordx4 v38, s[4:7], s32 offen offset:0 lds\n" \
  "s_cselect_b32 s31, -28672, s31\n" \
  "s_add_u32 m0, s33, s31\n" \
  "s_add_u32 s32, s32, 9437184\n" \
  "buffer_load_dwordx4 v38, s[4:7], s32 offen offset:0 lds\n" \
  "s_waitcnt vmcnt(6)\n" \
  "s_barrier\n" \
  "v_and_b32 v249, 65535, v42\n" \
  "ds_read_u16 v2, v249 offset:19584\n" \
  "ds_read_u16 v3, v249 offset:20162\n" \
  "ds_read_u16 v4, v249 offset:20740\n" \
  "ds_read_u16 v5, v249 offset:21318\n" \
  "ds_read_u16 v6, v249 offset:25728\n" \
  "ds_read_u16 v7, v249 offset:26306\n" \
  "ds_read_u16 v8, v249 offset:26884\n" \
  "ds_read_u16 v9, v249 offset:27462\n" \
  "s_add_u32 s32, s82, 2392064\n" \
  "s_cmp_eq_u32 s20, 1\n" \
  "s_cselect_b64 vcc, 255, s[58:59]\n" \
  "v_mov_b32 v249, 0\n" \
  "v_and_b32 v248, 65535, v43\n" \
  "ds_read_u16 v10, v248 offset:19584\n" \
  "ds_read_u16 v11, v248 offset:20162\n" \
  "ds_read_u16 v12, v248 offset:20740\n" \
  "ds_read_u16 v13, v248 offset:21318\n" \
  "ds_read_u16 v14, v248 offset:25728\n" \
  "ds_read_u16 v15, v248 offset:26306\n" \
  "ds_read_u16 v16, v248 offset:26884\n" \
  "ds_read_u16 v17, v248 offset:27462\n" \
  "v_lshrrev_b32 v248, 16, v42\n" \
  "s_waitcnt lgkmcnt(11)\n" \
  "v_lshl_or_b32 v2, v6, 16, v2\n" \
  "s_waitcnt lgkmcnt(10)\n" \
  "v_lshl_or_b32 v3, v7, 16, v3\n" \
  "s_waitcnt lgkmcnt(9)\n" \
  "v_lshl_or_b32 v4, v8, 16, v4\n" \
  "s_waitcnt lgkmcnt(8)\n" \
  "v_lshl_or_b32 v5, v9, 16, v5\n" \
  "buffer_store_dwordx4 v[2:5], v248, s[36:39], s32 offen offset:0 sc1\n" \
  "v_and_b32 v6, 2147450879, v2\n" \
  "v_pk_max_u16 v249, v249, v6\n" \
  "v_and_b32 v7, 2147450879, v3\n" \
  "v_pk_max_u16 v249, v249, v7\n" \
  "v_and_b32 v8, 2147450879, v4\n" \
  "v_pk_max_u16 v249, v249, v8\n" \
  "v_and_b32 v9, 2147450879, v5\n" \
  "v_pk_max_u16 v249, v249, v9\n" \
  "v_and_b32 v248, 65535, v44\n" \
  "ds_read_u16 v2, v248 offset:19584\n" \
  "ds_read_u16 v3, v248 offset:20162\n" \
  "ds_read_u16 v4, v248 offset:20740\n" \
  "ds_read_u16 v5, v248 offset:21318\n" \
  "ds_read_u16 v6, v248 offset:25728\n" \
  "ds_read_u16 v7, v248 offset:26306\n" \
  "ds_read_u16 v8, v248 offset:26884\n" \
  "ds_read_u16 v9, v248 offset:27462\n" \
  "v_lshrrev_b32 v248, 16, v43\n" \
  "s_waitcnt lgkmcnt(11)\n" \
  "v_lshl_or_b32 v10, v14, 16, v10\n" \
  "s_waitcnt lgkmcnt(10)\n" \
  "v_lshl_or_b32 v11, v15, 16, v11\n" \
  "s_waitcnt lgkmcnt(9)\n" \
  "v_lshl_or_b32 v12, v16, 16, v12\n" \
  "s_waitcnt lgkmcnt(8)\n" \
  "v_lshl_or_b32 v13, v17, 16, v13\n" \
  "buffer_store_dwordx4 v[10:13], v248, s[36:39], s32 offen offset:0 sc1\n" \
  "v_and_b32 v14, 2147450879, v10\n" \
  "v_pk_max_u16 v249, v249, v14\n" \
  "v_and_b32 v15, 2147450879, v11\n" \
  "v_pk_max_u16 v249, v249, v15\n" \
  "v_and_b32 v16, 2147450879, v12\n" \
  "v_pk_max_u16 v249, v249, v16\n" \
  "v_and_b32 v17, 2147450879, v13\n" \
  "v_pk_max_u16 v249, v249, v17\n" \
  "s_mov_b64 s[96:97], vcc\n" \
  "v_lshrrev_b32 v248, 16, v44\n" \
  "s_waitcnt lgkmcnt(3)\n" \
  "v_lshl_or_b32 v2, v6, 16, v2\n" \
  "s_waitcnt lgkmcnt(2)\n" \
  "v_lshl_or_b32 v3, v7, 16, v3\n" \
  "s_waitcnt lgkmcnt(1)\n" \
  "v_lshl_or_b32 v4, v8, 16, v4\n" \
  "s_waitcnt lgkmcnt(0)\n" \
  "v_lshl_or_b32 v5, v9, 16, v5\n" \
  "s_mov_b64 exec, s[96:97]\n" \
  "buffer_store_dwordx4 v[2:5], v248, s[36:39], s32 offen offset:0 sc1\n" \
  "s_mov_b64 exec, -1\n" \
  "v_and_b32 v6, 2147450879, v2\n" \
  "v_pk_max_u16 v249, v249, v6\n" \
  "v_and_b32 v7, 2147450879, v3\n" \
  "v_pk_max_u16 v249, v249, v7\n" \
  "v_and_b32 v8, 2147450879, v4\n" \
  "v_pk_max_u16 v249, v249, v8\n" \
  "v_and_b32 v9, 2147450879, v5\n" \
  "v_pk_max_u16 v249, v249, v9\n" \
  "v_lshlrev_b32 v255, 16, v249\n" \
  "v_max_u32 v249, v249, v255\n" \
  "v_mov_b32 v255, 161664\n" \
  "s_nop 0\n" \
  "v_max_u32_dpp v249, v249, v249 quad_perm:[1,0,3,2] row_mask:0xf bank_mask:0xf\n" \
  "s_nop 1\n" \
  "v_max_u32_dpp v249, v249, v249 quad_perm:[2,3,0,1] row_mask:0xf bank_mask:0xf\n" \
  "s_nop 1\n" \
  "v_max_u32_dpp v249, v249, v249 row_half_mirror row_mask:0xf bank_mask:0xf\n" \
  "s_nop 1\n" \
  "v_max_u32_dpp v249, v249, v249 row_mirror row_mask:0xf bank_mask:0xf\n" \
  "s_mov_b32 exec_lo, 65537\n" \
  "s_mov_b32 exec_hi, 65537\n" \
  "ds_max_u32 v255, v249 offset:4\n" \
  "s_mov_b64 exec, -1\n" \
  "v_mul_u32_u24 v249, 2448, v253\n" \
  "s_mul_i32 s98, s20, 4896\n" \
  "v_add_u32 v248, s98, v249\n" \
  "v_lshrrev_b32 v249, 3, v254\n" \
  "v_lshlrev_b32 v240, 4, v249\n" \
  "v_lshl_add_u32 v249, v254, 5, v240\n" \
  "v_add_u32 v240, v249, v248\n" \
  "v_lshl_add_u32 v249, v254, 3, v248\n" \
  "v_lshlrev_b32 v2, 16, v18\n" \
  "v_and_b32 v3, 4294901760, v18\n" \
  "v_lshlrev_b32 v4, 16, v19\n" \
  "v_and_b32 v5, 4294901760, v19\n" \
  "v_lshlrev_b32 v6, 16, v20\n" \
  "v_and_b32 v7, 4294901760, v20\n" \
  "v_lshlrev_b32 v8, 16, v21\n" \
  "v_and_b32 v9, 4294901760, v21\n" \
  "v_lshlrev_b32 v10, 16, v22\n" \
  "v_and_b32 v11, 4294901760, v22\n" \
  "v_lshlrev_b32 v12, 16, v23\n" \
  "v_and_b32 v13, 4294901760, v23\n" \
  "v_lshlrev_b32 v14, 16, v24\n" \
  "v_and_b32 v15, 4294901760, v24\n" \
  "v_lshlrev_b32 v16, 16, v25\n" \
  "v_and_b32 v17, 4294901760, v25\n" \
  "v_lshlrev_b32 v30, 16, v247\n" \
  "v_and_b32 v31, 4294901760, v247\n" \
  "v_mov_b32 v33, 925353388\n" \
  "v_mov_b32_dpp v34, v2 row_newbcast:0 row_mask:0xf bank_mask:0xf\n" \
  "v_mov_b32_dpp v248, v2 row_newbcast:0 row_mask:0xf bank_mask:0xf\n" \
  "s_nop 1\n" \
  "v_permlane16_swap_b32 v34, v248\n" \
  "v_pk_add_f32 v[2:3], v[2:3], v[34:35] op_sel_hi:[1,0] neg_lo:[0,1] neg_hi:[0,1]\n" \
  "v_pk_add_f32 v[4:5], v[4:5], v[34:35] op_sel_hi:[1,0] neg_lo:[0,1] neg_hi:[0,1]\n" \
  "v_pk_add_f32 v[6:7], v[6:7], v[34:35] op_sel_hi:[1,0] neg_lo:[0,1] neg_hi:[0,1]\n" \
  "v_pk_add_f32 v[8:9], v[8:9], v[34:35] op_sel_hi:[1,0] neg_lo:[0,1] neg_hi:[0,1]\n" \
  "v_pk_add_f32 v[10:11], v[10:11], v[34:35] op_sel_hi:[1,0] neg_lo:[0,1] neg_hi:[0,1]\n" \
  "v_pk_add_f32 v[12:13], v[12:13], v[34:35] op_sel_hi:[1,0] neg_lo:[0,1] neg_hi:[0,1]\n" \
  "v_pk_add_f32 v[14:15], v[14:15], v[34:35] op_sel_hi:[1,0] neg_lo:[0,1] neg_hi:[0,1]\n" \
  "v_pk_add_f32 v[16:17], v[16:17], v[34:35] op_sel_hi:[1,0] neg_lo:[0,1] neg_hi:[0,1]\n" \
  "v_pk_add_f32 v[30:31], v[30:31], v[34:35] op_sel_hi:[1,0] neg_lo:[0,1] neg_hi:[0,1]\n" \
  "v_pk_add_f32 v[34:35], v[2:3], v[4:5]\n" \
  "v_pk_mul_f32 v[36:37], v[2:3], v[2:3]\n" \
  "v_pk_fma_f32 v[36:37], v[4:5], v[4:5], v[36:37]\n" \
  "v_pk_add_f32 v[34:35], v[34:35], v[6:7]\n" \
  "v_pk_fma_f32 v[36:37], v[6:7], v[6:7], v[36:37]\n" \
  "v_pk_add_f32 v[34:35], v[34:35], v[8:9]\n" \
  "v_pk_fma_f32 v[36:37], v[8:9], v[8:9], v[36:37]\n" \
  "v_pk_add_f32 v[34:35], v[34:35], v[10:11]\n" \
  "v_pk_fma_f32 v[36:37], v[10:11], v[10:11], v[36:37]\n" \
  "v_pk_add_f32 v[34:35], v[34:35], v[12:13]\n" \
  "v_pk_fma_f32 v[36:37], v[12:13], v[12:13], v[36:37]\n" \
  "v_pk_add_f32 v[34:35], v[34:35], v[14:15]\n" \
  "v_pk_fma_f32 v[36:37], v[14:15], v[14:15], v[36:37]\n" \
  "v_pk_add_f32 v[34:35], v[34:35], v[16:17]\n" \
  "v_pk_fma_f32 v[36:37], v[16:17], v[16:17], v[36:37]\n" \
  "v_pk_add_f32 v[34:35], v[34:35], v[30:31]\n" \
  "v_pk_fma_f32 v[36:37], v[30:31], v[30:31], v[36:37]\n" \
  "v_add_f32 v248, v34, v35\n" \
  "v_add_f32 v239, v36, v37\n" \
  "s_nop 1\n" \
  "v_permlane16_swap_b32 v248, v239\n" \
  "v_add_f32 v248, v248, v239\n" \
  "s_nop 1\n" \
  "v_add_f32_dpp v248, v248, v248 quad_perm:[1,0,3,2] row_mask:0xf bank_mask:0xf\n" \
  "s_nop 1\n" \
  "v_add_f32_dpp v248, v248, v248 quad_perm:[2,3,0,1] row_mask:0xf bank_mask:0xf\n" \
  "s_nop 1\n" \
  "v_add_f32_dpp v248, v248, v248 row_half_mirror row_mask:0xf bank_mask:0xf\n" \
  "s_nop 1\n" \
  "v_add_f32_dpp v248, v248, v248 row_mirror row_mask:0xf bank_mask:0xf\n" \
  "v_mov_b32 v239, v248\n" \
  "s_nop 1\n" \
  "v_permlane16_swap_b32 v248, v239\n" \
  "v_mul_f32 v248, s35, v248\n" \
  "v_fma_f32 v239, v239, s35, v33\n" \
  "v_fma_f32 v239, -v248, v248, v239\n" \
  "v_max_f32 v239, v239, v33\n" \
  "v_rsq_f32 v32, v239\n" \
  "s_nop 0\n" \
  "v_mul_f32_e64 v33, -v248, v32\n" \
  "v_pk_fma_f32 v[34:35], v[2:3], v[32:33], v[32:33] op_sel:[0,0,1] op_sel_hi:[1,0,1]\n" \
  "v_pk_fma_f32 v[36:37], v[4:5], v[32:33], v[32:33] op_sel:[0,0,1] op_sel_hi:[1,0,1]\n" \
  "ds_write_b128 v240, v[34:37] offset:0\n" \
  "v_pk_fma_f32 v[2:3], v[6:7], v[32:33], v[32:33] op_sel:[0,0,1] op_sel_hi:[1,0,1]\n" \
  "v_pk_fma_f32 v[4:5], v[8:9], v[32:33], v[32:33] op_sel:[0,0,1] op_sel_hi:[1,0,1]\n" \
  "ds_write_b128 v240, v[2:5] offset:16\n" \
  "v_pk_fma_f32 v[2:3], v[10:11], v[32:33], v[32:33] op_sel:[0,0,1] op_sel_hi:[1,0,1]\n" \
  "v_pk_fma_f32 v[4:5], v[12:13], v[32:33], v[32:33] op_sel:[0,0,1] op_sel_hi:[1,0,1]\n" \
  "ds_write_b128 v240, v[2:5] offset:1088\n" \
  "v_pk_fma_f32 v[2:3], v[14:15], v[32:33], v[32:33] op_sel:[0,0,1] op_sel_hi:[1,0,1]\n" \
  "v_pk_fma_f32 v[4:5], v[16:17], v[32:33], v[32:33] op_sel:[0,0,1] op_sel_hi:[1,0,1]\n" \
  "ds_write_b128 v240, v[2:5] offset:1104\n" \
  "v_pk_fma_f32 v[30:31], v[30:31], v[32:33], v[32:33] op_sel:[0,0,1] op_sel_hi:[1,0,1]\n" \
  "ds_write_b64 v249, v[30:31] offset:2176\n" \
  "s_mul_i32 s33, s20, 4896\n" \
  "v_and_b32 v249, 65535, v26\n" \
  "v_add_u32 v248, s33, v249\n" \
  "v_and_b32 v240, 65535, v27\n" \
  "v_add_u32 v239, s33, v240\n" \
  "v_and_b32 v238, 65535, v28\n" \
  "v_add_u32 v237, s33, v238\n" \
  "v_and_b32 v236, 65535, v29\n" \
  "v_add_u32 v235, s33, v236\n" \
  "v_lshrrev_b32 v234, 16, v26\n" \
  "v_add_u32 v233, s33, v234\n" \
  "v_lshrrev_b32 v232, 16, v27\n" \
  "v_add_u32 v231, s33, v232\n" \
  "v_lshrrev_b32 v230, 16, v28\n" \
  "v_add_u32 v229, s33, v230\n" \
  "v_lshrrev_b32 v228, 16, v29\n" \
  "v_add_u32 v227, s33, v228\n" \
  "ds_read_b64 v[2:3], v248 offset:0\n" \
  "ds_read_b64 v[4:5], v239 offset:0\n" \
  "ds_read_b64 v[6:7], v237 offset:0\n" \
  "ds_read_b64 v[8:9], v235 offset:0\n" \
  "ds_read_b64 v[10:11], v233 offset:0\n" \
  "ds_read_b64 v[12:13], v231 offset:0\n" \
  "ds_read_b64 v[14:15], v229 offset:0\n" \
  "ds_read_b64 v[16:17], v227 offset:0\n" \
  "ds_read_b64 v[26:27], v248 offset:2448\n" \
  "ds_read_b64 v[28:29], v239 offset:2448\n" \
  "ds_read_b64 v[30:31], v237 offset:2448\n" \
  "ds_read_b64 v[32:33], v235 offset:2448\n" \
  "ds_read_b64 v[34:35], v233 offset:2448\n" \
  "ds_read_b64 v[36:37], v231 offset:2448\n" \
  "ds_read_b64 v[42:43], v229 offset:2448\n" \
  "ds_read_b64 v[44:45], v227 offset:2448\n" \
  "s_movk_i32 s32, 8192\n" \
  "buffer_load_dwordx4 v[46:49], v243, s[36:39], s32 offen offset:0 sc1\n" \
  "s_movk_i32 s32, 17408\n" \
  "buffer_load_dwordx4 v[50:53], v243, s[36:39], s32 offen offset:0 sc1\n" \
  "s_movk_i32 s32, 26624\n" \
  "buffer_load_dwordx4 v[54:57], v243, s[36:39], s32 offen offset:0 sc1\n" \
  "s_movk_i32 s32, 12288\n" \
  "buffer_load_dwordx4 v[58:61], v243, s[36:39], s32 offen offset:0 sc1\n" \
  "s_movk_i32 s32, 21504\n" \
  "buffer_load_dwordx4 v[62:65], v243, s[36:39], s32 offen offset:0 sc1\n" \
  "s_movk_i32 s32, 30720\n" \
  "buffer_load_dwordx4 v[66:69], v243, s[36:39], s32 offen offset:0 sc1\n" \
  "s_movk_i32 s32, 16384\n" \
  "buffer_load_dwordx4 v[70:73], v242, s[36:39], s32 offen offset:0 sc1\n" \
  "s_movk_i32 s32, 25600\n" \
  "buffer_load_dwordx4 v[74:77], v242, s[36:39], s32 offen offset:0 sc1\n" \
  "s_mov_b32 s32, 34816\n" \
  "buffer_load_dwordx4 v[78:81], v242, s[36:39], s32 offen offset:0 sc1\n" \
  "s_lshl_b32 s98, s2, 3\n" \
  "s_add_u32 s56, s98, 4096\n" \
  "s_lshl_b32 s98, s20, 10\n" \
  "s_add_u32 s33, s98, 62848\n" \
  "v_mov_b32 v82, s18\n" \
  "s_and_b32 s32, s19, 16777215\n" \
  "s_waitcnt vmcnt(9) lgkmcnt(0)\n" \
  "s_barrier\n" \
  "ds_read_b32 v83, v255 offset:4\n" \
  "s_cmp_eq_u32 s20, 3\n" \
  "s_waitcnt lgkmcnt(0)\n" \
  "v_lshrrev_b32 v83, 23, v83\n" \
  "v_lshl_or_b32 v83, v83, 24, s32\n" \
  "s_cselect_b64 exec, 1, 0\n" \
  "buffer_store_dwordx2 v[82:83], off, s[36:39], s56 sc1\n" \
  "s_mov_b64 exec, -1\n" \
  "s_add_u32 m0, s33, 0\n" \
  "s_movk_i32 s32, 0\n" \
  "buffer_load_dwordx4 v243, s[76:79], s32 offen offset:0 lds\n" \
  "s_add_u32 m0, s33, 30720\n" \
  "s_movk_i32 s32, 4096\n" \
  "buffer_load_dwordx4 v243, s[76:79], s32 offen offset:0 lds\n" \
  "s_add_u32 m0, s33, 61440\n" \
  "s_movk_i32 s32, 8192\n" \
  "buffer_load_dwordx4 v243, s[76:79], s32 offen offset:0 lds\n" \
  "s_movk_i32 s32, 14336\n" \
  "buffer_load_dwordx4 v[82:85], v243, s[76:79], s32 offen offset:0\n" \
  "s_movk_i32 s32, 23552\n" \
  "buffer_load_dwordx4 v[86:89], v252, s[76:79], s32 offen offset:0\n" \
  "s_mul_i32 s98, s20, 589824\n" \
  "s_mul_i32 s100, s84, 1152\n" \
  "s_add_u32 s98, s98, s100\n" \
  "s_and_b32 s100, s85, 7\n" \
  "s_lshl_b32 s100, s100, 7\n" \
  "s_add_u32 s12, s98, s100\n" \
  "s_mul_i32 s98, s20, 30720\n" \
  "s_add_u32 s57, s98, 38272\n" \
  "s_lshl_b32 s98, s20, 10\n" \
  "s_add_u32 s8, s98, s82\n" \
  "s_lshl_b32 s98, s84, 2\n" \
  "s_add_u32 s98, s98, s20\n" \
  "s_lshl_b32 s98, s98, 16\n" \
  "s_lshl_b32 s100, s85, 11\n" \
  "s_add_u32 s98, s98, s100\n" \
  "s_add_u32 s24, s98, 5308416\n" \
  "s_mul_i32 s81, s88, 294912\n" \
  "s_sub_u32 s13, s12, s81\n" \
  "s_add_u32 s12, s12, s81\n" \
  "v_lshrrev_b32 v248, 5, v0\n" \
  "v_lshlrev_b32 v239, 4, v248\n" \
  "v_lshl_add_u32 v248, v0, 3, v239\n" \
  "v_add_u32 v239, 35216, v248\n" \
  "v_add_u32 v237, 32496, v248\n" \
  "v_add_u32 v235, 37392, v248\n" \
  "v_add_u32 v233, 34672, v248\n" \
  "v_and_b32 v248, 48, v191\n" \
  "v_lshl_add_u32 v231, v246, 6, v248\n" \
  "v_cmp_gt_u32 vcc, 32, v0\n" \
  "v_mov_b32 v90, 1.0\n" \
  "s_nop 0\n" \
  "v_cndmask_b32 v237, v237, v235, vcc\n" \
  "v_cndmask_b32 v90, v90, 0, vcc\n" \
  "v_cndmask_b32 v248, v248, v231, s[58:59]\n" \
  "v_mov_b32 v95, 0\n" \
  "v_mov_b32 v99, 0\n" \
  "v_mov_b32 v103, 0\n" \
  "v_mov_b32 v107, 0\n" \
  "s_lshr_b32 s98, s4, 7\n" \
  "s_sub_u32 s98, 3, s98\n" \
  "s_sub_u32 s98, s98, s84\n" \
  "s_and_b32 s49, s98, 7\n" \
  "s_and_b32 s48, s85, 7\n" \
  "s_cmp_eq_u32 s48, s49\n" \
  "s_cselect_b32 s48, 2147483648, 0\n" \
  "v_or_b32 v108, s48, v38\n" \
  "v_or_b32 v109, s48, v39\n" \
  "v_or_b32 v110, s48, v40\n" \
  "v_or_b32 v111, s48, v41\n" \
  "s_waitcnt vmcnt(6)\n" \
  ".Lanc_%=:\n" \
  "v_xor_b32 v235, s18, v46\n" \
  "v_bitop3_b32 v235, v47, s19, v235 bitop3:0xbe\n" \
  "v_bitop3_b32 v235, v50, s18, v235 bitop3:0xbe\n" \
  "v_bitop3_b32 v235, v51, s19, v235 bitop3:0xbe\n" \
  "v_bitop3_b32 v235, v54, s18, v235 bitop3:0xbe\n" \
  "v_bitop3_b32 v235, v55, s19, v235 bitop3:0xbe\n" \
  "v_bitop3_b32 v235, v58, s18, v235 bitop3:0xbe\n" \
  "v_bitop3_b32 v235, v59, s19, v235 bitop3:0xbe\n" \
  "v_bitop3_b32 v235, v62, s18, v235 bitop3:0xbe\n" \
  "v_bitop3_b32 v235, v63, s19, v235 bitop3:0xbe\n" \
  "v_bitop3_b32 v235, v66, s18, v235 bitop3:0xbe\n" \
  "v_bitop3_b32 v235, v67, s19, v235 bitop3:0xbe\n" \
  "v_xor_b32 v229, s18, v70\n" \
  "v_bitop3_b32 v229, v71, s19, v229 bitop3:0xbe\n" \
  "v_bitop3_b32 v229, v74, s18, v229 bitop3:0xbe\n" \
  "v_bitop3_b32 v229, v75, s19, v229 bitop3:0xbe\n" \
  "v_bitop3_b32 v229, v78, s18, v229 bitop3:0xbe\n" \
  "v_bitop3_b32 v229, v79, s19, v229 bitop3:0xbe\n" \
  "v_bitop3_b32 v235, v229, s58, v235 bitop3:0xea\n" \
  "v_cmp_ne_u32 vcc, 0, v235\n" \
  "s_cbranch_vccnz .Lans_%=\n" \
  ".Land_%=:\n" \
  "v_mov_b32 v242, 0\n" \
  "v_pk_add_f32 v[112:113], v[48:49], v[52:53]\n" \
  "v_pk_add_f32 v[112:113], v[112:113], v[56:57]\n" \
  "s_add_u32 m0, s57, 0\n" \
  "s_add_u32 s31, s12, 0\n" \
  "buffer_load_dwordx4 v108, s[4:7], s31 offen offset:0 lds\n" \
  "v_cvt_pk_bf16_f32 v235, v112, v113\n" \
  "v_lshlrev_b32 v112, 16, v235\n" \
  "v_and_b32 v113, 4294901760, v235\n" \
  "ds_write_b64 v239, v[112:113] offset:0\n" \
  "v_max3_f32 v242, |v112|, |v113|, v242\n" \
  "buffer_load_dwordx4 v109, s[4:7], s31 offen offset:1024 lds\n" \
  "v_pk_add_f32 v[46:47], v[60:61], v[64:65]\n" \
  "v_pk_add_f32 v[46:47], v[46:47], v[68:69]\n" \
  "buffer_load_dwordx4 v110, s[4:7], s31 offen offset:2048 lds\n" \
  "v_cvt_pk_bf16_f32 v239, v46, v47\n" \
  "v_lshlrev_b32 v46, 16, v239\n" \
  "v_and_b32 v47, 4294901760, v239\n" \
  "v_pk_add_f32 v[46:47], v[46:47], v[90:91] op_sel_hi:[1,0]\n" \
  "v_cvt_pk_bf16_f32 v239, v46, v47\n" \
  "v_lshlrev_b32 v46, 16, v239\n" \
  "v_and_b32 v47, 4294901760, v239\n" \
  "ds_write_b64 v237, v[46:47] offset:0\n" \
  "v_max3_f32 v242, |v46|, |v47|, v242\n" \
  "buffer_load_dwordx4 v111, s[4:7], s31 offen offset:3072 lds\n" \
  "s_mov_b64 exec, s[58:59]\n" \
  "v_pk_add_f32 v[46:47], v[72:73], v[76:77]\n" \
  "v_pk_add_f32 v[46:47], v[46:47], v[80:81]\n" \
  "v_cvt_pk_bf16_f32 v239, v46, v47\n" \
  "v_lshlrev_b32 v46, 16, v239\n" \
  "v_and_b32 v47, 4294901760, v239\n" \
  "v_add_f32 v46, 1.0, v46\n" \
  "v_add_f32 v47, 1.0, v47\n" \
  "v_cvt_pk_bf16_f32 v239, v46, v47\n" \
  "v_lshlrev_b32 v46, 16, v239\n" \
  "v_and_b32 v47, 4294901760, v239\n" \
  "ds_write_b64 v233, v[46:47] offset:0\n" \
  "v_max3_f32 v242, |v46|, |v47|, v242\n" \
  "s_mov_b64 exec, -1\n" \
  "s_add_u32 m0, s57, 4096\n" \
  "s_add_u32 s31, s12, 9437184\n" \
  "buffer_load_dwordx4 v108, s[4:7], s31 offen offset:0 lds\n" \
  "v_max_u32_dpp v242, v242, v242 quad_perm:[1,0,3,2] row_mask:0xf bank_mask:0xf\n" \
  "buffer_load_dwordx4 v109, s[4:7], s31 offen offset:1024 lds\n" \
  "s_nop 0\n" \
  "v_max_u32_dpp v242, v242, v242 quad_perm:[2,3,0,1] row_mask:0xf bank_mask:0xf\n" \
  "buffer_load_dwordx4 v110, s[4:7], s31 offen offset:2048 lds\n" \
  "s_nop 0\n" \
  "v_max_u32_dpp v242, v242, v242 row_half_mirror row_mask:0xf bank_mask:0xf\n" \
  "buffer_load_dwordx4 v111, s[4:7], s31 offen offset:3072 lds\n" \
  "s_nop 0\n" \
  "v_max_u32_dpp v242, v242, v242 row_mirror row_mask:0xf bank_mask:0xf\n" \
  "s_mov_b32 exec_lo, 65537\n" \
  "s_mov_b32 exec_hi, 65537\n" \
  "ds_max_u32 v255, v242\n" \
  "s_mov_b64 exec, -1\n" \
  "s_waitcnt vmcnt(8) lgkmcnt(0)\n" \
  "s_barrier\n" \
  "ds_read_b32 v46, v255 offset:0\n" \
  "ds_read_b64 v[48:49], v249 offset:32768\n" \
  "ds_read_b64 v[50:51], v249 offset:35216\n" \
  "ds_read_b64 v[52:53], v240 offset:32768\n" \
  "ds_read_b64 v[54:55], v240 offset:35216\n" \
  "ds_read_b64 v[56:57], v238 offset:32768\n" \
  "ds_read_b64 v[58:59], v238 offset:35216\n" \
  "ds_read_b64 v[60:61], v236 offset:32768\n" \
  "ds_read_b64 v[62:63], v236 offset:35216\n" \
  "ds_read_b64 v[64:65], v234 offset:32768\n" \
  "ds_read_b64 v[66:67], v234 offset:35216\n" \
  "ds_read_b64 v[68:69], v232 offset:32768\n" \
  "ds_read_b64 v[70:71], v232 offset:35216\n" \
  "ds_read_b64 v[72:73], v230 offset:32768\n" \
  "ds_read_b64 v[74:75], v230 offset:35216\n" \
  "ds_read_b64 v[76:77], v228 offset:32768\n" \
  "ds_read_b64 v[78:79], v228 offset:35216\n" \
  "v_add_u32 v255, 62848, v252\n" \
  "ds_read_b128 a[200:203], v255 offset:0\n" \
  "ds_read_b128 a[204:207], v255 offset:1024\n" \
  "ds_read_b128 a[168:171], v255 offset:2048\n" \
  "ds_read_b128 a[172:175], v255 offset:3072\n" \
  "s_waitcnt lgkmcnt(15)\n" \
  "v_lshrrev_b32 v46, 23, v46\n" \
  "v_max_u32 v46, 64, v46\n" \
  "s_nop 0\n" \
  "v_readfirstlane_b32 s26, v46\n" \
  "v_sub_u32 v46, 256, v46\n" \
  "v_lshlrev_b32 v46, 23, v46\n" \
  "v_pk_mul_f32 v[48:49], v[48:49], v[46:47] op_sel_hi:[1,0]\n" \
  "v_pk_mul_f32 v[50:51], v[50:51], v[46:47] op_sel_hi:[1,0]\n" \
  "v_pk_fma_f32 v[80:81], v[2:3], v[48:49], v[50:51]\n" \
  "v_pk_fma_f32 v[2:3], v[26:27], v[48:49], v[50:51]\n" \
  "v_pk_mul_f32 v[52:53], v[52:53], v[46:47] op_sel_hi:[1,0]\n" \
  "v_pk_mul_f32 v[54:55], v[54:55], v[46:47] op_sel_hi:[1,0]\n" \
  "v_pk_fma_f32 v[26:27], v[4:5], v[52:53], v[54:55]\n" \
  "v_pk_fma_f32 v[4:5], v[28:29], v[52:53], v[54:55]\n" \
  "v_pk_mul_f32 v[56:57], v[56:57], v[46:47] op_sel_hi:[1,0]\n" \
  "s_waitcnt lgkmcnt(14)\n" \
  "v_pk_mul_f32 v[58:59], v[58:59], v[46:47] op_sel_hi:[1,0]\n" \
  "v_pk_fma_f32 v[28:29], v[6:7], v[56:57], v[58:59]\n" \
  "v_pk_fma_f32 v[6:7], v[30:31], v[56:57], v[58:59]\n" \
  "s_waitcnt lgkmcnt(13)\n" \
  "v_pk_mul_f32 v[60:61], v[60:61], v[46:47] op_sel_hi:[1,0]\n" \
  "s_waitcnt lgkmcnt(12)\n" \
  "v_pk_mul_f32 v[62:63], v[62:63], v[46:47] op_sel_hi:[1,0]\n" \
  "v_pk_fma_f32 v[30:31], v[8:9], v[60:61], v[62:63]\n" \
  "v_pk_fma_f32 v[8:9], v[32:33], v[60:61], v[62:63]\n" \
  "s_waitcnt lgkmcnt(11)\n" \
  "v_pk_mul_f32 v[64:65], v[64:65], v[46:47] op_sel_hi:[1,0]\n" \
  "s_waitcnt lgkmcnt(10)\n" \
  "v_pk_mul_f32 v[66:67], v[66:67], v[46:47] op_sel_hi:[1,0]\n" \
  "v_pk_fma_f32 v[32:33], v[10:11], v[64:65], v[66:67]\n" \
  "v_pk_fma_f32 v[10:11], v[34:35], v[64:65], v[66:67]\n" \
  "s_waitcnt lgkmcnt(9)\n" \
  "v_pk_mul_f32 v[68:69], v[68:69], v[46:47] op_sel_hi:[1,0]\n" \
  "s_waitcnt lgkmcnt(8)\n" \
  "v_pk_mul_f32 v[70:71], v[70:71], v[46:47] op_sel_hi:[1,0]\n" \
  "v_pk_fma_f32 v[34:35], v[12:13], v[68:69], v[70:71]\n" \
  "v_pk_fma_f32 v[12:13], v[36:37], v[68:69], v[70:71]\n" \
  "s_waitcnt lgkmcnt(7)\n" \
  "v_pk_mul_f32 v[72:73], v[72:73], v[46:47] op_sel_hi:[1,0]\n" \
  "s_waitcnt lgkmcnt(6)\n" \
  "v_pk_mul_f32 v[74:75], v[74:75], v[46:47] op_sel_hi:[1,0]\n" \
  "v_pk_fma_f32 v[36:37], v[14:15], v[72:73], v[74:75]\n" \
  "v_pk_fma_f32 v[14:15], v[42:43], v[72:73], v[74:75]\n" \
  "s_waitcnt lgkmcnt(5)\n" \
  "v_pk_mul_f32 v[76:77], v[76:77], v[46:47] op_sel_hi:[1,0]\n" \
  "s_waitcnt lgkmcnt(4)\n" \
  "v_pk_mul_f32 v[78:79], v[78:79], v[46:47] op_sel_hi:[1,0]\n" \
  "v_pk_fma_f32 v[42:43], v[16:17], v[76:77], v[78:79]\n" \
  "v_pk_fma_f32 v[16:17], v[44:45], v[76:77], v[78:79]\n" \
  "v_cvt_pk_f16_f32 v44, v80, v81\n" \
  "v_cvt_pk_f16_f32 v45, v26, v27\n" \
  "v_cvt_pk_f16_f32 v46, v28, v29\n" \
  "v_cvt_pk_f16_f32 v47, v30, v31\n" \
  "v_cvt_pk_f16_f32 v26, v32, v33\n" \
  "v_cvt_pk_f16_f32 v27, v34, v35\n" \
  "s_waitcnt lgkmcnt(3)\n" \
  "v_mfma_f32_16x16x32_f16 v[30:33], v[44:47], a[200:203], 0\n" \
  "v_cvt_pk_f16_f32 v28, v36, v37\n" \
  "v_cvt_pk_f16_f32 v29, v42, v43\n" \
  "s_waitcnt lgkmcnt(2)\n" \
  "v_mfma_f32_16x16x32_f16 v[34:37], v[44:47], a[204:207], 0\n" \
  "v_cvt_pk_f16_f32 v42, v2, v3\n" \
  "v_cvt_pk_f16_f32 v43, v4, v5\n" \
  "s_waitcnt lgkmcnt(1)\n" \
  "v_mfma_f32_16x16x32_f16 v[2:5], v[26:29], a[168:171], 0\n" \
  "v_cvt_pk_f16_f32 v44, v6, v7\n" \
  "v_cvt_pk_f16_f32 v45, v8, v9\n" \
  "s_waitcnt lgkmcnt(0)\n" \
  "v_mfma_f32_16x16x32_f16 v[6:9], v[26:29], a[172:175], 0\n" \
  "v_cvt_pk_f16_f32 v26, v10, v11\n" \
  "v_cvt_pk_f16_f32 v27, v12, v13\n" \
  "v_mfma_f32_16x16x32_f16 v[10:13], v[42:45], a[200:203], 0\n" \
  "v_cvt_pk_f16_f32 v28, v14, v15\n" \
  "v_cvt_pk_f16_f32 v29, v16, v17\n" \
  "v_mfma_f32_16x16x32_f16 v[14:17], v[42:45], a[204:207], 0\n" \
  "ds_read_b128 a[208:211], v255 offset:30720\n" \
  "v_mfma_f32_16x16x32_f16 v[42:45], v[26:29], a[168:171], 0\n" \
  "ds_read_b128 a[212:215], v255 offset:31744\n" \
  "v_mfma_f32_16x16x32_f16 v[46:49], v[26:29], a[172:175], 0\n" \
  "ds_read_b128 a[220:223], v255 offset:32768\n" \
  "ds_read_b128 a[224:227], v255 offset:33792\n" \
  "v_pk_add_f32 v[26:27], v[30:31], v[2:3]\n" \
  "v_pk_add_f32 v[28:29], v[32:33], v[4:5]\n" \
  "v_pk_add_f32 v[50:51], v[34:35], v[6:7]\n" \
  "v_pk_add_f32 v[52:53], v[36:37], v[8:9]\n" \
  "v_pk_add_f32 v[54:55], v[30:31], v[2:3] neg_lo:[0,1] neg_hi:[0,1]\n" \
  "v_pk_add_f32 v[56:57], v[32:33], v[4:5] neg_lo:[0,1] neg_hi:[0,1]\n" \
  "v_pk_add_f32 v[2:3], v[34:35], v[6:7] neg_lo:[0,1] neg_hi:[0,1]\n" \
  "v_pk_add_f32 v[4:5], v[36:37], v[8:9] neg_lo:[0,1] neg_hi:[0,1]\n" \
  "v_pk_add_f32 v[6:7], v[10:11], v[42:43]\n" \
  "v_pk_add_f32 v[8:9], v[12:13], v[44:45]\n" \
  "v_pk_add_f32 v[30:31], v[14:15], v[46:47]\n" \
  "v_pk_add_f32 v[32:33], v[16:17], v[48:49]\n" \
  "v_pk_add_f32 v[34:35], v[10:11], v[42:43] neg_lo:[0,1] neg_hi:[0,1]\n" \
  "v_pk_add_f32 v[36:37], v[12:13], v[44:45] neg_lo:[0,1] neg_hi:[0,1]\n" \
  "v_pk_add_f32 v[10:11], v[14:15], v[46:47] neg_lo:[0,1] neg_hi:[0,1]\n" \
  "v_pk_add_f32 v[12:13], v[16:17], v[48:49] neg_lo:[0,1] neg_hi:[0,1]\n" \
  "v_cvt_pk_f16_f32 v92, v26, v50\n" \
  "v_cvt_pk_f16_f32 v93, v27, v51\n" \
  "v_cvt_pk_f16_f32 v94, v28, v52\n" \
  "v_cvt_pk_f16_f32 v96, v54, v2\n" \
  "v_cvt_pk_f16_f32 v97, v55, v3\n" \
  "s_waitcnt lgkmcnt(3)\n" \
  "v_mfma_f32_16x16x32_f16 v[14:17], a[208:211], v[92:95], 0\n" \
  "v_cvt_pk_f16_f32 v98, v56, v4\n" \
  "s_waitcnt lgkmcnt(2)\n" \
  "v_mfma_f32_16x16x32_f16 v[2:5], a[212:215], v[92:95], 0\n" \
  "v_cvt_pk_f16_f32 v100, v6, v30\n" \
  "v_cvt_pk_f16_f32 v101, v7, v31\n" \
  "v_mfma_f32_16x16x32_f16 v[26:29], a[208:211], v[96:99], 0\n" \
  "v_cvt_pk_f16_f32 v102, v8, v32\n" \
  "v_mfma_f32_16x16x32_f16 v[6:9], a[212:215], v[96:99], 0\n" \
  "v_cvt_pk_f16_f32 v104, v34, v10\n" \
  "v_cvt_pk_f16_f32 v105, v35, v11\n" \
  "v_mfma_f32_16x16x32_f16 v[30:33], a[208:211], v[100:103], 0\n" \
  "v_cvt_pk_f16_f32 v106, v36, v12\n" \
  "v_mfma_f32_16x16x32_f16 v[10:13], a[212:215], v[100:103], 0\n" \
  "s_nop 0\n" \
  "v_mfma_f32_16x16x32_f16 v[34:37], a[208:211], v[104:107], 0\n" \
  "v_mfma_f32_16x16x32_f16 v[42:45], a[212:215], v[104:107], 0\n" \
  "s_mov_b32 exec_hi, 65535\n" \
  "v_cvt_pk_f16_f32 v46, v14, v2\n" \
  "s_nop 2\n" \
  "v_cvt_pk_f16_f32 v47, v30, v10\n" \
  "ds_write_b64 v82, v[46:47] offset:0\n" \
  "v_cvt_pk_f16_f32 v46, v15, v3\n" \
  "v_cvt_pk_f16_f32 v47, v31, v11\n" \
  "ds_write_b64 v82, v[46:47] offset:512\n" \
  "v_cvt_pk_f16_f32 v46, v16, v4\n" \
  "v_cvt_pk_f16_f32 v47, v32, v12\n" \
  "ds_write_b64 v82, v[46:47] offset:1024\n" \
  "v_cvt_pk_f16_f32 v2, v26, v6\n" \
  "v_cvt_pk_f16_f32 v3, v34, v42\n" \
  "ds_write_b64 v82, v[2:3] offset:256\n" \
  "v_cvt_pk_f16_f32 v2, v27, v7\n" \
  "v_cvt_pk_f16_f32 v3, v35, v43\n" \
  "ds_write_b64 v82, v[2:3] offset:768\n" \
  "v_cvt_pk_f16_f32 v2, v28, v8\n" \
  "v_cvt_pk_f16_f32 v3, v36, v44\n" \
  "ds_write_b64 v82, v[2:3] offset:1280\n" \
  "s_mov_b64 exec, -1\n" \
  "s_waitcnt lgkmcnt(0)\n" \
  "s_barrier\n" \
  "ds_read_b128 v[2:5], v83 offset:0\n" \
  "ds_read_b128 v[6:9], v84 offset:0\n" \
  "ds_read_b128 v[10:13], v85 offset:0\n" \
  "v_lshrrev_b32 v255, 1, v191\n" \
  "v_and_b32 v249, 1, v191\n" \
  "s_lshl_b32 s98, s20, 1\n" \
  "v_add_u32 v242, s98, v249\n" \
  "v_and_b32 v249, 6, v255\n" \
  "v_xor_b32 v240, v242, v249\n" \
  "v_lshlrev_b32 v249, 4, v240\n" \
  "v_lshl_add_u32 v242, v255, 7, v249\n" \
  "v_add_u32 v255, 155008, v242\n" \
  "v_lshlrev_b32 v249, 15, v253\n" \
  "v_sub_u32 v242, v255, v249\n" \
  "s_cmp_eq_u32 s20, 1\n" \
  "s_cselect_b32 s92, 65537, s58\n" \
  "s_cselect_b32 s93, 65537, s59\n" \
  "s_add_u32 s32, s8, 4096\n" \
  "s_add_u32 s33, s8, 8192\n" \
  "s_waitcnt lgkmcnt(2)\n" \
  "v_mfma_f32_16x16x32_f16 v[14:17], a[160:163], v[2:5], 0\n" \
  "v_mfma_f32_16x16x32_f16 v[26:29], a[164:167], v[2:5], 0\n" \
  "s_waitcnt lgkmcnt(1)\n" \
  "v_mfma_f32_16x16x32_f16 v[2:5], a[160:163], v[6:9], 0\n" \
  "v_mfma_f32_16x16x32_f16 v[30:33], a[164:167], v[6:9], 0\n" \
  "s_waitcnt lgkmcnt(0)\n" \
  "v_mfma_f32_16x16x32_f16 v[6:9], a[160:163], v[10:13], 0\n" \
  "v_mfma_f32_16x16x32_f16 v[34:37], a[164:167], v[10:13], 0\n" \
  "ds_read_b128 v[10:13], v255 offset:0\n" \
  "ds_read_b128 v[42:45], v242 offset:4096\n" \
  "v_cvt_pk_f16_f32 v46, v14, v26\n" \
  "v_cvt_pk_f16_f32 v47, v15, v27\n" \
  "v_cvt_pk_f16_f32 v48, v16, v28\n" \
  "v_cvt_pk_f16_f32 v49, v17, v29\n" \
  "v_and_b32 v46, -2, v46\n" \
  "buffer_store_dwordx4 v[46:49], v231, s[36:39], s8 offen offset:0 sc1\n" \
  "v_cvt_pk_f16_f32 v14, v2, v30\n" \
  "v_cvt_pk_f16_f32 v15, v3, v31\n" \
  "v_cvt_pk_f16_f32 v16, v4, v32\n" \
  "v_cvt_pk_f16_f32 v17, v5, v33\n" \
  "v_and_b32 v14, -2, v14\n" \
  "buffer_store_dwordx4 v[14:17], v231, s[36:39], s32 offen offset:0 sc1\n" \
  "v_cvt_pk_f16_f32 v2, v6, v34\n" \
  "v_cvt_pk_f16_f32 v3, v7, v35\n" \
  "v_cvt_pk_f16_f32 v4, v8, v36\n" \
  "v_cvt_pk_f16_f32 v5, v9, v37\n" \
  "v_and_b32 v2, -2, v2\n" \
  "s_mov_b64 exec, s[92:93]\n" \
  "buffer_store_dwordx4 v[2:5], v231, s[36:39], s33 offen offset:0 sc1\n" \
  "s_mov_b64 exec, -1\n" \
  "s_mov_b32 s56, s48\n" \
  "s_waitcnt lgkmcnt(1)\n" \
  "buffer_store_dwordx4 v[10:13], v252, s[36:39], s24 offen offset:0 sc1\n" \
  "s_waitcnt lgkmcnt(0)\n" \
  "buffer_store_dwordx4 v[42:45], v252, s[36:39], s24 offen offset:1024 sc1\n" \
  "s_movk_i32 s32, 4096\n" \
  "buffer_load_dwordx4 v[2:5], v252, s[36:39], s32 offen offset:0 sc1\n" \
  "buffer_load_dwordx4 v[6:9], v252, s[36:39], s32 offen offset:1024 sc1\n" \
  "s_add_u32 s10, s87, 7405568\n" \
  "s_lshl_b32 s98, s89, 16\n" \
  "s_lshl_b32 s100, s20, 14\n" \
  "s_add_u32 s98, s98, s100\n" \
  "s_add_u32 s14, s98, 5308416\n" \
  "s_mul_i32 s98, s88, 4505600\n" \
  "s_lshl_b32 s100, s90, 2\n" \
  "s_lshr_b32 s101, s84, 1\n" \
  "s_add_u32 s100, s100, s101\n" \
  "s_lshl_b32 s100, s100, 12\n" \
  "s_add_u32 s98, s98, s100\n" \
  "s_add_u32 s16, s98, 13090816\n" \
  "s_mul_i32 s98, s20, 4896\n" \
  "s_add_u32 s21, s98, 9216\n" \
  "s_mul_i32 s27, s88, 9232\n" \
  "s_lshl_b32 s98, s20, 2\n" \
  "s_add_u32 s28, s98, s28\n" \
  "s_lshl_b32 s98, s84, 8\n" \
  "s_add_u32 s29, s98, 2048\n" \
  "s_lshl_b32 s98, s85, 3\n" \
  "s_add_u32 s30, s98, s29\n" \
  "s_cmp_lt_u32 s20, 2\n" \
  "s_cselect_b32 s9, 8192, 0\n" \
  "s_add_u32 s9, s9, s8\n" \
  "s_lshl_b32 s81, s88, 13\n" \
  "s_sub_u32 s15, s14, s81\n" \
  "s_add_u32 s14, s14, s81\n" \
  "s_mul_i32 s98, s84, 9\n" \
  "s_add_u32 s81, s98, s85\n" \
  "s_lshl_b32 s98, s81, 7\n" \
  "s_add_u32 s25, s98, 35840\n" \
  "s_cmp_lt_u32 s2, 72\n" \
  "s_cselect_b32 s66, 3981312, 0\n" \
  "s_lshl_b32 s33, s20, 10\n" \
  "s_lshl_b32 s98, s81, 6\n" \
  "s_add_u32 s63, s98, 2304\n" \
  "s_barrier\n" \
  "s_add_u32 m0, s33, 0\n" \
  "s_add_u32 s32, s63, 0\n" \
  "buffer_load_dwordx4 v245, s[64:67], s32 offen offset:0 lds\n" \
  "s_add_u32 m0, s33, 4096\n" \
  "s_add_u32 s32, s63, 442368\n" \
  "buffer_load_dwordx4 v245, s[64:67], s32 offen offset:0 lds\n" \
  "s_add_u32 m0, s33, 8192\n" \
  "s_add_u32 s32, s63, 884736\n" \
  "buffer_load_dwordx4 v245, s[64:67], s32 offen offset:0 lds\n" \
  "s_add_u32 m0, s33, 12288\n" \
  "s_add_u32 s32, s63, 1327104\n" \
  "buffer_load_dwordx4 v245, s[64:67], s32 offen offset:0 lds\n" \
  "s_add_u32 m0, s33, 16384\n" \
  "s_add_u32 s32, s63, 1769472\n" \
  "buffer_load_dwordx4 v245, s[64:67], s32 offen offset:0 lds\n" \
  "s_add_u32 m0, s33, 20480\n" \
  "s_add_u32 s32, s63, 2211840\n" \
  "buffer_load_dwordx4 v245, s[64:67], s32 offen offset:0 lds\n" \
  "s_add_u32 m0, s33, 24576\n" \
  "s_add_u32 s32, s63, 2654208\n" \
  "buffer_load_dwordx4 v245, s[64:67], s32 offen offset:0 lds\n" \
  "s_add_u32 m0, s33, 28672\n" \
  "s_add_u32 s32, s63, 3096576\n" \
  "buffer_load_dwordx4 v245, s[64:67], s32 offen offset:0 lds\n" \
  "s_add_u32 m0, s33, 32768\n" \
  "s_add_u32 s32, s63, 3538944\n" \
  "buffer_load_dwordx4 v245, s[64:67], s32 offen offset:0 lds\n" \
  "v_and_b32 v255, 1, v191\n" \
  "v_lshl_add_u32 v249, v253, 1, v255\n" \
  "v_lshlrev_b32 v255, 3, v249\n" \
  "v_and_b32 v249, 7, v191\n" \
  "v_lshlrev_b32 v241, 1, v249\n" \
  "v_and_b32 v249, 12, v191\n" \
  "v_add_u32 v245, v249, v244\n" \
  "s_lshl_b32 s98, s20, 4\n" \
  "v_add_u32 v249, s98, v245\n" \
  "v_lshlrev_b32 v245, 1, v249\n" \
  "v_bfe_u32 v249, v191, 1, 1\n" \
  "v_lshrrev_b32 v242, 2, v191\n" \
  "v_and_b32 v240, 12, v242\n" \
  "v_lshl_add_u32 v242, v249, 6, v240\n" \
  "s_lshr_b32 s98, s84, 1\n" \
  "s_lshl_b32 s98, s98, 4\n" \
  "s_add_u32 s98, s98, s20\n" \
  "v_add_u32 v249, s98, v242\n" \
  "v_lshlrev_b32 v242, 1, v249\n" \
  "s_mul_i32 s98, s84, 1152\n" \
  "s_and_b32 s100, s85, 7\n" \
  "s_lshl_b32 s100, s100, 7\n" \
  "s_add_u32 s98, s98, s100\n" \
  "s_lshl_b32 s100, s20, 5\n" \
  "s_add_u32 s60, s98, s100\n" \
  "s_mul_i32 s98, s84, 1152\n" \
  "s_and_b32 s100, s85, 6\n" \
  "s_lshl_b32 s100, s100, 4\n" \
  "s_add_u32 s98, s98, s100\n" \
  "s_lshr_b32 s100, s20, 1\n" \
  "s_lshl_b32 s100, s100, 4\n" \
  "s_add_u32 s98, s98, s100\n" \
  "s_add_u32 s61, s98, 1024\n" \
  "s_lshl_b32 s62, s90, 8\n" \
  "buffer_load_dwordx2 v[16:17], v255, s[68:71], s60 offen offset:0\n" \
  "buffer_load_ushort v255, v241, s[68:71], s61 offen offset:0\n" \
  "buffer_load_ushort v249, v245, s[72:75], s62 offen offset:0\n" \
  "buffer_load_ushort v241, v245, s[72:75], s62 offen offset:128\n" \
  "buffer_load_ushort v245, v242, s[72:75], s62 offen offset:0\n" \
  "s_add_u32 s98, s84, 1\n" \
  "s_lshr_b32 s98, s98, 3\n" \
  "s_sub_u32 s48, 14, s98\n" \
  "s_lshr_b32 s98, s4, 7\n" \
  "s_sub_u32 s98, 3, s98\n" \
  "s_sub_u32 s98, s98, s84\n" \
  "s_and_b32 s49, s98, 7\n" \
  "v_lshrrev_b32 v242, 5, v191\n" \
  "v_and_b32 v240, 31, v191\n" \
  "v_lshl_add_u32 v239, v242, 6, v240\n" \
  "v_mul_u32_u24 v242, 9216, v239\n" \
  "s_mul_i32 s98, s20, s48\n" \
  "s_add_u32 s50, s98, s85\n" \
  "s_mul_i32 s98, s50, 205\n" \
  "s_lshr_b32 s51, s98, 10\n" \
  "s_mul_i32 s98, s51, 5\n" \
  "s_sub_u32 s52, s50, s98\n" \
  "s_lshr_b32 s53, s50, 2\n" \
  "s_and_b32 s54, s50, 3\n" \
  "s_cmp_eq_u32 s84, 7\n" \
  "s_cselect_b32 s51, s51, s53\n" \
  "s_cselect_b32 s52, s52, s54\n" \
  "s_sub_u32 s53, 9, s52\n" \
  "s_cmp_lt_u32 s52, 2\n" \
  "s_cselect_b32 s53, s49, s53\n" \
  "s_cmp_eq_u32 s52, 0\n" \
  "s_cselect_b32 s54, 0, 294912\n" \
  "s_cmp_lt_u32 s51, 16\n" \
  "s_cselect_b64 s[68:69], -1, 0\n" \
  "s_mul_i32 s98, s51, 1179648\n" \
  "s_lshl_b32 s100, s53, 7\n" \
  "s_add_u32 s98, s98, s100\n" \
  "s_mul_i32 s100, s84, 1152\n" \
  "s_add_u32 s98, s98, s100\n" \
  "s_add_u32 s72, s98, s54\n" \
  "s_add_u32 s98, s20, 4\n" \
  "s_mul_i32 s98, s98, s48\n" \
  "s_add_u32 s50, s98, s85\n" \
  "s_mul_i32 s98, s50, 205\n" \
  "s_lshr_b32 s51, s98, 10\n" \
  "s_mul_i32 s98, s51, 5\n" \
  "s_sub_u32 s52, s50, s98\n" \
  "s_lshr_b32 s53, s50, 2\n" \
  "s_and_b32 s54, s50, 3\n" \
  "s_cmp_eq_u32 s84, 7\n" \
  "s_cselect_b32 s51, s51, s53\n" \
  "s_cselect_b32 s52, s52, s54\n" \
  "s_sub_u32 s53, 9, s52\n" \
  "s_cmp_lt_u32 s52, 2\n" \
  "s_cselect_b32 s53, s49, s53\n" \
  "s_cmp_eq_u32 s52, 0\n" \
  "s_cselect_b32 s54, 0, 294912\n" \
  "s_cmp_lt_u32 s51, 16\n" \
  "s_cselect_b64 s[70:71], -1, 0\n" \
  "s_mul_i32 s98, s51, 1179648\n" \
  "s_lshl_b32 s100, s53, 7\n" \
  "s_add_u32 s98, s98, s100\n" \
  "s_mul_i32 s100, s84, 1152\n" \
  "s_add_u32 s98, s98, s100\n" \
  "s_add_u32 s73, s98, s54\n" \
  "s_mov_b32 m0, 162176\n" \
  "s_mov_b64 exec, s[68:69]\n" \
  "buffer_load_dword v242, s[4:7], s72 offen lds\n" \
  "s_mov_b64 exec, s[70:71]\n" \
  "buffer_load_dword v242, s[4:7], s73 offen lds\n" \
  "s_mov_b64 exec, -1\n" \
  "s_cmp_lg_u32 s56, 0\n" \
  "s_cbranch_scc1 .Lc3s_%=\n" \
  ".Lc3d_%=:\n" \
  "s_add_u32 m0, s57, 8192\n" \
  "s_add_u32 s31, s12, 73728\n" \
  "buffer_load_dwordx4 v38, s[4:7], s31 offen offset:0 lds\n" \
  "buffer_load_dwordx4 v39, s[4:7], s31 offen offset:1024 lds\n" \
  "buffer_load_dwordx4 v40, s[4:7], s31 offen offset:2048 lds\n" \
  "buffer_load_dwordx4 v41, s[4:7], s31 offen offset:3072 lds\n" \
  "v_and_b32 v242, 3, v191\n" \
  "v_bfe_u32 v240, v191, 2, 2\n" \
  "v_lshl_add_u32 v239, v244, 3, v240\n" \
  "s_lshr_b32 s98, s20, 1\n" \
  "s_mul_i32 s98, s98, 18432\n" \
  "s_and_b32 s100, s20, 1\n" \
  "s_lshl_b32 s100, s100, 5\n" \
  "s_add_u32 s98, s98, s100\n" \
  "v_lshl_add_u32 v240, v239, 6, s98\n" \
  "v_lshl_add_u32 v239, v242, 3, v240\n" \
  "s_lshr_b32 s98, s20, 1\n" \
  "s_mul_i32 s98, s98, 576\n" \
  "s_add_u32 s98, s98, 162432\n" \
  "v_lshl_add_u32 v242, v244, 4, s98\n" \
  "s_lshl_b32 s98, s20, 6\n" \
  "s_add_u32 s98, s98, 161152\n" \
  "v_lshl_add_u32 v244, v246, 2, s98\n" \
  "v_and_b32 v246, 7, v191\n" \
  "v_lshlrev_b32 v240, 4, v246\n" \
  "v_add_u32 v246, 161152, v240\n" \
  "v_lshlrev_b32 v240, 5, v191\n" \
  "v_lshlrev_b32 v238, 2, v191\n" \
  "v_and_b32 v240, 1920, v240\n" \
  "v_and_b32 v238, 96, v238\n" \
  "v_add3_u32 v240, v240, v238, s57\n" \
  "v_and_b32 v238, 3, v191\n" \
  "v_lshl_add_u32 v26, v238, 3, v240\n" \
  "v_xor_b32 v27, 32, v26\n" \
  "v_xor_b32 v28, 64, v26\n" \
  "v_xor_b32 v29, 96, v26\n" \
  "ds_read_b128 v[10:13], v242 offset:0\n" \
  "ds_read_b128 v[30:33], v242 offset:64\n" \
  "ds_read_b128 v[34:37], v242 offset:128\n" \
  "ds_read_b128 v[42:45], v242 offset:192\n" \
  "ds_read_b128 v[46:49], v242 offset:256\n" \
  "ds_read_b128 v[50:53], v242 offset:320\n" \
  "ds_read_b128 v[54:57], v242 offset:384\n" \
  "ds_read_b128 v[58:61], v242 offset:448\n" \
  "ds_read_b128 v[62:65], v242 offset:512\n" \
  "s_waitcnt vmcnt(11)\n" \
  "s_barrier\n" \
  "ds_read_b64_tr_b16 v[66:67], v239 offset:0\n" \
  "ds_read_b64_tr_b16 v[68:69], v239 offset:256\n" \
  "ds_read_b64_tr_b16 v[70:71], v239 offset:2048\n" \
  "ds_read_b64_tr_b16 v[72:73], v239 offset:2304\n" \
  "ds_read_b64_tr_b16 v[74:75], v239 offset:4096\n" \
  "ds_read_b64_tr_b16 v[76:77], v239 offset:4352\n" \
  "ds_read_b64_tr_b16 v[78:79], v239 offset:6144\n" \
  "ds_read_b64_tr_b16 v[80:81], v239 offset:6400\n" \
  "ds_read_b64_tr_b16 v[108:109], v239 offset:8192\n" \
  "ds_read_b64_tr_b16 v[110:111], v239 offset:8448\n" \
  "ds_read_b64_tr_b16 v[112:113], v239 offset:10240\n" \
  "ds_read_b64_tr_b16 v[114:115], v239 offset:10496\n" \
  "ds_read_b64_tr_b16 v[116:117], v239 offset:12288\n" \
  "ds_read_b64_tr_b16 v[118:119], v239 offset:12544\n" \
  "ds_read_b64_tr_b16 v[120:121], v239 offset:14336\n" \
  "ds_read_b64_tr_b16 v[122:123], v239 offset:14592\n" \
  "ds_read_b64_tr_b16 v[124:125], v239 offset:16384\n" \
  "ds_read_b64_tr_b16 v[126:127], v239 offset:16640\n" \
  "s_waitcnt lgkmcnt(15)\n" \
  "v_mfma_f32_16x16x32_bf16 v[128:131], v[10:13], v[66:69], 0\n" \
  "s_waitcnt lgkmcnt(14)\n" \
  "v_mfma_f32_16x16x32_bf16 v[128:131], v[30:33], v[70:73], v[128:131]\n" \
  "s_waitcnt lgkmcnt(12)\n" \
  "v_mfma_f32_16x16x32_bf16 v[128:131], v[34:37], v[74:77], v[128:131]\n" \
  "s_waitcnt lgkmcnt(10)\n" \
  "v_mfma_f32_16x16x32_bf16 v[128:131], v[42:45], v[78:81], v[128:131]\n" \
  "s_waitcnt lgkmcnt(8)\n" \
  "v_mfma_f32_16x16x32_bf16 v[128:131], v[46:49], v[108:111], v[128:131]\n" \
  "s_waitcnt lgkmcnt(6)\n" \
  "v_mfma_f32_16x16x32_bf16 v[128:131], v[50:53], v[112:115], v[128:131]\n" \
  "s_waitcnt lgkmcnt(4)\n" \
  "v_mfma_f32_16x16x32_bf16 v[128:131], v[54:57], v[116:119], v[128:131]\n" \
  "s_waitcnt lgkmcnt(2)\n" \
  "v_mfma_f32_16x16x32_bf16 v[128:131], v[58:61], v[120:123], v[128:131]\n" \
  "s_waitcnt lgkmcnt(0)\n" \
  "v_mfma_f32_16x16x32_bf16 v[128:131], v[62:65], v[124:127], v[128:131]\n" \
  "v_mov_b32 v10, s18\n" \
  "v_mov_b32 v11, s19\n" \
  "s_lshr_b32 s32, s84, 1\n" \
  "s_cmp_eq_u32 s32, 2\n" \
  "s_cselect_b32 s32, 1065353216, 0\n" \
  "s_cmp_lt_u32 s2, 72\n" \
  "s_cselect_b64 s[96:97], 255, 0\n" \
  "s_and_b64 s[96:97], s[96:97], s[58:59]\n" \
  "ds_write_b32 v244, v128 offset:0\n" \
  "s_waitcnt lgkmcnt(0)\n" \
  "s_barrier\n" \
  "ds_read_b128 v[30:33], v246 offset:0\n" \
  "ds_read_b128 v[34:37], v246 offset:128\n" \
  "s_waitcnt lgkmcnt(0)\n" \
  "v_pk_add_f32 v[30:31], v[30:31], v[34:35]\n" \
  "v_pk_add_f32 v[32:33], v[32:33], v[36:37]\n" \
  "v_cvt_pk_bf16_f32 v12, v30, v31\n" \
  "v_cvt_pk_bf16_f32 v13, v32, v33\n" \
  "v_lshlrev_b32 v30, 16, v12\n" \
  "v_and_b32 v31, 4294901760, v12\n" \
  "v_lshlrev_b32 v32, 16, v13\n" \
  "v_and_b32 v33, 4294901760, v13\n" \
  "v_add_f32 v30, s32, v30\n" \
  "v_add_f32 v31, s32, v31\n" \
  "v_add_f32 v32, s32, v32\n" \
  "v_add_f32 v33, s32, v33\n" \
  "v_cvt_pk_bf16_f32 v12, v30, v31\n" \
  "v_cvt_pk_bf16_f32 v13, v32, v33\n" \
  "s_mov_b64 exec, s[96:97]\n" \
  "buffer_store_dwordx4 v[10:13], v252, s[36:39], s25 offen offset:0 sc1\n" \
  "s_cmp_eq_u32 s20, 3\n" \
  "s_cselect_b64 exec, 1, 0\n" \
  "buffer_store_dwordx2 v[10:11], off, s[36:39], s30 sc1\n" \
  "s_mov_b64 exec, -1\n" \
  "s_add_u32 m0, s57, 12288\n" \
  "s_add_u32 s31, s12, 9510912\n" \
  "buffer_load_dwordx4 v38, s[4:7], s31 offen offset:0 lds\n" \
  "buffer_load_dwordx4 v39, s[4:7], s31 offen offset:1024 lds\n" \
  "buffer_load_dwordx4 v40, s[4:7], s31 offen offset:2048 lds\n" \
  "buffer_load_dwordx4 v41, s[4:7], s31 offen offset:3072 lds\n" \
  "s_add_u32 m0, s57, 16384\n" \
  "s_add_u32 s31, s12, 147456\n" \
  "buffer_load_dwordx4 v38, s[4:7], s31 offen offset:0 lds\n" \
  "buffer_load_dwordx4 v39, s[4:7], s31 offen offset:1024 lds\n" \
  "buffer_load_dwordx4 v40, s[4:7], s31 offen offset:2048 lds\n" \
  "buffer_load_dwordx4 v41, s[4:7], s31 offen offset:3072 lds\n" \
  "s_add_u32 m0, s57, 20480\n" \
  "s_add_u32 s31, s12, 9584640\n" \
  "buffer_load_dwordx4 v38, s[4:7], s31 offen offset:0 lds\n" \
  "buffer_load_dwordx4 v39, s[4:7], s31 offen offset:1024 lds\n" \
  "buffer_load_dwordx4 v40, s[4:7], s31 offen offset:2048 lds\n" \
  "buffer_load_dwordx4 v41, s[4:7], s31 offen offset:3072 lds\n" \
  "s_waitcnt vmcnt(8)\n" \
  "ds_read_b64_tr_b16 a[0:1], v26 offset:0\n" \
  "ds_read_b64_tr_b16 a[4:5], v27 offset:0\n" \
  "ds_read_b64_tr_b16 a[8:9], v28 offset:0\n" \
  "ds_read_b64_tr_b16 a[12:13], v29 offset:0\n" \
  "ds_read_b64_tr_b16 a[20:21], v26 offset:2048\n" \
  "ds_read_b64_tr_b16 a[24:25], v27 offset:2048\n" \
  "ds_read_b64_tr_b16 a[28:29], v28 offset:2048\n" \
  "ds_read_b64_tr_b16 a[32:33], v29 offset:2048\n" \
  "ds_read_b64_tr_b16 a[2:3], v26 offset:4096\n" \
  "ds_read_b64_tr_b16 a[6:7], v27 offset:4096\n" \
  "ds_read_b64_tr_b16 a[10:11], v28 offset:4096\n" \
  "ds_read_b64_tr_b16 a[14:15], v29 offset:4096\n" \
  "ds_read_b64_tr_b16 a[22:23], v26 offset:6144\n" \
  "ds_read_b64_tr_b16 a[26:27], v27 offset:6144\n" \
  "ds_read_b64_tr_b16 a[30:31], v28 offset:6144\n" \
  "ds_read_b64_tr_b16 a[34:35], v29 offset:6144\n" \
  "s_add_u32 m0, s57, 0\n" \
  "s_add_u32 s31, s12, 221184\n" \
  "s_waitcnt lgkmcnt(0)\n" \
  "buffer_load_dwordx4 v38, s[4:7], s31 offen offset:0 lds\n" \
  "buffer_load_dwordx4 v39, s[4:7], s31 offen offset:1024 lds\n" \
  "buffer_load_dwordx4 v40, s[4:7], s31 offen offset:2048 lds\n" \
  "buffer_load_dwordx4 v41, s[4:7], s31 offen offset:3072 lds\n" \
  "s_add_u32 m0, s57, 4096\n" \
  "s_add_u32 s31, s12, 9658368\n" \
  "buffer_load_dwordx4 v38, s[4:7], s31 offen offset:0 lds\n" \
  "buffer_load_dwordx4 v39, s[4:7], s31 offen offset:1024 lds\n" \
  "buffer_load_dwordx4 v40, s[4:7], s31 offen offset:2048 lds\n" \
  "buffer_load_dwordx4 v41, s[4:7], s31 offen offset:3072 lds\n" \
  ".Lync_%=:\n" \
  "v_xor_b32 v246, s18, v2\n" \
  "v_bitop3_b32 v246, v4, s18, v246 bitop3:0xbe\n" \
  "v_bitop3_b32 v246, v6, s18, v246 bitop3:0xbe\n" \
  "v_bitop3_b32 v246, v8, s18, v246 bitop3:0xbe\n" \
  "v_xor_b32 v244, s19, v3\n" \
  "v_bitop3_b32 v244, v5, s19, v244 bitop3:0xbe\n" \
  "v_bitop3_b32 v244, v7, s19, v244 bitop3:0xbe\n" \
  "v_bitop3_b32 v244, v9, s19, v244 bitop3:0xbe\n" \
  "v_lshl_or_b32 v246, v244, 8, v246\n" \
  "v_cmp_ne_u32 vcc, 0, v246\n" \
  "s_cbranch_vccnz .Lyns_%=\n" \
  ".Lynd_%=:\n" \
  "s_or_b32 s32, s2, s20\n" \
  "s_cmp_eq_u32 s32, 0\n" \
  "s_cselect_b64 exec, 1, 0\n" \
  "buffer_store_dwordx2 v[10:11], off, s[36:39], 0 sc1\n" \
  "s_mov_b64 exec, -1\n" \
  "v_max3_u32 v246, v3, v5, v7\n" \
  "v_max_u32 v246, v246, v9\n" \
  "s_nop 1\n" \
  "v_max_u32_dpp v246, v246, v246 quad_perm:[1,0,3,2] row_mask:0xf bank_mask:0xf\n" \
  "s_nop 1\n" \
  "v_max_u32_dpp v246, v246, v246 quad_perm:[2,3,0,1] row_mask:0xf bank_mask:0xf\n" \
  "s_nop 1\n" \
  "v_max_u32_dpp v246, v246, v246 row_half_mirror row_mask:0xf bank_mask:0xf\n" \
  "s_nop 1\n" \
  "v_max_u32_dpp v246, v246, v246 row_mirror row_mask:0xf bank_mask:0xf\n" \
  "s_nop 0\n" \
  "v_readlane_b32 s48, v246, 0\n" \
  "v_readlane_b32 s49, v246, 16\n" \
  "v_readlane_b32 s50, v246, 32\n" \
  "v_readlane_b32 s51, v246, 48\n" \
  "s_max_u32 s48, s48, s49\n" \
  "s_max_u32 s50, s50, s51\n" \
  "s_max_u32 s48, s48, s50\n" \
  "s_lshr_b32 s48, s48, 24\n" \
  "s_max_u32 s48, s48, 64\n" \
  "s_min_u32 s48, s48, 250\n" \
  "s_add_u32 s23, s26, s48\n" \
  "s_sub_u32 s23, s23, 253\n" \
  "v_lshlrev_b32 v2, 16, v18\n" \
  "v_and_b32 v3, 4294901760, v18\n" \
  "v_lshlrev_b32 v4, 16, v19\n" \
  "v_and_b32 v5, 4294901760, v19\n" \
  "v_lshlrev_b32 v6, 16, v20\n" \
  "v_and_b32 v7, 4294901760, v20\n" \
  "v_lshlrev_b32 v8, 16, v21\n" \
  "v_and_b32 v9, 4294901760, v21\n" \
  "v_lshlrev_b32 v10, 16, v22\n" \
  "v_and_b32 v11, 4294901760, v22\n" \
  "v_lshlrev_b32 v12, 16, v23\n" \
  "v_and_b32 v13, 4294901760, v23\n" \
  "v_lshlrev_b32 v14, 16, v24\n" \
  "v_and_b32 v15, 4294901760, v24\n" \
  "v_lshlrev_b32 v18, 16, v25\n" \
  "v_and_b32 v19, 4294901760, v25\n" \
  "v_lshlrev_b32 v20, 16, v247\n" \
  "v_and_b32 v21, 4294901760, v247\n" \
  "v_and_b32 v30, 65535, v86\n" \
  "v_add_u32 v30, s21, v30\n" \
  "v_and_b32 v31, 65535, v87\n" \
  "v_add_u32 v31, s21, v31\n" \
  "v_and_b32 v32, 65535, v88\n" \
  "v_add_u32 v32, s21, v32\n" \
  "v_and_b32 v33, 65535, v89\n" \
  "v_add_u32 v33, s21, v33\n" \
  "v_lshrrev_b32 v34, 16, v86\n" \
  "v_add_u32 v34, s21, v34\n" \
  "v_lshrrev_b32 v35, 16, v87\n" \
  "v_add_u32 v35, s21, v35\n" \
  "v_lshrrev_b32 v36, 16, v88\n" \
  "v_add_u32 v36, s21, v36\n" \
  "v_lshrrev_b32 v37, 16, v89\n" \
  "v_add_u32 v37, s21, v37\n" \
  "v_mul_u32_u24 v247, 2448, v253\n" \
  "v_add_u32 v253, s21, v247\n" \
  "v_lshl_add_u32 v247, v254, 4, v253\n" \
  "v_lshl_add_u32 v246, v254, 2, v253\n" \
  "v_lshrrev_b32 v253, 3, v254\n" \
  "v_lshl_add_u32 v244, v253, 4, v247\n" \
  "v_lshrrev_b32 v253, 4, v191\n" \
  "v_lshl_add_u32 v242, v253, 2, s20\n" \
  "v_and_b32 v253, 3, v191\n" \
  "v_bfe_u32 v240, v191, 2, 2\n" \
  "v_mul_u32_u24 v239, 4896, v240\n" \
  "v_lshl_add_u32 v240, v253, 3, v239\n" \
  "v_lshl_add_u32 v253, v242, 5, v240\n" \
  "v_and_b32 v240, 15, v191\n" \
  "v_lshlrev_b32 v239, 4, v240\n" \
  "v_lshl_add_u32 v240, v242, 16, v239\n" \
  "v_lshlrev_b32 v242, 3, v254\n" \
  "s_lshr_b32 s98, s27, 8\n" \
  "s_and_b32 s98, s98, 255\n" \
  "v_subrev_u32 v239, s98, v0\n" \
  "v_bfrev_b32 v238, 1\n" \
  "v_cmp_gt_u32 s[96:97], 36, v239\n" \
  "v_cndmask_b32 v238, v238, v243, s[94:95]\n" \
  "s_cmp_eq_u32 s20, 1\n" \
  "s_cselect_b32 s92, 65537, s58\n" \
  "s_and_b32 s93, s59, 4294901758\n" \
  "ds_read_b64_tr_b16 a[40:41], v26 offset:8192\n" \
  "ds_read_b64_tr_b16 a[44:45], v27 offset:8192\n" \
  "ds_read_b64_tr_b16 a[48:49], v28 offset:8192\n" \
  "ds_read_b64_tr_b16 a[52:53], v29 offset:8192\n" \
  "ds_read_b64_tr_b16 a[60:61], v26 offset:10240\n" \
  "ds_read_b64_tr_b16 a[64:65], v27 offset:10240\n" \
  "ds_read_b64_tr_b16 a[68:69], v28 offset:10240\n" \
  "ds_read_b64_tr_b16 a[72:73], v29 offset:10240\n" \
  "ds_read_b64_tr_b16 a[42:43], v26 offset:12288\n" \
  "ds_read_b64_tr_b16 a[46:47], v27 offset:12288\n" \
  "ds_read_b64_tr_b16 a[50:51], v28 offset:12288\n" \
  "ds_read_b64_tr_b16 a[54:55], v29 offset:12288\n" \
  "ds_read_b64_tr_b16 a[62:63], v26 offset:14336\n" \
  "ds_read_b64_tr_b16 a[66:67], v27 offset:14336\n" \
  "ds_read_b64_tr_b16 a[70:71], v28 offset:14336\n" \
  "ds_read_b64_tr_b16 a[74:75], v29 offset:14336\n" \
  "s_add_u32 m0, s57, 8192\n" \
  "s_add_u32 s31, s13, 294912\n" \
  "s_waitcnt lgkmcnt(0)\n" \
  "buffer_load_dwordx4 v38, s[4:7], s31 offen offset:0 lds\n" \
  "buffer_load_dwordx4 v39, s[4:7], s31 offen offset:1024 lds\n" \
  "buffer_load_dwordx4 v40, s[4:7], s31 offen offset:2048 lds\n" \
  "buffer_load_dwordx4 v41, s[4:7], s31 offen offset:3072 lds\n" \
  "s_add_u32 m0, s57, 12288\n" \
  "s_add_u32 s31, s13, 9732096\n" \
  "buffer_load_dwordx4 v38, s[4:7], s31 offen offset:0 lds\n" \
  "buffer_load_dwordx4 v39, s[4:7], s31 offen offset:1024 lds\n" \
  "buffer_load_dwordx4 v40, s[4:7], s31 offen offset:2048 lds\n" \
  "buffer_load_dwordx4 v41, s[4:7], s31 offen offset:3072 lds\n" \
  "s_waitcnt vmcnt(17)\n" \
  "ds_read_b64_tr_b16 a[80:81], v26 offset:16384\n" \
  "ds_read_b64_tr_b16 a[84:85], v27 offset:16384\n" \
  "ds_read_b64_tr_b16 a[88:89], v28 offset:16384\n" \
  "ds_read_b64_tr_b16 a[92:93], v29 offset:16384\n" \
  "ds_read_b64_tr_b16 a[100:101], v26 offset:18432\n" \
  "ds_read_b64_tr_b16 a[104:105], v27 offset:18432\n" \
  "ds_read_b64_tr_b16 a[108:109], v28 offset:18432\n" \
  "ds_read_b64_tr_b16 a[112:113], v29 offset:18432\n" \
  "ds_read_b64_tr_b16 a[82:83], v26 offset:20480\n" \
  "ds_read_b64_tr_b16 a[86:87], v27 offset:20480\n" \
  "ds_read_b64_tr_b16 a[90:91], v28 offset:20480\n" \
  "ds_read_b64_tr_b16 a[94:95], v29 offset:20480\n" \
  "ds_read_b64_tr_b16 a[102:103], v26 offset:22528\n" \
  "ds_read_b64_tr_b16 a[106:107], v27 offset:22528\n" \
  "ds_read_b64_tr_b16 a[110:111], v28 offset:22528\n" \
  "ds_read_b64_tr_b16 a[114:115], v29 offset:22528\n" \
  "s_mov_b32 s32, 35840\n" \
  "buffer_load_dwordx4 v[22:25], v238, s[36:39], s32 offen offset:0 sc1\n" \
  "buffer_load_dwordx4 v[42:45], v238, s[36:39], s32 offen offset:2304 sc1\n" \
  "s_mov_b32 s32, 40448\n" \
  "buffer_load_dwordx4 v[46:49], v238, s[36:39], s32 offen offset:0 sc1\n" \
  "buffer_load_dwordx4 v[50:53], v238, s[36:39], s32 offen offset:2304 sc1\n" \
  "s_add_u32 s32, s8, 4096\n" \
  "buffer_load_dwordx4 v[54:57], v231, s[36:39], s8 offen offset:0 sc1\n" \
  "buffer_load_dwordx4 v[58:61], v231, s[36:39], s32 offen offset:0 sc1\n" \
  "buffer_load_dwordx4 v[62:65], v248, s[36:39], s9 offen offset:0 sc1\n" \
  "buffer_load_dwordx2 v[66:67], v242, s[36:39], s29 offen offset:0 sc1\n" \
  "s_waitcnt lgkmcnt(0)\n" \
  "s_barrier\n" \
  "s_waitcnt vmcnt(4)\n" \
  ".Lmnc_%=:\n" \
  "v_xor_b32 v239, s18, v22\n" \
  "v_bitop3_b32 v239, v23, s19, v239 bitop3:0xbe\n" \
  "v_bitop3_b32 v239, v42, s18, v239 bitop3:0xbe\n" \
  "v_bitop3_b32 v239, v43, s19, v239 bitop3:0xbe\n" \
  "v_bitop3_b32 v239, v46, s18, v239 bitop3:0xbe\n" \
  "v_bitop3_b32 v239, v47, s19, v239 bitop3:0xbe\n" \
  "v_bitop3_b32 v239, v50, s18, v239 bitop3:0xbe\n" \
  "v_bitop3_b32 v239, v51, s19, v239 bitop3:0xbe\n" \
  "v_cmp_ne_u32 vcc, 0, v239\n" \
  "s_and_b64 vcc, vcc, s[94:95]\n" \
  "s_cbranch_scc1 .Lmns_%=\n" \
  ".Lmnd_%=:\n" \
  "v_lshlrev_b32 v68, 16, v24\n" \
  "v_and_b32 v69, 4294901760, v24\n" \
  "v_lshlrev_b32 v70, 16, v25\n" \
  "v_and_b32 v71, 4294901760, v25\n" \
  "v_lshlrev_b32 v22, 16, v44\n" \
  "v_and_b32 v23, 4294901760, v44\n" \
  "v_lshlrev_b32 v24, 16, v45\n" \
  "v_and_b32 v25, 4294901760, v45\n" \
  "v_lshlrev_b32 v42, 16, v48\n" \
  "v_and_b32 v43, 4294901760, v48\n" \
  "v_lshlrev_b32 v44, 16, v49\n" \
  "v_and_b32 v45, 4294901760, v49\n" \
  "v_lshlrev_b32 v46, 16, v52\n" \
  "v_and_b32 v47, 4294901760, v52\n" \
  "v_lshlrev_b32 v48, 16, v53\n" \
  "v_and_b32 v49, 4294901760, v53\n" \
  "v_mul_f32 v68, 1055100473, v68\n" \
  "v_ldexp_f32 v68, v68, s23\n" \
  "v_mul_f32 v69, 1055100473, v69\n" \
  "v_ldexp_f32 v69, v69, s23\n" \
  "v_mul_f32 v70, 1055100473, v70\n" \
  "v_ldexp_f32 v70, v70, s23\n" \
  "v_mul_f32 v71, 1055100473, v71\n" \
  "v_ldexp_f32 v71, v71, s23\n" \
  "s_mov_b64 exec, s[94:95]\n" \
  "ds_write_b128 v243, v[22:25] offset:31104\n" \
  "ds_write_b128 v243, v[42:45] offset:33408\n" \
  "ds_write_b128 v243, v[68:71] offset:28800\n" \
  "s_bfe_u32 s32, s27, 524296\n" \
  "s_lshl_b32 s32, s32, 4\n" \
  "s_sub_u32 s32, 163200, s32\n" \
  "v_add_u32 v239, s32, v243\n" \
  "s_mov_b64 exec, s[96:97]\n" \
  "ds_write_b128 v239, v[46:49] offset:0\n" \
  "s_mov_b64 exec, -1\n" \
  "s_waitcnt vmcnt(1)\n" \
  ".Lrnc_%=:\n" \
  "v_bitop3_b32 v243, v54, v58, v62 bitop3:0x80\n" \
  "v_and_b32 v243, 1, v243\n" \
  "v_cmp_eq_u32 vcc, 0, v243\n" \
  "s_cbranch_vccnz .Lrns_%=\n" \
  ".Lrnd_%=:\n" \
  "v_mfma_f32_16x16x32_f16 v[22:25], a[228:231], v[54:57], 0\n" \
  "s_add_u32 m0, s57, 16384\n" \
  "s_add_u32 s31, s13, 368640\n" \
  "buffer_load_dwordx4 v38, s[4:7], s31 offen offset:0 lds\n" \
  "v_mfma_f32_16x16x32_f16 v[42:45], a[232:235], v[54:57], 0\n" \
  "buffer_load_dwordx4 v39, s[4:7], s31 offen offset:1024 lds\n" \
  "v_mfma_f32_16x16x32_f16 v[46:49], a[228:231], v[58:61], 0\n" \
  "buffer_load_dwordx4 v40, s[4:7], s31 offen offset:2048 lds\n" \
  "v_mfma_f32_16x16x32_f16 v[50:53], a[232:235], v[58:61], 0\n" \
  "buffer_load_dwordx4 v41, s[4:7], s31 offen offset:3072 lds\n" \
  "v_mfma_f32_16x16x32_f16 v[54:57], a[228:231], v[62:65], 0\n" \
  "s_add_u32 m0, s57, 20480\n" \
  "s_add_u32 s31, s13, 9805824\n" \
  "buffer_load_dwordx4 v38, s[4:7], s31 offen offset:0 lds\n" \
  "v_mfma_f32_16x16x32_f16 v[58:61], a[232:235], v[62:65], 0\n" \
  "buffer_load_dwordx4 v39, s[4:7], s31 offen offset:1024 lds\n" \
  "v_cvt_pk_f16_f32 v62, v42, v22\n" \
  "v_cvt_pk_f16_f32 v63, v43, v23\n" \
  "v_cvt_pk_f16_f32 v64, v44, v24\n" \
  "v_cvt_pk_f16_f32 v65, v45, v25\n" \
  "ds_write_b128 v83, v[62:65] offset:0\n" \
  "v_cvt_pk_f16_f32 v22, v50, v46\n" \
  "v_cvt_pk_f16_f32 v23, v51, v47\n" \
  "v_cvt_pk_f16_f32 v24, v52, v48\n" \
  "v_cvt_pk_f16_f32 v25, v53, v49\n" \
  "ds_write_b128 v84, v[22:25] offset:0\n" \
  "v_cvt_pk_f16_f32 v22, v58, v54\n" \
  "v_cvt_pk_f16_f32 v23, v59, v55\n" \
  "v_cvt_pk_f16_f32 v24, v60, v56\n" \
  "v_cvt_pk_f16_f32 v25, v61, v57\n" \
  "s_mov_b64 exec, s[92:93]\n" \
  "ds_write_b128 v85, v[22:25] offset:0\n" \
  "s_mov_b64 exec, -1\n" \
  "s_waitcnt lgkmcnt(0)\n" \
  "s_barrier\n" \
  "ds_read_b64 v[22:23], v82 offset:0\n" \
  "ds_read_b64 v[24:25], v82 offset:512\n" \
  "ds_read_b64 v[42:43], v82 offset:1024\n" \
  "ds_read_b64 v[44:45], v82 offset:256\n" \
  "ds_read_b64 v[46:47], v82 offset:768\n" \
  "ds_read_b64 v[48:49], v82 offset:1280\n" \
  "v_lshlrev_b32 v248, 5, v254\n" \
  "buffer_load_dwordx4 v40, s[4:7], s31 offen offset:2048 lds\n" \
  "buffer_load_dwordx4 v41, s[4:7], s31 offen offset:3072 lds\n" \
  "s_waitcnt lgkmcnt(5)\n" \
  "v_mov_b32 v92, v22\n" \
  "s_waitcnt lgkmcnt(4)\n" \
  "v_mov_b32 v93, v24\n" \
  "s_waitcnt lgkmcnt(3)\n" \
  "v_mov_b32 v94, v42\n" \
  "v_mov_b32 v100, v23\n" \
  "v_mov_b32 v101, v25\n" \
  "v_mfma_f32_16x16x32_f16 v[22:25], v[92:95], a[208:211], 0\n" \
  "v_mov_b32 v102, v43\n" \
  "s_waitcnt lgkmcnt(2)\n" \
  "v_mov_b32 v96, v44\n" \
  "v_mfma_f32_16x16x32_f16 v[50:53], v[92:95], a[212:215], 0\n" \
  "s_waitcnt lgkmcnt(1)\n" \
  "v_mov_b32 v97, v46\n" \
  "s_waitcnt lgkmcnt(0)\n" \
  "v_mov_b32 v98, v48\n" \
  "v_mfma_f32_16x16x32_f16 v[54:57], v[100:103], a[208:211], 0\n" \
  "v_mov_b32 v104, v45\n" \
  "v_mov_b32 v105, v47\n" \
  "v_mfma_f32_16x16x32_f16 v[42:45], v[100:103], a[212:215], 0\n" \
  "v_mov_b32 v106, v49\n" \
  "v_mfma_f32_16x16x32_f16 v[46:49], v[96:99], a[208:211], 0\n" \
  "ds_read_b128 v[58:61], v248 offset:31104\n" \
  "ds_read_b128 v[62:65], v248 offset:31120\n" \
  "v_mfma_f32_16x16x32_f16 v[68:71], v[96:99], a[212:215], 0\n" \
  "ds_read_b128 v[72:75], v248 offset:32128\n" \
  "ds_read_b128 v[76:79], v248 offset:32144\n" \
  "v_mfma_f32_16x16x32_f16 v[80:83], v[104:107], a[208:211], 0\n" \
  "ds_read_b64 v[84:85], v242 offset:33152\n" \
  "v_mfma_f32_16x16x32_f16 v[86:89], v[104:107], a[212:215], 0\n" \
  "ds_read_b128 v[90:93], v248 offset:28800\n" \
  "ds_read_b128 v[94:97], v248 offset:28816\n" \
  "ds_read_b128 v[98:101], v248 offset:29824\n" \
  "ds_read_b128 v[102:105], v248 offset:29840\n" \
  "ds_read_b64 v[106:107], v242 offset:30848\n" \
  "v_pk_add_f32 v[108:109], v[22:23], v[46:47]\n" \
  "v_pk_add_f32 v[110:111], v[24:25], v[48:49]\n" \
  "v_pk_add_f32 v[112:113], v[50:51], v[68:69]\n" \
  "v_pk_add_f32 v[114:115], v[52:53], v[70:71]\n" \
  "v_pk_add_f32 v[116:117], v[22:23], v[46:47] neg_lo:[0,1] neg_hi:[0,1]\n" \
  "v_pk_add_f32 v[118:119], v[24:25], v[48:49] neg_lo:[0,1] neg_hi:[0,1]\n" \
  "v_pk_add_f32 v[22:23], v[50:51], v[68:69] neg_lo:[0,1] neg_hi:[0,1]\n" \
  "v_pk_add_f32 v[24:25], v[52:53], v[70:71] neg_lo:[0,1] neg_hi:[0,1]\n" \
  "v_pk_add_f32 v[46:47], v[54:55], v[80:81]\n" \
  "v_pk_add_f32 v[48:49], v[56:57], v[82:83]\n" \
  "v_pk_add_f32 v[50:51], v[42:43], v[86:87]\n" \
  "v_pk_add_f32 v[52:53], v[44:45], v[88:89]\n" \
  "v_pk_add_f32 v[68:69], v[54:55], v[80:81] neg_lo:[0,1] neg_hi:[0,1]\n" \
  "v_pk_add_f32 v[70:71], v[56:57], v[82:83] neg_lo:[0,1] neg_hi:[0,1]\n" \
  "v_pk_add_f32 v[54:55], v[42:43], v[86:87] neg_lo:[0,1] neg_hi:[0,1]\n" \
  "v_pk_add_f32 v[56:57], v[44:45], v[88:89] neg_lo:[0,1] neg_hi:[0,1]\n" \
  "v_cvt_pk_f16_f32 v42, v108, v112\n" \
  "v_cvt_pk_f16_f32 v43, v109, v113\n" \
  "v_cvt_pk_f16_f32 v44, v110, v114\n" \
  "v_cvt_pk_f16_f32 v45, v111, v115\n" \
  "v_cvt_pk_f16_f32 v80, v116, v22\n" \
  "v_cvt_pk_f16_f32 v81, v117, v23\n" \
  "v_mfma_f32_16x16x32_f16 v[86:89], a[200:203], v[42:45], 0\n" \
  "v_cvt_pk_f16_f32 v82, v118, v24\n" \
  "v_cvt_pk_f16_f32 v83, v119, v25\n" \
  "v_mfma_f32_16x16x32_f16 v[22:25], a[204:207], v[42:45], 0\n" \
  "v_cvt_pk_f16_f32 v42, v46, v50\n" \
  "v_cvt_pk_f16_f32 v43, v47, v51\n" \
  "v_mfma_f32_16x16x32_f16 v[108:111], a[220:223], v[80:83], 0\n" \
  "v_cvt_pk_f16_f32 v44, v48, v52\n" \
  "v_cvt_pk_f16_f32 v45, v49, v53\n" \
  "v_mfma_f32_16x16x32_f16 v[46:49], a[224:227], v[80:83], 0\n" \
  "v_cvt_pk_f16_f32 v50, v68, v54\n" \
  "v_cvt_pk_f16_f32 v51, v69, v55\n" \
  "v_mfma_f32_16x16x32_f16 v[80:83], a[200:203], v[42:45], 0\n" \
  "v_cvt_pk_f16_f32 v52, v70, v56\n" \
  "v_cvt_pk_f16_f32 v53, v71, v57\n" \
  "v_mfma_f32_16x16x32_f16 v[54:57], a[204:207], v[42:45], 0\n" \
  "ds_read_b128 v[42:45], v248 offset:33408\n" \
  "v_mfma_f32_16x16x32_f16 v[68:71], a[220:223], v[50:53], 0\n" \
  "ds_read_b128 v[112:115], v248 offset:33424\n" \
  "v_mfma_f32_16x16x32_f16 v[116:119], a[224:227], v[50:53], 0\n" \
  "ds_read_b128 v[50:53], v248 offset:34432\n" \
  "ds_read_b128 v[120:123], v248 offset:34448\n" \
  "ds_read_b64 v[124:125], v242 offset:35456\n" \
  "s_waitcnt lgkmcnt(14)\n" \
  "v_readfirstlane_b32 s32, v58\n" \
  "s_mov_b32 exec_lo, 125241207\n" \
  "s_mov_b32 exec_hi, 125241207\n" \
  "v_cvt_pk_f16_f32 v254, v22, v86\n" \
  "ds_write_b32 v30, v254 offset:0\n" \
  "v_cvt_pk_f16_f32 v254, v23, v87\n" \
  "ds_write_b32 v31, v254 offset:0\n" \
  "v_cvt_pk_f16_f32 v254, v24, v88\n" \
  "ds_write_b32 v32, v254 offset:0\n" \
  "v_cvt_pk_f16_f32 v254, v25, v89\n" \
  "ds_write_b32 v33, v254 offset:0\n" \
  "v_cvt_pk_f16_f32 v254, v46, v108\n" \
  "ds_write_b32 v34, v254 offset:0\n" \
  "v_cvt_pk_f16_f32 v254, v47, v109\n" \
  "ds_write_b32 v35, v254 offset:0\n" \
  "v_cvt_pk_f16_f32 v254, v48, v110\n" \
  "ds_write_b32 v36, v254 offset:0\n" \
  "v_cvt_pk_f16_f32 v254, v49, v111\n" \
  "ds_write_b32 v37, v254 offset:0\n" \
  "v_cvt_pk_f16_f32 v254, v54, v80\n" \
  "ds_write_b32 v30, v254 offset:2448\n" \
  "v_cvt_pk_f16_f32 v254, v55, v81\n" \
  "ds_write_b32 v31, v254 offset:2448\n" \
  "v_cvt_pk_f16_f32 v254, v56, v82\n" \
  "ds_write_b32 v32, v254 offset:2448\n" \
  "v_cvt_pk_f16_f32 v254, v57, v83\n" \
  "ds_write_b32 v33, v254 offset:2448\n" \
  "v_cvt_pk_f16_f32 v254, v116, v68\n" \
  "ds_write_b32 v34, v254 offset:2448\n" \
  "v_cvt_pk_f16_f32 v254, v117, v69\n" \
  "ds_write_b32 v35, v254 offset:2448\n" \
  "v_cvt_pk_f16_f32 v254, v118, v70\n" \
  "ds_write_b32 v36, v254 offset:2448\n" \
  "v_cvt_pk_f16_f32 v254, v119, v71\n" \
  "ds_write_b32 v37, v254 offset:2448\n" \
  "s_mov_b64 exec, -1\n" \
  "ds_read_b128 v[22:25], v244 offset:0\n" \
  "ds_read_b128 v[30:33], v244 offset:576\n" \
  "ds_read_b32 v254, v246 offset:1152\n" \
  "v_mov_b32 v34, s32\n" \
  "v_pk_add_f32 v[58:59], v[58:59], v[34:35] op_sel_hi:[1,0] neg_lo:[0,1] neg_hi:[0,1]\n" \
  "v_pk_add_f32 v[60:61], v[60:61], v[34:35] op_sel_hi:[1,0] neg_lo:[0,1] neg_hi:[0,1]\n" \
  "s_waitcnt lgkmcnt(15)\n" \
  "v_pk_add_f32 v[62:63], v[62:63], v[34:35] op_sel_hi:[1,0] neg_lo:[0,1] neg_hi:[0,1]\n" \
  "v_pk_add_f32 v[64:65], v[64:65], v[34:35] op_sel_hi:[1,0] neg_lo:[0,1] neg_hi:[0,1]\n" \
  "v_pk_add_f32 v[72:73], v[72:73], v[34:35] op_sel_hi:[1,0] neg_lo:[0,1] neg_hi:[0,1]\n" \
  "v_pk_add_f32 v[74:75], v[74:75], v[34:35] op_sel_hi:[1,0] neg_lo:[0,1] neg_hi:[0,1]\n" \
  "v_pk_add_f32 v[76:77], v[76:77], v[34:35] op_sel_hi:[1,0] neg_lo:[0,1] neg_hi:[0,1]\n" \
  "v_pk_add_f32 v[78:79], v[78:79], v[34:35] op_sel_hi:[1,0] neg_lo:[0,1] neg_hi:[0,1]\n" \
  "v_pk_add_f32 v[84:85], v[84:85], v[34:35] op_sel_hi:[1,0] neg_lo:[0,1] neg_hi:[0,1]\n" \
  "s_waitcnt lgkmcnt(2)\n" \
  "v_fma_mix_f32 v2, v22, v90, v2 op_sel:[0,0,0] op_sel_hi:[1,0,0]\n" \
  "v_fma_mix_f32 v3, v22, v91, v3 op_sel:[1,0,0] op_sel_hi:[1,0,0]\n" \
  "v_fma_mix_f32 v4, v23, v92, v4 op_sel:[0,0,0] op_sel_hi:[1,0,0]\n" \
  "v_fma_mix_f32 v5, v23, v93, v5 op_sel:[1,0,0] op_sel_hi:[1,0,0]\n" \
  "v_fma_mix_f32 v6, v24, v94, v6 op_sel:[0,0,0] op_sel_hi:[1,0,0]\n" \
  "v_fma_mix_f32 v7, v24, v95, v7 op_sel:[1,0,0] op_sel_hi:[1,0,0]\n" \
  "v_fma_mix_f32 v8, v25, v96, v8 op_sel:[0,0,0] op_sel_hi:[1,0,0]\n" \
  "v_fma_mix_f32 v9, v25, v97, v9 op_sel:[1,0,0] op_sel_hi:[1,0,0]\n" \
  "s_waitcnt lgkmcnt(1)\n" \
  "v_fma_mix_f32 v10, v30, v98, v10 op_sel:[0,0,0] op_sel_hi:[1,0,0]\n" \
  "v_fma_mix_f32 v11, v30, v99, v11 op_sel:[1,0,0] op_sel_hi:[1,0,0]\n" \
  "v_fma_mix_f32 v12, v31, v100, v12 op_sel:[0,0,0] op_sel_hi:[1,0,0]\n" \
  "v_fma_mix_f32 v13, v31, v101, v13 op_sel:[1,0,0] op_sel_hi:[1,0,0]\n" \
  "v_fma_mix_f32 v14, v32, v102, v14 op_sel:[0,0,0] op_sel_hi:[1,0,0]\n" \
  "v_fma_mix_f32 v15, v32, v103, v15 op_sel:[1,0,0] op_sel_hi:[1,0,0]\n" \
  "v_fma_mix_f32 v18, v33, v104, v18 op_sel:[0,0,0] op_sel_hi:[1,0,0]\n" \
  "v_fma_mix_f32 v19, v33, v105, v19 op_sel:[1,0,0] op_sel_hi:[1,0,0]\n" \
  "s_waitcnt lgkmcnt(0)\n" \
  "v_fma_mix_f32 v20, v254, v106, v20 op_sel:[0,0,0] op_sel_hi:[1,0,0]\n" \
  "v_fma_mix_f32 v21, v254, v107, v21 op_sel:[1,0,0] op_sel_hi:[1,0,0]\n" \
  "v_mov_b32 v23, 925353388\n" \
  "v_mov_b32_dpp v24, v2 row_newbcast:0 row_mask:0xf bank_mask:0xf\n" \
  "v_mov_b32_dpp v254, v2 row_newbcast:0 row_mask:0xf bank_mask:0xf\n" \
  "v_cvt_pk_bf16_f32 v30, v2, v3\n" \
  "v_cvt_pk_bf16_f32 v31, v4, v5\n" \
  "v_cvt_pk_bf16_f32 v32, v6, v7\n" \
  "v_cvt_pk_bf16_f32 v33, v8, v9\n" \
  "v_cvt_pk_bf16_f32 v34, v10, v11\n" \
  "v_cvt_pk_bf16_f32 v35, v12, v13\n" \
  "v_cvt_pk_bf16_f32 v36, v14, v15\n" \
  "v_cvt_pk_bf16_f32 v37, v18, v19\n" \
  "v_cvt_pk_bf16_f32 v248, v20, v21\n" \
  "v_permlane16_swap_b32 v24, v254\n" \
  "v_pk_add_f32 v[2:3], v[2:3], v[24:25] op_sel_hi:[1,0] neg_lo:[0,1] neg_hi:[0,1]\n" \
  "v_pk_add_f32 v[4:5], v[4:5], v[24:25] op_sel_hi:[1,0] neg_lo:[0,1] neg_hi:[0,1]\n" \
  "v_pk_add_f32 v[6:7], v[6:7], v[24:25] op_sel_hi:[1,0] neg_lo:[0,1] neg_hi:[0,1]\n" \
  "v_pk_add_f32 v[8:9], v[8:9], v[24:25] op_sel_hi:[1,0] neg_lo:[0,1] neg_hi:[0,1]\n" \
  "v_pk_add_f32 v[10:11], v[10:11], v[24:25] op_sel_hi:[1,0] neg_lo:[0,1] neg_hi:[0,1]\n" \
  "v_pk_add_f32 v[12:13], v[12:13], v[24:25] op_sel_hi:[1,0] neg_lo:[0,1] neg_hi:[0,1]\n" \
  "v_pk_add_f32 v[14:15], v[14:15], v[24:25] op_sel_hi:[1,0] neg_lo:[0,1] neg_hi:[0,1]\n" \
  "v_pk_add_f32 v[18:19], v[18:19], v[24:25] op_sel_hi:[1,0] neg_lo:[0,1] neg_hi:[0,1]\n" \
  "v_pk_add_f32 v[20:21], v[20:21], v[24:25] op_sel_hi:[1,0] neg_lo:[0,1] neg_hi:[0,1]\n" \
  "v_pk_add_f32 v[24:25], v[2:3], v[4:5]\n" \
  "v_pk_mul_f32 v[46:47], v[2:3], v[2:3]\n" \
  "v_pk_fma_f32 v[46:47], v[4:5], v[4:5], v[46:47]\n" \
  "v_pk_add_f32 v[24:25], v[24:25], v[6:7]\n" \
  "v_pk_fma_f32 v[46:47], v[6:7], v[6:7], v[46:47]\n" \
  "v_pk_add_f32 v[24:25], v[24:25], v[8:9]\n" \
  "v_pk_fma_f32 v[46:47], v[8:9], v[8:9], v[46:47]\n" \
  "v_pk_add_f32 v[24:25], v[24:25], v[10:11]\n" \
  "v_pk_fma_f32 v[46:47], v[10:11], v[10:11], v[46:47]\n" \
  "v_pk_add_f32 v[24:25], v[24:25], v[12:13]\n" \
  "v_pk_fma_f32 v[46:47], v[12:13], v[12:13], v[46:47]\n" \
  "v_pk_add_f32 v[24:25], v[24:25], v[14:15]\n" \
  "v_pk_fma_f32 v[46:47], v[14:15], v[14:15], v[46:47]\n" \
  "v_pk_add_f32 v[24:25], v[24:25], v[18:19]\n" \
  "v_pk_fma_f32 v[46:47], v[18:19], v[18:19], v[46:47]\n" \
  "v_pk_add_f32 v[24:25], v[24:25], v[20:21]\n" \
  "v_pk_fma_f32 v[46:47], v[20:21], v[20:21], v[46:47]\n" \
  "v_add_f32 v254, v24, v25\n" \
  "v_add_f32 v244, v46, v47\n" \
  "ds_read_b64_tr_b16 a[120:121], v26 offset:0\n" \
  "ds_read_b64_tr_b16 a[124:125], v27 offset:0\n" \
  "ds_read_b64_tr_b16 a[128:129], v28 offset:0\n" \
  "ds_read_b64_tr_b16 a[132:133], v29 offset:0\n" \
  "v_permlane16_swap_b32 v254, v244\n" \
  "v_add_f32 v254, v254, v244\n" \
  "buffer_store_dwordx4 v[30:33], v251, s[36:39], s10 offen offset:0 sc1\n" \
  "ds_read_b64_tr_b16 a[140:141], v26 offset:2048\n" \
  "ds_read_b64_tr_b16 a[144:145], v27 offset:2048\n" \
  "ds_read_b64_tr_b16 a[148:149], v28 offset:2048\n" \
  "v_add_f32_dpp v254, v254, v254 quad_perm:[1,0,3,2] row_mask:0xf bank_mask:0xf\n" \
  "buffer_store_dwordx4 v[34:37], v251, s[36:39], s10 offen offset:512 sc1\n" \
  "ds_read_b64_tr_b16 a[152:153], v29 offset:2048\n" \
  "ds_read_b64_tr_b16 a[122:123], v26 offset:4096\n" \
  "ds_read_b64_tr_b16 a[126:127], v27 offset:4096\n" \
  "v_add_f32_dpp v254, v254, v254 quad_perm:[2,3,0,1] row_mask:0xf bank_mask:0xf\n" \
  "buffer_store_dword v248, v250, s[36:39], s10 offen offset:1024 sc1\n" \
  "ds_read_b64_tr_b16 a[130:131], v28 offset:4096\n" \
  "ds_read_b64_tr_b16 a[134:135], v29 offset:4096\n" \
  "ds_read_b64_tr_b16 a[142:143], v26 offset:6144\n" \
  "v_add_f32_dpp v254, v254, v254 row_half_mirror row_mask:0xf bank_mask:0xf\n" \
  "ds_read_b64_tr_b16 a[146:147], v27 offset:6144\n" \
  "ds_read_b64_tr_b16 a[150:151], v28 offset:6144\n" \
  "ds_read_b64_tr_b16 a[154:155], v29 offset:6144\n" \
  "v_add_f32_dpp v254, v254, v254 row_mirror row_mask:0xf bank_mask:0xf\n" \
  "v_mov_b32 v244, v254\n" \
  "ds_read_b64_tr_b16 a[160:161], v26 offset:8192\n" \
  "ds_read_b64_tr_b16 a[164:165], v27 offset:8192\n" \
  "ds_read_b64_tr_b16 a[168:169], v28 offset:8192\n" \
  "ds_read_b64_tr_b16 a[172:173], v29 offset:8192\n" \
  "v_permlane16_swap_b32 v254, v244\n" \
  "v_mul_f32 v254, s35, v254\n" \
  "v_fma_f32 v244, v244, s35, v23\n" \
  "v_fma_f32 v244, -v254, v254, v244\n" \
  "v_max_f32 v244, v244, v23\n" \
  "v_rsq_f32 v22, v244\n" \
  "ds_read_b64_tr_b16 a[180:181], v26 offset:10240\n" \
  "ds_read_b64_tr_b16 a[184:185], v27 offset:10240\n" \
  "ds_read_b64_tr_b16 a[188:189], v28 offset:10240\n" \
  "ds_read_b64_tr_b16 a[192:193], v29 offset:10240\n" \
  "v_mul_f32_e64 v23, -v254, v22\n" \
  "v_pk_fma_f32 v[2:3], v[2:3], v[22:23], v[22:23] op_sel:[0,0,1] op_sel_hi:[1,0,1]\n" \
  "v_pk_fma_f32 v[2:3], v[2:3], v[42:43], v[58:59]\n" \
  "v_pk_fma_f32 v[4:5], v[4:5], v[22:23], v[22:23] op_sel:[0,0,1] op_sel_hi:[1,0,1]\n" \
  "v_pk_fma_f32 v[4:5], v[4:5], v[44:45], v[60:61]\n" \
  "v_pk_fma_f32 v[6:7], v[6:7], v[22:23], v[22:23] op_sel:[0,0,1] op_sel_hi:[1,0,1]\n" \
  "v_pk_fma_f32 v[6:7], v[6:7], v[112:113], v[62:63]\n" \
  "ds_read_b64_tr_b16 a[162:163], v26 offset:12288\n" \
  "ds_read_b64_tr_b16 a[166:167], v27 offset:12288\n" \
  "s_add_u32 m0, s57, 0\n" \
  "s_add_u32 s31, s13, 442368\n" \
  "s_waitcnt lgkmcnt(10)\n" \
  "buffer_load_dwordx4 v38, s[4:7], s31 offen offset:0 lds\n" \
  "v_pk_fma_f32 v[8:9], v[8:9], v[22:23], v[22:23] op_sel:[0,0,1] op_sel_hi:[1,0,1]\n" \
  "v_pk_fma_f32 v[8:9], v[8:9], v[114:115], v[64:65]\n" \
  "v_pk_fma_f32 v[10:11], v[10:11], v[22:23], v[22:23] op_sel:[0,0,1] op_sel_hi:[1,0,1]\n" \
  "v_pk_fma_f32 v[10:11], v[10:11], v[50:51], v[72:73]\n" \
  "ds_read_b64_tr_b16 a[170:171], v28 offset:12288\n" \
  "ds_read_b64_tr_b16 a[174:175], v29 offset:12288\n" \
  "buffer_load_dwordx4 v39, s[4:7], s31 offen offset:1024 lds\n" \
  "v_pk_fma_f32 v[12:13], v[12:13], v[22:23], v[22:23] op_sel:[0,0,1] op_sel_hi:[1,0,1]\n" \
  "v_pk_fma_f32 v[12:13], v[12:13], v[52:53], v[74:75]\n" \
  "v_pk_fma_f32 v[14:15], v[14:15], v[22:23], v[22:23] op_sel:[0,0,1] op_sel_hi:[1,0,1]\n" \
  "v_pk_fma_f32 v[14:15], v[14:15], v[120:121], v[76:77]\n" \
  "ds_read_b64_tr_b16 a[182:183], v26 offset:14336\n" \
  "ds_read_b64_tr_b16 a[186:187], v27 offset:14336\n" \
  "buffer_load_dwordx4 v40, s[4:7], s31 offen offset:2048 lds\n" \
  "v_pk_fma_f32 v[18:19], v[18:19], v[22:23], v[22:23] op_sel:[0,0,1] op_sel_hi:[1,0,1]\n" \
  "v_pk_fma_f32 v[18:19], v[18:19], v[122:123], v[78:79]\n" \
  "v_pk_fma_f32 v[20:21], v[20:21], v[22:23], v[22:23] op_sel:[0,0,1] op_sel_hi:[1,0,1]\n" \
  "v_pk_fma_f32 v[20:21], v[20:21], v[124:125], v[84:85]\n" \
  "ds_read_b64_tr_b16 a[190:191], v28 offset:14336\n" \
  "ds_read_b64_tr_b16 a[194:195], v29 offset:14336\n" \
  "buffer_load_dwordx4 v41, s[4:7], s31 offen offset:3072 lds\n" \
  "v_mov_b32 v23, 925353388\n" \
  "v_pk_add_f32 v[24:25], v[2:3], v[4:5]\n" \
  "v_pk_mul_f32 v[30:31], v[2:3], v[2:3]\n" \
  "v_pk_fma_f32 v[30:31], v[4:5], v[4:5], v[30:31]\n" \
  "v_pk_add_f32 v[24:25], v[24:25], v[6:7]\n" \
  "v_pk_fma_f32 v[30:31], v[6:7], v[6:7], v[30:31]\n" \
  "v_pk_add_f32 v[24:25], v[24:25], v[8:9]\n" \
  "v_pk_fma_f32 v[30:31], v[8:9], v[8:9], v[30:31]\n" \
  "v_pk_add_f32 v[24:25], v[24:25], v[10:11]\n" \
  "v_pk_fma_f32 v[30:31], v[10:11], v[10:11], v[30:31]\n" \
  "v_pk_add_f32 v[24:25], v[24:25], v[12:13]\n" \
  "v_pk_fma_f32 v[30:31], v[12:13], v[12:13], v[30:31]\n" \
  "v_pk_add_f32 v[24:25], v[24:25], v[14:15]\n" \
  "v_pk_fma_f32 v[30:31], v[14:15], v[14:15], v[30:31]\n" \
  "v_pk_add_f32 v[24:25], v[24:25], v[18:19]\n" \
  "v_pk_fma_f32 v[30:31], v[18:19], v[18:19], v[30:31]\n" \
  "v_pk_add_f32 v[24:25], v[24:25], v[20:21]\n" \
  "v_pk_fma_f32 v[30:31], v[20:21], v[20:21], v[30:31]\n" \
  "v_add_f32 v254, v24, v25\n" \
  "v_add_f32 v251, v30, v31\n" \
  "s_waitcnt vmcnt(7)\n" \
  "ds_read_b64_tr_b16 a[200:201], v26 offset:16384\n" \
  "ds_read_b64_tr_b16 a[204:205], v27 offset:16384\n" \
  "ds_read_b64_tr_b16 a[208:209], v28 offset:16384\n" \
  "s_add_u32 m0, s57, 4096\n" \
  "s_add_u32 s31, s13, 9879552\n" \
  "buffer_load_dwordx4 v38, s[4:7], s31 offen offset:0 lds\n" \
  "v_permlane16_swap_b32 v254, v251\n" \
  "v_add_f32 v254, v254, v251\n" \
  "ds_read_b64_tr_b16 a[212:213], v29 offset:16384\n" \
  "ds_read_b64_tr_b16 a[220:221], v26 offset:18432\n" \
  "ds_read_b64_tr_b16 a[224:225], v27 offset:18432\n" \
  "buffer_load_dwordx4 v39, s[4:7], s31 offen offset:1024 lds\n" \
  "v_add_f32_dpp v254, v254, v254 quad_perm:[1,0,3,2] row_mask:0xf bank_mask:0xf\n" \
  "ds_read_b64_tr_b16 a[228:229], v28 offset:18432\n" \
  "ds_read_b64_tr_b16 a[232:233], v29 offset:18432\n" \
  "ds_read_b64_tr_b16 a[202:203], v26 offset:20480\n" \
  "buffer_load_dwordx4 v40, s[4:7], s31 offen offset:2048 lds\n" \
  "v_add_f32_dpp v254, v254, v254 quad_perm:[2,3,0,1] row_mask:0xf bank_mask:0xf\n" \
  "ds_read_b64_tr_b16 a[206:207], v27 offset:20480\n" \
  "ds_read_b64_tr_b16 a[210:211], v28 offset:20480\n" \
  "ds_read_b64_tr_b16 a[214:215], v29 offset:20480\n" \
  "buffer_load_dwordx4 v41, s[4:7], s31 offen offset:3072 lds\n" \
  "v_add_f32_dpp v254, v254, v254 row_half_mirror row_mask:0xf bank_mask:0xf\n" \
  "ds_read_b64_tr_b16 a[222:223], v26 offset:22528\n" \
  "ds_read_b64_tr_b16 a[226:227], v27 offset:22528\n" \
  "ds_read_b64_tr_b16 a[230:231], v28 offset:22528\n" \
  "ds_read_b64_tr_b16 a[234:235], v29 offset:22528\n" \
  "s_add_u32 m0, s57, 8192\n" \
  "s_add_u32 s31, s13, 516096\n" \
  "s_waitcnt lgkmcnt(15)\n" \
  "buffer_load_dwordx4 v38, s[4:7], s31 offen offset:0 lds\n" \
  "v_add_f32_dpp v254, v254, v254 row_mirror row_mask:0xf bank_mask:0xf\n" \
  "v_mov_b32 v251, v254\n" \
  "buffer_load_dwordx4 v39, s[4:7], s31 offen offset:1024 lds\n" \
  "buffer_load_dwordx4 v40, s[4:7], s31 offen offset:2048 lds\n" \
  "v_permlane16_swap_b32 v254, v251\n" \
  "v_mul_f32 v254, s35, v254\n" \
  "v_fma_f32 v251, v251, s35, v23\n" \
  "v_fma_f32 v251, -v254, v254, v251\n" \
  "v_max_f32 v251, v251, v23\n" \
  "v_rsq_f32 v22, v251\n" \
  "buffer_load_dwordx4 v41, s[4:7], s31 offen offset:3072 lds\n" \
  "v_mul_f32_e64 v23, -v254, v22\n" \
  "v_pk_fma_f32 v[2:3], v[2:3], v[22:23], v[22:23] op_sel:[0,0,1] op_sel_hi:[1,0,1]\n" \
  "v_pk_fma_f32 v[4:5], v[4:5], v[22:23], v[22:23] op_sel:[0,0,1] op_sel_hi:[1,0,1]\n" \
  "v_pk_fma_f32 v[6:7], v[6:7], v[22:23], v[22:23] op_sel:[0,0,1] op_sel_hi:[1,0,1]\n" \
  "v_pk_fma_f32 v[8:9], v[8:9], v[22:23], v[22:23] op_sel:[0,0,1] op_sel_hi:[1,0,1]\n" \
  "v_pk_fma_f32 v[10:11], v[10:11], v[22:23], v[22:23] op_sel:[0,0,1] op_sel_hi:[1,0,1]\n" \
  "v_pk_fma_f32 v[12:13], v[12:13], v[22:23], v[22:23] op_sel:[0,0,1] op_sel_hi:[1,0,1]\n" \
  "v_pk_fma_f32 v[14:15], v[14:15], v[22:23], v[22:23] op_sel:[0,0,1] op_sel_hi:[1,0,1]\n" \
  "v_pk_fma_f32 v[18:19], v[18:19], v[22:23], v[22:23] op_sel:[0,0,1] op_sel_hi:[1,0,1]\n" \
  "v_pk_fma_f32 v[20:21], v[20:21], v[22:23], v[22:23] op_sel:[0,0,1] op_sel_hi:[1,0,1]\n" \
  "v_cvt_pk_bf16_f32 v22, v2, v3\n" \
  "v_cvt_pk_bf16_f32 v23, v4, v5\n" \
  "v_cvt_pk_bf16_f32 v24, v6, v7\n" \
  "v_cvt_pk_bf16_f32 v25, v8, v9\n" \
  "ds_write_b128 v247, v[22:25] offset:0\n" \
  "v_cvt_pk_bf16_f32 v2, v10, v11\n" \
  "v_cvt_pk_bf16_f32 v3, v12, v13\n" \
  "v_cvt_pk_bf16_f32 v4, v14, v15\n" \
  "v_cvt_pk_bf16_f32 v5, v18, v19\n" \
  "ds_write_b128 v247, v[2:5] offset:512\n" \
  "v_cvt_pk_bf16_f32 v254, v20, v21\n" \
  "ds_write_b32 v246, v254 offset:1024\n" \
  "s_add_u32 m0, s57, 12288\n" \
  "s_add_u32 s31, s13, 9953280\n" \
  "buffer_load_dwordx4 v38, s[4:7], s31 offen offset:0 lds\n" \
  "buffer_load_dwordx4 v39, s[4:7], s31 offen offset:1024 lds\n" \
  "s_waitcnt lgkmcnt(0)\n" \
  "s_barrier\n" \
  "ds_read_b64_tr_b16 v[2:3], v253 offset:9216\n" \
  "ds_read_b64_tr_b16 v[4:5], v253 offset:11664\n" \
  "ds_read_b64_tr_b16 v[6:7], v253 offset:9728\n" \
  "ds_read_b64_tr_b16 v[8:9], v253 offset:12176\n" \
  "ds_read_b64_tr_b16 v[10:11], v253 offset:10240\n" \
  "ds_read_b64_tr_b16 v[12:13], v253 offset:12688\n" \
  "buffer_load_dwordx4 v40, s[4:7], s31 offen offset:2048 lds\n" \
  "buffer_load_dwordx4 v41, s[4:7], s31 offen offset:3072 lds\n" \
  "s_add_u32 s32, s11, 1048576\n" \
  "s_add_u32 s33, s11, 2097152\n" \
  "s_waitcnt lgkmcnt(4)\n" \
  "buffer_store_dwordx4 v[2:5], v240, s[36:39], s11 offen offset:0 sc1\n" \
  "s_waitcnt lgkmcnt(2)\n" \
  "buffer_store_dwordx4 v[6:9], v240, s[36:39], s32 offen offset:0 sc1\n" \
  "s_mov_b64 exec, 65535\n" \
  "s_waitcnt lgkmcnt(0)\n" \
  "buffer_store_dwordx4 v[10:13], v240, s[36:39], s33 offen offset:0 sc1\n" \
  "s_mov_b64 exec, -1\n" \
  ".Lsnc_%=:\n" \
  "v_xor_b32 v254, s18, v66\n" \
  "v_bitop3_b32 v254, v67, s19, v254 bitop3:0xbe\n" \
  "v_cmp_ne_u32 vcc, 0, v254\n" \
  "s_cbranch_vccnz .Lsns_%=\n" \
  ".Lsnd_%=:\n" \
  "s_add_u32 m0, s57, 16384\n" \
  "s_add_u32 s31, s14, 0\n" \
  "buffer_load_dwordx4 v252, s[36:39], s31 offen offset:0 sc1 lds\n" \
  "buffer_load_dwordx4 v252, s[36:39], s31 offen offset:1024 sc1 lds\n" \
  "buffer_load_dwordx4 v252, s[36:39], s31 offen offset:2048 sc1 lds\n" \
  "buffer_load_dwordx4 v252, s[36:39], s31 offen offset:3072 sc1 lds\n" \
  "s_add_u32 m0, s57, 20480\n" \
  "s_add_u32 s31, s14, 4096\n" \
  "buffer_load_dwordx4 v252, s[36:39], s31 offen offset:0 sc1 lds\n" \
  "buffer_load_dwordx4 v252, s[36:39], s31 offen offset:1024 sc1 lds\n" \
  "buffer_load_dwordx4 v252, s[36:39], s31 offen offset:2048 sc1 lds\n" \
  "buffer_load_dwordx4 v252, s[36:39], s31 offen offset:3072 sc1 lds\n" \
  "s_lshr_b32 s0, s2, 6\n" \
  "s_lshr_b32 s1, s2, 1\n" \
  "s_and_b32 s34, s1, 3\n" \
  "s_lshr_b32 s1, s2, 3\n" \
  "s_and_b32 s84, s1, 1\n" \
  "s_and_b32 s1, s2, 7\n" \
  "s_lshl_b32 s1, s1, 2\n" \
  "s_lshr_b32 s100, s2, 4\n" \
  "s_and_b32 s100, s100, 3\n" \
  "s_add_u32 s88, s1, s100\n" \
  "s_and_b32 s1, s2, 1\n" \
  "s_lshl_b32 s1, s1, 3\n" \
  "s_lshr_b32 s100, s2, 3\n" \
  "s_and_b32 s100, s100, 7\n" \
  "s_add_u32 s85, s1, s100\n" \
  "s_mul_i32 s1, s0, 4505600\n" \
  "s_add_u32 s52, s1, 12123648\n" \
  "s_add_u32 s53, s52, 3276800\n" \
  "s_add_u32 s83, s52, 2048000\n" \
  "s_lshl_b32 s1, s0, 5\n" \
  "s_add_u32 s1, s1, s88\n" \
  "s_lshl_b32 s1, s1, 1\n" \
  "s_add_u32 s1, s1, s84\n" \
  "s_lshl_b32 s1, s1, 7\n" \
  "s_lshl_b32 s100, s20, 5\n" \
  "s_add_u32 s1, s1, s100\n" \
  "s_add_u32 s54, s1, 262144\n" \
  "s_lshl_b32 s1, s20, 5\n" \
  "s_sub_u32 s1, s54, s1\n" \
  "s_lshl_b32 s100, s34, 11\n" \
  "s_sub_u32 s1, s1, s100\n" \
  "s_add_u32 s16, s1, 65536\n" \
  "s_mov_b32 s55, 3184710631\n" \
  "s_mul_i32 s56, s20, 9568\n" \
  "s_lshl_b32 s58, s20, 13\n" \
  "s_lshr_b32 s1, s88, 1\n" \
  "s_mul_i32 s90, s1, 18\n" \
  "s_and_b32 s1, s88, 1\n" \
  "s_lshl_b32 s1, s1, 3\n" \
  "s_add_u32 s1, s90, s1\n" \
  "s_lshl_b32 s100, s84, 2\n" \
  "s_add_u32 s1, s1, s100\n" \
  "s_add_u32 s1, s1, s20\n" \
  "s_add_u32 s1, s1, 1\n" \
  "s_lshl_b32 s63, s1, 9\n" \
  "s_and_b32 s1, s88, 1\n" \
  "s_add_u32 s1, s90, s1\n" \
  "s_add_u32 s1, s1, 17\n" \
  "s_lshl_b32 s1, s1, 9\n" \
  "s_lshl_b32 s100, s20, 7\n" \
  "s_add_u32 s64, s1, s100\n" \
  "s_lshl_b32 s1, s0, 5\n" \
  "s_lshl_b32 s100, s34, 3\n" \
  "s_add_u32 s1, s1, s100\n" \
  "s_lshl_b32 s100, s20, 1\n" \
  "s_add_u32 s1, s1, s100\n" \
  "s_lshl_b32 s1, s1, 8\n" \
  "s_add_u32 s1, s1, 262144\n" \
  "s_sub_u32 s59, s1, s63\n" \
  "s_add_u32 s75, s59, 196608\n" \
  "s_lshl_b32 s1, s85, 7\n" \
  "s_mul_i32 s1, s1, 576\n" \
  "s_mul_i32 s100, s0, 144\n" \
  "s_add_u32 s90, s1, s100\n" \
  "s_mul_i32 s1, s20, 9216\n" \
  "s_add_u32 s1, s90, s1\n" \
  "s_lshl_b32 s100, s34, 5\n" \
  "s_add_u32 s1, s1, s100\n" \
  "s_lshl_b32 s61, s1, 1\n" \
  "s_lshl_b32 s1, s34, 4\n" \
  "s_add_u32 s1, s1, s20\n" \
  "s_mul_i32 s1, s1, 576\n" \
  "s_add_u32 s1, s90, s1\n" \
  "s_add_u32 s1, s1, 128\n" \
  "s_lshl_b32 s62, s1, 1\n" \
  "v_and_b32 v254, 768, v252\n" \
  "v_lshlrev_b32 v254, 2, v254\n" \
  "v_and_b32 v253, 192, v252\n" \
  "v_lshl_or_b32 v254, v253, 1, v254\n" \
  "v_bfe_u32 v253, v191, 3, 2\n" \
  "v_lshl_or_b32 v254, v253, 5, v254\n" \
  "v_and_b32 v251, 3, v191\n" \
  "v_lshl_or_b32 v254, v251, 3, v254\n" \
  "v_add_u32 v8, s57, v254\n" \
  "v_mov_b32 v63, v8\n" \
  "v_lshl_or_b32 v9, v241, 16, v249\n" \
  "v_and_b32 v254, 12, v191\n" \
  "v_lshrrev_b32 v253, 4, v191\n" \
  "v_add_u32 v253, v253, v254\n" \
  "v_mul_u32_u24 v253, 1152, v253\n" \
  "v_lshl_add_u32 v253, v251, 3, v253\n" \
  "v_add_u32 v10, s61, v253\n" \
  "v_and_b32 v251, 1, v191\n" \
  "v_lshl_add_u32 v254, v251, 1, v254\n" \
  "v_bfe_u32 v253, v191, 1, 1\n" \
  "v_lshlrev_b32 v253, 6, v253\n" \
  "v_lshrrev_b32 v11, 2, v191\n" \
  "v_and_or_b32 v253, v11, 12, v253\n" \
  "v_mul_u32_u24 v253, 1152, v253\n" \
  "v_lshl_add_u32 v253, v254, 1, v253\n" \
  "v_add_u32 v11, s62, v253\n" \
  "v_lshrrev_b32 v254, 3, v252\n" \
  "v_bitop3_b32 v254, v254, 32, v252 bitop3:0x6a\n" \
  "v_lshl_or_b32 v254, v254, 5, v254\n" \
  "v_and_b32 v12, 28784, v254\n" \
  "v_xor_b32 v254, 64, v12\n" \
  "v_add_u32 v13, 31744, v254\n" \
  "v_lshlrev_b32 v14, 16, v245\n" \
  "v_add_u32 v254, s57, v252\n" \
  "v_add_u32 v22, 28672, v254\n" \
  "v_lshlrev_b32 v52, 16, v16\n" \
  "v_and_b32 v53, 4294901760, v16\n" \
  "v_lshlrev_b32 v54, 16, v17\n" \
  "v_and_b32 v55, 4294901760, v17\n" \
  "v_lshlrev_b32 v56, 16, v255\n" \
  "v_mov_b32 v57, 3222493025\n" \
  "s_mul_i32 s32, s20, 1056\n" \
  "v_bfe_u32 v254, v191, 1, 1\n" \
  "v_mul_u32_u24 v254, 544, v254\n" \
  "v_lshl_add_u32 v254, v251, 4, v254\n" \
  "v_bfe_u32 v253, v191, 2, 1\n" \
  "v_lshl_add_u32 v254, v253, 8, v254\n" \
  "v_bfe_u32 v253, v191, 3, 2\n" \
  "v_lshl_add_u32 v254, v253, 6, v254\n" \
  "v_and_b32 v253, 32, v191\n" \
  "v_add3_u32 v58, v254, v253, s32\n" \
  "v_add_u32 v59, 524288, v252\n" \
  "v_bfe_u32 v254, v191, 3, 2\n" \
  "v_mul_u32_u24 v254, 288, v254\n" \
  "v_lshl_add_u32 v254, v253, 1, v254\n" \
  "v_and_b32 v253, 7, v191\n" \
  "v_lshl_add_u32 v254, v253, 2, v254\n" \
  "s_and_b32 s32, s20, 1\n" \
  "s_lshl_b32 s32, s32, 7\n" \
  "s_lshr_b32 s33, s20, 1\n" \
  "s_lshl_b32 s33, s33, 5\n" \
  "s_add_u32 s32, s32, s33\n" \
  "s_add_u32 s32, s32, 4224\n" \
  "v_add_u32 v60, s32, v254\n" \
  "v_lshl_add_u32 v61, v191, 3, s63\n" \
  "v_lshl_add_u32 v62, v191, 1, s64\n" \
  "s_mul_i32 s1, s34, 1152\n" \
  "s_lshr_b32 s100, s20, 1\n" \
  "s_mul_i32 s100, s100, 576\n" \
  "s_add_u32 s1, s1, s100\n" \
  "s_lshl_b32 s1, s1, 12\n" \
  "s_lshl_b32 s100, s85, 8\n" \
  "s_add_u32 s65, s1, s100\n" \
  "s_and_b32 s1, s20, 1\n" \
  "s_lshl_b32 s1, s1, 20\n" \
  "s_add_u32 s1, s65, s1\n" \
  "s_add_u32 s60, s1, 655488\n" \
  "s_and_b32 s1, s20, 1\n" \
  "s_lshl_b32 s1, s1, 17\n" \
  "s_add_u32 s1, s65, s1\n" \
  "s_add_u32 s65, s1, 2097152\n" \
  "s_add_u32 s66, s65, 128\n" \
  "s_add_u32 s61, s60, 130944\n" \
  "s_add_u32 s62, s60, 131072\n" \
  "s_add_u32 s63, s60, 262016\n" \
  "s_add_u32 s64, s60, 262144\n" \
  "s_lshl_b32 s1, s34, 7\n" \
  "s_add_u32 s67, s1, 163200\n" \
  "s_lshl_b32 s1, s34, 2\n" \
  "s_add_u32 s98, s1, s20\n" \
  "s_mul_i32 s1, s98, 9216\n" \
  "s_add_u32 s1, s1, s52\n" \
  "s_add_u32 s68, s1, 819712\n" \
  "s_add_u32 s69, s68, 3072\n" \
  "s_add_u32 s70, s68, 6144\n" \
  "s_lshl_b32 s1, s85, 14\n" \
  "s_add_u32 s1, s1, s52\n" \
  "s_add_u32 s90, s1, 967168\n" \
  "s_lshl_b32 s1, s98, 8\n" \
  "s_add_u32 s79, s1, s90\n" \
  "s_add_u32 s80, s79, 4096\n" \
  "s_add_u32 s81, s79, 8192\n" \
  "s_add_u32 s82, s79, 12288\n" \
  "s_lshl_b32 s99, s20, 10\n" \
  "s_add_u32 s87, s99, 65536\n" \
  "s_add_u32 s90, s90, s99\n" \
  "s_lshl_b32 s1, s2, 2\n" \
  "s_add_u32 s1, s1, s20\n" \
  "s_lshl_b32 s1, s1, 8\n" \
  "s_add_u32 s91, s1, 69206016\n" \
  "s_mul_i32 s1, s0, 589824\n" \
  "s_lshl_b32 s100, s20, 14\n" \
  "s_add_u32 s1, s1, s100\n" \
  "s_add_u32 s48, s1, 9764864\n" \
  "s_sub_u32 s1, s34, 1\n" \
  "s_and_b32 s1, s1, 3\n" \
  "s_lshl_b32 s1, s1, 12\n" \
  "s_add_u32 s98, s1, s90\n" \
  "s_add_u32 s71, s98, 4292919296\n" \
  "s_add_u32 s76, s98, 4293328896\n" \
  "s_sub_u32 s1, s34, 2\n" \
  "s_and_b32 s1, s1, 3\n" \
  "s_lshl_b32 s1, s1, 12\n" \
  "s_add_u32 s98, s1, s90\n" \
  "s_add_u32 s72, s98, 4293328896\n" \
  "s_add_u32 s77, s98, 4293328896\n" \
  "s_sub_u32 s1, s34, 3\n" \
  "s_and_b32 s1, s1, 3\n" \
  "s_lshl_b32 s1, s1, 12\n" \
  "s_add_u32 s98, s1, s90\n" \
  "s_add_u32 s73, s98, 4293328896\n" \
  "s_add_u32 s78, s98, 4293738496\n" \
  "s_and_b32 s1, s34, 3\n" \
  "s_lshl_b32 s1, s1, 12\n" \
  "s_add_u32 s98, s1, s90\n" \
  "s_add_u32 s74, s98, 4294148096\n" \
  "s_and_b32 s1, s0, 1\n" \
  "s_lshl_b32 s98, s1, 13\n" \
  "s_sub_u32 s1, s48, s98\n" \
  "s_add_u32 s50, s1, 8192\n" \
  "s_add_u32 s51, s50, 4096\n" \
  "s_add_u32 s48, s48, s98\n" \
  "s_add_u32 s49, s48, 4096\n" \
  "s_lshr_b32 s1, s2, 3\n" \
  "s_lshl_b32 s1, s1, 2\n" \
  "s_add_u32 s98, s1, s20\n" \
  "s_and_b32 s1, s98, 31\n" \
  "s_lshl_b32 s1, s1, 2\n" \
  "s_add_u32 s90, s1, 448\n" \
  "s_and_b32 s1, s98, 8\n" \
  "s_lshl_b32 s1, s1, 5\n" \
  "s_and_b32 s100, s98, 7\n" \
  "s_lshl_b32 s100, s100, 2\n" \
  "s_add_u32 s1, s1, s100\n" \
  "s_add_u32 s89, s1, 160\n" \
  "s_and_b32 s1, s98, 15\n" \
  "s_lshl_b32 s1, s1, 2\n" \
  "s_add_u32 s86, s1, 192\n" \
  "s_bitcmp1_b32 s98, 4\n" \
  "s_cselect_b32 s89, s89, s86\n" \
  "s_bitcmp1_b32 s98, 5\n" \
  "s_cselect_b32 s89, s90, s89\n" \
  "s_lshr_b32 s1, s98, 6\n" \
  "s_mul_i32 s1, s1, 576\n" \
  "s_mul_i32 s100, s34, 1152\n" \
  "s_add_u32 s1, s1, s100\n" \
  "s_add_u32 s1, s1, s89\n" \
  "s_lshl_b32 s1, s1, 12\n" \
  "s_and_b32 s100, s2, 1\n" \
  "s_lshl_b32 s100, s100, 11\n" \
  "s_add_u32 s1, s1, s100\n" \
  "s_lshr_b32 s100, s2, 3\n" \
  "s_and_b32 s100, s100, 3\n" \
  "s_add_u32 s1, s1, s100\n" \
  "s_add_u32 s89, s1, 1\n" \
  "s_sub_u32 s98, s57, s99\n" \
  "s_lshl_b32 s1, s34, 17\n" \
  "s_add_u32 s85, s1, 65536\n" \
  "s_mov_b32 s90, 7405568\n" \
  "s_add_u32 s1, s0, 3\n" \
  "s_and_b32 s1, s1, 3\n" \
  "s_lshl_b32 s88, s1, 12\n" \
  "s_mov_b32 s30, s16\n" \
  "v_mov_b32 v2, v12\n" \
  "v_mov_b32 v3, v13\n" \
  "v_add_u32 v4, 63488, v12\n" \
  "v_add_u32 v5, 63488, v13\n" \
  "v_mov_b32 v190, 1\n" \
  "v_lshrrev_b32 v189, 1, v252\n" \
  "v_add_u32 v255, s57, v189\n" \
  "s_lshl_b32 s32, s20, 11\n" \
  "v_add_u32 v189, s32, v189\n" \
  "s_waitcnt vmcnt(19)\n" \
  "ds_read_b64_tr_b16 a[240:241], v26 offset:0\n" \
  "ds_read_b64_tr_b16 a[244:245], v27 offset:0\n" \
  "ds_read_b64_tr_b16 a[248:249], v28 offset:0\n" \
  "ds_read_b64_tr_b16 a[252:253], v29 offset:0\n" \
  "ds_read_b64_tr_b16 v[196:197], v26 offset:2048\n" \
  "ds_read_b64_tr_b16 v[200:201], v27 offset:2048\n" \
  "ds_read_b64_tr_b16 v[204:205], v28 offset:2048\n" \
  "ds_read_b64_tr_b16 v[208:209], v29 offset:2048\n" \
  "ds_read_b64_tr_b16 a[242:243], v26 offset:4096\n" \
  "ds_read_b64_tr_b16 a[246:247], v27 offset:4096\n" \
  "ds_read_b64_tr_b16 a[250:251], v28 offset:4096\n" \
  "ds_read_b64_tr_b16 a[254:255], v29 offset:4096\n" \
  "ds_read_b64_tr_b16 v[198:199], v26 offset:6144\n" \
  "ds_read_b64_tr_b16 v[202:203], v27 offset:6144\n" \
  "ds_read_b64_tr_b16 v[206:207], v28 offset:6144\n" \
  "ds_read_b64_tr_b16 v[210:211], v29 offset:6144\n" \
  "s_waitcnt vmcnt(11)\n" \
  "ds_read_b64_tr_b16 v[216:217], v26 offset:8192\n" \
  "ds_read_b64_tr_b16 v[220:221], v27 offset:8192\n" \
  "ds_read_b64_tr_b16 v[224:225], v28 offset:8192\n" \
  "ds_read_b64_tr_b16 v[228:229], v29 offset:8192\n" \
  "ds_read_b64_tr_b16 v[236:237], v26 offset:10240\n" \
  "ds_read_b64_tr_b16 v[240:241], v27 offset:10240\n" \
  "ds_read_b64_tr_b16 v[244:245], v28 offset:10240\n" \
  "ds_read_b64_tr_b16 v[248:249], v29 offset:10240\n" \
  "ds_read_b64_tr_b16 v[218:219], v26 offset:12288\n" \
  "ds_read_b64_tr_b16 v[222:223], v27 offset:12288\n" \
  "ds_read_b64_tr_b16 v[226:227], v28 offset:12288\n" \
  "ds_read_b64_tr_b16 v[230:231], v29 offset:12288\n" \
  "ds_read_b64_tr_b16 v[238:239], v26 offset:14336\n" \
  "ds_read_b64_tr_b16 v[242:243], v27 offset:14336\n" \
  "ds_read_b64_tr_b16 v[246:247], v28 offset:14336\n" \
  "ds_read_b64_tr_b16 v[250:251], v29 offset:14336\n" \
  "s_waitcnt vmcnt(8)\n" \
  "s_mov_b64 exec, 1\n" \
  "buffer_store_dword v190, off, s[36:39], s28 sc1\n" \
  "s_mov_b64 exec, -1\n" \
  "s_add_u32 m0, s57, 24576\n" \
  "s_add_u32 s31, s15, 8192\n" \
  "buffer_load_dwordx4 v252, s[36:39], s31 offen offset:0 sc1 lds\n" \
  "buffer_load_dwordx4 v252, s[36:39], s31 offen offset:1024 sc1 lds\n" \
  "buffer_load_dwordx4 v252, s[36:39], s31 offen offset:2048 sc1 lds\n" \
  "buffer_load_dwordx4 v252, s[36:39], s31 offen offset:3072 sc1 lds\n" \
  "s_add_u32 m0, s57, 28672\n" \
  "s_add_u32 s31, s15, 12288\n" \
  "buffer_load_dwordx4 v252, s[36:39], s31 offen offset:0 sc1 lds\n" \
  "buffer_load_dwordx4 v252, s[36:39], s31 offen offset:1024 sc1 lds\n" \
  "s_lshl_b32 m0, s20, 11\n" \
  "s_add_u32 s31, s31, 2048\n" \
  "buffer_load_dwordx4 v252, s[36:39], s31 offen offset:0 sc1 lds\n" \
  "buffer_load_dwordx4 v252, s[36:39], s31 offen offset:1024 sc1 lds\n" \
  "s_bfe_u32 s32, s27, 131076\n" \
  "s_sub_u32 s100, s60, 655488\n" \
  "s_sub_u32 s101, s60, 720896\n" \
  "s_lshl_b32 s32, s32, 12\n" \
  "s_add_u32 s32, s32, 0\n" \
  "s_cmp_ge_u32 s32, 16384\n" \
  "s_cselect_b32 s32, 0, s32\n" \
  "s_bitcmp1_b32 s32, 12\n" \
  "s_cselect_b32 s33, s101, s100\n" \
  "s_lshl4_add_u32 s31, s32, s33\n" \
  "s_add_u32 s33, s32, s57\n" \
  "s_add_u32 m0, s33, 0\n" \
  "s_waitcnt lgkmcnt(0)\n" \
  "buffer_load_dwordx4 v2, s[40:43], s31 offen offset:0 lds\n" \
  "buffer_load_dwordx4 v3, s[40:43], s31 offen offset:1024 lds\n" \
  "buffer_load_dwordx4 v4, s[40:43], s31 offen offset:2048 lds\n" \
  "buffer_load_dwordx4 v5, s[40:43], s31 offen offset:3072 lds\n" \
  "s_add_u32 s32, s32, 4096\n" \
  "s_cmp_ge_u32 s32, 16384\n" \
  "s_cselect_b32 s32, 0, s32\n" \
  "s_bitcmp1_b32 s32, 12\n" \
  "s_cselect_b32 s33, s101, s100\n" \
  "s_lshl4_add_u32 s31, s32, s33\n" \
  "s_add_u32 s33, s32, s57\n" \
  "s_add_u32 m0, s33, 0\n" \
  "s_nop 0\n" \
  "buffer_load_dwordx4 v2, s[40:43], s31 offen offset:0 lds\n" \
  "buffer_load_dwordx4 v3, s[40:43], s31 offen offset:1024 lds\n" \
  "buffer_load_dwordx4 v4, s[40:43], s31 offen offset:2048 lds\n" \
  "buffer_load_dwordx4 v5, s[40:43], s31 offen offset:3072 lds\n" \
  "s_add_u32 s32, s32, 4096\n" \
  "s_cmp_ge_u32 s32, 16384\n" \
  "s_cselect_b32 s32, 0, s32\n" \
  "s_bitcmp1_b32 s32, 12\n" \
  "s_cselect_b32 s33, s101, s100\n" \
  "s_lshl4_add_u32 s31, s32, s33\n" \
  "s_add_u32 s33, s32, s57\n" \
  "s_add_u32 m0, s33, 0\n" \
  "s_nop 0\n" \
  "buffer_load_dwordx4 v2, s[40:43], s31 offen offset:0 lds\n" \
  "buffer_load_dwordx4 v3, s[40:43], s31 offen offset:1024 lds\n" \
  "buffer_load_dwordx4 v4, s[40:43], s31 offen offset:2048 lds\n" \
  "buffer_load_dwordx4 v5, s[40:43], s31 offen offset:3072 lds\n" \
  "s_add_u32 s32, s32, 4096\n" \
  "s_cmp_ge_u32 s32, 16384\n" \
  "s_cselect_b32 s32, 0, s32\n" \
  "s_bitcmp1_b32 s32, 12\n" \
  "s_cselect_b32 s33, s101, s100\n" \
  "s_lshl4_add_u32 s31, s32, s33\n" \
  "s_add_u32 s33, s32, s57\n" \
  "s_add_u32 m0, s33, 0\n" \
  "s_nop 0\n" \
  "buffer_load_dwordx4 v2, s[40:43], s31 offen offset:0 lds\n" \
  "buffer_load_dwordx4 v3, s[40:43], s31 offen offset:1024 lds\n" \
  "buffer_load_dwordx4 v4, s[40:43], s31 offen offset:2048 lds\n" \
  "buffer_load_dwordx4 v5, s[40:43], s31 offen offset:3072 lds\n" \
  "s_waitcnt vmcnt(16)\n" \
  "ds_read_b64_tr_b16 a[16:17], v255 offset:16384\n" \
  "ds_read_b64_tr_b16 a[36:37], v255 offset:16896\n" \
  "ds_read_b64_tr_b16 a[18:19], v255 offset:17408\n" \
  "ds_read_b64_tr_b16 a[38:39], v255 offset:17920\n" \
  "ds_read_b64_tr_b16 a[56:57], v255 offset:18432\n" \
  "ds_read_b64_tr_b16 a[76:77], v255 offset:18944\n" \
  "ds_read_b64_tr_b16 a[58:59], v255 offset:19456\n" \
  "ds_read_b64_tr_b16 a[78:79], v255 offset:19968\n" \
  "ds_read_b64_tr_b16 a[96:97], v255 offset:20480\n" \
  "ds_read_b64_tr_b16 a[116:117], v255 offset:20992\n" \
  "ds_read_b64_tr_b16 a[98:99], v255 offset:21504\n" \
  "ds_read_b64_tr_b16 a[118:119], v255 offset:22016\n" \
  "ds_read_b64_tr_b16 a[136:137], v255 offset:22528\n" \
  "ds_read_b64_tr_b16 a[156:157], v255 offset:23040\n" \
  "ds_read_b64_tr_b16 a[138:139], v255 offset:23552\n" \
  "ds_read_b64_tr_b16 a[158:159], v255 offset:24064\n" \
  "ds_read_b64_tr_b16 a[176:177], v255 offset:24576\n" \
  "ds_read_b64_tr_b16 a[196:197], v255 offset:25088\n" \
  "ds_read_b64_tr_b16 a[178:179], v255 offset:25600\n" \
  "ds_read_b64_tr_b16 a[198:199], v255 offset:26112\n" \
  "ds_read_b64_tr_b16 a[216:217], v255 offset:26624\n" \
  "ds_read_b64_tr_b16 a[236:237], v255 offset:27136\n" \
  "ds_read_b64_tr_b16 a[218:219], v255 offset:27648\n" \
  "ds_read_b64_tr_b16 a[238:239], v255 offset:28160\n" \
  "ds_read_b64_tr_b16 v[192:193], v255 offset:28672\n" \
  "ds_read_b64_tr_b16 v[212:213], v255 offset:29184\n" \
  "ds_read_b64_tr_b16 v[194:195], v255 offset:29696\n" \
  "ds_read_b64_tr_b16 v[214:215], v255 offset:30208\n" \
  "ds_read_b64_tr_b16 v[232:233], v189 offset:0\n" \
  "ds_read_b64_tr_b16 v[252:253], v189 offset:512\n" \
  "ds_read_b64_tr_b16 v[234:235], v189 offset:1024\n" \
  "ds_read_b64_tr_b16 v[254:255], v189 offset:1536\n" \
  "v_and_b32 v189, 12, v191\n" \
  "v_and_b32 v190, 1, v191\n" \
  "v_lshl_add_u32 v189, v190, 1, v189\n" \
  "v_lshlrev_b32 v189, 2, v189\n" \
  "v_add_u32 v189, 163712, v189\n" \
  "ds_read_b64 v[6:7], v189 offset:0\n" \
  "s_bfe_u32 s32, s27, 131076\n" \
  "s_sub_u32 s100, s60, 131200\n" \
  "s_sub_u32 s101, s60, 196608\n" \
  "s_lshl_b32 s32, s32, 12\n" \
  "s_add_u32 s32, s32, 0\n" \
  "s_cmp_ge_u32 s32, 12288\n" \
  "s_cselect_b32 s32, 0, s32\n" \
  "s_bitcmp1_b32 s32, 12\n" \
  "s_cselect_b32 s33, s101, s100\n" \
  "s_lshl4_add_u32 s31, s32, s33\n" \
  "s_add_u32 s33, s32, s57\n" \
  "s_add_u32 m0, s33, 16384\n" \
  "s_waitcnt lgkmcnt(1)\n" \
  "buffer_load_dwordx4 v2, s[40:43], s31 offen offset:0 lds\n" \
  "buffer_load_dwordx4 v3, s[40:43], s31 offen offset:1024 lds\n" \
  "buffer_load_dwordx4 v4, s[40:43], s31 offen offset:2048 lds\n" \
  "buffer_load_dwordx4 v5, s[40:43], s31 offen offset:3072 lds\n" \
  "s_add_u32 s32, s32, 4096\n" \
  "s_cmp_ge_u32 s32, 12288\n" \
  "s_cselect_b32 s32, 0, s32\n" \
  "s_bitcmp1_b32 s32, 12\n" \
  "s_cselect_b32 s33, s101, s100\n" \
  "s_lshl4_add_u32 s31, s32, s33\n" \
  "s_add_u32 s33, s32, s57\n" \
  "s_add_u32 m0, s33, 16384\n" \
  "s_nop 0\n" \
  "buffer_load_dwordx4 v2, s[40:43], s31 offen offset:0 lds\n" \
  "buffer_load_dwordx4 v3, s[40:43], s31 offen offset:1024 lds\n" \
  "buffer_load_dwordx4 v4, s[40:43], s31 offen offset:2048 lds\n" \
  "buffer_load_dwordx4 v5, s[40:43], s31 offen offset:3072 lds\n" \
  "s_add_u32 s32, s32, 4096\n" \
  "s_cmp_ge_u32 s32, 12288\n" \
  "s_cselect_b32 s32, 0, s32\n" \
  "s_bitcmp1_b32 s32, 12\n" \
  "s_cselect_b32 s33, s101, s100\n" \
  "s_lshl4_add_u32 s31, s32, s33\n" \
  "s_add_u32 s33, s32, s57\n" \
  "s_add_u32 m0, s33, 16384\n" \
  "s_nop 0\n" \
  "buffer_load_dwordx4 v2, s[40:43], s31 offen offset:0 lds\n" \
  "buffer_load_dwordx4 v3, s[40:43], s31 offen offset:1024 lds\n" \
  "buffer_load_dwordx4 v4, s[40:43], s31 offen offset:2048 lds\n" \
  "buffer_load_dwordx4 v5, s[40:43], s31 offen offset:3072 lds\n" \
  "s_waitcnt lgkmcnt(0)\n" \
  "v_lshrrev_b32 v6, 16, v6\n" \
  "v_or_b32 v15, v6, v7\n" \
  "s_branch .Lgo_%=\n" \
  ".Lans_%=:\n" \
  "v_xor_b32 v235, s18, v46\n" \
  "v_bitop3_b32 v235, v47, s19, v235 bitop3:0xbe\n" \
  "s_mov_b32 s32, 8192\n" \
  "v_cmpx_ne_u32 vcc, 0, v235\n" \
  "buffer_load_dwordx4 v[46:49], v243, s[36:39], s32 offen sc1\n" \
  "s_mov_b64 exec, -1\n" \
  "v_xor_b32 v235, s18, v50\n" \
  "v_bitop3_b32 v235, v51, s19, v235 bitop3:0xbe\n" \
  "s_mov_b32 s32, 17408\n" \
  "v_cmpx_ne_u32 vcc, 0, v235\n" \
  "buffer_load_dwordx4 v[50:53], v243, s[36:39], s32 offen sc1\n" \
  "s_mov_b64 exec, -1\n" \
  "v_xor_b32 v235, s18, v54\n" \
  "v_bitop3_b32 v235, v55, s19, v235 bitop3:0xbe\n" \
  "s_mov_b32 s32, 26624\n" \
  "v_cmpx_ne_u32 vcc, 0, v235\n" \
  "buffer_load_dwordx4 v[54:57], v243, s[36:39], s32 offen sc1\n" \
  "s_mov_b64 exec, -1\n" \
  "v_xor_b32 v235, s18, v58\n" \
  "v_bitop3_b32 v235, v59, s19, v235 bitop3:0xbe\n" \
  "s_mov_b32 s32, 12288\n" \
  "v_cmpx_ne_u32 vcc, 0, v235\n" \
  "buffer_load_dwordx4 v[58:61], v243, s[36:39], s32 offen sc1\n" \
  "s_mov_b64 exec, -1\n" \
  "v_xor_b32 v235, s18, v62\n" \
  "v_bitop3_b32 v235, v63, s19, v235 bitop3:0xbe\n" \
  "s_mov_b32 s32, 21504\n" \
  "v_cmpx_ne_u32 vcc, 0, v235\n" \
  "buffer_load_dwordx4 v[62:65], v243, s[36:39], s32 offen sc1\n" \
  "s_mov_b64 exec, -1\n" \
  "v_xor_b32 v235, s18, v66\n" \
  "v_bitop3_b32 v235, v67, s19, v235 bitop3:0xbe\n" \
  "s_mov_b32 s32, 30720\n" \
  "v_cmpx_ne_u32 vcc, 0, v235\n" \
  "buffer_load_dwordx4 v[66:69], v243, s[36:39], s32 offen sc1\n" \
  "s_mov_b64 exec, -1\n" \
  "v_xor_b32 v235, s18, v70\n" \
  "v_bitop3_b32 v235, v71, s19, v235 bitop3:0xbe\n" \
  "s_mov_b32 s32, 16384\n" \
  "v_cmpx_ne_u32 vcc, 0, v235\n" \
  "buffer_load_dwordx4 v[70:73], v242, s[36:39], s32 offen sc1\n" \
  "s_mov_b64 exec, -1\n" \
  "v_xor_b32 v235, s18, v74\n" \
  "v_bitop3_b32 v235, v75, s19, v235 bitop3:0xbe\n" \
  "s_mov_b32 s32, 25600\n" \
  "v_cmpx_ne_u32 vcc, 0, v235\n" \
  "buffer_load_dwordx4 v[74:77], v242, s[36:39], s32 offen sc1\n" \
  "s_mov_b64 exec, -1\n" \
  "v_xor_b32 v235, s18, v78\n" \
  "v_bitop3_b32 v235, v79, s19, v235 bitop3:0xbe\n" \
  "s_mov_b32 s32, 34816\n" \
  "v_cmpx_ne_u32 vcc, 0, v235\n" \
  "buffer_load_dwordx4 v[78:81], v242, s[36:39], s32 offen sc1\n" \
  "s_mov_b64 exec, -1\n" \
  "s_memrealtime s[100:101]\n" \
  "s_waitcnt vmcnt(0) lgkmcnt(0)\n" \
  "s_sub_u32 s100, s100, s17\n" \
  "s_cmp_lt_u32 s100, 0x4c4b40\n" \
  "s_cbranch_scc1 .Lanc_%=\n" \
  "s_branch .Land_%=\n" \
  ".Lyns_%=:\n" \
  "s_sleep 2\n" \
  "s_movk_i32 s32, 4096\n" \
  "buffer_load_dwordx4 v[2:5], v252, s[36:39], s32 offen sc1\n" \
  "buffer_load_dwordx4 v[6:9], v252, s[36:39], s32 offen offset:1024 sc1\n" \
  "s_memrealtime s[100:101]\n" \
  "s_waitcnt vmcnt(0) lgkmcnt(0)\n" \
  "s_sub_u32 s100, s100, s17\n" \
  "s_cmp_lt_u32 s100, 0x4c4b40\n" \
  "s_cbranch_scc1 .Lync_%=\n" \
  "s_branch .Lynd_%=\n" \
  ".Lc3s_%=:\n" \
  "s_add_u32 m0, s57, 0\n" \
  "s_add_u32 s31, s12, 0\n" \
  "buffer_load_dwordx4 v38, s[4:7], s31 offen offset:0 lds\n" \
  "buffer_load_dwordx4 v39, s[4:7], s31 offen offset:1024 lds\n" \
  "buffer_load_dwordx4 v40, s[4:7], s31 offen offset:2048 lds\n" \
  "buffer_load_dwordx4 v41, s[4:7], s31 offen offset:3072 lds\n" \
  "s_add_u32 m0, s57, 4096\n" \
  "s_add_u32 s31, s12, 9437184\n" \
  "buffer_load_dwordx4 v38, s[4:7], s31 offen offset:0 lds\n" \
  "buffer_load_dwordx4 v39, s[4:7], s31 offen offset:1024 lds\n" \
  "buffer_load_dwordx4 v40, s[4:7], s31 offen offset:2048 lds\n" \
  "buffer_load_dwordx4 v41, s[4:7], s31 offen offset:3072 lds\n" \
  "s_branch .Lc3d_%=\n" \
  ".Lrns_%=:\n" \
  "s_add_u32 s32, s8, 4096\n" \
  "v_and_b32 v243, 1, v54\n" \
  "v_cmpx_eq_u32 vcc, 0, v243\n" \
  "buffer_load_dwordx4 v[54:57], v231, s[36:39], s8 offen sc1\n" \
  "s_mov_b64 exec, -1\n" \
  "v_and_b32 v243, 1, v58\n" \
  "v_cmpx_eq_u32 vcc, 0, v243\n" \
  "buffer_load_dwordx4 v[58:61], v231, s[36:39], s32 offen sc1\n" \
  "s_mov_b64 exec, -1\n" \
  "v_and_b32 v243, 1, v62\n" \
  "v_cmpx_eq_u32 vcc, 0, v243\n" \
  "buffer_load_dwordx4 v[62:65], v248, s[36:39], s9 offen sc1\n" \
  "s_mov_b64 exec, -1\n" \
  "s_memrealtime s[100:101]\n" \
  "s_waitcnt vmcnt(0) lgkmcnt(0)\n" \
  "s_sub_u32 s100, s100, s17\n" \
  "s_cmp_lt_u32 s100, 0x4c4b40\n" \
  "s_cbranch_scc1 .Lrnc_%=\n" \
  "s_branch .Lrnd_%=\n" \
  ".Lmns_%=:\n" \
  "s_sleep 2\n" \
  "s_mov_b32 s32, 35840\n" \
  "s_mov_b32 s33, 40448\n" \
  "buffer_load_dwordx4 v[22:25], v238, s[36:39], s32 offen offset:0 sc1\n" \
  "buffer_load_dwordx4 v[42:45], v238, s[36:39], s32 offen offset:2304 sc1\n" \
  "buffer_load_dwordx4 v[46:49], v238, s[36:39], s33 offen offset:0 sc1\n" \
  "buffer_load_dwordx4 v[50:53], v238, s[36:39], s33 offen offset:2304 sc1\n" \
  "s_memrealtime s[100:101]\n" \
  "s_waitcnt vmcnt(0) lgkmcnt(0)\n" \
  "s_sub_u32 s100, s100, s17\n" \
  "s_cmp_lt_u32 s100, 0x4c4b40\n" \
  "s_cbranch_scc1 .Lmnc_%=\n" \
  "s_branch .Lmnd_%=\n" \
  ".Lsns_%=:\n" \
  "buffer_load_dwordx2 v[66:67], v242, s[36:39], s29 offen sc1\n" \
  "s_memrealtime s[100:101]\n" \
  "s_waitcnt vmcnt(0) lgkmcnt(0)\n" \
  "s_sub_u32 s100, s100, s17\n" \
  "s_cmp_lt_u32 s100, 0x4c4b40\n" \
  "s_cbranch_scc1 .Lsnc_%=\n" \
  "s_branch .Lsnd_%=\n" \
  ".Lacs_%=:\n" \
  "v_xor_b32 v248, s18, v66\n" \
  "v_bitop3_b32 v248, v67, s19, v248 bitop3:0xbe\n" \
  "s_mov_b32 s32, 8192\n" \
  "v_cmpx_ne_u32 vcc, 0, v248\n" \
  "buffer_load_dwordx4 v[66:69], v246, s[36:39], s32 offen sc1\n" \
  "s_mov_b64 exec, -1\n" \
  "v_xor_b32 v248, s18, v70\n" \
  "v_bitop3_b32 v248, v71, s19, v248 bitop3:0xbe\n" \
  "s_mov_b32 s32, 17408\n" \
  "v_cmpx_ne_u32 vcc, 0, v248\n" \
  "buffer_load_dwordx4 v[70:73], v246, s[36:39], s32 offen sc1\n" \
  "s_mov_b64 exec, -1\n" \
  "v_xor_b32 v248, s18, v74\n" \
  "v_bitop3_b32 v248, v75, s19, v248 bitop3:0xbe\n" \
  "s_mov_b32 s32, 26624\n" \
  "v_cmpx_ne_u32 vcc, 0, v248\n" \
  "buffer_load_dwordx4 v[74:77], v246, s[36:39], s32 offen sc1\n" \
  "s_mov_b64 exec, -1\n" \
  "v_xor_b32 v248, s18, v78\n" \
  "v_bitop3_b32 v248, v79, s19, v248 bitop3:0xbe\n" \
  "s_mov_b32 s32, 12288\n" \
  "v_cmpx_ne_u32 vcc, 0, v248\n" \
  "buffer_load_dwordx4 v[78:81], v246, s[36:39], s32 offen sc1\n" \
  "s_mov_b64 exec, -1\n" \
  "v_xor_b32 v248, s18, v82\n" \
  "v_bitop3_b32 v248, v83, s19, v248 bitop3:0xbe\n" \
  "s_mov_b32 s32, 21504\n" \
  "v_cmpx_ne_u32 vcc, 0, v248\n" \
  "buffer_load_dwordx4 v[82:85], v246, s[36:39], s32 offen sc1\n" \
  "s_mov_b64 exec, -1\n" \
  "v_xor_b32 v248, s18, v86\n" \
  "v_bitop3_b32 v248, v87, s19, v248 bitop3:0xbe\n" \
  "s_mov_b32 s32, 30720\n" \
  "v_cmpx_ne_u32 vcc, 0, v248\n" \
  "buffer_load_dwordx4 v[86:89], v246, s[36:39], s32 offen sc1\n" \
  "s_mov_b64 exec, -1\n" \
  "v_xor_b32 v248, s18, v90\n" \
  "v_bitop3_b32 v248, v91, s19, v248 bitop3:0xbe\n" \
  "s_mov_b32 s32, 16384\n" \
  "v_cmpx_ne_u32 vcc, 0, v248\n" \
  "buffer_load_dwordx4 v[90:93], v245, s[36:39], s32 offen sc1\n" \
  "s_mov_b64 exec, -1\n" \
  "v_xor_b32 v248, s18, v94\n" \
  "v_bitop3_b32 v248, v95, s19, v248 bitop3:0xbe\n" \
  "s_mov_b32 s32, 25600\n" \
  "v_cmpx_ne_u32 vcc, 0, v248\n" \
  "buffer_load_dwordx4 v[94:97], v245, s[36:39], s32 offen sc1\n" \
  "s_mov_b64 exec, -1\n" \
  "v_xor_b32 v248, s18, v98\n" \
  "v_bitop3_b32 v248, v99, s19, v248 bitop3:0xbe\n" \
  "s_mov_b32 s32, 34816\n" \
  "v_cmpx_ne_u32 vcc, 0, v248\n" \
  "buffer_load_dwordx4 v[98:101], v245, s[36:39], s32 offen sc1\n" \
  "s_mov_b64 exec, -1\n" \
  "s_memrealtime s[100:101]\n" \
  "s_waitcnt vmcnt(0) lgkmcnt(0)\n" \
  "s_sub_u32 s100, s100, s17\n" \
  "s_cmp_lt_u32 s100, 0x4c4b40\n" \
  "s_cbranch_scc1 .Lacc_%=\n" \
  "s_branch .Lacd_%=\n" \
  ".Lycs_%=:\n" \
  "s_sleep 2\n" \
  "s_movk_i32 s32, 4096\n" \
  "buffer_load_dwordx4 v[42:45], v252, s[36:39], s32 offen sc1\n" \
  "buffer_load_dwordx4 v[46:49], v252, s[36:39], s32 offen offset:1024 sc1\n" \
  "s_memrealtime s[100:101]\n" \
  "s_waitcnt vmcnt(0) lgkmcnt(0)\n" \
  "s_sub_u32 s100, s100, s17\n" \
  "s_cmp_lt_u32 s100, 0x4c4b40\n" \
  "s_cbranch_scc1 .Lycc_%=\n" \
  "s_branch .Lycd_%=\n" \
  ".Lws_%=:\n" \
  "s_lshl_b32 s33, s20, 10\n" \
  "s_add_u32 m0, s33, 85376\n" \
  "s_mov_b32 s32, 2925632\n" \
  "buffer_load_dwordx4 v225, s[36:39], s32 offen sc1 lds\n" \
  "s_add_u32 m0, s33, 89472\n" \
  "s_mov_b32 s32, 3523648\n" \
  "buffer_load_dwordx4 v225, s[36:39], s32 offen sc1 lds\n" \
  "s_add_u32 m0, s33, 116096\n" \
  "s_mov_b32 s32, 4121664\n" \
  "buffer_load_dwordx4 v225, s[36:39], s32 offen sc1 lds\n" \
  "s_add_u32 m0, s33, 120192\n" \
  "s_mov_b32 s32, 4719680\n" \
  "buffer_load_dwordx4 v225, s[36:39], s32 offen sc1 lds\n" \
  "s_branch .Lwd_%=\n" \
  ".Lxs_%=:\n" \
  "v_bfe_u32 v245, v191, 5, 1\n" \
  "v_mul_u32_u24 v245, 22528, v245\n" \
  "v_add_u32 v255, v112, v245\n" \
  "v_sub_u32 v248, v113, v245\n" \
  "v_add_u32 v248, 55296, v248\n" \
  "v_add_u32 v255, 0x8000, v255\n" \
  "ds_read_b64 v[14:15], v248 offset:39296\n" \
  "ds_read_b64 v[26:27], v248 offset:38272\n" \
  "ds_read_b64 v[28:29], v248 offset:37248\n" \
  "ds_read_b64 v[30:31], v248 offset:36224\n" \
  "ds_read_b64 v[32:33], v255 offset:36224\n" \
  "ds_read_b64 v[34:35], v255 offset:37248\n" \
  "ds_read_b64 v[36:37], v255 offset:38272\n" \
  "ds_read_b64 v[38:39], v255 offset:39296\n" \
  "s_waitcnt lgkmcnt(0)\n" \
  "v_lshlrev_b32 v245, 16, v15\n" \
  "v_add_f32 v40, v40, v245\n" \
  "v_and_b32 v245, 0xffff0000, v15\n" \
  "v_sub_f32 v44, v44, v245\n" \
  "v_lshlrev_b32 v245, 16, v32\n" \
  "v_add_f32 v66, v66, v245\n" \
  "v_and_b32 v245, 0xffff0000, v32\n" \
  "v_sub_f32 v70, v70, v245\n" \
  "v_lshlrev_b32 v245, 16, v14\n" \
  "v_add_f32 v82, v82, v245\n" \
  "v_and_b32 v245, 0xffff0000, v14\n" \
  "v_sub_f32 v86, v86, v245\n" \
  "v_lshlrev_b32 v245, 16, v33\n" \
  "v_add_f32 v90, v90, v245\n" \
  "v_and_b32 v245, 0xffff0000, v33\n" \
  "v_sub_f32 v94, v94, v245\n" \
  "v_lshlrev_b32 v245, 16, v27\n" \
  "v_add_f32 v41, v41, v245\n" \
  "v_and_b32 v245, 0xffff0000, v27\n" \
  "v_sub_f32 v45, v45, v245\n" \
  "v_lshlrev_b32 v245, 16, v34\n" \
  "v_add_f32 v67, v67, v245\n" \
  "v_and_b32 v245, 0xffff0000, v34\n" \
  "v_sub_f32 v71, v71, v245\n" \
  "v_lshlrev_b32 v245, 16, v26\n" \
  "v_add_f32 v83, v83, v245\n" \
  "v_and_b32 v245, 0xffff0000, v26\n" \
  "v_sub_f32 v87, v87, v245\n" \
  "v_lshlrev_b32 v245, 16, v35\n" \
  "v_add_f32 v91, v91, v245\n" \
  "v_and_b32 v245, 0xffff0000, v35\n" \
  "v_sub_f32 v95, v95, v245\n" \
  "v_lshlrev_b32 v245, 16, v29\n" \
  "v_add_f32 v42, v42, v245\n" \
  "v_and_b32 v245, 0xffff0000, v29\n" \
  "v_sub_f32 v46, v46, v245\n" \
  "v_lshlrev_b32 v245, 16, v36\n" \
  "v_add_f32 v68, v68, v245\n" \
  "v_and_b32 v245, 0xffff0000, v36\n" \
  "v_sub_f32 v72, v72, v245\n" \
  "v_lshlrev_b32 v245, 16, v28\n" \
  "v_add_f32 v84, v84, v245\n" \
  "v_and_b32 v245, 0xffff0000, v28\n" \
  "v_sub_f32 v88, v88, v245\n" \
  "v_lshlrev_b32 v245, 16, v37\n" \
  "v_add_f32 v92, v92, v245\n" \
  "v_and_b32 v245, 0xffff0000, v37\n" \
  "v_sub_f32 v96, v96, v245\n" \
  "v_lshlrev_b32 v245, 16, v31\n" \
  "v_add_f32 v43, v43, v245\n" \
  "v_and_b32 v245, 0xffff0000, v31\n" \
  "v_sub_f32 v47, v47, v245\n" \
  "v_lshlrev_b32 v245, 16, v38\n" \
  "v_add_f32 v69, v69, v245\n" \
  "v_and_b32 v245, 0xffff0000, v38\n" \
  "v_sub_f32 v73, v73, v245\n" \
  "v_lshlrev_b32 v245, 16, v30\n" \
  "v_add_f32 v85, v85, v245\n" \
  "v_and_b32 v245, 0xffff0000, v30\n" \
  "v_sub_f32 v89, v89, v245\n" \
  "v_lshlrev_b32 v245, 16, v39\n" \
  "v_add_f32 v93, v93, v245\n" \
  "v_and_b32 v245, 0xffff0000, v39\n" \
  "v_sub_f32 v97, v97, v245\n" \
  "s_branch .Lxd_%=\n" \
  ".Lts_%=:\n" \
  "v_and_b32 v239, 15, v191\n" \
  "v_lshrrev_b32 v237, 4, v191\n" \
  "v_lshl_or_b32 v239, v237, 6, v239\n" \
  "v_mul_u32_u24 v239, 9344, v239\n" \
  "v_lshl_add_u32 v239, s20, 4, v239\n" \
  "s_add_u32 s32, s22, 0\n" \
  "v_and_b32 v237, 1, v106\n" \
  "v_cmpx_ne_u32 vcc, 0, v237\n" \
  "buffer_load_dwordx4 v[106:109], v239, s[36:39], s32 offen sc1\n" \
  "s_mov_b64 exec, -1\n" \
  "s_add_u32 s32, s22, 149504\n" \
  "v_and_b32 v237, 1, v124\n" \
  "v_cmpx_ne_u32 vcc, 0, v237\n" \
  "buffer_load_dwordx4 v[124:127], v239, s[36:39], s32 offen sc1\n" \
  "s_mov_b64 exec, -1\n" \
  "s_add_u32 s32, s22, 299008\n" \
  "v_and_b32 v237, 1, v128\n" \
  "v_cmpx_ne_u32 vcc, 0, v237\n" \
  "buffer_load_dwordx4 v[128:131], v239, s[36:39], s32 offen sc1\n" \
  "s_mov_b64 exec, -1\n" \
  "s_add_u32 s32, s22, 448512\n" \
  "v_and_b32 v237, 1, v132\n" \
  "v_cmpx_ne_u32 vcc, 0, v237\n" \
  "buffer_load_dwordx4 v[132:135], v239, s[36:39], s32 offen sc1\n" \
  "s_mov_b64 exec, -1\n" \
  "s_memrealtime s[100:101]\n" \
  "s_waitcnt vmcnt(0) lgkmcnt(0)\n" \
  "s_sub_u32 s100, s100, s17\n" \
  "s_cmp_lt_u32 s100, 0x4c4b40\n" \
  "s_cbranch_scc1 .Ltc_%=\n" \
  "s_branch .Ltd_%=\n" \
  ".Lrcs_%=:\n" \
  "s_add_u32 s32, s8, 4096\n" \
  "v_and_b32 v246, 1, v44\n" \
  "v_cmpx_eq_u32 vcc, 0, v246\n" \
  "buffer_load_dwordx4 v[44:47], v230, s[36:39], s8 offen sc1\n" \
  "s_mov_b64 exec, -1\n" \
  "v_and_b32 v246, 1, v66\n" \
  "v_cmpx_eq_u32 vcc, 0, v246\n" \
  "buffer_load_dwordx4 v[66:69], v230, s[36:39], s32 offen sc1\n" \
  "s_mov_b64 exec, -1\n" \
  "v_and_b32 v246, 1, v70\n" \
  "v_cmpx_eq_u32 vcc, 0, v246\n" \
  "buffer_load_dwordx4 v[70:73], v240, s[36:39], s9 offen sc1\n" \
  "s_mov_b64 exec, -1\n" \
  "s_memrealtime s[100:101]\n" \
  "s_waitcnt vmcnt(0) lgkmcnt(0)\n" \
  "s_sub_u32 s100, s100, s17\n" \
  "s_cmp_lt_u32 s100, 0x4c4b40\n" \
  "s_cbranch_scc1 .Lrcc_%=\n" \
  "s_branch .Lrcd_%=\n" \
  ".Lmcs_%=:\n" \
  "s_sleep 2\n" \
  "s_mov_b32 s32, 35840\n" \
  "s_mov_b32 s33, 40448\n" \
  "buffer_load_dwordx4 v[28:31], v238, s[36:39], s32 offen offset:0 sc1\n" \
  "buffer_load_dwordx4 v[32:35], v238, s[36:39], s32 offen offset:2304 sc1\n" \
  "buffer_load_dwordx4 v[36:39], v238, s[36:39], s33 offen offset:0 sc1\n" \
  "buffer_load_dwordx4 v[40:43], v238, s[36:39], s33 offen offset:2304 sc1\n" \
  "s_memrealtime s[100:101]\n" \
  "s_waitcnt vmcnt(0) lgkmcnt(0)\n" \
  "s_sub_u32 s100, s100, s17\n" \
  "s_cmp_lt_u32 s100, 0x4c4b40\n" \
  "s_cbranch_scc1 .Lmcc_%=\n" \
  "s_branch .Lmcd_%=\n" \
  ".Lscs_%=:\n" \
  "buffer_load_dwordx2 v[8:9], v245, s[36:39], s29 offen sc1\n" \
  "s_memrealtime s[100:101]\n" \
  "s_waitcnt vmcnt(0) lgkmcnt(0)\n" \
  "s_sub_u32 s100, s100, s17\n" \
  "s_cmp_lt_u32 s100, 0x4c4b40\n" \
  "s_cbranch_scc1 .Lscc_%=\n" \
  "s_branch .Lscd_%=\n" \
  ".Lcol_%=:\n" \
  "s_load_dwordx2 s[48:49], s[0:1], 0\n" \
  "v_lshrrev_b32 v255, 6, v0\n" \
  "s_getpc_b64 s[80:81]\n" \
  ".Lpcc_%=:\n" \
  "v_readfirstlane_b32 s20, v255\n" \
  "s_mov_b32 s82, .Ladm_b-.Lpcc_%=\n" \
  "s_mov_b32 s83, 131072\n" \
  "s_mov_b32 m0, 162176\n" \
  "s_lshr_b32 s98, s2, 3\n" \
  "s_mul_i32 s98, s98, 20\n" \
  "v_add_u32 v254, s98, v0\n" \
  "v_lshlrev_b32 v253, 7, v254\n" \
  "s_cmp_eq_u32 s20, 0\n" \
  "s_cselect_b64 exec, 1048575, 0\n" \
  "buffer_load_dword v253, s[80:83], 0 offen lds\n" \
  "s_mov_b64 exec, -1\n" \
  "s_mov_b32 s50, 2359296\n" \
  "s_mov_b32 s51, 131072\n" \
  "s_mov_b32 s78, 26944\n" \
  "s_mov_b32 s79, 131072\n" \
  "v_and_b32 v191, 63, v0\n" \
  "v_and_b32 v254, 31, v191\n" \
  "v_lshrrev_b32 v253, 5, v191\n" \
  "v_mul_u32_u24 v252, 1179648, v253\n" \
  "v_lshl_add_u32 v251, v254, 4, v252\n" \
  "v_lshl_add_u32 v250, v254, 2, v252\n" \
  "v_lshlrev_b32 v252, 4, v191\n" \
  "s_and_b32 s84, s2, 7\n" \
  "s_lshr_b32 s85, s2, 3\n" \
  "s_lshl_b32 s98, s84, 5\n" \
  "s_add_u32 s86, s98, s85\n" \
  "s_lshl_b32 s98, s20, 8\n" \
  "s_add_u32 s98, s98, s86\n" \
  "s_mul_i32 s87, s98, 1152\n" \
  "s_waitcnt lgkmcnt(0)\n" \
  "buffer_load_dwordx4 v[2:5], v251, s[48:51], s87 offen offset:0\n" \
  "buffer_load_dwordx4 v[6:9], v251, s[48:51], s87 offen offset:512\n" \
  "buffer_load_dword v249, v250, s[48:51], s87 offen offset:1024\n" \
  "s_movk_i32 s32, 22528\n" \
  "buffer_load_dwordx4 v[10:13], v252, s[76:79], s32 offen offset:0\n" \
  "s_memrealtime s[16:17]\n" \
  "s_lshr_b32 s32, s20, 1\n" \
  "s_lshl3_add_u32 s32, s32, 8\n" \
  "s_load_dwordx2 s[52:53], s[0:1], s32\n" \
  "s_load_dwordx2 s[4:5], s[0:1], 24\n" \
  "s_load_dwordx2 s[64:65], s[0:1], 40\n" \
  "s_load_dwordx2 s[36:37], s[0:1], 48\n" \
  "s_load_dwordx2 s[68:69], s[0:1], 56\n" \
  "s_load_dwordx2 s[72:73], s[0:1], 64\n" \
  "s_load_dwordx2 s[40:41], s[0:1], 72\n" \
  "s_load_dwordx2 s[44:45], s[0:1], 80\n" \
  "s_mov_b32 s6, 18874368\n" \
  "s_mov_b32 s7, 131072\n" \
  "s_mov_b32 s66, 3981312\n" \
  "s_mov_b32 s67, 131072\n" \
  "s_mov_b32 s38, 134217728\n" \
  "s_mov_b32 s39, 131072\n" \
  "s_mov_b32 s70, 9216\n" \
  "s_mov_b32 s71, 131072\n" \
  "s_mov_b32 s74, 4096\n" \
  "s_mov_b32 s75, 131072\n" \
  "s_mov_b32 s42, 18874368\n" \
  "s_mov_b32 s43, 131072\n" \
  "s_mov_b32 s46, 2359296\n" \
  "s_mov_b32 s47, 131072\n" \
  "s_mov_b32 s54, 1183744\n" \
  "s_mov_b32 s55, 131072\n" \
  "v_and_b32 v248, 15, v191\n" \
  "v_lshrrev_b32 v247, 4, v191\n" \
  "v_lshlrev_b32 v246, 4, v0\n" \
  "v_lshrrev_b32 v245, 1, v248\n" \
  "v_and_b32 v244, 1, v247\n" \
  "v_lshl_add_u32 v243, v244, 9, s86\n" \
  "v_and_b32 v244, 1, v248\n" \
  "v_and_b32 v242, 3, v245\n" \
  "v_lshl_add_u32 v241, v244, 2, v242\n" \
  "v_lshl_add_u32 v242, v241, 8, s86\n" \
  "v_lshlrev_b32 v241, 1, v247\n" \
  "v_cvt_f32_u32 v240, v245\n" \
  "v_cvt_f32_u32 v245, v243\n" \
  "v_cmp_eq_u32 vcc, v244, v253\n" \
  "v_mul_f32 v240, 3120562176, v240\n" \
  "v_mov_b32 v243, 1048576000\n" \
  "v_cndmask_b32 v243, 0, v243, vcc\n" \
  "v_lshlrev_b32 v239, 31, v244\n" \
  "v_mul_f32 v245, v240, v245\n" \
  "v_xor_b32 v239, v243, v239\n" \
  "v_mul_f32 v240, 1132462080, v240\n" \
  "v_fma_f32 v238, v240, 0, v245\n" \
  "v_cos_f32 v237, v238\n" \
  "v_sin_f32 v236, v238\n" \
  "v_mul_f32 v235, v237, v243\n" \
  "v_mul_f32 v234, v236, v239\n" \
  "v_mul_f32 v233, v236, v243\n" \
  "v_mul_f32 v232, v237, v239\n" \
  "v_cvt_pk_f16_f32 v235, v235, -v234\n" \
  "v_cvt_pk_f16_f32 v233, v233, v232\n" \
  "v_accvgpr_write_b32 a160, v235\n" \
  "v_accvgpr_write_b32 a164, v233\n" \
  "v_fma_f32 v238, v240, 4.0, v245\n" \
  "v_cos_f32 v237, v238\n" \
  "v_sin_f32 v236, v238\n" \
  "v_mul_f32 v235, v237, v243\n" \
  "v_mul_f32 v234, v236, v239\n" \
  "v_mul_f32 v233, v236, v243\n" \
  "v_mul_f32 v232, v237, v239\n" \
  "v_cvt_pk_f16_f32 v235, v235, -v234\n" \
  "v_cvt_pk_f16_f32 v233, v233, v232\n" \
  "v_accvgpr_write_b32 a161, v235\n" \
  "v_accvgpr_write_b32 a165, v233\n" \
  "v_fma_f32 v238, v240, 1.0, v245\n" \
  "v_cos_f32 v237, v238\n" \
  "v_sin_f32 v236, v238\n" \
  "v_mul_f32 v235, v237, v243\n" \
  "v_mul_f32 v234, v236, v239\n" \
  "v_mul_f32 v233, v236, v243\n" \
  "v_mul_f32 v232, v237, v239\n" \
  "v_cvt_pk_f16_f32 v235, v235, -v234\n" \
  "v_cvt_pk_f16_f32 v233, v233, v232\n" \
  "v_accvgpr_write_b32 a162, v235\n" \
  "v_accvgpr_write_b32 a166, v233\n" \
  "v_fma_f32 v238, v240, 4.0, v245\n" \
  "v_add_f32 v238, v238, v240\n" \
  "v_cos_f32 v237, v238\n" \
  "v_sin_f32 v236, v238\n" \
  "v_mul_f32 v235, v237, v243\n" \
  "v_mul_f32 v234, v236, v239\n" \
  "v_mul_f32 v233, v236, v243\n" \
  "v_mul_f32 v232, v237, v239\n" \
  "v_cvt_pk_f16_f32 v235, v235, -v234\n" \
  "v_cvt_pk_f16_f32 v233, v233, v232\n" \
  "v_accvgpr_write_b32 a163, v235\n" \
  "v_accvgpr_write_b32 a167, v233\n" \
  "v_cvt_f32_u32 v240, v242\n" \
  "v_cvt_f32_u32 v245, v241\n" \
  "v_cmp_lt_u32 vcc, 7, v248\n" \
  "v_mul_f32 v240, 973078528, v240\n" \
  "v_mul_f32 v238, v240, v245\n" \
  "v_cos_f32 v237, v238\n" \
  "v_sin_f32 v236, v238\n" \
  "v_mul_f32 v237, 0.5, v237\n" \
  "v_mul_f32 v236, 0.5, v236\n" \
  "v_cvt_pk_f16_f32 v235, v237, -v236\n" \
  "v_cvt_pk_f16_f32 v234, v236, v237\n" \
  "v_cndmask_b32 v233, v235, 0, vcc\n" \
  "v_cndmask_b32 v235, 0, v235, vcc\n" \
  "v_cndmask_b32 v232, v234, 0, vcc\n" \
  "v_xor_b32 v234, 2147516416, v234\n" \
  "v_accvgpr_write_b32 a228, v233\n" \
  "v_cndmask_b32 v234, 0, v234, vcc\n" \
  "v_accvgpr_write_b32 a229, v235\n" \
  "v_accvgpr_write_b32 a232, v232\n" \
  "v_accvgpr_write_b32 a233, v234\n" \
  "v_add_f32 v238, v238, v240\n" \
  "v_cos_f32 v237, v238\n" \
  "v_sin_f32 v236, v238\n" \
  "v_mul_f32 v237, 0.5, v237\n" \
  "v_mul_f32 v236, 0.5, v236\n" \
  "v_cvt_pk_f16_f32 v235, v237, -v236\n" \
  "v_cvt_pk_f16_f32 v234, v236, v237\n" \
  "v_cndmask_b32 v233, v235, 0, vcc\n" \
  "v_cndmask_b32 v235, 0, v235, vcc\n" \
  "v_cndmask_b32 v232, v234, 0, vcc\n" \
  "v_xor_b32 v234, 2147516416, v234\n" \
  "v_accvgpr_write_b32 a230, v233\n" \
  "v_cndmask_b32 v234, 0, v234, vcc\n" \
  "v_accvgpr_write_b32 a231, v235\n" \
  "v_accvgpr_write_b32 a234, v232\n" \
  "v_accvgpr_write_b32 a235, v234\n" \
  "s_lshr_b32 s88, s2, 6\n" \
  "s_lshl_b32 s98, s84, 2\n" \
  "s_lshr_b32 s100, s85, 1\n" \
  "s_and_b32 s100, s100, 3\n" \
  "s_add_u32 s89, s98, s100\n" \
  "s_mul_i32 s98, s86, 9344\n" \
  "s_add_u32 s82, s98, 524288\n" \
  "s_lshl_b32 s98, s86, 8\n" \
  "s_add_u32 s11, s98, 9764864\n" \
  "s_lshl_b32 s98, s86, 4\n" \
  "s_add_u32 s28, s98, 65536\n" \
  "s_and_b32 s98, s84, 1\n" \
  "s_lshl_b32 s98, s98, 3\n" \
  "s_and_b32 s100, s85, 7\n" \
  "s_add_u32 s90, s98, s100\n" \
  "s_lshl_b32 s98, s88, 5\n" \
  "s_add_u32 s98, s98, s89\n" \
  "s_lshl_b32 s98, s98, 1\n" \
  "s_and_b32 s100, s85, 1\n" \
  "s_add_u32 s98, s98, s100\n" \
  "s_lshl_b32 s98, s98, 7\n" \
  "s_add_u32 s80, s98, 262144\n" \
  "s_mov_b32 s35, 987991609\n" \
  "v_mul_u32_u24 v245, 9, v247\n" \
  "v_add_u32 v243, 8, v245\n" \
  "v_lshlrev_b32 v245, 4, v248\n" \
  "v_lshl_add_u32 v242, v243, 16, v245\n" \
  "v_lshrrev_b32 v245, 3, v191\n" \
  "v_and_b32 v243, 7, v191\n" \
  "v_lshlrev_b32 v241, 4, v243\n" \
  "v_lshl_add_u32 v243, v245, 16, v241\n" \
  "v_add_u32 v245, s80, v243\n" \
  "s_add_u32 s83, s82, 4096\n" \
  "s_add_u32 s91, s82, 8192\n" \
  "v_mov_b32 v14, -1\n" \
  "v_mov_b32 v15, -1\n" \
  "v_mov_b32 v16, -1\n" \
  "v_mov_b32 v17, -1\n" \
  "v_cmp_eq_u32 vcc, 24, v191\n" \
  "v_mov_b32 v255, s28\n" \
  "s_cmp_eq_u32 s20, 2\n" \
  "v_cndmask_b32 v245, v245, v255, vcc\n" \
  "s_cselect_b64 vcc, -1, 0\n" \
  "s_cselect_b32 s91, s11, s91\n" \
  "v_cndmask_b32 v243, v246, v242, vcc\n" \
  "s_cmp_eq_u32 s20, 3\n" \
  "s_cselect_b64 vcc, -1, 0\n" \
  "s_cselect_b32 s91, 0, s91\n" \
  "s_cselect_b32 s32, 0, -1\n" \
  "v_cndmask_b32 v243, v243, v245, vcc\n" \
  "s_cselect_b32 s92, 33554431, -1\n" \
  "s_cselect_b32 s93, 0, -1\n" \
  "s_cmp_eq_u32 s20, 1\n" \
  "s_cselect_b64 s[92:93], 15, s[92:93]\n" \
  "v_mov_b32 v18, s32\n" \
  "v_mov_b32 v19, s32\n" \
  "v_mov_b32 v20, s32\n" \
  "v_mov_b32 v21, s32\n" \
  "s_movk_i32 s32, 768\n" \
  "v_lshlrev_b32 v245, 5, v191\n" \
  "v_bfe_u32 v242, v191, 5, 1\n" \
  "v_and_or_b32 v245, v245, s32, v242\n" \
  "v_lshrrev_b32 v242, 3, v191\n" \
  "v_mul_u32_u24 v245, 9216, v245\n" \
  "v_bitop3_b32 v242, v242, v191, 6 bitop3:0x6c\n" \
  "v_and_b32 v242, 7, v242\n" \
  "v_lshl_or_b32 v22, v242, 4, v245\n" \
  "v_add_u32 v23, 17408, v22\n" \
  "v_add_u32 v24, 34816, v22\n" \
  "v_add_u32 v25, 52224, v22\n" \
  "s_cmp_eq_u32 s20, 0\n" \
  "s_cselect_b64 s[58:59], -1, 0\n" \
  "s_movk_i32 s34, 144\n" \
  "v_cmp_gt_u32 s[94:95], s34, v0\n" \
  "v_mov_b32 v26, 0\n" \
  "v_mov_b32 v27, 0\n" \
  "v_mov_b32 v255, 161664\n" \
  "ds_write_b64 v255, v[26:27] offset:0\n" \
  "v_bfrev_b32 v245, 1\n" \
  "v_cndmask_b32 v245, v245, v246, s[58:59]\n" \
  "v_and_b32 v242, 1, v191\n" \
  "v_lshl_add_u32 v241, v253, 1, v242\n" \
  "v_lshlrev_b32 v242, 3, v241\n" \
  "v_and_b32 v241, 7, v191\n" \
  "v_lshlrev_b32 v244, 1, v241\n" \
  "v_and_b32 v241, 12, v191\n" \
  "v_add_u32 v240, v241, v247\n" \
  "s_lshl_b32 s98, s20, 4\n" \
  "v_add_u32 v247, s98, v240\n" \
  "v_lshlrev_b32 v241, 1, v247\n" \
  "v_bfe_u32 v247, v191, 1, 1\n" \
  "v_lshrrev_b32 v240, 2, v191\n" \
  "v_and_b32 v239, 12, v240\n" \
  "v_lshl_add_u32 v240, v247, 6, v239\n" \
  "s_lshr_b32 s98, s84, 1\n" \
  "s_lshl_b32 s98, s98, 4\n" \
  "s_add_u32 s98, s98, s20\n" \
  "v_add_u32 v247, s98, v240\n" \
  "v_lshlrev_b32 v240, 1, v247\n" \
  "s_mul_i32 s98, s84, 1152\n" \
  "s_and_b32 s100, s85, 7\n" \
  "s_lshl_b32 s100, s100, 7\n" \
  "s_add_u32 s98, s98, s100\n" \
  "s_lshl_b32 s100, s20, 5\n" \
  "s_add_u32 s25, s98, s100\n" \
  "s_mul_i32 s98, s84, 1152\n" \
  "s_and_b32 s100, s85, 6\n" \
  "s_lshl_b32 s100, s100, 4\n" \
  "s_add_u32 s98, s98, s100\n" \
  "s_lshr_b32 s100, s20, 1\n" \
  "s_lshl_b32 s100, s100, 4\n" \
  "s_add_u32 s98, s98, s100\n" \
  "s_add_u32 s13, s98, 1024\n" \
  "s_lshl_b32 s15, s90, 8\n" \
  "s_waitcnt lgkmcnt(0)\n" \
  "s_mov_b32 s17, s16\n" \
  "v_and_b32 v247, 127, v0\n" \
  "s_mul_i32 s98, s86, 4624\n" \
  "v_lshl_add_u32 v239, v247, 4, s98\n" \
  "v_add_u32 v238, 3440, v239\n" \
  "v_add_u32 v237, 2048, v239\n" \
  "v_add_u32 v236, 1392, v239\n" \
  "s_lshr_b32 s98, s20, 1\n" \
  "s_mul_i32 s98, s98, 6144\n" \
  "s_and_b32 s100, s20, 1\n" \
  "s_lshl_b32 s100, s100, 10\n" \
  "s_add_u32 s98, s98, s100\n" \
  "s_add_u32 s56, s98, 19584\n" \
  "v_cmp_gt_u32 vcc, 57, v247\n" \
  "v_subrev_u32 v255, 1183744, v238\n" \
  "s_mov_b32 m0, s56\n" \
  "v_cndmask_b32 v237, v236, v237, vcc\n" \
  "buffer_load_dwordx4 v239, s[52:55], 0 offen offset:0 lds\n" \
  "v_min_u32 v238, v238, v255\n" \
  "s_add_u32 m0, s56, 2048\n" \
  "s_bitcmp1_b32 s20, 0\n" \
  "buffer_load_dwordx4 v237, s[52:55], 0 offen offset:0 lds\n" \
  "s_add_u32 m0, s56, 4096\n" \
  "s_cselect_b32 exec_hi, 32767, -1\n" \
  "buffer_load_dwordx4 v238, s[52:55], 0 offen offset:0 lds\n" \
  "s_mov_b64 exec, -1\n" \
  "s_movk_i32 s32, 10240\n" \
  "buffer_load_dwordx4 v[26:29], v246, s[76:79], s32 offen offset:0\n" \
  "buffer_load_dwordx2 v[30:31], off, s[36:39], 0 sc1\n" \
  "buffer_store_dwordx4 v[14:17], v246, s[36:39], s82 offen offset:0 sc1\n" \
  "buffer_store_dwordx4 v[14:17], v246, s[36:39], s83 offen offset:0 sc1\n" \
  "s_mov_b64 exec, s[92:93]\n" \
  "buffer_store_dwordx4 v[18:21], v243, s[36:39], s91 offen offset:0 sc1\n" \
  "s_mov_b64 exec, -1\n" \
  "s_lshl_b32 s98, s85, 3\n" \
  "s_lshl_b32 s100, s20, 1\n" \
  "s_add_u32 s98, s98, s100\n" \
  "s_mul_i32 s98, s98, 9216\n" \
  "s_mul_i32 s100, s84, 1152\n" \
  "s_add_u32 s98, s98, s100\n" \
  "s_add_u32 s32, s98, 1024\n" \
  "s_lshl_b32 s98, s20, 10\n" \
  "s_add_u32 s33, s98, 155008\n" \
  "s_mov_b32 m0, s33\n" \
  "s_movk_i32 s31, 4096\n" \
  "s_cmp_gt_u32 s20, 1\n" \
  "buffer_load_dwordx4 v22, s[4:7], s32 offen offset:0 lds\n" \
  "s_cselect_b32 s31, -28672, s31\n" \
  "s_add_u32 m0, s33, s31\n" \
  "s_add_u32 s32, s32, 9437184\n" \
  "buffer_load_dwordx4 v22, s[4:7], s32 offen offset:0 lds\n" \
  "buffer_load_dwordx2 v[16:17], v242, s[68:71], s25 offen offset:0\n" \
  "buffer_load_ushort v247, v244, s[68:71], s13 offen offset:0\n" \
  "buffer_load_ushort v244, v241, s[72:75], s15 offen offset:0\n" \
  "buffer_load_ushort v243, v241, s[72:75], s15 offen offset:128\n" \
  "buffer_load_ushort v242, v240, s[72:75], s15 offen offset:0\n" \
  "v_mul_u32_u24 v241, 2448, v253\n" \
  "s_mul_i32 s98, s20, 4896\n" \
  "v_add_u32 v240, s98, v241\n" \
  "v_lshrrev_b32 v241, 3, v254\n" \
  "v_lshlrev_b32 v239, 4, v241\n" \
  "v_lshl_add_u32 v241, v254, 5, v239\n" \
  "v_add_u32 v239, v241, v240\n" \
  "v_lshl_add_u32 v241, v254, 3, v240\n" \
  "s_waitcnt vmcnt(18)\n" \
  "v_lshlrev_b32 v14, 16, v2\n" \
  "v_and_b32 v15, 4294901760, v2\n" \
  "v_lshlrev_b32 v18, 16, v3\n" \
  "v_and_b32 v19, 4294901760, v3\n" \
  "v_lshlrev_b32 v20, 16, v4\n" \
  "v_and_b32 v21, 4294901760, v4\n" \
  "v_lshlrev_b32 v32, 16, v5\n" \
  "v_and_b32 v33, 4294901760, v5\n" \
  "s_waitcnt vmcnt(17)\n" \
  "v_lshlrev_b32 v34, 16, v6\n" \
  "v_and_b32 v35, 4294901760, v6\n" \
  "v_lshlrev_b32 v36, 16, v7\n" \
  "v_and_b32 v37, 4294901760, v7\n" \
  "v_lshlrev_b32 v38, 16, v8\n" \
  "v_and_b32 v39, 4294901760, v8\n" \
  "v_lshlrev_b32 v40, 16, v9\n" \
  "v_and_b32 v41, 4294901760, v9\n" \
  "s_waitcnt vmcnt(16)\n" \
  "v_lshlrev_b32 v42, 16, v249\n" \
  "v_and_b32 v43, 4294901760, v249\n" \
  "v_mov_b32 v45, 925353388\n" \
  "v_mov_b32_dpp v46, v14 row_newbcast:0 row_mask:0xf bank_mask:0xf\n" \
  "v_mov_b32_dpp v240, v14 row_newbcast:0 row_mask:0xf bank_mask:0xf\n" \
  "s_nop 1\n" \
  "v_permlane16_swap_b32 v46, v240\n" \
  "v_pk_add_f32 v[14:15], v[14:15], v[46:47] op_sel_hi:[1,0] neg_lo:[0,1] neg_hi:[0,1]\n" \
  "v_pk_add_f32 v[18:19], v[18:19], v[46:47] op_sel_hi:[1,0] neg_lo:[0,1] neg_hi:[0,1]\n" \
  "v_pk_add_f32 v[20:21], v[20:21], v[46:47] op_sel_hi:[1,0] neg_lo:[0,1] neg_hi:[0,1]\n" \
  "v_pk_add_f32 v[32:33], v[32:33], v[46:47] op_sel_hi:[1,0] neg_lo:[0,1] neg_hi:[0,1]\n" \
  "v_pk_add_f32 v[34:35], v[34:35], v[46:47] op_sel_hi:[1,0] neg_lo:[0,1] neg_hi:[0,1]\n" \
  "v_pk_add_f32 v[36:37], v[36:37], v[46:47] op_sel_hi:[1,0] neg_lo:[0,1] neg_hi:[0,1]\n" \
  "v_pk_add_f32 v[38:39], v[38:39], v[46:47] op_sel_hi:[1,0] neg_lo:[0,1] neg_hi:[0,1]\n" \
  "v_pk_add_f32 v[40:41], v[40:41], v[46:47] op_sel_hi:[1,0] neg_lo:[0,1] neg_hi:[0,1]\n" \
  "v_pk_add_f32 v[42:43], v[42:43], v[46:47] op_sel_hi:[1,0] neg_lo:[0,1] neg_hi:[0,1]\n" \
  "v_pk_add_f32 v[46:47], v[14:15], v[18:19]\n" \
  "v_pk_mul_f32 v[48:49], v[14:15], v[14:15]\n" \
  "v_pk_fma_f32 v[48:49], v[18:19], v[18:19], v[48:49]\n" \
  "v_pk_add_f32 v[46:47], v[46:47], v[20:21]\n" \
  "v_pk_fma_f32 v[48:49], v[20:21], v[20:21], v[48:49]\n" \
  "v_pk_add_f32 v[46:47], v[46:47], v[32:33]\n" \
  "v_pk_fma_f32 v[48:49], v[32:33], v[32:33], v[48:49]\n" \
  "v_pk_add_f32 v[46:47], v[46:47], v[34:35]\n" \
  "v_pk_fma_f32 v[48:49], v[34:35], v[34:35], v[48:49]\n" \
  "v_pk_add_f32 v[46:47], v[46:47], v[36:37]\n" \
  "v_pk_fma_f32 v[48:49], v[36:37], v[36:37], v[48:49]\n" \
  "v_pk_add_f32 v[46:47], v[46:47], v[38:39]\n" \
  "v_pk_fma_f32 v[48:49], v[38:39], v[38:39], v[48:49]\n" \
  "v_pk_add_f32 v[46:47], v[46:47], v[40:41]\n" \
  "v_pk_fma_f32 v[48:49], v[40:41], v[40:41], v[48:49]\n" \
  "v_pk_add_f32 v[46:47], v[46:47], v[42:43]\n" \
  "v_pk_fma_f32 v[48:49], v[42:43], v[42:43], v[48:49]\n" \
  "v_add_f32 v240, v46, v47\n" \
  "v_add_f32 v238, v48, v49\n" \
  "s_nop 1\n" \
  "v_permlane16_swap_b32 v240, v238\n" \
  "v_add_f32 v240, v240, v238\n" \
  "s_nop 1\n" \
  "v_add_f32_dpp v240, v240, v240 quad_perm:[1,0,3,2] row_mask:0xf bank_mask:0xf\n" \
  "s_nop 1\n" \
  "v_add_f32_dpp v240, v240, v240 quad_perm:[2,3,0,1] row_mask:0xf bank_mask:0xf\n" \
  "s_nop 1\n" \
  "v_add_f32_dpp v240, v240, v240 row_half_mirror row_mask:0xf bank_mask:0xf\n" \
  "s_nop 1\n" \
  "v_add_f32_dpp v240, v240, v240 row_mirror row_mask:0xf bank_mask:0xf\n" \
  "v_mov_b32 v238, v240\n" \
  "s_nop 1\n" \
  "v_permlane16_swap_b32 v240, v238\n" \
  "v_mul_f32 v240, s35, v240\n" \
  "v_fma_f32 v238, v238, s35, v45\n" \
  "v_fma_f32 v238, -v240, v240, v238\n" \
  "v_max_f32 v238, v238, v45\n" \
  "v_rsq_f32 v44, v238\n" \
  "s_nop 0\n" \
  "v_mul_f32_e64 v45, -v240, v44\n" \
  "v_pk_fma_f32 v[46:47], v[14:15], v[44:45], v[44:45] op_sel:[0,0,1] op_sel_hi:[1,0,1]\n" \
  "v_pk_fma_f32 v[48:49], v[18:19], v[44:45], v[44:45] op_sel:[0,0,1] op_sel_hi:[1,0,1]\n" \
  "ds_write_b128 v239, v[46:49] offset:0\n" \
  "v_pk_fma_f32 v[46:47], v[20:21], v[44:45], v[44:45] op_sel:[0,0,1] op_sel_hi:[1,0,1]\n" \
  "v_pk_fma_f32 v[48:49], v[32:33], v[44:45], v[44:45] op_sel:[0,0,1] op_sel_hi:[1,0,1]\n" \
  "ds_write_b128 v239, v[46:49] offset:16\n" \
  "v_pk_fma_f32 v[18:19], v[34:35], v[44:45], v[44:45] op_sel:[0,0,1] op_sel_hi:[1,0,1]\n" \
  "v_pk_fma_f32 v[20:21], v[36:37], v[44:45], v[44:45] op_sel:[0,0,1] op_sel_hi:[1,0,1]\n" \
  "ds_write_b128 v239, v[18:21] offset:1088\n" \
  "v_pk_fma_f32 v[18:19], v[38:39], v[44:45], v[44:45] op_sel:[0,0,1] op_sel_hi:[1,0,1]\n" \
  "v_pk_fma_f32 v[20:21], v[40:41], v[44:45], v[44:45] op_sel:[0,0,1] op_sel_hi:[1,0,1]\n" \
  "ds_write_b128 v239, v[18:21] offset:1104\n" \
  "v_pk_fma_f32 v[42:43], v[42:43], v[44:45], v[44:45] op_sel:[0,0,1] op_sel_hi:[1,0,1]\n" \
  "ds_write_b64 v241, v[42:43] offset:2176\n" \
  "s_waitcnt vmcnt(10)\n" \
  "v_readfirstlane_b32 s18, v30\n" \
  "v_readfirstlane_b32 s19, v31\n" \
  "s_add_u32 s18, s18, 1\n" \
  "s_addc_u32 s19, s19, 0\n" \
  "s_waitcnt lgkmcnt(0)\n" \
  "s_barrier\n" \
  "v_and_b32 v241, 65535, v26\n" \
  "ds_read_u16 v18, v241 offset:19584\n" \
  "ds_read_u16 v19, v241 offset:20162\n" \
  "ds_read_u16 v20, v241 offset:20740\n" \
  "ds_read_u16 v21, v241 offset:21318\n" \
  "ds_read_u16 v30, v241 offset:25728\n" \
  "ds_read_u16 v31, v241 offset:26306\n" \
  "ds_read_u16 v32, v241 offset:26884\n" \
  "ds_read_u16 v33, v241 offset:27462\n" \
  "s_add_u32 s32, s82, 2392064\n" \
  "s_cmp_eq_u32 s20, 1\n" \
  "s_cselect_b64 vcc, 255, s[58:59]\n" \
  "v_mov_b32 v241, 0\n" \
  "v_and_b32 v240, 65535, v27\n" \
  "ds_read_u16 v34, v240 offset:19584\n" \
  "ds_read_u16 v35, v240 offset:20162\n" \
  "ds_read_u16 v36, v240 offset:20740\n" \
  "ds_read_u16 v37, v240 offset:21318\n" \
  "ds_read_u16 v38, v240 offset:25728\n" \
  "ds_read_u16 v39, v240 offset:26306\n" \
  "ds_read_u16 v40, v240 offset:26884\n" \
  "ds_read_u16 v41, v240 offset:27462\n" \
  "v_lshrrev_b32 v240, 16, v26\n" \
  "s_waitcnt lgkmcnt(11)\n" \
  "v_lshl_or_b32 v18, v30, 16, v18\n" \
  "s_waitcnt lgkmcnt(10)\n" \
  "v_lshl_or_b32 v19, v31, 16, v19\n" \
  "s_waitcnt lgkmcnt(9)\n" \
  "v_lshl_or_b32 v20, v32, 16, v20\n" \
  "s_waitcnt lgkmcnt(8)\n" \
  "v_lshl_or_b32 v21, v33, 16, v21\n" \
  "buffer_store_dwordx4 v[18:21], v240, s[36:39], s32 offen offset:0 sc1\n" \
  "v_and_b32 v30, 2147450879, v18\n" \
  "v_pk_max_u16 v241, v241, v30\n" \
  "v_and_b32 v31, 2147450879, v19\n" \
  "v_pk_max_u16 v241, v241, v31\n" \
  "v_and_b32 v32, 2147450879, v20\n" \
  "v_pk_max_u16 v241, v241, v32\n" \
  "v_and_b32 v33, 2147450879, v21\n" \
  "v_pk_max_u16 v241, v241, v33\n" \
  "v_and_b32 v240, 65535, v28\n" \
  "ds_read_u16 v18, v240 offset:19584\n" \
  "ds_read_u16 v19, v240 offset:20162\n" \
  "ds_read_u16 v20, v240 offset:20740\n" \
  "ds_read_u16 v21, v240 offset:21318\n" \
  "ds_read_u16 v30, v240 offset:25728\n" \
  "ds_read_u16 v31, v240 offset:26306\n" \
  "ds_read_u16 v32, v240 offset:26884\n" \
  "ds_read_u16 v33, v240 offset:27462\n" \
  "v_lshrrev_b32 v240, 16, v27\n" \
  "s_waitcnt lgkmcnt(11)\n" \
  "v_lshl_or_b32 v34, v38, 16, v34\n" \
  "s_waitcnt lgkmcnt(10)\n" \
  "v_lshl_or_b32 v35, v39, 16, v35\n" \
  "s_waitcnt lgkmcnt(9)\n" \
  "v_lshl_or_b32 v36, v40, 16, v36\n" \
  "s_waitcnt lgkmcnt(8)\n" \
  "v_lshl_or_b32 v37, v41, 16, v37\n" \
  "buffer_store_dwordx4 v[34:37], v240, s[36:39], s32 offen offset:0 sc1\n" \
  "v_and_b32 v38, 2147450879, v34\n" \
  "v_pk_max_u16 v241, v241, v38\n" \
  "v_and_b32 v39, 2147450879, v35\n" \
  "v_pk_max_u16 v241, v241, v39\n" \
  "v_and_b32 v40, 2147450879, v36\n" \
  "v_pk_max_u16 v241, v241, v40\n" \
  "v_and_b32 v41, 2147450879, v37\n" \
  "v_pk_max_u16 v241, v241, v41\n" \
  "s_mov_b64 s[96:97], vcc\n" \
  "v_lshrrev_b32 v240, 16, v28\n" \
  "s_waitcnt lgkmcnt(3)\n" \
  "v_lshl_or_b32 v18, v30, 16, v18\n" \
  "s_waitcnt lgkmcnt(2)\n" \
  "v_lshl_or_b32 v19, v31, 16, v19\n" \
  "s_waitcnt lgkmcnt(1)\n" \
  "v_lshl_or_b32 v20, v32, 16, v20\n" \
  "s_waitcnt lgkmcnt(0)\n" \
  "v_lshl_or_b32 v21, v33, 16, v21\n" \
  "s_mov_b64 exec, s[96:97]\n" \
  "buffer_store_dwordx4 v[18:21], v240, s[36:39], s32 offen offset:0 sc1\n" \
  "s_mov_b64 exec, -1\n" \
  "v_and_b32 v30, 2147450879, v18\n" \
  "v_pk_max_u16 v241, v241, v30\n" \
  "v_and_b32 v31, 2147450879, v19\n" \
  "v_pk_max_u16 v241, v241, v31\n" \
  "v_and_b32 v32, 2147450879, v20\n" \
  "v_pk_max_u16 v241, v241, v32\n" \
  "v_and_b32 v33, 2147450879, v21\n" \
  "v_pk_max_u16 v241, v241, v33\n" \
  "v_lshlrev_b32 v255, 16, v241\n" \
  "v_max_u32 v241, v241, v255\n" \
  "v_mov_b32 v255, 161664\n" \
  "s_nop 0\n" \
  "v_max_u32_dpp v241, v241, v241 quad_perm:[1,0,3,2] row_mask:0xf bank_mask:0xf\n" \
  "s_nop 1\n" \
  "v_max_u32_dpp v241, v241, v241 quad_perm:[2,3,0,1] row_mask:0xf bank_mask:0xf\n" \
  "s_nop 1\n" \
  "v_max_u32_dpp v241, v241, v241 row_half_mirror row_mask:0xf bank_mask:0xf\n" \
  "s_nop 1\n" \
  "v_max_u32_dpp v241, v241, v241 row_mirror row_mask:0xf bank_mask:0xf\n" \
  "s_mov_b32 exec_lo, 65537\n" \
  "s_mov_b32 exec_hi, 65537\n" \
  "ds_max_u32 v255, v241 offset:4\n" \
  "s_mov_b64 exec, -1\n" \
  "s_mul_i32 s33, s20, 4896\n" \
  "v_and_b32 v241, 65535, v10\n" \
  "v_add_u32 v240, s33, v241\n" \
  "v_and_b32 v239, 65535, v11\n" \
  "v_add_u32 v238, s33, v239\n" \
  "v_and_b32 v237, 65535, v12\n" \
  "v_add_u32 v236, s33, v237\n" \
  "v_and_b32 v235, 65535, v13\n" \
  "v_add_u32 v234, s33, v235\n" \
  "v_lshrrev_b32 v233, 16, v10\n" \
  "v_add_u32 v232, s33, v233\n" \
  "v_lshrrev_b32 v231, 16, v11\n" \
  "v_add_u32 v230, s33, v231\n" \
  "v_lshrrev_b32 v229, 16, v12\n" \
  "v_add_u32 v228, s33, v229\n" \
  "v_lshrrev_b32 v227, 16, v13\n" \
  "v_add_u32 v226, s33, v227\n" \
  "ds_read_b64 v[10:11], v240 offset:0\n" \
  "ds_read_b64 v[12:13], v238 offset:0\n" \
  "ds_read_b64 v[14:15], v236 offset:0\n" \
  "ds_read_b64 v[18:19], v234 offset:0\n" \
  "ds_read_b64 v[20:21], v232 offset:0\n" \
  "ds_read_b64 v[26:27], v230 offset:0\n" \
  "ds_read_b64 v[28:29], v228 offset:0\n" \
  "ds_read_b64 v[30:31], v226 offset:0\n" \
  "ds_read_b64 v[32:33], v240 offset:2448\n" \
  "ds_read_b64 v[34:35], v238 offset:2448\n" \
  "ds_read_b64 v[36:37], v236 offset:2448\n" \
  "ds_read_b64 v[38:39], v234 offset:2448\n" \
  "ds_read_b64 v[40:41], v232 offset:2448\n" \
  "ds_read_b64 v[42:43], v230 offset:2448\n" \
  "ds_read_b64 v[44:45], v228 offset:2448\n" \
  "ds_read_b64 v[46:47], v226 offset:2448\n" \
  "s_mul_i32 s98, s20, 589824\n" \
  "s_mul_i32 s100, s84, 1152\n" \
  "s_add_u32 s98, s98, s100\n" \
  "s_and_b32 s100, s85, 7\n" \
  "s_lshl_b32 s100, s100, 7\n" \
  "s_add_u32 s12, s98, s100\n" \
  "s_mul_i32 s98, s20, 30720\n" \
  "s_add_u32 s57, s98, 38272\n" \
  "s_lshl_b32 s98, s20, 10\n" \
  "s_add_u32 s8, s98, s82\n" \
  "s_lshl_b32 s98, s84, 2\n" \
  "s_add_u32 s98, s98, s20\n" \
  "s_lshl_b32 s98, s98, 16\n" \
  "s_lshl_b32 s100, s85, 11\n" \
  "s_add_u32 s98, s98, s100\n" \
  "s_add_u32 s24, s98, 5308416\n" \
  "s_add_u32 s10, s87, 7405568\n" \
  "s_lshl_b32 s98, s89, 16\n" \
  "s_lshl_b32 s100, s20, 14\n" \
  "s_add_u32 s98, s98, s100\n" \
  "s_add_u32 s14, s98, 5308416\n" \
  "s_mul_i32 s98, s88, 4505600\n" \
  "s_lshl_b32 s100, s90, 2\n" \
  "s_lshr_b32 s101, s84, 1\n" \
  "s_add_u32 s100, s100, s101\n" \
  "s_lshl_b32 s100, s100, 12\n" \
  "s_add_u32 s98, s98, s100\n" \
  "s_add_u32 s16, s98, 13090816\n" \
  "s_mul_i32 s98, s20, 4896\n" \
  "s_add_u32 s21, s98, 9216\n" \
  "s_mul_i32 s27, s88, 9232\n" \
  "s_lshl_b32 s98, s20, 2\n" \
  "s_add_u32 s28, s98, s28\n" \
  "s_lshl_b32 s98, s84, 8\n" \
  "s_add_u32 s29, s98, 2048\n" \
  "s_lshl_b32 s98, s85, 3\n" \
  "s_add_u32 s30, s98, s29\n" \
  "s_cmp_lt_u32 s20, 2\n" \
  "s_cselect_b32 s9, 8192, 0\n" \
  "s_add_u32 s9, s9, s8\n" \
  "s_mul_i32 s98, s84, 18\n" \
  "s_add_u32 s98, s98, s85\n" \
  "s_sub_u32 s81, s98, 14\n" \
  "s_cmp_lt_u32 s2, 112\n" \
  "s_cselect_b32 s81, 144, s81\n" \
  "s_cmp_eq_u32 s81, 128\n" \
  "s_cselect_b32 s32, 2, 0\n" \
  "s_or_b32 s27, s27, s32\n" \
  "s_lshl_b32 s98, s81, 6\n" \
  "s_add_u32 s22, s98, 524288\n" \
  "s_lshl_b32 s98, s81, 4\n" \
  "s_add_u32 s32, s98, 24576\n" \
  "s_load_dwordx4 s[60:63], s[76:77], s32\n" \
  "v_lshrrev_b32 v240, 5, v0\n" \
  "v_lshlrev_b32 v238, 4, v240\n" \
  "v_lshl_add_u32 v240, v0, 3, v238\n" \
  "v_add_u32 v238, 35216, v240\n" \
  "v_add_u32 v236, 32496, v240\n" \
  "v_add_u32 v234, 37392, v240\n" \
  "v_add_u32 v232, 34672, v240\n" \
  "v_and_b32 v240, 48, v191\n" \
  "v_lshl_add_u32 v230, v248, 6, v240\n" \
  "v_cmp_gt_u32 vcc, 32, v0\n" \
  "v_mov_b32 v48, 1.0\n" \
  "s_nop 0\n" \
  "v_cndmask_b32 v236, v236, v234, vcc\n" \
  "v_cndmask_b32 v48, v48, 0, vcc\n" \
  "v_cndmask_b32 v240, v240, v230, s[58:59]\n" \
  "v_mov_b32 v53, 0\n" \
  "v_mov_b32 v57, 0\n" \
  "v_mov_b32 v61, 0\n" \
  "v_mov_b32 v65, 0\n" \
  "s_sub_u32 s98, s85, 14\n" \
  "s_mul_i32 s98, s98, 160\n" \
  "v_add_u32 v248, s98, v0\n" \
  "v_mul_u32_u24 v234, 52429, v248\n" \
  "v_lshrrev_b32 v228, 18, v234\n" \
  "v_mul_u32_u24 v234, 6272, v228\n" \
  "v_lshl_add_u32 v228, v248, 7, v234\n" \
  "s_mul_i32 s98, s84, 576\n" \
  "s_add_u32 s98, s98, 2304\n" \
  "s_and_b32 s25, s98, 16777088\n" \
  "v_lshrrev_b32 v248, 2, v0\n" \
  "v_mul_u32_u24 v234, 9344, v248\n" \
  "v_and_b32 v248, 3, v0\n" \
  "v_lshl_add_u32 v226, v248, 4, v234\n" \
  "v_lshrrev_b32 v248, 4, v0\n" \
  "v_xor_b32 v225, v0, v248\n" \
  "v_and_b32 v248, 3, v225\n" \
  "v_lshl_add_u32 v225, v248, 4, v234\n" \
  "s_movk_i32 s32, 8192\n" \
  "buffer_load_dwordx4 v[66:69], v246, s[36:39], s32 offen offset:0 sc1\n" \
  "s_movk_i32 s32, 17408\n" \
  "buffer_load_dwordx4 v[70:73], v246, s[36:39], s32 offen offset:0 sc1\n" \
  "s_movk_i32 s32, 26624\n" \
  "buffer_load_dwordx4 v[74:77], v246, s[36:39], s32 offen offset:0 sc1\n" \
  "s_movk_i32 s32, 12288\n" \
  "buffer_load_dwordx4 v[78:81], v246, s[36:39], s32 offen offset:0 sc1\n" \
  "s_movk_i32 s32, 21504\n" \
  "buffer_load_dwordx4 v[82:85], v246, s[36:39], s32 offen offset:0 sc1\n" \
  "s_movk_i32 s32, 30720\n" \
  "buffer_load_dwordx4 v[86:89], v246, s[36:39], s32 offen offset:0 sc1\n" \
  "s_movk_i32 s32, 16384\n" \
  "buffer_load_dwordx4 v[90:93], v245, s[36:39], s32 offen offset:0 sc1\n" \
  "s_movk_i32 s32, 25600\n" \
  "buffer_load_dwordx4 v[94:97], v245, s[36:39], s32 offen offset:0 sc1\n" \
  "s_mov_b32 s32, 34816\n" \
  "buffer_load_dwordx4 v[98:101], v245, s[36:39], s32 offen offset:0 sc1\n" \
  "s_lshl_b32 s98, s2, 3\n" \
  "s_add_u32 s56, s98, 4096\n" \
  "s_lshl_b32 s98, s20, 10\n" \
  "s_add_u32 s33, s98, 62848\n" \
  "v_mov_b32 v102, s18\n" \
  "s_and_b32 s32, s19, 16777215\n" \
  "s_waitcnt vmcnt(9) lgkmcnt(0)\n" \
  "s_barrier\n" \
  "ds_read_b32 v103, v255 offset:4\n" \
  "s_cmp_eq_u32 s20, 3\n" \
  "s_waitcnt lgkmcnt(0)\n" \
  "v_lshrrev_b32 v103, 23, v103\n" \
  "v_lshl_or_b32 v103, v103, 24, s32\n" \
  "s_cselect_b64 exec, 1, 0\n" \
  "buffer_store_dwordx2 v[102:103], off, s[36:39], s56 sc1\n" \
  "s_mov_b64 exec, -1\n" \
  "s_add_u32 m0, s33, 0\n" \
  "s_movk_i32 s32, 0\n" \
  "buffer_load_dwordx4 v246, s[76:79], s32 offen offset:0 lds\n" \
  "s_add_u32 m0, s33, 30720\n" \
  "s_movk_i32 s32, 4096\n" \
  "buffer_load_dwordx4 v246, s[76:79], s32 offen offset:0 lds\n" \
  "s_add_u32 m0, s33, 61440\n" \
  "s_movk_i32 s32, 8192\n" \
  "buffer_load_dwordx4 v246, s[76:79], s32 offen offset:0 lds\n" \
  "s_movk_i32 s32, 14336\n" \
  "buffer_load_dwordx4 v[102:105], v246, s[76:79], s32 offen offset:0\n" \
  "s_movk_i32 s32, 23552\n" \
  "buffer_load_dwordx4 v[106:109], v252, s[76:79], s32 offen offset:0\n" \
  "s_movk_i32 s32, 18432\n" \
  "buffer_load_dwordx4 v[110:113], v246, s[76:79], s32 offen offset:0\n" \
  "s_waitcnt vmcnt(7)\n" \
  ".Lacc_%=:\n" \
  "v_xor_b32 v248, s18, v66\n" \
  "v_bitop3_b32 v248, v67, s19, v248 bitop3:0xbe\n" \
  "v_bitop3_b32 v248, v70, s18, v248 bitop3:0xbe\n" \
  "v_bitop3_b32 v248, v71, s19, v248 bitop3:0xbe\n" \
  "v_bitop3_b32 v248, v74, s18, v248 bitop3:0xbe\n" \
  "v_bitop3_b32 v248, v75, s19, v248 bitop3:0xbe\n" \
  "v_bitop3_b32 v248, v78, s18, v248 bitop3:0xbe\n" \
  "v_bitop3_b32 v248, v79, s19, v248 bitop3:0xbe\n" \
  "v_bitop3_b32 v248, v82, s18, v248 bitop3:0xbe\n" \
  "v_bitop3_b32 v248, v83, s19, v248 bitop3:0xbe\n" \
  "v_bitop3_b32 v248, v86, s18, v248 bitop3:0xbe\n" \
  "v_bitop3_b32 v248, v87, s19, v248 bitop3:0xbe\n" \
  "v_xor_b32 v234, s18, v90\n" \
  "v_bitop3_b32 v234, v91, s19, v234 bitop3:0xbe\n" \
  "v_bitop3_b32 v234, v94, s18, v234 bitop3:0xbe\n" \
  "v_bitop3_b32 v234, v95, s19, v234 bitop3:0xbe\n" \
  "v_bitop3_b32 v234, v98, s18, v234 bitop3:0xbe\n" \
  "v_bitop3_b32 v234, v99, s19, v234 bitop3:0xbe\n" \
  "v_bitop3_b32 v248, v234, s58, v248 bitop3:0xea\n" \
  "v_cmp_ne_u32 vcc, 0, v248\n" \
  "s_cbranch_vccnz .Lacs_%=\n" \
  ".Lacd_%=:\n" \
  "s_mov_b32 m0, 162176\n" \
  "s_cmp_lt_u32 s20, 2\n" \
  "s_cselect_b64 exec, -1, 0\n" \
  "s_cmp_eq_u32 s20, 2\n" \
  "s_cselect_b32 exec_lo, -1, exec_lo\n" \
  "s_cmp_lt_u32 s2, 112\n" \
  "s_cselect_b64 exec, 0, exec\n" \
  "buffer_load_dword v228, s[64:67], s25 offen lds\n" \
  "s_mov_b64 exec, -1\n" \
  "v_mov_b32 v248, 0\n" \
  "v_pk_add_f32 v[114:115], v[68:69], v[72:73]\n" \
  "v_pk_add_f32 v[114:115], v[114:115], v[76:77]\n" \
  "v_cvt_pk_bf16_f32 v245, v114, v115\n" \
  "v_lshlrev_b32 v114, 16, v245\n" \
  "v_and_b32 v115, 4294901760, v245\n" \
  "ds_write_b64 v238, v[114:115] offset:0\n" \
  "v_max3_f32 v248, |v114|, |v115|, v248\n" \
  "v_pk_add_f32 v[66:67], v[80:81], v[84:85]\n" \
  "v_pk_add_f32 v[66:67], v[66:67], v[88:89]\n" \
  "v_cvt_pk_bf16_f32 v245, v66, v67\n" \
  "v_lshlrev_b32 v66, 16, v245\n" \
  "v_and_b32 v67, 4294901760, v245\n" \
  "v_pk_add_f32 v[66:67], v[66:67], v[48:49] op_sel_hi:[1,0]\n" \
  "v_cvt_pk_bf16_f32 v245, v66, v67\n" \
  "v_lshlrev_b32 v66, 16, v245\n" \
  "v_and_b32 v67, 4294901760, v245\n" \
  "ds_write_b64 v236, v[66:67] offset:0\n" \
  "v_max3_f32 v248, |v66|, |v67|, v248\n" \
  "s_mov_b64 exec, s[58:59]\n" \
  "v_pk_add_f32 v[48:49], v[92:93], v[96:97]\n" \
  "v_pk_add_f32 v[48:49], v[48:49], v[100:101]\n" \
  "v_cvt_pk_bf16_f32 v245, v48, v49\n" \
  "v_lshlrev_b32 v48, 16, v245\n" \
  "v_and_b32 v49, 4294901760, v245\n" \
  "v_add_f32 v48, 1.0, v48\n" \
  "v_add_f32 v49, 1.0, v49\n" \
  "v_cvt_pk_bf16_f32 v245, v48, v49\n" \
  "v_lshlrev_b32 v48, 16, v245\n" \
  "v_and_b32 v49, 4294901760, v245\n" \
  "ds_write_b64 v232, v[48:49] offset:0\n" \
  "v_max3_f32 v248, |v48|, |v49|, v248\n" \
  "s_mov_b64 exec, -1\n" \
  "s_nop 0\n" \
  "v_max_u32_dpp v248, v248, v248 quad_perm:[1,0,3,2] row_mask:0xf bank_mask:0xf\n" \
  "s_nop 1\n" \
  "v_max_u32_dpp v248, v248, v248 quad_perm:[2,3,0,1] row_mask:0xf bank_mask:0xf\n" \
  "s_nop 1\n" \
  "v_max_u32_dpp v248, v248, v248 row_half_mirror row_mask:0xf bank_mask:0xf\n" \
  "s_nop 1\n" \
  "v_max_u32_dpp v248, v248, v248 row_mirror row_mask:0xf bank_mask:0xf\n" \
  "s_mov_b32 exec_lo, 65537\n" \
  "s_mov_b32 exec_hi, 65537\n" \
  "ds_max_u32 v255, v248\n" \
  "s_mov_b64 exec, -1\n" \
  "s_waitcnt vmcnt(1) lgkmcnt(0)\n" \
  "s_barrier\n" \
  "ds_read_b32 v48, v255 offset:0\n" \
  "ds_read_b64 v[66:67], v241 offset:32768\n" \
  "ds_read_b64 v[68:69], v241 offset:35216\n" \
  "ds_read_b64 v[70:71], v239 offset:32768\n" \
  "ds_read_b64 v[72:73], v239 offset:35216\n" \
  "ds_read_b64 v[74:75], v237 offset:32768\n" \
  "ds_read_b64 v[76:77], v237 offset:35216\n" \
  "ds_read_b64 v[78:79], v235 offset:32768\n" \
  "ds_read_b64 v[80:81], v235 offset:35216\n" \
  "ds_read_b64 v[82:83], v233 offset:32768\n" \
  "ds_read_b64 v[84:85], v233 offset:35216\n" \
  "ds_read_b64 v[86:87], v231 offset:32768\n" \
  "ds_read_b64 v[88:89], v231 offset:35216\n" \
  "ds_read_b64 v[90:91], v229 offset:32768\n" \
  "ds_read_b64 v[92:93], v229 offset:35216\n" \
  "ds_read_b64 v[94:95], v227 offset:32768\n" \
  "ds_read_b64 v[96:97], v227 offset:35216\n" \
  "v_add_u32 v255, 62848, v252\n" \
  "ds_read_b128 a[200:203], v255 offset:0\n" \
  "ds_read_b128 a[204:207], v255 offset:1024\n" \
  "ds_read_b128 a[168:171], v255 offset:2048\n" \
  "ds_read_b128 a[172:175], v255 offset:3072\n" \
  "s_waitcnt lgkmcnt(15)\n" \
  "v_lshrrev_b32 v48, 23, v48\n" \
  "v_max_u32 v48, 64, v48\n" \
  "s_nop 0\n" \
  "v_readfirstlane_b32 s26, v48\n" \
  "v_sub_u32 v48, 256, v48\n" \
  "v_lshlrev_b32 v48, 23, v48\n" \
  "v_pk_mul_f32 v[66:67], v[66:67], v[48:49] op_sel_hi:[1,0]\n" \
  "v_pk_mul_f32 v[68:69], v[68:69], v[48:49] op_sel_hi:[1,0]\n" \
  "v_pk_fma_f32 v[98:99], v[10:11], v[66:67], v[68:69]\n" \
  "v_pk_fma_f32 v[10:11], v[32:33], v[66:67], v[68:69]\n" \
  "v_pk_mul_f32 v[70:71], v[70:71], v[48:49] op_sel_hi:[1,0]\n" \
  "v_pk_mul_f32 v[72:73], v[72:73], v[48:49] op_sel_hi:[1,0]\n" \
  "v_pk_fma_f32 v[32:33], v[12:13], v[70:71], v[72:73]\n" \
  "v_pk_fma_f32 v[12:13], v[34:35], v[70:71], v[72:73]\n" \
  "v_pk_mul_f32 v[74:75], v[74:75], v[48:49] op_sel_hi:[1,0]\n" \
  "s_waitcnt lgkmcnt(14)\n" \
  "v_pk_mul_f32 v[76:77], v[76:77], v[48:49] op_sel_hi:[1,0]\n" \
  "v_pk_fma_f32 v[34:35], v[14:15], v[74:75], v[76:77]\n" \
  "v_pk_fma_f32 v[14:15], v[36:37], v[74:75], v[76:77]\n" \
  "s_waitcnt lgkmcnt(13)\n" \
  "v_pk_mul_f32 v[78:79], v[78:79], v[48:49] op_sel_hi:[1,0]\n" \
  "s_waitcnt lgkmcnt(12)\n" \
  "v_pk_mul_f32 v[80:81], v[80:81], v[48:49] op_sel_hi:[1,0]\n" \
  "v_pk_fma_f32 v[36:37], v[18:19], v[78:79], v[80:81]\n" \
  "v_pk_fma_f32 v[18:19], v[38:39], v[78:79], v[80:81]\n" \
  "s_waitcnt lgkmcnt(11)\n" \
  "v_pk_mul_f32 v[82:83], v[82:83], v[48:49] op_sel_hi:[1,0]\n" \
  "s_waitcnt lgkmcnt(10)\n" \
  "v_pk_mul_f32 v[84:85], v[84:85], v[48:49] op_sel_hi:[1,0]\n" \
  "v_pk_fma_f32 v[38:39], v[20:21], v[82:83], v[84:85]\n" \
  "v_pk_fma_f32 v[20:21], v[40:41], v[82:83], v[84:85]\n" \
  "s_waitcnt lgkmcnt(9)\n" \
  "v_pk_mul_f32 v[86:87], v[86:87], v[48:49] op_sel_hi:[1,0]\n" \
  "s_waitcnt lgkmcnt(8)\n" \
  "v_pk_mul_f32 v[88:89], v[88:89], v[48:49] op_sel_hi:[1,0]\n" \
  "v_pk_fma_f32 v[40:41], v[26:27], v[86:87], v[88:89]\n" \
  "v_pk_fma_f32 v[26:27], v[42:43], v[86:87], v[88:89]\n" \
  "s_waitcnt lgkmcnt(7)\n" \
  "v_pk_mul_f32 v[90:91], v[90:91], v[48:49] op_sel_hi:[1,0]\n" \
  "s_waitcnt lgkmcnt(6)\n" \
  "v_pk_mul_f32 v[92:93], v[92:93], v[48:49] op_sel_hi:[1,0]\n" \
  "v_pk_fma_f32 v[42:43], v[28:29], v[90:91], v[92:93]\n" \
  "v_pk_fma_f32 v[28:29], v[44:45], v[90:91], v[92:93]\n" \
  "s_waitcnt lgkmcnt(5)\n" \
  "v_pk_mul_f32 v[94:95], v[94:95], v[48:49] op_sel_hi:[1,0]\n" \
  "s_waitcnt lgkmcnt(4)\n" \
  "v_pk_mul_f32 v[96:97], v[96:97], v[48:49] op_sel_hi:[1,0]\n" \
  "v_pk_fma_f32 v[44:45], v[30:31], v[94:95], v[96:97]\n" \
  "v_pk_fma_f32 v[30:31], v[46:47], v[94:95], v[96:97]\n" \
  "v_cvt_pk_f16_f32 v46, v98, v99\n" \
  "v_cvt_pk_f16_f32 v47, v32, v33\n" \
  "v_cvt_pk_f16_f32 v48, v34, v35\n" \
  "v_cvt_pk_f16_f32 v49, v36, v37\n" \
  "v_cvt_pk_f16_f32 v32, v38, v39\n" \
  "v_cvt_pk_f16_f32 v33, v40, v41\n" \
  "s_waitcnt lgkmcnt(3)\n" \
  "v_mfma_f32_16x16x32_f16 v[36:39], v[46:49], a[200:203], 0\n" \
  "v_cvt_pk_f16_f32 v34, v42, v43\n" \
  "v_cvt_pk_f16_f32 v35, v44, v45\n" \
  "s_waitcnt lgkmcnt(2)\n" \
  "v_mfma_f32_16x16x32_f16 v[40:43], v[46:49], a[204:207], 0\n" \
  "v_cvt_pk_f16_f32 v44, v10, v11\n" \
  "v_cvt_pk_f16_f32 v45, v12, v13\n" \
  "s_waitcnt lgkmcnt(1)\n" \
  "v_mfma_f32_16x16x32_f16 v[10:13], v[32:35], a[168:171], 0\n" \
  "v_cvt_pk_f16_f32 v46, v14, v15\n" \
  "v_cvt_pk_f16_f32 v47, v18, v19\n" \
  "s_waitcnt lgkmcnt(0)\n" \
  "v_mfma_f32_16x16x32_f16 v[66:69], v[32:35], a[172:175], 0\n" \
  "v_cvt_pk_f16_f32 v32, v20, v21\n" \
  "v_cvt_pk_f16_f32 v33, v26, v27\n" \
  "v_mfma_f32_16x16x32_f16 v[18:21], v[44:47], a[200:203], 0\n" \
  "v_cvt_pk_f16_f32 v34, v28, v29\n" \
  "v_cvt_pk_f16_f32 v35, v30, v31\n" \
  "v_mfma_f32_16x16x32_f16 v[26:29], v[44:47], a[204:207], 0\n" \
  "ds_read_b128 a[208:211], v255 offset:30720\n" \
  "v_mfma_f32_16x16x32_f16 v[44:47], v[32:35], a[168:171], 0\n" \
  "ds_read_b128 a[212:215], v255 offset:31744\n" \
  "v_mfma_f32_16x16x32_f16 v[70:73], v[32:35], a[172:175], 0\n" \
  "ds_read_b128 a[220:223], v255 offset:32768\n" \
  "ds_read_b128 a[224:227], v255 offset:33792\n" \
  "ds_read_b128 v[74:77], v255 offset:61440\n" \
  "ds_read_b128 v[78:81], v255 offset:62464\n" \
  "v_pk_add_f32 v[30:31], v[36:37], v[10:11]\n" \
  "v_pk_add_f32 v[32:33], v[38:39], v[12:13]\n" \
  "v_pk_add_f32 v[82:83], v[40:41], v[66:67]\n" \
  "v_pk_add_f32 v[84:85], v[42:43], v[68:69]\n" \
  "v_pk_add_f32 v[86:87], v[36:37], v[10:11] neg_lo:[0,1] neg_hi:[0,1]\n" \
  "v_pk_add_f32 v[88:89], v[38:39], v[12:13] neg_lo:[0,1] neg_hi:[0,1]\n" \
  "v_pk_add_f32 v[10:11], v[40:41], v[66:67] neg_lo:[0,1] neg_hi:[0,1]\n" \
  "v_pk_add_f32 v[12:13], v[42:43], v[68:69] neg_lo:[0,1] neg_hi:[0,1]\n" \
  "v_pk_add_f32 v[34:35], v[18:19], v[44:45]\n" \
  "v_pk_add_f32 v[36:37], v[20:21], v[46:47]\n" \
  "v_pk_add_f32 v[38:39], v[26:27], v[70:71]\n" \
  "v_pk_add_f32 v[40:41], v[28:29], v[72:73]\n" \
  "v_pk_add_f32 v[66:67], v[18:19], v[44:45] neg_lo:[0,1] neg_hi:[0,1]\n" \
  "v_pk_add_f32 v[68:69], v[20:21], v[46:47] neg_lo:[0,1] neg_hi:[0,1]\n" \
  "v_pk_add_f32 v[18:19], v[26:27], v[70:71] neg_lo:[0,1] neg_hi:[0,1]\n" \
  "v_pk_add_f32 v[20:21], v[28:29], v[72:73] neg_lo:[0,1] neg_hi:[0,1]\n" \
  "v_cvt_pk_f16_f32 v50, v30, v82\n" \
  "v_cvt_pk_f16_f32 v51, v31, v83\n" \
  "v_cvt_pk_f16_f32 v52, v32, v84\n" \
  "v_cvt_pk_f16_f32 v54, v86, v10\n" \
  "v_cvt_pk_f16_f32 v55, v87, v11\n" \
  "s_waitcnt lgkmcnt(5)\n" \
  "v_mfma_f32_16x16x32_f16 v[26:29], a[208:211], v[50:53], 0\n" \
  "v_cvt_pk_f16_f32 v56, v88, v12\n" \
  "s_waitcnt lgkmcnt(4)\n" \
  "v_mfma_f32_16x16x32_f16 v[10:13], a[212:215], v[50:53], 0\n" \
  "v_cvt_pk_f16_f32 v58, v34, v38\n" \
  "v_cvt_pk_f16_f32 v59, v35, v39\n" \
  "v_mfma_f32_16x16x32_f16 v[30:33], a[208:211], v[54:57], 0\n" \
  "v_cvt_pk_f16_f32 v60, v36, v40\n" \
  "v_mfma_f32_16x16x32_f16 v[34:37], a[212:215], v[54:57], 0\n" \
  "v_cvt_pk_f16_f32 v62, v66, v18\n" \
  "v_cvt_pk_f16_f32 v63, v67, v19\n" \
  "v_mfma_f32_16x16x32_f16 v[38:41], a[208:211], v[58:61], 0\n" \
  "v_cvt_pk_f16_f32 v64, v68, v20\n" \
  "v_mfma_f32_16x16x32_f16 v[18:21], a[212:215], v[58:61], 0\n" \
  "s_movk_i32 s32, 4096\n" \
  "buffer_load_dwordx4 v[42:45], v252, s[36:39], s32 offen offset:0 sc1\n" \
  "buffer_load_dwordx4 v[46:49], v252, s[36:39], s32 offen offset:1024 sc1\n" \
  "v_mfma_f32_16x16x32_f16 v[66:69], a[208:211], v[62:65], 0\n" \
  "v_mfma_f32_16x16x32_f16 v[70:73], a[212:215], v[62:65], 0\n" \
  "s_mov_b32 exec_hi, 65535\n" \
  "v_cvt_pk_f16_f32 v14, v26, v10\n" \
  "s_nop 0\n" \
  "v_cvt_pk_f16_f32 v15, v38, v18\n" \
  "ds_write_b64 v102, v[14:15] offset:0\n" \
  "v_cvt_pk_f16_f32 v14, v27, v11\n" \
  "v_cvt_pk_f16_f32 v15, v39, v19\n" \
  "ds_write_b64 v102, v[14:15] offset:512\n" \
  "v_cvt_pk_f16_f32 v14, v28, v12\n" \
  "v_cvt_pk_f16_f32 v15, v40, v20\n" \
  "ds_write_b64 v102, v[14:15] offset:1024\n" \
  "v_cvt_pk_f16_f32 v10, v30, v34\n" \
  "v_cvt_pk_f16_f32 v11, v66, v70\n" \
  "ds_write_b64 v102, v[10:11] offset:256\n" \
  "v_cvt_pk_f16_f32 v10, v31, v35\n" \
  "v_cvt_pk_f16_f32 v11, v67, v71\n" \
  "ds_write_b64 v102, v[10:11] offset:768\n" \
  "v_cvt_pk_f16_f32 v10, v32, v36\n" \
  "v_cvt_pk_f16_f32 v11, v68, v72\n" \
  "ds_write_b64 v102, v[10:11] offset:1280\n" \
  "s_mov_b64 exec, -1\n" \
  "s_waitcnt lgkmcnt(0)\n" \
  "s_barrier\n" \
  "ds_read_b128 v[10:13], v103 offset:0\n" \
  "ds_read_b128 v[18:21], v104 offset:0\n" \
  "ds_read_b128 v[26:29], v105 offset:0\n" \
  "v_lshrrev_b32 v255, 1, v191\n" \
  "v_and_b32 v248, 1, v191\n" \
  "s_lshl_b32 s98, s20, 1\n" \
  "v_add_u32 v245, s98, v248\n" \
  "v_and_b32 v248, 6, v255\n" \
  "v_xor_b32 v241, v245, v248\n" \
  "v_lshlrev_b32 v248, 4, v241\n" \
  "v_lshl_add_u32 v245, v255, 7, v248\n" \
  "v_add_u32 v255, 155008, v245\n" \
  "v_lshlrev_b32 v248, 15, v253\n" \
  "v_sub_u32 v245, v255, v248\n" \
  "s_cmp_eq_u32 s20, 1\n" \
  "s_cselect_b32 s92, 65537, s58\n" \
  "s_cselect_b32 s93, 65537, s59\n" \
  "s_add_u32 s32, s8, 4096\n" \
  "s_add_u32 s33, s8, 8192\n" \
  "s_waitcnt lgkmcnt(2)\n" \
  "v_mfma_f32_16x16x32_f16 v[30:33], a[160:163], v[10:13], 0\n" \
  "v_mfma_f32_16x16x32_f16 v[34:37], a[164:167], v[10:13], 0\n" \
  "s_waitcnt lgkmcnt(1)\n" \
  "v_mfma_f32_16x16x32_f16 v[10:13], a[160:163], v[18:21], 0\n" \
  "v_mfma_f32_16x16x32_f16 v[38:41], a[164:167], v[18:21], 0\n" \
  "s_waitcnt lgkmcnt(0)\n" \
  "v_mfma_f32_16x16x32_f16 v[18:21], a[160:163], v[26:29], 0\n" \
  "v_mfma_f32_16x16x32_f16 v[66:69], a[164:167], v[26:29], 0\n" \
  "ds_read_b128 v[26:29], v255 offset:0\n" \
  "ds_read_b128 v[70:73], v245 offset:4096\n" \
  "v_cvt_pk_f16_f32 v82, v30, v34\n" \
  "v_cvt_pk_f16_f32 v83, v31, v35\n" \
  "v_cvt_pk_f16_f32 v84, v32, v36\n" \
  "v_cvt_pk_f16_f32 v85, v33, v37\n" \
  "v_and_b32 v82, -2, v82\n" \
  "buffer_store_dwordx4 v[82:85], v230, s[36:39], s8 offen offset:0 sc1\n" \
  "v_cvt_pk_f16_f32 v30, v10, v38\n" \
  "v_cvt_pk_f16_f32 v31, v11, v39\n" \
  "v_cvt_pk_f16_f32 v32, v12, v40\n" \
  "v_cvt_pk_f16_f32 v33, v13, v41\n" \
  "v_and_b32 v30, -2, v30\n" \
  "buffer_store_dwordx4 v[30:33], v230, s[36:39], s32 offen offset:0 sc1\n" \
  "v_cvt_pk_f16_f32 v10, v18, v66\n" \
  "v_cvt_pk_f16_f32 v11, v19, v67\n" \
  "v_cvt_pk_f16_f32 v12, v20, v68\n" \
  "v_cvt_pk_f16_f32 v13, v21, v69\n" \
  "v_and_b32 v10, -2, v10\n" \
  "s_mov_b64 exec, s[92:93]\n" \
  "buffer_store_dwordx4 v[10:13], v230, s[36:39], s33 offen offset:0 sc1\n" \
  "s_mov_b64 exec, -1\n" \
  "s_waitcnt vmcnt(3)\n" \
  ".Lycc_%=:\n" \
  "v_xor_b32 v255, s18, v42\n" \
  "v_bitop3_b32 v255, v44, s18, v255 bitop3:0xbe\n" \
  "v_bitop3_b32 v255, v46, s18, v255 bitop3:0xbe\n" \
  "v_bitop3_b32 v255, v48, s18, v255 bitop3:0xbe\n" \
  "v_xor_b32 v248, s19, v43\n" \
  "v_bitop3_b32 v248, v45, s19, v248 bitop3:0xbe\n" \
  "v_bitop3_b32 v248, v47, s19, v248 bitop3:0xbe\n" \
  "v_bitop3_b32 v248, v49, s19, v248 bitop3:0xbe\n" \
  "v_lshl_or_b32 v255, v248, 8, v255\n" \
  "v_cmp_ne_u32 vcc, 0, v255\n" \
  "s_cbranch_vccnz .Lycs_%=\n" \
  ".Lycd_%=:\n" \
  "s_lshl_b32 s33, s20, 10\n" \
  "s_add_u32 m0, s33, 16384\n" \
  "s_add_u32 s32, s22, 2392064\n" \
  "buffer_load_dwordx4 v225, s[36:39], s32 offen offset:0 sc1 lds\n" \
  "s_add_u32 m0, s33, 20480\n" \
  "s_add_u32 s32, s22, 2990080\n" \
  "buffer_load_dwordx4 v225, s[36:39], s32 offen offset:0 sc1 lds\n" \
  "s_add_u32 m0, s33, 24576\n" \
  "s_add_u32 s32, s22, 3588096\n" \
  "buffer_load_dwordx4 v225, s[36:39], s32 offen offset:0 sc1 lds\n" \
  "s_add_u32 m0, s33, 28672\n" \
  "s_add_u32 s32, s22, 4186112\n" \
  "buffer_load_dwordx4 v225, s[36:39], s32 offen offset:0 sc1 lds\n" \
  "s_bitcmp1_b32 s27, 1\n" \
  "s_cbranch_scc1 .Lws_%=\n" \
  ".Lwd_%=:\n" \
  "s_waitcnt lgkmcnt(1)\n" \
  "buffer_store_dwordx4 v[26:29], v252, s[36:39], s24 offen offset:0 sc1\n" \
  "s_waitcnt lgkmcnt(0)\n" \
  "buffer_store_dwordx4 v[70:73], v252, s[36:39], s24 offen offset:1024 sc1\n" \
  "v_max3_u32 v255, v43, v45, v47\n" \
  "v_max_u32 v255, v255, v49\n" \
  "s_nop 1\n" \
  "v_max_u32_dpp v255, v255, v255 quad_perm:[1,0,3,2] row_mask:0xf bank_mask:0xf\n" \
  "s_nop 1\n" \
  "v_max_u32_dpp v255, v255, v255 quad_perm:[2,3,0,1] row_mask:0xf bank_mask:0xf\n" \
  "s_nop 1\n" \
  "v_max_u32_dpp v255, v255, v255 row_half_mirror row_mask:0xf bank_mask:0xf\n" \
  "s_nop 1\n" \
  "v_max_u32_dpp v255, v255, v255 row_mirror row_mask:0xf bank_mask:0xf\n" \
  "s_nop 0\n" \
  "v_readlane_b32 s48, v255, 0\n" \
  "v_readlane_b32 s49, v255, 16\n" \
  "v_readlane_b32 s50, v255, 32\n" \
  "v_readlane_b32 s51, v255, 48\n" \
  "s_max_u32 s48, s48, s49\n" \
  "s_max_u32 s50, s50, s51\n" \
  "s_max_u32 s48, s48, s50\n" \
  "s_lshr_b32 s48, s48, 24\n" \
  "s_max_u32 s48, s48, 64\n" \
  "s_min_u32 s48, s48, 250\n" \
  "s_add_u32 s23, s26, s48\n" \
  "s_sub_u32 s23, s23, 253\n" \
  "s_bfe_u32 s32, s27, 65537\n" \
  "s_add_u32 s32, s32, s48\n" \
  "s_sub_u32 s32, 251, s32\n" \
  "s_lshl_b32 s32, s32, 23\n" \
  "v_mov_b32 v255, s32\n" \
  "v_mul_f32 v10, s60, v255\n" \
  "v_mul_f32 v11, s61, v255\n" \
  "v_mul_f32 v12, s62, v255\n" \
  "v_lshlrev_b32 v255, 5, v191\n" \
  "v_lshlrev_b32 v248, 2, v191\n" \
  "v_and_b32 v255, 1920, v255\n" \
  "v_and_b32 v248, 96, v248\n" \
  "v_add3_u32 v255, v255, v248, s57\n" \
  "v_and_b32 v248, 3, v191\n" \
  "v_lshl_add_u32 v18, v248, 3, v255\n" \
  "v_xor_b32 v19, 32, v18\n" \
  "v_xor_b32 v20, 64, v18\n" \
  "v_xor_b32 v21, 96, v18\n" \
  "s_lshl_b32 s33, s20, 10\n" \
  "s_waitcnt vmcnt(2)\n" \
  "s_barrier\n" \
  "ds_read_b64 v[14:15], v112 offset:0\n" \
  "ds_read_b64 v[26:27], v112 offset:1024\n" \
  "ds_read_b64 v[28:29], v112 offset:2048\n" \
  "ds_read_b64 v[30:31], v112 offset:3072\n" \
  "ds_read_b64 v[32:33], v113 offset:3072\n" \
  "ds_read_b64 v[34:35], v113 offset:2048\n" \
  "ds_read_b64 v[36:37], v113 offset:1024\n" \
  "ds_read_b64 v[38:39], v113 offset:0\n" \
  "s_add_u32 m0, s33, 0\n" \
  "s_add_u32 s32, s22, 0\n" \
  "buffer_load_dwordx4 v225, s[36:39], s32 offen offset:0 sc1 lds\n" \
  "s_add_u32 m0, s33, 4096\n" \
  "s_add_u32 s32, s22, 598016\n" \
  "buffer_load_dwordx4 v225, s[36:39], s32 offen offset:0 sc1 lds\n" \
  "s_add_u32 m0, s33, 8192\n" \
  "s_add_u32 s32, s22, 1196032\n" \
  "buffer_load_dwordx4 v225, s[36:39], s32 offen offset:0 sc1 lds\n" \
  "s_add_u32 m0, s33, 12288\n" \
  "s_add_u32 s32, s22, 1794048\n" \
  "buffer_load_dwordx4 v225, s[36:39], s32 offen offset:0 sc1 lds\n" \
  "s_waitcnt lgkmcnt(7)\n" \
  "v_lshlrev_b32 v40, 16, v14\n" \
  "v_and_b32 v44, 4294901760, v14\n" \
  "s_waitcnt lgkmcnt(3)\n" \
  "v_lshlrev_b32 v66, 16, v33\n" \
  "v_and_b32 v70, 4294901760, v33\n" \
  "v_lshlrev_b32 v41, 16, v26\n" \
  "v_and_b32 v45, 4294901760, v26\n" \
  "s_waitcnt lgkmcnt(2)\n" \
  "v_lshlrev_b32 v67, 16, v35\n" \
  "v_and_b32 v71, 4294901760, v35\n" \
  "v_lshlrev_b32 v42, 16, v28\n" \
  "v_and_b32 v46, 4294901760, v28\n" \
  "s_waitcnt lgkmcnt(1)\n" \
  "v_lshlrev_b32 v68, 16, v37\n" \
  "s_add_u32 m0, s57, 0\n" \
  "s_add_u32 s31, s12, 0\n" \
  "buffer_load_dwordx4 v22, s[4:7], s31 offen offset:0 lds\n" \
  "v_and_b32 v72, 4294901760, v37\n" \
  "v_lshlrev_b32 v43, 16, v30\n" \
  "v_and_b32 v47, 4294901760, v30\n" \
  "s_waitcnt lgkmcnt(0)\n" \
  "v_lshlrev_b32 v69, 16, v39\n" \
  "v_and_b32 v73, 4294901760, v39\n" \
  "v_lshlrev_b32 v82, 16, v15\n" \
  "v_and_b32 v86, 4294901760, v15\n" \
  "v_lshlrev_b32 v90, 16, v32\n" \
  "v_and_b32 v94, 4294901760, v32\n" \
  "buffer_load_dwordx4 v23, s[4:7], s31 offen offset:1024 lds\n" \
  "v_lshlrev_b32 v83, 16, v27\n" \
  "v_and_b32 v87, 4294901760, v27\n" \
  "v_lshlrev_b32 v91, 16, v34\n" \
  "v_and_b32 v95, 4294901760, v34\n" \
  "v_lshlrev_b32 v84, 16, v29\n" \
  "v_and_b32 v88, 4294901760, v29\n" \
  "v_lshlrev_b32 v92, 16, v36\n" \
  "v_and_b32 v96, 4294901760, v36\n" \
  "v_lshlrev_b32 v85, 16, v31\n" \
  "buffer_load_dwordx4 v24, s[4:7], s31 offen offset:2048 lds\n" \
  "v_and_b32 v89, 4294901760, v31\n" \
  "v_lshlrev_b32 v93, 16, v38\n" \
  "v_and_b32 v97, 4294901760, v38\n" \
  "s_nop 1\n" \
  "s_bitcmp1_b32 s27, 1\n" \
  "s_cbranch_scc1 .Lxs_%=\n" \
  ".Lxd_%=:\n" \
  "v_pk_mul_f32 v[14:15], v[40:41], v[10:11] op_sel:[0,0] op_sel_hi:[1,0]\n" \
  "v_pk_mul_f32 v[26:27], v[44:45], v[10:11] op_sel:[0,0] op_sel_hi:[1,0]\n" \
  "v_pk_mul_f32 v[28:29], v[40:41], v[10:11] op_sel:[0,1] op_sel_hi:[1,1]\n" \
  "v_pk_mul_f32 v[32:33], v[44:45], v[10:11] op_sel:[0,1] op_sel_hi:[1,1]\n" \
  "v_pk_add_f32 v[36:37], v[44:45], v[70:71]\n" \
  "buffer_load_dwordx4 v25, s[4:7], s31 offen offset:3072 lds\n" \
  "v_pk_add_f32 v[38:39], v[40:41], v[66:67] neg_lo:[0,1] neg_hi:[0,1]\n" \
  "v_pk_fma_f32 v[40:41], v[66:67], v[10:11], v[14:15] op_sel:[0,1,0] op_sel_hi:[1,1,1]\n" \
  "v_pk_fma_f32 v[44:45], v[70:71], v[10:11], v[26:27] op_sel:[0,1,0] op_sel_hi:[1,1,1] neg_lo:[1,0,0] neg_hi:[1,0,0]\n" \
  "v_pk_fma_f32 v[28:29], v[66:67], v[10:11], v[28:29] op_sel:[0,0,0] op_sel_hi:[1,0,1]\n" \
  "v_pk_fma_f32 v[32:33], v[70:71], v[10:11], v[32:33] op_sel:[0,0,0] op_sel_hi:[1,0,1] neg_lo:[1,0,0] neg_hi:[1,0,0]\n" \
  "v_pk_mul_f32 v[66:67], v[36:37], v[12:13] op_sel:[0,0] op_sel_hi:[1,0] neg_lo:[1,0] neg_hi:[1,0]\n" \
  "v_pk_mul_f32 v[70:71], v[38:39], v[12:13] op_sel:[0,0] op_sel_hi:[1,0]\n" \
  "v_pk_mul_f32 v[14:15], v[42:43], v[10:11] op_sel:[0,0] op_sel_hi:[1,0]\n" \
  "v_pk_mul_f32 v[26:27], v[46:47], v[10:11] op_sel:[0,0] op_sel_hi:[1,0]\n" \
  "s_add_u32 m0, s57, 4096\n" \
  "s_add_u32 s31, s12, 9437184\n" \
  "buffer_load_dwordx4 v22, s[4:7], s31 offen offset:0 lds\n" \
  "v_pk_mul_f32 v[30:31], v[42:43], v[10:11] op_sel:[0,1] op_sel_hi:[1,1]\n" \
  "v_pk_mul_f32 v[34:35], v[46:47], v[10:11] op_sel:[0,1] op_sel_hi:[1,1]\n" \
  "v_pk_add_f32 v[36:37], v[46:47], v[72:73]\n" \
  "v_pk_add_f32 v[38:39], v[42:43], v[68:69] neg_lo:[0,1] neg_hi:[0,1]\n" \
  "v_pk_fma_f32 v[42:43], v[68:69], v[10:11], v[14:15] op_sel:[0,1,0] op_sel_hi:[1,1,1]\n" \
  "v_pk_fma_f32 v[46:47], v[72:73], v[10:11], v[26:27] op_sel:[0,1,0] op_sel_hi:[1,1,1] neg_lo:[1,0,0] neg_hi:[1,0,0]\n" \
  "v_pk_fma_f32 v[30:31], v[68:69], v[10:11], v[30:31] op_sel:[0,0,0] op_sel_hi:[1,0,1]\n" \
  "v_pk_fma_f32 v[34:35], v[72:73], v[10:11], v[34:35] op_sel:[0,0,0] op_sel_hi:[1,0,1] neg_lo:[1,0,0] neg_hi:[1,0,0]\n" \
  "v_pk_mul_f32 v[68:69], v[36:37], v[12:13] op_sel:[0,0] op_sel_hi:[1,0] neg_lo:[1,0] neg_hi:[1,0]\n" \
  "buffer_load_dwordx4 v23, s[4:7], s31 offen offset:1024 lds\n" \
  "v_pk_mul_f32 v[72:73], v[38:39], v[12:13] op_sel:[0,0] op_sel_hi:[1,0]\n" \
  "v_pk_mul_f32 v[14:15], v[82:83], v[10:11] op_sel:[0,0] op_sel_hi:[1,0]\n" \
  "v_pk_mul_f32 v[26:27], v[86:87], v[10:11] op_sel:[0,0] op_sel_hi:[1,0]\n" \
  "v_pk_mul_f32 v[36:37], v[82:83], v[10:11] op_sel:[0,1] op_sel_hi:[1,1]\n" \
  "v_pk_mul_f32 v[98:99], v[86:87], v[10:11] op_sel:[0,1] op_sel_hi:[1,1]\n" \
  "v_pk_add_f32 v[48:49], v[86:87], v[94:95]\n" \
  "v_pk_add_f32 v[114:115], v[82:83], v[90:91] neg_lo:[0,1] neg_hi:[0,1]\n" \
  "v_pk_fma_f32 v[82:83], v[90:91], v[10:11], v[14:15] op_sel:[0,1,0] op_sel_hi:[1,1,1]\n" \
  "v_pk_fma_f32 v[86:87], v[94:95], v[10:11], v[26:27] op_sel:[0,1,0] op_sel_hi:[1,1,1] neg_lo:[1,0,0] neg_hi:[1,0,0]\n" \
  "buffer_load_dwordx4 v24, s[4:7], s31 offen offset:2048 lds\n" \
  "v_pk_fma_f32 v[36:37], v[90:91], v[10:11], v[36:37] op_sel:[0,0,0] op_sel_hi:[1,0,1]\n" \
  "v_pk_fma_f32 v[98:99], v[94:95], v[10:11], v[98:99] op_sel:[0,0,0] op_sel_hi:[1,0,1] neg_lo:[1,0,0] neg_hi:[1,0,0]\n" \
  "v_pk_mul_f32 v[90:91], v[48:49], v[12:13] op_sel:[0,0] op_sel_hi:[1,0] neg_lo:[1,0] neg_hi:[1,0]\n" \
  "v_pk_mul_f32 v[94:95], v[114:115], v[12:13] op_sel:[0,0] op_sel_hi:[1,0]\n" \
  "v_pk_mul_f32 v[14:15], v[84:85], v[10:11] op_sel:[0,0] op_sel_hi:[1,0]\n" \
  "v_pk_mul_f32 v[26:27], v[88:89], v[10:11] op_sel:[0,0] op_sel_hi:[1,0]\n" \
  "v_pk_mul_f32 v[38:39], v[84:85], v[10:11] op_sel:[0,1] op_sel_hi:[1,1]\n" \
  "v_pk_mul_f32 v[100:101], v[88:89], v[10:11] op_sel:[0,1] op_sel_hi:[1,1]\n" \
  "v_pk_add_f32 v[48:49], v[88:89], v[96:97]\n" \
  "buffer_load_dwordx4 v25, s[4:7], s31 offen offset:3072 lds\n" \
  "v_pk_add_f32 v[114:115], v[84:85], v[92:93] neg_lo:[0,1] neg_hi:[0,1]\n" \
  "v_pk_fma_f32 v[84:85], v[92:93], v[10:11], v[14:15] op_sel:[0,1,0] op_sel_hi:[1,1,1]\n" \
  "v_pk_fma_f32 v[88:89], v[96:97], v[10:11], v[26:27] op_sel:[0,1,0] op_sel_hi:[1,1,1] neg_lo:[1,0,0] neg_hi:[1,0,0]\n" \
  "v_pk_fma_f32 v[38:39], v[92:93], v[10:11], v[38:39] op_sel:[0,0,0] op_sel_hi:[1,0,1]\n" \
  "v_pk_fma_f32 v[100:101], v[96:97], v[10:11], v[100:101] op_sel:[0,0,0] op_sel_hi:[1,0,1] neg_lo:[1,0,0] neg_hi:[1,0,0]\n" \
  "v_pk_mul_f32 v[92:93], v[48:49], v[12:13] op_sel:[0,0] op_sel_hi:[1,0] neg_lo:[1,0] neg_hi:[1,0]\n" \
  "v_pk_mul_f32 v[96:97], v[114:115], v[12:13] op_sel:[0,0] op_sel_hi:[1,0]\n" \
  "v_lshlrev_b32 v10, 16, v2\n" \
  "v_and_b32 v11, 4294901760, v2\n" \
  "v_lshlrev_b32 v12, 16, v3\n" \
  "v_and_b32 v13, 4294901760, v3\n" \
  "v_lshlrev_b32 v14, 16, v4\n" \
  "v_and_b32 v15, 4294901760, v4\n" \
  "v_lshlrev_b32 v26, 16, v5\n" \
  "v_and_b32 v27, 4294901760, v5\n" \
  "v_lshlrev_b32 v2, 16, v6\n" \
  "v_and_b32 v3, 4294901760, v6\n" \
  "v_lshlrev_b32 v4, 16, v7\n" \
  "v_and_b32 v5, 4294901760, v7\n" \
  "v_lshlrev_b32 v48, 16, v8\n" \
  "v_and_b32 v49, 4294901760, v8\n" \
  "v_lshlrev_b32 v114, 16, v9\n" \
  "v_and_b32 v115, 4294901760, v9\n" \
  "v_lshlrev_b32 v6, 16, v249\n" \
  "v_and_b32 v7, 4294901760, v249\n" \
  "v_and_b32 v116, 65535, v106\n" \
  "v_add_u32 v116, s21, v116\n" \
  "v_and_b32 v117, 65535, v107\n" \
  "v_add_u32 v117, s21, v117\n" \
  "v_and_b32 v118, 65535, v108\n" \
  "v_add_u32 v118, s21, v118\n" \
  "v_and_b32 v119, 65535, v109\n" \
  "v_add_u32 v119, s21, v119\n" \
  "v_lshrrev_b32 v120, 16, v106\n" \
  "v_add_u32 v120, s21, v120\n" \
  "v_lshrrev_b32 v121, 16, v107\n" \
  "v_add_u32 v121, s21, v121\n" \
  "v_lshrrev_b32 v122, 16, v108\n" \
  "v_add_u32 v122, s21, v122\n" \
  "v_lshrrev_b32 v123, 16, v109\n" \
  "v_add_u32 v123, s21, v123\n" \
  "v_mul_u32_u24 v255, 2448, v253\n" \
  "v_add_u32 v253, s21, v255\n" \
  "v_lshl_add_u32 v255, v254, 4, v253\n" \
  "v_lshl_add_u32 v249, v254, 2, v253\n" \
  "v_lshrrev_b32 v253, 3, v254\n" \
  "v_lshl_add_u32 v248, v253, 4, v255\n" \
  "v_lshrrev_b32 v253, 4, v191\n" \
  "v_lshl_add_u32 v245, v253, 2, s20\n" \
  "v_and_b32 v253, 3, v191\n" \
  "v_bfe_u32 v241, v191, 2, 2\n" \
  "v_mul_u32_u24 v239, 4896, v241\n" \
  "v_lshl_add_u32 v241, v253, 3, v239\n" \
  "v_lshl_add_u32 v253, v245, 5, v241\n" \
  "v_and_b32 v241, 15, v191\n" \
  "v_lshlrev_b32 v239, 4, v241\n" \
  "v_lshl_add_u32 v241, v245, 16, v239\n" \
  "v_lshlrev_b32 v245, 3, v254\n" \
  "s_lshr_b32 s98, s27, 8\n" \
  "s_and_b32 s98, s98, 255\n" \
  "v_subrev_u32 v239, s98, v0\n" \
  "v_bfrev_b32 v238, 1\n" \
  "v_cmp_gt_u32 s[96:97], 36, v239\n" \
  "v_cndmask_b32 v238, v238, v246, s[94:95]\n" \
  "s_cmp_eq_u32 s20, 1\n" \
  "s_cselect_b32 s92, 65537, s58\n" \
  "s_and_b32 s93, s59, 4294901758\n" \
  "v_mov_b32 v8, s18\n" \
  "v_mov_b32 v9, s19\n" \
  "s_waitcnt vmcnt(8)\n" \
  "s_barrier\n" \
  "ds_read_b128 v[106:109], v111 offset:0\n" \
  "ds_read_b128 v[124:127], v111 offset:1024\n" \
  "ds_read_b128 v[128:131], v111 offset:2048\n" \
  "ds_read_b128 v[132:135], v111 offset:3072\n" \
  "s_cmp_eq_u32 s20, 3\n" \
  "s_cselect_b64 exec, 1, 0\n" \
  "buffer_store_dwordx2 v[8:9], off, s[36:39], s30 sc1\n" \
  "s_mov_b64 exec, -1\n" \
  "s_waitcnt lgkmcnt(0)\n" \
  ".Ltc_%=:\n" \
  "v_or3_b32 v239, v106, v124, v128\n" \
  "v_bitop3_b32 v239, v239, 1, v132 bitop3:0xc8\n" \
  "v_cmp_ne_u32 vcc, 0, v239\n" \
  "s_cbranch_vccnz .Lts_%=\n" \
  ".Ltd_%=:\n" \
  "v_mov_b32 v136, v106\n" \
  "v_mov_b32 v137, v124\n" \
  "v_mov_b32 v138, v128\n" \
  "v_mov_b32 v139, v132\n" \
  "v_mov_b32 v140, v107\n" \
  "v_mov_b32 v141, v125\n" \
  "v_mfma_f32_16x16x32_f16 v[144:147], v[136:139], a[200:203], 0\n" \
  "v_mov_b32 v142, v129\n" \
  "v_mov_b32 v143, v133\n" \
  "v_mfma_f32_16x16x32_f16 v[148:151], v[136:139], a[204:207], 0\n" \
  "v_mov_b32 v136, v108\n" \
  "v_mov_b32 v137, v126\n" \
  "v_mfma_f32_16x16x32_f16 v[152:155], v[140:143], a[200:203], 0\n" \
  "v_mov_b32 v138, v130\n" \
  "v_mov_b32 v139, v134\n" \
  "v_mfma_f32_16x16x32_f16 v[156:159], v[140:143], a[204:207], 0\n" \
  "v_mov_b32 v140, v109\n" \
  "v_mov_b32 v141, v127\n" \
  "v_mfma_f32_16x16x32_f16 v[106:109], v[136:139], a[200:203], 0\n" \
  "v_mov_b32 v142, v131\n" \
  "v_mov_b32 v143, v135\n" \
  "v_mfma_f32_16x16x32_f16 v[124:127], v[136:139], a[204:207], 0\n" \
  "s_add_u32 m0, s57, 8192\n" \
  "s_add_u32 s31, s12, 294912\n" \
  "buffer_load_dwordx4 v22, s[4:7], s31 offen offset:0 lds\n" \
  "v_mfma_f32_16x16x32_f16 v[128:131], v[140:143], a[200:203], 0\n" \
  "buffer_load_dwordx4 v23, s[4:7], s31 offen offset:1024 lds\n" \
  "v_mfma_f32_16x16x32_f16 v[132:135], v[140:143], a[204:207], 0\n" \
  "buffer_load_dwordx4 v24, s[4:7], s31 offen offset:2048 lds\n" \
  "buffer_load_dwordx4 v25, s[4:7], s31 offen offset:3072 lds\n" \
  "s_add_u32 m0, s57, 12288\n" \
  "s_add_u32 s31, s12, 9732096\n" \
  "buffer_load_dwordx4 v22, s[4:7], s31 offen offset:0 lds\n" \
  "v_pk_mul_f32 v[136:137], v[144:145], v[74:75]\n" \
  "v_pk_mul_f32 v[140:141], v[144:145], v[78:79]\n" \
  "v_pk_mul_f32 v[138:139], v[146:147], v[76:77]\n" \
  "v_pk_mul_f32 v[142:143], v[146:147], v[80:81]\n" \
  "v_pk_fma_f32 v[136:137], v[148:149], v[78:79], v[136:137] neg_lo:[1,0,0] neg_hi:[1,0,0]\n" \
  "v_pk_fma_f32 v[140:141], v[148:149], v[74:75], v[140:141]\n" \
  "v_pk_fma_f32 v[138:139], v[150:151], v[80:81], v[138:139] neg_lo:[1,0,0] neg_hi:[1,0,0]\n" \
  "v_pk_fma_f32 v[142:143], v[150:151], v[76:77], v[142:143]\n" \
  "v_pk_mul_f32 v[144:145], v[152:153], v[74:75]\n" \
  "v_pk_mul_f32 v[148:149], v[152:153], v[78:79]\n" \
  "v_pk_mul_f32 v[146:147], v[154:155], v[76:77]\n" \
  "v_pk_mul_f32 v[150:151], v[154:155], v[80:81]\n" \
  "v_pk_fma_f32 v[144:145], v[156:157], v[78:79], v[144:145] neg_lo:[1,0,0] neg_hi:[1,0,0]\n" \
  "v_pk_fma_f32 v[148:149], v[156:157], v[74:75], v[148:149]\n" \
  "v_pk_fma_f32 v[146:147], v[158:159], v[80:81], v[146:147] neg_lo:[1,0,0] neg_hi:[1,0,0]\n" \
  "v_pk_fma_f32 v[150:151], v[158:159], v[76:77], v[150:151]\n" \
  "v_pk_mul_f32 v[152:153], v[106:107], v[74:75]\n" \
  "v_pk_mul_f32 v[156:157], v[106:107], v[78:79]\n" \
  "v_pk_mul_f32 v[154:155], v[108:109], v[76:77]\n" \
  "v_pk_mul_f32 v[158:159], v[108:109], v[80:81]\n" \
  "v_pk_fma_f32 v[152:153], v[124:125], v[78:79], v[152:153] neg_lo:[1,0,0] neg_hi:[1,0,0]\n" \
  "v_pk_fma_f32 v[156:157], v[124:125], v[74:75], v[156:157]\n" \
  "v_pk_fma_f32 v[154:155], v[126:127], v[80:81], v[154:155] neg_lo:[1,0,0] neg_hi:[1,0,0]\n" \
  "v_pk_fma_f32 v[158:159], v[126:127], v[76:77], v[158:159]\n" \
  "v_pk_mul_f32 v[106:107], v[128:129], v[74:75]\n" \
  "v_pk_mul_f32 v[124:125], v[128:129], v[78:79]\n" \
  "v_pk_mul_f32 v[108:109], v[130:131], v[76:77]\n" \
  "v_pk_mul_f32 v[126:127], v[130:131], v[80:81]\n" \
  "v_pk_fma_f32 v[106:107], v[132:133], v[78:79], v[106:107] neg_lo:[1,0,0] neg_hi:[1,0,0]\n" \
  "v_pk_fma_f32 v[124:125], v[132:133], v[74:75], v[124:125]\n" \
  "v_pk_fma_f32 v[108:109], v[134:135], v[80:81], v[108:109] neg_lo:[1,0,0] neg_hi:[1,0,0]\n" \
  "v_pk_fma_f32 v[126:127], v[134:135], v[76:77], v[126:127]\n" \
  "v_cvt_pk_f16_f32 v128, v136, v140\n" \
  "v_cvt_pk_f16_f32 v129, v137, v141\n" \
  "v_cvt_pk_f16_f32 v130, v138, v142\n" \
  "v_cvt_pk_f16_f32 v131, v139, v143\n" \
  "v_cvt_pk_f16_f32 v132, v144, v148\n" \
  "v_cvt_pk_f16_f32 v133, v145, v149\n" \
  "v_mfma_f32_16x16x32_f16 v[136:139], a[200:203], v[128:131], 0\n" \
  "v_cvt_pk_f16_f32 v134, v146, v150\n" \
  "v_cvt_pk_f16_f32 v135, v147, v151\n" \
  "v_mfma_f32_16x16x32_f16 v[140:143], a[204:207], v[128:131], 0\n" \
  "v_cvt_pk_f16_f32 v128, v152, v156\n" \
  "v_cvt_pk_f16_f32 v129, v153, v157\n" \
  "v_mfma_f32_16x16x32_f16 v[144:147], a[200:203], v[132:135], 0\n" \
  "v_cvt_pk_f16_f32 v130, v154, v158\n" \
  "v_cvt_pk_f16_f32 v131, v155, v159\n" \
  "v_mfma_f32_16x16x32_f16 v[148:151], a[204:207], v[132:135], 0\n" \
  "v_cvt_pk_f16_f32 v132, v106, v124\n" \
  "v_cvt_pk_f16_f32 v133, v107, v125\n" \
  "v_mfma_f32_16x16x32_f16 v[152:155], a[200:203], v[128:131], 0\n" \
  "v_cvt_pk_f16_f32 v134, v108, v126\n" \
  "v_cvt_pk_f16_f32 v135, v109, v127\n" \
  "v_mfma_f32_16x16x32_f16 v[106:109], a[204:207], v[128:131], 0\n" \
  "buffer_load_dwordx4 v23, s[4:7], s31 offen offset:1024 lds\n" \
  "v_mfma_f32_16x16x32_f16 v[124:127], a[200:203], v[132:135], 0\n" \
  "buffer_load_dwordx4 v24, s[4:7], s31 offen offset:2048 lds\n" \
  "v_mfma_f32_16x16x32_f16 v[128:131], a[204:207], v[132:135], 0\n" \
  "buffer_load_dwordx4 v25, s[4:7], s31 offen offset:3072 lds\n" \
  "s_add_u32 m0, s57, 16384\n" \
  "s_add_u32 s31, s12, 73728\n" \
  "buffer_load_dwordx4 v22, s[4:7], s31 offen offset:0 lds\n" \
  "buffer_load_dwordx4 v23, s[4:7], s31 offen offset:1024 lds\n" \
  "v_pk_mul_f32 v[132:133], v[40:41], v[140:141]\n" \
  "v_pk_mul_f32 v[156:157], v[40:41], v[136:137]\n" \
  "v_pk_mul_f32 v[160:161], v[28:29], v[148:149]\n" \
  "v_pk_mul_f32 v[164:165], v[28:29], v[144:145]\n" \
  "v_pk_fma_f32 v[132:133], v[44:45], v[136:137], v[132:133]\n" \
  "v_pk_fma_f32 v[156:157], v[44:45], v[140:141], v[156:157] neg_lo:[1,0,0] neg_hi:[1,0,0]\n" \
  "v_pk_fma_f32 v[160:161], v[32:33], v[144:145], v[160:161]\n" \
  "v_pk_fma_f32 v[164:165], v[32:33], v[148:149], v[164:165] neg_lo:[1,0,0] neg_hi:[1,0,0]\n" \
  "v_pk_fma_f32 v[132:133], v[66:67], v[148:149], v[132:133]\n" \
  "v_pk_fma_f32 v[156:157], v[66:67], v[144:145], v[156:157]\n" \
  "v_pk_fma_f32 v[160:161], v[66:67], v[140:141], v[160:161] neg_lo:[1,0,0] neg_hi:[1,0,0]\n" \
  "v_pk_fma_f32 v[164:165], v[66:67], v[136:137], v[164:165] neg_lo:[1,0,0] neg_hi:[1,0,0]\n" \
  "v_pk_fma_f32 v[132:133], v[70:71], v[144:145], v[132:133]\n" \
  "v_pk_fma_f32 v[156:157], v[70:71], v[148:149], v[156:157] neg_lo:[1,0,0] neg_hi:[1,0,0]\n" \
  "v_pk_fma_f32 v[160:161], v[70:71], v[136:137], v[160:161] neg_lo:[1,0,0] neg_hi:[1,0,0]\n" \
  "v_pk_fma_f32 v[164:165], v[70:71], v[140:141], v[164:165]\n" \
  "v_pk_mul_f32 v[134:135], v[42:43], v[142:143]\n" \
  "v_pk_mul_f32 v[158:159], v[42:43], v[138:139]\n" \
  "v_pk_mul_f32 v[162:163], v[30:31], v[150:151]\n" \
  "v_pk_mul_f32 v[166:167], v[30:31], v[146:147]\n" \
  "v_pk_fma_f32 v[134:135], v[46:47], v[138:139], v[134:135]\n" \
  "v_pk_fma_f32 v[158:159], v[46:47], v[142:143], v[158:159] neg_lo:[1,0,0] neg_hi:[1,0,0]\n" \
  "v_pk_fma_f32 v[162:163], v[34:35], v[146:147], v[162:163]\n" \
  "v_pk_fma_f32 v[166:167], v[34:35], v[150:151], v[166:167] neg_lo:[1,0,0] neg_hi:[1,0,0]\n" \
  "v_pk_fma_f32 v[134:135], v[68:69], v[150:151], v[134:135]\n" \
  "v_pk_fma_f32 v[158:159], v[68:69], v[146:147], v[158:159]\n" \
  "v_pk_fma_f32 v[162:163], v[68:69], v[142:143], v[162:163] neg_lo:[1,0,0] neg_hi:[1,0,0]\n" \
  "v_pk_fma_f32 v[166:167], v[68:69], v[138:139], v[166:167] neg_lo:[1,0,0] neg_hi:[1,0,0]\n" \
  "v_pk_fma_f32 v[134:135], v[72:73], v[146:147], v[134:135]\n" \
  "v_pk_fma_f32 v[158:159], v[72:73], v[150:151], v[158:159] neg_lo:[1,0,0] neg_hi:[1,0,0]\n" \
  "v_pk_fma_f32 v[162:163], v[72:73], v[138:139], v[162:163] neg_lo:[1,0,0] neg_hi:[1,0,0]\n" \
  "v_pk_fma_f32 v[166:167], v[72:73], v[142:143], v[166:167]\n" \
  "v_pk_mul_f32 v[28:29], v[82:83], v[106:107]\n" \
  "v_pk_mul_f32 v[32:33], v[82:83], v[152:153]\n" \
  "v_pk_mul_f32 v[40:41], v[36:37], v[128:129]\n" \
  "v_pk_mul_f32 v[44:45], v[36:37], v[124:125]\n" \
  "v_pk_fma_f32 v[28:29], v[86:87], v[152:153], v[28:29]\n" \
  "v_pk_fma_f32 v[32:33], v[86:87], v[106:107], v[32:33] neg_lo:[1,0,0] neg_hi:[1,0,0]\n" \
  "v_pk_fma_f32 v[40:41], v[98:99], v[124:125], v[40:41]\n" \
  "v_pk_fma_f32 v[44:45], v[98:99], v[128:129], v[44:45] neg_lo:[1,0,0] neg_hi:[1,0,0]\n" \
  "v_pk_fma_f32 v[28:29], v[90:91], v[128:129], v[28:29]\n" \
  "v_pk_fma_f32 v[32:33], v[90:91], v[124:125], v[32:33]\n" \
  "v_pk_fma_f32 v[40:41], v[90:91], v[106:107], v[40:41] neg_lo:[1,0,0] neg_hi:[1,0,0]\n" \
  "v_pk_fma_f32 v[44:45], v[90:91], v[152:153], v[44:45] neg_lo:[1,0,0] neg_hi:[1,0,0]\n" \
  "v_pk_fma_f32 v[28:29], v[94:95], v[124:125], v[28:29]\n" \
  "v_pk_fma_f32 v[32:33], v[94:95], v[128:129], v[32:33] neg_lo:[1,0,0] neg_hi:[1,0,0]\n" \
  "v_pk_fma_f32 v[40:41], v[94:95], v[152:153], v[40:41] neg_lo:[1,0,0] neg_hi:[1,0,0]\n" \
  "v_pk_fma_f32 v[44:45], v[94:95], v[106:107], v[44:45]\n" \
  "v_pk_mul_f32 v[30:31], v[84:85], v[108:109]\n" \
  "v_pk_mul_f32 v[34:35], v[84:85], v[154:155]\n" \
  "v_pk_mul_f32 v[42:43], v[38:39], v[130:131]\n" \
  "v_pk_mul_f32 v[46:47], v[38:39], v[126:127]\n" \
  "v_pk_fma_f32 v[30:31], v[88:89], v[154:155], v[30:31]\n" \
  "v_pk_fma_f32 v[34:35], v[88:89], v[108:109], v[34:35] neg_lo:[1,0,0] neg_hi:[1,0,0]\n" \
  "v_pk_fma_f32 v[42:43], v[100:101], v[126:127], v[42:43]\n" \
  "v_pk_fma_f32 v[46:47], v[100:101], v[130:131], v[46:47] neg_lo:[1,0,0] neg_hi:[1,0,0]\n" \
  "v_pk_fma_f32 v[30:31], v[92:93], v[130:131], v[30:31]\n" \
  "v_pk_fma_f32 v[34:35], v[92:93], v[126:127], v[34:35]\n" \
  "v_pk_fma_f32 v[42:43], v[92:93], v[108:109], v[42:43] neg_lo:[1,0,0] neg_hi:[1,0,0]\n" \
  "v_pk_fma_f32 v[46:47], v[92:93], v[154:155], v[46:47] neg_lo:[1,0,0] neg_hi:[1,0,0]\n" \
  "v_pk_fma_f32 v[30:31], v[96:97], v[126:127], v[30:31]\n" \
  "v_pk_fma_f32 v[34:35], v[96:97], v[130:131], v[34:35] neg_lo:[1,0,0] neg_hi:[1,0,0]\n" \
  "v_pk_fma_f32 v[42:43], v[96:97], v[154:155], v[42:43] neg_lo:[1,0,0] neg_hi:[1,0,0]\n" \
  "v_pk_fma_f32 v[46:47], v[96:97], v[108:109], v[46:47]\n" \
  "v_cvt_pk_f16_f32 v36, v132, v156\n" \
  "v_cvt_pk_f16_f32 v37, v133, v157\n" \
  "v_cvt_pk_f16_f32 v38, v134, v158\n" \
  "v_cvt_pk_f16_f32 v39, v135, v159\n" \
  "v_cvt_pk_f16_f32 v66, v160, v164\n" \
  "v_cvt_pk_f16_f32 v67, v161, v165\n" \
  "v_mfma_f32_16x16x32_f16 v[70:73], v[36:39], a[200:203], 0\n" \
  "v_cvt_pk_f16_f32 v68, v162, v166\n" \
  "v_cvt_pk_f16_f32 v69, v163, v167\n" \
  "v_mfma_f32_16x16x32_f16 v[82:85], v[36:39], a[204:207], 0\n" \
  "v_cvt_pk_f16_f32 v36, v28, v32\n" \
  "v_cvt_pk_f16_f32 v37, v29, v33\n" \
  "v_mfma_f32_16x16x32_f16 v[86:89], v[66:69], a[200:203], 0\n" \
  "v_cvt_pk_f16_f32 v38, v30, v34\n" \
  "v_cvt_pk_f16_f32 v39, v31, v35\n" \
  "v_mfma_f32_16x16x32_f16 v[28:31], v[66:69], a[204:207], 0\n" \
  "v_cvt_pk_f16_f32 v32, v40, v44\n" \
  "v_cvt_pk_f16_f32 v33, v41, v45\n" \
  "v_mfma_f32_16x16x32_f16 v[66:69], v[36:39], a[200:203], 0\n" \
  "v_cvt_pk_f16_f32 v34, v42, v46\n" \
  "v_cvt_pk_f16_f32 v35, v43, v47\n" \
  "v_mfma_f32_16x16x32_f16 v[40:43], v[36:39], a[204:207], 0\n" \
  "buffer_load_dwordx4 v24, s[4:7], s31 offen offset:2048 lds\n" \
  "v_mfma_f32_16x16x32_f16 v[36:39], v[32:35], a[200:203], 0\n" \
  "buffer_load_dwordx4 v25, s[4:7], s31 offen offset:3072 lds\n" \
  "v_mfma_f32_16x16x32_f16 v[44:47], v[32:35], a[204:207], 0\n" \
  "s_add_u32 m0, s57, 20480\n" \
  "s_add_u32 s31, s12, 9510912\n" \
  "buffer_load_dwordx4 v22, s[4:7], s31 offen offset:0 lds\n" \
  "buffer_load_dwordx4 v23, s[4:7], s31 offen offset:1024 lds\n" \
  "buffer_load_dwordx4 v24, s[4:7], s31 offen offset:2048 lds\n" \
  "v_pk_mul_f32 v[32:33], v[70:71], v[74:75]\n" \
  "v_pk_mul_f32 v[90:91], v[70:71], v[78:79]\n" \
  "v_pk_mul_f32 v[34:35], v[72:73], v[76:77]\n" \
  "v_pk_mul_f32 v[92:93], v[72:73], v[80:81]\n" \
  "v_pk_fma_f32 v[32:33], v[82:83], v[78:79], v[32:33] neg_lo:[1,0,0] neg_hi:[1,0,0]\n" \
  "v_pk_fma_f32 v[90:91], v[82:83], v[74:75], v[90:91]\n" \
  "v_pk_fma_f32 v[34:35], v[84:85], v[80:81], v[34:35] neg_lo:[1,0,0] neg_hi:[1,0,0]\n" \
  "v_pk_fma_f32 v[92:93], v[84:85], v[76:77], v[92:93]\n" \
  "v_pk_mul_f32 v[70:71], v[86:87], v[74:75]\n" \
  "v_pk_mul_f32 v[82:83], v[86:87], v[78:79]\n" \
  "v_pk_mul_f32 v[72:73], v[88:89], v[76:77]\n" \
  "v_pk_mul_f32 v[84:85], v[88:89], v[80:81]\n" \
  "v_pk_fma_f32 v[70:71], v[28:29], v[78:79], v[70:71] neg_lo:[1,0,0] neg_hi:[1,0,0]\n" \
  "v_pk_fma_f32 v[82:83], v[28:29], v[74:75], v[82:83]\n" \
  "v_pk_fma_f32 v[72:73], v[30:31], v[80:81], v[72:73] neg_lo:[1,0,0] neg_hi:[1,0,0]\n" \
  "v_pk_fma_f32 v[84:85], v[30:31], v[76:77], v[84:85]\n" \
  "v_pk_mul_f32 v[28:29], v[66:67], v[74:75]\n" \
  "v_pk_mul_f32 v[86:87], v[66:67], v[78:79]\n" \
  "v_pk_mul_f32 v[30:31], v[68:69], v[76:77]\n" \
  "v_pk_mul_f32 v[88:89], v[68:69], v[80:81]\n" \
  "v_pk_fma_f32 v[28:29], v[40:41], v[78:79], v[28:29] neg_lo:[1,0,0] neg_hi:[1,0,0]\n" \
  "v_pk_fma_f32 v[86:87], v[40:41], v[74:75], v[86:87]\n" \
  "v_pk_fma_f32 v[30:31], v[42:43], v[80:81], v[30:31] neg_lo:[1,0,0] neg_hi:[1,0,0]\n" \
  "v_pk_fma_f32 v[88:89], v[42:43], v[76:77], v[88:89]\n" \
  "v_pk_mul_f32 v[40:41], v[36:37], v[74:75]\n" \
  "v_pk_mul_f32 v[66:67], v[36:37], v[78:79]\n" \
  "v_pk_mul_f32 v[42:43], v[38:39], v[76:77]\n" \
  "v_pk_mul_f32 v[68:69], v[38:39], v[80:81]\n" \
  "v_pk_fma_f32 v[40:41], v[44:45], v[78:79], v[40:41] neg_lo:[1,0,0] neg_hi:[1,0,0]\n" \
  "v_pk_fma_f32 v[66:67], v[44:45], v[74:75], v[66:67]\n" \
  "v_pk_fma_f32 v[42:43], v[46:47], v[80:81], v[42:43] neg_lo:[1,0,0] neg_hi:[1,0,0]\n" \
  "v_pk_fma_f32 v[68:69], v[46:47], v[76:77], v[68:69]\n" \
  "v_cvt_pk_f16_f32 v36, v32, v90\n" \
  "v_cvt_pk_f16_f32 v37, v33, v91\n" \
  "v_cvt_pk_f16_f32 v38, v34, v92\n" \
  "v_cvt_pk_f16_f32 v39, v35, v93\n" \
  "v_cvt_pk_f16_f32 v32, v70, v82\n" \
  "v_cvt_pk_f16_f32 v33, v71, v83\n" \
  "v_mfma_f32_16x16x32_f16 v[44:47], a[200:203], v[36:39], 0\n" \
  "v_cvt_pk_f16_f32 v34, v72, v84\n" \
  "v_cvt_pk_f16_f32 v35, v73, v85\n" \
  "v_mfma_f32_16x16x32_f16 v[70:73], a[204:207], v[36:39], 0\n" \
  "v_cvt_pk_f16_f32 v36, v28, v86\n" \
  "v_cvt_pk_f16_f32 v37, v29, v87\n" \
  "v_mfma_f32_16x16x32_f16 v[74:77], a[200:203], v[32:35], 0\n" \
  "v_cvt_pk_f16_f32 v38, v30, v88\n" \
  "v_cvt_pk_f16_f32 v39, v31, v89\n" \
  "v_mfma_f32_16x16x32_f16 v[28:31], a[204:207], v[32:35], 0\n" \
  "v_cvt_pk_f16_f32 v32, v40, v66\n" \
  "v_cvt_pk_f16_f32 v33, v41, v67\n" \
  "v_mfma_f32_16x16x32_f16 v[78:81], a[200:203], v[36:39], 0\n" \
  "v_cvt_pk_f16_f32 v34, v42, v68\n" \
  "v_cvt_pk_f16_f32 v35, v43, v69\n" \
  "v_mfma_f32_16x16x32_f16 v[40:43], a[204:207], v[36:39], 0\n" \
  "buffer_load_dwordx4 v25, s[4:7], s31 offen offset:3072 lds\n" \
  "v_mfma_f32_16x16x32_f16 v[36:39], a[200:203], v[32:35], 0\n" \
  "v_mfma_f32_16x16x32_f16 v[66:69], a[204:207], v[32:35], 0\n" \
  "v_cvt_pk_f16_f32 v32, v70, v44\n" \
  "v_cvt_pk_f16_f32 v33, v28, v74\n" \
  "s_nop 2\n" \
  "v_cvt_pk_f16_f32 v34, v40, v78\n" \
  "s_nop 1\n" \
  "v_cvt_pk_f16_f32 v35, v66, v36\n" \
  "v_or_b32 v32, 1, v32\n" \
  "ds_write_b128 v111, v[32:35] offset:0\n" \
  "v_cvt_pk_f16_f32 v32, v71, v45\n" \
  "v_cvt_pk_f16_f32 v33, v29, v75\n" \
  "v_cvt_pk_f16_f32 v34, v41, v79\n" \
  "v_cvt_pk_f16_f32 v35, v67, v37\n" \
  "v_or_b32 v32, 1, v32\n" \
  "ds_write_b128 v111, v[32:35] offset:1024\n" \
  "v_cvt_pk_f16_f32 v32, v72, v46\n" \
  "v_cvt_pk_f16_f32 v33, v30, v76\n" \
  "v_cvt_pk_f16_f32 v34, v42, v80\n" \
  "v_cvt_pk_f16_f32 v35, v68, v38\n" \
  "v_or_b32 v32, 1, v32\n" \
  "ds_write_b128 v111, v[32:35] offset:2048\n" \
  "v_cvt_pk_f16_f32 v32, v73, v47\n" \
  "v_cvt_pk_f16_f32 v33, v31, v77\n" \
  "v_cvt_pk_f16_f32 v34, v43, v81\n" \
  "v_cvt_pk_f16_f32 v35, v69, v39\n" \
  "v_or_b32 v32, 1, v32\n" \
  "ds_write_b128 v111, v[32:35] offset:3072\n" \
  "s_waitcnt lgkmcnt(0)\n" \
  "s_barrier\n" \
  "ds_read_b128 v[28:31], v110 offset:0\n" \
  "ds_read_b128 v[32:35], v110 offset:4096\n" \
  "ds_read_b128 v[36:39], v110 offset:8192\n" \
  "ds_read_b128 v[40:43], v110 offset:12288\n" \
  "s_add_u32 s32, s22, 598016\n" \
  "s_waitcnt lgkmcnt(3)\n" \
  "buffer_store_dwordx4 v[28:31], v226, s[36:39], s22 offen offset:0 sc1\n" \
  "s_add_u32 s33, s22, 1196032\n" \
  "s_waitcnt lgkmcnt(2)\n" \
  "buffer_store_dwordx4 v[32:35], v226, s[36:39], s32 offen offset:0 sc1\n" \
  "s_add_u32 s32, s22, 1794048\n" \
  "s_waitcnt lgkmcnt(1)\n" \
  "buffer_store_dwordx4 v[36:39], v226, s[36:39], s33 offen offset:0 sc1\n" \
  "s_waitcnt lgkmcnt(0)\n" \
  "buffer_store_dwordx4 v[40:43], v226, s[36:39], s32 offen offset:0 sc1\n" \
  "s_mov_b32 s32, 35840\n" \
  "buffer_load_dwordx4 v[28:31], v238, s[36:39], s32 offen offset:0 sc1\n" \
  "buffer_load_dwordx4 v[32:35], v238, s[36:39], s32 offen offset:2304 sc1\n" \
  "s_mov_b32 s32, 40448\n" \
  "buffer_load_dwordx4 v[36:39], v238, s[36:39], s32 offen offset:0 sc1\n" \
  "buffer_load_dwordx4 v[40:43], v238, s[36:39], s32 offen offset:2304 sc1\n" \
  "s_add_u32 s32, s8, 4096\n" \
  "buffer_load_dwordx4 v[44:47], v230, s[36:39], s8 offen offset:0 sc1\n" \
  "buffer_load_dwordx4 v[66:69], v230, s[36:39], s32 offen offset:0 sc1\n" \
  "buffer_load_dwordx4 v[70:73], v240, s[36:39], s9 offen offset:0 sc1\n" \
  "buffer_load_dwordx2 v[8:9], v245, s[36:39], s29 offen offset:0 sc1\n" \
  "s_waitcnt vmcnt(29)\n" \
  "ds_read_b64_tr_b16 a[0:1], v18 offset:0\n" \
  "ds_read_b64_tr_b16 a[4:5], v19 offset:0\n" \
  "ds_read_b64_tr_b16 a[8:9], v20 offset:0\n" \
  "ds_read_b64_tr_b16 a[12:13], v21 offset:0\n" \
  "ds_read_b64_tr_b16 a[20:21], v18 offset:2048\n" \
  "ds_read_b64_tr_b16 a[24:25], v19 offset:2048\n" \
  "ds_read_b64_tr_b16 a[28:29], v20 offset:2048\n" \
  "ds_read_b64_tr_b16 a[32:33], v21 offset:2048\n" \
  "ds_read_b64_tr_b16 a[2:3], v18 offset:4096\n" \
  "ds_read_b64_tr_b16 a[6:7], v19 offset:4096\n" \
  "ds_read_b64_tr_b16 a[10:11], v20 offset:4096\n" \
  "ds_read_b64_tr_b16 a[14:15], v21 offset:4096\n" \
  "ds_read_b64_tr_b16 a[22:23], v18 offset:6144\n" \
  "ds_read_b64_tr_b16 a[26:27], v19 offset:6144\n" \
  "ds_read_b64_tr_b16 a[30:31], v20 offset:6144\n" \
  "ds_read_b64_tr_b16 a[34:35], v21 offset:6144\n" \
  "s_waitcnt vmcnt(20)\n" \
  "ds_read_b64_tr_b16 a[80:81], v18 offset:8192\n" \
  "ds_read_b64_tr_b16 a[84:85], v19 offset:8192\n" \
  "ds_read_b64_tr_b16 a[88:89], v20 offset:8192\n" \
  "ds_read_b64_tr_b16 a[92:93], v21 offset:8192\n" \
  "ds_read_b64_tr_b16 a[100:101], v18 offset:10240\n" \
  "ds_read_b64_tr_b16 a[104:105], v19 offset:10240\n" \
  "ds_read_b64_tr_b16 a[108:109], v20 offset:10240\n" \
  "ds_read_b64_tr_b16 a[112:113], v21 offset:10240\n" \
  "ds_read_b64_tr_b16 a[82:83], v18 offset:12288\n" \
  "ds_read_b64_tr_b16 a[86:87], v19 offset:12288\n" \
  "ds_read_b64_tr_b16 a[90:91], v20 offset:12288\n" \
  "ds_read_b64_tr_b16 a[94:95], v21 offset:12288\n" \
  "ds_read_b64_tr_b16 a[102:103], v18 offset:14336\n" \
  "ds_read_b64_tr_b16 a[106:107], v19 offset:14336\n" \
  "ds_read_b64_tr_b16 a[110:111], v20 offset:14336\n" \
  "ds_read_b64_tr_b16 a[114:115], v21 offset:14336\n" \
  "s_waitcnt vmcnt(12)\n" \
  "ds_read_b64_tr_b16 a[40:41], v18 offset:16384\n" \
  "ds_read_b64_tr_b16 a[44:45], v19 offset:16384\n" \
  "ds_read_b64_tr_b16 a[48:49], v20 offset:16384\n" \
  "ds_read_b64_tr_b16 a[52:53], v21 offset:16384\n" \
  "ds_read_b64_tr_b16 a[60:61], v18 offset:18432\n" \
  "ds_read_b64_tr_b16 a[64:65], v19 offset:18432\n" \
  "ds_read_b64_tr_b16 a[68:69], v20 offset:18432\n" \
  "ds_read_b64_tr_b16 a[72:73], v21 offset:18432\n" \
  "ds_read_b64_tr_b16 a[42:43], v18 offset:20480\n" \
  "ds_read_b64_tr_b16 a[46:47], v19 offset:20480\n" \
  "ds_read_b64_tr_b16 a[50:51], v20 offset:20480\n" \
  "ds_read_b64_tr_b16 a[54:55], v21 offset:20480\n" \
  "ds_read_b64_tr_b16 a[62:63], v18 offset:22528\n" \
  "ds_read_b64_tr_b16 a[66:67], v19 offset:22528\n" \
  "ds_read_b64_tr_b16 a[70:71], v20 offset:22528\n" \
  "ds_read_b64_tr_b16 a[74:75], v21 offset:22528\n" \
  "s_waitcnt lgkmcnt(0)\n" \
  "s_barrier\n" \
  "s_add_u32 m0, s57, 0\n" \
  "s_add_u32 s31, s12, 368640\n" \
  "buffer_load_dwordx4 v22, s[4:7], s31 offen offset:0 lds\n" \
  "buffer_load_dwordx4 v23, s[4:7], s31 offen offset:1024 lds\n" \
  "buffer_load_dwordx4 v24, s[4:7], s31 offen offset:2048 lds\n" \
  "buffer_load_dwordx4 v25, s[4:7], s31 offen offset:3072 lds\n" \
  "s_add_u32 m0, s57, 4096\n" \
  "s_add_u32 s31, s12, 9805824\n" \
  "buffer_load_dwordx4 v22, s[4:7], s31 offen offset:0 lds\n" \
  "buffer_load_dwordx4 v23, s[4:7], s31 offen offset:1024 lds\n" \
  "buffer_load_dwordx4 v24, s[4:7], s31 offen offset:2048 lds\n" \
  "buffer_load_dwordx4 v25, s[4:7], s31 offen offset:3072 lds\n" \
  "s_add_u32 m0, s57, 8192\n" \
  "s_add_u32 s31, s12, 147456\n" \
  "buffer_load_dwordx4 v22, s[4:7], s31 offen offset:0 lds\n" \
  "buffer_load_dwordx4 v23, s[4:7], s31 offen offset:1024 lds\n" \
  "buffer_load_dwordx4 v24, s[4:7], s31 offen offset:2048 lds\n" \
  "buffer_load_dwordx4 v25, s[4:7], s31 offen offset:3072 lds\n" \
  "s_waitcnt vmcnt(16)\n" \
  ".Lmcc_%=:\n" \
  "v_xor_b32 v239, s18, v28\n" \
  "v_bitop3_b32 v239, v29, s19, v239 bitop3:0xbe\n" \
  "v_bitop3_b32 v239, v32, s18, v239 bitop3:0xbe\n" \
  "v_bitop3_b32 v239, v33, s19, v239 bitop3:0xbe\n" \
  "v_bitop3_b32 v239, v36, s18, v239 bitop3:0xbe\n" \
  "v_bitop3_b32 v239, v37, s19, v239 bitop3:0xbe\n" \
  "v_bitop3_b32 v239, v40, s18, v239 bitop3:0xbe\n" \
  "v_bitop3_b32 v239, v41, s19, v239 bitop3:0xbe\n" \
  "v_cmp_ne_u32 vcc, 0, v239\n" \
  "s_and_b64 vcc, vcc, s[94:95]\n" \
  "s_cbranch_scc1 .Lmcs_%=\n" \
  ".Lmcd_%=:\n" \
  "v_lshlrev_b32 v74, 16, v30\n" \
  "v_and_b32 v75, 4294901760, v30\n" \
  "v_lshlrev_b32 v76, 16, v31\n" \
  "v_and_b32 v77, 4294901760, v31\n" \
  "v_lshlrev_b32 v28, 16, v34\n" \
  "v_and_b32 v29, 4294901760, v34\n" \
  "v_lshlrev_b32 v30, 16, v35\n" \
  "v_and_b32 v31, 4294901760, v35\n" \
  "v_lshlrev_b32 v32, 16, v38\n" \
  "v_and_b32 v33, 4294901760, v38\n" \
  "v_lshlrev_b32 v34, 16, v39\n" \
  "v_and_b32 v35, 4294901760, v39\n" \
  "v_lshlrev_b32 v36, 16, v42\n" \
  "v_and_b32 v37, 4294901760, v42\n" \
  "v_lshlrev_b32 v38, 16, v43\n" \
  "v_and_b32 v39, 4294901760, v43\n" \
  "v_mul_f32 v74, 1055100473, v74\n" \
  "v_ldexp_f32 v74, v74, s23\n" \
  "v_mul_f32 v75, 1055100473, v75\n" \
  "v_ldexp_f32 v75, v75, s23\n" \
  "v_mul_f32 v76, 1055100473, v76\n" \
  "v_ldexp_f32 v76, v76, s23\n" \
  "v_mul_f32 v77, 1055100473, v77\n" \
  "v_ldexp_f32 v77, v77, s23\n" \
  "s_mov_b64 exec, s[94:95]\n" \
  "ds_write_b128 v246, v[28:31] offset:31104\n" \
  "ds_write_b128 v246, v[32:35] offset:33408\n" \
  "ds_write_b128 v246, v[74:77] offset:28800\n" \
  "s_bfe_u32 s32, s27, 524296\n" \
  "s_lshl_b32 s32, s32, 4\n" \
  "s_sub_u32 s32, 163200, s32\n" \
  "v_add_u32 v239, s32, v246\n" \
  "s_mov_b64 exec, s[96:97]\n" \
  "ds_write_b128 v239, v[36:39] offset:0\n" \
  "s_mov_b64 exec, -1\n" \
  "s_waitcnt vmcnt(13)\n" \
  ".Lrcc_%=:\n" \
  "v_bitop3_b32 v246, v44, v66, v70 bitop3:0x80\n" \
  "v_and_b32 v246, 1, v246\n" \
  "v_cmp_eq_u32 vcc, 0, v246\n" \
  "s_cbranch_vccnz .Lrcs_%=\n" \
  ".Lrcd_%=:\n" \
  "v_mfma_f32_16x16x32_f16 v[28:31], a[228:231], v[44:47], 0\n" \
  "s_add_u32 m0, s57, 12288\n" \
  "s_add_u32 s31, s12, 9584640\n" \
  "buffer_load_dwordx4 v22, s[4:7], s31 offen offset:0 lds\n" \
  "v_mfma_f32_16x16x32_f16 v[32:35], a[232:235], v[44:47], 0\n" \
  "buffer_load_dwordx4 v23, s[4:7], s31 offen offset:1024 lds\n" \
  "v_mfma_f32_16x16x32_f16 v[36:39], a[228:231], v[66:69], 0\n" \
  "buffer_load_dwordx4 v24, s[4:7], s31 offen offset:2048 lds\n" \
  "v_mfma_f32_16x16x32_f16 v[40:43], a[232:235], v[66:69], 0\n" \
  "buffer_load_dwordx4 v25, s[4:7], s31 offen offset:3072 lds\n" \
  "v_mfma_f32_16x16x32_f16 v[44:47], a[228:231], v[70:73], 0\n" \
  "s_add_u32 m0, s57, 16384\n" \
  "s_add_u32 s31, s12, 442368\n" \
  "buffer_load_dwordx4 v22, s[4:7], s31 offen offset:0 lds\n" \
  "v_mfma_f32_16x16x32_f16 v[66:69], a[232:235], v[70:73], 0\n" \
  "buffer_load_dwordx4 v23, s[4:7], s31 offen offset:1024 lds\n" \
  "v_cvt_pk_f16_f32 v70, v32, v28\n" \
  "v_cvt_pk_f16_f32 v71, v33, v29\n" \
  "v_cvt_pk_f16_f32 v72, v34, v30\n" \
  "v_cvt_pk_f16_f32 v73, v35, v31\n" \
  "ds_write_b128 v103, v[70:73] offset:0\n" \
  "v_cvt_pk_f16_f32 v28, v40, v36\n" \
  "v_cvt_pk_f16_f32 v29, v41, v37\n" \
  "v_cvt_pk_f16_f32 v30, v42, v38\n" \
  "v_cvt_pk_f16_f32 v31, v43, v39\n" \
  "ds_write_b128 v104, v[28:31] offset:0\n" \
  "v_cvt_pk_f16_f32 v28, v66, v44\n" \
  "v_cvt_pk_f16_f32 v29, v67, v45\n" \
  "v_cvt_pk_f16_f32 v30, v68, v46\n" \
  "v_cvt_pk_f16_f32 v31, v69, v47\n" \
  "s_mov_b64 exec, s[92:93]\n" \
  "ds_write_b128 v105, v[28:31] offset:0\n" \
  "s_mov_b64 exec, -1\n" \
  "s_waitcnt lgkmcnt(0)\n" \
  "s_barrier\n" \
  "ds_read_b64 v[28:29], v102 offset:0\n" \
  "ds_read_b64 v[30:31], v102 offset:512\n" \
  "ds_read_b64 v[32:33], v102 offset:1024\n" \
  "ds_read_b64 v[34:35], v102 offset:256\n" \
  "ds_read_b64 v[36:37], v102 offset:768\n" \
  "ds_read_b64 v[38:39], v102 offset:1280\n" \
  "v_lshlrev_b32 v246, 5, v254\n" \
  "buffer_load_dwordx4 v24, s[4:7], s31 offen offset:2048 lds\n" \
  "buffer_load_dwordx4 v25, s[4:7], s31 offen offset:3072 lds\n" \
  "s_add_u32 m0, s57, 20480\n" \
  "s_add_u32 s31, s12, 9879552\n" \
  "buffer_load_dwordx4 v22, s[4:7], s31 offen offset:0 lds\n" \
  "s_waitcnt lgkmcnt(5)\n" \
  "v_mov_b32 v50, v28\n" \
  "s_waitcnt lgkmcnt(4)\n" \
  "v_mov_b32 v51, v30\n" \
  "s_waitcnt lgkmcnt(3)\n" \
  "v_mov_b32 v52, v32\n" \
  "v_mov_b32 v58, v29\n" \
  "v_mov_b32 v59, v31\n" \
  "v_mfma_f32_16x16x32_f16 v[28:31], v[50:53], a[208:211], 0\n" \
  "v_mov_b32 v60, v33\n" \
  "s_waitcnt lgkmcnt(2)\n" \
  "v_mov_b32 v54, v34\n" \
  "v_mfma_f32_16x16x32_f16 v[40:43], v[50:53], a[212:215], 0\n" \
  "s_waitcnt lgkmcnt(1)\n" \
  "v_mov_b32 v55, v36\n" \
  "s_waitcnt lgkmcnt(0)\n" \
  "v_mov_b32 v56, v38\n" \
  "v_mfma_f32_16x16x32_f16 v[44:47], v[58:61], a[208:211], 0\n" \
  "v_mov_b32 v62, v35\n" \
  "v_mov_b32 v63, v37\n" \
  "v_mfma_f32_16x16x32_f16 v[32:35], v[58:61], a[212:215], 0\n" \
  "v_mov_b32 v64, v39\n" \
  "buffer_load_dwordx4 v23, s[4:7], s31 offen offset:1024 lds\n" \
  "v_mfma_f32_16x16x32_f16 v[36:39], v[54:57], a[208:211], 0\n" \
  "ds_read_b128 v[50:53], v246 offset:31104\n" \
  "ds_read_b128 v[58:61], v246 offset:31120\n" \
  "v_mfma_f32_16x16x32_f16 v[66:69], v[54:57], a[212:215], 0\n" \
  "ds_read_b128 v[54:57], v246 offset:32128\n" \
  "ds_read_b128 v[70:73], v246 offset:32144\n" \
  "v_mfma_f32_16x16x32_f16 v[74:77], v[62:65], a[208:211], 0\n" \
  "ds_read_b64 v[78:79], v245 offset:33152\n" \
  "buffer_load_dwordx4 v24, s[4:7], s31 offen offset:2048 lds\n" \
  "v_mfma_f32_16x16x32_f16 v[80:83], v[62:65], a[212:215], 0\n" \
  "ds_read_b128 v[62:65], v246 offset:28800\n" \
  "ds_read_b128 v[84:87], v246 offset:28816\n" \
  "ds_read_b128 v[88:91], v246 offset:29824\n" \
  "ds_read_b128 v[92:95], v246 offset:29840\n" \
  "ds_read_b64 v[96:97], v245 offset:30848\n" \
  "buffer_load_dwordx4 v25, s[4:7], s31 offen offset:3072 lds\n" \
  "v_pk_add_f32 v[98:99], v[28:29], v[36:37]\n" \
  "v_pk_add_f32 v[100:101], v[30:31], v[38:39]\n" \
  "v_pk_add_f32 v[102:103], v[40:41], v[66:67]\n" \
  "v_pk_add_f32 v[104:105], v[42:43], v[68:69]\n" \
  "v_pk_add_f32 v[106:107], v[28:29], v[36:37] neg_lo:[0,1] neg_hi:[0,1]\n" \
  "v_pk_add_f32 v[108:109], v[30:31], v[38:39] neg_lo:[0,1] neg_hi:[0,1]\n" \
  "v_pk_add_f32 v[28:29], v[40:41], v[66:67] neg_lo:[0,1] neg_hi:[0,1]\n" \
  "v_pk_add_f32 v[30:31], v[42:43], v[68:69] neg_lo:[0,1] neg_hi:[0,1]\n" \
  "v_pk_add_f32 v[36:37], v[44:45], v[74:75]\n" \
  "v_pk_add_f32 v[38:39], v[46:47], v[76:77]\n" \
  "v_pk_add_f32 v[40:41], v[32:33], v[80:81]\n" \
  "v_pk_add_f32 v[42:43], v[34:35], v[82:83]\n" \
  "v_pk_add_f32 v[66:67], v[44:45], v[74:75] neg_lo:[0,1] neg_hi:[0,1]\n" \
  "v_pk_add_f32 v[68:69], v[46:47], v[76:77] neg_lo:[0,1] neg_hi:[0,1]\n" \
  "v_pk_add_f32 v[44:45], v[32:33], v[80:81] neg_lo:[0,1] neg_hi:[0,1]\n" \
  "v_pk_add_f32 v[46:47], v[34:35], v[82:83] neg_lo:[0,1] neg_hi:[0,1]\n" \
  "v_cvt_pk_f16_f32 v32, v98, v102\n" \
  "v_cvt_pk_f16_f32 v33, v99, v103\n" \
  "v_cvt_pk_f16_f32 v34, v100, v104\n" \
  "v_cvt_pk_f16_f32 v35, v101, v105\n" \
  "v_cvt_pk_f16_f32 v74, v106, v28\n" \
  "v_cvt_pk_f16_f32 v75, v107, v29\n" \
  "v_mfma_f32_16x16x32_f16 v[80:83], a[200:203], v[32:35], 0\n" \
  "v_cvt_pk_f16_f32 v76, v108, v30\n" \
  "v_cvt_pk_f16_f32 v77, v109, v31\n" \
  "v_mfma_f32_16x16x32_f16 v[28:31], a[204:207], v[32:35], 0\n" \
  "v_cvt_pk_f16_f32 v32, v36, v40\n" \
  "v_cvt_pk_f16_f32 v33, v37, v41\n" \
  "v_mfma_f32_16x16x32_f16 v[98:101], a[220:223], v[74:77], 0\n" \
  "v_cvt_pk_f16_f32 v34, v38, v42\n" \
  "v_cvt_pk_f16_f32 v35, v39, v43\n" \
  "v_mfma_f32_16x16x32_f16 v[36:39], a[224:227], v[74:77], 0\n" \
  "v_cvt_pk_f16_f32 v40, v66, v44\n" \
  "v_cvt_pk_f16_f32 v41, v67, v45\n" \
  "v_mfma_f32_16x16x32_f16 v[74:77], a[200:203], v[32:35], 0\n" \
  "v_cvt_pk_f16_f32 v42, v68, v46\n" \
  "v_cvt_pk_f16_f32 v43, v69, v47\n" \
  "v_mfma_f32_16x16x32_f16 v[44:47], a[204:207], v[32:35], 0\n" \
  "ds_read_b128 v[32:35], v246 offset:33408\n" \
  "v_mfma_f32_16x16x32_f16 v[66:69], a[220:223], v[40:43], 0\n" \
  "ds_read_b128 v[102:105], v246 offset:33424\n" \
  "v_mfma_f32_16x16x32_f16 v[106:109], a[224:227], v[40:43], 0\n" \
  "ds_read_b128 v[40:43], v246 offset:34432\n" \
  "ds_read_b128 v[110:113], v246 offset:34448\n" \
  "ds_read_b64 v[124:125], v245 offset:35456\n" \
  "s_waitcnt lgkmcnt(14)\n" \
  "v_readfirstlane_b32 s32, v50\n" \
  "s_mov_b32 exec_lo, 125241207\n" \
  "s_mov_b32 exec_hi, 125241207\n" \
  "v_cvt_pk_f16_f32 v254, v28, v80\n" \
  "ds_write_b32 v116, v254 offset:0\n" \
  "v_cvt_pk_f16_f32 v254, v29, v81\n" \
  "ds_write_b32 v117, v254 offset:0\n" \
  "v_cvt_pk_f16_f32 v254, v30, v82\n" \
  "ds_write_b32 v118, v254 offset:0\n" \
  "v_cvt_pk_f16_f32 v254, v31, v83\n" \
  "ds_write_b32 v119, v254 offset:0\n" \
  "v_cvt_pk_f16_f32 v254, v36, v98\n" \
  "ds_write_b32 v120, v254 offset:0\n" \
  "v_cvt_pk_f16_f32 v254, v37, v99\n" \
  "ds_write_b32 v121, v254 offset:0\n" \
  "v_cvt_pk_f16_f32 v254, v38, v100\n" \
  "ds_write_b32 v122, v254 offset:0\n" \
  "v_cvt_pk_f16_f32 v254, v39, v101\n" \
  "ds_write_b32 v123, v254 offset:0\n" \
  "v_cvt_pk_f16_f32 v254, v44, v74\n" \
  "ds_write_b32 v116, v254 offset:2448\n" \
  "v_cvt_pk_f16_f32 v254, v45, v75\n" \
  "ds_write_b32 v117, v254 offset:2448\n" \
  "v_cvt_pk_f16_f32 v254, v46, v76\n" \
  "ds_write_b32 v118, v254 offset:2448\n" \
  "v_cvt_pk_f16_f32 v254, v47, v77\n" \
  "ds_write_b32 v119, v254 offset:2448\n" \
  "v_cvt_pk_f16_f32 v254, v106, v66\n" \
  "ds_write_b32 v120, v254 offset:2448\n" \
  "v_cvt_pk_f16_f32 v254, v107, v67\n" \
  "ds_write_b32 v121, v254 offset:2448\n" \
  "v_cvt_pk_f16_f32 v254, v108, v68\n" \
  "ds_write_b32 v122, v254 offset:2448\n" \
  "v_cvt_pk_f16_f32 v254, v109, v69\n" \
  "ds_write_b32 v123, v254 offset:2448\n" \
  "s_mov_b64 exec, -1\n" \
  "ds_read_b128 v[28:31], v248 offset:0\n" \
  "ds_read_b128 v[36:39], v248 offset:576\n" \
  "ds_read_b32 v254, v249 offset:1152\n" \
  "v_mov_b32 v44, s32\n" \
  "v_pk_add_f32 v[50:51], v[50:51], v[44:45] op_sel_hi:[1,0] neg_lo:[0,1] neg_hi:[0,1]\n" \
  "v_pk_add_f32 v[52:53], v[52:53], v[44:45] op_sel_hi:[1,0] neg_lo:[0,1] neg_hi:[0,1]\n" \
  "s_waitcnt lgkmcnt(15)\n" \
  "v_pk_add_f32 v[58:59], v[58:59], v[44:45] op_sel_hi:[1,0] neg_lo:[0,1] neg_hi:[0,1]\n" \
  "v_pk_add_f32 v[60:61], v[60:61], v[44:45] op_sel_hi:[1,0] neg_lo:[0,1] neg_hi:[0,1]\n" \
  "v_pk_add_f32 v[54:55], v[54:55], v[44:45] op_sel_hi:[1,0] neg_lo:[0,1] neg_hi:[0,1]\n" \
  "v_pk_add_f32 v[56:57], v[56:57], v[44:45] op_sel_hi:[1,0] neg_lo:[0,1] neg_hi:[0,1]\n" \
  "v_pk_add_f32 v[70:71], v[70:71], v[44:45] op_sel_hi:[1,0] neg_lo:[0,1] neg_hi:[0,1]\n" \
  "v_pk_add_f32 v[72:73], v[72:73], v[44:45] op_sel_hi:[1,0] neg_lo:[0,1] neg_hi:[0,1]\n" \
  "v_pk_add_f32 v[78:79], v[78:79], v[44:45] op_sel_hi:[1,0] neg_lo:[0,1] neg_hi:[0,1]\n" \
  "s_waitcnt lgkmcnt(2)\n" \
  "v_fma_mix_f32 v10, v28, v62, v10 op_sel:[0,0,0] op_sel_hi:[1,0,0]\n" \
  "v_fma_mix_f32 v11, v28, v63, v11 op_sel:[1,0,0] op_sel_hi:[1,0,0]\n" \
  "v_fma_mix_f32 v12, v29, v64, v12 op_sel:[0,0,0] op_sel_hi:[1,0,0]\n" \
  "v_fma_mix_f32 v13, v29, v65, v13 op_sel:[1,0,0] op_sel_hi:[1,0,0]\n" \
  "v_fma_mix_f32 v14, v30, v84, v14 op_sel:[0,0,0] op_sel_hi:[1,0,0]\n" \
  "v_fma_mix_f32 v15, v30, v85, v15 op_sel:[1,0,0] op_sel_hi:[1,0,0]\n" \
  "v_fma_mix_f32 v26, v31, v86, v26 op_sel:[0,0,0] op_sel_hi:[1,0,0]\n" \
  "v_fma_mix_f32 v27, v31, v87, v27 op_sel:[1,0,0] op_sel_hi:[1,0,0]\n" \
  "s_waitcnt lgkmcnt(1)\n" \
  "v_fma_mix_f32 v2, v36, v88, v2 op_sel:[0,0,0] op_sel_hi:[1,0,0]\n" \
  "v_fma_mix_f32 v3, v36, v89, v3 op_sel:[1,0,0] op_sel_hi:[1,0,0]\n" \
  "v_fma_mix_f32 v4, v37, v90, v4 op_sel:[0,0,0] op_sel_hi:[1,0,0]\n" \
  "v_fma_mix_f32 v5, v37, v91, v5 op_sel:[1,0,0] op_sel_hi:[1,0,0]\n" \
  "v_fma_mix_f32 v48, v38, v92, v48 op_sel:[0,0,0] op_sel_hi:[1,0,0]\n" \
  "v_fma_mix_f32 v49, v38, v93, v49 op_sel:[1,0,0] op_sel_hi:[1,0,0]\n" \
  "v_fma_mix_f32 v114, v39, v94, v114 op_sel:[0,0,0] op_sel_hi:[1,0,0]\n" \
  "v_fma_mix_f32 v115, v39, v95, v115 op_sel:[1,0,0] op_sel_hi:[1,0,0]\n" \
  "s_waitcnt lgkmcnt(0)\n" \
  "v_fma_mix_f32 v6, v254, v96, v6 op_sel:[0,0,0] op_sel_hi:[1,0,0]\n" \
  "v_fma_mix_f32 v7, v254, v97, v7 op_sel:[1,0,0] op_sel_hi:[1,0,0]\n" \
  "v_mov_b32 v29, 925353388\n" \
  "v_mov_b32_dpp v30, v10 row_newbcast:0 row_mask:0xf bank_mask:0xf\n" \
  "v_mov_b32_dpp v254, v10 row_newbcast:0 row_mask:0xf bank_mask:0xf\n" \
  "v_cvt_pk_bf16_f32 v36, v10, v11\n" \
  "v_cvt_pk_bf16_f32 v37, v12, v13\n" \
  "v_cvt_pk_bf16_f32 v38, v14, v15\n" \
  "v_cvt_pk_bf16_f32 v39, v26, v27\n" \
  "v_cvt_pk_bf16_f32 v44, v2, v3\n" \
  "v_cvt_pk_bf16_f32 v45, v4, v5\n" \
  "v_cvt_pk_bf16_f32 v46, v48, v49\n" \
  "v_cvt_pk_bf16_f32 v47, v114, v115\n" \
  "v_cvt_pk_bf16_f32 v248, v6, v7\n" \
  "v_permlane16_swap_b32 v30, v254\n" \
  "v_pk_add_f32 v[10:11], v[10:11], v[30:31] op_sel_hi:[1,0] neg_lo:[0,1] neg_hi:[0,1]\n" \
  "v_pk_add_f32 v[12:13], v[12:13], v[30:31] op_sel_hi:[1,0] neg_lo:[0,1] neg_hi:[0,1]\n" \
  "v_pk_add_f32 v[14:15], v[14:15], v[30:31] op_sel_hi:[1,0] neg_lo:[0,1] neg_hi:[0,1]\n" \
  "v_pk_add_f32 v[26:27], v[26:27], v[30:31] op_sel_hi:[1,0] neg_lo:[0,1] neg_hi:[0,1]\n" \
  "v_pk_add_f32 v[2:3], v[2:3], v[30:31] op_sel_hi:[1,0] neg_lo:[0,1] neg_hi:[0,1]\n" \
  "v_pk_add_f32 v[4:5], v[4:5], v[30:31] op_sel_hi:[1,0] neg_lo:[0,1] neg_hi:[0,1]\n" \
  "v_pk_add_f32 v[48:49], v[48:49], v[30:31] op_sel_hi:[1,0] neg_lo:[0,1] neg_hi:[0,1]\n" \
  "v_pk_add_f32 v[114:115], v[114:115], v[30:31] op_sel_hi:[1,0] neg_lo:[0,1] neg_hi:[0,1]\n" \
  "v_pk_add_f32 v[6:7], v[6:7], v[30:31] op_sel_hi:[1,0] neg_lo:[0,1] neg_hi:[0,1]\n" \
  "v_pk_add_f32 v[30:31], v[10:11], v[12:13]\n" \
  "v_pk_mul_f32 v[62:63], v[10:11], v[10:11]\n" \
  "v_pk_fma_f32 v[62:63], v[12:13], v[12:13], v[62:63]\n" \
  "v_pk_add_f32 v[30:31], v[30:31], v[14:15]\n" \
  "v_pk_fma_f32 v[62:63], v[14:15], v[14:15], v[62:63]\n" \
  "v_pk_add_f32 v[30:31], v[30:31], v[26:27]\n" \
  "v_pk_fma_f32 v[62:63], v[26:27], v[26:27], v[62:63]\n" \
  "v_pk_add_f32 v[30:31], v[30:31], v[2:3]\n" \
  "v_pk_fma_f32 v[62:63], v[2:3], v[2:3], v[62:63]\n" \
  "v_pk_add_f32 v[30:31], v[30:31], v[4:5]\n" \
  "v_pk_fma_f32 v[62:63], v[4:5], v[4:5], v[62:63]\n" \
  "v_pk_add_f32 v[30:31], v[30:31], v[48:49]\n" \
  "v_pk_fma_f32 v[62:63], v[48:49], v[48:49], v[62:63]\n" \
  "v_pk_add_f32 v[30:31], v[30:31], v[114:115]\n" \
  "v_pk_fma_f32 v[62:63], v[114:115], v[114:115], v[62:63]\n" \
  "v_pk_add_f32 v[30:31], v[30:31], v[6:7]\n" \
  "v_pk_fma_f32 v[62:63], v[6:7], v[6:7], v[62:63]\n" \
  "v_add_f32 v254, v30, v31\n" \
  "v_add_f32 v246, v62, v63\n" \
  "s_waitcnt vmcnt(16)\n" \
  "ds_read_b64_tr_b16 a[120:121], v18 offset:0\n" \
  "ds_read_b64_tr_b16 a[124:125], v19 offset:0\n" \
  "ds_read_b64_tr_b16 a[128:129], v20 offset:0\n" \
  "ds_read_b64_tr_b16 a[132:133], v21 offset:0\n" \
  "v_permlane16_swap_b32 v254, v246\n" \
  "v_add_f32 v254, v254, v246\n" \
  "buffer_store_dwordx4 v[36:39], v251, s[36:39], s10 offen offset:0 sc1\n" \
  "ds_read_b64_tr_b16 a[140:141], v18 offset:2048\n" \
  "ds_read_b64_tr_b16 a[144:145], v19 offset:2048\n" \
  "ds_read_b64_tr_b16 a[148:149], v20 offset:2048\n" \
  "v_add_f32_dpp v254, v254, v254 quad_perm:[1,0,3,2] row_mask:0xf bank_mask:0xf\n" \
  "buffer_store_dwordx4 v[44:47], v251, s[36:39], s10 offen offset:512 sc1\n" \
  "ds_read_b64_tr_b16 a[152:153], v21 offset:2048\n" \
  "ds_read_b64_tr_b16 a[122:123], v18 offset:4096\n" \
  "ds_read_b64_tr_b16 a[126:127], v19 offset:4096\n" \
  "v_add_f32_dpp v254, v254, v254 quad_perm:[2,3,0,1] row_mask:0xf bank_mask:0xf\n" \
  "buffer_store_dword v248, v250, s[36:39], s10 offen offset:1024 sc1\n" \
  "ds_read_b64_tr_b16 a[130:131], v20 offset:4096\n" \
  "ds_read_b64_tr_b16 a[134:135], v21 offset:4096\n" \
  "ds_read_b64_tr_b16 a[142:143], v18 offset:6144\n" \
  "v_add_f32_dpp v254, v254, v254 row_half_mirror row_mask:0xf bank_mask:0xf\n" \
  "ds_read_b64_tr_b16 a[146:147], v19 offset:6144\n" \
  "ds_read_b64_tr_b16 a[150:151], v20 offset:6144\n" \
  "ds_read_b64_tr_b16 a[154:155], v21 offset:6144\n" \
  "v_add_f32_dpp v254, v254, v254 row_mirror row_mask:0xf bank_mask:0xf\n" \
  "v_mov_b32 v246, v254\n" \
  "s_waitcnt vmcnt(11)\n" \
  "ds_read_b64_tr_b16 a[160:161], v18 offset:8192\n" \
  "ds_read_b64_tr_b16 a[164:165], v19 offset:8192\n" \
  "ds_read_b64_tr_b16 a[168:169], v20 offset:8192\n" \
  "ds_read_b64_tr_b16 a[172:173], v21 offset:8192\n" \
  "v_permlane16_swap_b32 v254, v246\n" \
  "v_mul_f32 v254, s35, v254\n" \
  "v_fma_f32 v246, v246, s35, v29\n" \
  "v_fma_f32 v246, -v254, v254, v246\n" \
  "v_max_f32 v246, v246, v29\n" \
  "v_rsq_f32 v28, v246\n" \
  "ds_read_b64_tr_b16 a[180:181], v18 offset:10240\n" \
  "ds_read_b64_tr_b16 a[184:185], v19 offset:10240\n" \
  "ds_read_b64_tr_b16 a[188:189], v20 offset:10240\n" \
  "ds_read_b64_tr_b16 a[192:193], v21 offset:10240\n" \
  "v_mul_f32_e64 v29, -v254, v28\n" \
  "v_pk_fma_f32 v[10:11], v[10:11], v[28:29], v[28:29] op_sel:[0,0,1] op_sel_hi:[1,0,1]\n" \
  "v_pk_fma_f32 v[10:11], v[10:11], v[32:33], v[50:51]\n" \
  "v_pk_fma_f32 v[12:13], v[12:13], v[28:29], v[28:29] op_sel:[0,0,1] op_sel_hi:[1,0,1]\n" \
  "v_pk_fma_f32 v[12:13], v[12:13], v[34:35], v[52:53]\n" \
  "v_pk_fma_f32 v[14:15], v[14:15], v[28:29], v[28:29] op_sel:[0,0,1] op_sel_hi:[1,0,1]\n" \
  "v_pk_fma_f32 v[14:15], v[14:15], v[102:103], v[58:59]\n" \
  "ds_read_b64_tr_b16 a[162:163], v18 offset:12288\n" \
  "ds_read_b64_tr_b16 a[166:167], v19 offset:12288\n" \
  "s_add_u32 m0, s57, 0\n" \
  "s_add_u32 s31, s12, 221184\n" \
  "s_waitcnt lgkmcnt(10)\n" \
  "buffer_load_dwordx4 v22, s[4:7], s31 offen offset:0 lds\n" \
  "v_pk_fma_f32 v[26:27], v[26:27], v[28:29], v[28:29] op_sel:[0,0,1] op_sel_hi:[1,0,1]\n" \
  "v_pk_fma_f32 v[26:27], v[26:27], v[104:105], v[60:61]\n" \
  "v_pk_fma_f32 v[2:3], v[2:3], v[28:29], v[28:29] op_sel:[0,0,1] op_sel_hi:[1,0,1]\n" \
  "v_pk_fma_f32 v[2:3], v[2:3], v[40:41], v[54:55]\n" \
  "ds_read_b64_tr_b16 a[170:171], v20 offset:12288\n" \
  "ds_read_b64_tr_b16 a[174:175], v21 offset:12288\n" \
  "buffer_load_dwordx4 v23, s[4:7], s31 offen offset:1024 lds\n" \
  "v_pk_fma_f32 v[4:5], v[4:5], v[28:29], v[28:29] op_sel:[0,0,1] op_sel_hi:[1,0,1]\n" \
  "v_pk_fma_f32 v[4:5], v[4:5], v[42:43], v[56:57]\n" \
  "v_pk_fma_f32 v[48:49], v[48:49], v[28:29], v[28:29] op_sel:[0,0,1] op_sel_hi:[1,0,1]\n" \
  "v_pk_fma_f32 v[48:49], v[48:49], v[110:111], v[70:71]\n" \
  "ds_read_b64_tr_b16 a[182:183], v18 offset:14336\n" \
  "ds_read_b64_tr_b16 a[186:187], v19 offset:14336\n" \
  "buffer_load_dwordx4 v24, s[4:7], s31 offen offset:2048 lds\n" \
  "v_pk_fma_f32 v[114:115], v[114:115], v[28:29], v[28:29] op_sel:[0,0,1] op_sel_hi:[1,0,1]\n" \
  "v_pk_fma_f32 v[114:115], v[114:115], v[112:113], v[72:73]\n" \
  "v_pk_fma_f32 v[6:7], v[6:7], v[28:29], v[28:29] op_sel:[0,0,1] op_sel_hi:[1,0,1]\n" \
  "v_pk_fma_f32 v[6:7], v[6:7], v[124:125], v[78:79]\n" \
  "ds_read_b64_tr_b16 a[190:191], v20 offset:14336\n" \
  "ds_read_b64_tr_b16 a[194:195], v21 offset:14336\n" \
  "buffer_load_dwordx4 v25, s[4:7], s31 offen offset:3072 lds\n" \
  "v_mov_b32 v29, 925353388\n" \
  "v_pk_add_f32 v[30:31], v[10:11], v[12:13]\n" \
  "v_pk_mul_f32 v[32:33], v[10:11], v[10:11]\n" \
  "v_pk_fma_f32 v[32:33], v[12:13], v[12:13], v[32:33]\n" \
  "v_pk_add_f32 v[30:31], v[30:31], v[14:15]\n" \
  "v_pk_fma_f32 v[32:33], v[14:15], v[14:15], v[32:33]\n" \
  "v_pk_add_f32 v[30:31], v[30:31], v[26:27]\n" \
  "v_pk_fma_f32 v[32:33], v[26:27], v[26:27], v[32:33]\n" \
  "v_pk_add_f32 v[30:31], v[30:31], v[2:3]\n" \
  "v_pk_fma_f32 v[32:33], v[2:3], v[2:3], v[32:33]\n" \
  "v_pk_add_f32 v[30:31], v[30:31], v[4:5]\n" \
  "v_pk_fma_f32 v[32:33], v[4:5], v[4:5], v[32:33]\n" \
  "v_pk_add_f32 v[30:31], v[30:31], v[48:49]\n" \
  "v_pk_fma_f32 v[32:33], v[48:49], v[48:49], v[32:33]\n" \
  "v_pk_add_f32 v[30:31], v[30:31], v[114:115]\n" \
  "v_pk_fma_f32 v[32:33], v[114:115], v[114:115], v[32:33]\n" \
  "v_pk_add_f32 v[30:31], v[30:31], v[6:7]\n" \
  "v_pk_fma_f32 v[32:33], v[6:7], v[6:7], v[32:33]\n" \
  "v_add_f32 v254, v30, v31\n" \
  "v_add_f32 v251, v32, v33\n" \
  "s_waitcnt vmcnt(7)\n" \
  "ds_read_b64_tr_b16 a[240:241], v18 offset:16384\n" \
  "ds_read_b64_tr_b16 a[244:245], v19 offset:16384\n" \
  "ds_read_b64_tr_b16 a[248:249], v20 offset:16384\n" \
  "s_add_u32 m0, s57, 4096\n" \
  "s_add_u32 s31, s12, 9658368\n" \
  "buffer_load_dwordx4 v22, s[4:7], s31 offen offset:0 lds\n" \
  "v_permlane16_swap_b32 v254, v251\n" \
  "v_add_f32 v254, v254, v251\n" \
  "ds_read_b64_tr_b16 a[252:253], v21 offset:16384\n" \
  "ds_read_b64_tr_b16 v[196:197], v18 offset:18432\n" \
  "ds_read_b64_tr_b16 v[200:201], v19 offset:18432\n" \
  "buffer_load_dwordx4 v23, s[4:7], s31 offen offset:1024 lds\n" \
  "v_add_f32_dpp v254, v254, v254 quad_perm:[1,0,3,2] row_mask:0xf bank_mask:0xf\n" \
  "ds_read_b64_tr_b16 v[204:205], v20 offset:18432\n" \
  "ds_read_b64_tr_b16 v[208:209], v21 offset:18432\n" \
  "ds_read_b64_tr_b16 a[242:243], v18 offset:20480\n" \
  "buffer_load_dwordx4 v24, s[4:7], s31 offen offset:2048 lds\n" \
  "v_add_f32_dpp v254, v254, v254 quad_perm:[2,3,0,1] row_mask:0xf bank_mask:0xf\n" \
  "ds_read_b64_tr_b16 a[246:247], v19 offset:20480\n" \
  "ds_read_b64_tr_b16 a[250:251], v20 offset:20480\n" \
  "ds_read_b64_tr_b16 a[254:255], v21 offset:20480\n" \
  "buffer_load_dwordx4 v25, s[4:7], s31 offen offset:3072 lds\n" \
  "v_add_f32_dpp v254, v254, v254 row_half_mirror row_mask:0xf bank_mask:0xf\n" \
  "ds_read_b64_tr_b16 v[198:199], v18 offset:22528\n" \
  "ds_read_b64_tr_b16 v[202:203], v19 offset:22528\n" \
  "ds_read_b64_tr_b16 v[206:207], v20 offset:22528\n" \
  "ds_read_b64_tr_b16 v[210:211], v21 offset:22528\n" \
  "s_add_u32 m0, s57, 8192\n" \
  "s_add_u32 s31, s12, 516096\n" \
  "s_waitcnt lgkmcnt(15)\n" \
  "buffer_load_dwordx4 v22, s[4:7], s31 offen offset:0 lds\n" \
  "v_add_f32_dpp v254, v254, v254 row_mirror row_mask:0xf bank_mask:0xf\n" \
  "v_mov_b32 v251, v254\n" \
  "buffer_load_dwordx4 v23, s[4:7], s31 offen offset:1024 lds\n" \
  "buffer_load_dwordx4 v24, s[4:7], s31 offen offset:2048 lds\n" \
  "v_permlane16_swap_b32 v254, v251\n" \
  "v_mul_f32 v254, s35, v254\n" \
  "v_fma_f32 v251, v251, s35, v29\n" \
  "v_fma_f32 v251, -v254, v254, v251\n" \
  "v_max_f32 v251, v251, v29\n" \
  "v_rsq_f32 v28, v251\n" \
  "buffer_load_dwordx4 v25, s[4:7], s31 offen offset:3072 lds\n" \
  "v_mul_f32_e64 v29, -v254, v28\n" \
  "v_pk_fma_f32 v[10:11], v[10:11], v[28:29], v[28:29] op_sel:[0,0,1] op_sel_hi:[1,0,1]\n" \
  "v_pk_fma_f32 v[12:13], v[12:13], v[28:29], v[28:29] op_sel:[0,0,1] op_sel_hi:[1,0,1]\n" \
  "v_pk_fma_f32 v[14:15], v[14:15], v[28:29], v[28:29] op_sel:[0,0,1] op_sel_hi:[1,0,1]\n" \
  "v_pk_fma_f32 v[26:27], v[26:27], v[28:29], v[28:29] op_sel:[0,0,1] op_sel_hi:[1,0,1]\n" \
  "v_pk_fma_f32 v[2:3], v[2:3], v[28:29], v[28:29] op_sel:[0,0,1] op_sel_hi:[1,0,1]\n" \
  "v_pk_fma_f32 v[4:5], v[4:5], v[28:29], v[28:29] op_sel:[0,0,1] op_sel_hi:[1,0,1]\n" \
  "v_pk_fma_f32 v[48:49], v[48:49], v[28:29], v[28:29] op_sel:[0,0,1] op_sel_hi:[1,0,1]\n" \
  "v_pk_fma_f32 v[114:115], v[114:115], v[28:29], v[28:29] op_sel:[0,0,1] op_sel_hi:[1,0,1]\n" \
  "v_pk_fma_f32 v[6:7], v[6:7], v[28:29], v[28:29] op_sel:[0,0,1] op_sel_hi:[1,0,1]\n" \
  "v_cvt_pk_bf16_f32 v28, v10, v11\n" \
  "v_cvt_pk_bf16_f32 v29, v12, v13\n" \
  "v_cvt_pk_bf16_f32 v30, v14, v15\n" \
  "v_cvt_pk_bf16_f32 v31, v26, v27\n" \
  "ds_write_b128 v255, v[28:31] offset:0\n" \
  "v_cvt_pk_bf16_f32 v10, v2, v3\n" \
  "v_cvt_pk_bf16_f32 v11, v4, v5\n" \
  "v_cvt_pk_bf16_f32 v12, v48, v49\n" \
  "v_cvt_pk_bf16_f32 v13, v114, v115\n" \
  "ds_write_b128 v255, v[10:13] offset:512\n" \
  "v_cvt_pk_bf16_f32 v255, v6, v7\n" \
  "ds_write_b32 v249, v255 offset:1024\n" \
  "s_add_u32 m0, s57, 12288\n" \
  "s_add_u32 s31, s12, 9953280\n" \
  "buffer_load_dwordx4 v22, s[4:7], s31 offen offset:0 lds\n" \
  "buffer_load_dwordx4 v23, s[4:7], s31 offen offset:1024 lds\n" \
  "s_waitcnt lgkmcnt(0)\n" \
  "s_barrier\n" \
  "ds_read_b64_tr_b16 v[2:3], v253 offset:9216\n" \
  "ds_read_b64_tr_b16 v[4:5], v253 offset:11664\n" \
  "ds_read_b64_tr_b16 v[10:11], v253 offset:9728\n" \
  "ds_read_b64_tr_b16 v[12:13], v253 offset:12176\n" \
  "ds_read_b64_tr_b16 v[26:27], v253 offset:10240\n" \
  "ds_read_b64_tr_b16 v[28:29], v253 offset:12688\n" \
  "buffer_load_dwordx4 v24, s[4:7], s31 offen offset:2048 lds\n" \
  "buffer_load_dwordx4 v25, s[4:7], s31 offen offset:3072 lds\n" \
  "s_add_u32 s32, s11, 1048576\n" \
  "s_add_u32 s33, s11, 2097152\n" \
  "s_waitcnt lgkmcnt(4)\n" \
  "buffer_store_dwordx4 v[2:5], v241, s[36:39], s11 offen offset:0 sc1\n" \
  "s_waitcnt lgkmcnt(2)\n" \
  "buffer_store_dwordx4 v[10:13], v241, s[36:39], s32 offen offset:0 sc1\n" \
  "s_mov_b64 exec, 65535\n" \
  "s_waitcnt lgkmcnt(0)\n" \
  "buffer_store_dwordx4 v[26:29], v241, s[36:39], s33 offen offset:0 sc1\n" \
  "s_mov_b64 exec, -1\n" \
  ".Lscc_%=:\n" \
  "v_xor_b32 v255, s18, v8\n" \
  "v_bitop3_b32 v255, v9, s19, v255 bitop3:0xbe\n" \
  "v_cmp_ne_u32 vcc, 0, v255\n" \
  "s_cbranch_vccnz .Lscs_%=\n" \
  ".Lscd_%=:\n" \
  "s_add_u32 m0, s57, 16384\n" \
  "s_add_u32 s31, s14, 0\n" \
  "buffer_load_dwordx4 v252, s[36:39], s31 offen offset:0 sc1 lds\n" \
  "buffer_load_dwordx4 v252, s[36:39], s31 offen offset:1024 sc1 lds\n" \
  "buffer_load_dwordx4 v252, s[36:39], s31 offen offset:2048 sc1 lds\n" \
  "buffer_load_dwordx4 v252, s[36:39], s31 offen offset:3072 sc1 lds\n" \
  "s_add_u32 m0, s57, 20480\n" \
  "s_add_u32 s31, s14, 8192\n" \
  "buffer_load_dwordx4 v252, s[36:39], s31 offen offset:0 sc1 lds\n" \
  "buffer_load_dwordx4 v252, s[36:39], s31 offen offset:1024 sc1 lds\n" \
  "buffer_load_dwordx4 v252, s[36:39], s31 offen offset:2048 sc1 lds\n" \
  "buffer_load_dwordx4 v252, s[36:39], s31 offen offset:3072 sc1 lds\n" \
  "s_lshr_b32 s0, s2, 6\n" \
  "s_lshr_b32 s1, s2, 1\n" \
  "s_and_b32 s34, s1, 3\n" \
  "s_lshr_b32 s1, s2, 3\n" \
  "s_and_b32 s84, s1, 1\n" \
  "s_and_b32 s1, s2, 7\n" \
  "s_lshl_b32 s1, s1, 2\n" \
  "s_lshr_b32 s100, s2, 4\n" \
  "s_and_b32 s100, s100, 3\n" \
  "s_add_u32 s88, s1, s100\n" \
  "s_and_b32 s1, s2, 1\n" \
  "s_lshl_b32 s1, s1, 3\n" \
  "s_lshr_b32 s100, s2, 3\n" \
  "s_and_b32 s100, s100, 7\n" \
  "s_add_u32 s85, s1, s100\n" \
  "s_mul_i32 s1, s0, 4505600\n" \
  "s_add_u32 s52, s1, 12123648\n" \
  "s_add_u32 s53, s52, 3276800\n" \
  "s_add_u32 s83, s52, 2048000\n" \
  "s_lshl_b32 s1, s0, 5\n" \
  "s_add_u32 s1, s1, s88\n" \
  "s_lshl_b32 s1, s1, 1\n" \
  "s_add_u32 s1, s1, s84\n" \
  "s_lshl_b32 s1, s1, 7\n" \
  "s_lshl_b32 s100, s20, 5\n" \
  "s_add_u32 s1, s1, s100\n" \
  "s_add_u32 s54, s1, 262144\n" \
  "s_lshl_b32 s1, s20, 5\n" \
  "s_sub_u32 s1, s54, s1\n" \
  "s_lshl_b32 s100, s34, 11\n" \
  "s_sub_u32 s1, s1, s100\n" \
  "s_add_u32 s16, s1, 65536\n" \
  "s_mov_b32 s55, 3184710631\n" \
  "s_mul_i32 s56, s20, 9568\n" \
  "s_lshl_b32 s58, s20, 13\n" \
  "s_lshr_b32 s1, s88, 1\n" \
  "s_mul_i32 s90, s1, 18\n" \
  "s_and_b32 s1, s88, 1\n" \
  "s_lshl_b32 s1, s1, 3\n" \
  "s_add_u32 s1, s90, s1\n" \
  "s_lshl_b32 s100, s84, 2\n" \
  "s_add_u32 s1, s1, s100\n" \
  "s_add_u32 s1, s1, s20\n" \
  "s_add_u32 s1, s1, 1\n" \
  "s_lshl_b32 s63, s1, 9\n" \
  "s_and_b32 s1, s88, 1\n" \
  "s_add_u32 s1, s90, s1\n" \
  "s_add_u32 s1, s1, 17\n" \
  "s_lshl_b32 s1, s1, 9\n" \
  "s_lshl_b32 s100, s20, 7\n" \
  "s_add_u32 s64, s1, s100\n" \
  "s_lshl_b32 s1, s0, 5\n" \
  "s_lshl_b32 s100, s34, 3\n" \
  "s_add_u32 s1, s1, s100\n" \
  "s_lshl_b32 s100, s20, 1\n" \
  "s_add_u32 s1, s1, s100\n" \
  "s_lshl_b32 s1, s1, 8\n" \
  "s_add_u32 s1, s1, 262144\n" \
  "s_sub_u32 s59, s1, s63\n" \
  "s_add_u32 s75, s59, 196608\n" \
  "s_lshl_b32 s1, s85, 7\n" \
  "s_mul_i32 s1, s1, 576\n" \
  "s_mul_i32 s100, s0, 144\n" \
  "s_add_u32 s90, s1, s100\n" \
  "s_mul_i32 s1, s20, 9216\n" \
  "s_add_u32 s1, s90, s1\n" \
  "s_lshl_b32 s100, s34, 5\n" \
  "s_add_u32 s1, s1, s100\n" \
  "s_lshl_b32 s61, s1, 1\n" \
  "s_lshl_b32 s1, s34, 4\n" \
  "s_add_u32 s1, s1, s20\n" \
  "s_mul_i32 s1, s1, 576\n" \
  "s_add_u32 s1, s90, s1\n" \
  "s_add_u32 s1, s1, 128\n" \
  "s_lshl_b32 s62, s1, 1\n" \
  "v_and_b32 v255, 768, v252\n" \
  "v_lshlrev_b32 v255, 2, v255\n" \
  "v_and_b32 v254, 192, v252\n" \
  "v_lshl_or_b32 v255, v254, 1, v255\n" \
  "v_bfe_u32 v254, v191, 3, 2\n" \
  "v_lshl_or_b32 v255, v254, 5, v255\n" \
  "v_and_b32 v253, 3, v191\n" \
  "v_lshl_or_b32 v255, v253, 3, v255\n" \
  "v_add_u32 v8, s57, v255\n" \
  "v_mov_b32 v63, v8\n" \
  "v_lshl_or_b32 v9, v243, 16, v244\n" \
  "v_and_b32 v255, 12, v191\n" \
  "v_lshrrev_b32 v254, 4, v191\n" \
  "v_add_u32 v254, v254, v255\n" \
  "v_mul_u32_u24 v254, 1152, v254\n" \
  "v_lshl_add_u32 v254, v253, 3, v254\n" \
  "v_add_u32 v10, s61, v254\n" \
  "v_and_b32 v253, 1, v191\n" \
  "v_lshl_add_u32 v255, v253, 1, v255\n" \
  "v_bfe_u32 v254, v191, 1, 1\n" \
  "v_lshlrev_b32 v254, 6, v254\n" \
  "v_lshrrev_b32 v11, 2, v191\n" \
  "v_and_or_b32 v254, v11, 12, v254\n" \
  "v_mul_u32_u24 v254, 1152, v254\n" \
  "v_lshl_add_u32 v254, v255, 1, v254\n" \
  "v_add_u32 v11, s62, v254\n" \
  "v_lshrrev_b32 v255, 3, v252\n" \
  "v_bitop3_b32 v255, v255, 32, v252 bitop3:0x6a\n" \
  "v_lshl_or_b32 v255, v255, 5, v255\n" \
  "v_and_b32 v12, 28784, v255\n" \
  "v_xor_b32 v255, 64, v12\n" \
  "v_add_u32 v13, 31744, v255\n" \
  "v_lshlrev_b32 v14, 16, v242\n" \
  "v_add_u32 v255, s57, v252\n" \
  "v_add_u32 v22, 28672, v255\n" \
  "v_lshlrev_b32 v52, 16, v16\n" \
  "v_and_b32 v53, 4294901760, v16\n" \
  "v_lshlrev_b32 v54, 16, v17\n" \
  "v_and_b32 v55, 4294901760, v17\n" \
  "v_lshlrev_b32 v56, 16, v247\n" \
  "v_mov_b32 v57, 3222493025\n" \
  "s_mul_i32 s32, s20, 1056\n" \
  "v_bfe_u32 v255, v191, 1, 1\n" \
  "v_mul_u32_u24 v255, 544, v255\n" \
  "v_lshl_add_u32 v255, v253, 4, v255\n" \
  "v_bfe_u32 v254, v191, 2, 1\n" \
  "v_lshl_add_u32 v255, v254, 8, v255\n" \
  "v_bfe_u32 v254, v191, 3, 2\n" \
  "v_lshl_add_u32 v255, v254, 6, v255\n" \
  "v_and_b32 v254, 32, v191\n" \
  "v_add3_u32 v58, v255, v254, s32\n" \
  "v_add_u32 v59, 524288, v252\n" \
  "v_bfe_u32 v255, v191, 3, 2\n" \
  "v_mul_u32_u24 v255, 288, v255\n" \
  "v_lshl_add_u32 v255, v254, 1, v255\n" \
  "v_and_b32 v254, 7, v191\n" \
  "v_lshl_add_u32 v255, v254, 2, v255\n" \
  "s_and_b32 s32, s20, 1\n" \
  "s_lshl_b32 s32, s32, 7\n" \
  "s_lshr_b32 s33, s20, 1\n" \
  "s_lshl_b32 s33, s33, 5\n" \
  "s_add_u32 s32, s32, s33\n" \
  "s_add_u32 s32, s32, 4224\n" \
  "v_add_u32 v60, s32, v255\n" \
  "v_lshl_add_u32 v61, v191, 3, s63\n" \
  "v_lshl_add_u32 v62, v191, 1, s64\n" \
  "s_mul_i32 s1, s34, 1152\n" \
  "s_lshr_b32 s100, s20, 1\n" \
  "s_mul_i32 s100, s100, 576\n" \
  "s_add_u32 s1, s1, s100\n" \
  "s_lshl_b32 s1, s1, 12\n" \
  "s_lshl_b32 s100, s85, 8\n" \
  "s_add_u32 s65, s1, s100\n" \
  "s_and_b32 s1, s20, 1\n" \
  "s_lshl_b32 s1, s1, 20\n" \
  "s_add_u32 s1, s65, s1\n" \
  "s_add_u32 s60, s1, 655488\n" \
  "s_and_b32 s1, s20, 1\n" \
  "s_lshl_b32 s1, s1, 17\n" \
  "s_add_u32 s1, s65, s1\n" \
  "s_add_u32 s65, s1, 2097152\n" \
  "s_add_u32 s66, s65, 128\n" \
  "s_add_u32 s61, s60, 130944\n" \
  "s_add_u32 s62, s60, 131072\n" \
  "s_add_u32 s63, s60, 262016\n" \
  "s_add_u32 s64, s60, 262144\n" \
  "s_lshl_b32 s1, s34, 7\n" \
  "s_add_u32 s67, s1, 163200\n" \
  "s_lshl_b32 s1, s34, 2\n" \
  "s_add_u32 s98, s1, s20\n" \
  "s_mul_i32 s1, s98, 9216\n" \
  "s_add_u32 s1, s1, s52\n" \
  "s_add_u32 s68, s1, 819712\n" \
  "s_add_u32 s69, s68, 3072\n" \
  "s_add_u32 s70, s68, 6144\n" \
  "s_lshl_b32 s1, s85, 14\n" \
  "s_add_u32 s1, s1, s52\n" \
  "s_add_u32 s90, s1, 967168\n" \
  "s_lshl_b32 s1, s98, 8\n" \
  "s_add_u32 s79, s1, s90\n" \
  "s_add_u32 s80, s79, 4096\n" \
  "s_add_u32 s81, s79, 8192\n" \
  "s_add_u32 s82, s79, 12288\n" \
  "s_lshl_b32 s99, s20, 10\n" \
  "s_add_u32 s87, s99, 65536\n" \
  "s_add_u32 s90, s90, s99\n" \
  "s_lshl_b32 s1, s2, 2\n" \
  "s_add_u32 s1, s1, s20\n" \
  "s_lshl_b32 s1, s1, 8\n" \
  "s_add_u32 s91, s1, 69206016\n" \
  "s_mul_i32 s1, s0, 589824\n" \
  "s_lshl_b32 s100, s20, 14\n" \
  "s_add_u32 s1, s1, s100\n" \
  "s_add_u32 s48, s1, 9764864\n" \
  "s_sub_u32 s1, s34, 1\n" \
  "s_and_b32 s1, s1, 3\n" \
  "s_lshl_b32 s1, s1, 12\n" \
  "s_add_u32 s98, s1, s90\n" \
  "s_add_u32 s71, s98, 4292919296\n" \
  "s_add_u32 s76, s98, 4293328896\n" \
  "s_sub_u32 s1, s34, 2\n" \
  "s_and_b32 s1, s1, 3\n" \
  "s_lshl_b32 s1, s1, 12\n" \
  "s_add_u32 s98, s1, s90\n" \
  "s_add_u32 s72, s98, 4293328896\n" \
  "s_add_u32 s77, s98, 4293328896\n" \
  "s_sub_u32 s1, s34, 3\n" \
  "s_and_b32 s1, s1, 3\n" \
  "s_lshl_b32 s1, s1, 12\n" \
  "s_add_u32 s98, s1, s90\n" \
  "s_add_u32 s73, s98, 4293328896\n" \
  "s_add_u32 s78, s98, 4293738496\n" \
  "s_and_b32 s1, s34, 3\n" \
  "s_lshl_b32 s1, s1, 12\n" \
  "s_add_u32 s98, s1, s90\n" \
  "s_add_u32 s74, s98, 4294148096\n" \
  "s_add_u32 s49, s48, 8192\n" \
  "s_add_u32 s50, s48, 4096\n" \
  "s_add_u32 s51, s48, 12288\n" \
  "s_lshr_b32 s1, s2, 3\n" \
  "s_lshl_b32 s1, s1, 2\n" \
  "s_add_u32 s98, s1, s20\n" \
  "s_and_b32 s1, s98, 31\n" \
  "s_lshl_b32 s1, s1, 2\n" \
  "s_add_u32 s90, s1, 448\n" \
  "s_and_b32 s1, s98, 8\n" \
  "s_lshl_b32 s1, s1, 5\n" \
  "s_and_b32 s100, s98, 7\n" \
  "s_lshl_b32 s100, s100, 2\n" \
  "s_add_u32 s1, s1, s100\n" \
  "s_add_u32 s89, s1, 160\n" \
  "s_and_b32 s1, s98, 15\n" \
  "s_lshl_b32 s1, s1, 2\n" \
  "s_add_u32 s86, s1, 192\n" \
  "s_bitcmp1_b32 s98, 4\n" \
  "s_cselect_b32 s89, s89, s86\n" \
  "s_bitcmp1_b32 s98, 5\n" \
  "s_cselect_b32 s89, s90, s89\n" \
  "s_lshr_b32 s1, s98, 6\n" \
  "s_mul_i32 s1, s1, 576\n" \
  "s_mul_i32 s100, s34, 1152\n" \
  "s_add_u32 s1, s1, s100\n" \
  "s_add_u32 s1, s1, s89\n" \
  "s_lshl_b32 s1, s1, 12\n" \
  "s_and_b32 s100, s2, 1\n" \
  "s_lshl_b32 s100, s100, 11\n" \
  "s_add_u32 s1, s1, s100\n" \
  "s_lshr_b32 s100, s2, 3\n" \
  "s_and_b32 s100, s100, 3\n" \
  "s_add_u32 s1, s1, s100\n" \
  "s_add_u32 s89, s1, 1\n" \
  "s_sub_u32 s98, s57, s99\n" \
  "s_lshl_b32 s1, s34, 17\n" \
  "s_add_u32 s85, s1, 65536\n" \
  "s_mov_b32 s90, 7405568\n" \
  "s_add_u32 s1, s0, 3\n" \
  "s_and_b32 s1, s1, 3\n" \
  "s_lshl_b32 s88, s1, 12\n" \
  "s_mov_b32 s30, s16\n" \
  "v_mov_b32 v2, v12\n" \
  "v_mov_b32 v3, v13\n" \
  "v_add_u32 v4, 63488, v12\n" \
  "v_add_u32 v5, 63488, v13\n" \
  "v_mov_b32 v190, 1\n" \
  "v_lshrrev_b32 v189, 1, v252\n" \
  "v_add_u32 v255, s57, v189\n" \
  "s_lshl_b32 s32, s20, 11\n" \
  "v_add_u32 v189, s32, v189\n" \
  "s_waitcnt vmcnt(19)\n" \
  "ds_read_b64_tr_b16 a[200:201], v18 offset:0\n" \
  "ds_read_b64_tr_b16 a[204:205], v19 offset:0\n" \
  "ds_read_b64_tr_b16 a[208:209], v20 offset:0\n" \
  "ds_read_b64_tr_b16 a[212:213], v21 offset:0\n" \
  "ds_read_b64_tr_b16 a[220:221], v18 offset:2048\n" \
  "ds_read_b64_tr_b16 a[224:225], v19 offset:2048\n" \
  "ds_read_b64_tr_b16 a[228:229], v20 offset:2048\n" \
  "ds_read_b64_tr_b16 a[232:233], v21 offset:2048\n" \
  "ds_read_b64_tr_b16 a[202:203], v18 offset:4096\n" \
  "ds_read_b64_tr_b16 a[206:207], v19 offset:4096\n" \
  "ds_read_b64_tr_b16 a[210:211], v20 offset:4096\n" \
  "ds_read_b64_tr_b16 a[214:215], v21 offset:4096\n" \
  "ds_read_b64_tr_b16 a[222:223], v18 offset:6144\n" \
  "ds_read_b64_tr_b16 a[226:227], v19 offset:6144\n" \
  "ds_read_b64_tr_b16 a[230:231], v20 offset:6144\n" \
  "ds_read_b64_tr_b16 a[234:235], v21 offset:6144\n" \
  "s_waitcnt vmcnt(11)\n" \
  "ds_read_b64_tr_b16 v[216:217], v18 offset:8192\n" \
  "ds_read_b64_tr_b16 v[220:221], v19 offset:8192\n" \
  "ds_read_b64_tr_b16 v[224:225], v20 offset:8192\n" \
  "ds_read_b64_tr_b16 v[228:229], v21 offset:8192\n" \
  "ds_read_b64_tr_b16 v[236:237], v18 offset:10240\n" \
  "ds_read_b64_tr_b16 v[240:241], v19 offset:10240\n" \
  "ds_read_b64_tr_b16 v[244:245], v20 offset:10240\n" \
  "ds_read_b64_tr_b16 v[248:249], v21 offset:10240\n" \
  "ds_read_b64_tr_b16 v[218:219], v18 offset:12288\n" \
  "ds_read_b64_tr_b16 v[222:223], v19 offset:12288\n" \
  "ds_read_b64_tr_b16 v[226:227], v20 offset:12288\n" \
  "ds_read_b64_tr_b16 v[230:231], v21 offset:12288\n" \
  "ds_read_b64_tr_b16 v[238:239], v18 offset:14336\n" \
  "ds_read_b64_tr_b16 v[242:243], v19 offset:14336\n" \
  "ds_read_b64_tr_b16 v[246:247], v20 offset:14336\n" \
  "ds_read_b64_tr_b16 v[250:251], v21 offset:14336\n" \
  "s_waitcnt vmcnt(8)\n" \
  "s_mov_b64 exec, 1\n" \
  "buffer_store_dword v190, off, s[36:39], s28 sc1\n" \
  "s_mov_b64 exec, -1\n" \
  "s_add_u32 m0, s57, 24576\n" \
  "s_add_u32 s31, s14, 4096\n" \
  "buffer_load_dwordx4 v252, s[36:39], s31 offen offset:0 sc1 lds\n" \
  "buffer_load_dwordx4 v252, s[36:39], s31 offen offset:1024 sc1 lds\n" \
  "buffer_load_dwordx4 v252, s[36:39], s31 offen offset:2048 sc1 lds\n" \
  "buffer_load_dwordx4 v252, s[36:39], s31 offen offset:3072 sc1 lds\n" \
  "s_add_u32 m0, s57, 28672\n" \
  "s_add_u32 s31, s14, 12288\n" \
  "buffer_load_dwordx4 v252, s[36:39], s31 offen offset:0 sc1 lds\n" \
  "buffer_load_dwordx4 v252, s[36:39], s31 offen offset:1024 sc1 lds\n" \
  "s_lshl_b32 m0, s20, 11\n" \
  "s_add_u32 s31, s31, 2048\n" \
  "buffer_load_dwordx4 v252, s[36:39], s31 offen offset:0 sc1 lds\n" \
  "buffer_load_dwordx4 v252, s[36:39], s31 offen offset:1024 sc1 lds\n" \
  "s_bfe_u32 s32, s27, 131076\n" \
  "s_sub_u32 s100, s60, 655488\n" \
  "s_sub_u32 s101, s60, 720896\n" \
  "s_lshl_b32 s32, s32, 12\n" \
  "s_add_u32 s32, s32, 0\n" \
  "s_cmp_ge_u32 s32, 16384\n" \
  "s_cselect_b32 s32, 0, s32\n" \
  "s_bitcmp1_b32 s32, 12\n" \
  "s_cselect_b32 s33, s101, s100\n" \
  "s_lshl4_add_u32 s31, s32, s33\n" \
  "s_add_u32 s33, s32, s57\n" \
  "s_add_u32 m0, s33, 0\n" \
  "s_waitcnt lgkmcnt(0)\n" \
  "buffer_load_dwordx4 v2, s[40:43], s31 offen offset:0 lds\n" \
  "buffer_load_dwordx4 v3, s[40:43], s31 offen offset:1024 lds\n" \
  "buffer_load_dwordx4 v4, s[40:43], s31 offen offset:2048 lds\n" \
  "buffer_load_dwordx4 v5, s[40:43], s31 offen offset:3072 lds\n" \
  "s_add_u32 s32, s32, 4096\n" \
  "s_cmp_ge_u32 s32, 16384\n" \
  "s_cselect_b32 s32, 0, s32\n" \
  "s_bitcmp1_b32 s32, 12\n" \
  "s_cselect_b32 s33, s101, s100\n" \
  "s_lshl4_add_u32 s31, s32, s33\n" \
  "s_add_u32 s33, s32, s57\n" \
  "s_add_u32 m0, s33, 0\n" \
  "s_nop 0\n" \
  "buffer_load_dwordx4 v2, s[40:43], s31 offen offset:0 lds\n" \
  "buffer_load_dwordx4 v3, s[40:43], s31 offen offset:1024 lds\n" \
  "buffer_load_dwordx4 v4, s[40:43], s31 offen offset:2048 lds\n" \
  "buffer_load_dwordx4 v5, s[40:43], s31 offen offset:3072 lds\n" \
  "s_add_u32 s32, s32, 4096\n" \
  "s_cmp_ge_u32 s32, 16384\n" \
  "s_cselect_b32 s32, 0, s32\n" \
  "s_bitcmp1_b32 s32, 12\n" \
  "s_cselect_b32 s33, s101, s100\n" \
  "s_lshl4_add_u32 s31, s32, s33\n" \
  "s_add_u32 s33, s32, s57\n" \
  "s_add_u32 m0, s33, 0\n" \
  "s_nop 0\n" \
  "buffer_load_dwordx4 v2, s[40:43], s31 offen offset:0 lds\n" \
  "buffer_load_dwordx4 v3, s[40:43], s31 offen offset:1024 lds\n" \
  "buffer_load_dwordx4 v4, s[40:43], s31 offen offset:2048 lds\n" \
  "buffer_load_dwordx4 v5, s[40:43], s31 offen offset:3072 lds\n" \
  "s_add_u32 s32, s32, 4096\n" \
  "s_cmp_ge_u32 s32, 16384\n" \
  "s_cselect_b32 s32, 0, s32\n" \
  "s_bitcmp1_b32 s32, 12\n" \
  "s_cselect_b32 s33, s101, s100\n" \
  "s_lshl4_add_u32 s31, s32, s33\n" \
  "s_add_u32 s33, s32, s57\n" \
  "s_add_u32 m0, s33, 0\n" \
  "s_nop 0\n" \
  "buffer_load_dwordx4 v2, s[40:43], s31 offen offset:0 lds\n" \
  "buffer_load_dwordx4 v3, s[40:43], s31 offen offset:1024 lds\n" \
  "buffer_load_dwordx4 v4, s[40:43], s31 offen offset:2048 lds\n" \
  "buffer_load_dwordx4 v5, s[40:43], s31 offen offset:3072 lds\n" \
  "s_waitcnt vmcnt(16)\n" \
  "ds_read_b64_tr_b16 a[16:17], v255 offset:16384\n" \
  "ds_read_b64_tr_b16 a[36:37], v255 offset:16896\n" \
  "ds_read_b64_tr_b16 a[18:19], v255 offset:17408\n" \
  "ds_read_b64_tr_b16 a[38:39], v255 offset:17920\n" \
  "ds_read_b64_tr_b16 a[56:57], v255 offset:18432\n" \
  "ds_read_b64_tr_b16 a[76:77], v255 offset:18944\n" \
  "ds_read_b64_tr_b16 a[58:59], v255 offset:19456\n" \
  "ds_read_b64_tr_b16 a[78:79], v255 offset:19968\n" \
  "ds_read_b64_tr_b16 a[96:97], v255 offset:20480\n" \
  "ds_read_b64_tr_b16 a[116:117], v255 offset:20992\n" \
  "ds_read_b64_tr_b16 a[98:99], v255 offset:21504\n" \
  "ds_read_b64_tr_b16 a[118:119], v255 offset:22016\n" \
  "ds_read_b64_tr_b16 a[136:137], v255 offset:22528\n" \
  "ds_read_b64_tr_b16 a[156:157], v255 offset:23040\n" \
  "ds_read_b64_tr_b16 a[138:139], v255 offset:23552\n" \
  "ds_read_b64_tr_b16 a[158:159], v255 offset:24064\n" \
  "ds_read_b64_tr_b16 a[176:177], v255 offset:24576\n" \
  "ds_read_b64_tr_b16 a[196:197], v255 offset:25088\n" \
  "ds_read_b64_tr_b16 a[178:179], v255 offset:25600\n" \
  "ds_read_b64_tr_b16 a[198:199], v255 offset:26112\n" \
  "ds_read_b64_tr_b16 a[216:217], v255 offset:26624\n" \
  "ds_read_b64_tr_b16 a[236:237], v255 offset:27136\n" \
  "ds_read_b64_tr_b16 a[218:219], v255 offset:27648\n" \
  "ds_read_b64_tr_b16 a[238:239], v255 offset:28160\n" \
  "ds_read_b64_tr_b16 v[192:193], v255 offset:28672\n" \
  "ds_read_b64_tr_b16 v[212:213], v255 offset:29184\n" \
  "ds_read_b64_tr_b16 v[194:195], v255 offset:29696\n" \
  "ds_read_b64_tr_b16 v[214:215], v255 offset:30208\n" \
  "ds_read_b64_tr_b16 v[232:233], v189 offset:0\n" \
  "ds_read_b64_tr_b16 v[252:253], v189 offset:512\n" \
  "ds_read_b64_tr_b16 v[234:235], v189 offset:1024\n" \
  "ds_read_b64_tr_b16 v[254:255], v189 offset:1536\n" \
  "v_and_b32 v189, 12, v191\n" \
  "v_and_b32 v190, 1, v191\n" \
  "v_lshl_add_u32 v189, v190, 1, v189\n" \
  "v_lshlrev_b32 v189, 2, v189\n" \
  "v_add_u32 v189, 163712, v189\n" \
  "ds_read_b64 v[6:7], v189 offset:0\n" \
  "s_bfe_u32 s32, s27, 131076\n" \
  "s_sub_u32 s100, s60, 131200\n" \
  "s_sub_u32 s101, s60, 196608\n" \
  "s_lshl_b32 s32, s32, 12\n" \
  "s_add_u32 s32, s32, 0\n" \
  "s_cmp_ge_u32 s32, 12288\n" \
  "s_cselect_b32 s32, 0, s32\n" \
  "s_bitcmp1_b32 s32, 12\n" \
  "s_cselect_b32 s33, s101, s100\n" \
  "s_lshl4_add_u32 s31, s32, s33\n" \
  "s_add_u32 s33, s32, s57\n" \
  "s_add_u32 m0, s33, 16384\n" \
  "s_waitcnt lgkmcnt(1)\n" \
  "buffer_load_dwordx4 v2, s[40:43], s31 offen offset:0 lds\n" \
  "buffer_load_dwordx4 v3, s[40:43], s31 offen offset:1024 lds\n" \
  "buffer_load_dwordx4 v4, s[40:43], s31 offen offset:2048 lds\n" \
  "buffer_load_dwordx4 v5, s[40:43], s31 offen offset:3072 lds\n" \
  "s_add_u32 s32, s32, 4096\n" \
  "s_cmp_ge_u32 s32, 12288\n" \
  "s_cselect_b32 s32, 0, s32\n" \
  "s_bitcmp1_b32 s32, 12\n" \
  "s_cselect_b32 s33, s101, s100\n" \
  "s_lshl4_add_u32 s31, s32, s33\n" \
  "s_add_u32 s33, s32, s57\n" \
  "s_add_u32 m0, s33, 16384\n" \
  "s_nop 0\n" \
  "buffer_load_dwordx4 v2, s[40:43], s31 offen offset:0 lds\n" \
  "buffer_load_dwordx4 v3, s[40:43], s31 offen offset:1024 lds\n" \
  "buffer_load_dwordx4 v4, s[40:43], s31 offen offset:2048 lds\n" \
  "buffer_load_dwordx4 v5, s[40:43], s31 offen offset:3072 lds\n" \
  "s_add_u32 s32, s32, 4096\n" \
  "s_cmp_ge_u32 s32, 12288\n" \
  "s_cselect_b32 s32, 0, s32\n" \
  "s_bitcmp1_b32 s32, 12\n" \
  "s_cselect_b32 s33, s101, s100\n" \
  "s_lshl4_add_u32 s31, s32, s33\n" \
  "s_add_u32 s33, s32, s57\n" \
  "s_add_u32 m0, s33, 16384\n" \
  "s_nop 0\n" \
  "buffer_load_dwordx4 v2, s[40:43], s31 offen offset:0 lds\n" \
  "buffer_load_dwordx4 v3, s[40:43], s31 offen offset:1024 lds\n" \
  "buffer_load_dwordx4 v4, s[40:43], s31 offen offset:2048 lds\n" \
  "buffer_load_dwordx4 v5, s[40:43], s31 offen offset:3072 lds\n" \
  "s_waitcnt lgkmcnt(0)\n" \
  "v_lshrrev_b32 v6, 16, v6\n" \
  "v_or_b32 v15, v6, v7\n" \
  ".Lgo_%=:\n"
#define ASM_END \
  ".Ladm_b:\n"
#define ASM_CLOB "v1", "v2", "v3", "v4", "v5", "v6", "v7", "v8", "v9", "v10", "v11", "v12", "v13", "v14", "v15", "v16", "v17", "v18", "v19", "v20", "v21", "v22", "v23", "v24", "v25", "v26", "v27", "v28", "v29", "v30", "v31", "v32", "v33", "v34", "v35", "v36", "v37", "v38", "v39", "v40", "v41", "v42", "v43", "v44", "v45", "v46", "v47", "v48", "v49", "v50", "v51", "v52", "v53", "v54", "v55", "v56", "v57", "v58", "v59", "v60", "v61", "v62", "v63", "v64", "v65", "v66", "v67", "v68", "v69", "v70", "v71", "v72", "v73", "v74", "v75", "v76", "v77", "v78", "v79", "v80", "v81", "v82", "v83", "v84", "v85", "v86", "v87", "v88", "v89", "v90", "v91", "v92", "v93", "v94", "v95", "v96", "v97", "v98", "v99", "v100", "v101", "v102", "v103", "v104", "v105", "v106", "v107", "v108", "v109", "v110", "v111", "v112", "v113", "v114", "v115", "v116", "v117", "v118", "v119", "v120", "v121", "v122", "v123", "v124", "v125", "v126", "v127", "v128", "v129", "v130", "v131", "v132", "v133", "v134", "v135", "v136", "v137", "v138", "v139", "v140", "v141", "v142", "v143", "v144", "v145", "v146", "v147", "v148", "v149", "v150", "v151", "v152", "v153", "v154", "v155", "v156", "v157", "v158", "v159", "v160", "v161", "v162", "v163", "v164", "v165", "v166", "v167", "v168", "v169", "v170", "v171", "v172", "v173", "v174", "v175", "v176", "v177", "v178", "v179", "v180", "v181", "v182", "v183", "v184", "v185", "v186", "v187", "v188", "v189", "v190", "v191", "v192", "v193", "v194", "v195", "v196", "v197", "v198", "v199", "v200", "v201", "v202", "v203", "v204", "v205", "v206", "v207", "v208", "v209", "v210", "v211", "v212", "v213", "v214", "v215", "v216", "v217", "v218", "v219", "v220", "v221", "v222", "v223", "v224", "v225", "v226", "v227", "v228", "v229", "v230", "v231", "v232", "v233", "v234", "v235", "v236", "v237", "v238", "v239", "v240", "v241", "v242", "v243", "v244", "v245", "v246", "v247", "v248", "v249", "v250", "v251", "v252", "v253", "v254", "v255", "a0", "a1", "a2", "a3", "a4", "a5", "a6", "a7", "a8", "a9", "a10", "a11", "a12", "a13", "a14", "a15", "a16", "a17", "a18", "a19", "a20", "a21", "a22", "a23", "a24", "a25", "a26", "a27", "a28", "a29", "a30", "a31", "a32", "a33", "a34", "a35", "a36", "a37", "a38", "a39", "a40", "a41", "a42", "a43", "a44", "a45", "a46", "a47", "a48", "a49", "a50", "a51", "a52", "a53", "a54", "a55", "a56", "a57", "a58", "a59", "a60", "a61", "a62", "a63", "a64", "a65", "a66", "a67", "a68", "a69", "a70", "a71", "a72", "a73", "a74", "a75", "a76", "a77", "a78", "a79", "a80", "a81", "a82", "a83", "a84", "a85", "a86", "a87", "a88", "a89", "a90", "a91", "a92", "a93", "a94", "a95", "a96", "a97", "a98", "a99", "a100", "a101", "a102", "a103", "a104", "a105", "a106", "a107", "a108", "a109", "a110", "a111", "a112", "a113", "a114", "a115", "a116", "a117", "a118", "a119", "a120", "a121", "a122", "a123", "a124", "a125", "a126", "a127", "a128", "a129", "a130", "a131", "a132", "a133", "a134", "a135", "a136", "a137", "a138", "a139", "a140", "a141", "a142", "a143", "a144", "a145", "a146", "a147", "a148", "a149", "a150", "a151", "a152", "a153", "a154", "a155", "a156", "a157", "a158", "a159", "a160", "a161", "a162", "a163", "a164", "a165", "a166", "a167", "a168", "a169", "a170", "a171", "a172", "a173", "a174", "a175", "a176", "a177", "a178", "a179", "a180", "a181", "a182", "a183", "a184", "a185", "a186", "a187", "a188", "a189", "a190", "a191", "a192", "a193", "a194", "a195", "a196", "a197", "a198", "a199", "a200", "a201", "a202", "a203", "a204", "a205", "a206", "a207", "a208", "a209", "a210", "a211", "a212", "a213", "a214", "a215", "a216", "a217", "a218", "a219", "a220", "a221", "a222", "a223", "a224", "a225", "a226", "a227", "a228", "a229", "a230", "a231", "a232", "a233", "a234", "a235", "a236", "a237", "a238", "a239", "a240", "a241", "a242", "a243", "a244", "a245", "a246", "a247", "a248", "a249", "a250", "a251", "a252", "a253", "a254", "a255", "s3", "s4", "s5", "s6", "s7", "s8", "s9", "s10", "s11", "s12", "s13", "s14", "s15", "s16", "s17", "s18", "s19", "s20", "s21", "s22", "s23", "s24", "s25", "s26", "s27", "s28", "s29", "s30", "s31", "s32", "s33", "s34", "s35", "s36", "s37", "s38", "s39", "s40", "s41", "s42", "s43", "s44", "s45", "s46", "s47", "s48", "s49", "s50", "s51", "s52", "s53", "s54", "s55", "s56", "s57", "s58", "s59", "s60", "s61", "s62", "s63", "s64", "s65", "s66", "s67", "s68", "s69", "s70", "s71", "s72", "s73", "s74", "s75", "s78", "s79", "s80", "s81", "s82", "s83", "s84", "s85", "s86", "s87", "s88", "s89", "s90", "s91", "s92", "s93", "s94", "s95", "s96", "s97", "s98", "s99", "s100", "s101", "vcc", "scc", "memory"
#define ASM_GEMM \
  "s_memrealtime s[94:95]\n" \
  "s_waitcnt vmcnt(12)\n" \
  "v_xor_b32 v17, 32, v63\n" \
  "v_xor_b32 v18, 64, v63\n" \
  "v_xor_b32 v19, 96, v63\n" \
  "ds_read_b64_tr_b16 v[128:129], v63 offset:0\n" \
  "ds_read_b64_tr_b16 v[130:131], v63 offset:512\n" \
  "ds_read_b64_tr_b16 v[132:133], v17 offset:0\n" \
  "ds_read_b64_tr_b16 v[134:135], v17 offset:512\n" \
  "ds_read_b64_tr_b16 v[136:137], v18 offset:0\n" \
  "ds_read_b64_tr_b16 v[138:139], v18 offset:512\n" \
  "ds_read_b64_tr_b16 v[140:141], v19 offset:0\n" \
  "ds_read_b64_tr_b16 v[142:143], v19 offset:512\n" \
  "ds_read_b64_tr_b16 v[144:145], v63 offset:4096\n" \
  "ds_read_b64_tr_b16 v[146:147], v63 offset:4608\n" \
  "ds_read_b64_tr_b16 v[148:149], v17 offset:4096\n" \
  "ds_read_b64_tr_b16 v[150:151], v17 offset:4608\n" \
  "ds_read_b64_tr_b16 v[152:153], v18 offset:4096\n" \
  "ds_read_b64_tr_b16 v[154:155], v18 offset:4608\n" \
  "ds_read_b64_tr_b16 v[156:157], v19 offset:4096\n" \
  "ds_read_b64_tr_b16 v[158:159], v19 offset:4608\n" \
  "ds_read_b64_tr_b16 v[160:161], v63 offset:8192\n" \
  "ds_read_b64_tr_b16 v[162:163], v63 offset:8704\n" \
  "ds_read_b64_tr_b16 v[164:165], v17 offset:8192\n" \
  "ds_read_b64_tr_b16 v[166:167], v17 offset:8704\n" \
  "ds_read_b64_tr_b16 v[168:169], v18 offset:8192\n" \
  "ds_read_b64_tr_b16 v[170:171], v18 offset:8704\n" \
  "ds_read_b64_tr_b16 v[172:173], v19 offset:8192\n" \
  "ds_read_b64_tr_b16 v[174:175], v19 offset:8704\n" \
  "ds_read_b64_tr_b16 v[176:177], v63 offset:12288\n" \
  "ds_read_b64_tr_b16 v[178:179], v63 offset:12800\n" \
  "ds_read_b64_tr_b16 v[180:181], v17 offset:12288\n" \
  "ds_read_b64_tr_b16 v[182:183], v17 offset:12800\n" \
  "ds_read_b64_tr_b16 v[184:185], v18 offset:12288\n" \
  "ds_read_b64_tr_b16 v[186:187], v18 offset:12800\n" \
  "ds_read_b64_tr_b16 v[188:189], v19 offset:12288\n" \
  "ds_read_b64_tr_b16 v[190:191], v19 offset:12800\n" \
  "buffer_load_dwordx4 v[64:67], v59, s[36:39], s48 offen offset:0 sc1\n" \
  "buffer_load_dwordx4 v[68:71], v59, s[36:39], s48 offen offset:1024 sc1\n" \
  "buffer_load_dwordx4 v[72:75], v59, s[36:39], s48 offen offset:2048 sc1\n" \
  "buffer_load_dwordx4 v[76:79], v59, s[36:39], s48 offen offset:3072 sc1\n" \
  "buffer_load_dwordx4 v[80:83], v59, s[36:39], s49 offen offset:0 sc1\n" \
  "buffer_load_dwordx4 v[84:87], v59, s[36:39], s49 offen offset:1024 sc1\n" \
  "buffer_load_dwordx4 v[88:91], v59, s[36:39], s49 offen offset:2048 sc1\n" \
  "buffer_load_dwordx4 v[92:95], v59, s[36:39], s49 offen offset:3072 sc1\n" \
  "buffer_load_dwordx4 v[96:99], v59, s[36:39], s50 offen offset:0 sc1\n" \
  "buffer_load_dwordx4 v[100:103], v59, s[36:39], s50 offen offset:1024 sc1\n" \
  "buffer_load_dwordx4 v[104:107], v59, s[36:39], s50 offen offset:2048 sc1\n" \
  "buffer_load_dwordx4 v[108:111], v59, s[36:39], s50 offen offset:3072 sc1\n" \
  "buffer_load_dwordx4 v[112:115], v59, s[36:39], s51 offen offset:0 sc1\n" \
  "buffer_load_dwordx4 v[116:119], v59, s[36:39], s51 offen offset:1024 sc1\n" \
  "buffer_load_dwordx4 v[120:123], v59, s[36:39], s51 offen offset:2048 sc1\n" \
  "buffer_load_dwordx4 v[124:127], v59, s[36:39], s51 offen offset:3072 sc1\n" \
  "v_and_b32 v0, 0x3ff, v59\n" \
  "v_bfrev_b32 v1, 1\n" \
  "buffer_load_dwordx4 v[4:7], v0, s[36:39], s87 offen sc1\n" \
  "s_branch .Lz0_%=\n" \
  ".Lzr_%=:\n" \
  "s_waitcnt lgkmcnt(0)\n" \
  "ds_write_b128 v22, v[8:11]\n" \
  "ds_write_b128 v22, v[12:15] offset:1024\n" \
  "s_mov_b32 s101, s89\n" \
  "s_waitcnt lgkmcnt(0)\n" \
  "s_barrier\n" \
  "s_sub_u32 s31, s60, 0x60080\n" \
  "s_sub_u32 s32, s60, 0x70000\n" \
  "v_add_u32 v14, 0xf800, v12\n" \
  "v_add_u32 v15, 0xf800, v13\n" \
  "s_add_u32 s88, s88, 4096\n" \
  "s_cmp_eq_u32 s88, 16384\n" \
  "s_cselect_b32 s88, 0, s88\n" \
  "s_bitcmp1_b32 s88, 12\n" \
  "s_cselect_b32 s89, s32, s31\n" \
  "s_add_u32 m0, s88, s57\n" \
  "s_lshl4_add_u32 s33, s88, s89\n" \
  "buffer_load_dwordx4 v12, s[40:43], s33 offen offset:0 lds\n" \
  "buffer_load_dwordx4 v13, s[40:43], s33 offen offset:1024 lds\n" \
  "buffer_load_dwordx4 v14, s[40:43], s33 offen offset:2048 lds\n" \
  "buffer_load_dwordx4 v15, s[40:43], s33 offen offset:3072 lds\n" \
  "s_add_u32 s88, s88, 4096\n" \
  "s_cmp_eq_u32 s88, 16384\n" \
  "s_cselect_b32 s88, 0, s88\n" \
  "s_bitcmp1_b32 s88, 12\n" \
  "s_cselect_b32 s89, s32, s31\n" \
  "s_add_u32 m0, s88, s57\n" \
  "s_lshl4_add_u32 s33, s88, s89\n" \
  "buffer_load_dwordx4 v12, s[40:43], s33 offen offset:0 lds\n" \
  "buffer_load_dwordx4 v13, s[40:43], s33 offen offset:1024 lds\n" \
  "buffer_load_dwordx4 v14, s[40:43], s33 offen offset:2048 lds\n" \
  "buffer_load_dwordx4 v15, s[40:43], s33 offen offset:3072 lds\n" \
  "s_add_u32 s88, s88, 4096\n" \
  "s_cmp_eq_u32 s88, 16384\n" \
  "s_cselect_b32 s88, 0, s88\n" \
  "s_bitcmp1_b32 s88, 12\n" \
  "s_cselect_b32 s89, s32, s31\n" \
  "s_add_u32 m0, s88, s57\n" \
  "s_lshl4_add_u32 s33, s88, s89\n" \
  "buffer_load_dwordx4 v12, s[40:43], s33 offen offset:0 lds\n" \
  "buffer_load_dwordx4 v13, s[40:43], s33 offen offset:1024 lds\n" \
  "buffer_load_dwordx4 v14, s[40:43], s33 offen offset:2048 lds\n" \
  "buffer_load_dwordx4 v15, s[40:43], s33 offen offset:3072 lds\n" \
  "s_add_u32 s88, s88, 4096\n" \
  "s_cmp_eq_u32 s88, 16384\n" \
  "s_cselect_b32 s88, 0, s88\n" \
  "s_bitcmp1_b32 s88, 12\n" \
  "s_cselect_b32 s89, s32, s31\n" \
  "s_add_u32 m0, s88, s57\n" \
  "s_lshl4_add_u32 s33, s88, s89\n" \
  "buffer_load_dwordx4 v12, s[40:43], s33 offen offset:0 lds\n" \
  "buffer_load_dwordx4 v13, s[40:43], s33 offen offset:1024 lds\n" \
  "buffer_load_dwordx4 v14, s[40:43], s33 offen offset:2048 lds\n" \
  "buffer_load_dwordx4 v15, s[40:43], s33 offen offset:3072 lds\n" \
  "s_mov_b32 s89, s101\n" \
  "s_waitcnt vmcnt(16)\n" \
  "s_branch .Li_%=\n" \
  ".Lh_%=:\n" \
  "buffer_load_dwordx4 v[4:7], v0, s[36:39], s87 offen sc1\n" \
  "s_waitcnt vmcnt(0)\n" \
  ".Li_%=:\n" \
  "s_memrealtime s[96:97]\n" \
  "s_waitcnt lgkmcnt(0)\n" \
  "v_bitop3_b32 v4, v4, v5, v6 bitop3:0x80\n" \
  "v_and_b32 v4, v4, v7\n" \
  "v_cmp_ne_u32 vcc, 0, v4\n" \
  "v_max3_u32 v2, v64, v68, v72\n" \
  "v_max3_u32 v2, v2, v76, v80\n" \
  "v_max3_u32 v2, v2, v84, v88\n" \
  "v_max3_u32 v2, v2, v92, v96\n" \
  "v_max3_u32 v2, v2, v100, v104\n" \
  "v_max3_u32 v2, v2, v108, v112\n" \
  "v_max3_u32 v2, v2, v116, v120\n" \
  "v_max_u32 v2, v2, v124\n" \
  "v_cndmask_b32 v0, v0, v1, vcc\n" \
  "v_cmp_eq_u32 vcc, -1, v2\n" \
  "v_cmp_ne_u32 s[100:101], v0, v1\n" \
  "s_sub_u32 s96, s96, s94\n" \
  "s_cbranch_vccz .Lj_%=\n" \
  "v_cmpx_eq_u32 vcc, -1, v64\n" \
  "buffer_load_dwordx4 v[64:67], v59, s[36:39], s48 offen offset:0 sc1\n" \
  "s_mov_b64 exec, -1\n" \
  "v_cmpx_eq_u32 vcc, -1, v68\n" \
  "buffer_load_dwordx4 v[68:71], v59, s[36:39], s48 offen offset:1024 sc1\n" \
  "s_mov_b64 exec, -1\n" \
  "v_cmpx_eq_u32 vcc, -1, v72\n" \
  "buffer_load_dwordx4 v[72:75], v59, s[36:39], s48 offen offset:2048 sc1\n" \
  "s_mov_b64 exec, -1\n" \
  "v_cmpx_eq_u32 vcc, -1, v76\n" \
  "buffer_load_dwordx4 v[76:79], v59, s[36:39], s48 offen offset:3072 sc1\n" \
  "s_mov_b64 exec, -1\n" \
  "v_cmpx_eq_u32 vcc, -1, v80\n" \
  "buffer_load_dwordx4 v[80:83], v59, s[36:39], s49 offen offset:0 sc1\n" \
  "s_mov_b64 exec, -1\n" \
  "v_cmpx_eq_u32 vcc, -1, v84\n" \
  "buffer_load_dwordx4 v[84:87], v59, s[36:39], s49 offen offset:1024 sc1\n" \
  "s_mov_b64 exec, -1\n" \
  "v_cmpx_eq_u32 vcc, -1, v88\n" \
  "buffer_load_dwordx4 v[88:91], v59, s[36:39], s49 offen offset:2048 sc1\n" \
  "s_mov_b64 exec, -1\n" \
  "v_cmpx_eq_u32 vcc, -1, v92\n" \
  "buffer_load_dwordx4 v[92:95], v59, s[36:39], s49 offen offset:3072 sc1\n" \
  "s_mov_b64 exec, -1\n" \
  "v_cmpx_eq_u32 vcc, -1, v96\n" \
  "buffer_load_dwordx4 v[96:99], v59, s[36:39], s50 offen offset:0 sc1\n" \
  "s_mov_b64 exec, -1\n" \
  "v_cmpx_eq_u32 vcc, -1, v100\n" \
  "buffer_load_dwordx4 v[100:103], v59, s[36:39], s50 offen offset:1024 sc1\n" \
  "s_mov_b64 exec, -1\n" \
  "v_cmpx_eq_u32 vcc, -1, v104\n" \
  "buffer_load_dwordx4 v[104:107], v59, s[36:39], s50 offen offset:2048 sc1\n" \
  "s_mov_b64 exec, -1\n" \
  "v_cmpx_eq_u32 vcc, -1, v108\n" \
  "buffer_load_dwordx4 v[108:111], v59, s[36:39], s50 offen offset:3072 sc1\n" \
  "s_mov_b64 exec, -1\n" \
  "v_cmpx_eq_u32 vcc, -1, v112\n" \
  "buffer_load_dwordx4 v[112:115], v59, s[36:39], s51 offen offset:0 sc1\n" \
  "s_mov_b64 exec, -1\n" \
  "v_cmpx_eq_u32 vcc, -1, v116\n" \
  "buffer_load_dwordx4 v[116:119], v59, s[36:39], s51 offen offset:1024 sc1\n" \
  "s_mov_b64 exec, -1\n" \
  "v_cmpx_eq_u32 vcc, -1, v120\n" \
  "buffer_load_dwordx4 v[120:123], v59, s[36:39], s51 offen offset:2048 sc1\n" \
  "s_mov_b64 exec, -1\n" \
  "v_cmpx_eq_u32 vcc, -1, v124\n" \
  "buffer_load_dwordx4 v[124:127], v59, s[36:39], s51 offen offset:3072 sc1\n" \
  "s_mov_b64 exec, -1\n" \
  "s_or_b64 s[100:101], s[100:101], 1\n" \
  ".Lj_%=:\n" \
  "s_cmp_gt_u32 s96, 0x4c4b40\n" \
  "s_cbranch_scc1 .Lg_%=\n" \
  "s_cmp_lg_u64 s[100:101], 0\n" \
  "s_cbranch_scc1 .Lh_%=\n" \
  ".Lg_%=:\n" \
  "s_sub_u32 s31, s60, 0x60080\n" \
  "s_sub_u32 s32, s60, 0x70000\n" \
  "s_mov_b32 m0, s56\n" \
  "s_mov_b32 s94, s86\n" \
  "s_and_b32 s95, s89, 7\n" \
  "s_andn2_b32 s101, s89, 7\n" \
  "s_mul_i32 s95, s95, 409600\n" \
  "s_add_u32 s95, s95, s52\n" \
  "s_mov_b32 s86, 0x11111111\n" \
  "s_mov_b32 s87, 0x11111111\n" \
  "v_add_u32 v59, s85, v59\n" \
  "s_add_u32 s35, s52, 1638400\n" \
  "s_mov_b32 s34, s91\n" \
  "s_waitcnt lgkmcnt(0)\n" \
  "s_cmp_eq_u32 s84, 0\n" \
  "s_cbranch_scc0 .Ls_%=\n" \
  ".Lf_%=:\n" \
  "s_waitcnt vmcnt(16)\n" \
  "v_mfma_f32_16x16x32_bf16 v[0:3], v[64:67], a[0:3], 0\n" \
  "ds_read_b32 v44, v60 offset:0\n" \
  "v_add_f32 v42, 1.0, v42\n" \
  "v_mfma_f32_16x16x32_bf16 v[4:7], v[64:67], a[4:7], 0\n" \
  "ds_read_b32 v45, v60 offset:9568\n" \
  "v_add_f32 v43, 1.0, v43\n" \
  "v_mfma_f32_16x16x32_bf16 v[8:11], v[64:67], a[8:11], 0\n" \
  "ds_read_b32 v46, v60 offset:19136\n" \
  "v_add_u32 v59, 0x10000, v59\n" \
  "v_mfma_f32_16x16x32_bf16 v[12:15], v[64:67], a[12:15], 0\n" \
  "ds_read_b32 v47, v60 offset:28704\n" \
  "v_and_b32 v59, 0x7ffff, v59\n" \
  "v_mfma_f32_16x16x32_bf16 v[16:19], v[64:67], a[16:19], 0\n" \
  "buffer_load_dwordx4 v[64:67], v59, s[36:39], s48 offen offset:0 sc1\n" \
  "s_waitcnt vmcnt(16)\n" \
  "v_mfma_f32_16x16x32_bf16 v[0:3], v[68:71], a[20:23], v[0:3]\n" \
  "v_rcp_f32 v40, v40\n" \
  "v_mfma_f32_16x16x32_bf16 v[4:7], v[68:71], a[24:27], v[4:7]\n" \
  "v_rcp_f32 v41, v41\n" \
  "v_mfma_f32_16x16x32_bf16 v[8:11], v[68:71], a[28:31], v[8:11]\n" \
  "v_rcp_f32 v42, v42\n" \
  "v_mfma_f32_16x16x32_bf16 v[12:15], v[68:71], a[32:35], v[12:15]\n" \
  "v_rcp_f32 v43, v43\n" \
  "v_mfma_f32_16x16x32_bf16 v[16:19], v[68:71], a[36:39], v[16:19]\n" \
  "buffer_load_dwordx4 v[68:71], v59, s[36:39], s48 offen offset:1024 sc1\n" \
  "s_waitcnt vmcnt(15)\n" \
  "v_mfma_f32_16x16x32_bf16 v[0:3], v[72:75], a[40:43], v[0:3]\n" \
  "v_mul_f32 v36, v36, v40\n" \
  "v_mul_f32 v37, v37, v41\n" \
  "v_mfma_f32_16x16x32_bf16 v[4:7], v[72:75], a[44:47], v[4:7]\n" \
  "v_mul_f32 v38, v38, v42\n" \
  "v_mul_f32 v39, v39, v43\n" \
  "v_mfma_f32_16x16x32_bf16 v[8:11], v[72:75], a[48:51], v[8:11]\n" \
  "v_cvt_pk_bf16_f32 v40, v36, v37\n" \
  "v_cvt_pk_bf16_f32 v41, v38, v39\n" \
  "v_mfma_f32_16x16x32_bf16 v[12:15], v[72:75], a[52:55], v[12:15]\n" \
  "buffer_store_dwordx2 v[40:41], v61, s[36:39], s52 offen offset:0 sc1\n" \
  "ds_write_addtid_b32 v20 offset:5344\n" \
  "v_mfma_f32_16x16x32_bf16 v[16:19], v[72:75], a[56:59], v[16:19]\n" \
  "buffer_load_dwordx4 v[72:75], v59, s[36:39], s48 offen offset:2048 sc1\n" \
  "s_waitcnt vmcnt(16)\n" \
  "v_mfma_f32_16x16x32_bf16 v[0:3], v[76:79], a[60:63], v[0:3]\n" \
  "ds_write_addtid_b32 v21 offset:5888\n" \
  "s_waitcnt lgkmcnt(2)\n" \
  "v_mfma_f32_16x16x32_bf16 v[4:7], v[76:79], a[64:67], v[4:7]\n" \
  "ds_write_addtid_b32 v22 offset:5600\n" \
  "v_add_f32 v44, v44, v45\n" \
  "v_mfma_f32_16x16x32_bf16 v[8:11], v[76:79], a[68:71], v[8:11]\n" \
  "ds_write_addtid_b32 v23 offset:6144\n" \
  "v_add_f32 v46, v46, v47\n" \
  "v_mfma_f32_16x16x32_bf16 v[12:15], v[76:79], a[72:75], v[12:15]\n" \
  "ds_write_addtid_b32 v24 offset:6400\n" \
  "v_add_f32 v44, v44, v56\n" \
  "v_mfma_f32_16x16x32_bf16 v[16:19], v[76:79], a[76:79], v[16:19]\n" \
  "buffer_load_dwordx4 v[76:79], v59, s[36:39], s48 offen offset:3072 sc1\n" \
  "s_waitcnt vmcnt(16)\n" \
  "v_mfma_f32_16x16x32_bf16 v[0:3], v[80:83], a[80:83], v[0:3]\n" \
  "ds_write_addtid_b32 v25 offset:6944\n" \
  "v_add_f32 v44, v44, v46\n" \
  "v_mfma_f32_16x16x32_bf16 v[4:7], v[80:83], a[84:87], v[4:7]\n" \
  "ds_write_addtid_b32 v26 offset:6656\n" \
  "v_mul_f32 v45, v44, v44\n" \
  "v_mfma_f32_16x16x32_bf16 v[8:11], v[80:83], a[88:91], v[8:11]\n" \
  "ds_write_addtid_b32 v27 offset:7200\n" \
  "v_fma_f32 v45, v45, s55, v57\n" \
  "v_mfma_f32_16x16x32_bf16 v[12:15], v[80:83], a[92:95], v[12:15]\n" \
  "ds_write_addtid_b32 v28 offset:7456\n" \
  "v_mul_f32 v45, v44, v45\n" \
  "v_mfma_f32_16x16x32_bf16 v[16:19], v[80:83], a[96:99], v[16:19]\n" \
  "buffer_load_dwordx4 v[80:83], v59, s[36:39], s49 offen offset:0 sc1\n" \
  "s_waitcnt vmcnt(16)\n" \
  "v_mfma_f32_16x16x32_bf16 v[0:3], v[84:87], a[100:103], v[0:3]\n" \
  "ds_write_addtid_b32 v29 offset:8000\n" \
  "v_mfma_f32_16x16x32_bf16 v[4:7], v[84:87], a[104:107], v[4:7]\n" \
  "ds_write_addtid_b32 v30 offset:7712\n" \
  "v_mfma_f32_16x16x32_bf16 v[8:11], v[84:87], a[108:111], v[8:11]\n" \
  "ds_write_addtid_b32 v31 offset:8256\n" \
  "v_mfma_f32_16x16x32_bf16 v[12:15], v[84:87], a[112:115], v[12:15]\n" \
  "ds_write_addtid_b32 v32 offset:8512\n" \
  "v_mfma_f32_16x16x32_bf16 v[16:19], v[84:87], a[116:119], v[16:19]\n" \
  "buffer_load_dwordx4 v[84:87], v59, s[36:39], s49 offen offset:1024 sc1\n" \
  "s_waitcnt vmcnt(16)\n" \
  "v_mfma_f32_16x16x32_bf16 v[0:3], v[88:91], a[120:123], v[0:3]\n" \
  "ds_write_addtid_b32 v33 offset:9056\n" \
  "v_mfma_f32_16x16x32_bf16 v[4:7], v[88:91], a[124:127], v[4:7]\n" \
  "ds_write_addtid_b32 v34 offset:8768\n" \
  "v_mfma_f32_16x16x32_bf16 v[8:11], v[88:91], a[128:131], v[8:11]\n" \
  "ds_write_addtid_b32 v35 offset:9312\n" \
  "v_mfma_f32_16x16x32_bf16 v[12:15], v[88:91], a[132:135], v[12:15]\n" \
  "v_exp_f32 v45, v45\n" \
  "v_mfma_f32_16x16x32_bf16 v[16:19], v[88:91], a[136:139], v[16:19]\n" \
  "buffer_load_dwordx4 v[88:91], v59, s[36:39], s49 offen offset:2048 sc1\n" \
  "s_waitcnt vmcnt(16)\n" \
  "v_mfma_f32_16x16x32_bf16 v[0:3], v[92:95], a[140:143], v[0:3]\n" \
  "v_add_f32 v45, 1.0, v45\n" \
  "v_mfma_f32_16x16x32_bf16 v[4:7], v[92:95], a[144:147], v[4:7]\n" \
  "v_rcp_f32 v45, v45\n" \
  "v_mfma_f32_16x16x32_bf16 v[8:11], v[92:95], a[148:151], v[8:11]\n" \
  "v_mul_f32 v44, v44, v45\n" \
  "v_cvt_pk_bf16_f32 v45, v44, v44\n" \
  "v_mfma_f32_16x16x32_bf16 v[12:15], v[92:95], a[152:155], v[12:15]\n" \
  "s_waitcnt lgkmcnt(0)\n" \
  "s_barrier\n" \
  "v_mfma_f32_16x16x32_bf16 v[16:19], v[92:95], a[156:159], v[16:19]\n" \
  "buffer_load_dwordx4 v[92:95], v59, s[36:39], s49 offen offset:3072 sc1\n" \
  "s_waitcnt vmcnt(16)\n" \
  "v_mfma_f32_16x16x32_bf16 v[0:3], v[96:99], a[160:163], v[0:3]\n" \
  "buffer_store_short v45, v62, s[36:39], s52 offen offset:0 sc1\n" \
  "ds_read_b128 v[36:39], v58 offset:5344\n" \
  "v_mfma_f32_16x16x32_bf16 v[4:7], v[96:99], a[164:167], v[4:7]\n" \
  "ds_read_b128 v[40:43], v58 offset:14912\n" \
  "v_mfma_f32_16x16x32_bf16 v[8:11], v[96:99], a[168:171], v[8:11]\n" \
  "ds_read_b128 v[44:47], v58 offset:24480\n" \
  "v_mfma_f32_16x16x32_bf16 v[12:15], v[96:99], a[172:175], v[12:15]\n" \
  "ds_read_b128 v[48:51], v58 offset:34048\n" \
  "v_mfma_f32_16x16x32_bf16 v[16:19], v[96:99], a[176:179], v[16:19]\n" \
  "buffer_load_dwordx4 v[96:99], v59, s[36:39], s50 offen offset:0 sc1\n" \
  "s_waitcnt vmcnt(17)\n" \
  "v_mfma_f32_16x16x32_bf16 v[0:3], v[100:103], a[180:183], v[0:3]\n" \
  "s_cmp_eq_u32 s52, s83\n" \
  "s_cbranch_scc1 .Lxq5_%=\n" \
  ".Lyq5_%=:\n" \
  "v_mfma_f32_16x16x32_bf16 v[4:7], v[100:103], a[184:187], v[4:7]\n" \
  "v_mfma_f32_16x16x32_bf16 v[8:11], v[100:103], a[188:191], v[8:11]\n" \
  "v_mfma_f32_16x16x32_bf16 v[12:15], v[100:103], a[192:195], v[12:15]\n" \
  "v_mfma_f32_16x16x32_bf16 v[16:19], v[100:103], a[196:199], v[16:19]\n" \
  "buffer_load_dwordx4 v[100:103], v59, s[36:39], s50 offen offset:1024 sc1\n" \
  "s_waitcnt vmcnt(17)\n" \
  "v_mfma_f32_16x16x32_bf16 v[0:3], v[104:107], a[200:203], v[0:3]\n" \
  "s_waitcnt lgkmcnt(2)\n" \
  "v_add_f32 v36, v36, v40\n" \
  "v_mfma_f32_16x16x32_bf16 v[4:7], v[104:107], a[204:207], v[4:7]\n" \
  "v_add_f32 v37, v37, v41\n" \
  "v_add_f32 v38, v38, v42\n" \
  "v_mfma_f32_16x16x32_bf16 v[8:11], v[104:107], a[208:211], v[8:11]\n" \
  "v_add_f32 v39, v39, v43\n" \
  "v_add_f32 v36, v36, v52\n" \
  "v_mfma_f32_16x16x32_bf16 v[12:15], v[104:107], a[212:215], v[12:15]\n" \
  "v_add_f32 v37, v37, v53\n" \
  "v_add_f32 v38, v38, v54\n" \
  "v_mfma_f32_16x16x32_bf16 v[16:19], v[104:107], a[216:219], v[16:19]\n" \
  "buffer_load_dwordx4 v[104:107], v59, s[36:39], s50 offen offset:2048 sc1\n" \
  "s_waitcnt vmcnt(17)\n" \
  "v_mfma_f32_16x16x32_bf16 v[0:3], v[108:111], a[220:223], v[0:3]\n" \
  "v_add_f32 v39, v39, v55\n" \
  "s_waitcnt lgkmcnt(0)\n" \
  "v_mfma_f32_16x16x32_bf16 v[4:7], v[108:111], a[224:227], v[4:7]\n" \
  "v_add_f32 v44, v44, v48\n" \
  "v_add_f32 v45, v45, v49\n" \
  "v_mfma_f32_16x16x32_bf16 v[8:11], v[108:111], a[228:231], v[8:11]\n" \
  "v_add_f32 v46, v46, v50\n" \
  "v_add_f32 v47, v47, v51\n" \
  "v_mfma_f32_16x16x32_bf16 v[12:15], v[108:111], a[232:235], v[12:15]\n" \
  "v_add_f32 v36, v36, v44\n" \
  "v_add_f32 v37, v37, v45\n" \
  "v_mfma_f32_16x16x32_bf16 v[16:19], v[108:111], a[236:239], v[16:19]\n" \
  "buffer_load_dwordx4 v[108:111], v59, s[36:39], s50 offen offset:3072 sc1\n" \
  "s_waitcnt vmcnt(17)\n" \
  "v_mfma_f32_16x16x32_bf16 v[0:3], v[112:115], a[240:243], v[0:3]\n" \
  "v_add_f32 v38, v38, v46\n" \
  "v_add_f32 v39, v39, v47\n" \
  "v_mfma_f32_16x16x32_bf16 v[4:7], v[112:115], a[244:247], v[4:7]\n" \
  "v_mul_f32 v40, v36, v36\n" \
  "v_mul_f32 v41, v37, v37\n" \
  "v_mfma_f32_16x16x32_bf16 v[8:11], v[112:115], a[248:251], v[8:11]\n" \
  "v_mul_f32 v42, v38, v38\n" \
  "v_mul_f32 v43, v39, v39\n" \
  "v_mfma_f32_16x16x32_bf16 v[12:15], v[112:115], a[252:255], v[12:15]\n" \
  "v_fma_f32 v40, v40, s55, v57\n" \
  "v_fma_f32 v41, v41, s55, v57\n" \
  "v_mfma_f32_16x16x32_bf16 v[16:19], v[112:115], v[192:195], v[16:19]\n" \
  "buffer_load_dwordx4 v[112:115], v59, s[36:39], s51 offen offset:0 sc1\n" \
  "s_waitcnt vmcnt(17)\n" \
  "v_mfma_f32_16x16x32_bf16 v[0:3], v[116:119], v[196:199], v[0:3]\n" \
  "v_fma_f32 v42, v42, s55, v57\n" \
  "v_fma_f32 v43, v43, s55, v57\n" \
  "v_mfma_f32_16x16x32_bf16 v[4:7], v[116:119], v[200:203], v[4:7]\n" \
  "v_mul_f32 v40, v36, v40\n" \
  "v_mul_f32 v41, v37, v41\n" \
  "v_mfma_f32_16x16x32_bf16 v[8:11], v[116:119], v[204:207], v[8:11]\n" \
  "v_mul_f32 v42, v38, v42\n" \
  "v_mul_f32 v43, v39, v43\n" \
  "v_mfma_f32_16x16x32_bf16 v[12:15], v[116:119], v[208:211], v[12:15]\n" \
  "v_exp_f32 v40, v40\n" \
  "v_mfma_f32_16x16x32_bf16 v[16:19], v[116:119], v[212:215], v[16:19]\n" \
  "buffer_load_dwordx4 v[116:119], v59, s[36:39], s51 offen offset:1024 sc1\n" \
  "s_waitcnt vmcnt(17)\n" \
  "v_mfma_f32_16x16x32_bf16 v[0:3], v[120:123], v[216:219], v[0:3]\n" \
  "v_exp_f32 v41, v41\n" \
  "v_mfma_f32_16x16x32_bf16 v[4:7], v[120:123], v[220:223], v[4:7]\n" \
  "v_exp_f32 v42, v42\n" \
  "v_mfma_f32_16x16x32_bf16 v[8:11], v[120:123], v[224:227], v[8:11]\n" \
  "v_exp_f32 v43, v43\n" \
  "v_mfma_f32_16x16x32_bf16 v[12:15], v[120:123], v[228:231], v[12:15]\n" \
  "v_add_f32 v40, 1.0, v40\n" \
  "v_add_f32 v41, 1.0, v41\n" \
  "v_mfma_f32_16x16x32_bf16 v[16:19], v[120:123], v[232:235], v[16:19]\n" \
  "buffer_load_dwordx4 v[120:123], v59, s[36:39], s51 offen offset:2048 sc1\n" \
  "s_waitcnt vmcnt(17)\n" \
  "v_mfma_f32_16x16x32_bf16 v[0:3], v[124:127], v[236:239], v[0:3]\n" \
  "v_add_f32 v42, 1.0, v42\n" \
  "v_add_f32 v43, 1.0, v43\n" \
  "v_mfma_f32_16x16x32_bf16 v[4:7], v[124:127], v[240:243], v[4:7]\n" \
  "s_cmp_eq_u32 s52, s95\n" \
  "s_cbranch_scc1 .Lxt5_%=\n" \
  ".Lyt5_%=:\n" \
  "v_mfma_f32_16x16x32_bf16 v[8:11], v[124:127], v[244:247], v[8:11]\n" \
  "v_rcp_f32 v40, v40\n" \
  "v_mfma_f32_16x16x32_bf16 v[12:15], v[124:127], v[248:251], v[12:15]\n" \
  "v_rcp_f32 v41, v41\n" \
  "v_mfma_f32_16x16x32_bf16 v[16:19], v[124:127], v[252:255], v[16:19]\n" \
  "buffer_load_dwordx4 v[124:127], v59, s[36:39], s51 offen offset:3072 sc1\n" \
  "s_add_u32 s52, s52, 409600\n" \
  "s_cmp_eq_u32 s52, s53\n" \
  "s_cbranch_scc1 .Lls_%=\n" \
  ".Ls_%=:\n" \
  "s_waitcnt vmcnt(17)\n" \
  "v_mfma_f32_16x16x32_bf16 v[20:23], v[64:67], a[0:3], 0\n" \
  "v_rcp_f32 v42, v42\n" \
  "v_mfma_f32_16x16x32_bf16 v[24:27], v[64:67], a[4:7], 0\n" \
  "v_add_u32 v59, 0x10000, v59\n" \
  "ds_write_addtid_b32 v0 offset:0\n" \
  "v_mfma_f32_16x16x32_bf16 v[28:31], v[64:67], a[8:11], 0\n" \
  "v_and_b32 v59, 0x7ffff, v59\n" \
  "ds_write_addtid_b32 v1 offset:544\n" \
  "v_mfma_f32_16x16x32_bf16 v[32:35], v[64:67], a[12:15], 0\n" \
  "buffer_load_dwordx4 v[64:67], v59, s[36:39], s48 offen offset:0 sc1\n" \
  "s_waitcnt vmcnt(17)\n" \
  "v_mfma_f32_16x16x32_bf16 v[20:23], v[68:71], a[20:23], v[20:23]\n" \
  "v_rcp_f32 v43, v43\n" \
  "v_mfma_f32_16x16x32_bf16 v[24:27], v[68:71], a[24:27], v[24:27]\n" \
  "v_mul_f32 v36, v36, v40\n" \
  "v_mul_f32 v37, v37, v41\n" \
  "v_mfma_f32_16x16x32_bf16 v[28:31], v[68:71], a[28:31], v[28:31]\n" \
  "v_mul_f32 v38, v38, v42\n" \
  "v_mul_f32 v39, v39, v43\n" \
  "v_mfma_f32_16x16x32_bf16 v[32:35], v[68:71], a[32:35], v[32:35]\n" \
  "buffer_load_dwordx4 v[68:71], v59, s[36:39], s48 offen offset:1024 sc1\n" \
  "s_waitcnt vmcnt(16)\n" \
  "v_mfma_f32_16x16x32_bf16 v[20:23], v[72:75], a[40:43], v[20:23]\n" \
  "ds_write_addtid_b32 v2 offset:256\n" \
  "v_cvt_pk_bf16_f32 v40, v36, v37\n" \
  "v_mfma_f32_16x16x32_bf16 v[24:27], v[72:75], a[44:47], v[24:27]\n" \
  "ds_write_addtid_b32 v3 offset:800\n" \
  "v_cvt_pk_bf16_f32 v41, v38, v39\n" \
  "v_mfma_f32_16x16x32_bf16 v[28:31], v[72:75], a[48:51], v[28:31]\n" \
  "ds_write_addtid_b32 v4 offset:1056\n" \
  "buffer_store_dwordx2 v[40:41], v61, s[36:39], s52 offen offset:0 sc1\n" \
  "v_mfma_f32_16x16x32_bf16 v[32:35], v[72:75], a[52:55], v[32:35]\n" \
  "buffer_load_dwordx4 v[72:75], v59, s[36:39], s48 offen offset:2048 sc1\n" \
  "s_waitcnt vmcnt(17)\n" \
  "v_mfma_f32_16x16x32_bf16 v[20:23], v[76:79], a[60:63], v[20:23]\n" \
  "ds_write_addtid_b32 v5 offset:1600\n" \
  "v_mfma_f32_16x16x32_bf16 v[24:27], v[76:79], a[64:67], v[24:27]\n" \
  "ds_write_addtid_b32 v6 offset:1312\n" \
  "v_mfma_f32_16x16x32_bf16 v[28:31], v[76:79], a[68:71], v[28:31]\n" \
  "ds_write_addtid_b32 v7 offset:1856\n" \
  "v_mfma_f32_16x16x32_bf16 v[32:35], v[76:79], a[72:75], v[32:35]\n" \
  "buffer_load_dwordx4 v[76:79], v59, s[36:39], s48 offen offset:3072 sc1\n" \
  "s_waitcnt vmcnt(17)\n" \
  "v_mfma_f32_16x16x32_bf16 v[20:23], v[80:83], a[80:83], v[20:23]\n" \
  "ds_write_addtid_b32 v8 offset:2112\n" \
  "v_mfma_f32_16x16x32_bf16 v[24:27], v[80:83], a[84:87], v[24:27]\n" \
  "ds_write_addtid_b32 v9 offset:2656\n" \
  "v_mfma_f32_16x16x32_bf16 v[28:31], v[80:83], a[88:91], v[28:31]\n" \
  "ds_write_addtid_b32 v10 offset:2368\n" \
  "v_mfma_f32_16x16x32_bf16 v[32:35], v[80:83], a[92:95], v[32:35]\n" \
  "buffer_load_dwordx4 v[80:83], v59, s[36:39], s49 offen offset:0 sc1\n" \
  "s_waitcnt vmcnt(17)\n" \
  "v_mfma_f32_16x16x32_bf16 v[20:23], v[84:87], a[100:103], v[20:23]\n" \
  "ds_write_addtid_b32 v11 offset:2912\n" \
  "v_mfma_f32_16x16x32_bf16 v[24:27], v[84:87], a[104:107], v[24:27]\n" \
  "ds_write_addtid_b32 v12 offset:3168\n" \
  "v_mfma_f32_16x16x32_bf16 v[28:31], v[84:87], a[108:111], v[28:31]\n" \
  "ds_write_addtid_b32 v13 offset:3712\n" \
  "v_mfma_f32_16x16x32_bf16 v[32:35], v[84:87], a[112:115], v[32:35]\n" \
  "buffer_load_dwordx4 v[84:87], v59, s[36:39], s49 offen offset:1024 sc1\n" \
  "s_waitcnt vmcnt(17)\n" \
  "v_mfma_f32_16x16x32_bf16 v[20:23], v[88:91], a[120:123], v[20:23]\n" \
  "ds_write_addtid_b32 v14 offset:3424\n" \
  "v_mfma_f32_16x16x32_bf16 v[24:27], v[88:91], a[124:127], v[24:27]\n" \
  "ds_write_addtid_b32 v15 offset:3968\n" \
  "v_mfma_f32_16x16x32_bf16 v[28:31], v[88:91], a[128:131], v[28:31]\n" \
  "ds_write_addtid_b32 v16 offset:4224\n" \
  "v_mfma_f32_16x16x32_bf16 v[32:35], v[88:91], a[132:135], v[32:35]\n" \
  "buffer_load_dwordx4 v[88:91], v59, s[36:39], s49 offen offset:2048 sc1\n" \
  "s_waitcnt vmcnt(17)\n" \
  "v_mfma_f32_16x16x32_bf16 v[20:23], v[92:95], a[140:143], v[20:23]\n" \
  "ds_write_addtid_b32 v17 offset:4512\n" \
  "v_mfma_f32_16x16x32_bf16 v[24:27], v[92:95], a[144:147], v[24:27]\n" \
  "ds_write_addtid_b32 v18 offset:4800\n" \
  "v_mfma_f32_16x16x32_bf16 v[28:31], v[92:95], a[148:151], v[28:31]\n" \
  "ds_write_addtid_b32 v19 offset:5088\n" \
  "v_mfma_f32_16x16x32_bf16 v[32:35], v[92:95], a[152:155], v[32:35]\n" \
  "buffer_load_dwordx4 v[92:95], v59, s[36:39], s49 offen offset:3072 sc1\n" \
  "s_waitcnt vmcnt(16)\n" \
  "v_mfma_f32_16x16x32_bf16 v[20:23], v[96:99], a[160:163], v[20:23]\n" \
  "v_mfma_f32_16x16x32_bf16 v[24:27], v[96:99], a[164:167], v[24:27]\n" \
  "v_mfma_f32_16x16x32_bf16 v[28:31], v[96:99], a[168:171], v[28:31]\n" \
  "v_mfma_f32_16x16x32_bf16 v[32:35], v[96:99], a[172:175], v[32:35]\n" \
  "buffer_load_dwordx4 v[96:99], v59, s[36:39], s50 offen offset:0 sc1\n" \
  "s_waitcnt vmcnt(16)\n" \
  "v_mfma_f32_16x16x32_bf16 v[20:23], v[100:103], a[180:183], v[20:23]\n" \
  "s_waitcnt lgkmcnt(0)\n" \
  "s_barrier\n" \
  "v_mfma_f32_16x16x32_bf16 v[24:27], v[100:103], a[184:187], v[24:27]\n" \
  "ds_read_b128 v[36:39], v58 offset:0\n" \
  "v_mfma_f32_16x16x32_bf16 v[28:31], v[100:103], a[188:191], v[28:31]\n" \
  "ds_read_b128 v[40:43], v58 offset:9568\n" \
  "v_mfma_f32_16x16x32_bf16 v[32:35], v[100:103], a[192:195], v[32:35]\n" \
  "buffer_load_dwordx4 v[100:103], v59, s[36:39], s50 offen offset:1024 sc1\n" \
  "s_waitcnt vmcnt(16)\n" \
  "v_mfma_f32_16x16x32_bf16 v[20:23], v[104:107], a[200:203], v[20:23]\n" \
  "ds_read_b128 v[44:47], v58 offset:19136\n" \
  "v_mfma_f32_16x16x32_bf16 v[24:27], v[104:107], a[204:207], v[24:27]\n" \
  "ds_read_b128 v[48:51], v58 offset:28704\n" \
  "v_mfma_f32_16x16x32_bf16 v[28:31], v[104:107], a[208:211], v[28:31]\n" \
  "s_cmp_eq_u32 s52, s83\n" \
  "s_cbranch_scc1 .Lxq4_%=\n" \
  ".Lyq4_%=:\n" \
  "v_mfma_f32_16x16x32_bf16 v[32:35], v[104:107], a[212:215], v[32:35]\n" \
  "buffer_load_dwordx4 v[104:107], v59, s[36:39], s50 offen offset:2048 sc1\n" \
  "s_waitcnt vmcnt(16)\n" \
  "v_mfma_f32_16x16x32_bf16 v[20:23], v[108:111], a[220:223], v[20:23]\n" \
  "v_mfma_f32_16x16x32_bf16 v[24:27], v[108:111], a[224:227], v[24:27]\n" \
  "v_mfma_f32_16x16x32_bf16 v[28:31], v[108:111], a[228:231], v[28:31]\n" \
  "s_waitcnt lgkmcnt(2)\n" \
  "v_add_f32 v36, v36, v40\n" \
  "v_add_f32 v37, v37, v41\n" \
  "v_mfma_f32_16x16x32_bf16 v[32:35], v[108:111], a[232:235], v[32:35]\n" \
  "buffer_load_dwordx4 v[108:111], v59, s[36:39], s50 offen offset:3072 sc1\n" \
  "v_add_f32 v38, v38, v42\n" \
  "s_waitcnt vmcnt(16)\n" \
  "v_mfma_f32_16x16x32_bf16 v[20:23], v[112:115], a[240:243], v[20:23]\n" \
  "v_add_f32 v39, v39, v43\n" \
  "v_add_f32 v36, v36, v52\n" \
  "v_add_f32 v37, v37, v53\n" \
  "v_mfma_f32_16x16x32_bf16 v[24:27], v[112:115], a[244:247], v[24:27]\n" \
  "v_add_f32 v38, v38, v54\n" \
  "v_add_f32 v39, v39, v55\n" \
  "s_waitcnt lgkmcnt(0)\n" \
  "v_mfma_f32_16x16x32_bf16 v[28:31], v[112:115], a[248:251], v[28:31]\n" \
  "v_add_f32 v44, v44, v48\n" \
  "v_add_f32 v45, v45, v49\n" \
  "v_add_f32 v46, v46, v50\n" \
  "v_mfma_f32_16x16x32_bf16 v[32:35], v[112:115], a[252:255], v[32:35]\n" \
  "buffer_load_dwordx4 v[112:115], v59, s[36:39], s51 offen offset:0 sc1\n" \
  "v_add_f32 v47, v47, v51\n" \
  "s_waitcnt vmcnt(16)\n" \
  "v_mfma_f32_16x16x32_bf16 v[20:23], v[116:119], v[196:199], v[20:23]\n" \
  "v_add_f32 v36, v36, v44\n" \
  "v_add_f32 v37, v37, v45\n" \
  "v_add_f32 v38, v38, v46\n" \
  "v_mfma_f32_16x16x32_bf16 v[24:27], v[116:119], v[200:203], v[24:27]\n" \
  "v_add_f32 v39, v39, v47\n" \
  "v_mul_f32 v40, v36, v36\n" \
  "v_mul_f32 v41, v37, v37\n" \
  "v_mfma_f32_16x16x32_bf16 v[28:31], v[116:119], v[204:207], v[28:31]\n" \
  "v_mul_f32 v42, v38, v38\n" \
  "v_mul_f32 v43, v39, v39\n" \
  "v_fma_f32 v40, v40, s55, v57\n" \
  "v_mfma_f32_16x16x32_bf16 v[32:35], v[116:119], v[208:211], v[32:35]\n" \
  "buffer_load_dwordx4 v[116:119], v59, s[36:39], s51 offen offset:1024 sc1\n" \
  "v_fma_f32 v41, v41, s55, v57\n" \
  "s_waitcnt vmcnt(16)\n" \
  "v_mfma_f32_16x16x32_bf16 v[20:23], v[120:123], v[216:219], v[20:23]\n" \
  "v_fma_f32 v42, v42, s55, v57\n" \
  "v_fma_f32 v43, v43, s55, v57\n" \
  "v_mul_f32 v40, v36, v40\n" \
  "v_mfma_f32_16x16x32_bf16 v[24:27], v[120:123], v[220:223], v[24:27]\n" \
  "v_mul_f32 v41, v37, v41\n" \
  "v_mul_f32 v42, v38, v42\n" \
  "v_mfma_f32_16x16x32_bf16 v[28:31], v[120:123], v[224:227], v[28:31]\n" \
  "v_mul_f32 v43, v39, v43\n" \
  "v_mfma_f32_16x16x32_bf16 v[32:35], v[120:123], v[228:231], v[32:35]\n" \
  "buffer_load_dwordx4 v[120:123], v59, s[36:39], s51 offen offset:2048 sc1\n" \
  "s_waitcnt vmcnt(16)\n" \
  "v_mfma_f32_16x16x32_bf16 v[20:23], v[124:127], v[236:239], v[20:23]\n" \
  "s_cmp_eq_u32 s52, s95\n" \
  "s_cbranch_scc1 .Lxt4_%=\n" \
  ".Lyt4_%=:\n" \
  "v_mfma_f32_16x16x32_bf16 v[24:27], v[124:127], v[240:243], v[24:27]\n" \
  "v_exp_f32 v40, v40\n" \
  "v_mfma_f32_16x16x32_bf16 v[28:31], v[124:127], v[244:247], v[28:31]\n" \
  "v_exp_f32 v41, v41\n" \
  "v_mfma_f32_16x16x32_bf16 v[32:35], v[124:127], v[248:251], v[32:35]\n" \
  "buffer_load_dwordx4 v[124:127], v59, s[36:39], s51 offen offset:3072 sc1\n" \
  "v_exp_f32 v42, v42\n" \
  "v_exp_f32 v43, v43\n" \
  "v_add_f32 v40, 1.0, v40\n" \
  "s_add_u32 s52, s52, 409600\n" \
  "s_cmp_eq_u32 s52, s53\n" \
  "v_add_f32 v41, 1.0, v41\n" \
  "s_cbranch_scc0 .Lf_%=\n" \
  "v_mfma_f32_16x16x32_bf16 v[0:3], v[64:67], a[0:3], 0\n" \
  "ds_read_b32 v44, v60 offset:0\n" \
  "v_and_b32 v59, 0x3ff, v59\n" \
  "s_add_u32 s48, s68, 409600\n" \
  "v_mfma_f32_16x16x32_bf16 v[4:7], v[64:67], a[4:7], 0\n" \
  "ds_read_b32 v45, v60 offset:9568\n" \
  "s_add_u32 s49, s69, 409600\n" \
  "s_add_u32 s50, s70, 409600\n" \
  "v_mfma_f32_16x16x32_bf16 v[8:11], v[64:67], a[8:11], 0\n" \
  "ds_read_b32 v46, v60 offset:19136\n" \
  "s_add_u32 s100, s90, 73728\n" \
  "s_mov_b32 s35, 73728\n" \
  "v_mfma_f32_16x16x32_bf16 v[12:15], v[64:67], a[12:15], 0\n" \
  "ds_read_b32 v47, v60 offset:28704\n" \
  "ds_write_addtid_b32 v20 offset:5344\n" \
  "v_add_f32 v42, 1.0, v42\n" \
  "v_mfma_f32_16x16x32_bf16 v[16:19], v[64:67], a[16:19], 0\n" \
  "ds_write_addtid_b32 v21 offset:5888\n" \
  "ds_write_addtid_b32 v22 offset:5600\n" \
  "v_mfma_f32_16x16x32_bf16 v[0:3], v[68:71], a[20:23], v[0:3]\n" \
  "ds_write_addtid_b32 v23 offset:6144\n" \
  "ds_write_addtid_b32 v24 offset:6400\n" \
  "v_add_f32 v43, 1.0, v43\n" \
  "v_mfma_f32_16x16x32_bf16 v[4:7], v[68:71], a[24:27], v[4:7]\n" \
  "ds_write_addtid_b32 v25 offset:6944\n" \
  "ds_write_addtid_b32 v26 offset:6656\n" \
  "v_mfma_f32_16x16x32_bf16 v[8:11], v[68:71], a[28:31], v[8:11]\n" \
  "ds_write_addtid_b32 v27 offset:7200\n" \
  "ds_write_addtid_b32 v28 offset:7456\n" \
  "v_mfma_f32_16x16x32_bf16 v[12:15], v[68:71], a[32:35], v[12:15]\n" \
  "ds_write_addtid_b32 v29 offset:8000\n" \
  "ds_write_addtid_b32 v30 offset:7712\n" \
  "v_mfma_f32_16x16x32_bf16 v[16:19], v[68:71], a[36:39], v[16:19]\n" \
  "ds_write_addtid_b32 v31 offset:8256\n" \
  "ds_write_addtid_b32 v32 offset:8512\n" \
  "s_waitcnt vmcnt(15)\n" \
  "v_mfma_f32_16x16x32_bf16 v[0:3], v[72:75], a[40:43], v[0:3]\n" \
  "s_waitcnt vmcnt(6)\n" \
  "v_cmp_eq_u32 vcc, 0, v63\n" \
  "v_add_u32 v71, s57, v59\n" \
  "v_mfma_f32_16x16x32_bf16 v[4:7], v[72:75], a[44:47], v[4:7]\n" \
  "ds_read_b32 v64, v71 offset:28672\n" \
  "ds_read_b64 v[68:69], v71 offset:29696\n" \
  "s_waitcnt lgkmcnt(15)\n" \
  "v_mfma_f32_16x16x32_bf16 v[8:11], v[72:75], a[48:51], v[8:11]\n" \
  "s_and_b64 vcc, vcc, s[86:87]\n" \
  "s_cbranch_scc1 .LsaF_%=\n" \
  ".LbaF_%=:\n" \
  "ds_write_addtid_b32 v33 offset:9056\n" \
  "v_mfma_f32_16x16x32_bf16 v[12:15], v[72:75], a[52:55], v[12:15]\n" \
  "ds_write_addtid_b32 v34 offset:8768\n" \
  "ds_write_addtid_b32 v35 offset:9312\n" \
  "v_add_f32 v44, v44, v45\n" \
  "v_mfma_f32_16x16x32_bf16 v[16:19], v[72:75], a[56:59], v[16:19]\n" \
  "v_rcp_f32 v40, v40\n" \
  "s_waitcnt vmcnt(14)\n" \
  "v_mfma_f32_16x16x32_bf16 v[0:3], v[76:79], a[60:63], v[0:3]\n" \
  "buffer_load_dwordx4 v[72:75], v59, s[36:39], s68 offen offset:0 sc1\n" \
  "s_waitcnt lgkmcnt(3)\n" \
  "v_xor_b32 v65, 32, v64\n" \
  "v_mfma_f32_16x16x32_bf16 v[4:7], v[76:79], a[64:67], v[4:7]\n" \
  "v_xor_b32 v66, 64, v64\n" \
  "v_xor_b32 v67, 96, v64\n" \
  "v_add_u32 v70, 0xf800, v68\n" \
  "v_mfma_f32_16x16x32_bf16 v[8:11], v[76:79], a[68:71], v[8:11]\n" \
  "v_add_u32 v71, 0xf800, v69\n" \
  "ds_read_b64_tr_b16 a[0:1], v64 offset:0\n" \
  "ds_read_b64_tr_b16 a[2:3], v64 offset:512\n" \
  "v_mfma_f32_16x16x32_bf16 v[12:15], v[76:79], a[72:75], v[12:15]\n" \
  "ds_read_b64_tr_b16 a[4:5], v65 offset:0\n" \
  "ds_read_b64_tr_b16 a[6:7], v65 offset:512\n" \
  "v_add_f32 v46, v46, v47\n" \
  "v_mfma_f32_16x16x32_bf16 v[16:19], v[76:79], a[76:79], v[16:19]\n" \
  "s_waitcnt lgkmcnt(0)\n" \
  "s_barrier\n" \
  "v_mfma_f32_16x16x32_bf16 v[0:3], v[80:83], a[80:83], v[0:3]\n" \
  "buffer_load_dwordx4 v[76:79], v59, s[36:39], s68 offen offset:1024 sc1\n" \
  "ds_read_b64_tr_b16 a[8:9], v66 offset:0\n" \
  "ds_read_b64_tr_b16 a[10:11], v66 offset:512\n" \
  "v_mfma_f32_16x16x32_bf16 v[4:7], v[80:83], a[84:87], v[4:7]\n" \
  "ds_read_b64_tr_b16 a[12:13], v67 offset:0\n" \
  "ds_read_b64_tr_b16 a[14:15], v67 offset:512\n" \
  "v_add_f32 v44, v44, v56\n" \
  "v_mfma_f32_16x16x32_bf16 v[8:11], v[80:83], a[88:91], v[8:11]\n" \
  "v_rcp_f32 v41, v41\n" \
  "v_add_f32 v44, v44, v46\n" \
  "v_mfma_f32_16x16x32_bf16 v[12:15], v[80:83], a[92:95], v[12:15]\n" \
  "v_rcp_f32 v42, v42\n" \
  "v_mul_f32 v45, v44, v44\n" \
  "v_mfma_f32_16x16x32_bf16 v[16:19], v[80:83], a[96:99], v[16:19]\n" \
  "ds_read_b64_tr_b16 a[16:17], v64 offset:4096\n" \
  "ds_read_b64_tr_b16 a[18:19], v64 offset:4608\n" \
  "v_mfma_f32_16x16x32_bf16 v[0:3], v[84:87], a[100:103], v[0:3]\n" \
  "buffer_load_dwordx4 v[80:83], v59, s[36:39], s68 offen offset:2048 sc1\n" \
  "s_waitcnt lgkmcnt(2)\n" \
  "s_add_u32 m0, s57, 0\n" \
  "v_mfma_f32_16x16x32_bf16 v[4:7], v[84:87], a[104:107], v[4:7]\n" \
  "ds_read_b64_tr_b16 a[20:21], v65 offset:4096\n" \
  "ds_read_b64_tr_b16 a[22:23], v65 offset:4608\n" \
  "v_fma_f32 v45, v45, s55, v57\n" \
  "v_mfma_f32_16x16x32_bf16 v[8:11], v[84:87], a[108:111], v[8:11]\n" \
  "ds_read_b64_tr_b16 a[24:25], v66 offset:4096\n" \
  "ds_read_b64_tr_b16 a[26:27], v66 offset:4608\n" \
  "v_mul_f32 v45, v44, v45\n" \
  "v_mfma_f32_16x16x32_bf16 v[12:15], v[84:87], a[112:115], v[12:15]\n" \
  "buffer_load_dwordx4 v68, s[40:43], s60 offen offset:0 lds\n" \
  "ds_read_b64_tr_b16 a[28:29], v67 offset:4096\n" \
  "ds_read_b64_tr_b16 a[30:31], v67 offset:4608\n" \
  "v_mfma_f32_16x16x32_bf16 v[16:19], v[84:87], a[116:119], v[16:19]\n" \
  "v_rcp_f32 v43, v43\n" \
  "v_mfma_f32_16x16x32_bf16 v[0:3], v[88:91], a[120:123], v[0:3]\n" \
  "v_exp_f32 v45, v45\n" \
  "v_mul_f32 v36, v36, v40\n" \
  "v_mfma_f32_16x16x32_bf16 v[4:7], v[88:91], a[124:127], v[4:7]\n" \
  "buffer_load_dwordx4 v69, s[40:43], s60 offen offset:1024 lds\n" \
  "v_mul_f32 v37, v37, v41\n" \
  "v_add_f32 v45, 1.0, v45\n" \
  "v_mfma_f32_16x16x32_bf16 v[8:11], v[88:91], a[128:131], v[8:11]\n" \
  "v_mul_f32 v38, v38, v42\n" \
  "v_rcp_f32 v45, v45\n" \
  "v_mfma_f32_16x16x32_bf16 v[12:15], v[88:91], a[132:135], v[12:15]\n" \
  "v_mul_f32 v39, v39, v43\n" \
  "v_cvt_pk_bf16_f32 v40, v36, v37\n" \
  "v_mul_f32 v44, v44, v45\n" \
  "v_mfma_f32_16x16x32_bf16 v[16:19], v[88:91], a[136:139], v[16:19]\n" \
  "buffer_load_dwordx4 v70, s[40:43], s60 offen offset:2048 lds\n" \
  "ds_read_b64_tr_b16 a[32:33], v64 offset:8192\n" \
  "v_mfma_f32_16x16x32_bf16 v[0:3], v[92:95], a[140:143], v[0:3]\n" \
  "ds_read_b64_tr_b16 a[34:35], v64 offset:8704\n" \
  "ds_read_b64_tr_b16 a[36:37], v65 offset:8192\n" \
  "v_cvt_pk_bf16_f32 v41, v38, v39\n" \
  "v_mfma_f32_16x16x32_bf16 v[4:7], v[92:95], a[144:147], v[4:7]\n" \
  "ds_read_b64_tr_b16 a[38:39], v65 offset:8704\n" \
  "ds_read_b64_tr_b16 a[40:41], v66 offset:8192\n" \
  "v_cvt_pk_bf16_f32 v45, v44, v44\n" \
  "v_mfma_f32_16x16x32_bf16 v[8:11], v[92:95], a[148:151], v[8:11]\n" \
  "buffer_load_dwordx4 v71, s[40:43], s60 offen offset:3072 lds\n" \
  "ds_read_b64_tr_b16 a[42:43], v66 offset:8704\n" \
  "ds_read_b64_tr_b16 a[44:45], v67 offset:8192\n" \
  "v_mfma_f32_16x16x32_bf16 v[12:15], v[92:95], a[152:155], v[12:15]\n" \
  "ds_read_b64_tr_b16 a[46:47], v67 offset:8704\n" \
  "buffer_store_dwordx2 v[40:41], v61, s[36:39], s52 offen offset:0 sc1\n" \
  "buffer_store_short v45, v62, s[36:39], s52 offen offset:0 sc1\n" \
  "v_mfma_f32_16x16x32_bf16 v[16:19], v[92:95], a[156:159], v[16:19]\n" \
  "ds_read_b128 v[36:39], v58 offset:5344\n" \
  "ds_read_b128 v[40:43], v58 offset:14912\n" \
  "s_waitcnt vmcnt(18)\n" \
  "v_mfma_f32_16x16x32_bf16 v[0:3], v[96:99], a[160:163], v[0:3]\n" \
  "buffer_load_dwordx4 v[84:87], v59, s[36:39], s69 offen offset:0 sc1\n" \
  "ds_read_b128 v[44:47], v58 offset:24480\n" \
  "ds_read_b128 v[48:51], v58 offset:34048\n" \
  "v_mfma_f32_16x16x32_bf16 v[4:7], v[96:99], a[164:167], v[4:7]\n" \
  "s_add_u32 s52, s52, 409600\n" \
  "v_mfma_f32_16x16x32_bf16 v[8:11], v[96:99], a[168:171], v[8:11]\n" \
  "v_mfma_f32_16x16x32_bf16 v[12:15], v[96:99], a[172:175], v[12:15]\n" \
  "buffer_load_dwordx4 v[88:91], v59, s[36:39], s69 offen offset:1024 sc1\n" \
  "v_mfma_f32_16x16x32_bf16 v[16:19], v[96:99], a[176:179], v[16:19]\n" \
  "ds_read_b64_tr_b16 a[48:49], v64 offset:12288\n" \
  "ds_read_b64_tr_b16 a[50:51], v64 offset:12800\n" \
  "s_waitcnt vmcnt(19)\n" \
  "v_mfma_f32_16x16x32_bf16 v[0:3], v[100:103], a[180:183], v[0:3]\n" \
  "ds_read_b64_tr_b16 a[52:53], v65 offset:12288\n" \
  "ds_read_b64_tr_b16 a[54:55], v65 offset:12800\n" \
  "v_mfma_f32_16x16x32_bf16 v[4:7], v[100:103], a[184:187], v[4:7]\n" \
  "buffer_load_dwordx4 v[92:95], v59, s[36:39], s69 offen offset:2048 sc1\n" \
  "s_waitcnt lgkmcnt(15)\n" \
  "s_add_u32 m0, s57, 4096\n" \
  "v_mfma_f32_16x16x32_bf16 v[8:11], v[100:103], a[188:191], v[8:11]\n" \
  "ds_read_b64_tr_b16 a[56:57], v66 offset:12288\n" \
  "ds_read_b64_tr_b16 a[58:59], v66 offset:12800\n" \
  "s_waitcnt lgkmcnt(8)\n" \
  "v_mfma_f32_16x16x32_bf16 v[12:15], v[100:103], a[192:195], v[12:15]\n" \
  "ds_read_b64_tr_b16 a[60:61], v67 offset:12288\n" \
  "ds_read_b64_tr_b16 a[62:63], v67 offset:12800\n" \
  "v_add_f32 v36, v36, v40\n" \
  "v_mfma_f32_16x16x32_bf16 v[16:19], v[100:103], a[196:199], v[16:19]\n" \
  "buffer_load_dwordx4 v68, s[40:43], s61 offen offset:0 lds\n" \
  "v_add_f32 v37, v37, v41\n" \
  "s_waitcnt vmcnt(18)\n" \
  "v_mfma_f32_16x16x32_bf16 v[0:3], v[104:107], a[200:203], v[0:3]\n" \
  "v_add_f32 v38, v38, v42\n" \
  "v_add_f32 v39, v39, v43\n" \
  "v_add_f32 v36, v36, v52\n" \
  "v_mfma_f32_16x16x32_bf16 v[4:7], v[104:107], a[204:207], v[4:7]\n" \
  "v_add_f32 v37, v37, v53\n" \
  "v_add_f32 v38, v38, v54\n" \
  "v_add_f32 v39, v39, v55\n" \
  "v_mfma_f32_16x16x32_bf16 v[8:11], v[104:107], a[208:211], v[8:11]\n" \
  "buffer_load_dwordx4 v69, s[40:43], s61 offen offset:1024 lds\n" \
  "s_waitcnt lgkmcnt(8)\n" \
  "v_add_f32 v44, v44, v48\n" \
  "v_mfma_f32_16x16x32_bf16 v[12:15], v[104:107], a[212:215], v[12:15]\n" \
  "v_add_f32 v45, v45, v49\n" \
  "v_add_f32 v46, v46, v50\n" \
  "v_add_f32 v47, v47, v51\n" \
  "v_mfma_f32_16x16x32_bf16 v[16:19], v[104:107], a[216:219], v[16:19]\n" \
  "ds_read_b64_tr_b16 a[64:65], v64 offset:16384\n" \
  "ds_read_b64_tr_b16 a[66:67], v64 offset:16896\n" \
  "s_waitcnt vmcnt(18)\n" \
  "v_mfma_f32_16x16x32_bf16 v[0:3], v[108:111], a[220:223], v[0:3]\n" \
  "buffer_load_dwordx4 v70, s[40:43], s61 offen offset:2048 lds\n" \
  "ds_read_b64_tr_b16 a[68:69], v65 offset:16384\n" \
  "ds_read_b64_tr_b16 a[70:71], v65 offset:16896\n" \
  "v_mfma_f32_16x16x32_bf16 v[4:7], v[108:111], a[224:227], v[4:7]\n" \
  "ds_read_b64_tr_b16 a[72:73], v66 offset:16384\n" \
  "ds_read_b64_tr_b16 a[74:75], v66 offset:16896\n" \
  "v_add_f32 v36, v36, v44\n" \
  "v_mfma_f32_16x16x32_bf16 v[8:11], v[108:111], a[228:231], v[8:11]\n" \
  "ds_read_b64_tr_b16 a[76:77], v67 offset:16384\n" \
  "ds_read_b64_tr_b16 a[78:79], v67 offset:16896\n" \
  "v_add_f32 v37, v37, v45\n" \
  "v_mfma_f32_16x16x32_bf16 v[12:15], v[108:111], a[232:235], v[12:15]\n" \
  "buffer_load_dwordx4 v71, s[40:43], s61 offen offset:3072 lds\n" \
  "v_add_f32 v38, v38, v46\n" \
  "v_add_f32 v39, v39, v47\n" \
  "v_mfma_f32_16x16x32_bf16 v[16:19], v[108:111], a[236:239], v[16:19]\n" \
  "v_mul_f32 v40, v36, v36\n" \
  "v_mul_f32 v41, v37, v37\n" \
  "s_waitcnt vmcnt(19)\n" \
  "v_mfma_f32_16x16x32_bf16 v[0:3], v[112:115], a[240:243], v[0:3]\n" \
  "v_mul_f32 v42, v38, v38\n" \
  "v_mul_f32 v43, v39, v39\n" \
  "v_fma_f32 v40, v40, s55, v57\n" \
  "v_mfma_f32_16x16x32_bf16 v[4:7], v[112:115], a[244:247], v[4:7]\n" \
  "buffer_load_dwordx4 v[108:111], v59, s[36:39], s48 offen offset:0 sc1\n" \
  "s_add_u32 m0, s57, 8192\n" \
  "v_mfma_f32_16x16x32_bf16 v[8:11], v[112:115], a[248:251], v[8:11]\n" \
  "v_fma_f32 v41, v41, s55, v57\n" \
  "v_fma_f32 v42, v42, s55, v57\n" \
  "v_fma_f32 v43, v43, s55, v57\n" \
  "v_mfma_f32_16x16x32_bf16 v[12:15], v[112:115], a[252:255], v[12:15]\n" \
  "v_mul_f32 v40, v36, v40\n" \
  "v_mul_f32 v41, v37, v41\n" \
  "v_mul_f32 v42, v38, v42\n" \
  "v_mfma_f32_16x16x32_bf16 v[16:19], v[112:115], v[192:195], v[16:19]\n" \
  "buffer_load_dwordx4 v68, s[40:43], s62 offen offset:0 lds\n" \
  "ds_read_b64_tr_b16 a[80:81], v64 offset:20480\n" \
  "s_waitcnt vmcnt(20)\n" \
  "v_mfma_f32_16x16x32_bf16 v[0:3], v[116:119], v[196:199], v[0:3]\n" \
  "ds_read_b64_tr_b16 a[82:83], v64 offset:20992\n" \
  "ds_read_b64_tr_b16 a[84:85], v65 offset:20480\n" \
  "v_mul_f32 v43, v39, v43\n" \
  "v_mfma_f32_16x16x32_bf16 v[4:7], v[116:119], v[200:203], v[4:7]\n" \
  "ds_read_b64_tr_b16 a[86:87], v65 offset:20992\n" \
  "ds_read_b64_tr_b16 a[88:89], v66 offset:20480\n" \
  "v_mfma_f32_16x16x32_bf16 v[8:11], v[116:119], v[204:207], v[8:11]\n" \
  "buffer_load_dwordx4 v69, s[40:43], s62 offen offset:1024 lds\n" \
  "ds_read_b64_tr_b16 a[90:91], v66 offset:20992\n" \
  "ds_read_b64_tr_b16 a[92:93], v67 offset:20480\n" \
  "v_mfma_f32_16x16x32_bf16 v[12:15], v[116:119], v[208:211], v[12:15]\n" \
  "ds_read_b64_tr_b16 a[94:95], v67 offset:20992\n" \
  "v_exp_f32 v40, v40\n" \
  "v_mfma_f32_16x16x32_bf16 v[16:19], v[116:119], v[212:215], v[16:19]\n" \
  "v_exp_f32 v41, v41\n" \
  "s_waitcnt vmcnt(20)\n" \
  "v_mfma_f32_16x16x32_bf16 v[0:3], v[120:123], v[216:219], v[0:3]\n" \
  "buffer_load_dwordx4 v70, s[40:43], s62 offen offset:2048 lds\n" \
  "v_exp_f32 v42, v42\n" \
  "v_mfma_f32_16x16x32_bf16 v[4:7], v[120:123], v[220:223], v[4:7]\n" \
  "v_exp_f32 v43, v43\n" \
  "v_add_f32 v40, 1.0, v40\n" \
  "v_mfma_f32_16x16x32_bf16 v[8:11], v[120:123], v[224:227], v[8:11]\n" \
  "v_add_f32 v41, 1.0, v41\n" \
  "v_add_f32 v42, 1.0, v42\n" \
  "v_add_f32 v43, 1.0, v43\n" \
  "v_mfma_f32_16x16x32_bf16 v[12:15], v[120:123], v[228:231], v[12:15]\n" \
  "buffer_load_dwordx4 v71, s[40:43], s62 offen offset:3072 lds\n" \
  "v_rcp_f32 v40, v40\n" \
  "v_mfma_f32_16x16x32_bf16 v[16:19], v[120:123], v[232:235], v[16:19]\n" \
  "ds_read_b64_tr_b16 a[96:97], v64 offset:24576\n" \
  "ds_read_b64_tr_b16 a[98:99], v64 offset:25088\n" \
  "s_waitcnt vmcnt(21)\n" \
  "v_mfma_f32_16x16x32_bf16 v[0:3], v[124:127], v[236:239], v[0:3]\n" \
  "ds_read_b64_tr_b16 a[100:101], v65 offset:24576\n" \
  "ds_read_b64_tr_b16 a[102:103], v65 offset:25088\n" \
  "v_mfma_f32_16x16x32_bf16 v[4:7], v[124:127], v[240:243], v[4:7]\n" \
  "buffer_load_dwordx4 v[112:115], v59, s[36:39], s48 offen offset:1024 sc1\n" \
  "ds_read_b64_tr_b16 a[104:105], v66 offset:24576\n" \
  "ds_read_b64_tr_b16 a[106:107], v66 offset:25088\n" \
  "v_mfma_f32_16x16x32_bf16 v[8:11], v[124:127], v[244:247], v[8:11]\n" \
  "ds_read_b64_tr_b16 a[108:109], v67 offset:24576\n" \
  "ds_read_b64_tr_b16 a[110:111], v67 offset:25088\n" \
  "v_mfma_f32_16x16x32_bf16 v[12:15], v[124:127], v[248:251], v[12:15]\n" \
  "v_rcp_f32 v41, v41\n" \
  "v_mfma_f32_16x16x32_bf16 v[16:19], v[124:127], v[252:255], v[16:19]\n" \
  "buffer_load_dwordx4 v[116:119], v59, s[36:39], s48 offen offset:2048 sc1\n" \
  "s_mov_b32 m0, s56\n" \
  "s_waitcnt vmcnt(22)\n" \
  "v_mfma_f32_16x16x32_bf16 v[208:211], v[128:131], v[72:75], 0\n" \
  "v_rcp_f32 v42, v42\n" \
  "v_mfma_f32_16x16x32_bf16 v[212:215], v[132:135], v[72:75], 0\n" \
  "ds_write_addtid_b32 v0 offset:0\n" \
  "ds_write_addtid_b32 v1 offset:544\n" \
  "v_mfma_f32_16x16x32_bf16 v[216:219], v[136:139], v[72:75], 0\n" \
  "buffer_load_dwordx4 v[120:123], v59, s[36:39], s49 offen offset:0 sc1\n" \
  "ds_write_addtid_b32 v2 offset:256\n" \
  "ds_write_addtid_b32 v3 offset:800\n" \
  "v_mfma_f32_16x16x32_bf16 v[220:223], v[140:143], v[72:75], 0\n" \
  "ds_write_addtid_b32 v4 offset:1056\n" \
  "ds_write_addtid_b32 v5 offset:1600\n" \
  "v_mfma_f32_16x16x32_bf16 v[224:227], v[144:147], v[72:75], 0\n" \
  "ds_write_addtid_b32 v6 offset:1312\n" \
  "ds_write_addtid_b32 v7 offset:1856\n" \
  "v_mfma_f32_16x16x32_bf16 v[228:231], v[148:151], v[72:75], 0\n" \
  "buffer_load_dwordx4 v[124:127], v59, s[36:39], s49 offen offset:1024 sc1\n" \
  "ds_write_addtid_b32 v8 offset:2112\n" \
  "ds_write_addtid_b32 v9 offset:2656\n" \
  "v_mfma_f32_16x16x32_bf16 v[232:235], v[152:155], v[72:75], 0\n" \
  "ds_write_addtid_b32 v10 offset:2368\n" \
  "ds_write_addtid_b32 v11 offset:2912\n" \
  "v_rcp_f32 v43, v43\n" \
  "v_mfma_f32_16x16x32_bf16 v[236:239], v[156:159], v[72:75], 0\n" \
  "ds_write_addtid_b32 v12 offset:3168\n" \
  "ds_write_addtid_b32 v13 offset:3712\n" \
  "v_mul_f32 v36, v36, v40\n" \
  "s_waitcnt vmcnt(23)\n" \
  "v_mfma_f32_16x16x32_bf16 v[208:211], v[160:163], v[76:79], v[208:211]\n" \
  "v_mul_f32 v37, v37, v41\n" \
  "v_mul_f32 v38, v38, v42\n" \
  "v_mul_f32 v39, v39, v43\n" \
  "v_cvt_pk_bf16_f32 v40, v36, v37\n" \
  "v_cvt_pk_bf16_f32 v41, v38, v39\n" \
  "buffer_store_dwordx2 v[40:41], v61, s[36:39], s52 offen offset:0 sc1\n" \
  "v_mfma_f32_16x16x32_bf16 v[212:215], v[164:167], v[76:79], v[212:215]\n" \
  "buffer_load_dwordx4 v[192:195], v59, s[36:39], s49 offen offset:2048 sc1\n" \
  "ds_write_addtid_b32 v14 offset:3424\n" \
  "ds_write_addtid_b32 v15 offset:3968\n" \
  "v_mfma_f32_16x16x32_bf16 v[216:219], v[168:171], v[76:79], v[216:219]\n" \
  "ds_write_addtid_b32 v16 offset:4224\n" \
  "ds_write_addtid_b32 v17 offset:4512\n" \
  "s_add_u32 s52, s52, 409600\n" \
  "v_mfma_f32_16x16x32_bf16 v[220:223], v[172:175], v[76:79], v[220:223]\n" \
  "ds_write_addtid_b32 v18 offset:4800\n" \
  "ds_write_addtid_b32 v19 offset:5088\n" \
  "s_waitcnt vmcnt(20)\n" \
  "v_mfma_f32_16x16x32_bf16 v[224:227], v[176:179], v[76:79], v[224:227]\n" \
  "buffer_load_dwordx4 v[96:99], v59, s[36:39], s70 offen offset:0 sc1\n" \
  "s_add_u32 m0, s57, 12288\n" \
  "v_mfma_f32_16x16x32_bf16 v[228:231], v[180:183], v[76:79], v[228:231]\n" \
  "ds_read_b64_tr_b16 a[112:113], v64 offset:0\n" \
  "ds_read_b64_tr_b16 a[114:115], v64 offset:512\n" \
  "v_mfma_f32_16x16x32_bf16 v[232:235], v[184:187], v[76:79], v[232:235]\n" \
  "ds_read_b64_tr_b16 a[116:117], v65 offset:0\n" \
  "ds_read_b64_tr_b16 a[118:119], v65 offset:512\n" \
  "v_mfma_f32_16x16x32_bf16 v[236:239], v[188:191], v[76:79], v[236:239]\n" \
  "buffer_load_dwordx4 v68, s[40:43], s63 offen offset:0 lds\n" \
  "s_waitcnt lgkmcnt(15)\n" \
  "v_mfma_f32_16x16x32_bf16 v[208:211], a[0:3], v[80:83], v[208:211]\n" \
  "ds_read_b64_tr_b16 a[120:121], v66 offset:0\n" \
  "ds_read_b64_tr_b16 a[122:123], v66 offset:512\n" \
  "v_mfma_f32_16x16x32_bf16 v[212:215], a[4:7], v[80:83], v[212:215]\n" \
  "s_waitcnt lgkmcnt(0)\n" \
  "s_barrier\n" \
  "ds_read_b128 v[36:39], v58 offset:0\n" \
  "v_mfma_f32_16x16x32_bf16 v[216:219], a[8:11], v[80:83], v[216:219]\n" \
  "buffer_load_dwordx4 v69, s[40:43], s63 offen offset:1024 lds\n" \
  "ds_read_b128 v[40:43], v58 offset:9568\n" \
  "ds_read_b128 v[44:47], v58 offset:19136\n" \
  "v_mfma_f32_16x16x32_bf16 v[220:223], a[12:15], v[80:83], v[220:223]\n" \
  "ds_read_b128 v[48:51], v58 offset:28704\n" \
  "ds_read_b64_tr_b16 a[124:125], v67 offset:0\n" \
  "v_mfma_f32_16x16x32_bf16 v[224:227], a[16:19], v[80:83], v[224:227]\n" \
  "ds_read_b64_tr_b16 a[126:127], v67 offset:512\n" \
  "ds_read_b32 v252, v60 offset:0\n" \
  "v_mfma_f32_16x16x32_bf16 v[228:231], a[20:23], v[80:83], v[228:231]\n" \
  "buffer_load_dwordx4 v70, s[40:43], s63 offen offset:2048 lds\n" \
  "ds_read_b32 v253, v60 offset:9568\n" \
  "ds_read_b32 v254, v60 offset:19136\n" \
  "v_mfma_f32_16x16x32_bf16 v[232:235], a[24:27], v[80:83], v[232:235]\n" \
  "ds_read_b32 v255, v60 offset:28704\n" \
  "v_mfma_f32_16x16x32_bf16 v[236:239], a[28:31], v[80:83], v[236:239]\n" \
  "s_waitcnt vmcnt(21)\n" \
  "v_mfma_f32_16x16x32_bf16 v[208:211], a[32:35], v[84:87], v[208:211]\n" \
  "buffer_load_dwordx4 v71, s[40:43], s63 offen offset:3072 lds\n" \
  "s_add_u32 m0, s57, 16384\n" \
  "v_mfma_f32_16x16x32_bf16 v[212:215], a[36:39], v[84:87], v[212:215]\n" \
  "v_mfma_f32_16x16x32_bf16 v[216:219], a[40:43], v[84:87], v[216:219]\n" \
  "s_waitcnt lgkmcnt(8)\n" \
  "v_add_f32 v36, v36, v40\n" \
  "v_add_f32 v37, v37, v41\n" \
  "v_mfma_f32_16x16x32_bf16 v[220:223], a[44:47], v[84:87], v[220:223]\n" \
  "buffer_load_dwordx4 v68, s[40:43], s64 offen offset:0 lds\n" \
  "v_add_f32 v38, v38, v42\n" \
  "v_mfma_f32_16x16x32_bf16 v[224:227], a[48:51], v[84:87], v[224:227]\n" \
  "s_waitcnt vmcnt(17)\n" \
  "ds_read_b64_tr_b16 a[128:129], v64 offset:4096\n" \
  "ds_read_b64_tr_b16 a[130:131], v64 offset:4608\n" \
  "v_mfma_f32_16x16x32_bf16 v[228:231], a[52:55], v[84:87], v[228:231]\n" \
  "ds_read_b64_tr_b16 a[132:133], v65 offset:4096\n" \
  "ds_read_b64_tr_b16 a[134:135], v65 offset:4608\n" \
  "v_add_f32 v39, v39, v43\n" \
  "v_mfma_f32_16x16x32_bf16 v[232:235], a[56:59], v[84:87], v[232:235]\n" \
  "buffer_load_dwordx4 v69, s[40:43], s64 offen offset:1024 lds\n" \
  "ds_read_b64_tr_b16 a[136:137], v66 offset:4096\n" \
  "ds_read_b64_tr_b16 a[138:139], v66 offset:4608\n" \
  "v_mfma_f32_16x16x32_bf16 v[236:239], a[60:63], v[84:87], v[236:239]\n" \
  "ds_read_b64_tr_b16 a[140:141], v67 offset:4096\n" \
  "v_mfma_f32_16x16x32_bf16 v[208:211], a[64:67], v[88:91], v[208:211]\n" \
  "ds_read_b64_tr_b16 a[142:143], v67 offset:4608\n" \
  "v_add_f32 v36, v36, v52\n" \
  "v_add_f32 v37, v37, v53\n" \
  "v_mfma_f32_16x16x32_bf16 v[212:215], a[68:71], v[88:91], v[212:215]\n" \
  "buffer_load_dwordx4 v70, s[40:43], s64 offen offset:2048 lds\n" \
  "v_add_f32 v38, v38, v54\n" \
  "v_add_f32 v39, v39, v55\n" \
  "v_mfma_f32_16x16x32_bf16 v[216:219], a[72:75], v[88:91], v[216:219]\n" \
  "s_waitcnt lgkmcnt(14)\n" \
  "v_add_f32 v44, v44, v48\n" \
  "v_add_f32 v45, v45, v49\n" \
  "v_mfma_f32_16x16x32_bf16 v[220:223], a[76:79], v[88:91], v[220:223]\n" \
  "v_add_f32 v46, v46, v50\n" \
  "v_add_f32 v47, v47, v51\n" \
  "v_mfma_f32_16x16x32_bf16 v[224:227], a[80:83], v[88:91], v[224:227]\n" \
  "buffer_load_dwordx4 v71, s[40:43], s64 offen offset:3072 lds\n" \
  "v_add_f32 v36, v36, v44\n" \
  "v_add_f32 v37, v37, v45\n" \
  "v_mfma_f32_16x16x32_bf16 v[228:231], a[84:87], v[88:91], v[228:231]\n" \
  "v_add_f32 v38, v38, v46\n" \
  "v_add_f32 v39, v39, v47\n" \
  "v_mul_f32 v40, v36, v36\n" \
  "v_mfma_f32_16x16x32_bf16 v[232:235], a[88:91], v[88:91], v[232:235]\n" \
  "v_mul_f32 v41, v37, v37\n" \
  "v_mul_f32 v42, v38, v38\n" \
  "v_mul_f32 v43, v39, v39\n" \
  "v_mfma_f32_16x16x32_bf16 v[236:239], a[92:95], v[88:91], v[236:239]\n" \
  "buffer_load_dwordx4 v[100:103], v59, s[36:39], s70 offen offset:1024 sc1\n" \
  "v_mfma_f32_16x16x32_bf16 v[208:211], a[96:99], v[92:95], v[208:211]\n" \
  "s_add_u32 m0, s57, 20480\n" \
  "v_fma_f32 v40, v40, s55, v57\n" \
  "v_mfma_f32_16x16x32_bf16 v[212:215], a[100:103], v[92:95], v[212:215]\n" \
  "v_fma_f32 v41, v41, s55, v57\n" \
  "v_fma_f32 v42, v42, s55, v57\n" \
  "v_fma_f32 v43, v43, s55, v57\n" \
  "v_mfma_f32_16x16x32_bf16 v[216:219], a[104:107], v[92:95], v[216:219]\n" \
  "buffer_load_dwordx4 v68, s[40:43], s65 offen offset:0 lds\n" \
  "v_mul_f32 v40, v36, v40\n" \
  "v_mul_f32 v41, v37, v41\n" \
  "v_mfma_f32_16x16x32_bf16 v[220:223], a[108:111], v[92:95], v[220:223]\n" \
  "s_waitcnt vmcnt(17)\n" \
  "ds_read_b64_tr_b16 a[144:145], v64 offset:8192\n" \
  "v_mfma_f32_16x16x32_bf16 v[0:3], v[128:131], v[108:111], 0\n" \
  "ds_read_b64_tr_b16 a[146:147], v64 offset:8704\n" \
  "ds_read_b64_tr_b16 a[148:149], v65 offset:8192\n" \
  "v_mul_f32 v42, v38, v42\n" \
  "v_mfma_f32_16x16x32_bf16 v[4:7], v[132:135], v[108:111], 0\n" \
  "buffer_load_dwordx4 v69, s[40:43], s65 offen offset:1024 lds\n" \
  "ds_read_b64_tr_b16 a[150:151], v65 offset:8704\n" \
  "ds_read_b64_tr_b16 a[152:153], v66 offset:8192\n" \
  "v_mfma_f32_16x16x32_bf16 v[8:11], v[136:139], v[108:111], 0\n" \
  "ds_read_b64_tr_b16 a[154:155], v66 offset:8704\n" \
  "ds_read_b64_tr_b16 a[156:157], v67 offset:8192\n" \
  "v_mul_f32 v43, v39, v43\n" \
  "v_mfma_f32_16x16x32_bf16 v[12:15], v[140:143], v[108:111], 0\n" \
  "ds_read_b64_tr_b16 a[158:159], v67 offset:8704\n" \
  "v_exp_f32 v40, v40\n" \
  "v_mfma_f32_16x16x32_bf16 v[16:19], v[144:147], v[108:111], 0\n" \
  "buffer_load_dwordx4 v70, s[40:43], s65 offen offset:2048 lds\n" \
  "v_exp_f32 v41, v41\n" \
  "v_mfma_f32_16x16x32_bf16 v[20:23], v[148:151], v[108:111], 0\n" \
  "v_exp_f32 v42, v42\n" \
  "s_waitcnt lgkmcnt(15)\n" \
  "v_mfma_f32_16x16x32_bf16 v[24:27], v[152:155], v[108:111], 0\n" \
  "v_exp_f32 v43, v43\n" \
  "v_add_f32 v40, 1.0, v40\n" \
  "v_mfma_f32_16x16x32_bf16 v[28:31], v[156:159], v[108:111], 0\n" \
  "buffer_load_dwordx4 v71, s[40:43], s65 offen offset:3072 lds\n" \
  "v_add_f32 v41, 1.0, v41\n" \
  "s_waitcnt vmcnt(19)\n" \
  "v_mfma_f32_16x16x32_bf16 v[0:3], v[160:163], v[112:115], v[0:3]\n" \
  "v_add_f32 v42, 1.0, v42\n" \
  "v_add_f32 v43, 1.0, v43\n" \
  "v_add_f32 v252, v252, v253\n" \
  "v_mfma_f32_16x16x32_bf16 v[4:7], v[164:167], v[112:115], v[4:7]\n" \
  "v_rcp_f32 v40, v40\n" \
  "v_add_f32 v254, v254, v255\n" \
  "v_mfma_f32_16x16x32_bf16 v[8:11], v[168:171], v[112:115], v[8:11]\n" \
  "buffer_load_dwordx4 v[104:107], v59, s[36:39], s70 offen offset:2048 sc1\n" \
  "s_add_u32 m0, s57, 24576\n" \
  "v_mfma_f32_16x16x32_bf16 v[12:15], v[172:175], v[112:115], v[12:15]\n" \
  "v_rcp_f32 v41, v41\n" \
  "v_add_f32 v252, v252, v56\n" \
  "v_mfma_f32_16x16x32_bf16 v[16:19], v[176:179], v[112:115], v[16:19]\n" \
  "v_rcp_f32 v42, v42\n" \
  "v_add_f32 v252, v252, v254\n" \
  "v_mfma_f32_16x16x32_bf16 v[20:23], v[180:183], v[112:115], v[20:23]\n" \
  "buffer_load_dwordx4 v68, s[40:43], s66 offen offset:0 lds\n" \
  "v_rcp_f32 v43, v43\n" \
  "v_mfma_f32_16x16x32_bf16 v[24:27], v[184:187], v[112:115], v[24:27]\n" \
  "v_mul_f32 v36, v36, v40\n" \
  "v_mul_f32 v37, v37, v41\n" \
  "v_mul_f32 v38, v38, v42\n" \
  "v_mfma_f32_16x16x32_bf16 v[28:31], v[188:191], v[112:115], v[28:31]\n" \
  "v_mul_f32 v39, v39, v43\n" \
  "v_cvt_pk_bf16_f32 v40, v36, v37\n" \
  "s_waitcnt vmcnt(20)\n" \
  "v_mfma_f32_16x16x32_bf16 v[0:3], a[0:3], v[116:119], v[0:3]\n" \
  "buffer_load_dwordx4 v69, s[40:43], s66 offen offset:1024 lds\n" \
  "v_cvt_pk_bf16_f32 v41, v38, v39\n" \
  "v_mul_f32 v253, v252, v252\n" \
  "v_mfma_f32_16x16x32_bf16 v[4:7], a[4:7], v[116:119], v[4:7]\n" \
  "v_fma_f32 v253, v253, s55, v57\n" \
  "v_mul_f32 v253, v252, v253\n" \
  "v_mfma_f32_16x16x32_bf16 v[8:11], a[8:11], v[116:119], v[8:11]\n" \
  "v_exp_f32 v253, v253\n" \
  "v_mfma_f32_16x16x32_bf16 v[12:15], a[12:15], v[116:119], v[12:15]\n" \
  "buffer_load_dwordx4 v70, s[40:43], s66 offen offset:2048 lds\n" \
  "v_add_f32 v253, 1.0, v253\n" \
  "v_mfma_f32_16x16x32_bf16 v[16:19], a[16:19], v[116:119], v[16:19]\n" \
  "v_rcp_f32 v253, v253\n" \
  "v_mfma_f32_16x16x32_bf16 v[20:23], a[20:23], v[116:119], v[20:23]\n" \
  "v_mul_f32 v252, v252, v253\n" \
  "v_cvt_pk_bf16_f32 v253, v252, v252\n" \
  "v_mfma_f32_16x16x32_bf16 v[24:27], a[24:27], v[116:119], v[24:27]\n" \
  "buffer_load_dwordx4 v71, s[40:43], s66 offen offset:3072 lds\n" \
  "v_mfma_f32_16x16x32_bf16 v[28:31], a[28:31], v[116:119], v[28:31]\n" \
  "s_waitcnt vmcnt(22)\n" \
  "v_mfma_f32_16x16x32_bf16 v[0:3], a[32:35], v[120:123], v[0:3]\n" \
  "v_mfma_f32_16x16x32_bf16 v[4:7], a[36:39], v[120:123], v[4:7]\n" \
  "buffer_load_dwordx4 v[196:199], v59, s[36:39], s50 offen offset:0 sc1\n" \
  "v_mfma_f32_16x16x32_bf16 v[8:11], a[40:43], v[120:123], v[8:11]\n" \
  "v_mfma_f32_16x16x32_bf16 v[12:15], a[44:47], v[120:123], v[12:15]\n" \
  "v_mfma_f32_16x16x32_bf16 v[16:19], a[48:51], v[120:123], v[16:19]\n" \
  "buffer_load_dwordx4 v[200:203], v59, s[36:39], s50 offen offset:1024 sc1\n" \
  "v_mfma_f32_16x16x32_bf16 v[20:23], a[52:55], v[120:123], v[20:23]\n" \
  "v_mfma_f32_16x16x32_bf16 v[24:27], a[56:59], v[120:123], v[24:27]\n" \
  "s_waitcnt vmcnt(16)\n" \
  "ds_read_b64_tr_b16 a[160:161], v64 offset:12288\n" \
  "ds_read_b64_tr_b16 a[162:163], v64 offset:12800\n" \
  "v_mfma_f32_16x16x32_bf16 v[28:31], a[60:63], v[120:123], v[28:31]\n" \
  "buffer_load_dwordx4 v[204:207], v59, s[36:39], s50 offen offset:2048 sc1\n" \
  "ds_read_b64_tr_b16 a[164:165], v65 offset:12288\n" \
  "s_waitcnt vmcnt(24)\n" \
  "v_mfma_f32_16x16x32_bf16 v[0:3], a[64:67], v[124:127], v[0:3]\n" \
  "s_add_u32 s59, s59, 0x10000\n" \
  "buffer_load_dword v63, v61, s[36:39], s59 offen offset:0 sc1\n" \
  "v_add_u32 v59, 819200, v59\n" \
  "v_mfma_f32_16x16x32_bf16 v[4:7], a[68:71], v[124:127], v[4:7]\n" \
  "ds_read_b64_tr_b16 a[166:167], v65 offset:12800\n" \
  "ds_read_b64_tr_b16 a[168:169], v66 offset:12288\n" \
  "buffer_store_short v253, v62, s[36:39], s52 offen offset:0 sc1\n" \
  "v_mfma_f32_16x16x32_bf16 v[8:11], a[72:75], v[124:127], v[8:11]\n" \
  "v_and_b32 v58, 0x3ff, v59\n" \
  "v_add_u32 v58, s99, v58\n" \
  "v_add_u32 v46, s98, v58\n" \
  "v_mfma_f32_16x16x32_bf16 v[12:15], a[76:79], v[124:127], v[12:15]\n" \
  "ds_read_b64 v[56:57], v46 offset:28680\n" \
  "ds_read_b64_tr_b16 a[170:171], v66 offset:12800\n" \
  "buffer_store_dwordx2 v[40:41], v61, s[36:39], s52 offen offset:0 sc1\n" \
  "v_mfma_f32_16x16x32_bf16 v[16:19], a[80:83], v[124:127], v[16:19]\n" \
  "buffer_load_dwordx4 v[72:75], v59, s[36:39], s68 offen offset:0 sc1\n" \
  "ds_read_b64_tr_b16 a[172:173], v67 offset:12288\n" \
  "ds_read_b64_tr_b16 a[174:175], v67 offset:12800\n" \
  "v_mfma_f32_16x16x32_bf16 v[20:23], a[84:87], v[124:127], v[20:23]\n" \
  "v_mfma_f32_16x16x32_bf16 v[24:27], a[88:91], v[124:127], v[24:27]\n" \
  "v_mfma_f32_16x16x32_bf16 v[28:31], a[92:95], v[124:127], v[28:31]\n" \
  "buffer_load_dwordx4 v[76:79], v59, s[36:39], s68 offen offset:1024 sc1\n" \
  "s_waitcnt vmcnt(27)\n" \
  "v_mfma_f32_16x16x32_bf16 v[0:3], a[96:99], v[192:195], v[0:3]\n" \
  "s_waitcnt lgkmcnt(0)\n" \
  "s_barrier\n" \
  "s_mov_b32 m0, s58\n" \
  "v_mfma_f32_16x16x32_bf16 v[4:7], a[100:103], v[192:195], v[4:7]\n" \
  "v_mfma_f32_16x16x32_bf16 v[8:11], a[104:107], v[192:195], v[8:11]\n" \
  "buffer_load_dwordx4 v[80:83], v59, s[36:39], s68 offen offset:2048 sc1\n" \
  "s_waitcnt vmcnt(19)\n" \
  "ds_read_b64_tr_b16 a[176:177], v64 offset:16384\n" \
  "v_mfma_f32_16x16x32_bf16 v[12:15], a[108:111], v[192:195], v[12:15]\n" \
  "ds_read_b64_tr_b16 a[178:179], v64 offset:16896\n" \
  "ds_read_b64_tr_b16 a[180:181], v65 offset:16384\n" \
  "v_mfma_f32_16x16x32_bf16 v[224:227], a[112:115], v[92:95], v[224:227]\n" \
  "ds_read_b64_tr_b16 a[182:183], v65 offset:16896\n" \
  "ds_read_b64_tr_b16 a[184:185], v66 offset:16384\n" \
  "v_mfma_f32_16x16x32_bf16 v[228:231], a[116:119], v[92:95], v[228:231]\n" \
  "buffer_load_dwordx4 v[84:87], v59, s[36:39], s69 offen offset:0 sc1\n" \
  "ds_read_b64_tr_b16 a[186:187], v66 offset:16896\n" \
  "ds_read_b64_tr_b16 a[188:189], v67 offset:16384\n" \
  "v_mfma_f32_16x16x32_bf16 v[232:235], a[120:123], v[92:95], v[232:235]\n" \
  "ds_read_b64_tr_b16 a[190:191], v67 offset:16896\n" \
  "v_mfma_f32_16x16x32_bf16 v[236:239], a[124:127], v[92:95], v[236:239]\n" \
  "v_mfma_f32_16x16x32_bf16 v[208:211], a[128:131], v[96:99], v[208:211]\n" \
  "buffer_load_dwordx4 v[88:91], v59, s[36:39], s69 offen offset:1024 sc1\n" \
  "v_mfma_f32_16x16x32_bf16 v[212:215], a[132:135], v[96:99], v[212:215]\n" \
  "v_mfma_f32_16x16x32_bf16 v[216:219], a[136:139], v[96:99], v[216:219]\n" \
  "v_mfma_f32_16x16x32_bf16 v[220:223], a[140:143], v[96:99], v[220:223]\n" \
  "buffer_load_dwordx4 v[92:95], v59, s[36:39], s69 offen offset:2048 sc1\n" \
  "v_mfma_f32_16x16x32_bf16 v[224:227], a[144:147], v[96:99], v[224:227]\n" \
  "v_mfma_f32_16x16x32_bf16 v[228:231], a[148:151], v[96:99], v[228:231]\n" \
  "v_mfma_f32_16x16x32_bf16 v[232:235], a[152:155], v[96:99], v[232:235]\n" \
  "v_mfma_f32_16x16x32_bf16 v[236:239], a[156:159], v[96:99], v[236:239]\n" \
  "buffer_load_dwordx4 v[96:99], v59, s[36:39], s70 offen offset:0 sc1\n" \
  "s_waitcnt vmcnt(22)\n" \
  "v_mfma_f32_16x16x32_bf16 v[208:211], a[160:163], v[100:103], v[208:211]\n" \
  "s_waitcnt vmcnt(18)\n" \
  "ds_read_b64_tr_b16 a[192:193], v64 offset:20480\n" \
  "ds_read_b64_tr_b16 a[194:195], v64 offset:20992\n" \
  "v_mfma_f32_16x16x32_bf16 v[212:215], a[164:167], v[100:103], v[212:215]\n" \
  "ds_read_b64_tr_b16 a[196:197], v65 offset:20480\n" \
  "ds_read_b64_tr_b16 a[198:199], v65 offset:20992\n" \
  "v_mfma_f32_16x16x32_bf16 v[216:219], a[168:171], v[100:103], v[216:219]\n" \
  "ds_read_b64_tr_b16 a[200:201], v66 offset:20480\n" \
  "ds_read_b64_tr_b16 a[202:203], v66 offset:20992\n" \
  "v_mfma_f32_16x16x32_bf16 v[220:223], a[172:175], v[100:103], v[220:223]\n" \
  "ds_read_b64_tr_b16 a[204:205], v67 offset:20480\n" \
  "ds_read_b64_tr_b16 a[206:207], v67 offset:20992\n" \
  "s_waitcnt lgkmcnt(8)\n" \
  "v_mfma_f32_16x16x32_bf16 v[224:227], a[176:179], v[100:103], v[224:227]\n" \
  "s_waitcnt vmcnt(13)\n" \
  "ds_read_b64_tr_b16 a[208:209], v64 offset:24576\n" \
  "ds_read_b64_tr_b16 a[210:211], v64 offset:25088\n" \
  "v_mfma_f32_16x16x32_bf16 v[228:231], a[180:183], v[100:103], v[228:231]\n" \
  "ds_read_b64_tr_b16 a[212:213], v65 offset:24576\n" \
  "ds_read_b64_tr_b16 a[214:215], v65 offset:25088\n" \
  "v_mfma_f32_16x16x32_bf16 v[232:235], a[184:187], v[100:103], v[232:235]\n" \
  "ds_read_b64_tr_b16 a[216:217], v66 offset:24576\n" \
  "ds_read_b64_tr_b16 a[218:219], v66 offset:25088\n" \
  "v_mfma_f32_16x16x32_bf16 v[236:239], a[188:191], v[100:103], v[236:239]\n" \
  "ds_read_b64_tr_b16 a[220:221], v67 offset:24576\n" \
  "ds_read_b64_tr_b16 a[222:223], v67 offset:25088\n" \
  "s_waitcnt lgkmcnt(8)\n" \
  "v_mfma_f32_16x16x32_bf16 v[208:211], a[192:195], v[104:107], v[208:211]\n" \
  "buffer_load_dwordx4 v[100:103], v59, s[36:39], s70 offen offset:1024 sc1\n" \
  "v_mfma_f32_16x16x32_bf16 v[212:215], a[196:199], v[104:107], v[212:215]\n" \
  "v_mfma_f32_16x16x32_bf16 v[216:219], a[200:203], v[104:107], v[216:219]\n" \
  "v_mfma_f32_16x16x32_bf16 v[220:223], a[204:207], v[104:107], v[220:223]\n" \
  "s_waitcnt lgkmcnt(0)\n" \
  "v_mfma_f32_16x16x32_bf16 v[224:227], a[208:211], v[104:107], v[224:227]\n" \
  "v_mfma_f32_16x16x32_bf16 v[228:231], a[212:215], v[104:107], v[228:231]\n" \
  "v_mfma_f32_16x16x32_bf16 v[232:235], a[216:219], v[104:107], v[232:235]\n" \
  "v_mfma_f32_16x16x32_bf16 v[236:239], a[220:223], v[104:107], v[236:239]\n" \
  "buffer_load_dwordx4 v[104:107], v59, s[36:39], s70 offen offset:2048 sc1\n" \
  "v_mfma_f32_16x16x32_bf16 v[16:19], a[112:115], v[192:195], v[16:19]\n" \
  "v_mfma_f32_16x16x32_bf16 v[20:23], a[116:119], v[192:195], v[20:23]\n" \
  "ds_write_addtid_b32 v208 offset:0\n" \
  "ds_write_addtid_b32 v209 offset:256\n" \
  "v_mfma_f32_16x16x32_bf16 v[24:27], a[120:123], v[192:195], v[24:27]\n" \
  "ds_write_addtid_b32 v210 offset:512\n" \
  "ds_write_addtid_b32 v211 offset:768\n" \
  "v_mfma_f32_16x16x32_bf16 v[28:31], a[124:127], v[192:195], v[28:31]\n" \
  "ds_write_addtid_b32 v212 offset:1024\n" \
  "ds_write_addtid_b32 v213 offset:1280\n" \
  "s_waitcnt vmcnt(14)\n" \
  "v_mfma_f32_16x16x32_bf16 v[0:3], a[128:131], v[196:199], v[0:3]\n" \
  "ds_write_addtid_b32 v214 offset:1536\n" \
  "ds_write_addtid_b32 v215 offset:1792\n" \
  "v_mfma_f32_16x16x32_bf16 v[4:7], a[132:135], v[196:199], v[4:7]\n" \
  "ds_write_addtid_b32 v216 offset:2048\n" \
  "ds_write_addtid_b32 v217 offset:2304\n" \
  "v_mfma_f32_16x16x32_bf16 v[8:11], a[136:139], v[196:199], v[8:11]\n" \
  "ds_write_addtid_b32 v218 offset:2560\n" \
  "ds_write_addtid_b32 v219 offset:2816\n" \
  "v_mfma_f32_16x16x32_bf16 v[12:15], a[140:143], v[196:199], v[12:15]\n" \
  "ds_write_addtid_b32 v220 offset:3072\n" \
  "ds_write_addtid_b32 v221 offset:3328\n" \
  "v_mfma_f32_16x16x32_bf16 v[16:19], a[144:147], v[196:199], v[16:19]\n" \
  "ds_write_addtid_b32 v222 offset:3584\n" \
  "ds_write_addtid_b32 v223 offset:3840\n" \
  "v_mfma_f32_16x16x32_bf16 v[20:23], a[148:151], v[196:199], v[20:23]\n" \
  "ds_write_addtid_b32 v224 offset:4096\n" \
  "ds_write_addtid_b32 v225 offset:4352\n" \
  "v_mfma_f32_16x16x32_bf16 v[24:27], a[152:155], v[196:199], v[24:27]\n" \
  "ds_write_addtid_b32 v226 offset:4608\n" \
  "ds_write_addtid_b32 v227 offset:4864\n" \
  "v_mfma_f32_16x16x32_bf16 v[28:31], a[156:159], v[196:199], v[28:31]\n" \
  "ds_write_addtid_b32 v228 offset:5120\n" \
  "ds_write_addtid_b32 v229 offset:5376\n" \
  "s_waitcnt vmcnt(13)\n" \
  "v_mfma_f32_16x16x32_bf16 v[0:3], a[160:163], v[200:203], v[0:3]\n" \
  "ds_write_addtid_b32 v230 offset:5632\n" \
  "ds_write_addtid_b32 v231 offset:5888\n" \
  "v_mfma_f32_16x16x32_bf16 v[4:7], a[164:167], v[200:203], v[4:7]\n" \
  "ds_write_addtid_b32 v232 offset:6144\n" \
  "ds_write_addtid_b32 v233 offset:6400\n" \
  "v_mfma_f32_16x16x32_bf16 v[8:11], a[168:171], v[200:203], v[8:11]\n" \
  "ds_write_addtid_b32 v234 offset:6656\n" \
  "ds_write_addtid_b32 v235 offset:6912\n" \
  "v_mfma_f32_16x16x32_bf16 v[12:15], a[172:175], v[200:203], v[12:15]\n" \
  "ds_write_addtid_b32 v236 offset:7168\n" \
  "ds_write_addtid_b32 v237 offset:7424\n" \
  "v_mfma_f32_16x16x32_bf16 v[16:19], a[176:179], v[200:203], v[16:19]\n" \
  "ds_write_addtid_b32 v238 offset:7680\n" \
  "ds_write_addtid_b32 v239 offset:7936\n" \
  "v_mfma_f32_16x16x32_bf16 v[20:23], a[180:183], v[200:203], v[20:23]\n" \
  "v_mfma_f32_16x16x32_bf16 v[24:27], a[184:187], v[200:203], v[24:27]\n" \
  "v_mfma_f32_16x16x32_bf16 v[28:31], a[188:191], v[200:203], v[28:31]\n" \
  "s_waitcnt vmcnt(12)\n" \
  "v_mfma_f32_16x16x32_bf16 v[0:3], a[192:195], v[204:207], v[0:3]\n" \
  "v_mfma_f32_16x16x32_bf16 v[4:7], a[196:199], v[204:207], v[4:7]\n" \
  "v_mfma_f32_16x16x32_bf16 v[8:11], a[200:203], v[204:207], v[8:11]\n" \
  "s_waitcnt lgkmcnt(0)\n" \
  "s_barrier\n" \
  "ds_read_b128 v[32:35], v58 offset:0\n" \
  "v_mfma_f32_16x16x32_bf16 v[12:15], a[204:207], v[204:207], v[12:15]\n" \
  "ds_read_b128 v[36:39], v58 offset:8192\n" \
  "ds_read_b128 v[40:43], v58 offset:16384\n" \
  "v_mfma_f32_16x16x32_bf16 v[16:19], a[208:211], v[204:207], v[16:19]\n" \
  "ds_read_b128 v[44:47], v58 offset:24576\n" \
  "ds_read_b128 v[48:51], v58 offset:4096\n" \
  "v_mfma_f32_16x16x32_bf16 v[20:23], a[212:215], v[204:207], v[20:23]\n" \
  "ds_read_b128 v[240:243], v58 offset:12288\n" \
  "ds_read_b128 v[244:247], v58 offset:20480\n" \
  "v_mfma_f32_16x16x32_bf16 v[24:27], a[216:219], v[204:207], v[24:27]\n" \
  "ds_read_b128 v[248:251], v58 offset:28672\n" \
  "v_mfma_f32_16x16x32_bf16 v[28:31], a[220:223], v[204:207], v[28:31]\n" \
  "s_waitcnt vmcnt(7)\n" \
  "v_mfma_f32_16x16x32_bf16 v[208:211], v[128:131], v[72:75], 0\n" \
  "s_mov_b64 exec, 1\n" \
  "buffer_store_dword v61, off, s[36:39], s54 offset:0 sc1\n" \
  "s_mov_b64 exec, -1\n" \
  "v_mfma_f32_16x16x32_bf16 v[212:215], v[132:135], v[72:75], 0\n" \
  "v_cmp_eq_u32 vcc, 0, v63\n" \
  "ds_write_addtid_b32 v0 offset:32768\n" \
  "v_mfma_f32_16x16x32_bf16 v[216:219], v[136:139], v[72:75], 0\n" \
  "ds_write_addtid_b32 v1 offset:33024\n" \
  "ds_write_addtid_b32 v2 offset:33280\n" \
  "v_lshlrev_b32 v52, 5, v59\n" \
  "v_mfma_f32_16x16x32_bf16 v[220:223], v[140:143], v[72:75], 0\n" \
  "s_and_b64 vcc, vcc, s[86:87]\n" \
  "s_cbranch_scc1 .LsbF_%=\n" \
  ".LbbF_%=:\n" \
  "buffer_load_dwordx4 v[108:111], v59, s[36:39], s48 offen offset:0 sc1\n" \
  "v_mfma_f32_16x16x32_bf16 v[224:227], v[144:147], v[72:75], 0\n" \
  "ds_write_addtid_b32 v3 offset:33536\n" \
  "ds_write_addtid_b32 v4 offset:33792\n" \
  "s_waitcnt lgkmcnt(9)\n" \
  "v_mfma_f32_16x16x32_bf16 v[228:231], v[148:151], v[72:75], 0\n" \
  "ds_write_addtid_b32 v5 offset:34048\n" \
  "ds_write_addtid_b32 v6 offset:34304\n" \
  "v_add_f32 v32, v32, v36\n" \
  "v_mfma_f32_16x16x32_bf16 v[232:235], v[152:155], v[72:75], 0\n" \
  "buffer_load_dwordx4 v[112:115], v59, s[36:39], s48 offen offset:1024 sc1\n" \
  "ds_write_addtid_b32 v7 offset:34560\n" \
  "ds_write_addtid_b32 v8 offset:34816\n" \
  "v_mfma_f32_16x16x32_bf16 v[236:239], v[156:159], v[72:75], 0\n" \
  "ds_write_addtid_b32 v9 offset:35072\n" \
  "ds_write_addtid_b32 v10 offset:35328\n" \
  "v_add_f32 v33, v33, v37\n" \
  "v_mfma_f32_16x16x32_bf16 v[208:211], v[160:163], v[76:79], v[208:211]\n" \
  "ds_write_addtid_b32 v11 offset:35584\n" \
  "ds_write_addtid_b32 v12 offset:35840\n" \
  "v_add_f32 v34, v34, v38\n" \
  "v_mfma_f32_16x16x32_bf16 v[212:215], v[164:167], v[76:79], v[212:215]\n" \
  "buffer_load_dwordx4 v[116:119], v59, s[36:39], s48 offen offset:2048 sc1\n" \
  "ds_write_addtid_b32 v13 offset:36096\n" \
  "ds_write_addtid_b32 v14 offset:36352\n" \
  "v_mfma_f32_16x16x32_bf16 v[216:219], v[168:171], v[76:79], v[216:219]\n" \
  "ds_write_addtid_b32 v15 offset:36608\n" \
  "ds_write_addtid_b32 v16 offset:36864\n" \
  "v_add_f32 v35, v35, v39\n" \
  "v_mfma_f32_16x16x32_bf16 v[220:223], v[172:175], v[76:79], v[220:223]\n" \
  "ds_write_addtid_b32 v17 offset:37120\n" \
  "ds_write_addtid_b32 v18 offset:37376\n" \
  "v_add_f32 v40, v40, v44\n" \
  "v_mfma_f32_16x16x32_bf16 v[224:227], v[176:179], v[76:79], v[224:227]\n" \
  "buffer_load_dwordx4 v[120:123], v59, s[36:39], s49 offen offset:0 sc1\n" \
  "ds_write_addtid_b32 v19 offset:37632\n" \
  "ds_write_addtid_b32 v20 offset:37888\n" \
  "v_mfma_f32_16x16x32_bf16 v[228:231], v[180:183], v[76:79], v[228:231]\n" \
  "ds_write_addtid_b32 v21 offset:38144\n" \
  "ds_write_addtid_b32 v22 offset:38400\n" \
  "v_add_f32 v41, v41, v45\n" \
  "v_mfma_f32_16x16x32_bf16 v[232:235], v[184:187], v[76:79], v[232:235]\n" \
  "ds_write_addtid_b32 v23 offset:38656\n" \
  "ds_write_addtid_b32 v24 offset:38912\n" \
  "v_add_f32 v42, v42, v46\n" \
  "v_mfma_f32_16x16x32_bf16 v[236:239], v[188:191], v[76:79], v[236:239]\n" \
  "buffer_load_dwordx4 v[124:127], v59, s[36:39], s49 offen offset:1024 sc1\n" \
  "ds_write_addtid_b32 v25 offset:39168\n" \
  "s_waitcnt vmcnt(11)\n" \
  "v_mfma_f32_16x16x32_bf16 v[208:211], a[0:3], v[80:83], v[208:211]\n" \
  "ds_write_addtid_b32 v26 offset:39424\n" \
  "ds_write_addtid_b32 v27 offset:39680\n" \
  "v_add_f32 v43, v43, v47\n" \
  "v_mfma_f32_16x16x32_bf16 v[212:215], a[4:7], v[80:83], v[212:215]\n" \
  "ds_write_addtid_b32 v28 offset:39936\n" \
  "ds_write_addtid_b32 v29 offset:40192\n" \
  "v_add_f32 v32, v32, v40\n" \
  "v_mfma_f32_16x16x32_bf16 v[216:219], a[8:11], v[80:83], v[216:219]\n" \
  "buffer_load_dwordx4 v[192:195], v59, s[36:39], s49 offen offset:2048 sc1\n" \
  "ds_write_addtid_b32 v30 offset:40448\n" \
  "ds_write_addtid_b32 v31 offset:40704\n" \
  "v_mfma_f32_16x16x32_bf16 v[220:223], a[12:15], v[80:83], v[220:223]\n" \
  "v_add_f32 v33, v33, v41\n" \
  "v_add_f32 v34, v34, v42\n" \
  "v_add_f32 v35, v35, v43\n" \
  "v_mfma_f32_16x16x32_bf16 v[224:227], a[16:19], v[80:83], v[224:227]\n" \
  "v_cvt_pk_bf16_f32 v36, v32, v33\n" \
  "v_cvt_pk_bf16_f32 v37, v34, v35\n" \
  "s_waitcnt lgkmcnt(15)\n" \
  "v_mfma_f32_16x16x32_bf16 v[228:231], a[20:23], v[80:83], v[228:231]\n" \
  "buffer_load_dwordx4 v[196:199], v59, s[36:39], s50 offen offset:0 sc1\n" \
  "v_add_f32 v48, v48, v240\n" \
  "v_add_f32 v49, v49, v241\n" \
  "v_mfma_f32_16x16x32_bf16 v[232:235], a[24:27], v[80:83], v[232:235]\n" \
  "v_add_f32 v50, v50, v242\n" \
  "v_add_f32 v51, v51, v243\n" \
  "v_add_f32 v244, v244, v248\n" \
  "v_mfma_f32_16x16x32_bf16 v[236:239], a[28:31], v[80:83], v[236:239]\n" \
  "v_add_f32 v245, v245, v249\n" \
  "v_add_f32 v246, v246, v250\n" \
  "v_add_f32 v247, v247, v251\n" \
  "v_mfma_f32_16x16x32_bf16 v[208:211], a[32:35], v[84:87], v[208:211]\n" \
  "buffer_load_dwordx4 v[200:203], v59, s[36:39], s50 offen offset:1024 sc1\n" \
  "s_waitcnt lgkmcnt(0)\n" \
  "s_barrier\n" \
  "v_mfma_f32_16x16x32_bf16 v[212:215], a[36:39], v[84:87], v[212:215]\n" \
  "v_add_f32 v48, v48, v244\n" \
  "v_add_f32 v49, v49, v245\n" \
  "v_add_f32 v50, v50, v246\n" \
  "v_mfma_f32_16x16x32_bf16 v[216:219], a[40:43], v[84:87], v[216:219]\n" \
  "v_and_b32 v52, 0x1800, v52\n" \
  "v_add_f32 v51, v51, v247\n" \
  "v_and_b32 v53, 0x30, v59\n" \
  "v_mfma_f32_16x16x32_bf16 v[220:223], a[44:47], v[84:87], v[220:223]\n" \
  "buffer_load_dwordx4 v[204:207], v59, s[36:39], s50 offen offset:2048 sc1\n" \
  "v_cvt_pk_bf16_f32 v38, v48, v49\n" \
  "v_lshl_or_b32 v52, v53, 1, v52\n" \
  "v_mfma_f32_16x16x32_bf16 v[224:227], a[48:51], v[84:87], v[224:227]\n" \
  "v_cvt_pk_bf16_f32 v39, v50, v51\n" \
  "buffer_load_dword v53, v52, s[36:39], s30 offen offset:0 sc1\n" \
  "v_mfma_f32_16x16x32_bf16 v[228:231], a[52:55], v[84:87], v[228:231]\n" \
  "v_mfma_f32_16x16x32_bf16 v[232:235], a[56:59], v[84:87], v[232:235]\n" \
  "buffer_store_dwordx4 v[36:39], v59, s[36:39], s74 offen offset:0 sc1\n" \
  "ds_read_b128 v[32:35], v58 offset:32768\n" \
  "s_nop 0\n" \
  "ds_read_b128 v[36:39], v58 offset:40960\n" \
  "v_mfma_f32_16x16x32_bf16 v[236:239], a[60:63], v[84:87], v[236:239]\n" \
  "ds_read_b128 v[40:43], v58 offset:49152\n" \
  "ds_read_b128 v[44:47], v58 offset:57344\n" \
  "s_waitcnt vmcnt(15)\n" \
  "v_mfma_f32_16x16x32_bf16 v[208:211], a[64:67], v[88:91], v[208:211]\n" \
  "ds_read_b128 v[48:51], v58 offset:36864\n" \
  "ds_read_b128 v[240:243], v58 offset:45056\n" \
  "v_mfma_f32_16x16x32_bf16 v[212:215], a[68:71], v[88:91], v[212:215]\n" \
  "ds_read_b128 v[244:247], v58 offset:53248\n" \
  "ds_read_b128 v[248:251], v58 offset:61440\n" \
  "v_mfma_f32_16x16x32_bf16 v[216:219], a[72:75], v[88:91], v[216:219]\n" \
  "v_mfma_f32_16x16x32_bf16 v[220:223], a[76:79], v[88:91], v[220:223]\n" \
  "v_mfma_f32_16x16x32_bf16 v[224:227], a[80:83], v[88:91], v[224:227]\n" \
  "v_mfma_f32_16x16x32_bf16 v[228:231], a[84:87], v[88:91], v[228:231]\n" \
  "v_mfma_f32_16x16x32_bf16 v[232:235], a[88:91], v[88:91], v[232:235]\n" \
  "v_mfma_f32_16x16x32_bf16 v[236:239], a[92:95], v[88:91], v[236:239]\n" \
  "s_waitcnt lgkmcnt(4)\n" \
  "v_add_f32 v32, v32, v36\n" \
  "v_add_f32 v33, v33, v37\n" \
  "v_mfma_f32_16x16x32_bf16 v[208:211], a[96:99], v[92:95], v[208:211]\n" \
  "v_add_f32 v34, v34, v38\n" \
  "v_add_f32 v35, v35, v39\n" \
  "v_add_f32 v40, v40, v44\n" \
  "v_mfma_f32_16x16x32_bf16 v[212:215], a[100:103], v[92:95], v[212:215]\n" \
  "v_add_f32 v41, v41, v45\n" \
  "v_add_f32 v42, v42, v46\n" \
  "v_add_f32 v43, v43, v47\n" \
  "v_mfma_f32_16x16x32_bf16 v[216:219], a[104:107], v[92:95], v[216:219]\n" \
  "v_add_f32 v32, v32, v40\n" \
  "v_add_f32 v33, v33, v41\n" \
  "v_add_f32 v34, v34, v42\n" \
  "v_mfma_f32_16x16x32_bf16 v[220:223], a[108:111], v[92:95], v[220:223]\n" \
  "v_add_f32 v35, v35, v43\n" \
  "v_cvt_pk_bf16_f32 v36, v32, v33\n" \
  "v_cvt_pk_bf16_f32 v37, v34, v35\n" \
  "v_mfma_f32_16x16x32_bf16 v[224:227], a[112:115], v[92:95], v[224:227]\n" \
  "s_waitcnt lgkmcnt(0)\n" \
  "v_add_f32 v48, v48, v240\n" \
  "v_add_f32 v49, v49, v241\n" \
  "v_mfma_f32_16x16x32_bf16 v[228:231], a[116:119], v[92:95], v[228:231]\n" \
  "v_add_f32 v50, v50, v242\n" \
  "v_add_f32 v51, v51, v243\n" \
  "v_add_f32 v244, v244, v248\n" \
  "v_mfma_f32_16x16x32_bf16 v[232:235], a[120:123], v[92:95], v[232:235]\n" \
  "v_add_f32 v245, v245, v249\n" \
  "v_add_f32 v246, v246, v250\n" \
  "v_add_f32 v247, v247, v251\n" \
  "v_mfma_f32_16x16x32_bf16 v[236:239], a[124:127], v[92:95], v[236:239]\n" \
  "v_add_f32 v48, v48, v244\n" \
  "v_add_f32 v49, v49, v245\n" \
  "s_waitcnt vmcnt(13)\n" \
  "v_mfma_f32_16x16x32_bf16 v[208:211], a[128:131], v[96:99], v[208:211]\n" \
  "v_add_f32 v50, v50, v246\n" \
  "v_add_f32 v51, v51, v247\n" \
  "v_cvt_pk_bf16_f32 v38, v48, v49\n" \
  "v_mfma_f32_16x16x32_bf16 v[212:215], a[132:135], v[96:99], v[212:215]\n" \
  "v_cvt_pk_bf16_f32 v39, v50, v51\n" \
  "v_add_u32 v59, 409600, v59\n" \
  "buffer_store_dwordx4 v[36:39], v59, s[36:39], s74 offen offset:0 sc1\n" \
  "v_mfma_f32_16x16x32_bf16 v[216:219], a[136:139], v[96:99], v[216:219]\n" \
  "v_mfma_f32_16x16x32_bf16 v[220:223], a[140:143], v[96:99], v[220:223]\n" \
  "v_mfma_f32_16x16x32_bf16 v[224:227], a[144:147], v[96:99], v[224:227]\n" \
  "v_mfma_f32_16x16x32_bf16 v[228:231], a[148:151], v[96:99], v[228:231]\n" \
  "v_mfma_f32_16x16x32_bf16 v[232:235], a[152:155], v[96:99], v[232:235]\n" \
  "v_mfma_f32_16x16x32_bf16 v[236:239], a[156:159], v[96:99], v[236:239]\n" \
  "v_mfma_f32_16x16x32_bf16 v[208:211], a[160:163], v[100:103], v[208:211]\n" \
  "v_mfma_f32_16x16x32_bf16 v[212:215], a[164:167], v[100:103], v[212:215]\n" \
  "v_mfma_f32_16x16x32_bf16 v[216:219], a[168:171], v[100:103], v[216:219]\n" \
  "v_mfma_f32_16x16x32_bf16 v[220:223], a[172:175], v[100:103], v[220:223]\n" \
  "v_mfma_f32_16x16x32_bf16 v[224:227], a[176:179], v[100:103], v[224:227]\n" \
  "v_mfma_f32_16x16x32_bf16 v[228:231], a[180:183], v[100:103], v[228:231]\n" \
  "v_mfma_f32_16x16x32_bf16 v[232:235], a[184:187], v[100:103], v[232:235]\n" \
  "v_mfma_f32_16x16x32_bf16 v[236:239], a[188:191], v[100:103], v[236:239]\n" \
  "s_waitcnt vmcnt(13)\n" \
  "v_mfma_f32_16x16x32_bf16 v[208:211], a[192:195], v[104:107], v[208:211]\n" \
  "v_mfma_f32_16x16x32_bf16 v[212:215], a[196:199], v[104:107], v[212:215]\n" \
  "v_mfma_f32_16x16x32_bf16 v[216:219], a[200:203], v[104:107], v[216:219]\n" \
  "v_mfma_f32_16x16x32_bf16 v[220:223], a[204:207], v[104:107], v[220:223]\n" \
  "v_mfma_f32_16x16x32_bf16 v[224:227], a[208:211], v[104:107], v[224:227]\n" \
  "v_mfma_f32_16x16x32_bf16 v[228:231], a[212:215], v[104:107], v[228:231]\n" \
  "v_mfma_f32_16x16x32_bf16 v[232:235], a[216:219], v[104:107], v[232:235]\n" \
  "v_mfma_f32_16x16x32_bf16 v[236:239], a[220:223], v[104:107], v[236:239]\n" \
  "s_branch .Le_%=\n" \
  ".Lzl_%=:\n" \
  "s_branch .Lzr_%=\n" \
  ".Lls_%=:\n" \
  "v_mfma_f32_16x16x32_bf16 v[20:23], v[64:67], a[0:3], 0\n" \
  "v_and_b32 v59, 0x3ff, v59\n" \
  "s_add_u32 s48, s68, 409600\n" \
  "s_add_u32 s49, s69, 409600\n" \
  "v_mfma_f32_16x16x32_bf16 v[24:27], v[64:67], a[4:7], 0\n" \
  "s_add_u32 s50, s70, 409600\n" \
  "s_add_u32 s100, s90, 73728\n" \
  "s_mov_b32 s35, 73728\n" \
  "v_mfma_f32_16x16x32_bf16 v[28:31], v[64:67], a[8:11], 0\n" \
  "ds_write_addtid_b32 v0 offset:0\n" \
  "ds_write_addtid_b32 v1 offset:544\n" \
  "v_mfma_f32_16x16x32_bf16 v[32:35], v[64:67], a[12:15], 0\n" \
  "ds_write_addtid_b32 v2 offset:256\n" \
  "ds_write_addtid_b32 v3 offset:800\n" \
  "v_mfma_f32_16x16x32_bf16 v[20:23], v[68:71], a[20:23], v[20:23]\n" \
  "ds_write_addtid_b32 v4 offset:1056\n" \
  "ds_write_addtid_b32 v5 offset:1600\n" \
  "v_mfma_f32_16x16x32_bf16 v[24:27], v[68:71], a[24:27], v[24:27]\n" \
  "ds_write_addtid_b32 v6 offset:1312\n" \
  "ds_write_addtid_b32 v7 offset:1856\n" \
  "v_mfma_f32_16x16x32_bf16 v[28:31], v[68:71], a[28:31], v[28:31]\n" \
  "ds_write_addtid_b32 v8 offset:2112\n" \
  "ds_write_addtid_b32 v9 offset:2656\n" \
  "v_mfma_f32_16x16x32_bf16 v[32:35], v[68:71], a[32:35], v[32:35]\n" \
  "ds_write_addtid_b32 v10 offset:2368\n" \
  "ds_write_addtid_b32 v11 offset:2912\n" \
  "s_waitcnt vmcnt(16)\n" \
  "v_mfma_f32_16x16x32_bf16 v[20:23], v[72:75], a[40:43], v[20:23]\n" \
  "s_waitcnt vmcnt(7)\n" \
  "v_cmp_eq_u32 vcc, 0, v63\n" \
  "v_add_u32 v71, s57, v59\n" \
  "v_mfma_f32_16x16x32_bf16 v[24:27], v[72:75], a[44:47], v[24:27]\n" \
  "ds_read_b32 v64, v71 offset:28672\n" \
  "ds_read_b64 v[68:69], v71 offset:29696\n" \
  "v_mfma_f32_16x16x32_bf16 v[28:31], v[72:75], a[48:51], v[28:31]\n" \
  "s_and_b64 vcc, vcc, s[86:87]\n" \
  "s_cbranch_scc1 .LsaS_%=\n" \
  ".LbaS_%=:\n" \
  "ds_write_addtid_b32 v12 offset:3168\n" \
  "v_mfma_f32_16x16x32_bf16 v[32:35], v[72:75], a[52:55], v[32:35]\n" \
  "ds_write_addtid_b32 v13 offset:3712\n" \
  "ds_write_addtid_b32 v14 offset:3424\n" \
  "s_waitcnt vmcnt(15)\n" \
  "v_mfma_f32_16x16x32_bf16 v[20:23], v[76:79], a[60:63], v[20:23]\n" \
  "buffer_load_dwordx4 v[72:75], v59, s[36:39], s68 offen offset:0 sc1\n" \
  "ds_write_addtid_b32 v15 offset:3968\n" \
  "ds_write_addtid_b32 v16 offset:4224\n" \
  "v_mfma_f32_16x16x32_bf16 v[24:27], v[76:79], a[64:67], v[24:27]\n" \
  "s_waitcnt lgkmcnt(5)\n" \
  "v_xor_b32 v65, 32, v64\n" \
  "v_xor_b32 v66, 64, v64\n" \
  "v_mfma_f32_16x16x32_bf16 v[28:31], v[76:79], a[68:71], v[28:31]\n" \
  "v_xor_b32 v67, 96, v64\n" \
  "v_add_u32 v70, 0xf800, v68\n" \
  "v_add_u32 v71, 0xf800, v69\n" \
  "v_mfma_f32_16x16x32_bf16 v[32:35], v[76:79], a[72:75], v[32:35]\n" \
  "ds_write_addtid_b32 v17 offset:4512\n" \
  "ds_write_addtid_b32 v18 offset:4800\n" \
  "v_mfma_f32_16x16x32_bf16 v[20:23], v[80:83], a[80:83], v[20:23]\n" \
  "buffer_load_dwordx4 v[76:79], v59, s[36:39], s68 offen offset:1024 sc1\n" \
  "ds_write_addtid_b32 v19 offset:5088\n" \
  "ds_read_b64_tr_b16 a[0:1], v64 offset:0\n" \
  "v_mfma_f32_16x16x32_bf16 v[24:27], v[80:83], a[84:87], v[24:27]\n" \
  "ds_read_b64_tr_b16 a[2:3], v64 offset:512\n" \
  "ds_read_b64_tr_b16 a[4:5], v65 offset:0\n" \
  "v_mfma_f32_16x16x32_bf16 v[28:31], v[80:83], a[88:91], v[28:31]\n" \
  "ds_read_b64_tr_b16 a[6:7], v65 offset:512\n" \
  "ds_read_b64_tr_b16 a[8:9], v66 offset:0\n" \
  "v_mfma_f32_16x16x32_bf16 v[32:35], v[80:83], a[92:95], v[32:35]\n" \
  "ds_read_b64_tr_b16 a[10:11], v66 offset:512\n" \
  "ds_read_b64_tr_b16 a[12:13], v67 offset:0\n" \
  "v_mfma_f32_16x16x32_bf16 v[20:23], v[84:87], a[100:103], v[20:23]\n" \
  "buffer_load_dwordx4 v[80:83], v59, s[36:39], s68 offen offset:2048 sc1\n" \
  "ds_read_b64_tr_b16 a[14:15], v67 offset:512\n" \
  "v_mfma_f32_16x16x32_bf16 v[24:27], v[84:87], a[104:107], v[24:27]\n" \
  "v_rcp_f32 v42, v42\n" \
  "v_mfma_f32_16x16x32_bf16 v[28:31], v[84:87], a[108:111], v[28:31]\n" \
  "s_waitcnt lgkmcnt(0)\n" \
  "s_barrier\n" \
  "v_mfma_f32_16x16x32_bf16 v[32:35], v[84:87], a[112:115], v[32:35]\n" \
  "v_rcp_f32 v43, v43\n" \
  "v_mfma_f32_16x16x32_bf16 v[20:23], v[88:91], a[120:123], v[20:23]\n" \
  "buffer_load_dwordx4 v[84:87], v59, s[36:39], s69 offen offset:0 sc1\n" \
  "s_add_u32 m0, s57, 0\n" \
  "v_mfma_f32_16x16x32_bf16 v[24:27], v[88:91], a[124:127], v[24:27]\n" \
  "ds_read_b64_tr_b16 a[16:17], v64 offset:4096\n" \
  "ds_read_b64_tr_b16 a[18:19], v64 offset:4608\n" \
  "v_mul_f32 v36, v36, v40\n" \
  "v_mfma_f32_16x16x32_bf16 v[28:31], v[88:91], a[128:131], v[28:31]\n" \
  "ds_read_b64_tr_b16 a[20:21], v65 offset:4096\n" \
  "ds_read_b64_tr_b16 a[22:23], v65 offset:4608\n" \
  "v_mul_f32 v37, v37, v41\n" \
  "v_mfma_f32_16x16x32_bf16 v[32:35], v[88:91], a[132:135], v[32:35]\n" \
  "buffer_load_dwordx4 v68, s[40:43], s60 offen offset:0 lds\n" \
  "ds_read_b64_tr_b16 a[24:25], v66 offset:4096\n" \
  "v_mfma_f32_16x16x32_bf16 v[20:23], v[92:95], a[140:143], v[20:23]\n" \
  "ds_read_b64_tr_b16 a[26:27], v66 offset:4608\n" \
  "ds_read_b64_tr_b16 a[28:29], v67 offset:4096\n" \
  "v_mul_f32 v38, v38, v42\n" \
  "v_mfma_f32_16x16x32_bf16 v[24:27], v[92:95], a[144:147], v[24:27]\n" \
  "ds_read_b64_tr_b16 a[30:31], v67 offset:4608\n" \
  "v_mul_f32 v39, v39, v43\n" \
  "v_cvt_pk_bf16_f32 v40, v36, v37\n" \
  "v_mfma_f32_16x16x32_bf16 v[28:31], v[92:95], a[148:151], v[28:31]\n" \
  "buffer_load_dwordx4 v69, s[40:43], s60 offen offset:1024 lds\n" \
  "v_cvt_pk_bf16_f32 v41, v38, v39\n" \
  "buffer_store_dwordx2 v[40:41], v61, s[36:39], s52 offen offset:0 sc1\n" \
  "v_mfma_f32_16x16x32_bf16 v[32:35], v[92:95], a[152:155], v[32:35]\n" \
  "ds_read_b128 v[36:39], v58 offset:0\n" \
  "ds_read_b128 v[40:43], v58 offset:9568\n" \
  "v_mfma_f32_16x16x32_bf16 v[20:23], v[96:99], a[160:163], v[20:23]\n" \
  "ds_read_b128 v[44:47], v58 offset:19136\n" \
  "ds_read_b128 v[48:51], v58 offset:28704\n" \
  "s_add_u32 s52, s52, 409600\n" \
  "v_mfma_f32_16x16x32_bf16 v[24:27], v[96:99], a[164:167], v[24:27]\n" \
  "buffer_load_dwordx4 v70, s[40:43], s60 offen offset:2048 lds\n" \
  "v_mfma_f32_16x16x32_bf16 v[28:31], v[96:99], a[168:171], v[28:31]\n" \
  "ds_read_b64_tr_b16 a[32:33], v64 offset:8192\n" \
  "ds_read_b64_tr_b16 a[34:35], v64 offset:8704\n" \
  "v_mfma_f32_16x16x32_bf16 v[32:35], v[96:99], a[172:175], v[32:35]\n" \
  "ds_read_b64_tr_b16 a[36:37], v65 offset:8192\n" \
  "ds_read_b64_tr_b16 a[38:39], v65 offset:8704\n" \
  "s_waitcnt vmcnt(14)\n" \
  "v_mfma_f32_16x16x32_bf16 v[20:23], v[100:103], a[180:183], v[20:23]\n" \
  "buffer_load_dwordx4 v71, s[40:43], s60 offen offset:3072 lds\n" \
  "ds_read_b64_tr_b16 a[40:41], v66 offset:8192\n" \
  "ds_read_b64_tr_b16 a[42:43], v66 offset:8704\n" \
  "v_mfma_f32_16x16x32_bf16 v[24:27], v[100:103], a[184:187], v[24:27]\n" \
  "ds_read_b64_tr_b16 a[44:45], v67 offset:8192\n" \
  "ds_read_b64_tr_b16 a[46:47], v67 offset:8704\n" \
  "v_mfma_f32_16x16x32_bf16 v[28:31], v[100:103], a[188:191], v[28:31]\n" \
  "v_mfma_f32_16x16x32_bf16 v[32:35], v[100:103], a[192:195], v[32:35]\n" \
  "buffer_load_dwordx4 v[88:91], v59, s[36:39], s69 offen offset:1024 sc1\n" \
  "s_waitcnt lgkmcnt(10)\n" \
  "s_waitcnt vmcnt(15)\n" \
  "v_mfma_f32_16x16x32_bf16 v[20:23], v[104:107], a[200:203], v[20:23]\n" \
  "v_add_f32 v36, v36, v40\n" \
  "v_add_f32 v37, v37, v41\n" \
  "v_add_f32 v38, v38, v42\n" \
  "v_mfma_f32_16x16x32_bf16 v[24:27], v[104:107], a[204:207], v[24:27]\n" \
  "v_add_f32 v39, v39, v43\n" \
  "v_add_f32 v36, v36, v52\n" \
  "v_add_f32 v37, v37, v53\n" \
  "v_mfma_f32_16x16x32_bf16 v[28:31], v[104:107], a[208:211], v[28:31]\n" \
  "buffer_load_dwordx4 v[92:95], v59, s[36:39], s69 offen offset:2048 sc1\n" \
  "s_add_u32 m0, s57, 4096\n" \
  "v_mfma_f32_16x16x32_bf16 v[32:35], v[104:107], a[212:215], v[32:35]\n" \
  "v_add_f32 v38, v38, v54\n" \
  "v_add_f32 v39, v39, v55\n" \
  "s_waitcnt vmcnt(15)\n" \
  "v_mfma_f32_16x16x32_bf16 v[20:23], v[108:111], a[220:223], v[20:23]\n" \
  "ds_read_b64_tr_b16 a[48:49], v64 offset:12288\n" \
  "ds_read_b64_tr_b16 a[50:51], v64 offset:12800\n" \
  "s_waitcnt lgkmcnt(10)\n" \
  "v_mfma_f32_16x16x32_bf16 v[24:27], v[108:111], a[224:227], v[24:27]\n" \
  "buffer_load_dwordx4 v68, s[40:43], s61 offen offset:0 lds\n" \
  "ds_read_b64_tr_b16 a[52:53], v65 offset:12288\n" \
  "ds_read_b64_tr_b16 a[54:55], v65 offset:12800\n" \
  "v_mfma_f32_16x16x32_bf16 v[28:31], v[108:111], a[228:231], v[28:31]\n" \
  "ds_read_b64_tr_b16 a[56:57], v66 offset:12288\n" \
  "ds_read_b64_tr_b16 a[58:59], v66 offset:12800\n" \
  "v_add_f32 v44, v44, v48\n" \
  "v_mfma_f32_16x16x32_bf16 v[32:35], v[108:111], a[232:235], v[32:35]\n" \
  "ds_read_b64_tr_b16 a[60:61], v67 offset:12288\n" \
  "ds_read_b64_tr_b16 a[62:63], v67 offset:12800\n" \
  "s_waitcnt vmcnt(15)\n" \
  "v_mfma_f32_16x16x32_bf16 v[20:23], v[112:115], a[240:243], v[20:23]\n" \
  "buffer_load_dwordx4 v69, s[40:43], s61 offen offset:1024 lds\n" \
  "v_add_f32 v45, v45, v49\n" \
  "v_add_f32 v46, v46, v50\n" \
  "v_mfma_f32_16x16x32_bf16 v[24:27], v[112:115], a[244:247], v[24:27]\n" \
  "v_add_f32 v47, v47, v51\n" \
  "v_add_f32 v36, v36, v44\n" \
  "v_add_f32 v37, v37, v45\n" \
  "v_mfma_f32_16x16x32_bf16 v[28:31], v[112:115], a[248:251], v[28:31]\n" \
  "v_add_f32 v38, v38, v46\n" \
  "v_add_f32 v39, v39, v47\n" \
  "v_mul_f32 v40, v36, v36\n" \
  "v_mfma_f32_16x16x32_bf16 v[32:35], v[112:115], a[252:255], v[32:35]\n" \
  "buffer_load_dwordx4 v70, s[40:43], s61 offen offset:2048 lds\n" \
  "v_mul_f32 v41, v37, v37\n" \
  "s_waitcnt vmcnt(16)\n" \
  "v_mfma_f32_16x16x32_bf16 v[20:23], v[116:119], v[196:199], v[20:23]\n" \
  "v_mul_f32 v42, v38, v38\n" \
  "v_mul_f32 v43, v39, v39\n" \
  "v_fma_f32 v40, v40, s55, v57\n" \
  "v_mfma_f32_16x16x32_bf16 v[24:27], v[116:119], v[200:203], v[24:27]\n" \
  "v_fma_f32 v41, v41, s55, v57\n" \
  "v_fma_f32 v42, v42, s55, v57\n" \
  "v_fma_f32 v43, v43, s55, v57\n" \
  "v_mfma_f32_16x16x32_bf16 v[28:31], v[116:119], v[204:207], v[28:31]\n" \
  "buffer_load_dwordx4 v71, s[40:43], s61 offen offset:3072 lds\n" \
  "ds_read_b64_tr_b16 a[64:65], v64 offset:16384\n" \
  "ds_read_b64_tr_b16 a[66:67], v64 offset:16896\n" \
  "v_mfma_f32_16x16x32_bf16 v[32:35], v[116:119], v[208:211], v[32:35]\n" \
  "ds_read_b64_tr_b16 a[68:69], v65 offset:16384\n" \
  "ds_read_b64_tr_b16 a[70:71], v65 offset:16896\n" \
  "s_waitcnt vmcnt(16)\n" \
  "v_mfma_f32_16x16x32_bf16 v[20:23], v[120:123], v[216:219], v[20:23]\n" \
  "ds_read_b64_tr_b16 a[72:73], v66 offset:16384\n" \
  "ds_read_b64_tr_b16 a[74:75], v66 offset:16896\n" \
  "v_mul_f32 v40, v36, v40\n" \
  "v_mfma_f32_16x16x32_bf16 v[24:27], v[120:123], v[220:223], v[24:27]\n" \
  "buffer_load_dwordx4 v[108:111], v59, s[36:39], s48 offen offset:0 sc1\n" \
  "ds_read_b64_tr_b16 a[76:77], v67 offset:16384\n" \
  "ds_read_b64_tr_b16 a[78:79], v67 offset:16896\n" \
  "v_mfma_f32_16x16x32_bf16 v[28:31], v[120:123], v[224:227], v[28:31]\n" \
  "v_mul_f32 v41, v37, v41\n" \
  "v_mul_f32 v42, v38, v42\n" \
  "v_mul_f32 v43, v39, v43\n" \
  "v_mfma_f32_16x16x32_bf16 v[32:35], v[120:123], v[228:231], v[32:35]\n" \
  "v_exp_f32 v40, v40\n" \
  "s_waitcnt vmcnt(16)\n" \
  "v_mfma_f32_16x16x32_bf16 v[20:23], v[124:127], v[236:239], v[20:23]\n" \
  "buffer_load_dwordx4 v[112:115], v59, s[36:39], s48 offen offset:1024 sc1\n" \
  "v_exp_f32 v41, v41\n" \
  "v_mfma_f32_16x16x32_bf16 v[24:27], v[124:127], v[240:243], v[24:27]\n" \
  "v_exp_f32 v42, v42\n" \
  "ds_read_b32 v48, v60 offset:0\n" \
  "v_mfma_f32_16x16x32_bf16 v[28:31], v[124:127], v[244:247], v[28:31]\n" \
  "v_exp_f32 v43, v43\n" \
  "v_add_f32 v40, 1.0, v40\n" \
  "v_mfma_f32_16x16x32_bf16 v[32:35], v[124:127], v[248:251], v[32:35]\n" \
  "buffer_load_dwordx4 v[116:119], v59, s[36:39], s48 offen offset:2048 sc1\n" \
  "s_mov_b32 m0, s56\n" \
  "s_waitcnt vmcnt(17)\n" \
  "v_mfma_f32_16x16x32_bf16 v[208:211], v[128:131], v[72:75], 0\n" \
  "ds_read_b64_tr_b16 a[80:81], v64 offset:20480\n" \
  "ds_read_b64_tr_b16 a[82:83], v64 offset:20992\n" \
  "v_add_f32 v41, 1.0, v41\n" \
  "v_mfma_f32_16x16x32_bf16 v[212:215], v[132:135], v[72:75], 0\n" \
  "ds_write_addtid_b32 v20 offset:5344\n" \
  "ds_write_addtid_b32 v21 offset:5888\n" \
  "v_add_f32 v42, 1.0, v42\n" \
  "v_mfma_f32_16x16x32_bf16 v[216:219], v[136:139], v[72:75], 0\n" \
  "buffer_load_dwordx4 v[120:123], v59, s[36:39], s49 offen offset:0 sc1\n" \
  "ds_write_addtid_b32 v22 offset:5600\n" \
  "ds_write_addtid_b32 v23 offset:6144\n" \
  "v_mfma_f32_16x16x32_bf16 v[220:223], v[140:143], v[72:75], 0\n" \
  "ds_write_addtid_b32 v24 offset:6400\n" \
  "ds_write_addtid_b32 v25 offset:6944\n" \
  "v_add_f32 v43, 1.0, v43\n" \
  "v_mfma_f32_16x16x32_bf16 v[224:227], v[144:147], v[72:75], 0\n" \
  "ds_write_addtid_b32 v26 offset:6656\n" \
  "ds_write_addtid_b32 v27 offset:7200\n" \
  "v_rcp_f32 v40, v40\n" \
  "v_mfma_f32_16x16x32_bf16 v[228:231], v[148:151], v[72:75], 0\n" \
  "buffer_load_dwordx4 v[124:127], v59, s[36:39], s49 offen offset:1024 sc1\n" \
  "ds_write_addtid_b32 v28 offset:7456\n" \
  "ds_write_addtid_b32 v29 offset:8000\n" \
  "v_mfma_f32_16x16x32_bf16 v[232:235], v[152:155], v[72:75], 0\n" \
  "ds_write_addtid_b32 v30 offset:7712\n" \
  "ds_write_addtid_b32 v31 offset:8256\n" \
  "v_rcp_f32 v41, v41\n" \
  "v_mfma_f32_16x16x32_bf16 v[236:239], v[156:159], v[72:75], 0\n" \
  "ds_write_addtid_b32 v32 offset:8512\n" \
  "ds_write_addtid_b32 v33 offset:9056\n" \
  "s_waitcnt vmcnt(18)\n" \
  "v_mfma_f32_16x16x32_bf16 v[208:211], v[160:163], v[76:79], v[208:211]\n" \
  "v_rcp_f32 v42, v42\n" \
  "v_rcp_f32 v43, v43\n" \
  "v_mul_f32 v36, v36, v40\n" \
  "v_mul_f32 v37, v37, v41\n" \
  "v_mul_f32 v38, v38, v42\n" \
  "v_mul_f32 v39, v39, v43\n" \
  "v_cvt_pk_bf16_f32 v40, v36, v37\n" \
  "v_cvt_pk_bf16_f32 v41, v38, v39\n" \
  "buffer_store_dwordx2 v[40:41], v61, s[36:39], s52 offen offset:0 sc1\n" \
  "v_mfma_f32_16x16x32_bf16 v[212:215], v[164:167], v[76:79], v[212:215]\n" \
  "buffer_load_dwordx4 v[192:195], v59, s[36:39], s49 offen offset:2048 sc1\n" \
  "ds_write_addtid_b32 v34 offset:8768\n" \
  "ds_write_addtid_b32 v35 offset:9312\n" \
  "v_mfma_f32_16x16x32_bf16 v[216:219], v[168:171], v[76:79], v[216:219]\n" \
  "ds_read_b64_tr_b16 a[84:85], v65 offset:20480\n" \
  "ds_read_b64_tr_b16 a[86:87], v65 offset:20992\n" \
  "v_mfma_f32_16x16x32_bf16 v[220:223], v[172:175], v[76:79], v[220:223]\n" \
  "ds_read_b64_tr_b16 a[88:89], v66 offset:20480\n" \
  "ds_read_b64_tr_b16 a[90:91], v66 offset:20992\n" \
  "v_mfma_f32_16x16x32_bf16 v[224:227], v[176:179], v[76:79], v[224:227]\n" \
  "buffer_load_dwordx4 v[96:99], v59, s[36:39], s70 offen offset:0 sc1\n" \
  "s_waitcnt lgkmcnt(15)\n" \
  "s_add_u32 m0, s57, 8192\n" \
  "v_mfma_f32_16x16x32_bf16 v[228:231], v[180:183], v[76:79], v[228:231]\n" \
  "ds_read_b64_tr_b16 a[92:93], v67 offset:20480\n" \
  "ds_read_b64_tr_b16 a[94:95], v67 offset:20992\n" \
  "v_mfma_f32_16x16x32_bf16 v[232:235], v[184:187], v[76:79], v[232:235]\n" \
  "ds_read_b64_tr_b16 a[96:97], v64 offset:24576\n" \
  "ds_read_b64_tr_b16 a[98:99], v64 offset:25088\n" \
  "v_mfma_f32_16x16x32_bf16 v[236:239], v[188:191], v[76:79], v[236:239]\n" \
  "buffer_load_dwordx4 v68, s[40:43], s62 offen offset:0 lds\n" \
  "s_waitcnt lgkmcnt(15)\n" \
  "s_waitcnt vmcnt(21)\n" \
  "v_mfma_f32_16x16x32_bf16 v[208:211], a[0:3], v[80:83], v[208:211]\n" \
  "s_waitcnt lgkmcnt(0)\n" \
  "s_barrier\n" \
  "ds_read_b64_tr_b16 a[100:101], v65 offset:24576\n" \
  "v_mfma_f32_16x16x32_bf16 v[212:215], a[4:7], v[80:83], v[212:215]\n" \
  "ds_read_b64_tr_b16 a[102:103], v65 offset:25088\n" \
  "ds_read_b64_tr_b16 a[104:105], v66 offset:24576\n" \
  "v_mfma_f32_16x16x32_bf16 v[216:219], a[8:11], v[80:83], v[216:219]\n" \
  "buffer_load_dwordx4 v69, s[40:43], s62 offen offset:1024 lds\n" \
  "ds_read_b64_tr_b16 a[106:107], v66 offset:25088\n" \
  "ds_read_b64_tr_b16 a[108:109], v67 offset:24576\n" \
  "v_mfma_f32_16x16x32_bf16 v[220:223], a[12:15], v[80:83], v[220:223]\n" \
  "ds_read_b64_tr_b16 a[110:111], v67 offset:25088\n" \
  "ds_read_b32 v49, v60 offset:9568\n" \
  "v_mfma_f32_16x16x32_bf16 v[224:227], a[16:19], v[80:83], v[224:227]\n" \
  "ds_read_b32 v50, v60 offset:19136\n" \
  "ds_read_b32 v51, v60 offset:28704\n" \
  "v_mfma_f32_16x16x32_bf16 v[228:231], a[20:23], v[80:83], v[228:231]\n" \
  "buffer_load_dwordx4 v70, s[40:43], s62 offen offset:2048 lds\n" \
  "v_mfma_f32_16x16x32_bf16 v[232:235], a[24:27], v[80:83], v[232:235]\n" \
  "s_waitcnt vmcnt(17)\n" \
  "ds_read_b64_tr_b16 a[112:113], v64 offset:0\n" \
  "ds_read_b64_tr_b16 a[114:115], v64 offset:512\n" \
  "v_mfma_f32_16x16x32_bf16 v[236:239], a[28:31], v[80:83], v[236:239]\n" \
  "ds_read_b64_tr_b16 a[116:117], v65 offset:0\n" \
  "v_mfma_f32_16x16x32_bf16 v[208:211], a[32:35], v[84:87], v[208:211]\n" \
  "buffer_load_dwordx4 v71, s[40:43], s62 offen offset:3072 lds\n" \
  "s_add_u32 m0, s57, 12288\n" \
  "v_mfma_f32_16x16x32_bf16 v[212:215], a[36:39], v[84:87], v[212:215]\n" \
  "ds_read_b64_tr_b16 a[118:119], v65 offset:512\n" \
  "ds_read_b64_tr_b16 a[120:121], v66 offset:0\n" \
  "v_mfma_f32_16x16x32_bf16 v[216:219], a[40:43], v[84:87], v[216:219]\n" \
  "ds_read_b64_tr_b16 a[122:123], v66 offset:512\n" \
  "ds_read_b64_tr_b16 a[124:125], v67 offset:0\n" \
  "s_waitcnt lgkmcnt(7)\n" \
  "v_mfma_f32_16x16x32_bf16 v[220:223], a[44:47], v[84:87], v[220:223]\n" \
  "buffer_load_dwordx4 v68, s[40:43], s63 offen offset:0 lds\n" \
  "ds_read_b64_tr_b16 a[126:127], v67 offset:512\n" \
  "v_mfma_f32_16x16x32_bf16 v[224:227], a[48:51], v[84:87], v[224:227]\n" \
  "v_add_f32 v48, v48, v49\n" \
  "v_add_f32 v50, v50, v51\n" \
  "v_add_f32 v48, v48, v56\n" \
  "v_mfma_f32_16x16x32_bf16 v[228:231], a[52:55], v[84:87], v[228:231]\n" \
  "v_add_f32 v48, v48, v50\n" \
  "v_mul_f32 v49, v48, v48\n" \
  "v_fma_f32 v49, v49, s55, v57\n" \
  "v_mfma_f32_16x16x32_bf16 v[232:235], a[56:59], v[84:87], v[232:235]\n" \
  "buffer_load_dwordx4 v69, s[40:43], s63 offen offset:1024 lds\n" \
  "v_mul_f32 v49, v48, v49\n" \
  "v_mfma_f32_16x16x32_bf16 v[236:239], a[60:63], v[84:87], v[236:239]\n" \
  "s_waitcnt vmcnt(19)\n" \
  "v_mfma_f32_16x16x32_bf16 v[208:211], a[64:67], v[88:91], v[208:211]\n" \
  "v_exp_f32 v49, v49\n" \
  "v_mfma_f32_16x16x32_bf16 v[212:215], a[68:71], v[88:91], v[212:215]\n" \
  "buffer_load_dwordx4 v70, s[40:43], s63 offen offset:2048 lds\n" \
  "v_add_f32 v49, 1.0, v49\n" \
  "v_mfma_f32_16x16x32_bf16 v[216:219], a[72:75], v[88:91], v[216:219]\n" \
  "v_rcp_f32 v49, v49\n" \
  "v_mfma_f32_16x16x32_bf16 v[220:223], a[76:79], v[88:91], v[220:223]\n" \
  "v_mul_f32 v48, v48, v49\n" \
  "v_cvt_pk_bf16_f32 v49, v48, v48\n" \
  "v_mfma_f32_16x16x32_bf16 v[224:227], a[80:83], v[88:91], v[224:227]\n" \
  "buffer_load_dwordx4 v71, s[40:43], s63 offen offset:3072 lds\n" \
  "buffer_store_short v49, v62, s[36:39], s52 offen offset:0 sc1\n" \
  "ds_read_b128 v[36:39], v58 offset:5344\n" \
  "v_mfma_f32_16x16x32_bf16 v[228:231], a[84:87], v[88:91], v[228:231]\n" \
  "ds_read_b128 v[40:43], v58 offset:14912\n" \
  "ds_read_b128 v[44:47], v58 offset:24480\n" \
  "s_add_u32 s52, s52, 409600\n" \
  "v_mfma_f32_16x16x32_bf16 v[232:235], a[88:91], v[88:91], v[232:235]\n" \
  "ds_read_b128 v[48:51], v58 offset:34048\n" \
  "v_mfma_f32_16x16x32_bf16 v[236:239], a[92:95], v[88:91], v[236:239]\n" \
  "buffer_load_dwordx4 v[100:103], v59, s[36:39], s70 offen offset:1024 sc1\n" \
  "s_waitcnt vmcnt(22)\n" \
  "v_mfma_f32_16x16x32_bf16 v[208:211], a[96:99], v[92:95], v[208:211]\n" \
  "s_add_u32 m0, s57, 16384\n" \
  "s_waitcnt vmcnt(18)\n" \
  "v_mfma_f32_16x16x32_bf16 v[212:215], a[100:103], v[92:95], v[212:215]\n" \
  "ds_read_b64_tr_b16 a[128:129], v64 offset:4096\n" \
  "ds_read_b64_tr_b16 a[130:131], v64 offset:4608\n" \
  "v_mfma_f32_16x16x32_bf16 v[216:219], a[104:107], v[92:95], v[216:219]\n" \
  "buffer_load_dwordx4 v68, s[40:43], s64 offen offset:0 lds\n" \
  "ds_read_b64_tr_b16 a[132:133], v65 offset:4096\n" \
  "ds_read_b64_tr_b16 a[134:135], v65 offset:4608\n" \
  "v_mfma_f32_16x16x32_bf16 v[220:223], a[108:111], v[92:95], v[220:223]\n" \
  "ds_read_b64_tr_b16 a[136:137], v66 offset:4096\n" \
  "ds_read_b64_tr_b16 a[138:139], v66 offset:4608\n" \
  "s_waitcnt vmcnt(18)\n" \
  "v_mfma_f32_16x16x32_bf16 v[0:3], v[128:131], v[108:111], 0\n" \
  "ds_read_b64_tr_b16 a[140:141], v67 offset:4096\n" \
  "ds_read_b64_tr_b16 a[142:143], v67 offset:4608\n" \
  "v_mfma_f32_16x16x32_bf16 v[4:7], v[132:135], v[108:111], 0\n" \
  "buffer_load_dwordx4 v69, s[40:43], s64 offen offset:1024 lds\n" \
  "s_waitcnt lgkmcnt(10)\n" \
  "v_add_f32 v36, v36, v40\n" \
  "v_mfma_f32_16x16x32_bf16 v[8:11], v[136:139], v[108:111], 0\n" \
  "v_add_f32 v37, v37, v41\n" \
  "v_add_f32 v38, v38, v42\n" \
  "v_add_f32 v39, v39, v43\n" \
  "v_mfma_f32_16x16x32_bf16 v[12:15], v[140:143], v[108:111], 0\n" \
  "v_add_f32 v36, v36, v52\n" \
  "v_add_f32 v37, v37, v53\n" \
  "v_add_f32 v38, v38, v54\n" \
  "v_mfma_f32_16x16x32_bf16 v[16:19], v[144:147], v[108:111], 0\n" \
  "buffer_load_dwordx4 v70, s[40:43], s64 offen offset:2048 lds\n" \
  "v_add_f32 v39, v39, v55\n" \
  "s_waitcnt lgkmcnt(8)\n" \
  "v_mfma_f32_16x16x32_bf16 v[20:23], v[148:151], v[108:111], 0\n" \
  "v_add_f32 v44, v44, v48\n" \
  "v_add_f32 v45, v45, v49\n" \
  "v_add_f32 v46, v46, v50\n" \
  "v_mfma_f32_16x16x32_bf16 v[24:27], v[152:155], v[108:111], 0\n" \
  "v_add_f32 v47, v47, v51\n" \
  "v_add_f32 v36, v36, v44\n" \
  "v_add_f32 v37, v37, v45\n" \
  "v_mfma_f32_16x16x32_bf16 v[28:31], v[156:159], v[108:111], 0\n" \
  "buffer_load_dwordx4 v71, s[40:43], s64 offen offset:3072 lds\n" \
  "v_add_f32 v38, v38, v46\n" \
  "s_waitcnt vmcnt(20)\n" \
  "v_mfma_f32_16x16x32_bf16 v[0:3], v[160:163], v[112:115], v[0:3]\n" \
  "s_add_u32 m0, s57, 20480\n" \
  "v_add_f32 v39, v39, v47\n" \
  "v_mfma_f32_16x16x32_bf16 v[4:7], v[164:167], v[112:115], v[4:7]\n" \
  "v_mul_f32 v40, v36, v36\n" \
  "v_mul_f32 v41, v37, v37\n" \
  "v_mul_f32 v42, v38, v38\n" \
  "v_mfma_f32_16x16x32_bf16 v[8:11], v[168:171], v[112:115], v[8:11]\n" \
  "buffer_load_dwordx4 v68, s[40:43], s65 offen offset:0 lds\n" \
  "v_mul_f32 v43, v39, v39\n" \
  "v_fma_f32 v40, v40, s55, v57\n" \
  "v_mfma_f32_16x16x32_bf16 v[12:15], v[172:175], v[112:115], v[12:15]\n" \
  "v_fma_f32 v41, v41, s55, v57\n" \
  "v_fma_f32 v42, v42, s55, v57\n" \
  "v_fma_f32 v43, v43, s55, v57\n" \
  "v_mfma_f32_16x16x32_bf16 v[16:19], v[176:179], v[112:115], v[16:19]\n" \
  "v_mul_f32 v40, v36, v40\n" \
  "v_mul_f32 v41, v37, v41\n" \
  "v_mul_f32 v42, v38, v42\n" \
  "v_mfma_f32_16x16x32_bf16 v[20:23], v[180:183], v[112:115], v[20:23]\n" \
  "buffer_load_dwordx4 v69, s[40:43], s65 offen offset:1024 lds\n" \
  "v_mul_f32 v43, v39, v43\n" \
  "v_mfma_f32_16x16x32_bf16 v[24:27], v[184:187], v[112:115], v[24:27]\n" \
  "v_exp_f32 v40, v40\n" \
  "v_mfma_f32_16x16x32_bf16 v[28:31], v[188:191], v[112:115], v[28:31]\n" \
  "v_exp_f32 v41, v41\n" \
  "s_waitcnt vmcnt(21)\n" \
  "v_mfma_f32_16x16x32_bf16 v[0:3], a[0:3], v[116:119], v[0:3]\n" \
  "buffer_load_dwordx4 v70, s[40:43], s65 offen offset:2048 lds\n" \
  "v_exp_f32 v42, v42\n" \
  "v_mfma_f32_16x16x32_bf16 v[4:7], a[4:7], v[116:119], v[4:7]\n" \
  "v_exp_f32 v43, v43\n" \
  "v_add_f32 v40, 1.0, v40\n" \
  "v_add_f32 v41, 1.0, v41\n" \
  "v_mfma_f32_16x16x32_bf16 v[8:11], a[8:11], v[116:119], v[8:11]\n" \
  "v_add_f32 v42, 1.0, v42\n" \
  "v_add_f32 v43, 1.0, v43\n" \
  "v_rcp_f32 v40, v40\n" \
  "v_mfma_f32_16x16x32_bf16 v[12:15], a[12:15], v[116:119], v[12:15]\n" \
  "buffer_load_dwordx4 v71, s[40:43], s65 offen offset:3072 lds\n" \
  "v_rcp_f32 v41, v41\n" \
  "v_mfma_f32_16x16x32_bf16 v[16:19], a[16:19], v[116:119], v[16:19]\n" \
  "v_rcp_f32 v42, v42\n" \
  "v_mfma_f32_16x16x32_bf16 v[20:23], a[20:23], v[116:119], v[20:23]\n" \
  "v_rcp_f32 v43, v43\n" \
  "v_mul_f32 v36, v36, v40\n" \
  "v_mfma_f32_16x16x32_bf16 v[24:27], a[24:27], v[116:119], v[24:27]\n" \
  "v_mul_f32 v37, v37, v41\n" \
  "v_mul_f32 v38, v38, v42\n" \
  "v_mul_f32 v39, v39, v43\n" \
  "v_cvt_pk_bf16_f32 v40, v36, v37\n" \
  "v_cvt_pk_bf16_f32 v41, v38, v39\n" \
  "v_mfma_f32_16x16x32_bf16 v[28:31], a[28:31], v[116:119], v[28:31]\n" \
  "buffer_load_dwordx4 v[104:107], v59, s[36:39], s70 offen offset:2048 sc1\n" \
  "v_and_b32 v58, 0x3ff, v59\n" \
  "s_waitcnt vmcnt(23)\n" \
  "v_mfma_f32_16x16x32_bf16 v[0:3], a[32:35], v[120:123], v[0:3]\n" \
  "s_add_u32 m0, s57, 24576\n" \
  "v_add_u32 v58, s99, v58\n" \
  "v_mfma_f32_16x16x32_bf16 v[4:7], a[36:39], v[120:123], v[4:7]\n" \
  "v_add_u32 v46, s98, v58\n" \
  "ds_read_b64 v[56:57], v46 offset:28680\n" \
  "v_mfma_f32_16x16x32_bf16 v[8:11], a[40:43], v[120:123], v[8:11]\n" \
  "buffer_load_dwordx4 v68, s[40:43], s66 offen offset:0 lds\n" \
  "v_mfma_f32_16x16x32_bf16 v[12:15], a[44:47], v[120:123], v[12:15]\n" \
  "v_mfma_f32_16x16x32_bf16 v[16:19], a[48:51], v[120:123], v[16:19]\n" \
  "v_mfma_f32_16x16x32_bf16 v[20:23], a[52:55], v[120:123], v[20:23]\n" \
  "buffer_load_dwordx4 v69, s[40:43], s66 offen offset:1024 lds\n" \
  "v_mfma_f32_16x16x32_bf16 v[24:27], a[56:59], v[120:123], v[24:27]\n" \
  "s_waitcnt vmcnt(17)\n" \
  "ds_read_b64_tr_b16 a[144:145], v64 offset:8192\n" \
  "ds_read_b64_tr_b16 a[146:147], v64 offset:8704\n" \
  "v_mfma_f32_16x16x32_bf16 v[28:31], a[60:63], v[120:123], v[28:31]\n" \
  "ds_read_b64_tr_b16 a[148:149], v65 offset:8192\n" \
  "ds_read_b64_tr_b16 a[150:151], v65 offset:8704\n" \
  "v_mfma_f32_16x16x32_bf16 v[0:3], a[64:67], v[124:127], v[0:3]\n" \
  "buffer_load_dwordx4 v70, s[40:43], s66 offen offset:2048 lds\n" \
  "ds_read_b64_tr_b16 a[152:153], v66 offset:8192\n" \
  "ds_read_b64_tr_b16 a[154:155], v66 offset:8704\n" \
  "v_mfma_f32_16x16x32_bf16 v[4:7], a[68:71], v[124:127], v[4:7]\n" \
  "ds_read_b64_tr_b16 a[156:157], v67 offset:8192\n" \
  "ds_read_b64_tr_b16 a[158:159], v67 offset:8704\n" \
  "v_mfma_f32_16x16x32_bf16 v[8:11], a[72:75], v[124:127], v[8:11]\n" \
  "v_mfma_f32_16x16x32_bf16 v[12:15], a[76:79], v[124:127], v[12:15]\n" \
  "buffer_load_dwordx4 v71, s[40:43], s66 offen offset:3072 lds\n" \
  "v_mfma_f32_16x16x32_bf16 v[16:19], a[80:83], v[124:127], v[16:19]\n" \
  "s_waitcnt lgkmcnt(0)\n" \
  "s_barrier\n" \
  "s_mov_b32 m0, s58\n" \
  "v_mfma_f32_16x16x32_bf16 v[20:23], a[84:87], v[124:127], v[20:23]\n" \
  "v_mfma_f32_16x16x32_bf16 v[24:27], a[88:91], v[124:127], v[24:27]\n" \
  "buffer_load_dwordx4 v[196:199], v59, s[36:39], s50 offen offset:0 sc1\n" \
  "v_mfma_f32_16x16x32_bf16 v[28:31], a[92:95], v[124:127], v[28:31]\n" \
  "v_mfma_f32_16x16x32_bf16 v[0:3], a[96:99], v[192:195], v[0:3]\n" \
  "v_mfma_f32_16x16x32_bf16 v[4:7], a[100:103], v[192:195], v[4:7]\n" \
  "buffer_load_dwordx4 v[200:203], v59, s[36:39], s50 offen offset:1024 sc1\n" \
  "v_mfma_f32_16x16x32_bf16 v[8:11], a[104:107], v[192:195], v[8:11]\n" \
  "s_waitcnt vmcnt(17)\n" \
  "ds_read_b64_tr_b16 a[160:161], v64 offset:12288\n" \
  "ds_read_b64_tr_b16 a[162:163], v64 offset:12800\n" \
  "v_mfma_f32_16x16x32_bf16 v[12:15], a[108:111], v[192:195], v[12:15]\n" \
  "ds_read_b64_tr_b16 a[164:165], v65 offset:12288\n" \
  "ds_read_b64_tr_b16 a[166:167], v65 offset:12800\n" \
  "v_mfma_f32_16x16x32_bf16 v[224:227], a[112:115], v[92:95], v[224:227]\n" \
  "buffer_load_dwordx4 v[204:207], v59, s[36:39], s50 offen offset:2048 sc1\n" \
  "s_add_u32 s59, s59, 0x10000\n" \
  "buffer_load_dword v63, v61, s[36:39], s59 offen offset:0 sc1\n" \
  "v_mfma_f32_16x16x32_bf16 v[228:231], a[116:119], v[92:95], v[228:231]\n" \
  "v_add_u32 v59, 819200, v59\n" \
  "ds_read_b64_tr_b16 a[168:169], v66 offset:12288\n" \
  "ds_read_b64_tr_b16 a[170:171], v66 offset:12800\n" \
  "v_mfma_f32_16x16x32_bf16 v[232:235], a[120:123], v[92:95], v[232:235]\n" \
  "ds_read_b64_tr_b16 a[172:173], v67 offset:12288\n" \
  "ds_read_b64_tr_b16 a[174:175], v67 offset:12800\n" \
  "buffer_store_dwordx2 v[40:41], v61, s[36:39], s52 offen offset:0 sc1\n" \
  "v_mfma_f32_16x16x32_bf16 v[236:239], a[124:127], v[92:95], v[236:239]\n" \
  "buffer_load_dwordx4 v[72:75], v59, s[36:39], s68 offen offset:0 sc1\n" \
  "v_mfma_f32_16x16x32_bf16 v[208:211], a[128:131], v[96:99], v[208:211]\n" \
  "v_mfma_f32_16x16x32_bf16 v[212:215], a[132:135], v[96:99], v[212:215]\n" \
  "v_mfma_f32_16x16x32_bf16 v[216:219], a[136:139], v[96:99], v[216:219]\n" \
  "buffer_load_dwordx4 v[76:79], v59, s[36:39], s68 offen offset:1024 sc1\n" \
  "v_mfma_f32_16x16x32_bf16 v[220:223], a[140:143], v[96:99], v[220:223]\n" \
  "v_mfma_f32_16x16x32_bf16 v[224:227], a[144:147], v[96:99], v[224:227]\n" \
  "s_waitcnt vmcnt(16)\n" \
  "ds_read_b64_tr_b16 a[176:177], v64 offset:16384\n" \
  "ds_read_b64_tr_b16 a[178:179], v64 offset:16896\n" \
  "v_mfma_f32_16x16x32_bf16 v[228:231], a[148:151], v[96:99], v[228:231]\n" \
  "ds_read_b64_tr_b16 a[180:181], v65 offset:16384\n" \
  "ds_read_b64_tr_b16 a[182:183], v65 offset:16896\n" \
  "buffer_load_dwordx4 v[80:83], v59, s[36:39], s68 offen offset:2048 sc1\n" \
  "v_mfma_f32_16x16x32_bf16 v[232:235], a[152:155], v[96:99], v[232:235]\n" \
  "ds_read_b64_tr_b16 a[184:185], v66 offset:16384\n" \
  "ds_read_b64_tr_b16 a[186:187], v66 offset:16896\n" \
  "v_mfma_f32_16x16x32_bf16 v[236:239], a[156:159], v[96:99], v[236:239]\n" \
  "ds_read_b64_tr_b16 a[188:189], v67 offset:16384\n" \
  "ds_read_b64_tr_b16 a[190:191], v67 offset:16896\n" \
  "s_waitcnt lgkmcnt(8)\n" \
  "v_mfma_f32_16x16x32_bf16 v[208:211], a[160:163], v[100:103], v[208:211]\n" \
  "s_waitcnt vmcnt(13)\n" \
  "ds_read_b64_tr_b16 a[192:193], v64 offset:20480\n" \
  "ds_read_b64_tr_b16 a[194:195], v64 offset:20992\n" \
  "v_mfma_f32_16x16x32_bf16 v[212:215], a[164:167], v[100:103], v[212:215]\n" \
  "ds_read_b64_tr_b16 a[196:197], v65 offset:20480\n" \
  "ds_read_b64_tr_b16 a[198:199], v65 offset:20992\n" \
  "buffer_load_dwordx4 v[84:87], v59, s[36:39], s69 offen offset:0 sc1\n" \
  "v_mfma_f32_16x16x32_bf16 v[216:219], a[168:171], v[100:103], v[216:219]\n" \
  "ds_read_b64_tr_b16 a[200:201], v66 offset:20480\n" \
  "ds_read_b64_tr_b16 a[202:203], v66 offset:20992\n" \
  "v_mfma_f32_16x16x32_bf16 v[220:223], a[172:175], v[100:103], v[220:223]\n" \
  "ds_read_b64_tr_b16 a[204:205], v67 offset:20480\n" \
  "ds_read_b64_tr_b16 a[206:207], v67 offset:20992\n" \
  "s_waitcnt lgkmcnt(8)\n" \
  "v_mfma_f32_16x16x32_bf16 v[224:227], a[176:179], v[100:103], v[224:227]\n" \
  "s_waitcnt vmcnt(9)\n" \
  "ds_read_b64_tr_b16 a[208:209], v64 offset:24576\n" \
  "ds_read_b64_tr_b16 a[210:211], v64 offset:25088\n" \
  "v_mfma_f32_16x16x32_bf16 v[228:231], a[180:183], v[100:103], v[228:231]\n" \
  "ds_read_b64_tr_b16 a[212:213], v65 offset:24576\n" \
  "ds_read_b64_tr_b16 a[214:215], v65 offset:25088\n" \
  "buffer_load_dwordx4 v[88:91], v59, s[36:39], s69 offen offset:1024 sc1\n" \
  "v_mfma_f32_16x16x32_bf16 v[232:235], a[184:187], v[100:103], v[232:235]\n" \
  "ds_read_b64_tr_b16 a[216:217], v66 offset:24576\n" \
  "ds_read_b64_tr_b16 a[218:219], v66 offset:25088\n" \
  "v_mfma_f32_16x16x32_bf16 v[236:239], a[188:191], v[100:103], v[236:239]\n" \
  "ds_read_b64_tr_b16 a[220:221], v67 offset:24576\n" \
  "ds_read_b64_tr_b16 a[222:223], v67 offset:25088\n" \
  "s_waitcnt lgkmcnt(8)\n" \
  "v_mfma_f32_16x16x32_bf16 v[208:211], a[192:195], v[104:107], v[208:211]\n" \
  "buffer_load_dwordx4 v[92:95], v59, s[36:39], s69 offen offset:2048 sc1\n" \
  "v_mfma_f32_16x16x32_bf16 v[212:215], a[196:199], v[104:107], v[212:215]\n" \
  "v_mfma_f32_16x16x32_bf16 v[216:219], a[200:203], v[104:107], v[216:219]\n" \
  "v_mfma_f32_16x16x32_bf16 v[220:223], a[204:207], v[104:107], v[220:223]\n" \
  "buffer_load_dwordx4 v[96:99], v59, s[36:39], s70 offen offset:0 sc1\n" \
  "s_waitcnt lgkmcnt(0)\n" \
  "v_mfma_f32_16x16x32_bf16 v[224:227], a[208:211], v[104:107], v[224:227]\n" \
  "v_mfma_f32_16x16x32_bf16 v[228:231], a[212:215], v[104:107], v[228:231]\n" \
  "v_mfma_f32_16x16x32_bf16 v[232:235], a[216:219], v[104:107], v[232:235]\n" \
  "buffer_load_dwordx4 v[100:103], v59, s[36:39], s70 offen offset:1024 sc1\n" \
  "v_mfma_f32_16x16x32_bf16 v[236:239], a[220:223], v[104:107], v[236:239]\n" \
  "v_mfma_f32_16x16x32_bf16 v[16:19], a[112:115], v[192:195], v[16:19]\n" \
  "v_mfma_f32_16x16x32_bf16 v[20:23], a[116:119], v[192:195], v[20:23]\n" \
  "buffer_load_dwordx4 v[104:107], v59, s[36:39], s70 offen offset:2048 sc1\n" \
  "ds_write_addtid_b32 v208 offset:0\n" \
  "ds_write_addtid_b32 v209 offset:256\n" \
  "v_mfma_f32_16x16x32_bf16 v[24:27], a[120:123], v[192:195], v[24:27]\n" \
  "ds_write_addtid_b32 v210 offset:512\n" \
  "ds_write_addtid_b32 v211 offset:768\n" \
  "v_mfma_f32_16x16x32_bf16 v[28:31], a[124:127], v[192:195], v[28:31]\n" \
  "ds_write_addtid_b32 v212 offset:1024\n" \
  "ds_write_addtid_b32 v213 offset:1280\n" \
  "s_waitcnt vmcnt(13)\n" \
  "v_mfma_f32_16x16x32_bf16 v[0:3], a[128:131], v[196:199], v[0:3]\n" \
  "ds_write_addtid_b32 v214 offset:1536\n" \
  "ds_write_addtid_b32 v215 offset:1792\n" \
  "v_mfma_f32_16x16x32_bf16 v[4:7], a[132:135], v[196:199], v[4:7]\n" \
  "ds_write_addtid_b32 v216 offset:2048\n" \
  "ds_write_addtid_b32 v217 offset:2304\n" \
  "v_mfma_f32_16x16x32_bf16 v[8:11], a[136:139], v[196:199], v[8:11]\n" \
  "ds_write_addtid_b32 v218 offset:2560\n" \
  "ds_write_addtid_b32 v219 offset:2816\n" \
  "v_mfma_f32_16x16x32_bf16 v[12:15], a[140:143], v[196:199], v[12:15]\n" \
  "ds_write_addtid_b32 v220 offset:3072\n" \
  "ds_write_addtid_b32 v221 offset:3328\n" \
  "v_mfma_f32_16x16x32_bf16 v[16:19], a[144:147], v[196:199], v[16:19]\n" \
  "ds_write_addtid_b32 v222 offset:3584\n" \
  "ds_write_addtid_b32 v223 offset:3840\n" \
  "v_mfma_f32_16x16x32_bf16 v[20:23], a[148:151], v[196:199], v[20:23]\n" \
  "ds_write_addtid_b32 v224 offset:4096\n" \
  "ds_write_addtid_b32 v225 offset:4352\n" \
  "v_mfma_f32_16x16x32_bf16 v[24:27], a[152:155], v[196:199], v[24:27]\n" \
  "ds_write_addtid_b32 v226 offset:4608\n" \
  "ds_write_addtid_b32 v227 offset:4864\n" \
  "v_mfma_f32_16x16x32_bf16 v[28:31], a[156:159], v[196:199], v[28:31]\n" \
  "ds_write_addtid_b32 v228 offset:5120\n" \
  "ds_write_addtid_b32 v229 offset:5376\n" \
  "s_waitcnt vmcnt(12)\n" \
  "v_mfma_f32_16x16x32_bf16 v[0:3], a[160:163], v[200:203], v[0:3]\n" \
  "ds_write_addtid_b32 v230 offset:5632\n" \
  "ds_write_addtid_b32 v231 offset:5888\n" \
  "v_mfma_f32_16x16x32_bf16 v[4:7], a[164:167], v[200:203], v[4:7]\n" \
  "ds_write_addtid_b32 v232 offset:6144\n" \
  "ds_write_addtid_b32 v233 offset:6400\n" \
  "v_mfma_f32_16x16x32_bf16 v[8:11], a[168:171], v[200:203], v[8:11]\n" \
  "ds_write_addtid_b32 v234 offset:6656\n" \
  "ds_write_addtid_b32 v235 offset:6912\n" \
  "v_mfma_f32_16x16x32_bf16 v[12:15], a[172:175], v[200:203], v[12:15]\n" \
  "ds_write_addtid_b32 v236 offset:7168\n" \
  "ds_write_addtid_b32 v237 offset:7424\n" \
  "v_mfma_f32_16x16x32_bf16 v[16:19], a[176:179], v[200:203], v[16:19]\n" \
  "ds_write_addtid_b32 v238 offset:7680\n" \
  "ds_write_addtid_b32 v239 offset:7936\n" \
  "v_mfma_f32_16x16x32_bf16 v[20:23], a[180:183], v[200:203], v[20:23]\n" \
  "v_mfma_f32_16x16x32_bf16 v[24:27], a[184:187], v[200:203], v[24:27]\n" \
  "v_mfma_f32_16x16x32_bf16 v[28:31], a[188:191], v[200:203], v[28:31]\n" \
  "s_waitcnt vmcnt(11)\n" \
  "v_mfma_f32_16x16x32_bf16 v[0:3], a[192:195], v[204:207], v[0:3]\n" \
  "v_mfma_f32_16x16x32_bf16 v[4:7], a[196:199], v[204:207], v[4:7]\n" \
  "v_mfma_f32_16x16x32_bf16 v[8:11], a[200:203], v[204:207], v[8:11]\n" \
  "s_waitcnt lgkmcnt(0)\n" \
  "s_barrier\n" \
  "ds_read_b128 v[32:35], v58 offset:0\n" \
  "v_mfma_f32_16x16x32_bf16 v[12:15], a[204:207], v[204:207], v[12:15]\n" \
  "ds_read_b128 v[36:39], v58 offset:8192\n" \
  "ds_read_b128 v[40:43], v58 offset:16384\n" \
  "v_mfma_f32_16x16x32_bf16 v[16:19], a[208:211], v[204:207], v[16:19]\n" \
  "ds_read_b128 v[44:47], v58 offset:24576\n" \
  "ds_read_b128 v[48:51], v58 offset:4096\n" \
  "v_mfma_f32_16x16x32_bf16 v[20:23], a[212:215], v[204:207], v[20:23]\n" \
  "ds_read_b128 v[240:243], v58 offset:12288\n" \
  "ds_read_b128 v[244:247], v58 offset:20480\n" \
  "v_mfma_f32_16x16x32_bf16 v[24:27], a[216:219], v[204:207], v[24:27]\n" \
  "ds_read_b128 v[248:251], v58 offset:28672\n" \
  "v_mfma_f32_16x16x32_bf16 v[28:31], a[220:223], v[204:207], v[28:31]\n" \
  "s_waitcnt vmcnt(7)\n" \
  "v_mfma_f32_16x16x32_bf16 v[208:211], v[128:131], v[72:75], 0\n" \
  "s_mov_b64 exec, 1\n" \
  "buffer_store_dword v61, off, s[36:39], s54 offset:0 sc1\n" \
  "s_mov_b64 exec, -1\n" \
  "v_mfma_f32_16x16x32_bf16 v[212:215], v[132:135], v[72:75], 0\n" \
  "v_cmp_eq_u32 vcc, 0, v63\n" \
  "ds_write_addtid_b32 v0 offset:32768\n" \
  "v_mfma_f32_16x16x32_bf16 v[216:219], v[136:139], v[72:75], 0\n" \
  "ds_write_addtid_b32 v1 offset:33024\n" \
  "ds_write_addtid_b32 v2 offset:33280\n" \
  "v_lshlrev_b32 v52, 5, v59\n" \
  "v_mfma_f32_16x16x32_bf16 v[220:223], v[140:143], v[72:75], 0\n" \
  "s_and_b64 vcc, vcc, s[86:87]\n" \
  "s_cbranch_scc1 .LsbS_%=\n" \
  ".LbbS_%=:\n" \
  "buffer_load_dwordx4 v[108:111], v59, s[36:39], s48 offen offset:0 sc1\n" \
  "v_mfma_f32_16x16x32_bf16 v[224:227], v[144:147], v[72:75], 0\n" \
  "ds_write_addtid_b32 v3 offset:33536\n" \
  "ds_write_addtid_b32 v4 offset:33792\n" \
  "s_waitcnt lgkmcnt(9)\n" \
  "v_mfma_f32_16x16x32_bf16 v[228:231], v[148:151], v[72:75], 0\n" \
  "ds_write_addtid_b32 v5 offset:34048\n" \
  "ds_write_addtid_b32 v6 offset:34304\n" \
  "v_add_f32 v32, v32, v36\n" \
  "v_mfma_f32_16x16x32_bf16 v[232:235], v[152:155], v[72:75], 0\n" \
  "buffer_load_dwordx4 v[112:115], v59, s[36:39], s48 offen offset:1024 sc1\n" \
  "ds_write_addtid_b32 v7 offset:34560\n" \
  "ds_write_addtid_b32 v8 offset:34816\n" \
  "v_mfma_f32_16x16x32_bf16 v[236:239], v[156:159], v[72:75], 0\n" \
  "ds_write_addtid_b32 v9 offset:35072\n" \
  "ds_write_addtid_b32 v10 offset:35328\n" \
  "v_add_f32 v33, v33, v37\n" \
  "v_mfma_f32_16x16x32_bf16 v[208:211], v[160:163], v[76:79], v[208:211]\n" \
  "ds_write_addtid_b32 v11 offset:35584\n" \
  "ds_write_addtid_b32 v12 offset:35840\n" \
  "v_add_f32 v34, v34, v38\n" \
  "v_mfma_f32_16x16x32_bf16 v[212:215], v[164:167], v[76:79], v[212:215]\n" \
  "buffer_load_dwordx4 v[116:119], v59, s[36:39], s48 offen offset:2048 sc1\n" \
  "ds_write_addtid_b32 v13 offset:36096\n" \
  "ds_write_addtid_b32 v14 offset:36352\n" \
  "v_mfma_f32_16x16x32_bf16 v[216:219], v[168:171], v[76:79], v[216:219]\n" \
  "ds_write_addtid_b32 v15 offset:36608\n" \
  "ds_write_addtid_b32 v16 offset:36864\n" \
  "v_add_f32 v35, v35, v39\n" \
  "v_mfma_f32_16x16x32_bf16 v[220:223], v[172:175], v[76:79], v[220:223]\n" \
  "ds_write_addtid_b32 v17 offset:37120\n" \
  "ds_write_addtid_b32 v18 offset:37376\n" \
  "v_add_f32 v40, v40, v44\n" \
  "v_mfma_f32_16x16x32_bf16 v[224:227], v[176:179], v[76:79], v[224:227]\n" \
  "buffer_load_dwordx4 v[120:123], v59, s[36:39], s49 offen offset:0 sc1\n" \
  "ds_write_addtid_b32 v19 offset:37632\n" \
  "ds_write_addtid_b32 v20 offset:37888\n" \
  "v_mfma_f32_16x16x32_bf16 v[228:231], v[180:183], v[76:79], v[228:231]\n" \
  "ds_write_addtid_b32 v21 offset:38144\n" \
  "ds_write_addtid_b32 v22 offset:38400\n" \
  "v_add_f32 v41, v41, v45\n" \
  "v_mfma_f32_16x16x32_bf16 v[232:235], v[184:187], v[76:79], v[232:235]\n" \
  "ds_write_addtid_b32 v23 offset:38656\n" \
  "ds_write_addtid_b32 v24 offset:38912\n" \
  "v_add_f32 v42, v42, v46\n" \
  "v_mfma_f32_16x16x32_bf16 v[236:239], v[188:191], v[76:79], v[236:239]\n" \
  "buffer_load_dwordx4 v[124:127], v59, s[36:39], s49 offen offset:1024 sc1\n" \
  "ds_write_addtid_b32 v25 offset:39168\n" \
  "s_waitcnt vmcnt(11)\n" \
  "v_mfma_f32_16x16x32_bf16 v[208:211], a[0:3], v[80:83], v[208:211]\n" \
  "ds_write_addtid_b32 v26 offset:39424\n" \
  "ds_write_addtid_b32 v27 offset:39680\n" \
  "v_add_f32 v43, v43, v47\n" \
  "v_mfma_f32_16x16x32_bf16 v[212:215], a[4:7], v[80:83], v[212:215]\n" \
  "ds_write_addtid_b32 v28 offset:39936\n" \
  "ds_write_addtid_b32 v29 offset:40192\n" \
  "v_add_f32 v32, v32, v40\n" \
  "v_mfma_f32_16x16x32_bf16 v[216:219], a[8:11], v[80:83], v[216:219]\n" \
  "buffer_load_dwordx4 v[192:195], v59, s[36:39], s49 offen offset:2048 sc1\n" \
  "ds_write_addtid_b32 v30 offset:40448\n" \
  "ds_write_addtid_b32 v31 offset:40704\n" \
  "v_mfma_f32_16x16x32_bf16 v[220:223], a[12:15], v[80:83], v[220:223]\n" \
  "v_add_f32 v33, v33, v41\n" \
  "v_add_f32 v34, v34, v42\n" \
  "v_add_f32 v35, v35, v43\n" \
  "v_mfma_f32_16x16x32_bf16 v[224:227], a[16:19], v[80:83], v[224:227]\n" \
  "v_cvt_pk_bf16_f32 v36, v32, v33\n" \
  "v_cvt_pk_bf16_f32 v37, v34, v35\n" \
  "s_waitcnt lgkmcnt(15)\n" \
  "v_mfma_f32_16x16x32_bf16 v[228:231], a[20:23], v[80:83], v[228:231]\n" \
  "buffer_load_dwordx4 v[196:199], v59, s[36:39], s50 offen offset:0 sc1\n" \
  "v_add_f32 v48, v48, v240\n" \
  "v_add_f32 v49, v49, v241\n" \
  "v_mfma_f32_16x16x32_bf16 v[232:235], a[24:27], v[80:83], v[232:235]\n" \
  "v_add_f32 v50, v50, v242\n" \
  "v_add_f32 v51, v51, v243\n" \
  "v_add_f32 v244, v244, v248\n" \
  "v_mfma_f32_16x16x32_bf16 v[236:239], a[28:31], v[80:83], v[236:239]\n" \
  "v_add_f32 v245, v245, v249\n" \
  "v_add_f32 v246, v246, v250\n" \
  "v_add_f32 v247, v247, v251\n" \
  "v_mfma_f32_16x16x32_bf16 v[208:211], a[32:35], v[84:87], v[208:211]\n" \
  "buffer_load_dwordx4 v[200:203], v59, s[36:39], s50 offen offset:1024 sc1\n" \
  "s_waitcnt lgkmcnt(0)\n" \
  "s_barrier\n" \
  "v_mfma_f32_16x16x32_bf16 v[212:215], a[36:39], v[84:87], v[212:215]\n" \
  "v_add_f32 v48, v48, v244\n" \
  "v_add_f32 v49, v49, v245\n" \
  "v_add_f32 v50, v50, v246\n" \
  "v_mfma_f32_16x16x32_bf16 v[216:219], a[40:43], v[84:87], v[216:219]\n" \
  "v_and_b32 v52, 0x1800, v52\n" \
  "v_add_f32 v51, v51, v247\n" \
  "v_and_b32 v53, 0x30, v59\n" \
  "v_mfma_f32_16x16x32_bf16 v[220:223], a[44:47], v[84:87], v[220:223]\n" \
  "buffer_load_dwordx4 v[204:207], v59, s[36:39], s50 offen offset:2048 sc1\n" \
  "v_cvt_pk_bf16_f32 v38, v48, v49\n" \
  "v_lshl_or_b32 v52, v53, 1, v52\n" \
  "v_mfma_f32_16x16x32_bf16 v[224:227], a[48:51], v[84:87], v[224:227]\n" \
  "v_cvt_pk_bf16_f32 v39, v50, v51\n" \
  "buffer_load_dword v53, v52, s[36:39], s30 offen offset:0 sc1\n" \
  "v_mfma_f32_16x16x32_bf16 v[228:231], a[52:55], v[84:87], v[228:231]\n" \
  "v_mfma_f32_16x16x32_bf16 v[232:235], a[56:59], v[84:87], v[232:235]\n" \
  "buffer_store_dwordx4 v[36:39], v59, s[36:39], s74 offen offset:0 sc1\n" \
  "ds_read_b128 v[32:35], v58 offset:32768\n" \
  "s_nop 0\n" \
  "ds_read_b128 v[36:39], v58 offset:40960\n" \
  "v_mfma_f32_16x16x32_bf16 v[236:239], a[60:63], v[84:87], v[236:239]\n" \
  "ds_read_b128 v[40:43], v58 offset:49152\n" \
  "ds_read_b128 v[44:47], v58 offset:57344\n" \
  "s_waitcnt vmcnt(15)\n" \
  "v_mfma_f32_16x16x32_bf16 v[208:211], a[64:67], v[88:91], v[208:211]\n" \
  "ds_read_b128 v[48:51], v58 offset:36864\n" \
  "ds_read_b128 v[240:243], v58 offset:45056\n" \
  "v_mfma_f32_16x16x32_bf16 v[212:215], a[68:71], v[88:91], v[212:215]\n" \
  "ds_read_b128 v[244:247], v58 offset:53248\n" \
  "ds_read_b128 v[248:251], v58 offset:61440\n" \
  "v_mfma_f32_16x16x32_bf16 v[216:219], a[72:75], v[88:91], v[216:219]\n" \
  "v_mfma_f32_16x16x32_bf16 v[220:223], a[76:79], v[88:91], v[220:223]\n" \
  "v_mfma_f32_16x16x32_bf16 v[224:227], a[80:83], v[88:91], v[224:227]\n" \
  "v_mfma_f32_16x16x32_bf16 v[228:231], a[84:87], v[88:91], v[228:231]\n" \
  "v_mfma_f32_16x16x32_bf16 v[232:235], a[88:91], v[88:91], v[232:235]\n" \
  "v_mfma_f32_16x16x32_bf16 v[236:239], a[92:95], v[88:91], v[236:239]\n" \
  "s_waitcnt lgkmcnt(4)\n" \
  "v_add_f32 v32, v32, v36\n" \
  "v_add_f32 v33, v33, v37\n" \
  "v_mfma_f32_16x16x32_bf16 v[208:211], a[96:99], v[92:95], v[208:211]\n" \
  "v_add_f32 v34, v34, v38\n" \
  "v_add_f32 v35, v35, v39\n" \
  "v_add_f32 v40, v40, v44\n" \
  "v_mfma_f32_16x16x32_bf16 v[212:215], a[100:103], v[92:95], v[212:215]\n" \
  "v_add_f32 v41, v41, v45\n" \
  "v_add_f32 v42, v42, v46\n" \
  "v_add_f32 v43, v43, v47\n" \
  "v_mfma_f32_16x16x32_bf16 v[216:219], a[104:107], v[92:95], v[216:219]\n" \
  "v_add_f32 v32, v32, v40\n" \
  "v_add_f32 v33, v33, v41\n" \
  "v_add_f32 v34, v34, v42\n" \
  "v_mfma_f32_16x16x32_bf16 v[220:223], a[108:111], v[92:95], v[220:223]\n" \
  "v_add_f32 v35, v35, v43\n" \
  "v_cvt_pk_bf16_f32 v36, v32, v33\n" \
  "v_cvt_pk_bf16_f32 v37, v34, v35\n" \
  "v_mfma_f32_16x16x32_bf16 v[224:227], a[112:115], v[92:95], v[224:227]\n" \
  "s_waitcnt lgkmcnt(0)\n" \
  "v_add_f32 v48, v48, v240\n" \
  "v_add_f32 v49, v49, v241\n" \
  "v_mfma_f32_16x16x32_bf16 v[228:231], a[116:119], v[92:95], v[228:231]\n" \
  "v_add_f32 v50, v50, v242\n" \
  "v_add_f32 v51, v51, v243\n" \
  "v_add_f32 v244, v244, v248\n" \
  "v_mfma_f32_16x16x32_bf16 v[232:235], a[120:123], v[92:95], v[232:235]\n" \
  "v_add_f32 v245, v245, v249\n" \
  "v_add_f32 v246, v246, v250\n" \
  "v_add_f32 v247, v247, v251\n" \
  "v_mfma_f32_16x16x32_bf16 v[236:239], a[124:127], v[92:95], v[236:239]\n" \
  "v_add_f32 v48, v48, v244\n" \
  "v_add_f32 v49, v49, v245\n" \
  "s_waitcnt vmcnt(13)\n" \
  "v_mfma_f32_16x16x32_bf16 v[208:211], a[128:131], v[96:99], v[208:211]\n" \
  "v_add_f32 v50, v50, v246\n" \
  "v_add_f32 v51, v51, v247\n" \
  "v_cvt_pk_bf16_f32 v38, v48, v49\n" \
  "v_mfma_f32_16x16x32_bf16 v[212:215], a[132:135], v[96:99], v[212:215]\n" \
  "v_cvt_pk_bf16_f32 v39, v50, v51\n" \
  "v_add_u32 v59, 409600, v59\n" \
  "buffer_store_dwordx4 v[36:39], v59, s[36:39], s74 offen offset:0 sc1\n" \
  "v_mfma_f32_16x16x32_bf16 v[216:219], a[136:139], v[96:99], v[216:219]\n" \
  "v_mfma_f32_16x16x32_bf16 v[220:223], a[140:143], v[96:99], v[220:223]\n" \
  "v_mfma_f32_16x16x32_bf16 v[224:227], a[144:147], v[96:99], v[224:227]\n" \
  "v_mfma_f32_16x16x32_bf16 v[228:231], a[148:151], v[96:99], v[228:231]\n" \
  "v_mfma_f32_16x16x32_bf16 v[232:235], a[152:155], v[96:99], v[232:235]\n" \
  "v_mfma_f32_16x16x32_bf16 v[236:239], a[156:159], v[96:99], v[236:239]\n" \
  "v_mfma_f32_16x16x32_bf16 v[208:211], a[160:163], v[100:103], v[208:211]\n" \
  "v_mfma_f32_16x16x32_bf16 v[212:215], a[164:167], v[100:103], v[212:215]\n" \
  "v_mfma_f32_16x16x32_bf16 v[216:219], a[168:171], v[100:103], v[216:219]\n" \
  "v_mfma_f32_16x16x32_bf16 v[220:223], a[172:175], v[100:103], v[220:223]\n" \
  "v_mfma_f32_16x16x32_bf16 v[224:227], a[176:179], v[100:103], v[224:227]\n" \
  "v_mfma_f32_16x16x32_bf16 v[228:231], a[180:183], v[100:103], v[228:231]\n" \
  "v_mfma_f32_16x16x32_bf16 v[232:235], a[184:187], v[100:103], v[232:235]\n" \
  "v_mfma_f32_16x16x32_bf16 v[236:239], a[188:191], v[100:103], v[236:239]\n" \
  "s_waitcnt vmcnt(13)\n" \
  "v_mfma_f32_16x16x32_bf16 v[208:211], a[192:195], v[104:107], v[208:211]\n" \
  "v_mfma_f32_16x16x32_bf16 v[212:215], a[196:199], v[104:107], v[212:215]\n" \
  "v_mfma_f32_16x16x32_bf16 v[216:219], a[200:203], v[104:107], v[216:219]\n" \
  "v_mfma_f32_16x16x32_bf16 v[220:223], a[204:207], v[104:107], v[220:223]\n" \
  "v_mfma_f32_16x16x32_bf16 v[224:227], a[208:211], v[104:107], v[224:227]\n" \
  "v_mfma_f32_16x16x32_bf16 v[228:231], a[212:215], v[104:107], v[228:231]\n" \
  "v_mfma_f32_16x16x32_bf16 v[232:235], a[216:219], v[104:107], v[232:235]\n" \
  "v_mfma_f32_16x16x32_bf16 v[236:239], a[220:223], v[104:107], v[236:239]\n" \
  ".Le_%=:\n" \
  "s_waitcnt vmcnt(10)\n" \
  "v_mfma_f32_16x16x32_bf16 v[0:3], v[128:131], v[108:111], 0\n" \
  "v_add_u32 v59, 409600, v59\n" \
  "v_mfma_f32_16x16x32_bf16 v[4:7], v[132:135], v[108:111], 0\n" \
  "ds_write_addtid_b32 v208 offset:0\n" \
  "v_mfma_f32_16x16x32_bf16 v[8:11], v[136:139], v[108:111], 0\n" \
  "buffer_load_dwordx4 v[72:75], v59, s[36:39], s68 offen offset:0 sc1\n" \
  "ds_write_addtid_b32 v209 offset:256\n" \
  "v_mfma_f32_16x16x32_bf16 v[12:15], v[140:143], v[108:111], 0\n" \
  "ds_write_addtid_b32 v210 offset:512\n" \
  "v_mfma_f32_16x16x32_bf16 v[16:19], v[144:147], v[108:111], 0\n" \
  "ds_write_addtid_b32 v211 offset:768\n" \
  "v_mfma_f32_16x16x32_bf16 v[20:23], v[148:151], v[108:111], 0\n" \
  "ds_write_addtid_b32 v212 offset:1024\n" \
  "v_mfma_f32_16x16x32_bf16 v[24:27], v[152:155], v[108:111], 0\n" \
  "ds_write_addtid_b32 v213 offset:1280\n" \
  "v_mfma_f32_16x16x32_bf16 v[28:31], v[156:159], v[108:111], 0\n" \
  "ds_write_addtid_b32 v214 offset:1536\n" \
  "v_mfma_f32_16x16x32_bf16 v[0:3], v[160:163], v[112:115], v[0:3]\n" \
  "buffer_load_dwordx4 v[76:79], v59, s[36:39], s68 offen offset:1024 sc1\n" \
  "ds_write_addtid_b32 v215 offset:1792\n" \
  "v_mfma_f32_16x16x32_bf16 v[4:7], v[164:167], v[112:115], v[4:7]\n" \
  "ds_write_addtid_b32 v216 offset:2048\n" \
  "v_mfma_f32_16x16x32_bf16 v[8:11], v[168:171], v[112:115], v[8:11]\n" \
  "ds_write_addtid_b32 v217 offset:2304\n" \
  "v_mfma_f32_16x16x32_bf16 v[12:15], v[172:175], v[112:115], v[12:15]\n" \
  "ds_write_addtid_b32 v218 offset:2560\n" \
  "v_mfma_f32_16x16x32_bf16 v[16:19], v[176:179], v[112:115], v[16:19]\n" \
  "ds_write_addtid_b32 v219 offset:2816\n" \
  "v_mfma_f32_16x16x32_bf16 v[20:23], v[180:183], v[112:115], v[20:23]\n" \
  "ds_write_addtid_b32 v220 offset:3072\n" \
  "v_mfma_f32_16x16x32_bf16 v[24:27], v[184:187], v[112:115], v[24:27]\n" \
  "buffer_load_dwordx4 v[80:83], v59, s[36:39], s68 offen offset:2048 sc1\n" \
  "ds_write_addtid_b32 v221 offset:3328\n" \
  "v_mfma_f32_16x16x32_bf16 v[28:31], v[188:191], v[112:115], v[28:31]\n" \
  "ds_write_addtid_b32 v222 offset:3584\n" \
  "s_waitcnt vmcnt(11)\n" \
  "v_mfma_f32_16x16x32_bf16 v[0:3], a[0:3], v[116:119], v[0:3]\n" \
  "ds_write_addtid_b32 v223 offset:3840\n" \
  "v_mfma_f32_16x16x32_bf16 v[4:7], a[4:7], v[116:119], v[4:7]\n" \
  "ds_write_addtid_b32 v224 offset:4096\n" \
  "v_mfma_f32_16x16x32_bf16 v[8:11], a[8:11], v[116:119], v[8:11]\n" \
  "ds_write_addtid_b32 v225 offset:4352\n" \
  "v_mfma_f32_16x16x32_bf16 v[12:15], a[12:15], v[116:119], v[12:15]\n" \
  "ds_write_addtid_b32 v226 offset:4608\n" \
  "v_mfma_f32_16x16x32_bf16 v[16:19], a[16:19], v[116:119], v[16:19]\n" \
  "buffer_load_dwordx4 v[84:87], v59, s[36:39], s69 offen offset:0 sc1\n" \
  "ds_write_addtid_b32 v227 offset:4864\n" \
  "v_mfma_f32_16x16x32_bf16 v[20:23], a[20:23], v[116:119], v[20:23]\n" \
  "ds_write_addtid_b32 v228 offset:5120\n" \
  "v_mfma_f32_16x16x32_bf16 v[24:27], a[24:27], v[116:119], v[24:27]\n" \
  "ds_write_addtid_b32 v229 offset:5376\n" \
  "v_mfma_f32_16x16x32_bf16 v[28:31], a[28:31], v[116:119], v[28:31]\n" \
  "ds_write_addtid_b32 v230 offset:5632\n" \
  "v_mfma_f32_16x16x32_bf16 v[0:3], a[32:35], v[120:123], v[0:3]\n" \
  "s_add_u32 s59, s59, 0x10000\n" \
  "buffer_load_dword v63, v61, s[36:39], s59 offen offset:0 sc1\n" \
  "v_mfma_f32_16x16x32_bf16 v[4:7], a[36:39], v[120:123], v[4:7]\n" \
  "ds_write_addtid_b32 v231 offset:5888\n" \
  "v_mfma_f32_16x16x32_bf16 v[8:11], a[40:43], v[120:123], v[8:11]\n" \
  "buffer_load_dwordx4 v[88:91], v59, s[36:39], s69 offen offset:1024 sc1\n" \
  "ds_write_addtid_b32 v232 offset:6144\n" \
  "v_mfma_f32_16x16x32_bf16 v[12:15], a[44:47], v[120:123], v[12:15]\n" \
  "ds_write_addtid_b32 v233 offset:6400\n" \
  "v_mfma_f32_16x16x32_bf16 v[16:19], a[48:51], v[120:123], v[16:19]\n" \
  "ds_write_addtid_b32 v234 offset:6656\n" \
  "v_mfma_f32_16x16x32_bf16 v[20:23], a[52:55], v[120:123], v[20:23]\n" \
  "ds_write_addtid_b32 v235 offset:6912\n" \
  "v_mfma_f32_16x16x32_bf16 v[24:27], a[56:59], v[120:123], v[24:27]\n" \
  "ds_write_addtid_b32 v236 offset:7168\n" \
  "v_mfma_f32_16x16x32_bf16 v[28:31], a[60:63], v[120:123], v[28:31]\n" \
  "ds_write_addtid_b32 v237 offset:7424\n" \
  "s_waitcnt vmcnt(12)\n" \
  "v_mfma_f32_16x16x32_bf16 v[0:3], a[64:67], v[124:127], v[0:3]\n" \
  "buffer_load_dwordx4 v[92:95], v59, s[36:39], s69 offen offset:2048 sc1\n" \
  "ds_write_addtid_b32 v238 offset:7680\n" \
  "v_mfma_f32_16x16x32_bf16 v[4:7], a[68:71], v[124:127], v[4:7]\n" \
  "ds_write_addtid_b32 v239 offset:7936\n" \
  "v_mfma_f32_16x16x32_bf16 v[8:11], a[72:75], v[124:127], v[8:11]\n" \
  "s_waitcnt vmcnt(9)\n" \
  "v_cmp_eq_u32 vcc, 0, v53\n" \
  "v_mfma_f32_16x16x32_bf16 v[12:15], a[76:79], v[124:127], v[12:15]\n" \
  "v_mfma_f32_16x16x32_bf16 v[16:19], a[80:83], v[124:127], v[16:19]\n" \
  "s_cbranch_vccnz .Lspf_%=\n" \
  ".Lbpf_%=:\n" \
  "v_bfe_u32 v69, v59, 2, 8\n" \
  "v_mfma_f32_16x16x32_bf16 v[20:23], a[84:87], v[124:127], v[20:23]\n" \
  "buffer_load_dword v64, v69, s[36:39], s79 offen offset:0 sc1\n" \
  "buffer_load_dword v65, v69, s[36:39], s80 offen offset:0 sc1\n" \
  "v_mfma_f32_16x16x32_bf16 v[24:27], a[88:91], v[124:127], v[24:27]\n" \
  "buffer_load_dwordx4 v[96:99], v59, s[36:39], s70 offen offset:0 sc1\n" \
  "buffer_load_dword v66, v69, s[36:39], s81 offen offset:0 sc1\n" \
  "v_mfma_f32_16x16x32_bf16 v[28:31], a[92:95], v[124:127], v[28:31]\n" \
  "s_waitcnt lgkmcnt(0)\n" \
  "s_barrier\n" \
  "v_mfma_f32_16x16x32_bf16 v[0:3], a[96:99], v[192:195], v[0:3]\n" \
  "ds_read_b128 v[32:35], v58 offset:0\n" \
  "buffer_load_dword v67, v69, s[36:39], s82 offen offset:0 sc1\n" \
  "v_mfma_f32_16x16x32_bf16 v[4:7], a[100:103], v[192:195], v[4:7]\n" \
  "ds_read_b128 v[36:39], v58 offset:8192\n" \
  "buffer_load_dword v68, v57, s[36:39], s90 offen offset:0 sc1\n" \
  "v_mfma_f32_16x16x32_bf16 v[8:11], a[104:107], v[192:195], v[8:11]\n" \
  "ds_read_b128 v[40:43], v58 offset:16384\n" \
  "v_add_u32 v70, s98, v58\n" \
  "v_mfma_f32_16x16x32_bf16 v[12:15], a[108:111], v[192:195], v[12:15]\n" \
  "ds_read_b128 v[44:47], v58 offset:24576\n" \
  "v_mfma_f32_16x16x32_bf16 v[16:19], a[112:115], v[192:195], v[16:19]\n" \
  "buffer_load_dwordx4 v[100:103], v59, s[36:39], s70 offen offset:1024 sc1\n" \
  "ds_read_b128 v[48:51], v58 offset:4096\n" \
  "v_mfma_f32_16x16x32_bf16 v[20:23], a[116:119], v[192:195], v[20:23]\n" \
  "ds_read_b128 v[240:243], v58 offset:12288\n" \
  "v_mfma_f32_16x16x32_bf16 v[24:27], a[120:123], v[192:195], v[24:27]\n" \
  "ds_read_b128 v[244:247], v58 offset:20480\n" \
  "v_mfma_f32_16x16x32_bf16 v[28:31], a[124:127], v[192:195], v[28:31]\n" \
  "ds_read_b128 v[248:251], v58 offset:28672\n" \
  "v_mfma_f32_16x16x32_bf16 v[0:3], a[128:131], v[196:199], v[0:3]\n" \
  "ds_read_b64 v[70:71], v70 offset:29704\n" \
  "v_mfma_f32_16x16x32_bf16 v[4:7], a[132:135], v[196:199], v[4:7]\n" \
  "v_mfma_f32_16x16x32_bf16 v[8:11], a[136:139], v[196:199], v[8:11]\n" \
  "buffer_load_dwordx4 v[104:107], v59, s[36:39], s70 offen offset:2048 sc1\n" \
  "v_mfma_f32_16x16x32_bf16 v[12:15], a[140:143], v[196:199], v[12:15]\n" \
  "s_waitcnt lgkmcnt(5)\n" \
  "v_add_f32 v32, v32, v36\n" \
  "v_mfma_f32_16x16x32_bf16 v[16:19], a[144:147], v[196:199], v[16:19]\n" \
  "v_add_f32 v33, v33, v37\n" \
  "v_add_f32 v34, v34, v38\n" \
  "v_mfma_f32_16x16x32_bf16 v[20:23], a[148:151], v[196:199], v[20:23]\n" \
  "v_add_f32 v35, v35, v39\n" \
  "v_add_f32 v40, v40, v44\n" \
  "v_mfma_f32_16x16x32_bf16 v[24:27], a[152:155], v[196:199], v[24:27]\n" \
  "v_add_f32 v41, v41, v45\n" \
  "v_add_f32 v42, v42, v46\n" \
  "v_mfma_f32_16x16x32_bf16 v[28:31], a[156:159], v[196:199], v[28:31]\n" \
  "v_add_f32 v43, v43, v47\n" \
  "v_add_f32 v32, v32, v40\n" \
  "v_mfma_f32_16x16x32_bf16 v[0:3], a[160:163], v[200:203], v[0:3]\n" \
  "v_add_f32 v33, v33, v41\n" \
  "v_add_f32 v34, v34, v42\n" \
  "v_mfma_f32_16x16x32_bf16 v[4:7], a[164:167], v[200:203], v[4:7]\n" \
  "v_add_f32 v35, v35, v43\n" \
  "v_cvt_pk_bf16_f32 v36, v32, v33\n" \
  "v_mfma_f32_16x16x32_bf16 v[8:11], a[168:171], v[200:203], v[8:11]\n" \
  "v_cvt_pk_bf16_f32 v37, v34, v35\n" \
  "s_waitcnt lgkmcnt(1)\n" \
  "v_mfma_f32_16x16x32_bf16 v[12:15], a[172:175], v[200:203], v[12:15]\n" \
  "v_add_f32 v48, v48, v240\n" \
  "v_add_f32 v49, v49, v241\n" \
  "v_mfma_f32_16x16x32_bf16 v[16:19], a[176:179], v[200:203], v[16:19]\n" \
  "v_add_f32 v50, v50, v242\n" \
  "v_add_f32 v51, v51, v243\n" \
  "v_mfma_f32_16x16x32_bf16 v[20:23], a[180:183], v[200:203], v[20:23]\n" \
  "v_add_f32 v244, v244, v248\n" \
  "v_add_f32 v245, v245, v249\n" \
  "v_mfma_f32_16x16x32_bf16 v[24:27], a[184:187], v[200:203], v[24:27]\n" \
  "v_add_f32 v246, v246, v250\n" \
  "v_add_f32 v247, v247, v251\n" \
  "v_mfma_f32_16x16x32_bf16 v[28:31], a[188:191], v[200:203], v[28:31]\n" \
  "v_add_f32 v48, v48, v244\n" \
  "v_mfma_f32_16x16x32_bf16 v[0:3], a[192:195], v[204:207], v[0:3]\n" \
  "v_add_f32 v49, v49, v245\n" \
  "v_add_f32 v50, v50, v246\n" \
  "v_mfma_f32_16x16x32_bf16 v[4:7], a[196:199], v[204:207], v[4:7]\n" \
  "v_add_f32 v51, v51, v247\n" \
  "v_cvt_pk_bf16_f32 v38, v48, v49\n" \
  "v_mfma_f32_16x16x32_bf16 v[8:11], a[200:203], v[204:207], v[8:11]\n" \
  "v_cvt_pk_bf16_f32 v39, v50, v51\n" \
  "buffer_store_dwordx4 v[36:39], v59, s[36:39], s74 offen offset:0 sc1\n" \
  "v_mfma_f32_16x16x32_bf16 v[12:15], a[204:207], v[204:207], v[12:15]\n" \
  "v_mfma_f32_16x16x32_bf16 v[16:19], a[208:211], v[204:207], v[16:19]\n" \
  "v_mfma_f32_16x16x32_bf16 v[20:23], a[212:215], v[204:207], v[20:23]\n" \
  "v_mfma_f32_16x16x32_bf16 v[24:27], a[216:219], v[204:207], v[24:27]\n" \
  "v_mfma_f32_16x16x32_bf16 v[28:31], a[220:223], v[204:207], v[28:31]\n" \
  "s_waitcnt vmcnt(14)\n" \
  "v_mfma_f32_16x16x32_bf16 v[208:211], v[128:131], v[72:75], 0\n" \
  "v_add_u32 v59, 409600, v59\n" \
  "v_mfma_f32_16x16x32_bf16 v[212:215], v[132:135], v[72:75], 0\n" \
  "s_waitcnt vmcnt(11)\n" \
  "v_cmp_eq_u32 vcc, 0, v63\n" \
  "v_mfma_f32_16x16x32_bf16 v[216:219], v[136:139], v[72:75], 0\n" \
  "ds_write_addtid_b32 v0 offset:32768\n" \
  "v_mfma_f32_16x16x32_bf16 v[220:223], v[140:143], v[72:75], 0\n" \
  "s_and_b64 vcc, vcc, s[86:87]\n" \
  "s_cbranch_scc1 .Lsg4_%=\n" \
  ".Lbg4_%=:\n" \
  "v_mfma_f32_16x16x32_bf16 v[224:227], v[144:147], v[72:75], 0\n" \
  "buffer_load_dwordx4 v[108:111], v59, s[36:39], s68 offen offset:0 sc1\n" \
  "ds_write_addtid_b32 v1 offset:33024\n" \
  "v_mfma_f32_16x16x32_bf16 v[228:231], v[148:151], v[72:75], 0\n" \
  "ds_write_addtid_b32 v2 offset:33280\n" \
  "s_waitcnt vmcnt(4)\n" \
  "v_mfma_f32_16x16x32_bf16 v[232:235], v[152:155], v[72:75], 0\n" \
  "ds_write_addtid_b32 v3 offset:33536\n" \
  "v_max3_u32 v52, v64, v65, v66\n" \
  "v_mfma_f32_16x16x32_bf16 v[236:239], v[156:159], v[72:75], 0\n" \
  "ds_write_addtid_b32 v4 offset:33792\n" \
  "v_max_u32 v52, v52, v67\n" \
  "v_mfma_f32_16x16x32_bf16 v[208:211], v[160:163], v[76:79], v[208:211]\n" \
  "buffer_load_dwordx4 v[112:115], v59, s[36:39], s68 offen offset:1024 sc1\n" \
  "ds_write_addtid_b32 v5 offset:34048\n" \
  "v_mfma_f32_16x16x32_bf16 v[212:215], v[164:167], v[76:79], v[212:215]\n" \
  "ds_write_addtid_b32 v6 offset:34304\n" \
  "v_cmp_eq_u32 vcc, -1, v52\n" \
  "v_mfma_f32_16x16x32_bf16 v[216:219], v[168:171], v[76:79], v[216:219]\n" \
  "ds_write_addtid_b32 v7 offset:34560\n" \
  "v_mfma_f32_16x16x32_bf16 v[220:223], v[172:175], v[76:79], v[220:223]\n" \
  "ds_write_addtid_b32 v8 offset:34816\n" \
  "s_cbranch_vccnz .Lse_%=\n" \
  ".Lbe_%=:\n" \
  "v_mfma_f32_16x16x32_bf16 v[224:227], v[176:179], v[76:79], v[224:227]\n" \
  "ds_write_addtid_b32 v9 offset:35072\n" \
  "v_lshlrev_b32 v52, 16, v64\n" \
  "v_mfma_f32_16x16x32_bf16 v[228:231], v[180:183], v[76:79], v[228:231]\n" \
  "ds_write_addtid_b32 v10 offset:35328\n" \
  "v_and_b32 v53, 0xffff0000, v64\n" \
  "v_mfma_f32_16x16x32_bf16 v[232:235], v[184:187], v[76:79], v[232:235]\n" \
  "buffer_load_dwordx4 v[116:119], v59, s[36:39], s68 offen offset:2048 sc1\n" \
  "ds_write_addtid_b32 v11 offset:35584\n" \
  "v_mfma_f32_16x16x32_bf16 v[236:239], v[188:191], v[76:79], v[236:239]\n" \
  "ds_write_addtid_b32 v12 offset:35840\n" \
  "v_mfma_f32_16x16x32_bf16 v[208:211], a[0:3], v[80:83], v[208:211]\n" \
  "ds_write_addtid_b32 v13 offset:36096\n" \
  "v_lshlrev_b32 v54, 16, v65\n" \
  "v_mfma_f32_16x16x32_bf16 v[212:215], a[4:7], v[80:83], v[212:215]\n" \
  "ds_write_addtid_b32 v14 offset:36352\n" \
  "v_and_b32 v55, 0xffff0000, v65\n" \
  "v_mfma_f32_16x16x32_bf16 v[216:219], a[8:11], v[80:83], v[216:219]\n" \
  "ds_write_addtid_b32 v15 offset:36608\n" \
  "v_lshlrev_b32 v252, 16, v66\n" \
  "v_mfma_f32_16x16x32_bf16 v[220:223], a[12:15], v[80:83], v[220:223]\n" \
  "ds_write_addtid_b32 v16 offset:36864\n" \
  "v_and_b32 v253, 0xffff0000, v66\n" \
  "v_mfma_f32_16x16x32_bf16 v[224:227], a[16:19], v[80:83], v[224:227]\n" \
  "buffer_load_dwordx4 v[120:123], v59, s[36:39], s69 offen offset:0 sc1\n" \
  "ds_write_addtid_b32 v17 offset:37120\n" \
  "v_mfma_f32_16x16x32_bf16 v[228:231], a[20:23], v[80:83], v[228:231]\n" \
  "ds_write_addtid_b32 v18 offset:37376\n" \
  "v_lshlrev_b32 v254, 16, v67\n" \
  "v_mfma_f32_16x16x32_bf16 v[232:235], a[24:27], v[80:83], v[232:235]\n" \
  "ds_write_addtid_b32 v19 offset:37632\n" \
  "v_and_b32 v255, 0xffff0000, v67\n" \
  "v_mfma_f32_16x16x32_bf16 v[236:239], a[28:31], v[80:83], v[236:239]\n" \
  "ds_write_addtid_b32 v20 offset:37888\n" \
  "v_add_f32 v52, v52, v54\n" \
  "v_mfma_f32_16x16x32_bf16 v[208:211], a[32:35], v[84:87], v[208:211]\n" \
  "ds_write_addtid_b32 v21 offset:38144\n" \
  "v_add_f32 v53, v53, v55\n" \
  "v_mfma_f32_16x16x32_bf16 v[212:215], a[36:39], v[84:87], v[212:215]\n" \
  "ds_write_addtid_b32 v22 offset:38400\n" \
  "v_add_f32 v252, v252, v254\n" \
  "v_mfma_f32_16x16x32_bf16 v[216:219], a[40:43], v[84:87], v[216:219]\n" \
  "buffer_load_dwordx4 v[124:127], v59, s[36:39], s69 offen offset:1024 sc1\n" \
  "ds_write_addtid_b32 v23 offset:38656\n" \
  "v_mfma_f32_16x16x32_bf16 v[220:223], a[44:47], v[84:87], v[220:223]\n" \
  "ds_write_addtid_b32 v24 offset:38912\n" \
  "v_add_f32 v253, v253, v255\n" \
  "v_mfma_f32_16x16x32_bf16 v[224:227], a[48:51], v[84:87], v[224:227]\n" \
  "ds_write_addtid_b32 v25 offset:39168\n" \
  "s_waitcnt lgkmcnt(15)\n" \
  "v_mfma_f32_16x16x32_bf16 v[228:231], a[52:55], v[84:87], v[228:231]\n" \
  "ds_write_addtid_b32 v26 offset:39424\n" \
  "v_add_f32 v52, v52, v70\n" \
  "v_mfma_f32_16x16x32_bf16 v[232:235], a[56:59], v[84:87], v[232:235]\n" \
  "ds_write_addtid_b32 v27 offset:39680\n" \
  "v_add_f32 v53, v53, v70\n" \
  "v_mfma_f32_16x16x32_bf16 v[236:239], a[60:63], v[84:87], v[236:239]\n" \
  "ds_write_addtid_b32 v28 offset:39936\n" \
  "s_waitcnt vmcnt(14)\n" \
  "v_mfma_f32_16x16x32_bf16 v[208:211], a[64:67], v[88:91], v[208:211]\n" \
  "buffer_load_dwordx4 v[192:195], v59, s[36:39], s69 offen offset:2048 sc1\n" \
  "ds_write_addtid_b32 v29 offset:40192\n" \
  "v_mfma_f32_16x16x32_bf16 v[212:215], a[68:71], v[88:91], v[212:215]\n" \
  "ds_write_addtid_b32 v30 offset:40448\n" \
  "v_lshlrev_b32 v54, 16, v71\n" \
  "v_mfma_f32_16x16x32_bf16 v[216:219], a[72:75], v[88:91], v[216:219]\n" \
  "ds_write_addtid_b32 v31 offset:40704\n" \
  "v_and_b32 v55, 0xffff0000, v71\n" \
  "v_mfma_f32_16x16x32_bf16 v[220:223], a[76:79], v[88:91], v[220:223]\n" \
  "v_lshlrev_b32 v254, 16, v68\n" \
  "v_and_b32 v255, 0xffff0000, v68\n" \
  "v_mfma_f32_16x16x32_bf16 v[224:227], a[80:83], v[88:91], v[224:227]\n" \
  "v_add_f32 v52, v52, v252\n" \
  "v_add_f32 v53, v53, v253\n" \
  "v_mfma_f32_16x16x32_bf16 v[228:231], a[84:87], v[88:91], v[228:231]\n" \
  "v_fma_f32 v52, v54, v52, v254\n" \
  "v_fma_f32 v53, v55, v53, v255\n" \
  "v_mfma_f32_16x16x32_bf16 v[232:235], a[88:91], v[88:91], v[232:235]\n" \
  "buffer_load_dwordx4 v[196:199], v59, s[36:39], s70 offen offset:0 sc1\n" \
  "v_cvt_pk_bf16_f32 v52, v52, v53\n" \
  "v_mfma_f32_16x16x32_bf16 v[236:239], a[92:95], v[88:91], v[236:239]\n" \
  "buffer_store_dword v52, v57, s[44:47], 0 offen offset:0 sc1\n" \
  "v_mfma_f32_16x16x32_bf16 v[208:211], a[96:99], v[92:95], v[208:211]\n" \
  "s_waitcnt lgkmcnt(0)\n" \
  "s_barrier\n" \
  "v_mfma_f32_16x16x32_bf16 v[212:215], a[100:103], v[92:95], v[212:215]\n" \
  "ds_read_b128 v[32:35], v58 offset:32768\n" \
  "v_mfma_f32_16x16x32_bf16 v[216:219], a[104:107], v[92:95], v[216:219]\n" \
  "ds_read_b128 v[36:39], v58 offset:40960\n" \
  "v_mfma_f32_16x16x32_bf16 v[220:223], a[108:111], v[92:95], v[220:223]\n" \
  "ds_read_b128 v[40:43], v58 offset:49152\n" \
  "v_mfma_f32_16x16x32_bf16 v[224:227], a[112:115], v[92:95], v[224:227]\n" \
  "buffer_load_dwordx4 v[200:203], v59, s[36:39], s70 offen offset:1024 sc1\n" \
  "ds_read_b128 v[44:47], v58 offset:57344\n" \
  "v_mfma_f32_16x16x32_bf16 v[228:231], a[116:119], v[92:95], v[228:231]\n" \
  "ds_read_b128 v[48:51], v58 offset:36864\n" \
  "v_mfma_f32_16x16x32_bf16 v[232:235], a[120:123], v[92:95], v[232:235]\n" \
  "ds_read_b128 v[240:243], v58 offset:45056\n" \
  "v_mfma_f32_16x16x32_bf16 v[236:239], a[124:127], v[92:95], v[236:239]\n" \
  "ds_read_b128 v[244:247], v58 offset:53248\n" \
  "s_waitcnt vmcnt(11)\n" \
  "v_mfma_f32_16x16x32_bf16 v[208:211], a[128:131], v[96:99], v[208:211]\n" \
  "ds_read_b128 v[248:251], v58 offset:61440\n" \
  "v_mfma_f32_16x16x32_bf16 v[212:215], a[132:135], v[96:99], v[212:215]\n" \
  "v_mfma_f32_16x16x32_bf16 v[216:219], a[136:139], v[96:99], v[216:219]\n" \
  "buffer_load_dwordx4 v[204:207], v59, s[36:39], s70 offen offset:2048 sc1\n" \
  "v_mfma_f32_16x16x32_bf16 v[220:223], a[140:143], v[96:99], v[220:223]\n" \
  "v_mfma_f32_16x16x32_bf16 v[224:227], a[144:147], v[96:99], v[224:227]\n" \
  "s_waitcnt lgkmcnt(4)\n" \
  "v_add_f32 v32, v32, v36\n" \
  "v_mfma_f32_16x16x32_bf16 v[228:231], a[148:151], v[96:99], v[228:231]\n" \
  "v_add_f32 v33, v33, v37\n" \
  "v_add_f32 v34, v34, v38\n" \
  "v_mfma_f32_16x16x32_bf16 v[232:235], a[152:155], v[96:99], v[232:235]\n" \
  "v_add_f32 v35, v35, v39\n" \
  "v_add_f32 v40, v40, v44\n" \
  "v_mfma_f32_16x16x32_bf16 v[236:239], a[156:159], v[96:99], v[236:239]\n" \
  "v_add_f32 v41, v41, v45\n" \
  "v_add_f32 v42, v42, v46\n" \
  "v_mfma_f32_16x16x32_bf16 v[208:211], a[160:163], v[100:103], v[208:211]\n" \
  "v_add_f32 v43, v43, v47\n" \
  "v_add_f32 v32, v32, v40\n" \
  "v_mfma_f32_16x16x32_bf16 v[212:215], a[164:167], v[100:103], v[212:215]\n" \
  "v_add_f32 v33, v33, v41\n" \
  "v_add_f32 v34, v34, v42\n" \
  "v_mfma_f32_16x16x32_bf16 v[216:219], a[168:171], v[100:103], v[216:219]\n" \
  "v_add_f32 v35, v35, v43\n" \
  "v_cvt_pk_bf16_f32 v36, v32, v33\n" \
  "v_mfma_f32_16x16x32_bf16 v[220:223], a[172:175], v[100:103], v[220:223]\n" \
  "v_cvt_pk_bf16_f32 v37, v34, v35\n" \
  "s_waitcnt lgkmcnt(0)\n" \
  "v_mfma_f32_16x16x32_bf16 v[224:227], a[176:179], v[100:103], v[224:227]\n" \
  "v_add_f32 v48, v48, v240\n" \
  "v_add_f32 v49, v49, v241\n" \
  "v_mfma_f32_16x16x32_bf16 v[228:231], a[180:183], v[100:103], v[228:231]\n" \
  "v_add_f32 v50, v50, v242\n" \
  "v_add_f32 v51, v51, v243\n" \
  "v_mfma_f32_16x16x32_bf16 v[232:235], a[184:187], v[100:103], v[232:235]\n" \
  "v_add_f32 v244, v244, v248\n" \
  "v_add_f32 v245, v245, v249\n" \
  "v_mfma_f32_16x16x32_bf16 v[236:239], a[188:191], v[100:103], v[236:239]\n" \
  "v_add_f32 v246, v246, v250\n" \
  "s_waitcnt vmcnt(11)\n" \
  "v_mfma_f32_16x16x32_bf16 v[208:211], a[192:195], v[104:107], v[208:211]\n" \
  "v_add_f32 v247, v247, v251\n" \
  "v_add_f32 v48, v48, v244\n" \
  "v_mfma_f32_16x16x32_bf16 v[212:215], a[196:199], v[104:107], v[212:215]\n" \
  "v_add_f32 v49, v49, v245\n" \
  "v_add_f32 v50, v50, v246\n" \
  "v_mfma_f32_16x16x32_bf16 v[216:219], a[200:203], v[104:107], v[216:219]\n" \
  "v_add_f32 v51, v51, v247\n" \
  "v_cvt_pk_bf16_f32 v38, v48, v49\n" \
  "v_mfma_f32_16x16x32_bf16 v[220:223], a[204:207], v[104:107], v[220:223]\n" \
  "v_cvt_pk_bf16_f32 v39, v50, v51\n" \
  "buffer_store_dwordx4 v[36:39], v59, s[36:39], s74 offen offset:0 sc1\n" \
  "v_mfma_f32_16x16x32_bf16 v[224:227], a[208:211], v[104:107], v[224:227]\n" \
  "v_mfma_f32_16x16x32_bf16 v[228:231], a[212:215], v[104:107], v[228:231]\n" \
  "v_mfma_f32_16x16x32_bf16 v[232:235], a[216:219], v[104:107], v[232:235]\n" \
  "v_mfma_f32_16x16x32_bf16 v[236:239], a[220:223], v[104:107], v[236:239]\n" \
  "s_waitcnt vmcnt(9)\n" \
  "v_mfma_f32_16x16x32_bf16 v[0:3], v[128:131], v[108:111], 0\n" \
  "v_add_u32 v59, 409600, v59\n" \
  "v_add_u32 v60, s98, v58\n" \
  "v_mfma_f32_16x16x32_bf16 v[4:7], v[132:135], v[108:111], 0\n" \
  "ds_write_addtid_b32 v208 offset:0\n" \
  "v_mfma_f32_16x16x32_bf16 v[8:11], v[136:139], v[108:111], 0\n" \
  "buffer_load_dwordx4 v[72:75], v59, s[36:39], s68 offen offset:0 sc1\n" \
  "ds_write_addtid_b32 v209 offset:256\n" \
  "v_mfma_f32_16x16x32_bf16 v[12:15], v[140:143], v[108:111], 0\n" \
  "ds_write_addtid_b32 v210 offset:512\n" \
  "v_mfma_f32_16x16x32_bf16 v[16:19], v[144:147], v[108:111], 0\n" \
  "ds_write_addtid_b32 v211 offset:768\n" \
  "v_mfma_f32_16x16x32_bf16 v[20:23], v[148:151], v[108:111], 0\n" \
  "buffer_load_dwordx4 v[64:67], v59, s[36:39], s71 offen offset:0 sc1\n" \
  "ds_write_addtid_b32 v212 offset:1024\n" \
  "v_mfma_f32_16x16x32_bf16 v[24:27], v[152:155], v[108:111], 0\n" \
  "ds_write_addtid_b32 v213 offset:1280\n" \
  "v_mfma_f32_16x16x32_bf16 v[28:31], v[156:159], v[108:111], 0\n" \
  "ds_write_addtid_b32 v214 offset:1536\n" \
  "v_mfma_f32_16x16x32_bf16 v[0:3], v[160:163], v[112:115], v[0:3]\n" \
  "buffer_load_dwordx4 v[76:79], v59, s[36:39], s68 offen offset:1024 sc1\n" \
  "ds_write_addtid_b32 v215 offset:1792\n" \
  "v_mfma_f32_16x16x32_bf16 v[4:7], v[164:167], v[112:115], v[4:7]\n" \
  "ds_write_addtid_b32 v216 offset:2048\n" \
  "v_mfma_f32_16x16x32_bf16 v[8:11], v[168:171], v[112:115], v[8:11]\n" \
  "ds_write_addtid_b32 v217 offset:2304\n" \
  "v_mfma_f32_16x16x32_bf16 v[12:15], v[172:175], v[112:115], v[12:15]\n" \
  "ds_write_addtid_b32 v218 offset:2560\n" \
  "v_mfma_f32_16x16x32_bf16 v[16:19], v[176:179], v[112:115], v[16:19]\n" \
  "ds_write_addtid_b32 v219 offset:2816\n" \
  "v_mfma_f32_16x16x32_bf16 v[20:23], v[180:183], v[112:115], v[20:23]\n" \
  "ds_write_addtid_b32 v220 offset:3072\n" \
  "v_mfma_f32_16x16x32_bf16 v[24:27], v[184:187], v[112:115], v[24:27]\n" \
  "buffer_load_dwordx4 v[80:83], v59, s[36:39], s68 offen offset:2048 sc1\n" \
  "ds_write_addtid_b32 v221 offset:3328\n" \
  "v_mfma_f32_16x16x32_bf16 v[28:31], v[188:191], v[112:115], v[28:31]\n" \
  "ds_write_addtid_b32 v222 offset:3584\n" \
  "s_waitcnt vmcnt(11)\n" \
  "v_mfma_f32_16x16x32_bf16 v[0:3], a[0:3], v[116:119], v[0:3]\n" \
  "ds_write_addtid_b32 v223 offset:3840\n" \
  "v_mfma_f32_16x16x32_bf16 v[4:7], a[4:7], v[116:119], v[4:7]\n" \
  "buffer_load_dwordx4 v[52:55], v59, s[36:39], s76 offen offset:0 sc1\n" \
  "ds_write_addtid_b32 v224 offset:4096\n" \
  "v_mfma_f32_16x16x32_bf16 v[8:11], a[8:11], v[116:119], v[8:11]\n" \
  "ds_write_addtid_b32 v225 offset:4352\n" \
  "v_mfma_f32_16x16x32_bf16 v[12:15], a[12:15], v[116:119], v[12:15]\n" \
  "ds_write_addtid_b32 v226 offset:4608\n" \
  "v_mfma_f32_16x16x32_bf16 v[16:19], a[16:19], v[116:119], v[16:19]\n" \
  "buffer_load_dwordx4 v[84:87], v59, s[36:39], s69 offen offset:0 sc1\n" \
  "ds_write_addtid_b32 v227 offset:4864\n" \
  "v_mfma_f32_16x16x32_bf16 v[20:23], a[20:23], v[116:119], v[20:23]\n" \
  "ds_write_addtid_b32 v228 offset:5120\n" \
  "v_mfma_f32_16x16x32_bf16 v[24:27], a[24:27], v[116:119], v[24:27]\n" \
  "ds_write_addtid_b32 v229 offset:5376\n" \
  "v_mfma_f32_16x16x32_bf16 v[28:31], a[28:31], v[116:119], v[28:31]\n" \
  "ds_write_addtid_b32 v230 offset:5632\n" \
  "v_mfma_f32_16x16x32_bf16 v[0:3], a[32:35], v[120:123], v[0:3]\n" \
  "ds_write_addtid_b32 v231 offset:5888\n" \
  "v_mfma_f32_16x16x32_bf16 v[4:7], a[36:39], v[120:123], v[4:7]\n" \
  "ds_write_addtid_b32 v232 offset:6144\n" \
  "v_mfma_f32_16x16x32_bf16 v[8:11], a[40:43], v[120:123], v[8:11]\n" \
  "buffer_load_dwordx4 v[88:91], v59, s[36:39], s69 offen offset:1024 sc1\n" \
  "ds_write_addtid_b32 v233 offset:6400\n" \
  "v_mfma_f32_16x16x32_bf16 v[12:15], a[44:47], v[120:123], v[12:15]\n" \
  "ds_write_addtid_b32 v234 offset:6656\n" \
  "v_mfma_f32_16x16x32_bf16 v[16:19], a[48:51], v[120:123], v[16:19]\n" \
  "ds_write_addtid_b32 v235 offset:6912\n" \
  "v_mfma_f32_16x16x32_bf16 v[20:23], a[52:55], v[120:123], v[20:23]\n" \
  "ds_write_addtid_b32 v236 offset:7168\n" \
  "v_mfma_f32_16x16x32_bf16 v[24:27], a[56:59], v[120:123], v[24:27]\n" \
  "ds_write_addtid_b32 v237 offset:7424\n" \
  "v_mfma_f32_16x16x32_bf16 v[28:31], a[60:63], v[120:123], v[28:31]\n" \
  "ds_write_addtid_b32 v238 offset:7680\n" \
  "s_waitcnt vmcnt(12)\n" \
  "v_mfma_f32_16x16x32_bf16 v[0:3], a[64:67], v[124:127], v[0:3]\n" \
  "buffer_load_dwordx4 v[92:95], v59, s[36:39], s69 offen offset:2048 sc1\n" \
  "ds_write_addtid_b32 v239 offset:7936\n" \
  "v_mfma_f32_16x16x32_bf16 v[4:7], a[68:71], v[124:127], v[4:7]\n" \
  "ds_read_b32 v61, v60 offset:28676\n" \
  "v_mfma_f32_16x16x32_bf16 v[8:11], a[72:75], v[124:127], v[8:11]\n" \
  "v_mfma_f32_16x16x32_bf16 v[12:15], a[76:79], v[124:127], v[12:15]\n" \
  "v_mfma_f32_16x16x32_bf16 v[16:19], a[80:83], v[124:127], v[16:19]\n" \
  "v_mfma_f32_16x16x32_bf16 v[20:23], a[84:87], v[124:127], v[20:23]\n" \
  "v_mfma_f32_16x16x32_bf16 v[24:27], a[88:91], v[124:127], v[24:27]\n" \
  "buffer_load_dwordx4 v[96:99], v59, s[36:39], s70 offen offset:0 sc1\n" \
  "v_mfma_f32_16x16x32_bf16 v[28:31], a[92:95], v[124:127], v[28:31]\n" \
  "s_waitcnt lgkmcnt(0)\n" \
  "s_barrier\n" \
  "v_mfma_f32_16x16x32_bf16 v[0:3], a[96:99], v[192:195], v[0:3]\n" \
  "ds_read_b128 v[32:35], v58 offset:0\n" \
  "v_mfma_f32_16x16x32_bf16 v[4:7], a[100:103], v[192:195], v[4:7]\n" \
  "ds_read_b128 v[36:39], v58 offset:8192\n" \
  "v_mfma_f32_16x16x32_bf16 v[8:11], a[104:107], v[192:195], v[8:11]\n" \
  "ds_read_b128 v[40:43], v58 offset:16384\n" \
  "v_lshlrev_b32 v60, 16, v61\n" \
  "v_mfma_f32_16x16x32_bf16 v[12:15], a[108:111], v[192:195], v[12:15]\n" \
  "ds_read_b128 v[44:47], v58 offset:24576\n" \
  "v_and_b32 v61, 0xffff0000, v61\n" \
  "v_mfma_f32_16x16x32_bf16 v[16:19], a[112:115], v[192:195], v[16:19]\n" \
  "buffer_load_dwordx4 v[100:103], v59, s[36:39], s70 offen offset:1024 sc1\n" \
  "ds_read_b128 v[48:51], v58 offset:4096\n" \
  "v_mfma_f32_16x16x32_bf16 v[20:23], a[116:119], v[192:195], v[20:23]\n" \
  "ds_read_b128 v[240:243], v58 offset:12288\n" \
  "v_mfma_f32_16x16x32_bf16 v[24:27], a[120:123], v[192:195], v[24:27]\n" \
  "ds_read_b128 v[244:247], v58 offset:20480\n" \
  "v_mfma_f32_16x16x32_bf16 v[28:31], a[124:127], v[192:195], v[28:31]\n" \
  "ds_read_b128 v[248:251], v58 offset:28672\n" \
  "s_waitcnt vmcnt(12)\n" \
  "v_mfma_f32_16x16x32_bf16 v[0:3], a[128:131], v[196:199], v[0:3]\n" \
  "v_mfma_f32_16x16x32_bf16 v[4:7], a[132:135], v[196:199], v[4:7]\n" \
  "v_mfma_f32_16x16x32_bf16 v[8:11], a[136:139], v[196:199], v[8:11]\n" \
  "buffer_load_dwordx4 v[104:107], v59, s[36:39], s70 offen offset:2048 sc1\n" \
  "v_mfma_f32_16x16x32_bf16 v[12:15], a[140:143], v[196:199], v[12:15]\n" \
  "s_waitcnt lgkmcnt(4)\n" \
  "v_add_f32 v32, v32, v36\n" \
  "v_mfma_f32_16x16x32_bf16 v[16:19], a[144:147], v[196:199], v[16:19]\n" \
  "v_add_f32 v33, v33, v37\n" \
  "v_add_f32 v34, v34, v38\n" \
  "v_mfma_f32_16x16x32_bf16 v[20:23], a[148:151], v[196:199], v[20:23]\n" \
  "v_add_f32 v35, v35, v39\n" \
  "v_add_f32 v40, v40, v44\n" \
  "v_mfma_f32_16x16x32_bf16 v[24:27], a[152:155], v[196:199], v[24:27]\n" \
  "v_add_f32 v41, v41, v45\n" \
  "v_add_f32 v42, v42, v46\n" \
  "v_mfma_f32_16x16x32_bf16 v[28:31], a[156:159], v[196:199], v[28:31]\n" \
  "v_add_f32 v43, v43, v47\n" \
  "v_add_f32 v32, v32, v40\n" \
  "v_mfma_f32_16x16x32_bf16 v[0:3], a[160:163], v[200:203], v[0:3]\n" \
  "v_add_f32 v33, v33, v41\n" \
  "v_add_f32 v34, v34, v42\n" \
  "v_mfma_f32_16x16x32_bf16 v[4:7], a[164:167], v[200:203], v[4:7]\n" \
  "v_add_f32 v35, v35, v43\n" \
  "v_cvt_pk_bf16_f32 v36, v32, v33\n" \
  "v_mfma_f32_16x16x32_bf16 v[8:11], a[168:171], v[200:203], v[8:11]\n" \
  "v_cvt_pk_bf16_f32 v37, v34, v35\n" \
  "s_waitcnt lgkmcnt(0)\n" \
  "v_mfma_f32_16x16x32_bf16 v[12:15], a[172:175], v[200:203], v[12:15]\n" \
  "v_add_f32 v48, v48, v240\n" \
  "v_add_f32 v49, v49, v241\n" \
  "v_mfma_f32_16x16x32_bf16 v[16:19], a[176:179], v[200:203], v[16:19]\n" \
  "v_add_f32 v50, v50, v242\n" \
  "v_add_f32 v51, v51, v243\n" \
  "v_mfma_f32_16x16x32_bf16 v[20:23], a[180:183], v[200:203], v[20:23]\n" \
  "v_add_f32 v244, v244, v248\n" \
  "v_add_f32 v245, v245, v249\n" \
  "v_mfma_f32_16x16x32_bf16 v[24:27], a[184:187], v[200:203], v[24:27]\n" \
  "v_add_f32 v246, v246, v250\n" \
  "v_add_f32 v247, v247, v251\n" \
  "v_mfma_f32_16x16x32_bf16 v[28:31], a[188:191], v[200:203], v[28:31]\n" \
  "v_add_f32 v48, v48, v244\n" \
  "s_waitcnt vmcnt(12)\n" \
  "v_mfma_f32_16x16x32_bf16 v[0:3], a[192:195], v[204:207], v[0:3]\n" \
  "v_add_f32 v49, v49, v245\n" \
  "v_add_f32 v50, v50, v246\n" \
  "v_mfma_f32_16x16x32_bf16 v[4:7], a[196:199], v[204:207], v[4:7]\n" \
  "v_add_f32 v51, v51, v247\n" \
  "v_cvt_pk_bf16_f32 v38, v48, v49\n" \
  "v_mfma_f32_16x16x32_bf16 v[8:11], a[200:203], v[204:207], v[8:11]\n" \
  "v_cvt_pk_bf16_f32 v39, v50, v51\n" \
  "buffer_store_dwordx4 v[36:39], v59, s[36:39], s74 offen offset:0 sc1\n" \
  "v_mfma_f32_16x16x32_bf16 v[12:15], a[204:207], v[204:207], v[12:15]\n" \
  "v_mfma_f32_16x16x32_bf16 v[16:19], a[208:211], v[204:207], v[16:19]\n" \
  "v_mfma_f32_16x16x32_bf16 v[20:23], a[212:215], v[204:207], v[20:23]\n" \
  "v_mfma_f32_16x16x32_bf16 v[24:27], a[216:219], v[204:207], v[24:27]\n" \
  "v_mfma_f32_16x16x32_bf16 v[28:31], a[220:223], v[204:207], v[28:31]\n" \
  "s_waitcnt vmcnt(9)\n" \
  "v_mfma_f32_16x16x32_bf16 v[208:211], v[128:131], v[72:75], 0\n" \
  "v_add_u32 v59, 409600, v59\n" \
  "s_waitcnt vmcnt(7)\n" \
  "v_mfma_f32_16x16x32_bf16 v[212:215], v[132:135], v[72:75], 0\n" \
  "ds_write_addtid_b32 v0 offset:32768\n" \
  "v_max_u32 v62, v64, v52\n" \
  "v_mfma_f32_16x16x32_bf16 v[216:219], v[136:139], v[72:75], 0\n" \
  "buffer_load_dwordx4 v[108:111], v59, s[36:39], s68 offen offset:0 sc1\n" \
  "ds_write_addtid_b32 v1 offset:33024\n" \
  "v_mfma_f32_16x16x32_bf16 v[220:223], v[140:143], v[72:75], 0\n" \
  "ds_write_addtid_b32 v2 offset:33280\n" \
  "v_cmp_eq_u32 vcc, -1, v62\n" \
  "v_mfma_f32_16x16x32_bf16 v[224:227], v[144:147], v[72:75], 0\n" \
  "ds_write_addtid_b32 v3 offset:33536\n" \
  "v_mfma_f32_16x16x32_bf16 v[228:231], v[148:151], v[72:75], 0\n" \
  "ds_write_addtid_b32 v4 offset:33792\n" \
  "s_cbranch_vccnz .Lsp1_%=\n" \
  ".Lbp1_%=:\n" \
  "v_mfma_f32_16x16x32_bf16 v[232:235], v[152:155], v[72:75], 0\n" \
  "ds_write_addtid_b32 v5 offset:34048\n" \
  "v_and_b32 v71, 0xffff0000, v67\n" \
  "v_mfma_f32_16x16x32_bf16 v[236:239], v[156:159], v[72:75], 0\n" \
  "ds_write_addtid_b32 v6 offset:34304\n" \
  "v_lshlrev_b32 v70, 16, v67\n" \
  "v_mfma_f32_16x16x32_bf16 v[208:211], v[160:163], v[76:79], v[208:211]\n" \
  "buffer_load_dwordx4 v[112:115], v59, s[36:39], s68 offen offset:1024 sc1\n" \
  "ds_write_addtid_b32 v7 offset:34560\n" \
  "v_mfma_f32_16x16x32_bf16 v[212:215], v[164:167], v[76:79], v[212:215]\n" \
  "ds_write_addtid_b32 v8 offset:34816\n" \
  "v_and_b32 v69, 0xffff0000, v66\n" \
  "v_mfma_f32_16x16x32_bf16 v[216:219], v[168:171], v[76:79], v[216:219]\n" \
  "ds_write_addtid_b32 v9 offset:35072\n" \
  "v_lshlrev_b32 v68, 16, v66\n" \
  "v_mfma_f32_16x16x32_bf16 v[220:223], v[172:175], v[76:79], v[220:223]\n" \
  "ds_write_addtid_b32 v10 offset:35328\n" \
  "v_and_b32 v67, 0xffff0000, v65\n" \
  "v_mfma_f32_16x16x32_bf16 v[224:227], v[176:179], v[76:79], v[224:227]\n" \
  "ds_write_addtid_b32 v11 offset:35584\n" \
  "v_lshlrev_b32 v66, 16, v65\n" \
  "v_mfma_f32_16x16x32_bf16 v[228:231], v[180:183], v[76:79], v[228:231]\n" \
  "ds_write_addtid_b32 v12 offset:35840\n" \
  "v_and_b32 v65, 0xffff0000, v64\n" \
  "v_mfma_f32_16x16x32_bf16 v[232:235], v[184:187], v[76:79], v[232:235]\n" \
  "buffer_load_dwordx4 v[116:119], v59, s[36:39], s68 offen offset:2048 sc1\n" \
  "ds_write_addtid_b32 v13 offset:36096\n" \
  "v_mfma_f32_16x16x32_bf16 v[236:239], v[188:191], v[76:79], v[236:239]\n" \
  "ds_write_addtid_b32 v14 offset:36352\n" \
  "s_waitcnt vmcnt(9)\n" \
  "v_mfma_f32_16x16x32_bf16 v[208:211], a[0:3], v[80:83], v[208:211]\n" \
  "ds_write_addtid_b32 v15 offset:36608\n" \
  "v_lshlrev_b32 v64, 16, v64\n" \
  "v_mfma_f32_16x16x32_bf16 v[212:215], a[4:7], v[80:83], v[212:215]\n" \
  "ds_write_addtid_b32 v16 offset:36864\n" \
  "v_add_f32 v64, v64, v60\n" \
  "v_mfma_f32_16x16x32_bf16 v[216:219], a[8:11], v[80:83], v[216:219]\n" \
  "ds_write_addtid_b32 v17 offset:37120\n" \
  "v_add_f32 v65, v65, v60\n" \
  "v_mfma_f32_16x16x32_bf16 v[220:223], a[12:15], v[80:83], v[220:223]\n" \
  "ds_write_addtid_b32 v18 offset:37376\n" \
  "v_add_f32 v66, v66, v60\n" \
  "v_mfma_f32_16x16x32_bf16 v[224:227], a[16:19], v[80:83], v[224:227]\n" \
  "buffer_load_dwordx4 v[120:123], v59, s[36:39], s69 offen offset:0 sc1\n" \
  "ds_write_addtid_b32 v19 offset:37632\n" \
  "v_mfma_f32_16x16x32_bf16 v[228:231], a[20:23], v[80:83], v[228:231]\n" \
  "ds_write_addtid_b32 v20 offset:37888\n" \
  "v_add_f32 v67, v67, v60\n" \
  "v_mfma_f32_16x16x32_bf16 v[232:235], a[24:27], v[80:83], v[232:235]\n" \
  "ds_write_addtid_b32 v21 offset:38144\n" \
  "v_add_f32 v68, v68, v61\n" \
  "v_mfma_f32_16x16x32_bf16 v[236:239], a[28:31], v[80:83], v[236:239]\n" \
  "ds_write_addtid_b32 v22 offset:38400\n" \
  "v_add_f32 v69, v69, v61\n" \
  "v_mfma_f32_16x16x32_bf16 v[208:211], a[32:35], v[84:87], v[208:211]\n" \
  "ds_write_addtid_b32 v23 offset:38656\n" \
  "v_add_f32 v70, v70, v61\n" \
  "v_mfma_f32_16x16x32_bf16 v[212:215], a[36:39], v[84:87], v[212:215]\n" \
  "ds_write_addtid_b32 v24 offset:38912\n" \
  "v_add_f32 v71, v71, v61\n" \
  "v_mfma_f32_16x16x32_bf16 v[216:219], a[40:43], v[84:87], v[216:219]\n" \
  "buffer_load_dwordx4 v[124:127], v59, s[36:39], s69 offen offset:1024 sc1\n" \
  "ds_write_addtid_b32 v25 offset:39168\n" \
  "v_mfma_f32_16x16x32_bf16 v[220:223], a[44:47], v[84:87], v[220:223]\n" \
  "ds_write_addtid_b32 v26 offset:39424\n" \
  "v_and_b32 v255, 0xffff0000, v55\n" \
  "v_mfma_f32_16x16x32_bf16 v[224:227], a[48:51], v[84:87], v[224:227]\n" \
  "ds_write_addtid_b32 v27 offset:39680\n" \
  "v_lshlrev_b32 v254, 16, v55\n" \
  "v_mfma_f32_16x16x32_bf16 v[228:231], a[52:55], v[84:87], v[228:231]\n" \
  "ds_write_addtid_b32 v28 offset:39936\n" \
  "v_and_b32 v253, 0xffff0000, v54\n" \
  "v_mfma_f32_16x16x32_bf16 v[232:235], a[56:59], v[84:87], v[232:235]\n" \
  "ds_write_addtid_b32 v29 offset:40192\n" \
  "v_lshlrev_b32 v252, 16, v54\n" \
  "v_mfma_f32_16x16x32_bf16 v[236:239], a[60:63], v[84:87], v[236:239]\n" \
  "ds_write_addtid_b32 v30 offset:40448\n" \
  "s_waitcnt vmcnt(9)\n" \
  "v_mfma_f32_16x16x32_bf16 v[208:211], a[64:67], v[88:91], v[208:211]\n" \
  "buffer_load_dwordx4 v[192:195], v59, s[36:39], s69 offen offset:2048 sc1\n" \
  "ds_write_addtid_b32 v31 offset:40704\n" \
  "v_mfma_f32_16x16x32_bf16 v[212:215], a[68:71], v[88:91], v[212:215]\n" \
  "v_and_b32 v55, 0xffff0000, v53\n" \
  "v_lshlrev_b32 v54, 16, v53\n" \
  "v_mfma_f32_16x16x32_bf16 v[216:219], a[72:75], v[88:91], v[216:219]\n" \
  "v_and_b32 v53, 0xffff0000, v52\n" \
  "v_lshlrev_b32 v52, 16, v52\n" \
  "v_mfma_f32_16x16x32_bf16 v[220:223], a[76:79], v[88:91], v[220:223]\n" \
  "v_add_f32 v52, v52, v60\n" \
  "v_add_f32 v53, v53, v60\n" \
  "v_mfma_f32_16x16x32_bf16 v[224:227], a[80:83], v[88:91], v[224:227]\n" \
  "v_add_f32 v54, v54, v60\n" \
  "v_add_f32 v55, v55, v60\n" \
  "v_mfma_f32_16x16x32_bf16 v[228:231], a[84:87], v[88:91], v[228:231]\n" \
  "v_add_f32 v252, v252, v61\n" \
  "v_add_f32 v253, v253, v61\n" \
  "v_mfma_f32_16x16x32_bf16 v[232:235], a[88:91], v[88:91], v[232:235]\n" \
  "buffer_load_dwordx4 v[196:199], v59, s[36:39], s70 offen offset:0 sc1\n" \
  "v_add_f32 v254, v254, v61\n" \
  "v_mfma_f32_16x16x32_bf16 v[236:239], a[92:95], v[88:91], v[236:239]\n" \
  "s_waitcnt lgkmcnt(0)\n" \
  "s_barrier\n" \
  "v_mfma_f32_16x16x32_bf16 v[208:211], a[96:99], v[92:95], v[208:211]\n" \
  "ds_read_b128 v[32:35], v58 offset:32768\n" \
  "v_add_f32 v255, v255, v61\n" \
  "v_mfma_f32_16x16x32_bf16 v[212:215], a[100:103], v[92:95], v[212:215]\n" \
  "ds_read_b128 v[36:39], v58 offset:40960\n" \
  "v_mfma_f32_16x16x32_bf16 v[216:219], a[104:107], v[92:95], v[216:219]\n" \
  "ds_read_b128 v[40:43], v58 offset:49152\n" \
  "v_mfma_f32_16x16x32_bf16 v[220:223], a[108:111], v[92:95], v[220:223]\n" \
  "ds_read_b128 v[44:47], v58 offset:57344\n" \
  "v_mfma_f32_16x16x32_bf16 v[224:227], a[112:115], v[92:95], v[224:227]\n" \
  "buffer_load_dwordx4 v[200:203], v59, s[36:39], s70 offen offset:1024 sc1\n" \
  "ds_read_b128 v[48:51], v58 offset:36864\n" \
  "v_mfma_f32_16x16x32_bf16 v[228:231], a[116:119], v[92:95], v[228:231]\n" \
  "ds_read_b128 v[240:243], v58 offset:45056\n" \
  "v_mfma_f32_16x16x32_bf16 v[232:235], a[120:123], v[92:95], v[232:235]\n" \
  "ds_read_b128 v[244:247], v58 offset:53248\n" \
  "v_mfma_f32_16x16x32_bf16 v[236:239], a[124:127], v[92:95], v[236:239]\n" \
  "buffer_load_dwordx4 v[60:63], v59, s[36:39], s72 offen offset:0 sc1\n" \
  "s_waitcnt vmcnt(11)\n" \
  "v_mfma_f32_16x16x32_bf16 v[208:211], a[128:131], v[96:99], v[208:211]\n" \
  "ds_read_b128 v[248:251], v58 offset:61440\n" \
  "v_mfma_f32_16x16x32_bf16 v[212:215], a[132:135], v[96:99], v[212:215]\n" \
  "v_mfma_f32_16x16x32_bf16 v[216:219], a[136:139], v[96:99], v[216:219]\n" \
  "buffer_load_dwordx4 v[204:207], v59, s[36:39], s70 offen offset:2048 sc1\n" \
  "v_mfma_f32_16x16x32_bf16 v[220:223], a[140:143], v[96:99], v[220:223]\n" \
  "s_waitcnt lgkmcnt(4)\n" \
  "v_add_f32 v32, v32, v36\n" \
  "v_mfma_f32_16x16x32_bf16 v[224:227], a[144:147], v[96:99], v[224:227]\n" \
  "v_add_f32 v33, v33, v37\n" \
  "v_add_f32 v34, v34, v38\n" \
  "v_mfma_f32_16x16x32_bf16 v[228:231], a[148:151], v[96:99], v[228:231]\n" \
  "v_add_f32 v35, v35, v39\n" \
  "v_add_f32 v40, v40, v44\n" \
  "v_mfma_f32_16x16x32_bf16 v[232:235], a[152:155], v[96:99], v[232:235]\n" \
  "v_add_f32 v41, v41, v45\n" \
  "v_add_f32 v42, v42, v46\n" \
  "v_mfma_f32_16x16x32_bf16 v[236:239], a[156:159], v[96:99], v[236:239]\n" \
  "v_add_f32 v43, v43, v47\n" \
  "v_add_f32 v32, v32, v40\n" \
  "v_mfma_f32_16x16x32_bf16 v[208:211], a[160:163], v[100:103], v[208:211]\n" \
  "v_add_f32 v33, v33, v41\n" \
  "v_add_f32 v34, v34, v42\n" \
  "v_mfma_f32_16x16x32_bf16 v[212:215], a[164:167], v[100:103], v[212:215]\n" \
  "v_add_f32 v35, v35, v43\n" \
  "v_cvt_pk_bf16_f32 v36, v32, v33\n" \
  "v_mfma_f32_16x16x32_bf16 v[216:219], a[168:171], v[100:103], v[216:219]\n" \
  "v_cvt_pk_bf16_f32 v37, v34, v35\n" \
  "s_waitcnt lgkmcnt(0)\n" \
  "v_mfma_f32_16x16x32_bf16 v[220:223], a[172:175], v[100:103], v[220:223]\n" \
  "v_add_f32 v48, v48, v240\n" \
  "v_add_f32 v49, v49, v241\n" \
  "v_mfma_f32_16x16x32_bf16 v[224:227], a[176:179], v[100:103], v[224:227]\n" \
  "v_add_f32 v50, v50, v242\n" \
  "v_add_f32 v51, v51, v243\n" \
  "v_mfma_f32_16x16x32_bf16 v[228:231], a[180:183], v[100:103], v[228:231]\n" \
  "v_add_f32 v244, v244, v248\n" \
  "v_add_f32 v245, v245, v249\n" \
  "v_mfma_f32_16x16x32_bf16 v[232:235], a[184:187], v[100:103], v[232:235]\n" \
  "v_add_f32 v246, v246, v250\n" \
  "v_add_f32 v247, v247, v251\n" \
  "v_mfma_f32_16x16x32_bf16 v[236:239], a[188:191], v[100:103], v[236:239]\n" \
  "v_add_f32 v48, v48, v244\n" \
  "s_waitcnt vmcnt(11)\n" \
  "v_mfma_f32_16x16x32_bf16 v[208:211], a[192:195], v[104:107], v[208:211]\n" \
  "v_add_f32 v49, v49, v245\n" \
  "v_add_f32 v50, v50, v246\n" \
  "v_mfma_f32_16x16x32_bf16 v[212:215], a[196:199], v[104:107], v[212:215]\n" \
  "v_add_f32 v51, v51, v247\n" \
  "v_cvt_pk_bf16_f32 v38, v48, v49\n" \
  "v_mfma_f32_16x16x32_bf16 v[216:219], a[200:203], v[104:107], v[216:219]\n" \
  "v_cvt_pk_bf16_f32 v39, v50, v51\n" \
  "buffer_store_dwordx4 v[36:39], v59, s[36:39], s74 offen offset:0 sc1\n" \
  "v_mfma_f32_16x16x32_bf16 v[220:223], a[204:207], v[104:107], v[220:223]\n" \
  "v_mfma_f32_16x16x32_bf16 v[224:227], a[208:211], v[104:107], v[224:227]\n" \
  "v_mfma_f32_16x16x32_bf16 v[228:231], a[212:215], v[104:107], v[228:231]\n" \
  "v_mfma_f32_16x16x32_bf16 v[232:235], a[216:219], v[104:107], v[232:235]\n" \
  "v_mfma_f32_16x16x32_bf16 v[236:239], a[220:223], v[104:107], v[236:239]\n" \
  "s_waitcnt vmcnt(9)\n" \
  "v_mfma_f32_16x16x32_bf16 v[0:3], v[128:131], v[108:111], 0\n" \
  "v_add_u32 v59, 409600, v59\n" \
  "v_mfma_f32_16x16x32_bf16 v[4:7], v[132:135], v[108:111], 0\n" \
  "ds_write_addtid_b32 v208 offset:0\n" \
  "ds_write_addtid_b32 v209 offset:256\n" \
  "v_mfma_f32_16x16x32_bf16 v[8:11], v[136:139], v[108:111], 0\n" \
  "buffer_load_dwordx4 v[72:75], v59, s[36:39], s68 offen offset:0 sc1\n" \
  "ds_write_addtid_b32 v210 offset:512\n" \
  "v_mfma_f32_16x16x32_bf16 v[12:15], v[140:143], v[108:111], 0\n" \
  "ds_write_addtid_b32 v211 offset:768\n" \
  "ds_write_addtid_b32 v212 offset:1024\n" \
  "v_mfma_f32_16x16x32_bf16 v[16:19], v[144:147], v[108:111], 0\n" \
  "ds_write_addtid_b32 v213 offset:1280\n" \
  "ds_write_addtid_b32 v214 offset:1536\n" \
  "v_mfma_f32_16x16x32_bf16 v[20:23], v[148:151], v[108:111], 0\n" \
  "ds_write_addtid_b32 v215 offset:1792\n" \
  "ds_write_addtid_b32 v216 offset:2048\n" \
  "v_mfma_f32_16x16x32_bf16 v[24:27], v[152:155], v[108:111], 0\n" \
  "ds_write_addtid_b32 v217 offset:2304\n" \
  "ds_write_addtid_b32 v218 offset:2560\n" \
  "v_mfma_f32_16x16x32_bf16 v[28:31], v[156:159], v[108:111], 0\n" \
  "ds_write_addtid_b32 v219 offset:2816\n" \
  "ds_write_addtid_b32 v220 offset:3072\n" \
  "v_mfma_f32_16x16x32_bf16 v[0:3], v[160:163], v[112:115], v[0:3]\n" \
  "buffer_load_dwordx4 v[76:79], v59, s[36:39], s68 offen offset:1024 sc1\n" \
  "ds_write_addtid_b32 v221 offset:3328\n" \
  "v_mfma_f32_16x16x32_bf16 v[4:7], v[164:167], v[112:115], v[4:7]\n" \
  "ds_write_addtid_b32 v222 offset:3584\n" \
  "ds_write_addtid_b32 v223 offset:3840\n" \
  "v_mfma_f32_16x16x32_bf16 v[8:11], v[168:171], v[112:115], v[8:11]\n" \
  "ds_write_addtid_b32 v224 offset:4096\n" \
  "ds_write_addtid_b32 v225 offset:4352\n" \
  "v_mfma_f32_16x16x32_bf16 v[12:15], v[172:175], v[112:115], v[12:15]\n" \
  "buffer_load_dwordx4 v[108:111], v59, s[36:39], s77 offen offset:0 sc1\n" \
  "ds_write_addtid_b32 v226 offset:4608\n" \
  "v_mfma_f32_16x16x32_bf16 v[16:19], v[176:179], v[112:115], v[16:19]\n" \
  "ds_write_addtid_b32 v227 offset:4864\n" \
  "ds_write_addtid_b32 v228 offset:5120\n" \
  "v_mfma_f32_16x16x32_bf16 v[20:23], v[180:183], v[112:115], v[20:23]\n" \
  "ds_write_addtid_b32 v229 offset:5376\n" \
  "ds_write_addtid_b32 v230 offset:5632\n" \
  "v_mfma_f32_16x16x32_bf16 v[24:27], v[184:187], v[112:115], v[24:27]\n" \
  "buffer_load_dwordx4 v[80:83], v59, s[36:39], s68 offen offset:2048 sc1\n" \
  "ds_write_addtid_b32 v231 offset:5888\n" \
  "v_mfma_f32_16x16x32_bf16 v[28:31], v[188:191], v[112:115], v[28:31]\n" \
  "ds_write_addtid_b32 v232 offset:6144\n" \
  "s_waitcnt vmcnt(11)\n" \
  "v_mfma_f32_16x16x32_bf16 v[0:3], a[0:3], v[116:119], v[0:3]\n" \
  "ds_write_addtid_b32 v233 offset:6400\n" \
  "ds_write_addtid_b32 v234 offset:6656\n" \
  "v_mfma_f32_16x16x32_bf16 v[4:7], a[4:7], v[116:119], v[4:7]\n" \
  "ds_write_addtid_b32 v235 offset:6912\n" \
  "ds_write_addtid_b32 v236 offset:7168\n" \
  "v_mfma_f32_16x16x32_bf16 v[8:11], a[8:11], v[116:119], v[8:11]\n" \
  "ds_write_addtid_b32 v237 offset:7424\n" \
  "ds_write_addtid_b32 v238 offset:7680\n" \
  "v_mfma_f32_16x16x32_bf16 v[12:15], a[12:15], v[116:119], v[12:15]\n" \
  "ds_write_addtid_b32 v239 offset:7936\n" \
  "v_mfma_f32_16x16x32_bf16 v[16:19], a[16:19], v[116:119], v[16:19]\n" \
  "buffer_load_dwordx4 v[84:87], v59, s[36:39], s69 offen offset:0 sc1\n" \
  "v_mfma_f32_16x16x32_bf16 v[20:23], a[20:23], v[116:119], v[20:23]\n" \
  "v_mfma_f32_16x16x32_bf16 v[24:27], a[24:27], v[116:119], v[24:27]\n" \
  "v_mfma_f32_16x16x32_bf16 v[28:31], a[28:31], v[116:119], v[28:31]\n" \
  "v_mfma_f32_16x16x32_bf16 v[0:3], a[32:35], v[120:123], v[0:3]\n" \
  "v_mfma_f32_16x16x32_bf16 v[4:7], a[36:39], v[120:123], v[4:7]\n" \
  "s_waitcnt lgkmcnt(0)\n" \
  "s_barrier\n" \
  "v_mfma_f32_16x16x32_bf16 v[8:11], a[40:43], v[120:123], v[8:11]\n" \
  "buffer_load_dwordx4 v[88:91], v59, s[36:39], s69 offen offset:1024 sc1\n" \
  "ds_read_b128 v[32:35], v58 offset:0\n" \
  "v_mfma_f32_16x16x32_bf16 v[12:15], a[44:47], v[120:123], v[12:15]\n" \
  "ds_read_b128 v[36:39], v58 offset:8192\n" \
  "ds_read_b128 v[40:43], v58 offset:16384\n" \
  "v_mfma_f32_16x16x32_bf16 v[16:19], a[48:51], v[120:123], v[16:19]\n" \
  "ds_read_b128 v[44:47], v58 offset:24576\n" \
  "ds_read_b128 v[48:51], v58 offset:4096\n" \
  "v_mfma_f32_16x16x32_bf16 v[20:23], a[52:55], v[120:123], v[20:23]\n" \
  "ds_read_b128 v[240:243], v58 offset:12288\n" \
  "ds_read_b128 v[244:247], v58 offset:20480\n" \
  "v_mfma_f32_16x16x32_bf16 v[24:27], a[56:59], v[120:123], v[24:27]\n" \
  "ds_read_b128 v[248:251], v58 offset:28672\n" \
  "s_waitcnt vmcnt(8)\n" \
  "v_mfma_f32_16x16x32_bf16 v[28:31], a[60:63], v[120:123], v[28:31]\n" \
  "v_cmp_eq_u32 vcc, -1, v60\n" \
  "v_mfma_f32_16x16x32_bf16 v[0:3], a[64:67], v[124:127], v[0:3]\n" \
  "buffer_load_dwordx4 v[92:95], v59, s[36:39], s69 offen offset:2048 sc1\n" \
  "v_mfma_f32_16x16x32_bf16 v[4:7], a[68:71], v[124:127], v[4:7]\n" \
  "buffer_load_dwordx2 v[116:117], v56, s[36:39], s90 offen offset:0 sc1\n" \
  "s_cbranch_vccnz .Lsp2A_%=\n" \
  ".Lbp2A_%=:\n" \
  "v_mfma_f32_16x16x32_bf16 v[8:11], a[72:75], v[124:127], v[8:11]\n" \
  "buffer_load_dwordx2 v[118:119], v56, s[36:39], s90 offen offset:32 sc1\n" \
  "v_lshlrev_b32 v112, 16, v60\n" \
  "v_mfma_f32_16x16x32_bf16 v[12:15], a[76:79], v[124:127], v[12:15]\n" \
  "v_and_b32 v113, 0xffff0000, v60\n" \
  "v_add_f32 v64, v64, v112\n" \
  "v_mfma_f32_16x16x32_bf16 v[16:19], a[80:83], v[124:127], v[16:19]\n" \
  "s_waitcnt lgkmcnt(4)\n" \
  "v_add_f32 v32, v32, v36\n" \
  "v_mfma_f32_16x16x32_bf16 v[20:23], a[84:87], v[124:127], v[20:23]\n" \
  "v_add_f32 v33, v33, v37\n" \
  "v_add_f32 v34, v34, v38\n" \
  "v_mfma_f32_16x16x32_bf16 v[24:27], a[88:91], v[124:127], v[24:27]\n" \
  "buffer_load_dwordx4 v[96:99], v59, s[36:39], s70 offen offset:0 sc1\n" \
  "v_add_f32 v35, v35, v39\n" \
  "v_mfma_f32_16x16x32_bf16 v[28:31], a[92:95], v[124:127], v[28:31]\n" \
  "buffer_load_dwordx2 v[120:121], v56, s[36:39], s100 offen offset:0 sc1\n" \
  "v_add_f32 v40, v40, v44\n" \
  "v_mfma_f32_16x16x32_bf16 v[0:3], a[96:99], v[192:195], v[0:3]\n" \
  "buffer_load_dwordx2 v[122:123], v56, s[36:39], s100 offen offset:32 sc1\n" \
  "v_add_f32 v41, v41, v45\n" \
  "v_mfma_f32_16x16x32_bf16 v[4:7], a[100:103], v[192:195], v[4:7]\n" \
  "v_add_f32 v42, v42, v46\n" \
  "v_add_f32 v43, v43, v47\n" \
  "v_mfma_f32_16x16x32_bf16 v[8:11], a[104:107], v[192:195], v[8:11]\n" \
  "v_add_f32 v32, v32, v40\n" \
  "v_add_f32 v33, v33, v41\n" \
  "v_mfma_f32_16x16x32_bf16 v[12:15], a[108:111], v[192:195], v[12:15]\n" \
  "v_add_f32 v34, v34, v42\n" \
  "v_add_f32 v35, v35, v43\n" \
  "v_mfma_f32_16x16x32_bf16 v[16:19], a[112:115], v[192:195], v[16:19]\n" \
  "buffer_load_dwordx4 v[100:103], v59, s[36:39], s70 offen offset:1024 sc1\n" \
  "v_cvt_pk_bf16_f32 v36, v32, v33\n" \
  "v_mfma_f32_16x16x32_bf16 v[20:23], a[116:119], v[192:195], v[20:23]\n" \
  "v_cvt_pk_bf16_f32 v37, v34, v35\n" \
  "s_waitcnt lgkmcnt(0)\n" \
  "v_mfma_f32_16x16x32_bf16 v[24:27], a[120:123], v[192:195], v[24:27]\n" \
  "v_add_f32 v48, v48, v240\n" \
  "v_add_f32 v49, v49, v241\n" \
  "v_mfma_f32_16x16x32_bf16 v[28:31], a[124:127], v[192:195], v[28:31]\n" \
  "v_add_f32 v50, v50, v242\n" \
  "v_mfma_f32_16x16x32_bf16 v[0:3], a[128:131], v[196:199], v[0:3]\n" \
  "v_add_f32 v65, v65, v113\n" \
  "v_add_f32 v51, v51, v243\n" \
  "v_mfma_f32_16x16x32_bf16 v[4:7], a[132:135], v[196:199], v[4:7]\n" \
  "v_lshlrev_b32 v112, 16, v61\n" \
  "v_add_f32 v244, v244, v248\n" \
  "v_mfma_f32_16x16x32_bf16 v[8:11], a[136:139], v[196:199], v[8:11]\n" \
  "buffer_load_dwordx4 v[104:107], v59, s[36:39], s70 offen offset:2048 sc1\n" \
  "v_and_b32 v113, 0xffff0000, v61\n" \
  "v_mfma_f32_16x16x32_bf16 v[12:15], a[140:143], v[196:199], v[12:15]\n" \
  "v_add_f32 v245, v245, v249\n" \
  "v_add_f32 v66, v66, v112\n" \
  "v_mfma_f32_16x16x32_bf16 v[16:19], a[144:147], v[196:199], v[16:19]\n" \
  "v_add_f32 v246, v246, v250\n" \
  "v_add_f32 v67, v67, v113\n" \
  "v_mfma_f32_16x16x32_bf16 v[20:23], a[148:151], v[196:199], v[20:23]\n" \
  "v_add_f32 v247, v247, v251\n" \
  "v_lshlrev_b32 v112, 16, v62\n" \
  "v_mfma_f32_16x16x32_bf16 v[24:27], a[152:155], v[196:199], v[24:27]\n" \
  "v_add_f32 v48, v48, v244\n" \
  "v_and_b32 v113, 0xffff0000, v62\n" \
  "v_mfma_f32_16x16x32_bf16 v[28:31], a[156:159], v[196:199], v[28:31]\n" \
  "v_add_f32 v49, v49, v245\n" \
  "v_add_f32 v68, v68, v112\n" \
  "v_mfma_f32_16x16x32_bf16 v[0:3], a[160:163], v[200:203], v[0:3]\n" \
  "v_add_f32 v50, v50, v246\n" \
  "v_add_f32 v69, v69, v113\n" \
  "v_mfma_f32_16x16x32_bf16 v[4:7], a[164:167], v[200:203], v[4:7]\n" \
  "v_add_f32 v51, v51, v247\n" \
  "v_lshlrev_b32 v112, 16, v63\n" \
  "v_mfma_f32_16x16x32_bf16 v[8:11], a[168:171], v[200:203], v[8:11]\n" \
  "v_cvt_pk_bf16_f32 v38, v48, v49\n" \
  "v_and_b32 v113, 0xffff0000, v63\n" \
  "v_mfma_f32_16x16x32_bf16 v[12:15], a[172:175], v[200:203], v[12:15]\n" \
  "v_cvt_pk_bf16_f32 v39, v50, v51\n" \
  "v_add_f32 v70, v70, v112\n" \
  "v_mfma_f32_16x16x32_bf16 v[16:19], a[176:179], v[200:203], v[16:19]\n" \
  "buffer_store_dwordx4 v[36:39], v59, s[36:39], s74 offen offset:0 sc1\n" \
  "v_add_f32 v71, v71, v113\n" \
  "v_mfma_f32_16x16x32_bf16 v[20:23], a[180:183], v[200:203], v[20:23]\n" \
  "v_mfma_f32_16x16x32_bf16 v[24:27], a[184:187], v[200:203], v[24:27]\n" \
  "v_mfma_f32_16x16x32_bf16 v[28:31], a[188:191], v[200:203], v[28:31]\n" \
  "s_waitcnt vmcnt(16)\n" \
  "v_mfma_f32_16x16x32_bf16 v[0:3], a[192:195], v[204:207], v[0:3]\n" \
  "v_mfma_f32_16x16x32_bf16 v[4:7], a[196:199], v[204:207], v[4:7]\n" \
  "v_mfma_f32_16x16x32_bf16 v[8:11], a[200:203], v[204:207], v[8:11]\n" \
  "v_mfma_f32_16x16x32_bf16 v[12:15], a[204:207], v[204:207], v[12:15]\n" \
  "v_mfma_f32_16x16x32_bf16 v[16:19], a[208:211], v[204:207], v[16:19]\n" \
  "v_mfma_f32_16x16x32_bf16 v[20:23], a[212:215], v[204:207], v[20:23]\n" \
  "v_mfma_f32_16x16x32_bf16 v[24:27], a[216:219], v[204:207], v[24:27]\n" \
  "v_mfma_f32_16x16x32_bf16 v[28:31], a[220:223], v[204:207], v[28:31]\n" \
  "s_waitcnt vmcnt(13)\n" \
  "v_mfma_f32_16x16x32_bf16 v[208:211], v[128:131], v[72:75], 0\n" \
  "v_add_u32 v59, 409600, v59\n" \
  "s_waitcnt vmcnt(12)\n" \
  "v_mfma_f32_16x16x32_bf16 v[212:215], v[132:135], v[72:75], 0\n" \
  "ds_write_addtid_b32 v0 offset:32768\n" \
  "ds_write_addtid_b32 v1 offset:33024\n" \
  "v_mfma_f32_16x16x32_bf16 v[216:219], v[136:139], v[72:75], 0\n" \
  "ds_write_addtid_b32 v2 offset:33280\n" \
  "ds_write_addtid_b32 v3 offset:33536\n" \
  "v_mfma_f32_16x16x32_bf16 v[220:223], v[140:143], v[72:75], 0\n" \
  "buffer_load_dwordx4 v[112:115], v59, s[36:39], s73 offen offset:0 sc1\n" \
  "ds_write_addtid_b32 v4 offset:33792\n" \
  "v_mfma_f32_16x16x32_bf16 v[208:211], v[160:163], v[76:79], v[208:211]\n" \
  "ds_write_addtid_b32 v5 offset:34048\n" \
  "ds_write_addtid_b32 v6 offset:34304\n" \
  "v_mfma_f32_16x16x32_bf16 v[212:215], v[164:167], v[76:79], v[212:215]\n" \
  "ds_write_addtid_b32 v7 offset:34560\n" \
  "ds_write_addtid_b32 v8 offset:34816\n" \
  "v_mfma_f32_16x16x32_bf16 v[216:219], v[168:171], v[76:79], v[216:219]\n" \
  "ds_write_addtid_b32 v9 offset:35072\n" \
  "ds_write_addtid_b32 v10 offset:35328\n" \
  "v_mfma_f32_16x16x32_bf16 v[220:223], v[172:175], v[76:79], v[220:223]\n" \
  "ds_write_addtid_b32 v11 offset:35584\n" \
  "s_waitcnt vmcnt(11)\n" \
  "v_mfma_f32_16x16x32_bf16 v[208:211], a[0:3], v[80:83], v[208:211]\n" \
  "ds_write_addtid_b32 v12 offset:35840\n" \
  "ds_write_addtid_b32 v13 offset:36096\n" \
  "v_mfma_f32_16x16x32_bf16 v[212:215], a[4:7], v[80:83], v[212:215]\n" \
  "ds_write_addtid_b32 v14 offset:36352\n" \
  "ds_write_addtid_b32 v15 offset:36608\n" \
  "v_mfma_f32_16x16x32_bf16 v[216:219], a[8:11], v[80:83], v[216:219]\n" \
  "ds_write_addtid_b32 v16 offset:36864\n" \
  "ds_write_addtid_b32 v17 offset:37120\n" \
  "v_mfma_f32_16x16x32_bf16 v[220:223], a[12:15], v[80:83], v[220:223]\n" \
  "ds_write_addtid_b32 v18 offset:37376\n" \
  "ds_write_addtid_b32 v19 offset:37632\n" \
  "v_mfma_f32_16x16x32_bf16 v[208:211], a[32:35], v[84:87], v[208:211]\n" \
  "ds_write_addtid_b32 v20 offset:37888\n" \
  "ds_write_addtid_b32 v21 offset:38144\n" \
  "v_mfma_f32_16x16x32_bf16 v[212:215], a[36:39], v[84:87], v[212:215]\n" \
  "ds_write_addtid_b32 v22 offset:38400\n" \
  "ds_write_addtid_b32 v23 offset:38656\n" \
  "v_mfma_f32_16x16x32_bf16 v[216:219], a[40:43], v[84:87], v[216:219]\n" \
  "ds_write_addtid_b32 v24 offset:38912\n" \
  "ds_write_addtid_b32 v25 offset:39168\n" \
  "v_mfma_f32_16x16x32_bf16 v[220:223], a[44:47], v[84:87], v[220:223]\n" \
  "ds_write_addtid_b32 v26 offset:39424\n" \
  "s_waitcnt vmcnt(9)\n" \
  "v_mfma_f32_16x16x32_bf16 v[208:211], a[64:67], v[88:91], v[208:211]\n" \
  "ds_write_addtid_b32 v27 offset:39680\n" \
  "ds_write_addtid_b32 v28 offset:39936\n" \
  "v_mfma_f32_16x16x32_bf16 v[212:215], a[68:71], v[88:91], v[212:215]\n" \
  "ds_write_addtid_b32 v29 offset:40192\n" \
  "ds_write_addtid_b32 v30 offset:40448\n" \
  "v_mfma_f32_16x16x32_bf16 v[216:219], a[72:75], v[88:91], v[216:219]\n" \
  "ds_write_addtid_b32 v31 offset:40704\n" \
  "v_cmp_eq_u32 vcc, -1, v108\n" \
  "v_mfma_f32_16x16x32_bf16 v[220:223], a[76:79], v[88:91], v[220:223]\n" \
  "v_and_b32 v62, 0x30, v59\n" \
  "v_add_u32 v62, s67, v62\n" \
  "v_mfma_f32_16x16x32_bf16 v[208:211], a[96:99], v[92:95], v[208:211]\n" \
  "s_cbranch_vccnz .Lsp2B_%=\n" \
  ".Lbp2B_%=:\n" \
  "v_lshlrev_b32 v60, 16, v108\n" \
  "v_mfma_f32_16x16x32_bf16 v[212:215], a[100:103], v[92:95], v[212:215]\n" \
  "v_and_b32 v61, 0xffff0000, v108\n" \
  "v_add_f32 v52, v52, v60\n" \
  "v_mfma_f32_16x16x32_bf16 v[216:219], a[104:107], v[92:95], v[216:219]\n" \
  "v_add_f32 v53, v53, v61\n" \
  "v_lshlrev_b32 v60, 16, v109\n" \
  "v_mfma_f32_16x16x32_bf16 v[220:223], a[108:111], v[92:95], v[220:223]\n" \
  "v_and_b32 v61, 0xffff0000, v109\n" \
  "s_waitcnt vmcnt(3)\n" \
  "v_mfma_f32_16x16x32_bf16 v[208:211], a[128:131], v[96:99], v[208:211]\n" \
  "s_waitcnt lgkmcnt(0)\n" \
  "s_barrier\n" \
  "v_mfma_f32_16x16x32_bf16 v[212:215], a[132:135], v[96:99], v[212:215]\n" \
  "ds_read_b128 v[32:35], v58 offset:32768\n" \
  "ds_read_b128 v[36:39], v58 offset:40960\n" \
  "v_mfma_f32_16x16x32_bf16 v[216:219], a[136:139], v[96:99], v[216:219]\n" \
  "ds_read_b128 v[40:43], v58 offset:49152\n" \
  "ds_read_b128 v[44:47], v58 offset:57344\n" \
  "v_mfma_f32_16x16x32_bf16 v[220:223], a[140:143], v[96:99], v[220:223]\n" \
  "ds_read_b128 v[48:51], v58 offset:36864\n" \
  "ds_read_b128 v[240:243], v58 offset:45056\n" \
  "v_mfma_f32_16x16x32_bf16 v[208:211], a[160:163], v[100:103], v[208:211]\n" \
  "ds_read_b128 v[244:247], v58 offset:53248\n" \
  "ds_read_b128 v[248:251], v58 offset:61440\n" \
  "v_mfma_f32_16x16x32_bf16 v[212:215], a[164:167], v[100:103], v[212:215]\n" \
  "v_add_f32 v54, v54, v60\n" \
  "v_add_f32 v55, v55, v61\n" \
  "v_mfma_f32_16x16x32_bf16 v[216:219], a[168:171], v[100:103], v[216:219]\n" \
  "v_lshlrev_b32 v60, 16, v110\n" \
  "v_and_b32 v61, 0xffff0000, v110\n" \
  "v_mfma_f32_16x16x32_bf16 v[220:223], a[172:175], v[100:103], v[220:223]\n" \
  "v_add_f32 v252, v252, v60\n" \
  "s_waitcnt vmcnt(2)\n" \
  "v_mfma_f32_16x16x32_bf16 v[208:211], a[192:195], v[104:107], v[208:211]\n" \
  "v_add_f32 v253, v253, v61\n" \
  "v_lshlrev_b32 v60, 16, v111\n" \
  "v_mfma_f32_16x16x32_bf16 v[212:215], a[196:199], v[104:107], v[212:215]\n" \
  "v_and_b32 v61, 0xffff0000, v111\n" \
  "v_add_f32 v254, v254, v60\n" \
  "v_mfma_f32_16x16x32_bf16 v[216:219], a[200:203], v[104:107], v[216:219]\n" \
  "s_waitcnt lgkmcnt(4)\n" \
  "v_add_f32 v32, v32, v36\n" \
  "v_mfma_f32_16x16x32_bf16 v[220:223], a[204:207], v[104:107], v[220:223]\n" \
  "v_add_f32 v33, v33, v37\n" \
  "v_add_f32 v34, v34, v38\n" \
  "v_mfma_f32_16x16x32_bf16 v[224:227], v[144:147], v[72:75], 0\n" \
  "v_add_f32 v35, v35, v39\n" \
  "v_add_f32 v40, v40, v44\n" \
  "v_mfma_f32_16x16x32_bf16 v[228:231], v[148:151], v[72:75], 0\n" \
  "v_add_f32 v41, v41, v45\n" \
  "v_add_f32 v42, v42, v46\n" \
  "v_mfma_f32_16x16x32_bf16 v[232:235], v[152:155], v[72:75], 0\n" \
  "ds_write_addtid_b32 v208 offset:0\n" \
  "ds_write_addtid_b32 v209 offset:256\n" \
  "v_mfma_f32_16x16x32_bf16 v[236:239], v[156:159], v[72:75], 0\n" \
  "ds_write_addtid_b32 v210 offset:512\n" \
  "ds_write_addtid_b32 v211 offset:768\n" \
  "v_mfma_f32_16x16x32_bf16 v[224:227], v[176:179], v[76:79], v[224:227]\n" \
  "ds_write_addtid_b32 v212 offset:1024\n" \
  "ds_write_addtid_b32 v213 offset:1280\n" \
  "v_mfma_f32_16x16x32_bf16 v[228:231], v[180:183], v[76:79], v[228:231]\n" \
  "ds_write_addtid_b32 v214 offset:1536\n" \
  "ds_write_addtid_b32 v215 offset:1792\n" \
  "v_mfma_f32_16x16x32_bf16 v[232:235], v[184:187], v[76:79], v[232:235]\n" \
  "ds_write_addtid_b32 v216 offset:2048\n" \
  "ds_write_addtid_b32 v217 offset:2304\n" \
  "v_mfma_f32_16x16x32_bf16 v[236:239], v[188:191], v[76:79], v[236:239]\n" \
  "ds_write_addtid_b32 v218 offset:2560\n" \
  "ds_write_addtid_b32 v219 offset:2816\n" \
  "v_mfma_f32_16x16x32_bf16 v[224:227], a[16:19], v[80:83], v[224:227]\n" \
  "ds_write_addtid_b32 v220 offset:3072\n" \
  "ds_write_addtid_b32 v221 offset:3328\n" \
  "v_mfma_f32_16x16x32_bf16 v[228:231], a[20:23], v[80:83], v[228:231]\n" \
  "ds_write_addtid_b32 v222 offset:3584\n" \
  "ds_write_addtid_b32 v223 offset:3840\n" \
  "v_mfma_f32_16x16x32_bf16 v[232:235], a[24:27], v[80:83], v[232:235]\n" \
  "v_add_f32 v43, v43, v47\n" \
  "v_add_f32 v32, v32, v40\n" \
  "v_mfma_f32_16x16x32_bf16 v[236:239], a[28:31], v[80:83], v[236:239]\n" \
  "v_add_f32 v33, v33, v41\n" \
  "v_add_f32 v34, v34, v42\n" \
  "v_mfma_f32_16x16x32_bf16 v[224:227], a[48:51], v[84:87], v[224:227]\n" \
  "v_add_f32 v255, v255, v61\n" \
  "v_add_f32 v35, v35, v43\n" \
  "v_mfma_f32_16x16x32_bf16 v[228:231], a[52:55], v[84:87], v[228:231]\n" \
  "s_waitcnt vmcnt(0)\n" \
  "v_mfma_f32_16x16x32_bf16 v[232:235], a[56:59], v[84:87], v[232:235]\n" \
  "buffer_load_dwordx4 v[124:127], v59, s[36:39], s78 offen offset:0 sc1\n" \
  "v_cmp_eq_u32 vcc, -1, v112\n" \
  "v_mfma_f32_16x16x32_bf16 v[236:239], a[60:63], v[84:87], v[236:239]\n" \
  "s_waitcnt lgkmcnt(0)\n" \
  "s_barrier\n" \
  "v_mfma_f32_16x16x32_bf16 v[224:227], a[80:83], v[88:91], v[224:227]\n" \
  "ds_read_b128 v[16:19], v58 offset:0\n" \
  "ds_read_b128 v[20:23], v58 offset:8192\n" \
  "v_mfma_f32_16x16x32_bf16 v[228:231], a[84:87], v[88:91], v[228:231]\n" \
  "ds_read_b128 v[24:27], v58 offset:16384\n" \
  "ds_read_b128 v[28:31], v58 offset:24576\n" \
  "v_mfma_f32_16x16x32_bf16 v[232:235], a[88:91], v[88:91], v[232:235]\n" \
  "v_add_f32 v48, v48, v240\n" \
  "s_cbranch_vccnz .Lsp3A_%=\n" \
  ".Lbp3A_%=:\n" \
  "v_mfma_f32_16x16x32_bf16 v[236:239], a[92:95], v[88:91], v[236:239]\n" \
  "v_add_f32 v49, v49, v241\n" \
  "v_lshlrev_b32 v60, 16, v112\n" \
  "v_mfma_f32_16x16x32_bf16 v[224:227], a[112:115], v[92:95], v[224:227]\n" \
  "v_add_f32 v50, v50, v242\n" \
  "v_and_b32 v61, 0xffff0000, v112\n" \
  "v_mfma_f32_16x16x32_bf16 v[228:231], a[116:119], v[92:95], v[228:231]\n" \
  "v_add_f32 v51, v51, v243\n" \
  "v_add_f32 v64, v64, v60\n" \
  "v_mfma_f32_16x16x32_bf16 v[232:235], a[120:123], v[92:95], v[232:235]\n" \
  "v_add_f32 v244, v244, v248\n" \
  "v_add_f32 v65, v65, v61\n" \
  "v_mfma_f32_16x16x32_bf16 v[236:239], a[124:127], v[92:95], v[236:239]\n" \
  "v_add_f32 v245, v245, v249\n" \
  "v_lshlrev_b32 v60, 16, v113\n" \
  "v_mfma_f32_16x16x32_bf16 v[224:227], a[144:147], v[96:99], v[224:227]\n" \
  "v_add_f32 v246, v246, v250\n" \
  "v_and_b32 v61, 0xffff0000, v113\n" \
  "v_mfma_f32_16x16x32_bf16 v[228:231], a[148:151], v[96:99], v[228:231]\n" \
  "v_add_f32 v247, v247, v251\n" \
  "v_add_f32 v66, v66, v60\n" \
  "v_mfma_f32_16x16x32_bf16 v[232:235], a[152:155], v[96:99], v[232:235]\n" \
  "v_add_f32 v48, v48, v244\n" \
  "v_add_f32 v67, v67, v61\n" \
  "v_mfma_f32_16x16x32_bf16 v[236:239], a[156:159], v[96:99], v[236:239]\n" \
  "v_add_f32 v49, v49, v245\n" \
  "v_lshlrev_b32 v60, 16, v114\n" \
  "v_add_f32 v50, v50, v246\n" \
  "v_mfma_f32_16x16x32_bf16 v[224:227], a[176:179], v[100:103], v[224:227]\n" \
  "v_and_b32 v61, 0xffff0000, v114\n" \
  "v_add_f32 v51, v51, v247\n" \
  "v_add_f32 v68, v68, v60\n" \
  "v_mfma_f32_16x16x32_bf16 v[228:231], a[180:183], v[100:103], v[228:231]\n" \
  "v_add_f32 v69, v69, v61\n" \
  "v_lshlrev_b32 v60, 16, v115\n" \
  "v_and_b32 v61, 0xffff0000, v115\n" \
  "v_mfma_f32_16x16x32_bf16 v[232:235], a[184:187], v[100:103], v[232:235]\n" \
  "v_add_f32 v70, v70, v60\n" \
  "ds_read_b128 v[8:11], v62 offset:0\n" \
  "v_mfma_f32_16x16x32_bf16 v[236:239], a[188:191], v[100:103], v[236:239]\n" \
  "v_add_f32 v71, v71, v61\n" \
  "ds_read_b128 v[12:15], v62 offset:64\n" \
  "v_mfma_f32_16x16x32_bf16 v[224:227], a[208:211], v[104:107], v[224:227]\n" \
  "v_lshlrev_b32 v0, 16, v116\n" \
  "v_and_b32 v1, 0xffff0000, v116\n" \
  "v_mfma_f32_16x16x32_bf16 v[228:231], a[212:215], v[104:107], v[228:231]\n" \
  "v_lshlrev_b32 v2, 16, v117\n" \
  "v_and_b32 v3, 0xffff0000, v117\n" \
  "v_mfma_f32_16x16x32_bf16 v[232:235], a[216:219], v[104:107], v[232:235]\n" \
  "v_lshlrev_b32 v4, 16, v120\n" \
  "v_and_b32 v5, 0xffff0000, v120\n" \
  "v_mfma_f32_16x16x32_bf16 v[236:239], a[220:223], v[104:107], v[236:239]\n" \
  "v_lshlrev_b32 v6, 16, v121\n" \
  "v_and_b32 v7, 0xffff0000, v121\n" \
  "s_waitcnt lgkmcnt(0)\n" \
  "v_fma_f32 v0, v8, v64, v0\n" \
  "v_fma_f32 v1, v9, v65, v1\n" \
  "v_fma_f32 v2, v10, v66, v2\n" \
  "v_fma_f32 v3, v11, v67, v3\n" \
  "v_fma_f32 v4, v8, v68, v4\n" \
  "v_fma_f32 v5, v9, v69, v5\n" \
  "v_fma_f32 v6, v10, v70, v6\n" \
  "v_fma_f32 v7, v11, v71, v7\n" \
  "v_fma_f32 v0, v8, v32, v0\n" \
  "v_fma_f32 v1, v9, v33, v1\n" \
  "v_fma_f32 v2, v10, v34, v2\n" \
  "v_fma_f32 v3, v11, v35, v3\n" \
  "v_cvt_pk_bf16_f32 v0, v0, v1\n" \
  "v_cvt_pk_bf16_f32 v1, v2, v3\n" \
  "buffer_store_dwordx2 v[0:1], v56, s[44:47], 0 offen offset:0 sc1\n" \
  "v_fma_f32 v4, v8, v48, v4\n" \
  "v_fma_f32 v5, v9, v49, v5\n" \
  "v_fma_f32 v6, v10, v50, v6\n" \
  "v_fma_f32 v7, v11, v51, v7\n" \
  "v_cvt_pk_bf16_f32 v4, v4, v5\n" \
  "v_cvt_pk_bf16_f32 v5, v6, v7\n" \
  "buffer_store_dwordx2 v[4:5], v56, s[44:47], s35 offen offset:0 sc1\n" \
  "v_lshlrev_b32 v64, 16, v118\n" \
  "v_and_b32 v65, 0xffff0000, v118\n" \
  "v_lshlrev_b32 v66, 16, v119\n" \
  "v_and_b32 v67, 0xffff0000, v119\n" \
  "ds_write_addtid_b32 v224 offset:4096\n" \
  "ds_write_addtid_b32 v225 offset:4352\n" \
  "ds_write_addtid_b32 v226 offset:4608\n" \
  "ds_write_addtid_b32 v227 offset:4864\n" \
  "ds_write_addtid_b32 v228 offset:5120\n" \
  "ds_write_addtid_b32 v229 offset:5376\n" \
  "ds_write_addtid_b32 v230 offset:5632\n" \
  "ds_write_addtid_b32 v231 offset:5888\n" \
  "ds_write_addtid_b32 v232 offset:6144\n" \
  "ds_write_addtid_b32 v233 offset:6400\n" \
  "ds_write_addtid_b32 v234 offset:6656\n" \
  "ds_write_addtid_b32 v235 offset:6912\n" \
  "ds_write_addtid_b32 v236 offset:7168\n" \
  "ds_write_addtid_b32 v237 offset:7424\n" \
  "ds_write_addtid_b32 v238 offset:7680\n" \
  "ds_write_addtid_b32 v239 offset:7936\n" \
  "v_lshlrev_b32 v68, 16, v122\n" \
  "v_and_b32 v69, 0xffff0000, v122\n" \
  "v_lshlrev_b32 v70, 16, v123\n" \
  "v_and_b32 v71, 0xffff0000, v123\n" \
  "s_waitcnt lgkmcnt(15)\n" \
  "v_pk_add_f32 v[16:17], v[16:17], v[20:21]\n" \
  "v_pk_add_f32 v[18:19], v[18:19], v[22:23]\n" \
  "v_pk_add_f32 v[24:25], v[24:25], v[28:29]\n" \
  "v_pk_add_f32 v[26:27], v[26:27], v[30:31]\n" \
  "v_pk_add_f32 v[16:17], v[16:17], v[24:25]\n" \
  "v_pk_add_f32 v[18:19], v[18:19], v[26:27]\n" \
  "s_waitcnt lgkmcnt(0)\n" \
  "s_barrier\n" \
  "ds_read_b128 v[32:35], v58 offset:4096\n" \
  "ds_read_b128 v[36:39], v58 offset:12288\n" \
  "ds_read_b128 v[40:43], v58 offset:20480\n" \
  "ds_read_b128 v[44:47], v58 offset:28672\n" \
  "s_waitcnt vmcnt(2)\n" \
  "v_cmp_eq_u32 vcc, -1, v124\n" \
  "s_cbranch_vccnz .Lsp3B_%=\n" \
  ".Lbp3B_%=:\n" \
  "v_lshlrev_b32 v20, 16, v124\n" \
  "v_and_b32 v21, 0xffff0000, v124\n" \
  "v_lshlrev_b32 v22, 16, v125\n" \
  "v_and_b32 v23, 0xffff0000, v125\n" \
  "v_lshlrev_b32 v24, 16, v126\n" \
  "v_and_b32 v25, 0xffff0000, v126\n" \
  "v_lshlrev_b32 v26, 16, v127\n" \
  "v_and_b32 v27, 0xffff0000, v127\n" \
  "v_pk_add_f32 v[52:53], v[52:53], v[20:21]\n" \
  "v_pk_add_f32 v[54:55], v[54:55], v[22:23]\n" \
  "v_pk_add_f32 v[252:253], v[252:253], v[24:25]\n" \
  "v_pk_add_f32 v[254:255], v[254:255], v[26:27]\n" \
  "v_pk_fma_f32 v[64:65], v[12:13], v[52:53], v[64:65]\n" \
  "v_pk_fma_f32 v[66:67], v[14:15], v[54:55], v[66:67]\n" \
  "v_pk_fma_f32 v[68:69], v[12:13], v[252:253], v[68:69]\n" \
  "v_pk_fma_f32 v[70:71], v[14:15], v[254:255], v[70:71]\n" \
  "v_pk_fma_f32 v[64:65], v[12:13], v[16:17], v[64:65]\n" \
  "v_pk_fma_f32 v[66:67], v[14:15], v[18:19], v[66:67]\n" \
  "v_cvt_pk_bf16_f32 v64, v64, v65\n" \
  "v_cvt_pk_bf16_f32 v65, v66, v67\n" \
  "buffer_store_dwordx2 v[64:65], v56, s[44:47], 0 offen offset:32 sc1\n" \
  "s_waitcnt lgkmcnt(0)\n" \
  "v_pk_add_f32 v[32:33], v[32:33], v[36:37]\n" \
  "v_pk_add_f32 v[34:35], v[34:35], v[38:39]\n" \
  "v_pk_add_f32 v[40:41], v[40:41], v[44:45]\n" \
  "v_pk_add_f32 v[42:43], v[42:43], v[46:47]\n" \
  "v_pk_add_f32 v[32:33], v[32:33], v[40:41]\n" \
  "v_pk_add_f32 v[34:35], v[34:35], v[42:43]\n" \
  "v_pk_fma_f32 v[68:69], v[12:13], v[32:33], v[68:69]\n" \
  "v_pk_fma_f32 v[70:71], v[14:15], v[34:35], v[70:71]\n" \
  "v_cvt_pk_bf16_f32 v68, v68, v69\n" \
  "v_cvt_pk_bf16_f32 v69, v70, v71\n" \
  "buffer_store_dwordx2 v[68:69], v56, s[44:47], s35 offen offset:32 sc1\n" \
  "s_branch .Lend_%=\n" \
  ".LsaF_%=:\n" \
  "s_memrealtime s[94:95]\n" \
  "s_waitcnt lgkmcnt(0)\n" \
  ".LpaF_%=:\n" \
  "s_sleep 2\n" \
  "buffer_load_dword v63, v61, s[36:39], s59 offen offset:0 sc1\n" \
  "s_memrealtime s[96:97]\n" \
  "s_waitcnt vmcnt(0) lgkmcnt(0)\n" \
  "s_sub_u32 s96, s96, s94\n" \
  "s_cmp_gt_u32 s96, 0x4c4b40\n" \
  "s_cbranch_scc1 .LbaF_%=\n" \
  "v_cmp_eq_u32 vcc, 0, v63\n" \
  "s_and_b64 vcc, vcc, s[86:87]\n" \
  "s_cbranch_scc1 .LpaF_%=\n" \
  "s_branch .LbaF_%=\n" \
  ".LsbF_%=:\n" \
  "s_memrealtime s[94:95]\n" \
  "s_waitcnt lgkmcnt(0)\n" \
  ".LpbF_%=:\n" \
  "s_sleep 2\n" \
  "buffer_load_dword v63, v61, s[36:39], s59 offen offset:0 sc1\n" \
  "s_memrealtime s[96:97]\n" \
  "s_waitcnt vmcnt(0) lgkmcnt(0)\n" \
  "s_sub_u32 s96, s96, s94\n" \
  "s_cmp_gt_u32 s96, 0x4c4b40\n" \
  "s_cbranch_scc1 .LbbF_%=\n" \
  "v_cmp_eq_u32 vcc, 0, v63\n" \
  "s_and_b64 vcc, vcc, s[86:87]\n" \
  "s_cbranch_scc1 .LpbF_%=\n" \
  "s_branch .LbbF_%=\n" \
  ".LsaS_%=:\n" \
  "s_memrealtime s[94:95]\n" \
  "s_waitcnt lgkmcnt(0)\n" \
  ".LpaS_%=:\n" \
  "s_sleep 2\n" \
  "buffer_load_dword v63, v61, s[36:39], s59 offen offset:0 sc1\n" \
  "s_memrealtime s[96:97]\n" \
  "s_waitcnt vmcnt(0) lgkmcnt(0)\n" \
  "s_sub_u32 s96, s96, s94\n" \
  "s_cmp_gt_u32 s96, 0x4c4b40\n" \
  "s_cbranch_scc1 .LbaS_%=\n" \
  "v_cmp_eq_u32 vcc, 0, v63\n" \
  "s_and_b64 vcc, vcc, s[86:87]\n" \
  "s_cbranch_scc1 .LpaS_%=\n" \
  "s_branch .LbaS_%=\n" \
  ".LsbS_%=:\n" \
  "s_memrealtime s[94:95]\n" \
  "s_waitcnt lgkmcnt(0)\n" \
  ".LpbS_%=:\n" \
  "s_sleep 2\n" \
  "buffer_load_dword v63, v61, s[36:39], s59 offen offset:0 sc1\n" \
  "s_memrealtime s[96:97]\n" \
  "s_waitcnt vmcnt(0) lgkmcnt(0)\n" \
  "s_sub_u32 s96, s96, s94\n" \
  "s_cmp_gt_u32 s96, 0x4c4b40\n" \
  "s_cbranch_scc1 .LbbS_%=\n" \
  "v_cmp_eq_u32 vcc, 0, v63\n" \
  "s_and_b64 vcc, vcc, s[86:87]\n" \
  "s_cbranch_scc1 .LpbS_%=\n" \
  "s_branch .LbbS_%=\n" \
  ".Lsp3B_%=:\n" \
  "s_memrealtime s[94:95]\n" \
  "s_waitcnt lgkmcnt(0)\n" \
  ".Lpp3B_%=:\n" \
  "s_sub_u32 s101, s78, 0\n" \
  "buffer_load_dwordx4 v[124:127], v59, s[36:39], s101 offen offset:0 sc1\n" \
  "s_memrealtime s[96:97]\n" \
  "s_waitcnt vmcnt(0) lgkmcnt(0)\n" \
  "s_sub_u32 s96, s96, s94\n" \
  "s_cmp_gt_u32 s96, 0x4c4b40\n" \
  "s_cbranch_scc1 .Lbp3B_%=\n" \
  "v_cmp_eq_u32 vcc, -1, v124\n" \
  "s_cbranch_vccnz .Lpp3B_%=\n" \
  "s_branch .Lbp3B_%=\n" \
  ".Lspf_%=:\n" \
  "s_memrealtime s[94:95]\n" \
  "s_waitcnt lgkmcnt(0)\n" \
  ".Lppf_%=:\n" \
  "s_sleep 2\n" \
  "buffer_load_dword v53, v52, s[36:39], s30 offen offset:0 sc1\n" \
  "s_memrealtime s[96:97]\n" \
  "s_waitcnt vmcnt(0) lgkmcnt(0)\n" \
  "s_sub_u32 s96, s96, s94\n" \
  "s_cmp_gt_u32 s96, 0x4c4b40\n" \
  "s_cbranch_scc1 .Lbpf_%=\n" \
  "v_cmp_eq_u32 vcc, 0, v53\n" \
  "s_cbranch_vccnz .Lppf_%=\n" \
  "s_branch .Lbpf_%=\n" \
  ".Lsg4_%=:\n" \
  "s_memrealtime s[94:95]\n" \
  "s_waitcnt lgkmcnt(0)\n" \
  ".Lpg4_%=:\n" \
  "s_sleep 2\n" \
  "buffer_load_dword v63, v61, s[36:39], s59 offen offset:0 sc1\n" \
  "s_memrealtime s[96:97]\n" \
  "s_waitcnt vmcnt(0) lgkmcnt(0)\n" \
  "s_sub_u32 s96, s96, s94\n" \
  "s_cmp_gt_u32 s96, 0x4c4b40\n" \
  "s_cbranch_scc1 .Lbg4_%=\n" \
  "v_cmp_eq_u32 vcc, 0, v63\n" \
  "s_and_b64 vcc, vcc, s[86:87]\n" \
  "s_cbranch_scc1 .Lpg4_%=\n" \
  "s_branch .Lbg4_%=\n" \
  ".Lse_%=:\n" \
  "s_memrealtime s[94:95]\n" \
  "s_waitcnt lgkmcnt(0)\n" \
  ".Lpe_%=:\n" \
  "buffer_load_dword v64, v69, s[36:39], s79 offen offset:0 sc1\n" \
  "buffer_load_dword v65, v69, s[36:39], s80 offen offset:0 sc1\n" \
  "buffer_load_dword v66, v69, s[36:39], s81 offen offset:0 sc1\n" \
  "buffer_load_dword v67, v69, s[36:39], s82 offen offset:0 sc1\n" \
  "s_memrealtime s[96:97]\n" \
  "s_waitcnt vmcnt(0) lgkmcnt(0)\n" \
  "s_sub_u32 s96, s96, s94\n" \
  "s_cmp_gt_u32 s96, 0x4c4b40\n" \
  "s_cbranch_scc1 .Lbe_%=\n" \
  "v_max3_u32 v52, v64, v65, v66\n" \
  "v_max_u32 v52, v52, v67\n" \
  "v_cmp_eq_u32 vcc, -1, v52\n" \
  "s_cbranch_vccnz .Lpe_%=\n" \
  "s_branch .Lbe_%=\n" \
  ".Lsp1_%=:\n" \
  "s_memrealtime s[94:95]\n" \
  "s_waitcnt lgkmcnt(0)\n" \
  ".Lpp1_%=:\n" \
  "s_sub_u32 s101, s71, 409600\n" \
  "buffer_load_dwordx4 v[64:67], v59, s[36:39], s101 offen offset:0 sc1\n" \
  "s_sub_u32 s101, s76, 409600\n" \
  "buffer_load_dwordx4 v[52:55], v59, s[36:39], s101 offen offset:0 sc1\n" \
  "s_memrealtime s[96:97]\n" \
  "s_waitcnt vmcnt(0) lgkmcnt(0)\n" \
  "s_sub_u32 s96, s96, s94\n" \
  "s_cmp_gt_u32 s96, 0x4c4b40\n" \
  "s_cbranch_scc1 .Lbp1_%=\n" \
  "v_max_u32 v62, v64, v52\n" \
  "v_cmp_eq_u32 vcc, -1, v62\n" \
  "s_cbranch_vccnz .Lpp1_%=\n" \
  "s_branch .Lbp1_%=\n" \
  ".Lsp2A_%=:\n" \
  "s_memrealtime s[94:95]\n" \
  "s_waitcnt lgkmcnt(0)\n" \
  ".Lpp2A_%=:\n" \
  "s_sub_u32 s101, s72, 409600\n" \
  "buffer_load_dwordx4 v[60:63], v59, s[36:39], s101 offen offset:0 sc1\n" \
  "s_memrealtime s[96:97]\n" \
  "s_waitcnt vmcnt(0) lgkmcnt(0)\n" \
  "s_sub_u32 s96, s96, s94\n" \
  "s_cmp_gt_u32 s96, 0x4c4b40\n" \
  "s_cbranch_scc1 .Lbp2A_%=\n" \
  "v_cmp_eq_u32 vcc, -1, v60\n" \
  "s_cbranch_vccnz .Lpp2A_%=\n" \
  "s_branch .Lbp2A_%=\n" \
  ".Lsp2B_%=:\n" \
  "s_memrealtime s[94:95]\n" \
  "s_waitcnt lgkmcnt(0)\n" \
  ".Lpp2B_%=:\n" \
  "s_sub_u32 s101, s77, 409600\n" \
  "buffer_load_dwordx4 v[108:111], v59, s[36:39], s101 offen offset:0 sc1\n" \
  "s_memrealtime s[96:97]\n" \
  "s_waitcnt vmcnt(0) lgkmcnt(0)\n" \
  "s_sub_u32 s96, s96, s94\n" \
  "s_cmp_gt_u32 s96, 0x4c4b40\n" \
  "s_cbranch_scc1 .Lbp2B_%=\n" \
  "v_cmp_eq_u32 vcc, -1, v108\n" \
  "s_cbranch_vccnz .Lpp2B_%=\n" \
  "s_branch .Lbp2B_%=\n" \
  ".Lsp3A_%=:\n" \
  "s_memrealtime s[94:95]\n" \
  "s_waitcnt lgkmcnt(0)\n" \
  ".Lpp3A_%=:\n" \
  "s_sub_u32 s101, s73, 0\n" \
  "buffer_load_dwordx4 v[112:115], v59, s[36:39], s101 offen offset:0 sc1\n" \
  "s_memrealtime s[96:97]\n" \
  "s_waitcnt vmcnt(0) lgkmcnt(0)\n" \
  "s_sub_u32 s96, s96, s94\n" \
  "s_cmp_gt_u32 s96, 0x4c4b40\n" \
  "s_cbranch_scc1 .Lbp3A_%=\n" \
  "v_cmp_eq_u32 vcc, -1, v112\n" \
  "s_cbranch_vccnz .Lpp3A_%=\n" \
  "s_branch .Lbp3A_%=\n" \
  ".Lz0_%=:\n" \
  "s_branch .Lz1_%=\n" \
  ".Lxq5_%=:\n" \
  "s_mov_b64 exec, 1\n" \
  "buffer_store_dword v61, off, s[36:39], s54 offset:0 sc1\n" \
  "s_mov_b64 exec, -1\n" \
  "buffer_load_dword v63, v61, s[36:39], s75 offen offset:0 sc1\n" \
  "s_mov_b32 s75, s59\n" \
  "s_add_u32 s54, s54, 0x10000\n" \
  "s_add_u32 s83, s83, 819200\n" \
  "s_branch .Lyq5_%=\n" \
  ".Lz1_%=:\n" \
  "s_branch .Lz2_%=\n" \
  ".Lxt5_%=:\n" \
  "v_and_b32 v20, 0x300, v59\n" \
  "v_and_b32 v21, 0xf0, v59\n" \
  "v_lshlrev_b32 v20, 4, v20\n" \
  "s_mov_b32 m0, 162176\n" \
  "v_lshl_or_b32 v20, v21, 3, v20\n" \
  "buffer_load_dword v20, s[40:43], s101 offen lds\n" \
  "s_mov_b32 m0, s56\n" \
  "v_and_b32 v26, 0x3ff, v59\n" \
  "v_mov_b32 v22, -1\n" \
  "v_mov_b32 v23, -1\n" \
  "v_mov_b32 v24, -1\n" \
  "v_mov_b32 v25, -1\n" \
  "s_add_u32 s96, s74, 819200\n" \
  "buffer_store_dwordx4 v[22:25], v26, s[36:39], s96 offen offset:0 sc1\n" \
  "s_add_u32 s96, s74, 1228800\n" \
  "buffer_store_dwordx4 v[22:25], v26, s[36:39], s96 offen offset:0 sc1\n" \
  "s_add_u32 s96, s74, 1638400\n" \
  "buffer_store_dwordx4 v[22:25], v26, s[36:39], s96 offen offset:0 sc1\n" \
  "s_add_u32 s96, s74, 2048000\n" \
  "buffer_store_dwordx4 v[22:25], v26, s[36:39], s96 offen offset:0 sc1\n" \
  "s_add_u32 s96, s74, 2457600\n" \
  "buffer_store_dwordx4 v[22:25], v26, s[36:39], s96 offen offset:0 sc1\n" \
  "s_add_u32 s96, s74, 2867200\n" \
  "buffer_store_dwordx4 v[22:25], v26, s[36:39], s96 offen offset:0 sc1\n" \
  "s_add_u32 s96, s74, 3276800\n" \
  "buffer_store_dwordx4 v[22:25], v26, s[36:39], s96 offen offset:0 sc1\n" \
  "s_nop 0\n" \
  "s_branch .Lyt5_%=\n" \
  ".Lz2_%=:\n" \
  "s_branch .Lz3_%=\n" \
  ".Lxq4_%=:\n" \
  "s_mov_b64 exec, 1\n" \
  "buffer_store_dword v61, off, s[36:39], s54 offset:0 sc1\n" \
  "s_mov_b64 exec, -1\n" \
  "buffer_load_dword v63, v61, s[36:39], s75 offen offset:0 sc1\n" \
  "s_mov_b32 s75, s59\n" \
  "s_add_u32 s54, s54, 0x10000\n" \
  "s_add_u32 s83, s83, 819200\n" \
  "s_branch .Lyq4_%=\n" \
  ".Lz3_%=:\n" \
  "s_branch .Lz4_%=\n" \
  ".Lxt4_%=:\n" \
  "v_and_b32 v0, 0x300, v59\n" \
  "v_and_b32 v1, 0xf0, v59\n" \
  "v_lshlrev_b32 v0, 4, v0\n" \
  "s_mov_b32 m0, 162176\n" \
  "v_lshl_or_b32 v0, v1, 3, v0\n" \
  "buffer_load_dword v0, s[40:43], s101 offen lds\n" \
  "s_mov_b32 m0, s56\n" \
  "v_and_b32 v6, 0x3ff, v59\n" \
  "v_mov_b32 v2, -1\n" \
  "v_mov_b32 v3, -1\n" \
  "v_mov_b32 v4, -1\n" \
  "v_mov_b32 v5, -1\n" \
  "s_add_u32 s96, s74, 819200\n" \
  "buffer_store_dwordx4 v[2:5], v6, s[36:39], s96 offen offset:0 sc1\n" \
  "s_add_u32 s96, s74, 1228800\n" \
  "buffer_store_dwordx4 v[2:5], v6, s[36:39], s96 offen offset:0 sc1\n" \
  "s_add_u32 s96, s74, 1638400\n" \
  "buffer_store_dwordx4 v[2:5], v6, s[36:39], s96 offen offset:0 sc1\n" \
  "s_add_u32 s96, s74, 2048000\n" \
  "buffer_store_dwordx4 v[2:5], v6, s[36:39], s96 offen offset:0 sc1\n" \
  "s_add_u32 s96, s74, 2457600\n" \
  "buffer_store_dwordx4 v[2:5], v6, s[36:39], s96 offen offset:0 sc1\n" \
  "s_add_u32 s96, s74, 2867200\n" \
  "buffer_store_dwordx4 v[2:5], v6, s[36:39], s96 offen offset:0 sc1\n" \
  "s_add_u32 s96, s74, 3276800\n" \
  "buffer_store_dwordx4 v[2:5], v6, s[36:39], s96 offen offset:0 sc1\n" \
  "s_nop 0\n" \
  "s_branch .Lyt4_%=\n" \
  ".Lz4_%=:\n" \
  "s_branch .Lzl_%=\n" \
  ".Lend_%=:\n"
extern "C" __global__ __launch_bounds__(256, 1) void adm(
    const u16* x, const u16* wr, const u16* wi, const u16* w1, const u16* cvec, const u16* wmod, char* wsb, const u16* b1, const u16* b2, const u16* w2, u16* outp) {
  u64 ka = (u64)__builtin_amdgcn_kernarg_segment_ptr(), tab = (u64)TAB;
  unsigned bid = blockIdx.x, tid = threadIdx.x;
  asm volatile(ASM_FRONT ASM_GEMM ASM_END : "+{s[0:1]}"(ka), "+{s2}"(bid), "+{s[76:77]}"(tab), "+{v0}"(tid)::ASM_CLOB);
}

extern "C" void kernel_launch(void* const* d_in, const int* in_sizes, int n_in, void* d_out, int out_size, void* d_ws,
                              size_t ws_size, hipStream_t stream) {
  (void)hipFuncSetAttribute((const void*)adm, hipFuncAttributeMaxDynamicSharedMemorySize, 163840);
  adm<<<256, 256, 163840, stream>>>((const u16*)d_in[0], (const u16*)d_in[1], (const u16*)d_in[2], (const u16*)d_in[4], (const u16*)d_in[8], (const u16*)d_in[9],
                                    (char*)d_ws, (const u16*)d_in[5], (const u16*)d_in[7], (const u16*)d_in[6], (u16*)d_out);
}
```

```cpp
#include <hip/hip_runtime.h>
typedef unsigned short u16;
typedef unsigned long long u64;
static __device__ const unsigned __attribute__((aligned(128))) TAB[6736] = {
13312,13312,13312,13312,13312,0x2e1f3364,0x31a831a8,0x33642e1f,13312,0x31a831a8,0x34000000,0x31a8b1a8,13312,0x33642e1f,0x31a8b1a8,0xae1fb364,
13312,0x34000000,46080,0xb4000000,13312,0x3364ae1f,0xb1a8b1a8,0xae1f3364,13312,0x31a8b1a8,0xb4000000,0x31a831a8,13312,0x2e1fb364,0xb1a831a8,0x3364ae1f,
13312,46080,13312,46080,13312,0xae1fb364,0x31a831a8,0xb364ae1f,13312,0xb1a8b1a8,0x34000000,0xb1a831a8,13312,0xb364ae1f,0x31a8b1a8,0x2e1f3364,
13312,0xb4000000,46080,0x34000000,13312,0xb3642e1f,0xb1a8b1a8,0x2e1fb364,13312,0xb1a831a8,0xb4000000,0xb1a8b1a8,13312,0xae1f3364,0xb1a831a8,0xb3642e1f,
13312,13312,13312,13312,0x34000000,0x3364ae1f,0x31a8b1a8,0x2e1fb364,46080,0xb1a8b1a8,0xb4000000,0xb1a831a8,0xb4000000,0xae1f3364,0x31a831a8,0x3364ae1f,
13312,0x34000000,46080,0xb4000000,0x34000000,0xae1fb364,0xb1a831a8,0x33642e1f,46080,0xb1a831a8,0x34000000,0xb1a8b1a8,0xb4000000,0x33642e1f,0xb1a8b1a8,0x2e1f3364,
13312,46080,13312,46080,0x34000000,0xb3642e1f,0x31a8b1a8,0xae1f3364,46080,0x31a831a8,0xb4000000,0x31a8b1a8,0xb4000000,0x2e1fb364,0x31a831a8,0xb3642e1f,
13312,0xb4000000,46080,0x34000000,0x34000000,0x2e1f3364,0xb1a831a8,0xb364ae1f,46080,0x31a8b1a8,0x34000000,0x31a831a8,0xb4000000,0xb364ae1f,0xb1a8b1a8,0xae1fb364,
13312,13312,13312,13312,46080,0xae1fb364,0xb1a8b1a8,0xb364ae1f,13312,0x31a831a8,0x34000000,0x31a8b1a8,46080,0xb364ae1f,0xb1a831a8,0x2e1f3364,
13312,0x34000000,46080,0xb4000000,46080,0xb3642e1f,0x31a831a8,0x2e1fb364,13312,0x31a8b1a8,0xb4000000,0x31a831a8,46080,0xae1f3364,0x31a8b1a8,0xb3642e1f,
13312,46080,13312,46080,46080,0x2e1f3364,0xb1a8b1a8,0x33642e1f,13312,0xb1a8b1a8,0x34000000,0xb1a831a8,46080,0x33642e1f,0xb1a831a8,0xae1fb364,
13312,0xb4000000,46080,0x34000000,46080,0x3364ae1f,0x31a831a8,0xae1f3364,13312,0xb1a831a8,0xb4000000,0xb1a8b1a8,46080,0x2e1fb364,0x31a8b1a8,0x3364ae1f,
13312,13312,13312,13312,0xb4000000,0xb3642e1f,0xb1a831a8,0xae1f3364,46080,0xb1a8b1a8,0xb4000000,0xb1a831a8,0x34000000,0x2e1fb364,0xb1a8b1a8,0xb3642e1f,
13312,0x34000000,46080,0xb4000000,0xb4000000,0x2e1f3364,0x31a8b1a8,0xb364ae1f,46080,0xb1a831a8,0x34000000,0xb1a8b1a8,0x34000000,0xb364ae1f,0x31a831a8,0xae1fb364,
13312,46080,13312,46080,0xb4000000,0x3364ae1f,0xb1a831a8,0x2e1fb364,46080,0x31a831a8,0xb4000000,0x31a8b1a8,0x34000000,0xae1f3364,0xb1a8b1a8,0x3364ae1f,
13312,0xb4000000,46080,0x34000000,0xb4000000,0xae1fb364,0x31a8b1a8,0x33642e1f,46080,0x31a8b1a8,0x34000000,0x31a831a8,0x34000000,0x33642e1f,0x31a831a8,0x2e1f3364,
0x34000000,0x34000000,0x34000000,0x34000000,0x34000000,0x3364ae1f,0x31a8b1a8,0x2e1fb364,0x34000000,0x31a8b1a8,46080,0xb1a8b1a8,0x34000000,0x2e1fb364,0xb1a8b1a8,0xb3642e1f,
0x34000000,46080,0xb4000000,13312,0x34000000,0xae1fb364,0xb1a831a8,0x33642e1f,0x34000000,0xb1a8b1a8,13312,0x31a8b1a8,0x34000000,0xb364ae1f,0x31a831a8,0xae1fb364,
0x34000000,0xb4000000,0x34000000,0xb4000000,0x34000000,0xb3642e1f,0x31a8b1a8,0xae1f3364,0x34000000,0xb1a831a8,46080,0x31a831a8,0x34000000,0xae1f3364,0xb1a8b1a8,0x3364ae1f,
0x34000000,13312,0xb4000000,46080,0x34000000,0x2e1f3364,0xb1a831a8,0xb364ae1f,0x34000000,0x31a831a8,13312,0xb1a831a8,0x34000000,0x33642e1f,0x31a831a8,0x2e1f3364,
0x34000000,0x34000000,0x34000000,0x34000000,46080,0xae1fb364,0xb1a8b1a8,0xb364ae1f,0xb4000000,0xb1a831a8,13312,0x31a831a8,13312,0x33642e1f,0x31a8b1a8,0xae1fb364,
0x34000000,46080,0xb4000000,13312,46080,0xb3642e1f,0x31a831a8,0x2e1fb364,0xb4000000,0x31a831a8,46080,0xb1a831a8,13312,0x2e1fb364,0xb1a831a8,0x3364ae1f,
0x34000000,0xb4000000,0x34000000,0xb4000000,46080,0x2e1f3364,0xb1a8b1a8,0x33642e1f,0xb4000000,0x31a8b1a8,13312,0xb1a8b1a8,13312,0xb364ae1f,0x31a8b1a8,0x2e1f3364,
0x34000000,13312,0xb4000000,46080,46080,0x3364ae1f,0x31a831a8,0xae1f3364,0xb4000000,0xb1a8b1a8,46080,0x31a8b1a8,13312,0xae1f3364,0xb1a831a8,0xb3642e1f,
0x34000000,0x34000000,0x34000000,0x34000000,0xb4000000,0xb3642e1f,0xb1a831a8,0xae1f3364,0x34000000,0x31a8b1a8,46080,0xb1a8b1a8,0xb4000000,0xae1f3364,0x31a831a8,0x3364ae1f,
0x34000000,46080,0xb4000000,13312,0xb4000000,0x2e1f3364,0x31a8b1a8,0xb364ae1f,0x34000000,0xb1a8b1a8,13312,0x31a8b1a8,0xb4000000,0x33642e1f,0xb1a8b1a8,0x2e1f3364,
0x34000000,0xb4000000,0x34000000,0xb4000000,0xb4000000,0x3364ae1f,0xb1a831a8,0x2e1fb364,0x34000000,0xb1a831a8,46080,0x31a831a8,0xb4000000,0x2e1fb364,0x31a831a8,0xb3642e1f,
0x34000000,13312,0xb4000000,46080,0xb4000000,0xae1fb364,0x31a8b1a8,0x33642e1f,0x34000000,0x31a831a8,13312,0xb1a831a8,0xb4000000,0xb364ae1f,0xb1a8b1a8,0xae1fb364,
0x34000000,0x34000000,0x34000000,0x34000000,13312,0x2e1f3364,0x31a831a8,0x33642e1f,0xb4000000,0xb1a831a8,13312,0x31a831a8,46080,0xb364ae1f,0xb1a831a8,0x2e1f3364,
0x34000000,46080,0xb4000000,13312,13312,0x3364ae1f,0xb1a8b1a8,0xae1f3364,0xb4000000,0x31a831a8,46080,0xb1a831a8,46080,0xae1f3364,0x31a8b1a8,0xb3642e1f,
0x34000000,0xb4000000,0x34000000,0xb4000000,13312,0xae1fb364,0x31a831a8,0xb364ae1f,0xb4000000,0x31a8b1a8,13312,0xb1a8b1a8,46080,0x33642e1f,0xb1a831a8,0xae1fb364,
0x34000000,13312,0xb4000000,46080,13312,0xb3642e1f,0xb1a8b1a8,0x2e1fb364,0xb4000000,0xb1a8b1a8,46080,0x31a8b1a8,46080,0x2e1fb364,0x31a8b1a8,0x3364ae1f,
13312,13312,13312,13312,0x2a3e33d9,0x307232a7,0x32a73072,0x33d92a3e,0x2e1f3364,0x33642e1f,0x3364ae1f,0x2e1fb364,0x307232a7,0x33d9aa3e,0x2a3eb3d9,0xb2a7b072,
0x31a831a8,0x31a8b1a8,0xb1a8b1a8,0xb1a831a8,0x32a73072,0x2a3eb3d9,0xb3d92a3e,0x307232a7,0x33642e1f,0xae1fb364,0xae1f3364,0x3364ae1f,0x33d92a3e,0xb2a7b072,0x307232a7,0xaa3eb3d9,
0x34000000,0xb4000000,0x34000000,0xb4000000,0x33d9aa3e,0xb2a73072,0x3072b2a7,0xaa3e33d9,0x3364ae1f,0xae1f3364,0xae1fb364,0x33642e1f,0x32a7b072,0x2a3e33d9,0xb3d9aa3e,0x3072b2a7,
0x31a8b1a8,0x31a831a8,0xb1a831a8,0xb1a8b1a8,0x3072b2a7,0x33d92a3e,0x2a3e33d9,0xb2a73072,0x2e1fb364,0x3364ae1f,0x33642e1f,0x2e1f3364,0x2a3eb3d9,0x3072b2a7,0x32a7b072,0x33d9aa3e,
13312,13312,13312,13312,0x33d9aa3e,0x32a7b072,0x3072b2a7,0x2a3eb3d9,0xae1fb364,0xb364ae1f,0xb3642e1f,0xae1f3364,0xb2a73072,0x2a3e33d9,0x33d92a3e,0x3072b2a7,
0x31a831a8,0x31a8b1a8,0xb1a8b1a8,0xb1a831a8,0x3072b2a7,0xb3d9aa3e,0x2a3e33d9,0x32a7b072,0xb364ae1f,0x2e1f3364,0x2e1fb364,0xb3642e1f,0xaa3e33d9,0x3072b2a7,0xb2a73072,0x33d9aa3e,
0x34000000,0xb4000000,0x34000000,0xb4000000,0xaa3eb3d9,0x307232a7,0xb2a7b072,0x33d92a3e,0xb3642e1f,0x2e1fb364,0x2e1f3364,0xb364ae1f,0x307232a7,0xb3d92a3e,0x2a3eb3d9,0x32a73072,
0x31a8b1a8,0x31a831a8,0xb1a831a8,0xb1a8b1a8,0xb2a7b072,0x2a3eb3d9,0x33d9aa3e,0x307232a7,0xae1f3364,0xb3642e1f,0xb364ae1f,0xae1fb364,0x33d92a3e,0x32a73072,0x307232a7,0x2a3e33d9,
13312,13312,13312,13312,0xaa3eb3d9,0xb072b2a7,0xb2a7b072,0xb3d9aa3e,0x2e1f3364,0x33642e1f,0x3364ae1f,0x2e1fb364,0xb072b2a7,0xb3d92a3e,0xaa3e33d9,0x32a73072,
0x31a831a8,0x31a8b1a8,0xb1a8b1a8,0xb1a831a8,0xb2a7b072,0xaa3e33d9,0x33d9aa3e,0xb072b2a7,0x33642e1f,0xae1fb364,0xae1f3364,0x3364ae1f,0xb3d9aa3e,0x32a73072,0xb072b2a7,0x2a3e33d9,
0x34000000,0xb4000000,0x34000000,0xb4000000,0xb3d92a3e,0x32a7b072,0xb07232a7,0x2a3eb3d9,0x3364ae1f,0xae1f3364,0xae1fb364,0x33642e1f,0xb2a73072,0xaa3eb3d9,0x33d92a3e,0xb07232a7,
0x31a8b1a8,0x31a831a8,0xb1a831a8,0xb1a8b1a8,0xb07232a7,0xb3d9aa3e,0xaa3eb3d9,0x32a7b072,0x2e1fb364,0x3364ae1f,0x33642e1f,0x2e1f3364,0xaa3e33d9,0xb07232a7,0xb2a73072,0xb3d92a3e,
13312,13312,13312,13312,0xb3d92a3e,0xb2a73072,0xb07232a7,0xaa3e33d9,0xae1fb364,0xb364ae1f,0xb3642e1f,0xae1f3364,0x32a7b072,0xaa3eb3d9,0xb3d9aa3e,0xb07232a7,
0x31a831a8,0x31a8b1a8,0xb1a8b1a8,0xb1a831a8,0xb07232a7,0x33d92a3e,0xaa3eb3d9,0xb2a73072,0xb364ae1f,0x2e1f3364,0x2e1fb364,0xb3642e1f,0x2a3eb3d9,0xb07232a7,0x32a7b072,0xb3d92a3e,
0x34000000,0xb4000000,0x34000000,0xb4000000,0x2a3e33d9,0xb072b2a7,0x32a73072,0xb3d9aa3e,0xb3642e1f,0x2e1fb364,0x2e1f3364,0xb364ae1f,0xb072b2a7,0x33d9aa3e,0xaa3e33d9,0xb2a7b072,
0x31a8b1a8,0x31a831a8,0xb1a831a8,0xb1a8b1a8,0x32a73072,0xaa3e33d9,0xb3d92a3e,0xb072b2a7,0xae1f3364,0xb3642e1f,0xb364ae1f,0xae1fb364,0xb3d9aa3e,0xb2a7b072,0xb072b2a7,0xaa3eb3d9,
0x34000000,0x34000000,0x34000000,0x34000000,0x33d9aa3e,0x32a7b072,0x3072b2a7,0x2a3eb3d9,0x3364ae1f,0x2e1fb364,0xae1fb364,0xb364ae1f,0x32a7b072,0xaa3eb3d9,0xb3d9aa3e,0xb07232a7,
0x31a8b1a8,0xb1a8b1a8,0xb1a831a8,0x31a831a8,0x3072b2a7,0xb3d9aa3e,0x2a3e33d9,0x32a7b072,0x2e1fb364,0xb3642e1f,0x33642e1f,0xae1fb364,0x2a3eb3d9,0xb07232a7,0x32a7b072,0xb3d92a3e,
46080,13312,46080,13312,0xaa3eb3d9,0x307232a7,0xb2a7b072,0x33d92a3e,0xae1fb364,0x33642e1f,0xb3642e1f,0x2e1fb364,0xb072b2a7,0x33d9aa3e,0xaa3e33d9,0xb2a7b072,
0xb1a8b1a8,0x31a8b1a8,0x31a831a8,0xb1a831a8,0xb2a7b072,0x2a3eb3d9,0x33d9aa3e,0x307232a7,0xb364ae1f,0xae1fb364,0x2e1fb364,0x3364ae1f,0xb3d9aa3e,0xb2a7b072,0xb072b2a7,0xaa3eb3d9,
0x34000000,0x34000000,0x34000000,0x34000000,0xaa3eb3d9,0xb072b2a7,0xb2a7b072,0xb3d9aa3e,0xb3642e1f,0xae1f3364,0x2e1f3364,0x33642e1f,0x307232a7,0x33d9aa3e,0x2a3eb3d9,0xb2a7b072,
0x31a8b1a8,0xb1a8b1a8,0xb1a831a8,0x31a831a8,0xb2a7b072,0xaa3e33d9,0x33d9aa3e,0xb072b2a7,0xae1f3364,0x3364ae1f,0xb364ae1f,0x2e1f3364,0x33d92a3e,0xb2a7b072,0x307232a7,0xaa3eb3d9,
46080,13312,46080,13312,0xb3d92a3e,0x32a7b072,0xb07232a7,0x2a3eb3d9,0x2e1f3364,0xb364ae1f,0x3364ae1f,0xae1f3364,0x32a7b072,0x2a3e33d9,0xb3d9aa3e,0x3072b2a7,
0xb1a8b1a8,0x31a8b1a8,0x31a831a8,0xb1a831a8,0xb07232a7,0xb3d9aa3e,0xaa3eb3d9,0x32a7b072,0x33642e1f,0x2e1f3364,0xae1f3364,0xb3642e1f,0x2a3eb3d9,0x3072b2a7,0x32a7b072,0x33d9aa3e,
0x34000000,0x34000000,0x34000000,0x34000000,0xb3d92a3e,0xb2a73072,0xb07232a7,0xaa3e33d9,0x3364ae1f,0x2e1fb364,0xae1fb364,0xb364ae1f,0xb2a73072,0x2a3e33d9,0x33d92a3e,0x3072b2a7,
0x31a8b1a8,0xb1a8b1a8,0xb1a831a8,0x31a831a8,0xb07232a7,0x33d92a3e,0xaa3eb3d9,0xb2a73072,0x2e1fb364,0xb3642e1f,0x33642e1f,0xae1fb364,0xaa3e33d9,0x3072b2a7,0xb2a73072,0x33d9aa3e,
46080,13312,46080,13312,0x2a3e33d9,0xb072b2a7,0x32a73072,0xb3d9aa3e,0xae1fb364,0x33642e1f,0xb3642e1f,0x2e1fb364,0x307232a7,0xb3d92a3e,0x2a3eb3d9,0x32a73072,
0xb1a8b1a8,0x31a8b1a8,0x31a831a8,0xb1a831a8,0x32a73072,0xaa3e33d9,0xb3d92a3e,0xb072b2a7,0xb364ae1f,0xae1fb364,0x2e1fb364,0x3364ae1f,0x33d92a3e,0x32a73072,0x307232a7,0x2a3e33d9,
0x34000000,0x34000000,0x34000000,0x34000000,0x2a3e33d9,0x307232a7,0x32a73072,0x33d92a3e,0xb3642e1f,0xae1f3364,0x2e1f3364,0x33642e1f,0xb072b2a7,0xb3d92a3e,0xaa3e33d9,0x32a73072,
0x31a8b1a8,0xb1a8b1a8,0xb1a831a8,0x31a831a8,0x32a73072,0x2a3eb3d9,0xb3d92a3e,0x307232a7,0xae1f3364,0x3364ae1f,0xb364ae1f,0x2e1f3364,0xb3d9aa3e,0x32a73072,0xb072b2a7,0x2a3e33d9,
46080,13312,46080,13312,0x33d9aa3e,0xb2a73072,0x3072b2a7,0xaa3e33d9,0x2e1f3364,0xb364ae1f,0x3364ae1f,0xae1f3364,0xb2a73072,0xaa3eb3d9,0x33d92a3e,0xb07232a7,
0xb1a8b1a8,0x31a8b1a8,0x31a831a8,0xb1a831a8,0x3072b2a7,0x33d92a3e,0x2a3e33d9,0xb2a73072,0x33642e1f,0x2e1f3364,0xae1f3364,0xb3642e1f,0xaa3e33d9,0xb07232a7,0xb2a73072,0xb3d92a3e,
13312,13312,13312,0,13312,0x31243221,0x33e1298f,0,13312,0x33e1298f,0x2d79b384,0,0,0,0,0,
13312,0x32eeb000,0xb2eeb000,0,13312,0x2d79b384,0xb1243221,0,13312,0xad79b384,0x31243221,0,0,0,0,0,
13312,0xb2eeb000,0x32eeb000,0,13312,0xb3e1298f,0xad79b384,0,13312,0xb1243221,0xb3e1298f,0,0,0,0,0,
0,0,0,0,0,0,0,0,0,0,0,0,0,0,0,0,
13312,13312,13312,0,0x32eeb000,0x2d79b384,0xad79b384,0,0xb2eeb000,0xb1243221,0x31243221,0,0,0,0,0,
13312,0x32eeb000,0xb2eeb000,0,0x32eeb000,0xb3e1298f,0x33e1298f,0,0xb2eeb000,0x33e1298f,0xb3e1298f,0,0,0,0,0,
13312,0xb2eeb000,0x32eeb000,0,0x32eeb000,0x31243221,0xb1243221,0,0xb2eeb000,0xad79b384,0x2d79b384,0,0,0,0,0,
0,0,0,0,0,0,0,0,0,0,0,0,0,0,0,0,
13312,13312,13312,0,0xb2eeb000,0xb3e1298f,0xb1243221,0,0x32eeb000,0xad79b384,0xb3e1298f,0,0,0,0,0,
13312,0x32eeb000,0xb2eeb000,0,0xb2eeb000,0x31243221,0xad79b384,0,0x32eeb000,0xb1243221,0x2d79b384,0,0,0,0,0,
13312,0xb2eeb000,0x32eeb000,0,0xb2eeb000,0x2d79b384,0x33e1298f,0,0x32eeb000,0x33e1298f,0x31243221,0,0,0,0,0,
0,0,0,0,0,0,0,0,0,0,0,0,0,0,0,0,
0,0,0,0,0,0,0,0,0,0,0,0,0,0,0,0,
0,0,0,0,0,0,0,0,0,0,0,0,0,0,0,0,
0,0,0,0,0,0,0,0,0,0,0,0,0,0,0,0,
0,0,0,0,0,0,0,0,0,0,0,0,0,0,0,0,
0x34000000,0x34000000,0x34000000,0,0x34000000,0x3221b124,0x298fb3e1,0,0x34000000,0x298fb3e1,0xb384ad79,0,0,0,0,0,
0x34000000,0xb000b2ee,0xb00032ee,0,0x34000000,0xb384ad79,0x32213124,0,0x34000000,0xb3842d79,0x3221b124,0,0,0,0,0,
0x34000000,0xb00032ee,0xb000b2ee,0,0x34000000,0x298f33e1,0xb3842d79,0,0x34000000,0x32213124,0x298f33e1,0,0,0,0,0,
0,0,0,0,0,0,0,0,0,0,0,0,0,0,0,0,
0x34000000,0x34000000,0x34000000,0,0xb000b2ee,0xb384ad79,0xb3842d79,0,0xb00032ee,0x32213124,0x3221b124,0,0,0,0,0,
0x34000000,0xb000b2ee,0xb00032ee,0,0xb000b2ee,0x298f33e1,0x298fb3e1,0,0xb00032ee,0x298fb3e1,0x298f33e1,0,0,0,0,0,
0x34000000,0xb00032ee,0xb000b2ee,0,0xb000b2ee,0x3221b124,0x32213124,0,0xb00032ee,0xb3842d79,0xb384ad79,0,0,0,0,0,
0,0,0,0,0,0,0,0,0,0,0,0,0,0,0,0,
0x34000000,0x34000000,0x34000000,0,0xb00032ee,0x298f33e1,0x32213124,0,0xb000b2ee,0xb3842d79,0x298f33e1,0,0,0,0,0,
0x34000000,0xb000b2ee,0xb00032ee,0,0xb00032ee,0x3221b124,0xb3842d79,0,0xb000b2ee,0x32213124,0xb384ad79,0,0,0,0,0,
0x34000000,0xb00032ee,0xb000b2ee,0,0xb00032ee,0xb384ad79,0x298fb3e1,0,0xb000b2ee,0x298fb3e1,0x3221b124,0,0,0,0,0,
0,0,0,0,0,0,0,0,0,0,0,0,0,0,0,0,
0,0,0,0,0,0,0,0,0,0,0,0,0,0,0,0,
0,0,0,0,0,0,0,0,0,0,0,0,0,0,0,0,
0,0,0,0,0,0,0,0,0,0,0,0,0,0,0,0,
0,0,0,0,0,0,0,0,0,0,0,0,0,0,0,0,
13312,0x2a3e33d9,0x2e1f3364,0x307232a7,13312,0x307232a7,0x33642e1f,0x33d9aa3e,13312,0x32a73072,0x3364ae1f,0x2a3eb3d9,13312,0x33d92a3e,0x2e1fb364,0xb2a7b072,
13312,0x33d9aa3e,0xae1fb364,0xb2a73072,13312,0x32a7b072,0xb364ae1f,0x2a3e33d9,13312,0x3072b2a7,0xb3642e1f,0x33d92a3e,13312,0x2a3eb3d9,0xae1f3364,0x3072b2a7,
13312,0xaa3eb3d9,0x2e1f3364,0xb072b2a7,13312,0xb072b2a7,0x33642e1f,0xb3d92a3e,13312,0xb2a7b072,0x3364ae1f,0xaa3e33d9,13312,0xb3d9aa3e,0x2e1fb364,0x32a73072,
13312,0xb3d92a3e,0xae1fb364,0x32a7b072,13312,0xb2a73072,0xb364ae1f,0xaa3eb3d9,13312,0xb07232a7,0xb3642e1f,0xb3d9aa3e,13312,0xaa3e33d9,0xae1f3364,0xb07232a7,
0x31a831a8,0x32a73072,0x33642e1f,0x33d92a3e,0x31a8b1a8,0x2a3eb3d9,0xae1fb364,0xb2a7b072,0xb1a8b1a8,0xb3d92a3e,0xae1f3364,0x307232a7,0xb1a831a8,0x307232a7,0x3364ae1f,0xaa3eb3d9,
0x31a831a8,0x3072b2a7,0xb364ae1f,0xaa3e33d9,0x31a8b1a8,0xb3d9aa3e,0x2e1f3364,0x3072b2a7,0xb1a8b1a8,0x2a3e33d9,0x2e1fb364,0xb2a73072,0xb1a831a8,0x32a7b072,0xb3642e1f,0x33d9aa3e,
0x31a831a8,0xb2a7b072,0x33642e1f,0xb3d9aa3e,0x31a8b1a8,0xaa3e33d9,0xae1fb364,0x32a73072,0xb1a8b1a8,0x33d9aa3e,0xae1f3364,0xb072b2a7,0xb1a831a8,0xb072b2a7,0x3364ae1f,0x2a3e33d9,
0x31a831a8,0xb07232a7,0xb364ae1f,0x2a3eb3d9,0x31a8b1a8,0x33d92a3e,0x2e1f3364,0xb07232a7,0xb1a8b1a8,0xaa3eb3d9,0x2e1fb364,0x32a7b072,0xb1a831a8,0xb2a73072,0xb3642e1f,0xb3d92a3e,
0x34000000,0x33d9aa3e,0x3364ae1f,0x32a7b072,0xb4000000,0xb2a73072,0xae1f3364,0x2a3e33d9,0x34000000,0x3072b2a7,0xae1fb364,0xb3d9aa3e,0xb4000000,0xaa3e33d9,0x33642e1f,0x3072b2a7,
0x34000000,0xaa3eb3d9,0xb3642e1f,0x307232a7,0xb4000000,0x307232a7,0x2e1fb364,0xb3d92a3e,0x34000000,0xb2a7b072,0x2e1f3364,0x2a3eb3d9,0xb4000000,0x33d92a3e,0xb364ae1f,0x32a73072,
0x34000000,0xb3d92a3e,0x3364ae1f,0xb2a73072,0xb4000000,0x32a7b072,0xae1f3364,0xaa3eb3d9,0x34000000,0xb07232a7,0xae1fb364,0x33d92a3e,0xb4000000,0x2a3eb3d9,0x33642e1f,0xb07232a7,
0x34000000,0x2a3e33d9,0xb3642e1f,0xb072b2a7,0xb4000000,0xb072b2a7,0x2e1fb364,0x33d9aa3e,0x34000000,0x32a73072,0x2e1f3364,0xaa3e33d9,0xb4000000,0xb3d9aa3e,0xb364ae1f,0xb2a7b072,
0x31a8b1a8,0x3072b2a7,0x2e1fb364,0x2a3eb3d9,0x31a831a8,0x33d92a3e,0x3364ae1f,0x3072b2a7,0xb1a831a8,0x2a3e33d9,0x33642e1f,0x32a7b072,0xb1a8b1a8,0xb2a73072,0x2e1f3364,0x33d9aa3e,
0x31a8b1a8,0xb2a7b072,0xae1f3364,0x33d92a3e,0x31a831a8,0x2a3eb3d9,0xb3642e1f,0x32a73072,0xb1a831a8,0x33d9aa3e,0xb364ae1f,0x307232a7,0xb1a8b1a8,0x307232a7,0xae1fb364,0x2a3e33d9,
0x31a8b1a8,0xb07232a7,0x2e1fb364,0xaa3e33d9,0x31a831a8,0xb3d9aa3e,0x3364ae1f,0xb07232a7,0xb1a831a8,0xaa3eb3d9,0x33642e1f,0xb2a73072,0xb1a8b1a8,0x32a7b072,0x2e1f3364,0xb3d92a3e,
0x31a8b1a8,0x32a73072,0xae1f3364,0xb3d9aa3e,0x31a831a8,0xaa3e33d9,0xb3642e1f,0xb2a7b072,0xb1a831a8,0xb3d92a3e,0xb364ae1f,0xb072b2a7,0xb1a8b1a8,0xb072b2a7,0xae1fb364,0xaa3eb3d9,
0x34000000,0x33d9aa3e,0x3364ae1f,0x32a7b072,0x34000000,0x32a7b072,0x2e1fb364,0xaa3eb3d9,0x34000000,0x3072b2a7,0xae1fb364,0xb3d9aa3e,0x34000000,0x2a3eb3d9,0xb364ae1f,0xb07232a7,
0x34000000,0xaa3eb3d9,0xb3642e1f,0x307232a7,0x34000000,0xb072b2a7,0xae1f3364,0x33d9aa3e,0x34000000,0xb2a7b072,0x2e1f3364,0x2a3eb3d9,0x34000000,0xb3d9aa3e,0x33642e1f,0xb2a7b072,
0x34000000,0xb3d92a3e,0x3364ae1f,0xb2a73072,0x34000000,0xb2a73072,0x2e1fb364,0x2a3e33d9,0x34000000,0xb07232a7,0xae1fb364,0x33d92a3e,0x34000000,0xaa3e33d9,0xb364ae1f,0x3072b2a7,
0x34000000,0x2a3e33d9,0xb3642e1f,0xb072b2a7,0x34000000,0x307232a7,0xae1f3364,0xb3d92a3e,0x34000000,0x32a73072,0x2e1f3364,0xaa3e33d9,0x34000000,0x33d92a3e,0x33642e1f,0x32a73072,
0x31a8b1a8,0x3072b2a7,0x2e1fb364,0x2a3eb3d9,0xb1a8b1a8,0xb3d9aa3e,0xb3642e1f,0xb07232a7,0xb1a831a8,0x2a3e33d9,0x33642e1f,0x32a7b072,0x31a831a8,0x32a7b072,0xae1fb364,0xb3d92a3e,
0x31a8b1a8,0xb2a7b072,0xae1f3364,0x33d92a3e,0xb1a8b1a8,0xaa3e33d9,0x3364ae1f,0xb2a7b072,0xb1a831a8,0x33d9aa3e,0xb364ae1f,0x307232a7,0x31a831a8,0xb072b2a7,0x2e1f3364,0xaa3eb3d9,
0x31a8b1a8,0xb07232a7,0x2e1fb364,0xaa3e33d9,0xb1a8b1a8,0x33d92a3e,0xb3642e1f,0x3072b2a7,0xb1a831a8,0xaa3eb3d9,0x33642e1f,0xb2a73072,0x31a831a8,0xb2a73072,0xae1fb364,0x33d9aa3e,
0x31a8b1a8,0x32a73072,0xae1f3364,0xb3d9aa3e,0xb1a8b1a8,0x2a3eb3d9,0x3364ae1f,0x32a73072,0xb1a831a8,0xb3d92a3e,0xb364ae1f,0xb072b2a7,0x31a831a8,0x307232a7,0x2e1f3364,0x2a3e33d9,
46080,0xaa3eb3d9,0xae1fb364,0xb072b2a7,13312,0x307232a7,0x33642e1f,0x33d9aa3e,46080,0xb2a7b072,0xb3642e1f,0xaa3e33d9,13312,0x33d92a3e,0x2e1fb364,0xb2a7b072,
46080,0xb3d92a3e,0x2e1f3364,0x32a7b072,13312,0x32a7b072,0xb364ae1f,0x2a3e33d9,46080,0xb07232a7,0x3364ae1f,0xb3d9aa3e,13312,0x2a3eb3d9,0xae1f3364,0x3072b2a7,
46080,0x2a3e33d9,0xae1fb364,0x307232a7,13312,0xb072b2a7,0x33642e1f,0xb3d92a3e,46080,0x32a73072,0xb3642e1f,0x2a3eb3d9,13312,0xb3d9aa3e,0x2e1fb364,0x32a73072,
46080,0x33d9aa3e,0x2e1f3364,0xb2a73072,13312,0xb2a73072,0xb364ae1f,0xaa3eb3d9,46080,0x3072b2a7,0x3364ae1f,0x33d92a3e,13312,0xaa3e33d9,0xae1f3364,0xb07232a7,
0xb1a8b1a8,0xb2a7b072,0xb364ae1f,0xb3d9aa3e,0x31a8b1a8,0x2a3eb3d9,0xae1fb364,0xb2a7b072,0x31a831a8,0x33d9aa3e,0x2e1fb364,0xb072b2a7,0xb1a831a8,0x307232a7,0x3364ae1f,0xaa3eb3d9,
0xb1a8b1a8,0xb07232a7,0x33642e1f,0x2a3eb3d9,0x31a8b1a8,0xb3d9aa3e,0x2e1f3364,0x3072b2a7,0x31a831a8,0xaa3eb3d9,0xae1f3364,0x32a7b072,0xb1a831a8,0x32a7b072,0xb3642e1f,0x33d9aa3e,
0xb1a8b1a8,0x32a73072,0xb364ae1f,0x33d92a3e,0x31a8b1a8,0xaa3e33d9,0xae1fb364,0x32a73072,0x31a831a8,0xb3d92a3e,0x2e1fb364,0x307232a7,0xb1a831a8,0xb072b2a7,0x3364ae1f,0x2a3e33d9,
0xb1a8b1a8,0x3072b2a7,0x33642e1f,0xaa3e33d9,0x31a8b1a8,0x33d92a3e,0x2e1f3364,0xb07232a7,0x31a831a8,0x2a3e33d9,0xae1f3364,0xb2a73072,0xb1a831a8,0xb2a73072,0xb3642e1f,0xb3d92a3e,
0x3f800000,0x3f800000,0x3f800000,0x3f800000,0x3f800000,0x3f7fec43,0x3f7fb10f,0x3f7f4e6d,0x3f800000,0x3f7fb10f,0x3f7ec46d,0x3f7d3aac,0x3f800000,0x3f7f4e6d,0x3f7d3aac,0x3f79c79d,
0x3f800000,0x3f7ec46d,0x3f7b14be,0x3f74fa0b,0x3f800000,0x3f7e1324,0x3f7853f8,0x3f6ed89e,0x3f800000,0x3f7d3aac,0x3f74fa0b,0x3f676bd8,0x3f800000,0x3f7c3b28,0x3f710908,0x3f5ebe05,
0x3f800000,0x3f7b14be,0x3f6c835e,0x3f54db31,0x3f800000,0x3f79c79d,0x3f676bd8,0x3f49d112,0x3f800000,0x3f7853f8,0x3f61c598,0x3f3daef9,0x3f800000,0x3f76ba07,0x3f5b941a,0x3f3085bb,
0x3f800000,0x3f74fa0b,0x3f54db31,0x3f226799,0x3f800000,0x3f731447,0x3f4d9f02,0x3f13682a,0x3f800000,0x3f710908,0x3f45e403,0x3f039c3d,0x3f800000,0x3f6ed89e,0x3f3daef9,0x3ee63375,
0x3f800000,0x3f800000,0x3f800000,0x3f800000,0x3f7ec46d,0x3f7e1324,0x3f7d3aac,0x3f7c3b28,0x3f7b14be,0x3f7853f8,0x3f74fa0b,0x3f710908,0x3f74fa0b,0x3f6ed89e,0x3f676bd8,0x3f5ebe05,
0x3f6c835e,0x3f61c598,0x3f54db31,0x3f45e403,0x3f61c598,0x3f514d3d,0x3f3daef9,0x3f273656,0x3f54db31,0x3f3daef9,0x3f226799,0x3f039c3d,0x3f45e403,0x3f273656,0x3f039c3d,0x3eb8442a,
0x3f3504f3,0x3f0e39da,0x3ec3ef15,0x3e47c5c2,0x3f226799,0x3ee63375,0x3e78cfcc,0x3cc90ab0,0x3f0e39da,0x3eac7cd4,0x3dc8bd36,0xbe164083,0x3ef15aea,0x3e605c13,0xbd48fb30,0xbea09ae5,
0x3ec3ef15,0x3dc8bd36,0xbe47c5c2,0xbef15aea,0x3e94a031,0xbcc90ab0,0xbeac7cd4,0xbf1d7fd1,0x3e47c5c2,0xbe164083,0xbef15aea,0xbf3daef9,0x3dc8bd36,0xbe888e93,0xbf187fc0,0xbf584853,
0x3f800000,0x3f800000,0x3f800000,0x3f800000,0x3f7b14be,0x3f79c79d,0x3f7853f8,0x3f76ba07,0x3f6c835e,0x3f676bd8,0x3f61c598,0x3f5b941a,0x3f54db31,0x3f49d112,0x3f3daef9,0x3f3085bb,
0x3f3504f3,0x3f226799,0x3f0e39da,0x3ef15aea,0x3f0e39da,0x3ee63375,0x3eac7cd4,0x3e605c13,0x3ec3ef15,0x3e78cfcc,0x3dc8bd36,0xbd48fb30,0x3e47c5c2,0x3cc90ab0,0xbe164083,0xbea09ae5,
0x248d3132,0xbe47c5c2,0xbec3ef15,0xbf0e39da,0xbe47c5c2,0xbecf7bca,0xbf187fc0,0xbf41d870,0xbec3ef15,0xbf187fc0,0xbf45e403,0xbf676bd8,0xbf0e39da,0xbf41d870,0xbf676bd8,0xbf7c3b28,
0xbf3504f3,0xbf61c598,0xbf7b14be,0xbf7ec46d,0xbf54db31,0xbf76ba07,0xbf7fb10f,0xbf6ed89e,0xbf6c835e,0xbf7fb10f,0xbf74fa0b,0xbf4d9f02,0xbf7b14be,0xbf7c3b28,0xbf5b941a,0xbf1d7fd1,
0x3f800000,0x3f800000,0x3f800000,0x3f800000,0x3f74fa0b,0x3f731447,0x3f710908,0x3f6ed89e,0x3f54db31,0x3f4d9f02,0x3f45e403,0x3f3daef9,0x3f226799,0x3f13682a,0x3f039c3d,0x3ee63375,
0x3ec3ef15,0x3e94a031,0x3e47c5c2,0x3dc8bd36,0x3dc8bd36,0xbcc90ab0,0xbe164083,0xbe888e93,0xbe47c5c2,0xbeac7cd4,0xbef15aea,0xbf187fc0,0xbef15aea,0xbf1d7fd1,0xbf3daef9,0xbf584853,
0xbf3504f3,0xbf54db31,0xbf6c835e,0xbf7b14be,0xbf61c598,0xbf76ba07,0xbf7fb10f,0xbf7c3b28,0xbf7b14be,0xbf7fb10f,0xbf74fa0b,0xbf5b941a,0xbf7ec46d,0xbf6ed89e,0xbf4d9f02,0xbf1d7fd1,
0xbf6c835e,0xbf45e403,0xbf0e39da,0xbe94a031,0xbf45e403,0xbf08f59b,0xbe78cfcc,0x3d96a905,0xbf0e39da,0xbe78cfcc,0x3dc8bd36,0x3edae880,0xbe94a031,0x3d96a905,0x3edae880,0x3f396842,
0,0,0,0,0,0xbcc90ab0,0xbd48fb30,0xbd96a905,0,0xbd48fb30,0xbdc8bd36,0xbe164083,0,0xbd96a905,0xbe164083,0xbe605c13,
0,0xbdc8bd36,0xbe47c5c2,0xbe94a031,0,0xbdfab273,0xbe78cfcc,0xbeb8442a,0,0xbe164083,0xbe94a031,0xbedae880,0,0xbe2f10a2,0xbeac7cd4,0xbefc5d27,
0,0xbe47c5c2,0xbec3ef15,0xbf0e39da,0,0xbe605c13,0xbedae880,0xbf1d7fd1,0,0xbe78cfcc,0xbef15aea,0xbf2beb4a,0,0xbe888e93,0xbf039c3d,0xbf396842,
0,0xbe94a031,0xbf0e39da,0xbf45e403,0,0xbea09ae5,0xbf187fc0,0xbf514d3d,0,0xbeac7cd4,0xbf226799,0xbf5b941a,0,0xbeb8442a,0xbf2beb4a,0xbf64aa59,
0,0,0,0,0xbdc8bd36,0xbdfab273,0xbe164083,0xbe2f10a2,0xbe47c5c2,0xbe78cfcc,0xbe94a031,0xbeac7cd4,0xbe94a031,0xbeb8442a,0xbedae880,0xbefc5d27,
0xbec3ef15,0xbef15aea,0xbf0e39da,0xbf226799,0xbef15aea,0xbf13682a,0xbf2beb4a,0xbf41d870,0xbf0e39da,0xbf2beb4a,0xbf45e403,0xbf5b941a,0xbf226799,0xbf41d870,0xbf5b941a,0xbf6ed89e,
0xbf3504f3,0xbf54db31,0xbf6c835e,0xbf7b14be,0xbf45e403,0xbf64aa59,0xbf7853f8,0xbf7fec43,0xbf54db31,0xbf710908,0xbf7ec46d,0xbf7d3aac,0xbf61c598,0xbf79c79d,0xbf7fb10f,0xbf731447,
0xbf6c835e,0xbf7ec46d,0xbf7b14be,0xbf61c598,0xbf74fa0b,0xbf7fec43,0xbf710908,0xbf49d112,0xbf7b14be,0xbf7d3aac,0xbf61c598,0xbf2beb4a,0xbf7ec46d,0xbf76ba07,0xbf4d9f02,0xbf08f59b,
0,0,0,0,0xbe47c5c2,0xbe605c13,0xbe78cfcc,0xbe888e93,0xbec3ef15,0xbedae880,0xbef15aea,0xbf039c3d,0xbf0e39da,0xbf1d7fd1,0xbf2beb4a,0xbf396842,
0xbf3504f3,0xbf45e403,0xbf54db31,0xbf61c598,0xbf54db31,0xbf64aa59,0xbf710908,0xbf79c79d,0xbf6c835e,0xbf7853f8,0xbf7ec46d,0xbf7fb10f,0xbf7b14be,0xbf7fec43,0xbf7d3aac,0xbf731447,
0xbf800000,0xbf7b14be,0xbf6c835e,0xbf54db31,0xbf7b14be,0xbf6a09a7,0xbf4d9f02,0xbf273656,0xbf6c835e,0xbf4d9f02,0xbf226799,0xbedae880,0xbf54db31,0xbf273656,0xbedae880,0xbe2f10a2,
0xbf3504f3,0xbef15aea,0xbe47c5c2,0x3dc8bd36,0xbf0e39da,0xbe888e93,0x3d48fb30,0x3eb8442a,0xbec3ef15,0xbd48fb30,0x3e94a031,0x3f187fc0,0xbe47c5c2,0x3e2f10a2,0x3f039c3d,0x3f49d112,
0,0,0,0,0xbe94a031,0xbea09ae5,0xbeac7cd4,0xbeb8442a,0xbf0e39da,0xbf187fc0,0xbf226799,0xbf2beb4a,0xbf45e403,0xbf514d3d,0xbf5b941a,0xbf64aa59,
0xbf6c835e,0xbf74fa0b,0xbf7b14be,0xbf7ec46d,0xbf7ec46d,0xbf7fec43,0xbf7d3aac,0xbf76ba07,0xbf7b14be,0xbf710908,0xbf61c598,0xbf4d9f02,0xbf61c598,0xbf49d112,0xbf2beb4a,0xbf08f59b,
0xbf3504f3,0xbf0e39da,0xbec3ef15,0xbe47c5c2,0xbef15aea,0xbe888e93,0xbd48fb30,0x3e2f10a2,0xbe47c5c2,0x3d48fb30,0x3e94a031,0x3f039c3d,0x3dc8bd36,0x3eb8442a,0x3f187fc0,0x3f49d112,
0x3ec3ef15,0x3f226799,0x3f54db31,0x3f74fa0b,0x3f226799,0x3f584853,0x3f7853f8,0x3f7f4e6d,0x3f54db31,0x3f7853f8,0x3f7ec46d,0x3f676bd8,0x3f74fa0b,0x3f7f4e6d,0x3f676bd8,0x3f3085bb,
0x20000000,0x204001c2,0x7c000fe,0,0x20100b98,0x20500d5a,0x7d00c96,0,0x20200482,0x206002c0,0x7e00384,0,0x2030101a,0x20700e58,0x7f00f1c,0,
0x24400240,128,0x8000100,0,0x24500dd8,1051672,0x8100c98,0,0x24600242,2098178,0x8200382,0,0x24700dda,3149722,0x8300f1a,0,
4194306,0x8400082,0x10400102,0,5245850,0x8500c1a,0x10500c9a,0,6292608,0x8600400,0x10600380,0,7344152,0x8700f98,0x10700f18,0,
0x8800004,0x10800084,0x18800104,0,0x8900b9c,0x10900c1c,0x18900c9c,0,0x8a0047e,0x10a003fe,0x18a0037e,0,0x8b01016,0x10b00f96,0x18b00f16,0,
0x10c00006,0x18c00086,0x1f40013a,0,0x10d00b9e,0x18d00c1e,0x1f500cd2,0,0x10e0047c,0x18e003fc,0x1f600348,0,0x10f01014,0x18f00f94,0x1f700ee0,0,
0x19000008,0x1f0001b8,0x17000138,0,0x19100ba0,0x1f100d50,0x17100cd0,0,0x1920047a,0x1f2002ca,0x1720034a,0,0x19301012,0x1f300e62,0x17300ee2,0,
0x1ec00236,0x16c001b6,0xec00136,0,0x1ed00dce,0x16d00d4e,0xed00cce,0,0x1ee0024c,0x16e002cc,0xee0034c,0,0x1ef00de4,0x16f00e64,0xef00ee4,0,
0x16800234,0xe8001b4,0x6800134,0,0x16900dcc,0xe900d4c,0x6900ccc,0,0x16a0024e,0xea002ce,0x6a0034e,0,0x16b00de6,0xeb00e66,0x6b00ee6,0,
0xe400232,0x64001b2,0x21c0010e,0,0xe500dca,0x6500d4a,0x21d00ca6,0,0xe600250,0x66002d0,0x21e00374,0,0xe700de8,0x6700e68,0x21f00f0c,0,
0x6000230,0x22000090,0x2000110,0,0x6100dc8,0x22100c28,0x2100ca8,0,0x6200252,0x222003f2,0x2200372,0,0x6300dea,0x22300f8a,0x2300f0a,0,
0x22400012,0x2400092,0xa400112,0,0x22500baa,0x2500c2a,0xa500caa,0,0x22600470,0x26003f0,0xa600370,0,0x22701008,0x2700f88,0xa700f08,0,
0x2800014,0xa800094,0x12800114,0,0x2900bac,0xa900c2c,0x12900cac,0,0x2a0046e,0xaa003ee,0x12a0036e,0,0x2b01006,0xab00f86,0x12b00f06,0,
0xac00016,0x12c00096,0x1ac00116,0,0xad00bae,0x12d00c2e,0x1ad00cae,0,0xae0046c,0x12e003ec,0x1ae0036c,0,0xaf01004,0x12f00f84,0x1af00f04,0,
0x13000018,0x1b000098,0x1d000128,0,0x13100bb0,0x1b100c30,0x1d100cc0,0,0x1320046a,0x1b2003ea,0x1d20035a,0,0x13301002,0x1b300f82,0x1d300ef2,0,
0x1b40001a,0x1cc001a6,0x14c00126,0,0x1b500bb2,0x1cd00d3e,0x14d00cbe,0,0x1b600468,0x1ce002dc,0x14e0035c,0,0x1b701000,0x1cf00e74,0x14f00ef4,0,
0x1c800224,0x148001a4,0xc800124,0,0x1c900dbc,0x14900d3c,0xc900cbc,0,0x1ca0025e,0x14a002de,0xca0035e,0,0x1cb00df6,0x14b00e76,0xcb00ef6,0,
0x14400222,0xc4001a2,0x4400122,0,0x14500dba,0xc500d3a,0x4500cba,0,0x14600260,0xc6002e0,0x4600360,0,0x14700df8,0xc700e78,0x4700ef8,0,
0xc000220,0x40001a0,0x24000120,0,0xc100db8,0x4100d38,0x24100cb8,0,0xc200262,0x42002e2,0x24200362,0,0xc300dfa,0x4300e7a,0x24300efa,0,
0x3c0021e,0x23c0019e,0x24000120,0,0x3d00db6,0x23d00d36,0x24100cb8,0,0x3e00264,0x23e002e4,0x24200362,0,0x3f00dfc,0x23f00e7c,0x24300efa,0,
0x2380021c,0x48000a4,0x24000120,0,0x23900db4,0x4900c3c,0x24100cb8,0,0x23a00266,0x4a003de,0x24200362,0,0x23b00dfe,0x4b00f76,0x24300efa,0,
0x4c00026,0xcc000a6,0x24000120,0,0x4d00bbe,0xcd00c3e,0x24100cb8,0,0x4e0045c,0xce003dc,0x24200362,0,0x4f00ff4,0xcf00f74,0x24300efa,0,
0xd000028,0x150000a8,0x24000120,0,0xd100bc0,0x15100c40,0x24100cb8,0,0xd20045a,0x152003da,0x24200362,0,0xd300ff2,0x15300f72,0x24300efa,0,
0x1540002a,0x1d4000aa,0x24000120,0,0x15500bc2,0x1d500c42,0x24100cb8,0,0x15600458,0x1d6003d8,0x24200362,0,0x15700ff0,0x1d700f70,0x24300efa,0,
0x1d80002c,0x1a800194,0x24000120,0,0x1d900bc4,0x1a900d2c,0x24100cb8,0,0x1da00456,0x1aa002ee,0x24200362,0,0x1db00fee,0x1ab00e86,0x24300efa,0,
0x1a400212,0x12400192,0x24000120,0,0x1a500daa,0x12500d2a,0x24100cb8,0,0x1a600270,0x126002f0,0x24200362,0,0x1a700e08,0x12700e88,0x24300efa,0,
0x12000210,0xa000190,0x24000120,0,0x12100da8,0xa100d28,0x24100cb8,0,0x12200272,0xa2002f2,0x24200362,0,0x12300e0a,0xa300e8a,0x24300efa,0,
0x9c0020e,0x1c0018e,0x24000120,0,0x9d00da6,0x1d00d26,0x24100cb8,0,0x9e00274,0x1e002f4,0x24200362,0,0x9f00e0c,0x1f00e8c,0x24300efa,0,
0x180020c,0x2180018c,0x24000120,0,0x1900da4,0x21900d24,0x24100cb8,0,0x1a00276,0x21a002f6,0x24200362,0,0x1b00e0e,0x21b00e8e,0x24300efa,0,
0x2140020a,0x6c000b6,0x24000120,0,0x21500da2,0x6d00c4e,0x24100cb8,0,0x21600278,0x6e003cc,0x24200362,0,0x21700e10,0x6f00f64,0x24300efa,0,
0x7000038,0xf0000b8,0x24000120,0,0x7100bd0,0xf100c50,0x24100cb8,0,0x720044a,0xf2003ca,0x24200362,0,0x7300fe2,0xf300f62,0x24300efa,0,
0xf40003a,0x174000ba,0x24000120,0,0xf500bd2,0x17500c52,0x24100cb8,0,0xf600448,0x176003c8,0x24200362,0,0xf700fe0,0x17700f60,0x24300efa,0,
0x1780003c,0x1f8000bc,0x24000120,0,0x17900bd4,0x1f900c54,0x24100cb8,0,0x17a00446,0x1fa003c6,0x24200362,0,0x17b00fde,0x1fb00f5e,0x24300efa,0,
0x1fc0003e,0x18400182,0x24000120,0,0x1fd00bd6,0x18500d1a,0x24100cb8,0,0x1fe00444,0x18600300,0x24200362,0,0x1ff00fdc,0x18700e98,0x24300efa,0,
0x18000200,0x10000180,0x24000120,0,0x18100d98,0x10100d18,0x24100cb8,0,0x18200282,0x10200302,0x24200362,0,0x18300e1a,0x10300e9a,0x24300efa,0,
0x17c001fe,0xfc0017e,0x24000120,0,0x17d00d96,0xfd00d16,0x24100cb8,0,0x17e00284,0xfe00304,0x24200362,0,0x17f00e1c,0xff00e9c,0x24300efa,0,
0xf8001fc,0x780017c,0x24000120,0,0xf900d94,0x7900d14,0x24100cb8,0,0xfa00286,0x7a00306,0x24200362,0,0xfb00e1e,0x7b00e9e,0x24300efa,0,
0x74001fa,0x20c000c6,0x24000120,0,0x7500d92,0x20d00c5e,0x24100cb8,0,0x7600288,0x20e003bc,0x24200362,0,0x7700e20,0x20f00f54,0x24300efa,0,
0x21000048,0x10000c8,0x24000120,0,0x21100be0,0x1100c60,0x24100cb8,0,0x2120043a,0x12003ba,0x24200362,0,0x21300fd2,0x1300f52,0x24300efa,0,
0x140004a,0x94000ca,0x24000120,0,0x1500be2,0x9500c62,0x24100cb8,0,0x1600438,0x96003b8,0x24200362,0,0x1700fd0,0x9700f50,0x24300efa,0,
0x980004c,0x118000cc,0x24000120,0,0x9900be4,0x11900c64,0x24100cb8,0,0x9a00436,0x11a003b6,0x24200362,0,0x9b00fce,0x11b00f4e,0x24300efa,0,
0x11c0004e,0x19c000ce,0x24000120,0,0x11d00be6,0x19d00c66,0x24100cb8,0,0x11e00434,0x19e003b4,0x24200362,0,0x11f00fcc,0x19f00f4c,0x24300efa,0,
0x1a000050,0x1e000170,0x24000120,0,0x1a100be8,0x1e100d08,0x24100cb8,0,0x1a200432,0x1e200312,0x24200362,0,0x1a300fca,0x1e300eaa,0x24300efa,0,
0x1dc001ee,0x15c0016e,0x24000120,0,0x1dd00d86,0x15d00d06,0x24100cb8,0,0x1de00294,0x15e00314,0x24200362,0,0x1df00e2c,0x15f00eac,0x24300efa,0,
0x158001ec,0xd80016c,0x24000120,0,0x15900d84,0xd900d04,0x24100cb8,0,0x15a00296,0xda00316,0x24200362,0,0x15b00e2e,0xdb00eae,0x24300efa,0,
0xd4001ea,0x540016a,0x24000120,0,0xd500d82,0x5500d02,0x24100cb8,0,0xd600298,0x5600318,0x24200362,0,0xd700e30,0x5700eb0,0x24300efa,0,
0x50001e8,0x230000d8,0x24000120,0,0x5100d80,0x23100c70,0x24100cb8,0,0x520029a,0x232003aa,0x24200362,0,0x5300e32,0x23300f42,0x24300efa,0,
0x2340005a,0x34000da,0x24000120,0,0x23500bf2,0x3500c72,0x24100cb8,0,0x23600428,0x36003a8,0x24200362,0,0x23700fc0,0x3700f40,0x24300efa,0,
0x380005c,0xb8000dc,0x24000120,0,0x3900bf4,0xb900c74,0x24100cb8,0,0x3a00426,0xba003a6,0x24200362,0,0x3b00fbe,0xbb00f3e,0x24300efa,0,
0xbc0005e,0x13c000de,0x24000120,0,0xbd00bf6,0x13d00c76,0x24100cb8,0,0xbe00424,0x13e003a4,0x24200362,0,0xbf00fbc,0x13f00f3c,0x24300efa,0,
0x14000060,0x1c0000e0,0x24000120,0,0x14100bf8,0x1c100c78,0x24100cb8,0,0x14200422,0x1c2003a2,0x24200362,0,0x14300fba,0x1c300f3a,0x24300efa,0,
0x1c400062,0x1bc0015e,0x24000120,0,0x1c500bfa,0x1bd00cf6,0x24100cb8,0,0x1c600420,0x1be00324,0x24200362,0,0x1c700fb8,0x1bf00ebc,0x24300efa,0,
0x1b8001dc,0x1380015c,0x24000120,0,0x1b900d74,0x13900cf4,0x24100cb8,0,0x1ba002a6,0x13a00326,0x24200362,0,0x1bb00e3e,0x13b00ebe,0x24300efa,0,
0x134001da,0xb40015a,0x24000120,0,0x13500d72,0xb500cf2,0x24100cb8,0,0x136002a8,0xb600328,0x24200362,0,0x13700e40,0xb700ec0,0x24300efa,0,
0xb0001d8,0x3000158,0x24000120,0,0xb100d70,0x3100cf0,0x24100cb8,0,0xb2002aa,0x320032a,0x24200362,0,0xb300e42,0x3300ec2,0x24300efa,0,
0x2c001d6,0x22c00156,0x24000120,0,0x2d00d6e,0x22d00cee,0x24100cb8,0,0x2e002ac,0x22e0032c,0x24200362,0,0x2f00e44,0x22f00ec4,0x24300efa,0,
0x228001d4,0x58000ec,0x24000120,0,0x22900d6c,0x5900c84,0x24100cb8,0,0x22a002ae,0x5a00396,0x24200362,0,0x22b00e46,0x5b00f2e,0x24300efa,0,
0x5c0006e,0xdc000ee,0x24000120,0,0x5d00c06,0xdd00c86,0x24100cb8,0,0x5e00414,0xde00394,0x24200362,0,0x5f00fac,0xdf00f2c,0x24300efa,0,
0xe000070,0x160000f0,0x24000120,0,0xe100c08,0x16100c88,0x24100cb8,0,0xe200412,0x16200392,0x24200362,0,0xe300faa,0x16300f2a,0x24300efa,0,
0x16400072,0x1e4000f2,0x24000120,0,0x16500c0a,0x1e500c8a,0x24100cb8,0,0x16600410,0x1e600390,0x24200362,0,0x16700fa8,0x1e700f28,0x24300efa,0,
0x1e800074,0x1980014c,0x24000120,0,0x1e900c0c,0x19900ce4,0x24100cb8,0,0x1ea0040e,0x19a00336,0x24200362,0,0x1eb00fa6,0x19b00ece,0x24300efa,0,
0x194001ca,0x1140014a,0x24000120,0,0x19500d62,0x11500ce2,0x24100cb8,0,0x196002b8,0x11600338,0x24200362,0,0x19700e50,0x11700ed0,0x24300efa,0,
0x110001c8,0x9000148,0x24000120,0,0x11100d60,0x9100ce0,0x24100cb8,0,0x112002ba,0x920033a,0x24200362,0,0x11300e52,0x9300ed2,0x24300efa,0,
0x8c001c6,0xc00146,0x24000120,0,0x8d00d5e,0xd00cde,0x24100cb8,0,0x8e002bc,0xe0033c,0x24200362,0,0x8f00e54,0xf00ed4,0x24300efa,0,
8389060,0x20800144,0x24000120,0,9440604,0x20900cdc,0x24100cb8,0,0xa002be,0x20a0033e,0x24200362,0,0xb00e56,0x20b00ed6,0x24300efa,0,
0,512,1536,0,16,528,1552,16,32,544,1568,32,48,560,1584,48,
64,576,1600,64,80,592,1616,80,96,608,1632,96,112,624,1648,112,
128,640,1664,128,144,656,1680,144,160,672,1696,160,176,688,1712,176,
192,704,1728,192,208,720,1744,208,224,736,1760,224,240,752,1776,240,
1536,5120,6144,4608,1552,5136,6160,4624,1568,5152,6176,4640,1584,5168,6192,4656,
1600,5184,6208,4672,1616,5200,6224,4688,1632,5216,6240,4704,1648,5232,6256,4720,
1664,5248,6272,4736,1680,5264,6288,4752,1696,5280,6304,4768,1712,5296,6320,4784,
1728,5312,6336,4800,1744,5328,6352,4816,1760,5344,6368,4832,1776,5360,6384,4848,
3072,4096,3072,0,3088,4592,3568,496,3104,4576,3552,480,3120,4560,3536,464,
3136,4544,3520,448,3152,4528,3504,432,3168,4512,3488,416,3184,4496,3472,400,
3200,4480,3456,384,3216,4464,3440,368,3232,4448,3424,352,3248,4432,3408,336,
3264,4416,3392,320,3280,4400,3376,304,3296,4384,3360,288,3312,4368,3344,272,
3072,8704,7680,4608,3088,9200,8176,5104,3104,9184,8160,5088,3120,9168,8144,5072,
3136,9152,8128,5056,3152,9136,8112,5040,3168,9120,8096,5024,3184,9104,8080,5008,
3200,9088,8064,4992,3216,9072,8048,4976,3232,9056,8032,4960,3248,9040,8016,4944,
3264,9024,8000,4928,3280,9008,7984,4912,3296,8992,7968,4896,3312,8976,7952,4880,
8,768,1792,256,24,784,1808,272,40,800,1824,288,56,816,1840,304,
72,832,1856,320,88,848,1872,336,104,864,1888,352,120,880,1904,368,
136,896,1920,384,152,912,1936,400,168,928,1952,416,184,944,1968,432,
200,960,1984,448,216,976,2000,464,232,992,2016,480,248,1008,2032,496,
1544,5376,6400,4864,1560,5392,6416,4880,1576,5408,6432,4896,1592,5424,6448,4912,
1608,5440,6464,4928,1624,5456,6480,4944,1640,5472,6496,4960,1656,5488,6512,4976,
1672,5504,6528,4992,1688,5520,6544,5008,1704,5536,6560,5024,1720,5552,6576,5040,
1736,5568,6592,5056,1752,5584,6608,5072,1768,5600,6624,5088,1784,5616,6640,5104,
3080,4352,3328,256,3096,4336,3312,240,3112,4320,3296,224,3128,4304,3280,208,
3144,4288,3264,192,3160,4272,3248,176,3176,4256,3232,160,3192,4240,3216,144,
3208,4224,3200,128,3224,4208,3184,112,3240,4192,3168,96,3256,4176,3152,80,
3272,4160,3136,64,3288,4144,3120,48,3304,4128,3104,32,3320,4112,3088,16,
3080,8960,7936,4864,3096,8944,7920,4848,3112,8928,7904,4832,3128,8912,7888,4816,
3144,8896,7872,4800,3160,8880,7856,4784,3176,8864,7840,4768,3192,8848,7824,4752,
3208,8832,7808,4736,3224,8816,7792,4720,3240,8800,7776,4704,3256,8784,7760,4688,
3272,8768,7744,4672,3288,8752,7728,4656,3304,8736,7712,4640,3320,8720,7696,4624,
4608,1024,2048,0,4624,1040,2064,16,4640,1056,2080,32,4656,1072,2096,48,
4672,1088,2112,64,4688,1104,2128,80,4704,1120,2144,96,4720,1136,2160,112,
4736,1152,2176,128,4752,1168,2192,144,4768,1184,2208,160,4784,1200,2224,176,
4800,1216,2240,192,4816,1232,2256,208,4832,1248,2272,224,4848,1264,2288,240,
6144,5632,6656,4608,6160,5648,6672,4624,6176,5664,6688,4640,6192,5680,6704,4656,
6208,5696,6720,4672,6224,5712,6736,4688,6240,5728,6752,4704,6256,5744,6768,4720,
6272,5760,6784,4736,6288,5776,6800,4752,6304,5792,6816,4768,6320,5808,6832,4784,
6336,5824,6848,4800,6352,5840,6864,4816,6368,5856,6880,4832,6384,5872,6896,4848,
7680,3584,2560,0,7696,4080,3056,496,7712,4064,3040,480,7728,4048,3024,464,
7744,4032,3008,448,7760,4016,2992,432,7776,4000,2976,416,7792,3984,2960,400,
7808,3968,2944,384,7824,3952,2928,368,7840,3936,2912,352,7856,3920,2896,336,
7872,3904,2880,320,7888,3888,2864,304,7904,3872,2848,288,7920,3856,2832,272,
7680,8192,7168,4608,7696,8688,7664,5104,7712,8672,7648,5088,7728,8656,7632,5072,
7744,8640,7616,5056,7760,8624,7600,5040,7776,8608,7584,5024,7792,8592,7568,5008,
7808,8576,7552,4992,7824,8560,7536,4976,7840,8544,7520,4960,7856,8528,7504,4944,
7872,8512,7488,4928,7888,8496,7472,4912,7904,8480,7456,4896,7920,8464,7440,4880,
4616,1280,2304,256,4632,1296,2320,272,4648,1312,2336,288,4664,1328,2352,304,
4680,1344,2368,320,4696,1360,2384,336,4712,1376,2400,352,4728,1392,2416,368,
4744,1408,2432,384,4760,1424,2448,400,4776,1440,2464,416,4792,1456,2480,432,
4808,1472,2496,448,4824,1488,2512,464,4840,1504,2528,480,4856,1520,2544,496,
6152,5888,6912,4864,6168,5904,6928,4880,6184,5920,6944,4896,6200,5936,6960,4912,
6216,5952,6976,4928,6232,5968,6992,4944,6248,5984,7008,4960,6264,6000,7024,4976,
6280,6016,7040,4992,6296,6032,7056,5008,6312,6048,7072,5024,6328,6064,7088,5040,
6344,6080,7104,5056,6360,6096,7120,5072,6376,6112,7136,5088,6392,6128,7152,5104,
7688,3840,2816,256,7704,3824,2800,240,7720,3808,2784,224,7736,3792,2768,208,
7752,3776,2752,192,7768,3760,2736,176,7784,3744,2720,160,7800,3728,2704,144,
7816,3712,2688,128,7832,3696,2672,112,7848,3680,2656,96,7864,3664,2640,80,
7880,3648,2624,64,7896,3632,2608,48,7912,3616,2592,32,7928,3600,2576,16,
7688,8448,7424,4864,7704,8432,7408,4848,7720,8416,7392,4832,7736,8400,7376,4816,
7752,8384,7360,4800,7768,8368,7344,4784,7784,8352,7328,4768,7800,8336,7312,4752,
7816,8320,7296,4736,7832,8304,7280,4720,7848,8288,7264,4704,7864,8272,7248,4688,
7880,8256,7232,4672,7896,8240,7216,4656,7912,8224,7200,4640,7928,8208,7184,4624,
0,0,16384,29640,16,64,16448,29576,32,128,16512,29512,48,192,16576,29448,
64,272,16656,29400,80,336,16720,29336,96,400,16784,29272,112,464,16848,29208,
128,544,16928,29160,144,608,16992,29096,160,672,17056,29032,176,736,17120,28968,
192,816,17200,28920,208,880,17264,28856,224,944,17328,28792,240,1008,17392,28728,
272,4096,20480,25544,256,4160,20544,25480,304,4224,20608,25416,288,4288,20672,25352,
336,4368,20752,25304,320,4432,20816,25240,368,4496,20880,25176,352,4560,20944,25112,
400,4640,21024,25064,384,4704,21088,25000,432,4768,21152,24936,416,4832,21216,24872,
464,4912,21296,24824,448,4976,21360,24760,496,5040,21424,24696,480,5104,21488,24632,
544,8192,24576,21448,560,8256,24640,21384,512,8320,24704,21320,528,8384,24768,21256,
608,8464,24848,21208,624,8528,24912,21144,576,8592,24976,21080,592,8656,25040,21016,
672,8736,25120,20968,688,8800,25184,20904,640,8864,25248,20840,656,8928,25312,20776,
736,9008,25392,20728,752,9072,25456,20664,704,9136,25520,20600,720,9200,25584,20536,
816,12288,28672,17352,800,12352,28736,17288,784,12416,28800,17224,768,12480,28864,17160,
880,12560,28944,17112,864,12624,29008,17048,848,12688,29072,16984,832,12752,29136,16920,
944,12832,29216,16872,928,12896,29280,16808,912,12960,29344,16744,896,13024,29408,16680,
1008,13104,29488,16632,992,13168,29552,16568,976,13232,29616,16504,960,13296,29680,16440,
1024,16,16392,29632,1040,80,16456,29568,1056,144,16520,29504,1072,208,16584,29440,
1088,256,16664,29392,1104,320,16728,29328,1120,384,16792,29264,1136,448,16856,29200,
1152,560,16936,29152,1168,624,17000,29088,1184,688,17064,29024,1200,752,17128,28960,
1216,800,17208,28912,1232,864,17272,28848,1248,928,17336,28784,1264,992,17400,28720,
1296,4112,20488,25536,1280,4176,20552,25472,1328,4240,20616,25408,1312,4304,20680,25344,
1360,4352,20760,25296,1344,4416,20824,25232,1392,4480,20888,25168,1376,4544,20952,25104,
1424,4656,21032,25056,1408,4720,21096,24992,1456,4784,21160,24928,1440,4848,21224,24864,
1488,4896,21304,24816,1472,4960,21368,24752,1520,5024,21432,24688,1504,5088,21496,24624,
1568,8208,24584,21440,1584,8272,24648,21376,1536,8336,24712,21312,1552,8400,24776,21248,
1632,8448,24856,21200,1648,8512,24920,21136,1600,8576,24984,21072,1616,8640,25048,21008,
1696,8752,25128,20960,1712,8816,25192,20896,1664,8880,25256,20832,1680,8944,25320,20768,
1760,8992,25400,20720,1776,9056,25464,20656,1728,9120,25528,20592,1744,9184,25592,20528,
1840,12304,28680,17344,1824,12368,28744,17280,1808,12432,28808,17216,1792,12496,28872,17152,
1904,12544,28952,17104,1888,12608,29016,17040,1872,12672,29080,16976,1856,12736,29144,16912,
1968,12848,29224,16864,1952,12912,29288,16800,1936,12976,29352,16736,1920,13040,29416,16672,
2032,13088,29496,16624,2016,13152,29560,16560,2000,13216,29624,16496,1984,13280,29688,16432,
2048,32,16400,29656,2064,96,16464,29592,2080,160,16528,29528,2096,224,16592,29464,
2112,304,16640,29384,2128,368,16704,29320,2144,432,16768,29256,2160,496,16832,29192,
2176,512,16944,29176,2192,576,17008,29112,2208,640,17072,29048,2224,704,17136,28984,
2240,784,17184,28904,2256,848,17248,28840,2272,912,17312,28776,2288,976,17376,28712,
2320,4128,20496,25560,2304,4192,20560,25496,2352,4256,20624,25432,2336,4320,20688,25368,
2384,4400,20736,25288,2368,4464,20800,25224,2416,4528,20864,25160,2400,4592,20928,25096,
2448,4608,21040,25080,2432,4672,21104,25016,2480,4736,21168,24952,2464,4800,21232,24888,
2512,4880,21280,24808,2496,4944,21344,24744,2544,5008,21408,24680,2528,5072,21472,24616,
2592,8224,24592,21464,2608,8288,24656,21400,2560,8352,24720,21336,2576,8416,24784,21272,
2656,8496,24832,21192,2672,8560,24896,21128,2624,8624,24960,21064,2640,8688,25024,21000,
2720,8704,25136,20984,2736,8768,25200,20920,2688,8832,25264,20856,2704,8896,25328,20792,
2784,8976,25376,20712,2800,9040,25440,20648,2752,9104,25504,20584,2768,9168,25568,20520,
2864,12320,28688,17368,2848,12384,28752,17304,2832,12448,28816,17240,2816,12512,28880,17176,
2928,12592,28928,17096,2912,12656,28992,17032,2896,12720,29056,16968,2880,12784,29120,16904,
2992,12800,29232,16888,2976,12864,29296,16824,2960,12928,29360,16760,2944,12992,29424,16696,
3056,13072,29472,16616,3040,13136,29536,16552,3024,13200,29600,16488,3008,13264,29664,16424,
3072,48,16408,29648,3088,112,16472,29584,3104,176,16536,29520,3120,240,16600,29456,
3136,288,16648,29376,3152,352,16712,29312,3168,416,16776,29248,3184,480,16840,29184,
3200,528,16952,29168,3216,592,17016,29104,3232,656,17080,29040,3248,720,17144,28976,
3264,768,17192,28896,3280,832,17256,28832,3296,896,17320,28768,3312,960,17384,28704,
3344,4144,20504,25552,3328,4208,20568,25488,3376,4272,20632,25424,3360,4336,20696,25360,
3408,4384,20744,25280,3392,4448,20808,25216,3440,4512,20872,25152,3424,4576,20936,25088,
3472,4624,21048,25072,3456,4688,21112,25008,3504,4752,21176,24944,3488,4816,21240,24880,
3536,4864,21288,24800,3520,4928,21352,24736,3568,4992,21416,24672,3552,5056,21480,24608,
3616,8240,24600,21456,3632,8304,24664,21392,3584,8368,24728,21328,3600,8432,24792,21264,
3680,8480,24840,21184,3696,8544,24904,21120,3648,8608,24968,21056,3664,8672,25032,20992,
3744,8720,25144,20976,3760,8784,25208,20912,3712,8848,25272,20848,3728,8912,25336,20784,
3808,8960,25384,20704,3824,9024,25448,20640,3776,9088,25512,20576,3792,9152,25576,20512,
3888,12336,28696,17360,3872,12400,28760,17296,3856,12464,28824,17232,3840,12528,28888,17168,
3952,12576,28936,17088,3936,12640,29000,17024,3920,12704,29064,16960,3904,12768,29128,16896,
4016,12816,29240,16880,4000,12880,29304,16816,3984,12944,29368,16752,3968,13008,29432,16688,
4080,13056,29480,16608,4064,13120,29544,16544,4048,13184,29608,16480,4032,13248,29672,16416,
4718592,0xd80090,0x1780130,0x20801c0,0x1580110,0x1e801a0,0x2880240,0x31802d0,0x2680220,0x2f802b0,0x3980350,0x42803e0,0x3780330,0x40803c0,0x4a80460,0x53804f0,
0x3780330,0x40803c0,0x4a80460,0x53804f0,0x4880440,0x51804d0,0x5b80570,0x6480600,0x5980550,0x62805e0,0x6c80680,0x7580710,0x6a80660,0x73806f0,0x7d80790,0x8680820,
0x6a80660,0x73806f0,0x7d80790,0x8680820,0x7b80770,0x8480800,0x8e808a0,0x9780930,0x8c80880,0x9580910,6815776,0xf800b0,0x8c80880,0x9580910,6815776,0xf800b0,
0x8c80880,0x9580910,6815776,0xf800b0,0x8c80880,0x9580910,6815776,0xf800b0,0x8c80880,0x9580910,6815776,0xf800b0,0x8c80880,0x9580910,6815776,0xf800b0,
0x2a80260,0x34802f0,0x3d80390,0x4780420,0x3b80370,0x4580400,0x4e804a0,0x5880530,0x4c80480,0x5680510,0x5f805b0,0x6980640,0x5d80590,0x6780620,0x70806c0,0x7a80750,
0x5d80590,0x6780620,0x70806c0,0x7a80750,0x6e806a0,0x7880730,0x81807d0,0x8b80860,0x7f807b0,0x8980840,0x92808e0,3672432,0x90808c0,1575248,0xa80060,0x14800f0,
0x90808c0,1575248,0xa80060,0x14800f0,8912960,0x12800d0,0x1b80170,0x2580200,0x1980150,0x23801e0,0x2c80280,0x3680310,0x1980150,0x23801e0,0x2c80280,0x3680310,
0x1980150,0x23801e0,0x2c80280,0x3680310,0x1980150,0x23801e0,0x2c80280,0x3680310,0x1980150,0x23801e0,0x2c80280,0x3680310,0x1980150,0x23801e0,0x2c80280,0x3680310,
0x50804c0,0x5a80560,0x63805f0,0x6d80690,0x61805d0,0x6b80670,0x7480700,0x7e807a0,0x72806e0,0x7c80780,0x8580810,0x8f808b0,0x83807f0,0x8d80890,0x9680920,7864368,
0x83807f0,0x8d80890,0x9680920,7864368,0x9480900,5767184,0xe800a0,0x1880140,0xc80080,0x1680120,0x1f801b0,0x2980250,0x1d80190,0x2780230,0x30802c0,0x3a80360,
0x1d80190,0x2780230,0x30802c0,0x3a80360,0x2e802a0,0x3880340,0x41803d0,0x4b80470,0x3f803b0,0x4980450,0x52804e0,0x5c80580,0x3f803b0,0x4980450,0x52804e0,0x5c80580,
0x3f803b0,0x4980450,0x52804e0,0x5c80580,0x3f803b0,0x4980450,0x52804e0,0x5c80580,0x3f803b0,0x4980450,0x52804e0,0x5c80580,0x3f803b0,0x4980450,0x52804e0,0x5c80580,
0x7780720,0x80807c0,0x8a80850,0x93808f0,0x8880830,0x91808d0,2623840,0xb80070,526656,9961552,0x13800e0,0x1c80180,0x11800c0,0x1a80160,0x24801f0,0x2d80290,
0x11800c0,0x1a80160,0x24801f0,0x2d80290,0x22801d0,0x2b80270,0x3580300,0x3e803a0,0x33802e0,0x3c80380,0x4680410,0x4f804b0,0x44803f0,0x4d80490,0x5780520,0x60805c0,
0x44803f0,0x4d80490,0x5780520,0x60805c0,0x5580500,0x5e805a0,0x6880630,0x71806d0,0x6680610,0x6f806b0,0x7980740,0x82807e0,0x6680610,0x6f806b0,0x7980740,0x82807e0,
0x6680610,0x6f806b0,0x7980740,0x82807e0,0x6680610,0x6f806b0,0x7980740,0x82807e0,0x6680610,0x6f806b0,0x7980740,0x82807e0,0x6680610,0x6f806b0,0x7980740,0x82807e0,
2359296,7077960,0xc400a0,0x10c00e8,0xb40090,0xfc00d8,0x1540130,0x19c0178,0x1440120,0x18c0168,0x1e401c0,0x22c0208,0x1d401b0,0x21c01f8,0x2740250,0x2bc0298,
0x1d401b0,0x21c01f8,0x2740250,0x2bc0298,0x2640240,0x2ac0288,0x30402e0,0x34c0328,0x2f402d0,0x33c0318,0x3940370,0x3dc03b8,0x3840360,0x3cc03a8,0x4240400,0x46c0448,
0x3840360,0x3cc03a8,0x4240400,0x46c0448,0x41403f0,0x45c0438,0x4b40490,0x4fc04d8,0x4a40480,0x4ec04c8,3407888,8126552,0x4a40480,0x4ec04c8,3407888,8126552,
0x4a40480,0x4ec04c8,3407888,8126552,0x4a40480,0x4ec04c8,3407888,8126552,0x4a40480,0x4ec04c8,3407888,8126552,0x4a40480,0x4ec04c8,3407888,8126552,
0x1640140,0x1bc0188,0x20401e0,0x25c0228,0x1f401d0,0x24c0218,0x2940270,0x2ec02b8,0x2840260,0x2dc02a8,0x3240300,0x37c0348,0x31402f0,0x36c0338,0x3b40390,0x40c03d8,
0x31402f0,0x36c0338,0x3b40390,0x40c03d8,0x3a40380,0x3fc03c8,0x4440420,0x49c0468,0x4340410,0x48c0458,0x4d404b0,1836280,0x4c404a0,787688,5505072,0xac0078,
0x4c404a0,787688,5505072,0xac0078,4456480,0x9c0068,0xe400c0,0x13c0108,0xd400b0,0x12c00f8,0x1740150,0x1cc0198,0xd400b0,0x12c00f8,0x1740150,0x1cc0198,
0xd400b0,0x12c00f8,0x1740150,0x1cc0198,0xd400b0,0x12c00f8,0x1740150,0x1cc0198,0xd400b0,0x12c00f8,0x1740150,0x1cc0198,0xd400b0,0x12c00f8,0x1740150,0x1cc0198,
0x2a40280,0x2fc02d8,0x3440320,0x39c0378,0x3340310,0x38c0368,0x3d403b0,0x42c0408,0x3c403a0,0x41c03f8,0x4640440,0x4bc0498,0x4540430,0x4ac0488,0x4f404d0,3932184,
0x4540430,0x4ac0488,0x4f404d0,3932184,0x4e404c0,2883592,7602256,0xcc00a8,6553664,0xbc0098,0x10400e0,0x15c0138,0xf400d0,0x14c0128,0x1940170,0x1ec01c8,
0xf400d0,0x14c0128,0x1940170,0x1ec01c8,0x1840160,0x1dc01b8,0x2240200,0x27c0258,0x21401f0,0x26c0248,0x2b40290,0x30c02e8,0x21401f0,0x26c0248,0x2b40290,0x30c02e8,
0x21401f0,0x26c0248,0x2b40290,0x30c02e8,0x21401f0,0x26c0248,0x2b40290,0x30c02e8,0x21401f0,0x26c0248,0x2b40290,0x30c02e8,0x21401f0,0x26c0248,0x2b40290,0x30c02e8,
0x3f403c0,0x43c0418,0x4940460,0x4dc04b8,0x4840450,0x4cc04a8,1311984,6029368,263392,4980776,0xa40070,0xec00c8,9699424,0xdc00b8,0x1340100,0x17c0158,
9699424,0xdc00b8,0x1340100,0x17c0158,0x12400f0,0x16c0148,0x1c40190,0x20c01e8,0x1b40180,0x1fc01d8,0x2540220,0x29c0278,0x2440210,0x28c0268,0x2e402b0,0x32c0308,
0x2440210,0x28c0268,0x2e402b0,0x32c0308,0x2d402a0,0x31c02f8,0x3740340,0x3bc0398,0x3640330,0x3ac0388,0x40403d0,0x44c0428,0x3640330,0x3ac0388,0x40403d0,0x44c0428,
0x3640330,0x3ac0388,0x40403d0,0x44c0428,0x3640330,0x3ac0388,0x40403d0,0x44c0428,0x3640330,0x3ac0388,0x40403d0,0x44c0428,0x3640330,0x3ac0388,0x40403d0,0x44c0428,
0x3eb6e48b,0x3fd246dd,0x3f441b7d,0,0x3f7d3521,0x3f816570,0x3f7ffc1a,0,0x3ebf86f5,0x3fd01e43,0xbf47a682,0,0x3caf516f,0x3ffd42ba,0xbe52b743,0,
0x3de766dd,0x3ff18992,0x3eec6a50,0,0x3f1b757f,0x3fb24541,0x3f6b6e32,0,0x3f385d20,0x3fa3d170,0xbf75c5d4,0,0x3e32d4d8,0x3fe9a565,0xbf108a14,0,
0x3b79625f,0x3fff834f,0x3db27eb6,0,0x3e9208a2,0x3fdb7dd8,0x3f3308b4,0,0x3f64214d,0x3f8def5a,0x3f7e7a7a,0,0x3ee8356c,0x3fc5f2a5,0xbf56651d,0,
0x3d3d934b,0x3ffa1366,0xbe99f61c,0,0x3d93754e,0x3ff6c8ab,0x3ebec330,0,0x3f04ddec,0x3fbd910a,0x3f60712a,0,0x3f50cc75,0x3f9799c5,0xbf7b9c62,0,
0x3e6f920c,0x3fe20dbf,0xbf248dbb,0,0x38798a45,0x3ffffe0d,0xbc32b7da,0,0x3e6165f6,0x3fe3d341,0x3f203c85,0,0x3f4b522f,0x3f9a56e8,0x3f7a8575,0,
0x3f09cad8,0x3fbb1a94,0xbf631324,0,0x3da48e6d,0x3ff5b719,0xbec91503,0,0x3d23a2c4,0x3ffae2ea,0x3e8f45c0,0,0x3edeebd7,0x3fc8450a,0x3f534aca,0,
0x3f69b029,0x3f8b27eb,0xbf7f069e,0,0x3e99ee99,0x3fd9845a,0xbf36fbaf,0,0x3bc2c30c,0x3fff3d3d,0xbddef599,0,0x3e266b8d,0x3feb328e,0x3f0be54a,0,
0x3f3304f2,0x3fa67d87,0x3f7426cb,0,0x3f209e68,0x3fafb0cc,0xbf6d9156,0,0x3dfc76af,0x3ff03895,0xbef64429,0,0x3c8c73b0,0x3ffdce31,0x3e3cce2b,0,
0x3c78e8e7,0x3ffe0e2e,0x3e31d0d4,0,0x3eb2a24e,0x3fd3576c,0x3f424cfe,0,0x3f7a6a57,0x3f82cad5,0x3f7ff067,0,0x3ec3e6e0,0x3fcf0648,0xbf4962ed,0,
0x3cc22ee0,0x3ffcf744,0xbe5da259,0,0x3ddd31cd,0x3ff22ce3,0x3ee77284,0,0x3f18e598,0x3fb38d34,0x3f6a51da,0,0x3f3b0c88,0x3fa279bc,0xbf768a20,0,
0x3e393035,0x3fe8d9f9,0xbf12d5e8,0,0x3b3ef696,0x3fffa085,0x3d9c3ac6,0,0x3e8e2650,0x3fdc766c,0x3f310700,0,0x3f615b1a,0x3f8f5273,0x3f7e28c6,0,
0x3eece711,0x3fc4c63c,0xbf57e880,0,0x3d4b3e41,0x3ff9a60e,0xbe9f4757,0,0x3d8b3f75,0x3ff74c09,0x3eb9917b,0,0x3f026d0c,0x3fbec97a,0x3f5f15e7,0,
0x3f538bcb,0x3f963a1a,0xbf7c1c5c,0,0x3e76cc0a,0x3fe1267f,0xbf26aed9,0,0x3979885f,0x3ffff834,0xbcb2b521,0,0x3e5a744e,0x3fe4b176,0x3f1e0c90,0,
0x3f48976a,0x3f9bb44b,0x3f79ee89,0,0x3f0c46be,0x3fb9dca1,0xbf6459c6,0,0x3dad712d,0x3ff528ed,0xbece34d0,0,0x3d175dfc,0x3ffb4510,0x3e89e6f1,0,
0x3eda5430,0x3fc96af4,0x3f51b3f3,0,0x3f6c78a7,0x3f89c3ac,0xbf7f4109,0,0x3e9df200,0x3fd88380,0xbf38ecd8,0,0x3beb9ce3,0x3fff1463,0xbdf52731,0,
0x3e205dfe,0x3febf440,0x3f098c78,0,0x3f305c55,0x3fa7d1d6,0x3f734c1c,0,0x3f233743,0x3fae645f,0xbf6e9811,0,0x3e03a866,0x3fef8af3,0xbefb25e8,0,
0x3e0930a3,0x3feed9ec,0xbf000000,0,0x3c5ad5ed,0x3ffe4a54,0x3e26ce12,0,0x3eae6a3a,0x3fd46572,0x3f407893,0,0x3f779fb9,0x3f843024,0x3f7fdce9,0,
0x3ec8506c,0x3fcdebe5,0xbf4b1934,0,0x3cd5fff2,0x3ffca800,0xbe6886ae,0,0x3dd33462,0x3ff2ccba,0x3ee273aa,0,0x3f1658d5,0x3fb4d395,0x3f692e5f,0,
0x3f3dbe09,0x3fa120fc,0xbf7746ea,0,0x3e3fa51e,0x3fe80b5c,0xbf151d42,0,0x3b0c514f,0x3fffb9d7,0x3d85f213,0,0x3e8a4f42,0x3fdd6c30,0x3f2effe8,0,
0x3f5e95d6,0x3f90b515,0x3f7dcf55,0,0x3ef1a117,0x3fc397ba,0xbf596550,0,0x3d595fcb,0x3ff93502,0xbea493b7,0,0x3d8343c1,0x3ff7cbc4,0x3eb45a1e,0,
0x3f000000,0x3fc00000,0x3f5db3d7,0,0x3f564c7b,0x3f94d9c2,0xbf7c94a8,0,0x3e7e1db5,0x3fe03c49,0xbf28cae3,0,0x3a0c5aed,0x3fffee75,0xbd060472,0,
0x3e539b2f,0x3fe58c9a,0x3f1bd7ca,0,0x3f45de54,0x3f9d10d6,0x3f795000,0,0x3f0ec62b,0x3fb89cea,0xbf659974,0,0x3db68d07,0x3ff49730,0xbed34e56,0,
0x3d0b915c,0x3ffba375,0x3e8483ee,0,0x3ed5c57c,0x3fca8ea1,0x3f5016b8,0,0x3f6f41be,0x3f885f21,0xbf7f73af,0,0x3ea20031,0x3fd77ff4,0xbf3ad85f,0,
0x3c0c2ada,0x3ffee7aa,0xbe05a8a8,0,0x3e1a6abe,0x3fecb2a8,0x3f072f74,0,0x3f2db625,0x3fa924ee,0x3f726a03,0,0x3f25d2f1,0x3fad1688,0xbf6f9788,0,
0x3f28715e,0x3fabc751,0xbf708fb2,0,0x3e0ed3e4,0x3fee2584,0xbf026926,0,0x3c3eaf5d,0x3ffe82a1,0x3e1bc63c,0,0x3eaa3c6d,0x3fd570e5,0x3f3e9e4b,0,
0x3f74d55c,0x3f859552,0x3f7fc1a0,0,0x3eccc376,0x3fcccf22,0xbf4cc94c,0,0x3ceac409,0x3ffc54f0,0xbe7363ee,0,0x3dc96eeb,0x3ff36911,0x3edd6dea,0,
0x3f13cf4a,0x3fb6185b,0x3f6803ca,0,0x3f40718f,0x3f9fc739,0xbf77fc2c,0,0x3e463363,0x3fe73994,0xbf176012,0,0x3ac2e826,0x3fffcf46,0x3d5f4a99,0,
0x3e868396,0x3fde5f1b,0x3f2cf37b,0,0x3f5bd196,0x3f921735,0x3f7d6e28,0,0x3ef6635a,0x3fc2672a,0xbf5adb81,0,0x3d67f77b,0x3ff8c044,0xbea9db13,0,
0x3d7704df,0x3ff847d9,0x3eaf1d44,0,0x3efb2db4,0x3fc13493,0x3f5c4b07,0,0x3f590e71,0x3f9378c7,0xbf7d0543,0,0x3e82c36a,0x3fdf4f26,0xbf2ae1c9,0,
0x3a7980c5,0x3fffe0d0,0xbd32aa3e,0,0x3e4cdacf,0x3fe664a6,0x3f199e45,0,0x3f432704,0x3f9e6c7e,0x3f78a9e0,0,0x3f11490b,0x3fb75b7b,0xbf66d223,0,
0x3dbfe1b4,0x3ff401e5,0xbed8616c,0,0x3d003d42,0x3ffbfe16,0x3e7e39c4,0,0x3ed13fdd,0x3fcbb009,0x3f4e7327,0,0x3f720b56,0x3f86fa55,0xbf7f9e8c,0,
0x3ea6190a,0x3fd679bd,0xbf3cbe35,0,0x3c247613,0x3ffeb714,0xbe10b9a6,0,0x3e1491fc,0x3fed6dc1,0x3f04ce53,0,0x3f2b1276,0x3faa76c5,0x3f718088,0,
0x3f800000,0x3f800000,0x3f800000,0,0x3ebb30ce,0x3fd133cd,0xbf45e403,0,0x3c9d6830,0x3ffd8a5f,0xbe47c5c2,0,0x3df1d344,0x3ff0e2cc,0x3ef15aea,0,
0x3f1e0875,0x3fb0fbc5,0x3f6c835e,0,0x3f35afe7,0x3fa5280c,0xbf74fa0b,0,0x3e2c933b,0x3fea6d99,0xbf0e39da,0,0x3b9dc971,0x3fff6237,0x3dc8bd36,0,
0x3e95f61a,0x3fda827a,0x3f3504f3,0,0x3f66e859,0x3f8c8bd3,0x3f7ec46d,0,0x3ee38c4c,0x3fc71ced,0xbf54db31,0,0x3d305f55,0x3ffa7d05,0xbe94a031,0,
0x3d9be50c,0x3ff641af,0x3ec3ef15,0,0x3f07528b,0x3fbc56ba,0x3f61c598,0,0x3f4e0e90,0x3f98f8b8,0xbf7b14be,0,0x3e686ff3,0x3fe2f202,0xbf226799,0,
0,0x40000000,0x248d3132,0,0x3f800000,0x3f800000,0x3f800000,0,0x3f800000,0x3f800000,0x3f800000,0,0x3f800000,0x3f800000,0x3f800000,0,
};
#define ASM_FRONT \
  "s_cmp_gt_u32 s2, 110\n" \
  "s_cbranch_scc1 .Lcol_%=\n" \
  "s_load_dwordx2 s[36:37], s[0:1], 48\n" \
  "s_load_dwordx2 s[60:61], s[0:1], 32\n" \
  "s_load_dwordx2 s[64:65], s[0:1], 40\n" \
  "s_load_dwordx2 s[48:49], s[0:1], 0\n" \
  "v_lshrrev_b32 v255, 6, v0\n" \
  "s_getpc_b64 s[80:81]\n" \
  ".Lpcn_%=:\n" \
  "v_readfirstlane_b32 s20, v255\n" \
  "s_mov_b32 s82, .Ladm_b-.Lpcn_%=\n" \
  "s_mov_b32 s83, 131072\n" \
  "s_mov_b32 m0, 162176\n" \
  "s_lshr_b32 s98, s2, 3\n" \
  "s_mul_i32 s98, s98, 20\n" \
  "v_add_u32 v254, s98, v0\n" \
  "v_lshlrev_b32 v253, 7, v254\n" \
  "s_cmp_eq_u32 s20, 0\n" \
  "s_cselect_b64 exec, 1048575, 0\n" \
  "buffer_load_dword v253, s[80:83], 0 offen lds\n" \
  "s_mov_b64 exec, -1\n" \
  "s_mov_b32 s38, 134217728\n" \
  "s_mov_b32 s39, 131072\n" \
  "s_mov_b32 s62, 1152\n" \
  "s_mov_b32 s63, 131072\n" \
  "s_mov_b32 s66, 3981312\n" \
  "s_mov_b32 s67, 131072\n" \
  "s_mov_b32 s50, 2359296\n" \
  "s_mov_b32 s51, 131072\n" \
  "s_mov_b32 s78, 26944\n" \
  "s_mov_b32 s79, 131072\n" \
  "v_and_b32 v191, 63, v0\n" \
  "v_and_b32 v254, 31, v191\n" \
  "v_lshrrev_b32 v253, 5, v191\n" \
  "v_mul_u32_u24 v252, 1179648, v253\n" \
  "v_lshl_add_u32 v251, v254, 4, v252\n" \
  "v_lshl_add_u32 v250, v254, 2, v252\n" \
  "v_lshlrev_b32 v252, 4, v191\n" \
  "s_and_b32 s84, s2, 7\n" \
  "s_lshr_b32 s85, s2, 3\n" \
  "s_lshl_b32 s98, s84, 5\n" \
  "s_add_u32 s86, s98, s85\n" \
  "s_lshl_b32 s98, s20, 8\n" \
  "s_add_u32 s98, s98, s86\n" \
  "s_mul_i32 s87, s98, 1152\n" \
  "s_mul_i32 s98, s2, 57\n" \
  "s_lshr_b32 s22, s98, 11\n" \
  "s_mul_i32 s98, s22, 36\n" \
  "s_sub_u32 s23, s2, s98\n" \
  "v_lshlrev_b32 v249, 3, v0\n" \
  "v_lshrrev_b32 v248, 2, v0\n" \
  "v_and_b32 v247, 3, v0\n" \
  "v_mul_u32_u24 v246, 6912, v248\n" \
  "v_lshl_add_u32 v245, v247, 4, v246\n" \
  "s_mul_i32 s98, s22, 1327104\n" \
  "s_lshl_b32 s100, s23, 6\n" \
  "s_add_u32 s90, s98, s100\n" \
  "s_cmp_lt_u32 s2, 108\n" \
  "s_cselect_b32 s66, s66, 0\n" \
  "s_add_u32 s32, s90, 442368\n" \
  "s_add_u32 s33, s90, 884736\n" \
  "s_waitcnt lgkmcnt(0)\n" \
  "buffer_load_dwordx2 v[2:3], off, s[36:39], 0 sc1\n" \
  "buffer_load_dwordx2 v[4:5], v249, s[60:63], 0 offen offset:0\n" \
  "buffer_load_dwordx4 v[6:9], v245, s[64:67], s90 offen offset:0\n" \
  "buffer_load_dwordx4 v[10:13], v245, s[64:67], s32 offen offset:0\n" \
  "buffer_load_dwordx4 v[14:17], v245, s[64:67], s33 offen offset:0\n" \
  "buffer_load_dwordx4 v[18:21], v251, s[48:51], s87 offen offset:0\n" \
  "buffer_load_dwordx4 v[22:25], v251, s[48:51], s87 offen offset:512\n" \
  "buffer_load_dword v247, v250, s[48:51], s87 offen offset:1024\n" \
  "s_movk_i32 s32, 22528\n" \
  "buffer_load_dwordx4 v[26:29], v252, s[76:79], s32 offen offset:0\n" \
  "s_memrealtime s[16:17]\n" \
  "s_lshr_b32 s32, s20, 1\n" \
  "s_lshl3_add_u32 s32, s32, 8\n" \
  "s_load_dwordx2 s[52:53], s[0:1], s32\n" \
  "s_load_dwordx2 s[4:5], s[0:1], 24\n" \
  "s_load_dwordx2 s[68:69], s[0:1], 56\n" \
  "s_load_dwordx2 s[72:73], s[0:1], 64\n" \
  "s_load_dwordx2 s[40:41], s[0:1], 72\n" \
  "s_load_dwordx2 s[44:45], s[0:1], 80\n" \
  "s_mov_b32 s6, 18874368\n" \
  "s_mov_b32 s7, 131072\n" \
  "s_mov_b32 s70, 9216\n" \
  "s_mov_b32 s71, 131072\n" \
  "s_mov_b32 s74, 4096\n" \
  "s_mov_b32 s75, 131072\n" \
  "s_mov_b32 s42, 18874368\n" \
  "s_mov_b32 s43, 131072\n" \
  "s_mov_b32 s46, 2359296\n" \
  "s_mov_b32 s47, 131072\n" \
  "s_mov_b32 s54, 1183744\n" \
  "s_mov_b32 s55, 131072\n" \
  "v_and_b32 v246, 15, v191\n" \
  "v_lshrrev_b32 v244, 4, v191\n" \
  "v_lshlrev_b32 v243, 4, v0\n" \
  "v_lshrrev_b32 v242, 1, v246\n" \
  "v_and_b32 v241, 1, v244\n" \
  "v_lshl_add_u32 v240, v241, 9, s86\n" \
  "v_and_b32 v241, 1, v246\n" \
  "v_and_b32 v239, 3, v242\n" \
  "v_lshl_add_u32 v238, v241, 2, v239\n" \
  "v_lshl_add_u32 v239, v238, 8, s86\n" \
  "v_lshlrev_b32 v238, 1, v244\n" \
  "v_cvt_f32_u32 v237, v242\n" \
  "v_cvt_f32_u32 v242, v240\n" \
  "v_cmp_eq_u32 vcc, v241, v253\n" \
  "v_mul_f32 v237, 3120562176, v237\n" \
  "v_mov_b32 v240, 1048576000\n" \
  "v_cndmask_b32 v240, 0, v240, vcc\n" \
  "v_lshlrev_b32 v236, 31, v241\n" \
  "v_mul_f32 v242, v237, v242\n" \
  "v_xor_b32 v236, v240, v236\n" \
  "v_mul_f32 v237, 1132462080, v237\n" \
  "v_fma_f32 v235, v237, 0, v242\n" \
  "v_cos_f32 v234, v235\n" \
  "v_sin_f32 v233, v235\n" \
  "v_mul_f32 v232, v234, v240\n" \
  "v_mul_f32 v231, v233, v236\n" \
  "v_mul_f32 v230, v233, v240\n" \
  "v_mul_f32 v229, v234, v236\n" \
  "v_cvt_pk_f16_f32 v232, v232, -v231\n" \
  "v_cvt_pk_f16_f32 v230, v230, v229\n" \
  "v_accvgpr_write_b32 a160, v232\n" \
  "v_accvgpr_write_b32 a164, v230\n" \
  "v_fma_f32 v235, v237, 4.0, v242\n" \
  "v_cos_f32 v234, v235\n" \
  "v_sin_f32 v233, v235\n" \
  "v_mul_f32 v232, v234, v240\n" \
  "v_mul_f32 v231, v233, v236\n" \
  "v_mul_f32 v230, v233, v240\n" \
  "v_mul_f32 v229, v234, v236\n" \
  "v_cvt_pk_f16_f32 v232, v232, -v231\n" \
  "v_cvt_pk_f16_f32 v230, v230, v229\n" \
  "v_accvgpr_write_b32 a161, v232\n" \
  "v_accvgpr_write_b32 a165, v230\n" \
  "v_fma_f32 v235, v237, 1.0, v242\n" \
  "v_cos_f32 v234, v235\n" \
  "v_sin_f32 v233, v235\n" \
  "v_mul_f32 v232, v234, v240\n" \
  "v_mul_f32 v231, v233, v236\n" \
  "v_mul_f32 v230, v233, v240\n" \
  "v_mul_f32 v229, v234, v236\n" \
  "v_cvt_pk_f16_f32 v232, v232, -v231\n" \
  "v_cvt_pk_f16_f32 v230, v230, v229\n" \
  "v_accvgpr_write_b32 a162, v232\n" \
  "v_accvgpr_write_b32 a166, v230\n" \
  "v_fma_f32 v235, v237, 4.0, v242\n" \
  "v_add_f32 v235, v235, v237\n" \
  "v_cos_f32 v234, v235\n" \
  "v_sin_f32 v233, v235\n" \
  "v_mul_f32 v232, v234, v240\n" \
  "v_mul_f32 v231, v233, v236\n" \
  "v_mul_f32 v230, v233, v240\n" \
  "v_mul_f32 v229, v234, v236\n" \
  "v_cvt_pk_f16_f32 v232, v232, -v231\n" \
  "v_cvt_pk_f16_f32 v230, v230, v229\n" \
  "v_accvgpr_write_b32 a163, v232\n" \
  "v_accvgpr_write_b32 a167, v230\n" \
  "v_cvt_f32_u32 v237, v239\n" \
  "v_cvt_f32_u32 v242, v238\n" \
  "v_cmp_lt_u32 vcc, 7, v246\n" \
  "v_mul_f32 v237, 973078528, v237\n" \
  "v_mul_f32 v235, v237, v242\n" \
  "v_cos_f32 v234, v235\n" \
  "v_sin_f32 v233, v235\n" \
  "v_mul_f32 v234, 0.5, v234\n" \
  "v_mul_f32 v233, 0.5, v233\n" \
  "v_cvt_pk_f16_f32 v232, v234, -v233\n" \
  "v_cvt_pk_f16_f32 v231, v233, v234\n" \
  "v_cndmask_b32 v230, v232, 0, vcc\n" \
  "v_cndmask_b32 v232, 0, v232, vcc\n" \
  "v_cndmask_b32 v229, v231, 0, vcc\n" \
  "v_xor_b32 v231, 2147516416, v231\n" \
  "v_accvgpr_write_b32 a228, v230\n" \
  "v_cndmask_b32 v231, 0, v231, vcc\n" \
  "v_accvgpr_write_b32 a229, v232\n" \
  "v_accvgpr_write_b32 a232, v229\n" \
  "v_accvgpr_write_b32 a233, v231\n" \
  "v_add_f32 v235, v235, v237\n" \
  "v_cos_f32 v234, v235\n" \
  "v_sin_f32 v233, v235\n" \
  "v_mul_f32 v234, 0.5, v234\n" \
  "v_mul_f32 v233, 0.5, v233\n" \
  "v_cvt_pk_f16_f32 v232, v234, -v233\n" \
  "v_cvt_pk_f16_f32 v231, v233, v234\n" \
  "v_cndmask_b32 v230, v232, 0, vcc\n" \
  "v_cndmask_b32 v232, 0, v232, vcc\n" \
  "v_cndmask_b32 v229, v231, 0, vcc\n" \
  "v_xor_b32 v231, 2147516416, v231\n" \
  "v_accvgpr_write_b32 a230, v230\n" \
  "v_cndmask_b32 v231, 0, v231, vcc\n" \
  "v_accvgpr_write_b32 a231, v232\n" \
  "v_accvgpr_write_b32 a234, v229\n" \
  "v_accvgpr_write_b32 a235, v231\n" \
  "s_cmp_eq_u32 s20, 0\n" \
  "s_cselect_b64 s[58:59], -1, 0\n" \
  "s_movk_i32 s34, 144\n" \
  "v_cmp_gt_u32 s[94:95], s34, v0\n" \
  "v_mov_b32 v30, 0\n" \
  "v_mov_b32 v31, 0\n" \
  "v_mov_b32 v255, 161664\n" \
  "ds_write_b64 v255, v[30:31] offset:0\n" \
  "s_lshr_b32 s88, s2, 6\n" \
  "s_lshl_b32 s98, s84, 2\n" \
  "s_lshr_b32 s100, s85, 1\n" \
  "s_and_b32 s100, s100, 3\n" \
  "s_add_u32 s89, s98, s100\n" \
  "s_mul_i32 s98, s86, 9344\n" \
  "s_add_u32 s82, s98, 524288\n" \
  "s_lshl_b32 s98, s86, 8\n" \
  "s_add_u32 s11, s98, 9764864\n" \
  "s_lshl_b32 s98, s86, 4\n" \
  "s_add_u32 s28, s98, 65536\n" \
  "s_and_b32 s98, s84, 1\n" \
  "s_lshl_b32 s98, s98, 3\n" \
  "s_and_b32 s100, s85, 7\n" \
  "s_add_u32 s90, s98, s100\n" \
  "s_lshl_b32 s98, s88, 5\n" \
  "s_add_u32 s98, s98, s89\n" \
  "s_lshl_b32 s98, s98, 1\n" \
  "s_and_b32 s100, s85, 1\n" \
  "s_add_u32 s98, s98, s100\n" \
  "s_lshl_b32 s98, s98, 7\n" \
  "s_add_u32 s80, s98, 262144\n" \
  "s_mov_b32 s35, 987991609\n" \
  "v_mul_u32_u24 v242, 9, v244\n" \
  "v_add_u32 v240, 8, v242\n" \
  "v_lshlrev_b32 v242, 4, v246\n" \
  "v_lshl_add_u32 v239, v240, 16, v242\n" \
  "v_lshrrev_b32 v242, 3, v191\n" \
  "v_and_b32 v240, 7, v191\n" \
  "v_lshlrev_b32 v238, 4, v240\n" \
  "v_lshl_add_u32 v240, v242, 16, v238\n" \
  "v_add_u32 v242, s80, v240\n" \
  "s_add_u32 s83, s82, 4096\n" \
  "s_add_u32 s91, s82, 8192\n" \
  "v_mov_b32 v30, -1\n" \
  "v_mov_b32 v31, -1\n" \
  "v_mov_b32 v32, -1\n" \
  "v_mov_b32 v33, -1\n" \
  "v_cmp_eq_u32 vcc, 24, v191\n" \
  "v_mov_b32 v255, s28\n" \
  "s_cmp_eq_u32 s20, 2\n" \
  "v_cndmask_b32 v242, v242, v255, vcc\n" \
  "s_cselect_b64 vcc, -1, 0\n" \
  "s_cselect_b32 s91, s11, s91\n" \
  "v_cndmask_b32 v240, v243, v239, vcc\n" \
  "s_cmp_eq_u32 s20, 3\n" \
  "s_cselect_b64 vcc, -1, 0\n" \
  "s_cselect_b32 s91, 0, s91\n" \
  "s_cselect_b32 s32, 0, -1\n" \
  "v_cndmask_b32 v240, v240, v242, vcc\n" \
  "s_cselect_b32 s92, 33554431, -1\n" \
  "s_cselect_b32 s93, 0, -1\n" \
  "s_cmp_eq_u32 s20, 1\n" \
  "s_cselect_b64 s[92:93], 15, s[92:93]\n" \
  "v_mov_b32 v34, s32\n" \
  "v_mov_b32 v35, s32\n" \
  "v_mov_b32 v36, s32\n" \
  "v_mov_b32 v37, s32\n" \
  "s_movk_i32 s32, 768\n" \
  "v_lshlrev_b32 v242, 5, v191\n" \
  "v_bfe_u32 v239, v191, 5, 1\n" \
  "v_and_or_b32 v242, v242, s32, v239\n" \
  "v_lshrrev_b32 v239, 3, v191\n" \
  "v_mul_u32_u24 v242, 9216, v242\n" \
  "v_bitop3_b32 v239, v239, v191, 6 bitop3:0x6c\n" \
  "v_and_b32 v239, 7, v239\n" \
  "v_lshl_or_b32 v38, v239, 4, v242\n" \
  "v_add_u32 v39, 17408, v38\n" \
  "v_add_u32 v40, 34816, v38\n" \
  "v_add_u32 v41, 52224, v38\n" \
  "v_bfrev_b32 v242, 1\n" \
  "v_cndmask_b32 v242, v242, v243, s[58:59]\n" \
  "s_waitcnt lgkmcnt(0)\n" \
  "s_mov_b32 s17, s16\n" \
  "v_and_b32 v239, 127, v0\n" \
  "s_mul_i32 s98, s86, 4624\n" \
  "v_lshl_add_u32 v238, v239, 4, s98\n" \
  "v_add_u32 v237, 3440, v238\n" \
  "v_add_u32 v236, 2048, v238\n" \
  "v_add_u32 v235, 1392, v238\n" \
  "s_lshr_b32 s98, s20, 1\n" \
  "s_mul_i32 s98, s98, 6144\n" \
  "s_and_b32 s100, s20, 1\n" \
  "s_lshl_b32 s100, s100, 10\n" \
  "s_add_u32 s98, s98, s100\n" \
  "s_add_u32 s56, s98, 19584\n" \
  "v_cmp_gt_u32 vcc, 57, v239\n" \
  "v_subrev_u32 v255, 1183744, v237\n" \
  "s_mov_b32 m0, s56\n" \
  "v_cndmask_b32 v236, v235, v236, vcc\n" \
  "buffer_load_dwordx4 v238, s[52:55], 0 offen offset:0 lds\n" \
  "v_min_u32 v237, v237, v255\n" \
  "s_add_u32 m0, s56, 2048\n" \
  "s_bitcmp1_b32 s20, 0\n" \
  "buffer_load_dwordx4 v236, s[52:55], 0 offen offset:0 lds\n" \
  "s_add_u32 m0, s56, 4096\n" \
  "s_cselect_b32 exec_hi, 32767, -1\n" \
  "buffer_load_dwordx4 v237, s[52:55], 0 offen offset:0 lds\n" \
  "s_mov_b64 exec, -1\n" \
  "s_movk_i32 s32, 10240\n" \
  "buffer_load_dwordx4 v[42:45], v243, s[76:79], s32 offen offset:0\n" \
  "s_waitcnt vmcnt(12)\n" \
  "v_readfirstlane_b32 s18, v2\n" \
  "v_readfirstlane_b32 s19, v3\n" \
  "s_add_u32 s18, s18, 1\n" \
  "s_addc_u32 s19, s19, 0\n" \
  "s_waitcnt vmcnt(11)\n" \
  "v_lshlrev_b32 v2, 16, v4\n" \
  "v_and_b32 v3, 4294901760, v4\n" \
  "v_lshlrev_b32 v46, 16, v5\n" \
  "v_and_b32 v47, 4294901760, v5\n" \
  "v_mul_f32 v239, 3216550459, v2\n" \
  "v_mul_f32 v238, 3216550459, v3\n" \
  "v_mul_f32 v237, 3216550459, v46\n" \
  "v_mul_f32 v236, 3216550459, v47\n" \
  "v_exp_f32 v239, v239\n" \
  "v_exp_f32 v238, v238\n" \
  "v_exp_f32 v237, v237\n" \
  "v_exp_f32 v236, v236\n" \
  "v_add_f32 v239, 1.0, v239\n" \
  "v_add_f32 v238, 1.0, v238\n" \
  "v_add_f32 v237, 1.0, v237\n" \
  "v_add_f32 v236, 1.0, v236\n" \
  "v_rcp_f32 v239, v239\n" \
  "v_rcp_f32 v238, v238\n" \
  "v_rcp_f32 v237, v237\n" \
  "v_rcp_f32 v236, v236\n" \
  "v_mul_f32 v239, v239, v2\n" \
  "v_mul_f32 v238, v238, v3\n" \
  "v_mul_f32 v237, v237, v46\n" \
  "v_mul_f32 v236, v236, v47\n" \
  "v_cvt_pk_bf16_f32 v2, v239, v238\n" \
  "v_cvt_pk_bf16_f32 v3, v237, v236\n" \
  "v_add_u32 v239, 162432, v249\n" \
  "s_mul_i32 s98, s22, 384\n" \
  "s_add_u32 s98, s98, 162432\n" \
  "v_lshl_add_u32 v249, v248, 1, s98\n" \
  "v_and_b32 v248, 3, v191\n" \
  "v_lshlrev_b32 v238, 3, v244\n" \
  "v_lshl_add_u32 v237, v248, 5, v238\n" \
  "s_lshl_b32 s98, s20, 7\n" \
  "s_add_u32 s98, s98, 161152\n" \
  "v_add_u32 v248, s98, v237\n" \
  "v_lshlrev_b32 v238, 3, v246\n" \
  "v_add_u32 v237, 161152, v238\n" \
  "s_mul_i32 s98, s22, 9216\n" \
  "s_lshl_b32 s100, s23, 8\n" \
  "s_add_u32 s98, s98, s100\n" \
  "s_add_u32 s63, s98, 8192\n" \
  "s_mov_b64 exec, s[94:95]\n" \
  "ds_write_b64 v239, v[2:3] offset:0\n" \
  "s_mov_b64 exec, -1\n" \
  "s_cmp_lt_u32 s2, 108\n" \
  "s_cselect_b64 s[96:97], 65535, 0\n" \
  "s_and_b64 s[96:97], s[96:97], s[58:59]\n" \
  "s_waitcnt lgkmcnt(0)\n" \
  "s_barrier\n" \
  "ds_read_u16 v2, v249 offset:0\n" \
  "ds_read_u16 v4, v249 offset:128\n" \
  "ds_read_u16 v46, v249 offset:256\n" \
  "s_waitcnt lgkmcnt(2)\n" \
  "v_lshlrev_b32 v2, 16, v2\n" \
  "s_waitcnt lgkmcnt(1)\n" \
  "v_lshlrev_b32 v4, 16, v4\n" \
  "s_waitcnt lgkmcnt(0)\n" \
  "v_lshlrev_b32 v46, 16, v46\n" \
  "s_waitcnt vmcnt(10)\n" \
  "v_lshlrev_b32 v48, 16, v6\n" \
  "v_and_b32 v49, 4294901760, v6\n" \
  "v_pk_mul_f32 v[50:51], v[48:49], v[2:3] op_sel_hi:[1,0]\n" \
  "v_lshlrev_b32 v48, 16, v7\n" \
  "v_and_b32 v49, 4294901760, v7\n" \
  "v_pk_mul_f32 v[52:53], v[48:49], v[2:3] op_sel_hi:[1,0]\n" \
  "v_lshlrev_b32 v48, 16, v8\n" \
  "v_and_b32 v49, 4294901760, v8\n" \
  "v_pk_mul_f32 v[54:55], v[48:49], v[2:3] op_sel_hi:[1,0]\n" \
  "v_lshlrev_b32 v48, 16, v9\n" \
  "v_and_b32 v49, 4294901760, v9\n" \
  "v_pk_mul_f32 v[6:7], v[48:49], v[2:3] op_sel_hi:[1,0]\n" \
  "s_waitcnt vmcnt(9)\n" \
  "v_lshlrev_b32 v2, 16, v10\n" \
  "v_and_b32 v3, 4294901760, v10\n" \
  "v_pk_fma_f32 v[50:51], v[2:3], v[4:5], v[50:51] op_sel_hi:[1,0,1]\n" \
  "v_lshlrev_b32 v2, 16, v11\n" \
  "v_and_b32 v3, 4294901760, v11\n" \
  "v_pk_fma_f32 v[52:53], v[2:3], v[4:5], v[52:53] op_sel_hi:[1,0,1]\n" \
  "v_lshlrev_b32 v2, 16, v12\n" \
  "v_and_b32 v3, 4294901760, v12\n" \
  "v_pk_fma_f32 v[54:55], v[2:3], v[4:5], v[54:55] op_sel_hi:[1,0,1]\n" \
  "v_lshlrev_b32 v2, 16, v13\n" \
  "v_and_b32 v3, 4294901760, v13\n" \
  "v_pk_fma_f32 v[6:7], v[2:3], v[4:5], v[6:7] op_sel_hi:[1,0,1]\n" \
  "s_waitcnt vmcnt(8)\n" \
  "v_lshlrev_b32 v2, 16, v14\n" \
  "v_and_b32 v3, 4294901760, v14\n" \
  "v_pk_fma_f32 v[50:51], v[2:3], v[46:47], v[50:51] op_sel_hi:[1,0,1]\n" \
  "v_lshlrev_b32 v2, 16, v15\n" \
  "v_and_b32 v3, 4294901760, v15\n" \
  "v_pk_fma_f32 v[52:53], v[2:3], v[46:47], v[52:53] op_sel_hi:[1,0,1]\n" \
  "v_lshlrev_b32 v2, 16, v16\n" \
  "v_and_b32 v3, 4294901760, v16\n" \
  "v_pk_fma_f32 v[54:55], v[2:3], v[46:47], v[54:55] op_sel_hi:[1,0,1]\n" \
  "v_lshlrev_b32 v2, 16, v17\n" \
  "v_and_b32 v3, 4294901760, v17\n" \
  "v_pk_fma_f32 v[6:7], v[2:3], v[46:47], v[6:7] op_sel_hi:[1,0,1]\n" \
  "v_permlane32_swap_b32 v50, v54\n" \
  "v_permlane32_swap_b32 v51, v55\n" \
  "v_permlane32_swap_b32 v52, v6\n" \
  "v_permlane32_swap_b32 v53, v7\n" \
  "v_pk_add_f32 v[50:51], v[50:51], v[54:55]\n" \
  "v_pk_add_f32 v[52:53], v[52:53], v[6:7]\n" \
  "v_mov_b32 v2, s18\n" \
  "v_mov_b32 v3, s19\n" \
  "v_permlane16_swap_b32 v50, v52\n" \
  "v_permlane16_swap_b32 v51, v53\n" \
  "v_pk_add_f32 v[50:51], v[50:51], v[52:53]\n" \
  "s_nop 1\n" \
  "v_add_f32_dpp v50, v50, v50 row_ror:8 row_mask:0xf bank_mask:0xf\n" \
  "v_add_f32_dpp v51, v51, v51 row_ror:8 row_mask:0xf bank_mask:0xf\n" \
  "s_nop 0\n" \
  "v_add_f32_dpp v50, v50, v50 row_ror:4 row_mask:0xf bank_mask:0xf\n" \
  "v_add_f32_dpp v51, v51, v51 row_ror:4 row_mask:0xf bank_mask:0xf\n" \
  "ds_write_b64 v248, v[50:51] offset:0\n" \
  "s_waitcnt lgkmcnt(0)\n" \
  "s_barrier\n" \
  "ds_read_b64 v[50:51], v237 offset:0\n" \
  "ds_read_b64 v[52:53], v237 offset:128\n" \
  "ds_read_b64 v[54:55], v237 offset:256\n" \
  "ds_read_b64 v[6:7], v237 offset:384\n" \
  "s_waitcnt lgkmcnt(2)\n" \
  "v_pk_add_f32 v[50:51], v[50:51], v[52:53]\n" \
  "s_waitcnt lgkmcnt(0)\n" \
  "v_pk_add_f32 v[54:55], v[54:55], v[6:7]\n" \
  "v_pk_add_f32 v[4:5], v[50:51], v[54:55]\n" \
  "s_mov_b64 exec, s[96:97]\n" \
  "buffer_store_dwordx4 v[2:5], v252, s[36:39], s63 offen offset:0 sc1\n" \
  "s_mov_b64 exec, -1\n" \
  "buffer_store_dwordx4 v[30:33], v243, s[36:39], s82 offen offset:0 sc1\n" \
  "buffer_store_dwordx4 v[30:33], v243, s[36:39], s83 offen offset:0 sc1\n" \
  "s_mov_b64 exec, s[92:93]\n" \
  "buffer_store_dwordx4 v[34:37], v240, s[36:39], s91 offen offset:0 sc1\n" \
  "s_mov_b64 exec, -1\n" \
  "s_lshl_b32 s98, s85, 3\n" \
  "s_lshl_b32 s100, s20, 1\n" \
  "s_add_u32 s98, s98, s100\n" \
  "s_mul_i32 s98, s98, 9216\n" \
  "s_mul_i32 s100, s84, 1152\n" \
  "s_add_u32 s98, s98, s100\n" \
  "s_add_u32 s32, s98, 1024\n" \
  "s_lshl_b32 s98, s20, 10\n" \
  "s_add_u32 s33, s98, 155008\n" \
  "s_mov_b32 m0, s33\n" \
  "s_movk_i32 s31, 4096\n" \
  "s_cmp_gt_u32 s20, 1\n" \
  "buffer_load_dwordx4 v38, s[4:7], s32 offen offset:0 lds\n" \
  "s_cselect_b32 s31, -28672, s31\n" \
  "s_add_u32 m0, s33, s31\n" \
  "s_add_u32 s32, s32, 9437184\n" \
  "buffer_load_dwordx4 v38, s[4:7], s32 offen offset:0 lds\n" \
  "s_waitcnt vmcnt(6)\n" \
  "s_barrier\n" \
  "v_and_b32 v249, 65535, v42\n" \
  "ds_read_u16 v2, v249 offset:19584\n" \
  "ds_read_u16 v3, v249 offset:20162\n" \
  "ds_read_u16 v4, v249 offset:20740\n" \
  "ds_read_u16 v5, v249 offset:21318\n" \
  "ds_read_u16 v6, v249 offset:25728\n" \
  "ds_read_u16 v7, v249 offset:26306\n" \
  "ds_read_u16 v8, v249 offset:26884\n" \
  "ds_read_u16 v9, v249 offset:27462\n" \
  "s_add_u32 s32, s82, 2392064\n" \
  "s_cmp_eq_u32 s20, 1\n" \
  "s_cselect_b64 vcc, 255, s[58:59]\n" \
  "v_mov_b32 v249, 0\n" \
  "v_and_b32 v248, 65535, v43\n" \
  "ds_read_u16 v10, v248 offset:19584\n" \
  "ds_read_u16 v11, v248 offset:20162\n" \
  "ds_read_u16 v12, v248 offset:20740\n" \
  "ds_read_u16 v13, v248 offset:21318\n" \
  "ds_read_u16 v14, v248 offset:25728\n" \
  "ds_read_u16 v15, v248 offset:26306\n" \
  "ds_read_u16 v16, v248 offset:26884\n" \
  "ds_read_u16 v17, v248 offset:27462\n" \
  "v_lshrrev_b32 v248, 16, v42\n" \
  "s_waitcnt lgkmcnt(11)\n" \
  "v_lshl_or_b32 v2, v6, 16, v2\n" \
  "s_waitcnt lgkmcnt(10)\n" \
  "v_lshl_or_b32 v3, v7, 16, v3\n" \
  "s_waitcnt lgkmcnt(9)\n" \
  "v_lshl_or_b32 v4, v8, 16, v4\n" \
  "s_waitcnt lgkmcnt(8)\n" \
  "v_lshl_or_b32 v5, v9, 16, v5\n" \
  "buffer_store_dwordx4 v[2:5], v248, s[36:39], s32 offen offset:0 sc1\n" \
  "v_and_b32 v6, 2147450879, v2\n" \
  "v_pk_max_u16 v249, v249, v6\n" \
  "v_and_b32 v7, 2147450879, v3\n" \
  "v_pk_max_u16 v249, v249, v7\n" \
  "v_and_b32 v8, 2147450879, v4\n" \
  "v_pk_max_u16 v249, v249, v8\n" \
  "v_and_b32 v9, 2147450879, v5\n" \
  "v_pk_max_u16 v249, v249, v9\n" \
  "v_and_b32 v248, 65535, v44\n" \
  "ds_read_u16 v2, v248 offset:19584\n" \
  "ds_read_u16 v3, v248 offset:20162\n" \
  "ds_read_u16 v4, v248 offset:20740\n" \
  "ds_read_u16 v5, v248 offset:21318\n" \
  "ds_read_u16 v6, v248 offset:25728\n" \
  "ds_read_u16 v7, v248 offset:26306\n" \
  "ds_read_u16 v8, v248 offset:26884\n" \
  "ds_read_u16 v9, v248 offset:27462\n" \
  "v_lshrrev_b32 v248, 16, v43\n" \
  "s_waitcnt lgkmcnt(11)\n" \
  "v_lshl_or_b32 v10, v14, 16, v10\n" \
  "s_waitcnt lgkmcnt(10)\n" \
  "v_lshl_or_b32 v11, v15, 16, v11\n" \
  "s_waitcnt lgkmcnt(9)\n" \
  "v_lshl_or_b32 v12, v16, 16, v12\n" \
  "s_waitcnt lgkmcnt(8)\n" \
  "v_lshl_or_b32 v13, v17, 16, v13\n" \
  "buffer_store_dwordx4 v[10:13], v248, s[36:39], s32 offen offset:0 sc1\n" \
  "v_and_b32 v14, 2147450879, v10\n" \
  "v_pk_max_u16 v249, v249, v14\n" \
  "v_and_b32 v15, 2147450879, v11\n" \
  "v_pk_max_u16 v249, v249, v15\n" \
  "v_and_b32 v16, 2147450879, v12\n" \
  "v_pk_max_u16 v249, v249, v16\n" \
  "v_and_b32 v17, 2147450879, v13\n" \
  "v_pk_max_u16 v249, v249, v17\n" \
  "s_mov_b64 s[96:97], vcc\n" \
  "v_lshrrev_b32 v248, 16, v44\n" \
  "s_waitcnt lgkmcnt(3)\n" \
  "v_lshl_or_b32 v2, v6, 16, v2\n" \
  "s_waitcnt lgkmcnt(2)\n" \
  "v_lshl_or_b32 v3, v7, 16, v3\n" \
  "s_waitcnt lgkmcnt(1)\n" \
  "v_lshl_or_b32 v4, v8, 16, v4\n" \
  "s_waitcnt lgkmcnt(0)\n" \
  "v_lshl_or_b32 v5, v9, 16, v5\n" \
  "s_mov_b64 exec, s[96:97]\n" \
  "buffer_store_dwordx4 v[2:5], v248, s[36:39], s32 offen offset:0 sc1\n" \
  "s_mov_b64 exec, -1\n" \
  "v_and_b32 v6, 2147450879, v2\n" \
  "v_pk_max_u16 v249, v249, v6\n" \
  "v_and_b32 v7, 2147450879, v3\n" \
  "v_pk_max_u16 v249, v249, v7\n" \
  "v_and_b32 v8, 2147450879, v4\n" \
  "v_pk_max_u16 v249, v249, v8\n" \
  "v_and_b32 v9, 2147450879, v5\n" \
  "v_pk_max_u16 v249, v249, v9\n" \
  "v_lshlrev_b32 v255, 16, v249\n" \
  "v_max_u32 v249, v249, v255\n" \
  "v_mov_b32 v255, 161664\n" \
  "s_nop 0\n" \
  "v_max_u32_dpp v249, v249, v249 quad_perm:[1,0,3,2] row_mask:0xf bank_mask:0xf\n" \
  "s_nop 1\n" \
  "v_max_u32_dpp v249, v249, v249 quad_perm:[2,3,0,1] row_mask:0xf bank_mask:0xf\n" \
  "s_nop 1\n" \
  "v_max_u32_dpp v249, v249, v249 row_half_mirror row_mask:0xf bank_mask:0xf\n" \
  "s_nop 1\n" \
  "v_max_u32_dpp v249, v249, v249 row_mirror row_mask:0xf bank_mask:0xf\n" \
  "s_mov_b32 exec_lo, 65537\n" \
  "s_mov_b32 exec_hi, 65537\n" \
  "ds_max_u32 v255, v249 offset:4\n" \
  "s_mov_b64 exec, -1\n" \
  "v_mul_u32_u24 v249, 2448, v253\n" \
  "s_mul_i32 s98, s20, 4896\n" \
  "v_add_u32 v248, s98, v249\n" \
  "v_lshrrev_b32 v249, 3, v254\n" \
  "v_lshlrev_b32 v240, 4, v249\n" \
  "v_lshl_add_u32 v249, v254, 5, v240\n" \
  "v_add_u32 v240, v249, v248\n" \
  "v_lshl_add_u32 v249, v254, 3, v248\n" \
  "v_lshlrev_b32 v2, 16, v18\n" \
  "v_and_b32 v3, 4294901760, v18\n" \
  "v_lshlrev_b32 v4, 16, v19\n" \
  "v_and_b32 v5, 4294901760, v19\n" \
  "v_lshlrev_b32 v6, 16, v20\n" \
  "v_and_b32 v7, 4294901760, v20\n" \
  "v_lshlrev_b32 v8, 16, v21\n" \
  "v_and_b32 v9, 4294901760, v21\n" \
  "v_lshlrev_b32 v10, 16, v22\n" \
  "v_and_b32 v11, 4294901760, v22\n" \
  "v_lshlrev_b32 v12, 16, v23\n" \
  "v_and_b32 v13, 4294901760, v23\n" \
  "v_lshlrev_b32 v14, 16, v24\n" \
  "v_and_b32 v15, 4294901760, v24\n" \
  "v_lshlrev_b32 v16, 16, v25\n" \
  "v_and_b32 v17, 4294901760, v25\n" \
  "v_lshlrev_b32 v30, 16, v247\n" \
  "v_and_b32 v31, 4294901760, v247\n" \
  "v_mov_b32 v33, 925353388\n" \
  "v_mov_b32_dpp v34, v2 row_newbcast:0 row_mask:0xf bank_mask:0xf\n" \
  "v_mov_b32_dpp v248, v2 row_newbcast:0 row_mask:0xf bank_mask:0xf\n" \
  "s_nop 1\n" \
  "v_permlane16_swap_b32 v34, v248\n" \
  "v_pk_add_f32 v[2:3], v[2:3], v[34:35] op_sel_hi:[1,0] neg_lo:[0,1] neg_hi:[0,1]\n" \
  "v_pk_add_f32 v[4:5], v[4:5], v[34:35] op_sel_hi:[1,0] neg_lo:[0,1] neg_hi:[0,1]\n" \
  "v_pk_add_f32 v[6:7], v[6:7], v[34:35] op_sel_hi:[1,0] neg_lo:[0,1] neg_hi:[0,1]\n" \
  "v_pk_add_f32 v[8:9], v[8:9], v[34:35] op_sel_hi:[1,0] neg_lo:[0,1] neg_hi:[0,1]\n" \
  "v_pk_add_f32 v[10:11], v[10:11], v[34:35] op_sel_hi:[1,0] neg_lo:[0,1] neg_hi:[0,1]\n" \
  "v_pk_add_f32 v[12:13], v[12:13], v[34:35] op_sel_hi:[1,0] neg_lo:[0,1] neg_hi:[0,1]\n" \
  "v_pk_add_f32 v[14:15], v[14:15], v[34:35] op_sel_hi:[1,0] neg_lo:[0,1] neg_hi:[0,1]\n" \
  "v_pk_add_f32 v[16:17], v[16:17], v[34:35] op_sel_hi:[1,0] neg_lo:[0,1] neg_hi:[0,1]\n" \
  "v_pk_add_f32 v[30:31], v[30:31], v[34:35] op_sel_hi:[1,0] neg_lo:[0,1] neg_hi:[0,1]\n" \
  "v_pk_add_f32 v[34:35], v[2:3], v[4:5]\n" \
  "v_pk_mul_f32 v[36:37], v[2:3], v[2:3]\n" \
  "v_pk_fma_f32 v[36:37], v[4:5], v[4:5], v[36:37]\n" \
  "v_pk_add_f32 v[34:35], v[34:35], v[6:7]\n" \
  "v_pk_fma_f32 v[36:37], v[6:7], v[6:7], v[36:37]\n" \
  "v_pk_add_f32 v[34:35], v[34:35], v[8:9]\n" \
  "v_pk_fma_f32 v[36:37], v[8:9], v[8:9], v[36:37]\n" \
  "v_pk_add_f32 v[34:35], v[34:35], v[10:11]\n" \
  "v_pk_fma_f32 v[36:37], v[10:11], v[10:11], v[36:37]\n" \
  "v_pk_add_f32 v[34:35], v[34:35], v[12:13]\n" \
  "v_pk_fma_f32 v[36:37], v[12:13], v[12:13], v[36:37]\n" \
  "v_pk_add_f32 v[34:35], v[34:35], v[14:15]\n" \
  "v_pk_fma_f32 v[36:37], v[14:15], v[14:15], v[36:37]\n" \
  "v_pk_add_f32 v[34:35], v[34:35], v[16:17]\n" \
  "v_pk_fma_f32 v[36:37], v[16:17], v[16:17], v[36:37]\n" \
  "v_pk_add_f32 v[34:35], v[34:35], v[30:31]\n" \
  "v_pk_fma_f32 v[36:37], v[30:31], v[30:31], v[36:37]\n" \
  "v_add_f32 v248, v34, v35\n" \
  "v_add_f32 v239, v36, v37\n" \
  "s_nop 1\n" \
  "v_permlane16_swap_b32 v248, v239\n" \
  "v_add_f32 v248, v248, v239\n" \
  "s_nop 1\n" \
  "v_add_f32_dpp v248, v248, v248 quad_perm:[1,0,3,2] row_mask:0xf bank_mask:0xf\n" \
  "s_nop 1\n" \
  "v_add_f32_dpp v248, v248, v248 quad_perm:[2,3,0,1] row_mask:0xf bank_mask:0xf\n" \
  "s_nop 1\n" \
  "v_add_f32_dpp v248, v248, v248 row_half_mirror row_mask:0xf bank_mask:0xf\n" \
  "s_nop 1\n" \
  "v_add_f32_dpp v248, v248, v248 row_mirror row_mask:0xf bank_mask:0xf\n" \
  "v_mov_b32 v239, v248\n" \
  "s_nop 1\n" \
  "v_permlane16_swap_b32 v248, v239\n" \
  "v_mul_f32 v248, s35, v248\n" \
  "v_fma_f32 v239, v239, s35, v33\n" \
  "v_fma_f32 v239, -v248, v248, v239\n" \
  "v_max_f32 v239, v239, v33\n" \
  "v_rsq_f32 v32, v239\n" \
  "s_nop 0\n" \
  "v_mul_f32_e64 v33, -v248, v32\n" \
  "v_pk_fma_f32 v[34:35], v[2:3], v[32:33], v[32:33] op_sel:[0,0,1] op_sel_hi:[1,0,1]\n" \
  "v_pk_fma_f32 v[36:37], v[4:5], v[32:33], v[32:33] op_sel:[0,0,1] op_sel_hi:[1,0,1]\n" \
  "ds_write_b128 v240, v[34:37] offset:0\n" \
  "v_pk_fma_f32 v[2:3], v[6:7], v[32:33], v[32:33] op_sel:[0,0,1] op_sel_hi:[1,0,1]\n" \
  "v_pk_fma_f32 v[4:5], v[8:9], v[32:33], v[32:33] op_sel:[0,0,1] op_sel_hi:[1,0,1]\n" \
  "ds_write_b128 v240, v[2:5] offset:16\n" \
  "v_pk_fma_f32 v[2:3], v[10:11], v[32:33], v[32:33] op_sel:[0,0,1] op_sel_hi:[1,0,1]\n" \
  "v_pk_fma_f32 v[4:5], v[12:13], v[32:33], v[32:33] op_sel:[0,0,1] op_sel_hi:[1,0,1]\n" \
  "ds_write_b128 v240, v[2:5] offset:1088\n" \
  "v_pk_fma_f32 v[2:3], v[14:15], v[32:33], v[32:33] op_sel:[0,0,1] op_sel_hi:[1,0,1]\n" \
  "v_pk_fma_f32 v[4:5], v[16:17], v[32:33], v[32:33] op_sel:[0,0,1] op_sel_hi:[1,0,1]\n" \
  "ds_write_b128 v240, v[2:5] offset:1104\n" \
  "v_pk_fma_f32 v[30:31], v[30:31], v[32:33], v[32:33] op_sel:[0,0,1] op_sel_hi:[1,0,1]\n" \
  "ds_write_b64 v249, v[30:31] offset:2176\n" \
  "s_mul_i32 s33, s20, 4896\n" \
  "v_and_b32 v249, 65535, v26\n" \
  "v_add_u32 v248, s33, v249\n" \
  "v_and_b32 v240, 65535, v27\n" \
  "v_add_u32 v239, s33, v240\n" \
  "v_and_b32 v238, 65535, v28\n" \
  "v_add_u32 v237, s33, v238\n" \
  "v_and_b32 v236, 65535, v29\n" \
  "v_add_u32 v235, s33, v236\n" \
  "v_lshrrev_b32 v234, 16, v26\n" \
  "v_add_u32 v233, s33, v234\n" \
  "v_lshrrev_b32 v232, 16, v27\n" \
  "v_add_u32 v231, s33, v232\n" \
  "v_lshrrev_b32 v230, 16, v28\n" \
  "v_add_u32 v229, s33, v230\n" \
  "v_lshrrev_b32 v228, 16, v29\n" \
  "v_add_u32 v227, s33, v228\n" \
  "ds_read_b64 v[2:3], v248 offset:0\n" \
  "ds_read_b64 v[4:5], v239 offset:0\n" \
  "ds_read_b64 v[6:7], v237 offset:0\n" \
  "ds_read_b64 v[8:9], v235 offset:0\n" \
  "ds_read_b64 v[10:11], v233 offset:0\n" \
  "ds_read_b64 v[12:13], v231 offset:0\n" \
  "ds_read_b64 v[14:15], v229 offset:0\n" \
  "ds_read_b64 v[16:17], v227 offset:0\n" \
  "ds_read_b64 v[26:27], v248 offset:2448\n" \
  "ds_read_b64 v[28:29], v239 offset:2448\n" \
  "ds_read_b64 v[30:31], v237 offset:2448\n" \
  "ds_read_b64 v[32:33], v235 offset:2448\n" \
  "ds_read_b64 v[34:35], v233 offset:2448\n" \
  "ds_read_b64 v[36:37], v231 offset:2448\n" \
  "ds_read_b64 v[42:43], v229 offset:2448\n" \
  "ds_read_b64 v[44:45], v227 offset:2448\n" \
  "s_movk_i32 s32, 8192\n" \
  "buffer_load_dwordx4 v[46:49], v243, s[36:39], s32 offen offset:0 sc1\n" \
  "s_movk_i32 s32, 17408\n" \
  "buffer_load_dwordx4 v[50:53], v243, s[36:39], s32 offen offset:0 sc1\n" \
  "s_movk_i32 s32, 26624\n" \
  "buffer_load_dwordx4 v[54:57], v243, s[36:39], s32 offen offset:0 sc1\n" \
  "s_movk_i32 s32, 12288\n" \
  "buffer_load_dwordx4 v[58:61], v243, s[36:39], s32 offen offset:0 sc1\n" \
  "s_movk_i32 s32, 21504\n" \
  "buffer_load_dwordx4 v[62:65], v243, s[36:39], s32 offen offset:0 sc1\n" \
  "s_movk_i32 s32, 30720\n" \
  "buffer_load_dwordx4 v[66:69], v243, s[36:39], s32 offen offset:0 sc1\n" \
  "s_movk_i32 s32, 16384\n" \
  "buffer_load_dwordx4 v[70:73], v242, s[36:39], s32 offen offset:0 sc1\n" \
  "s_movk_i32 s32, 25600\n" \
  "buffer_load_dwordx4 v[74:77], v242, s[36:39], s32 offen offset:0 sc1\n" \
  "s_mov_b32 s32, 34816\n" \
  "buffer_load_dwordx4 v[78:81], v242, s[36:39], s32 offen offset:0 sc1\n" \
  "s_lshl_b32 s98, s2, 3\n" \
  "s_add_u32 s56, s98, 4096\n" \
  "s_lshl_b32 s98, s20, 10\n" \
  "s_add_u32 s33, s98, 62848\n" \
  "v_mov_b32 v82, s18\n" \
  "s_and_b32 s32, s19, 16777215\n" \
  "s_waitcnt vmcnt(9) lgkmcnt(0)\n" \
  "s_barrier\n" \
  "ds_read_b32 v83, v255 offset:4\n" \
  "s_cmp_eq_u32 s20, 3\n" \
  "s_waitcnt lgkmcnt(0)\n" \
  "v_lshrrev_b32 v83, 23, v83\n" \
  "v_lshl_or_b32 v83, v83, 24, s32\n" \
  "s_cselect_b64 exec, 1, 0\n" \
  "buffer_store_dwordx2 v[82:83], off, s[36:39], s56 sc1\n" \
  "s_mov_b64 exec, -1\n" \
  "s_add_u32 m0, s33, 0\n" \
  "s_movk_i32 s32, 0\n" \
  "buffer_load_dwordx4 v243, s[76:79], s32 offen offset:0 lds\n" \
  "s_add_u32 m0, s33, 30720\n" \
  "s_movk_i32 s32, 4096\n" \
  "buffer_load_dwordx4 v243, s[76:79], s32 offen offset:0 lds\n" \
  "s_add_u32 m0, s33, 61440\n" \
  "s_movk_i32 s32, 8192\n" \
  "buffer_load_dwordx4 v243, s[76:79], s32 offen offset:0 lds\n" \
  "s_movk_i32 s32, 14336\n" \
  "buffer_load_dwordx4 v[82:85], v243, s[76:79], s32 offen offset:0\n" \
  "s_movk_i32 s32, 23552\n" \
  "buffer_load_dwordx4 v[86:89], v252, s[76:79], s32 offen offset:0\n" \
  "s_mul_i32 s98, s20, 589824\n" \
  "s_mul_i32 s100, s84, 1152\n" \
  "s_add_u32 s98, s98, s100\n" \
  "s_and_b32 s100, s85, 7\n" \
  "s_lshl_b32 s100, s100, 7\n" \
  "s_add_u32 s12, s98, s100\n" \
  "s_mul_i32 s98, s20, 30720\n" \
  "s_add_u32 s57, s98, 38272\n" \
  "s_lshl_b32 s98, s20, 10\n" \
  "s_add_u32 s8, s98, s82\n" \
  "s_lshl_b32 s98, s84, 2\n" \
  "s_add_u32 s98, s98, s20\n" \
  "s_lshl_b32 s98, s98, 16\n" \
  "s_lshl_b32 s100, s85, 11\n" \
  "s_add_u32 s98, s98, s100\n" \
  "s_add_u32 s24, s98, 5308416\n" \
  "s_mul_i32 s81, s88, 294912\n" \
  "s_sub_u32 s13, s12, s81\n" \
  "s_add_u32 s12, s12, s81\n" \
  "v_lshrrev_b32 v248, 5, v0\n" \
  "v_lshlrev_b32 v239, 4, v248\n" \
  "v_lshl_add_u32 v248, v0, 3, v239\n" \
  "v_add_u32 v239, 35216, v248\n" \
  "v_add_u32 v237, 32496, v248\n" \
  "v_add_u32 v235, 37392, v248\n" \
  "v_add_u32 v233, 34672, v248\n" \
  "v_and_b32 v248, 48, v191\n" \
  "v_lshl_add_u32 v231, v246, 6, v248\n" \
  "v_cmp_gt_u32 vcc, 32, v0\n" \
  "v_mov_b32 v90, 1.0\n" \
  "s_nop 0\n" \
  "v_cndmask_b32 v237, v237, v235, vcc\n" \
  "v_cndmask_b32 v90, v90, 0, vcc\n" \
  "v_cndmask_b32 v248, v248, v231, s[58:59]\n" \
  "v_mov_b32 v95, 0\n" \
  "v_mov_b32 v99, 0\n" \
  "v_mov_b32 v103, 0\n" \
  "v_mov_b32 v107, 0\n" \
  "s_lshr_b32 s98, s4, 7\n" \
  "s_sub_u32 s98, 3, s98\n" \
  "s_sub_u32 s98, s98, s84\n" \
  "s_and_b32 s49, s98, 7\n" \
  "s_and_b32 s48, s85, 7\n" \
  "s_cmp_eq_u32 s48, s49\n" \
  "s_cselect_b32 s48, 2147483648, 0\n" \
  "v_or_b32 v108, s48, v38\n" \
  "v_or_b32 v109, s48, v39\n" \
  "v_or_b32 v110, s48, v40\n" \
  "v_or_b32 v111, s48, v41\n" \
  "s_waitcnt vmcnt(6)\n" \
  ".Lanc_%=:\n" \
  "v_xor_b32 v235, s18, v46\n" \
  "v_bitop3_b32 v235, v47, s19, v235 bitop3:0xbe\n" \
  "v_bitop3_b32 v235, v50, s18, v235 bitop3:0xbe\n" \
  "v_bitop3_b32 v235, v51, s19, v235 bitop3:0xbe\n" \
  "v_bitop3_b32 v235, v54, s18, v235 bitop3:0xbe\n" \
  "v_bitop3_b32 v235, v55, s19, v235 bitop3:0xbe\n" \
  "v_bitop3_b32 v235, v58, s18, v235 bitop3:0xbe\n" \
  "v_bitop3_b32 v235, v59, s19, v235 bitop3:0xbe\n" \
  "v_bitop3_b32 v235, v62, s18, v235 bitop3:0xbe\n" \
  "v_bitop3_b32 v235, v63, s19, v235 bitop3:0xbe\n" \
  "v_bitop3_b32 v235, v66, s18, v235 bitop3:0xbe\n" \
  "v_bitop3_b32 v235, v67, s19, v235 bitop3:0xbe\n" \
  "v_xor_b32 v229, s18, v70\n" \
  "v_bitop3_b32 v229, v71, s19, v229 bitop3:0xbe\n" \
  "v_bitop3_b32 v229, v74, s18, v229 bitop3:0xbe\n" \
  "v_bitop3_b32 v229, v75, s19, v229 bitop3:0xbe\n" \
  "v_bitop3_b32 v229, v78, s18, v229 bitop3:0xbe\n" \
  "v_bitop3_b32 v229, v79, s19, v229 bitop3:0xbe\n" \
  "v_bitop3_b32 v235, v229, s58, v235 bitop3:0xea\n" \
  "v_cmp_ne_u32 vcc, 0, v235\n" \
  "s_cbranch_vccnz .Lans_%=\n" \
  ".Land_%=:\n" \
  "v_mov_b32 v242, 0\n" \
  "v_pk_add_f32 v[112:113], v[48:49], v[52:53]\n" \
  "v_pk_add_f32 v[112:113], v[112:113], v[56:57]\n" \
  "s_add_u32 m0, s57, 0\n" \
  "s_add_u32 s31, s12, 0\n" \
  "buffer_load_dwordx4 v108, s[4:7], s31 offen offset:0 lds\n" \
  "v_cvt_pk_bf16_f32 v235, v112, v113\n" \
  "v_lshlrev_b32 v112, 16, v235\n" \
  "v_and_b32 v113, 4294901760, v235\n" \
  "ds_write_b64 v239, v[112:113] offset:0\n" \
  "v_max3_f32 v242, |v112|, |v113|, v242\n" \
  "buffer_load_dwordx4 v109, s[4:7], s31 offen offset:1024 lds\n" \
  "v_pk_add_f32 v[46:47], v[60:61], v[64:65]\n" \
  "v_pk_add_f32 v[46:47], v[46:47], v[68:69]\n" \
  "buffer_load_dwordx4 v110, s[4:7], s31 offen offset:2048 lds\n" \
  "v_cvt_pk_bf16_f32 v239, v46, v47\n" \
  "v_lshlrev_b32 v46, 16, v239\n" \
  "v_and_b32 v47, 4294901760, v239\n" \
  "v_pk_add_f32 v[46:47], v[46:47], v[90:91] op_sel_hi:[1,0]\n" \
  "v_cvt_pk_bf16_f32 v239, v46, v47\n" \
  "v_lshlrev_b32 v46, 16, v239\n" \
  "v_and_b32 v47, 4294901760, v239\n" \
  "ds_write_b64 v237, v[46:47] offset:0\n" \
  "v_max3_f32 v242, |v46|, |v47|, v242\n" \
  "buffer_load_dwordx4 v111, s[4:7], s31 offen offset:3072 lds\n" \
  "s_mov_b64 exec, s[58:59]\n" \
  "v_pk_add_f32 v[46:47], v[72:73], v[76:77]\n" \
  "v_pk_add_f32 v[46:47], v[46:47], v[80:81]\n" \
  "v_cvt_pk_bf16_f32 v239, v46, v47\n" \
  "v_lshlrev_b32 v46, 16, v239\n" \
  "v_and_b32 v47, 4294901760, v239\n" \
  "v_add_f32 v46, 1.0, v46\n" \
  "v_add_f32 v47, 1.0, v47\n" \
  "v_cvt_pk_bf16_f32 v239, v46, v47\n" \
  "v_lshlrev_b32 v46, 16, v239\n" \
  "v_and_b32 v47, 4294901760, v239\n" \
  "ds_write_b64 v233, v[46:47] offset:0\n" \
  "v_max3_f32 v242, |v46|, |v47|, v242\n" \
  "s_mov_b64 exec, -1\n" \
  "s_add_u32 m0, s57, 4096\n" \
  "s_add_u32 s31, s12, 9437184\n" \
  "buffer_load_dwordx4 v108, s[4:7], s31 offen offset:0 lds\n" \
  "v_max_u32_dpp v242, v242, v242 quad_perm:[1,0,3,2] row_mask:0xf bank_mask:0xf\n" \
  "buffer_load_dwordx4 v109, s[4:7], s31 offen offset:1024 lds\n" \
  "s_nop 0\n" \
  "v_max_u32_dpp v242, v242, v242 quad_perm:[2,3,0,1] row_mask:0xf bank_mask:0xf\n" \
  "buffer_load_dwordx4 v110, s[4:7], s31 offen offset:2048 lds\n" \
  "s_nop 0\n" \
  "v_max_u32_dpp v242, v242, v242 row_half_mirror row_mask:0xf bank_mask:0xf\n" \
  "buffer_load_dwordx4 v111, s[4:7], s31 offen offset:3072 lds\n" \
  "s_nop 0\n" \
  "v_max_u32_dpp v242, v242, v242 row_mirror row_mask:0xf bank_mask:0xf\n" \
  "s_mov_b32 exec_lo, 65537\n" \
  "s_mov_b32 exec_hi, 65537\n" \
  "ds_max_u32 v255, v242\n" \
  "s_mov_b64 exec, -1\n" \
  "s_waitcnt vmcnt(8) lgkmcnt(0)\n" \
  "s_barrier\n" \
  "ds_read_b32 v46, v255 offset:0\n" \
  "ds_read_b64 v[48:49], v249 offset:32768\n" \
  "ds_read_b64 v[50:51], v249 offset:35216\n" \
  "ds_read_b64 v[52:53], v240 offset:32768\n" \
  "ds_read_b64 v[54:55], v240 offset:35216\n" \
  "ds_read_b64 v[56:57], v238 offset:32768\n" \
  "ds_read_b64 v[58:59], v238 offset:35216\n" \
  "ds_read_b64 v[60:61], v236 offset:32768\n" \
  "ds_read_b64 v[62:63], v236 offset:35216\n" \
  "ds_read_b64 v[64:65], v234 offset:32768\n" \
  "ds_read_b64 v[66:67], v234 offset:35216\n" \
  "ds_read_b64 v[68:69], v232 offset:32768\n" \
  "ds_read_b64 v[70:71], v232 offset:35216\n" \
  "ds_read_b64 v[72:73], v230 offset:32768\n" \
  "ds_read_b64 v[74:75], v230 offset:35216\n" \
  "ds_read_b64 v[76:77], v228 offset:32768\n" \
  "ds_read_b64 v[78:79], v228 offset:35216\n" \
  "v_add_u32 v255, 62848, v252\n" \
  "ds_read_b128 a[200:203], v255 offset:0\n" \
  "ds_read_b128 a[204:207], v255 offset:1024\n" \
  "ds_read_b128 a[168:171], v255 offset:2048\n" \
  "ds_read_b128 a[172:175], v255 offset:3072\n" \
  "s_waitcnt lgkmcnt(15)\n" \
  "v_lshrrev_b32 v46, 23, v46\n" \
  "v_max_u32 v46, 64, v46\n" \
  "s_nop 0\n" \
  "v_readfirstlane_b32 s26, v46\n" \
  "v_sub_u32 v46, 256, v46\n" \
  "v_lshlrev_b32 v46, 23, v46\n" \
  "v_pk_mul_f32 v[48:49], v[48:49], v[46:47] op_sel_hi:[1,0]\n" \
  "v_pk_mul_f32 v[50:51], v[50:51], v[46:47] op_sel_hi:[1,0]\n" \
  "v_pk_fma_f32 v[80:81], v[2:3], v[48:49], v[50:51]\n" \
  "v_pk_fma_f32 v[2:3], v[26:27], v[48:49], v[50:51]\n" \
  "v_pk_mul_f32 v[52:53], v[52:53], v[46:47] op_sel_hi:[1,0]\n" \
  "v_pk_mul_f32 v[54:55], v[54:55], v[46:47] op_sel_hi:[1,0]\n" \
  "v_pk_fma_f32 v[26:27], v[4:5], v[52:53], v[54:55]\n" \
  "v_pk_fma_f32 v[4:5], v[28:29], v[52:53], v[54:55]\n" \
  "v_pk_mul_f32 v[56:57], v[56:57], v[46:47] op_sel_hi:[1,0]\n" \
  "s_waitcnt lgkmcnt(14)\n" \
  "v_pk_mul_f32 v[58:59], v[58:59], v[46:47] op_sel_hi:[1,0]\n" \
  "v_pk_fma_f32 v[28:29], v[6:7], v[56:57], v[58:59]\n" \
  "v_pk_fma_f32 v[6:7], v[30:31], v[56:57], v[58:59]\n" \
  "s_waitcnt lgkmcnt(13)\n" \
  "v_pk_mul_f32 v[60:61], v[60:61], v[46:47] op_sel_hi:[1,0]\n" \
  "s_waitcnt lgkmcnt(12)\n" \
  "v_pk_mul_f32 v[62:63], v[62:63], v[46:47] op_sel_hi:[1,0]\n" \
  "v_pk_fma_f32 v[30:31], v[8:9], v[60:61], v[62:63]\n" \
  "v_pk_fma_f32 v[8:9], v[32:33], v[60:61], v[62:63]\n" \
  "s_waitcnt lgkmcnt(11)\n" \
  "v_pk_mul_f32 v[64:65], v[64:65], v[46:47] op_sel_hi:[1,0]\n" \
  "s_waitcnt lgkmcnt(10)\n" \
  "v_pk_mul_f32 v[66:67], v[66:67], v[46:47] op_sel_hi:[1,0]\n" \
  "v_pk_fma_f32 v[32:33], v[10:11], v[64:65], v[66:67]\n" \
  "v_pk_fma_f32 v[10:11], v[34:35], v[64:65], v[66:67]\n" \
  "s_waitcnt lgkmcnt(9)\n" \
  "v_pk_mul_f32 v[68:69], v[68:69], v[46:47] op_sel_hi:[1,0]\n" \
  "s_waitcnt lgkmcnt(8)\n" \
  "v_pk_mul_f32 v[70:71], v[70:71], v[46:47] op_sel_hi:[1,0]\n" \
  "v_pk_fma_f32 v[34:35], v[12:13], v[68:69], v[70:71]\n" \
  "v_pk_fma_f32 v[12:13], v[36:37], v[68:69], v[70:71]\n" \
  "s_waitcnt lgkmcnt(7)\n" \
  "v_pk_mul_f32 v[72:73], v[72:73], v[46:47] op_sel_hi:[1,0]\n" \
  "s_waitcnt lgkmcnt(6)\n" \
  "v_pk_mul_f32 v[74:75], v[74:75], v[46:47] op_sel_hi:[1,0]\n" \
  "v_pk_fma_f32 v[36:37], v[14:15], v[72:73], v[74:75]\n" \
  "v_pk_fma_f32 v[14:15], v[42:43], v[72:73], v[74:75]\n" \
  "s_waitcnt lgkmcnt(5)\n" \
  "v_pk_mul_f32 v[76:77], v[76:77], v[46:47] op_sel_hi:[1,0]\n" \
  "s_waitcnt lgkmcnt(4)\n" \
  "v_pk_mul_f32 v[78:79], v[78:79], v[46:47] op_sel_hi:[1,0]\n" \
  "v_pk_fma_f32 v[42:43], v[16:17], v[76:77], v[78:79]\n" \
  "v_pk_fma_f32 v[16:17], v[44:45], v[76:77], v[78:79]\n" \
  "v_cvt_pk_f16_f32 v44, v80, v81\n" \
  "v_cvt_pk_f16_f32 v45, v26, v27\n" \
  "v_cvt_pk_f16_f32 v46, v28, v29\n" \
  "v_cvt_pk_f16_f32 v47, v30, v31\n" \
  "v_cvt_pk_f16_f32 v26, v32, v33\n" \
  "v_cvt_pk_f16_f32 v27, v34, v35\n" \
  "s_waitcnt lgkmcnt(3)\n" \
  "v_mfma_f32_16x16x32_f16 v[30:33], v[44:47], a[200:203], 0\n" \
  "v_cvt_pk_f16_f32 v28, v36, v37\n" \
  "v_cvt_pk_f16_f32 v29, v42, v43\n" \
  "s_waitcnt lgkmcnt(2)\n" \
  "v_mfma_f32_16x16x32_f16 v[34:37], v[44:47], a[204:207], 0\n" \
  "v_cvt_pk_f16_f32 v42, v2, v3\n" \
  "v_cvt_pk_f16_f32 v43, v4, v5\n" \
  "s_waitcnt lgkmcnt(1)\n" \
  "v_mfma_f32_16x16x32_f16 v[2:5], v[26:29], a[168:171], 0\n" \
  "v_cvt_pk_f16_f32 v44, v6, v7\n" \
  "v_cvt_pk_f16_f32 v45, v8, v9\n" \
  "s_waitcnt lgkmcnt(0)\n" \
  "v_mfma_f32_16x16x32_f16 v[6:9], v[26:29], a[172:175], 0\n" \
  "v_cvt_pk_f16_f32 v26, v10, v11\n" \
  "v_cvt_pk_f16_f32 v27, v12, v13\n" \
  "v_mfma_f32_16x16x32_f16 v[10:13], v[42:45], a[200:203], 0\n" \
  "v_cvt_pk_f16_f32 v28, v14, v15\n" \
  "v_cvt_pk_f16_f32 v29, v16, v17\n" \
  "v_mfma_f32_16x16x32_f16 v[14:17], v[42:45], a[204:207], 0\n" \
  "ds_read_b128 a[208:211], v255 offset:30720\n" \
  "v_mfma_f32_16x16x32_f16 v[42:45], v[26:29], a[168:171], 0\n" \
  "ds_read_b128 a[212:215], v255 offset:31744\n" \
  "v_mfma_f32_16x16x32_f16 v[46:49], v[26:29], a[172:175], 0\n" \
  "ds_read_b128 a[220:223], v255 offset:32768\n" \
  "ds_read_b128 a[224:227], v255 offset:33792\n" \
  "v_pk_add_f32 v[26:27], v[30:31], v[2:3]\n" \
  "v_pk_add_f32 v[28:29], v[32:33], v[4:5]\n" \
  "v_pk_add_f32 v[50:51], v[34:35], v[6:7]\n" \
  "v_pk_add_f32 v[52:53], v[36:37], v[8:9]\n" \
  "v_pk_add_f32 v[54:55], v[30:31], v[2:3] neg_lo:[0,1] neg_hi:[0,1]\n" \
  "v_pk_add_f32 v[56:57], v[32:33], v[4:5] neg_lo:[0,1] neg_hi:[0,1]\n" \
  "v_pk_add_f32 v[2:3], v[34:35], v[6:7] neg_lo:[0,1] neg_hi:[0,1]\n" \
  "v_pk_add_f32 v[4:5], v[36:37], v[8:9] neg_lo:[0,1] neg_hi:[0,1]\n" \
  "v_pk_add_f32 v[6:7], v[10:11], v[42:43]\n" \
  "v_pk_add_f32 v[8:9], v[12:13], v[44:45]\n" \
  "v_pk_add_f32 v[30:31], v[14:15], v[46:47]\n" \
  "v_pk_add_f32 v[32:33], v[16:17], v[48:49]\n" \
  "v_pk_add_f32 v[34:35], v[10:11], v[42:43] neg_lo:[0,1] neg_hi:[0,1]\n" \
  "v_pk_add_f32 v[36:37], v[12:13], v[44:45] neg_lo:[0,1] neg_hi:[0,1]\n" \
  "v_pk_add_f32 v[10:11], v[14:15], v[46:47] neg_lo:[0,1] neg_hi:[0,1]\n" \
  "v_pk_add_f32 v[12:13], v[16:17], v[48:49] neg_lo:[0,1] neg_hi:[0,1]\n" \
  "v_cvt_pk_f16_f32 v92, v26, v50\n" \
  "v_cvt_pk_f16_f32 v93, v27, v51\n" \
  "v_cvt_pk_f16_f32 v94, v28, v52\n" \
  "v_cvt_pk_f16_f32 v96, v54, v2\n" \
  "v_cvt_pk_f16_f32 v97, v55, v3\n" \
  "s_waitcnt lgkmcnt(3)\n" \
  "v_mfma_f32_16x16x32_f16 v[14:17], a[208:211], v[92:95], 0\n" \
  "v_cvt_pk_f16_f32 v98, v56, v4\n" \
  "s_waitcnt lgkmcnt(2)\n" \
  "v_mfma_f32_16x16x32_f16 v[2:5], a[212:215], v[92:95], 0\n" \
  "v_cvt_pk_f16_f32 v100, v6, v30\n" \
  "v_cvt_pk_f16_f32 v101, v7, v31\n" \
  "v_mfma_f32_16x16x32_f16 v[26:29], a[208:211], v[96:99], 0\n" \
  "v_cvt_pk_f16_f32 v102, v8, v32\n" \
  "v_mfma_f32_16x16x32_f16 v[6:9], a[212:215], v[96:99], 0\n" \
  "v_cvt_pk_f16_f32 v104, v34, v10\n" \
  "v_cvt_pk_f16_f32 v105, v35, v11\n" \
  "v_mfma_f32_16x16x32_f16 v[30:33], a[208:211], v[100:103], 0\n" \
  "v_cvt_pk_f16_f32 v106, v36, v12\n" \
  "v_mfma_f32_16x16x32_f16 v[10:13], a[212:215], v[100:103], 0\n" \
  "s_nop 0\n" \
  "v_mfma_f32_16x16x32_f16 v[34:37], a[208:211], v[104:107], 0\n" \
  "v_mfma_f32_16x16x32_f16 v[42:45], a[212:215], v[104:107], 0\n" \
  "s_mov_b32 exec_hi, 65535\n" \
  "v_cvt_pk_f16_f32 v46, v14, v2\n" \
  "s_nop 2\n" \
  "v_cvt_pk_f16_f32 v47, v30, v10\n" \
  "ds_write_b64 v82, v[46:47] offset:0\n" \
  "v_cvt_pk_f16_f32 v46, v15, v3\n" \
  "v_cvt_pk_f16_f32 v47, v31, v11\n" \
  "ds_write_b64 v82, v[46:47] offset:512\n" \
  "v_cvt_pk_f16_f32 v46, v16, v4\n" \
  "v_cvt_pk_f16_f32 v47, v32, v12\n" \
  "ds_write_b64 v82, v[46:47] offset:1024\n" \
  "v_cvt_pk_f16_f32 v2, v26, v6\n" \
  "v_cvt_pk_f16_f32 v3, v34, v42\n" \
  "ds_write_b64 v82, v[2:3] offset:256\n" \
  "v_cvt_pk_f16_f32 v2, v27, v7\n" \
  "v_cvt_pk_f16_f32 v3, v35, v43\n" \
  "ds_write_b64 v82, v[2:3] offset:768\n" \
  "v_cvt_pk_f16_f32 v2, v28, v8\n" \
  "v_cvt_pk_f16_f32 v3, v36, v44\n" \
  "ds_write_b64 v82, v[2:3] offset:1280\n" \
  "s_mov_b64 exec, -1\n" \
  "s_waitcnt lgkmcnt(0)\n" \
  "s_barrier\n" \
  "ds_read_b128 v[2:5], v83 offset:0\n" \
  "ds_read_b128 v[6:9], v84 offset:0\n" \
  "ds_read_b128 v[10:13], v85 offset:0\n" \
  "v_lshrrev_b32 v255, 1, v191\n" \
  "v_and_b32 v249, 1, v191\n" \
  "s_lshl_b32 s98, s20, 1\n" \
  "v_add_u32 v242, s98, v249\n" \
  "v_and_b32 v249, 6, v255\n" \
  "v_xor_b32 v240, v242, v249\n" \
  "v_lshlrev_b32 v249, 4, v240\n" \
  "v_lshl_add_u32 v242, v255, 7, v249\n" \
  "v_add_u32 v255, 155008, v242\n" \
  "v_lshlrev_b32 v249, 15, v253\n" \
  "v_sub_u32 v242, v255, v249\n" \
  "s_cmp_eq_u32 s20, 1\n" \
  "s_cselect_b32 s92, 65537, s58\n" \
  "s_cselect_b32 s93, 65537, s59\n" \
  "s_add_u32 s32, s8, 4096\n" \
  "s_add_u32 s33, s8, 8192\n" \
  "s_waitcnt lgkmcnt(2)\n" \
  "v_mfma_f32_16x16x32_f16 v[14:17], a[160:163], v[2:5], 0\n" \
  "v_mfma_f32_16x16x32_f16 v[26:29], a[164:167], v[2:5], 0\n" \
  "s_waitcnt lgkmcnt(1)\n" \
  "v_mfma_f32_16x16x32_f16 v[2:5], a[160:163], v[6:9], 0\n" \
  "v_mfma_f32_16x16x32_f16 v[30:33], a[164:167], v[6:9], 0\n" \
  "s_waitcnt lgkmcnt(0)\n" \
  "v_mfma_f32_16x16x32_f16 v[6:9], a[160:163], v[10:13], 0\n" \
  "v_mfma_f32_16x16x32_f16 v[34:37], a[164:167], v[10:13], 0\n" \
  "ds_read_b128 v[10:13], v255 offset:0\n" \
  "ds_read_b128 v[42:45], v242 offset:4096\n" \
  "v_cvt_pk_f16_f32 v46, v14, v26\n" \
  "v_cvt_pk_f16_f32 v47, v15, v27\n" \
  "v_cvt_pk_f16_f32 v48, v16, v28\n" \
  "v_cvt_pk_f16_f32 v49, v17, v29\n" \
  "v_and_b32 v46, -2, v46\n" \
  "buffer_store_dwordx4 v[46:49], v231, s[36:39], s8 offen offset:0 sc1\n" \
  "v_cvt_pk_f16_f32 v14, v2, v30\n" \
  "v_cvt_pk_f16_f32 v15, v3, v31\n" \
  "v_cvt_pk_f16_f32 v16, v4, v32\n" \
  "v_cvt_pk_f16_f32 v17, v5, v33\n" \
  "v_and_b32 v14, -2, v14\n" \
  "buffer_store_dwordx4 v[14:17], v231, s[36:39], s32 offen offset:0 sc1\n" \
  "v_cvt_pk_f16_f32 v2, v6, v34\n" \
  "v_cvt_pk_f16_f32 v3, v7, v35\n" \
  "v_cvt_pk_f16_f32 v4, v8, v36\n" \
  "v_cvt_pk_f16_f32 v5, v9, v37\n" \
  "v_and_b32 v2, -2, v2\n" \
  "s_mov_b64 exec, s[92:93]\n" \
  "buffer_store_dwordx4 v[2:5], v231, s[36:39], s33 offen offset:0 sc1\n" \
  "s_mov_b64 exec, -1\n" \
  "s_mov_b32 s56, s48\n" \
  "s_waitcnt lgkmcnt(1)\n" \
  "buffer_store_dwordx4 v[10:13], v252, s[36:39], s24 offen offset:0 sc1\n" \
  "s_waitcnt lgkmcnt(0)\n" \
  "buffer_store_dwordx4 v[42:45], v252, s[36:39], s24 offen offset:1024 sc1\n" \
  "s_movk_i32 s32, 4096\n" \
  "buffer_load_dwordx4 v[2:5], v252, s[36:39], s32 offen offset:0 sc1\n" \
  "buffer_load_dwordx4 v[6:9], v252, s[36:39], s32 offen offset:1024 sc1\n" \
  "s_add_u32 s10, s87, 7405568\n" \
  "s_lshl_b32 s98, s89, 16\n" \
  "s_lshl_b32 s100, s20, 14\n" \
  "s_add_u32 s98, s98, s100\n" \
  "s_add_u32 s14, s98, 5308416\n" \
  "s_mul_i32 s98, s88, 4505600\n" \
  "s_lshl_b32 s100, s90, 2\n" \
  "s_lshr_b32 s101, s84, 1\n" \
  "s_add_u32 s100, s100, s101\n" \
  "s_lshl_b32 s100, s100, 12\n" \
  "s_add_u32 s98, s98, s100\n" \
  "s_add_u32 s16, s98, 13090816\n" \
  "s_mul_i32 s98, s20, 4896\n" \
  "s_add_u32 s21, s98, 9216\n" \
  "s_mul_i32 s27, s88, 9232\n" \
  "s_lshl_b32 s98, s20, 2\n" \
  "s_add_u32 s28, s98, s28\n" \
  "s_lshl_b32 s98, s84, 8\n" \
  "s_add_u32 s29, s98, 2048\n" \
  "s_lshl_b32 s98, s85, 3\n" \
  "s_add_u32 s30, s98, s29\n" \
  "s_cmp_lt_u32 s20, 2\n" \
  "s_cselect_b32 s9, 8192, 0\n" \
  "s_add_u32 s9, s9, s8\n" \
  "s_lshl_b32 s81, s88, 13\n" \
  "s_sub_u32 s15, s14, s81\n" \
  "s_add_u32 s14, s14, s81\n" \
  "s_mul_i32 s98, s84, 9\n" \
  "s_add_u32 s81, s98, s85\n" \
  "s_lshl_b32 s98, s81, 7\n" \
  "s_add_u32 s25, s98, 35840\n" \
  "s_cmp_lt_u32 s2, 72\n" \
  "s_cselect_b32 s66, 3981312, 0\n" \
  "s_lshl_b32 s33, s20, 10\n" \
  "s_lshl_b32 s98, s81, 6\n" \
  "s_add_u32 s63, s98, 2304\n" \
  "s_barrier\n" \
  "s_add_u32 m0, s33, 0\n" \
  "s_add_u32 s32, s63, 0\n" \
  "buffer_load_dwordx4 v245, s[64:67], s32 offen offset:0 lds\n" \
  "s_add_u32 m0, s33, 4096\n" \
  "s_add_u32 s32, s63, 442368\n" \
  "buffer_load_dwordx4 v245, s[64:67], s32 offen offset:0 lds\n" \
  "s_add_u32 m0, s33, 8192\n" \
  "s_add_u32 s32, s63, 884736\n" \
  "buffer_load_dwordx4 v245, s[64:67], s32 offen offset:0 lds\n" \
  "s_add_u32 m0, s33, 12288\n" \
  "s_add_u32 s32, s63, 1327104\n" \
  "buffer_load_dwordx4 v245, s[64:67], s32 offen offset:0 lds\n" \
  "s_add_u32 m0, s33, 16384\n" \
  "s_add_u32 s32, s63, 1769472\n" \
  "buffer_load_dwordx4 v245, s[64:67], s32 offen offset:0 lds\n" \
  "s_add_u32 m0, s33, 20480\n" \
  "s_add_u32 s32, s63, 2211840\n" \
  "buffer_load_dwordx4 v245, s[64:67], s32 offen offset:0 lds\n" \
  "s_add_u32 m0, s33, 24576\n" \
  "s_add_u32 s32, s63, 2654208\n" \
  "buffer_load_dwordx4 v245, s[64:67], s32 offen offset:0 lds\n" \
  "s_add_u32 m0, s33, 28672\n" \
  "s_add_u32 s32, s63, 3096576\n" \
  "buffer_load_dwordx4 v245, s[64:67], s32 offen offset:0 lds\n" \
  "s_add_u32 m0, s33, 32768\n" \
  "s_add_u32 s32, s63, 3538944\n" \
  "buffer_load_dwordx4 v245, s[64:67], s32 offen offset:0 lds\n" \
  "v_and_b32 v255, 1, v191\n" \
  "v_lshl_add_u32 v249, v253, 1, v255\n" \
  "v_lshlrev_b32 v255, 3, v249\n" \
  "v_and_b32 v249, 7, v191\n" \
  "v_lshlrev_b32 v241, 1, v249\n" \
  "v_and_b32 v249, 12, v191\n" \
  "v_add_u32 v245, v249, v244\n" \
  "s_lshl_b32 s98, s20, 4\n" \
  "v_add_u32 v249, s98, v245\n" \
  "v_lshlrev_b32 v245, 1, v249\n" \
  "v_bfe_u32 v249, v191, 1, 1\n" \
  "v_lshrrev_b32 v242, 2, v191\n" \
  "v_and_b32 v240, 12, v242\n" \
  "v_lshl_add_u32 v242, v249, 6, v240\n" \
  "s_lshr_b32 s98, s84, 1\n" \
  "s_lshl_b32 s98, s98, 4\n" \
  "s_add_u32 s98, s98, s20\n" \
  "v_add_u32 v249, s98, v242\n" \
  "v_lshlrev_b32 v242, 1, v249\n" \
  "s_mul_i32 s98, s84, 1152\n" \
  "s_and_b32 s100, s85, 7\n" \
  "s_lshl_b32 s100, s100, 7\n" \
  "s_add_u32 s98, s98, s100\n" \
  "s_lshl_b32 s100, s20, 5\n" \
  "s_add_u32 s60, s98, s100\n" \
  "s_mul_i32 s98, s84, 1152\n" \
  "s_and_b32 s100, s85, 6\n" \
  "s_lshl_b32 s100, s100, 4\n" \
  "s_add_u32 s98, s98, s100\n" \
  "s_lshr_b32 s100, s20, 1\n" \
  "s_lshl_b32 s100, s100, 4\n" \
  "s_add_u32 s98, s98, s100\n" \
  "s_add_u32 s61, s98, 1024\n" \
  "s_lshl_b32 s62, s90, 8\n" \
  "buffer_load_dwordx2 v[16:17], v255, s[68:71], s60 offen offset:0\n" \
  "buffer_load_ushort v255, v241, s[68:71], s61 offen offset:0\n" \
  "buffer_load_ushort v249, v245, s[72:75], s62 offen offset:0\n" \
  "buffer_load_ushort v241, v245, s[72:75], s62 offen offset:128\n" \
  "buffer_load_ushort v245, v242, s[72:75], s62 offen offset:0\n" \
  "s_add_u32 s98, s84, 1\n" \
  "s_lshr_b32 s98, s98, 3\n" \
  "s_sub_u32 s48, 14, s98\n" \
  "s_lshr_b32 s98, s4, 7\n" \
  "s_sub_u32 s98, 3, s98\n" \
  "s_sub_u32 s98, s98, s84\n" \
  "s_and_b32 s49, s98, 7\n" \
  "v_lshrrev_b32 v242, 5, v191\n" \
  "v_and_b32 v240, 31, v191\n" \
  "v_lshl_add_u32 v239, v242, 6, v240\n" \
  "v_mul_u32_u24 v242, 9216, v239\n" \
  "s_mul_i32 s98, s20, s48\n" \
  "s_add_u32 s50, s98, s85\n" \
  "s_mul_i32 s98, s50, 205\n" \
  "s_lshr_b32 s51, s98, 10\n" \
  "s_mul_i32 s98, s51, 5\n" \
  "s_sub_u32 s52, s50, s98\n" \
  "s_lshr_b32 s53, s50, 2\n" \
  "s_and_b32 s54, s50, 3\n" \
  "s_cmp_eq_u32 s84, 7\n" \
  "s_cselect_b32 s51, s51, s53\n" \
  "s_cselect_b32 s52, s52, s54\n" \
  "s_sub_u32 s53, 9, s52\n" \
  "s_cmp_lt_u32 s52, 2\n" \
  "s_cselect_b32 s53, s49, s53\n" \
  "s_cmp_eq_u32 s52, 0\n" \
  "s_cselect_b32 s54, 0, 294912\n" \
  "s_cmp_lt_u32 s51, 16\n" \
  "s_cselect_b64 s[68:69], -1, 0\n" \
  "s_mul_i32 s98, s51, 1179648\n" \
  "s_lshl_b32 s100, s53, 7\n" \
  "s_add_u32 s98, s98, s100\n" \
  "s_mul_i32 s100, s84, 1152\n" \
  "s_add_u32 s98, s98, s100\n" \
  "s_add_u32 s72, s98, s54\n" \
  "s_add_u32 s98, s20, 4\n" \
  "s_mul_i32 s98, s98, s48\n" \
  "s_add_u32 s50, s98, s85\n" \
  "s_mul_i32 s98, s50, 205\n" \
  "s_lshr_b32 s51, s98, 10\n" \
  "s_mul_i32 s98, s51, 5\n" \
  "s_sub_u32 s52, s50, s98\n" \
  "s_lshr_b32 s53, s50, 2\n" \
  "s_and_b32 s54, s50, 3\n" \
  "s_cmp_eq_u32 s84, 7\n" \
  "s_cselect_b32 s51, s51, s53\n" \
  "s_cselect_b32 s52, s52, s54\n" \
  "s_sub_u32 s53, 9, s52\n" \
  "s_cmp_lt_u32 s52, 2\n" \
  "s_cselect_b32 s53, s49, s53\n" \
  "s_cmp_eq_u32 s52, 0\n" \
  "s_cselect_b32 s54, 0, 294912\n" \
  "s_cmp_lt_u32 s51, 16\n" \
  "s_cselect_b64 s[70:71], -1, 0\n" \
  "s_mul_i32 s98, s51, 1179648\n" \
  "s_lshl_b32 s100, s53, 7\n" \
  "s_add_u32 s98, s98, s100\n" \
  "s_mul_i32 s100, s84, 1152\n" \
  "s_add_u32 s98, s98, s100\n" \
  "s_add_u32 s73, s98, s54\n" \
  "s_mov_b32 m0, 162176\n" \
  "s_mov_b64 exec, s[68:69]\n" \
  "buffer_load_dword v242, s[4:7], s72 offen lds\n" \
  "s_mov_b64 exec, s[70:71]\n" \
  "buffer_load_dword v242, s[4:7], s73 offen lds\n" \
  "s_mov_b64 exec, -1\n" \
  "v_and_b32 v242, 3, v191\n" \
  "v_bfe_u32 v240, v191, 2, 2\n" \
  "v_lshl_add_u32 v239, v244, 3, v240\n" \
  "s_lshr_b32 s98, s20, 1\n" \
  "s_mul_i32 s98, s98, 18432\n" \
  "s_and_b32 s100, s20, 1\n" \
  "s_lshl_b32 s100, s100, 5\n" \
  "s_add_u32 s98, s98, s100\n" \
  "v_lshl_add_u32 v240, v239, 6, s98\n" \
  "v_lshl_add_u32 v239, v242, 3, v240\n" \
  "s_lshr_b32 s98, s20, 1\n" \
  "s_mul_i32 s98, s98, 576\n" \
  "s_add_u32 s98, s98, 162432\n" \
  "v_lshl_add_u32 v242, v244, 4, s98\n" \
  "s_lshl_b32 s98, s20, 6\n" \
  "s_add_u32 s98, s98, 161152\n" \
  "v_lshl_add_u32 v244, v246, 2, s98\n" \
  "v_and_b32 v246, 7, v191\n" \
  "v_lshlrev_b32 v240, 4, v246\n" \
  "v_add_u32 v246, 161152, v240\n" \
  "v_lshlrev_b32 v240, 5, v191\n" \
  "v_lshlrev_b32 v238, 2, v191\n" \
  "v_and_b32 v240, 1920, v240\n" \
  "v_and_b32 v238, 96, v238\n" \
  "v_add3_u32 v240, v240, v238, s57\n" \
  "v_and_b32 v238, 3, v191\n" \
  "v_lshl_add_u32 v26, v238, 3, v240\n" \
  "v_xor_b32 v27, 32, v26\n" \
  "v_xor_b32 v28, 64, v26\n" \
  "v_xor_b32 v29, 96, v26\n" \
  "ds_read_b128 v[10:13], v242 offset:0\n" \
  "ds_read_b128 v[30:33], v242 offset:64\n" \
  "ds_read_b128 v[34:37], v242 offset:128\n" \
  "ds_read_b128 v[42:45], v242 offset:192\n" \
  "ds_read_b128 v[46:49], v242 offset:256\n" \
  "ds_read_b128 v[50:53], v242 offset:320\n" \
  "ds_read_b128 v[54:57], v242 offset:384\n" \
  "ds_read_b128 v[58:61], v242 offset:448\n" \
  "ds_read_b128 v[62:65], v242 offset:512\n" \
  "s_waitcnt vmcnt(7)\n" \
  "s_barrier\n" \
  "ds_read_b64_tr_b16 v[66:67], v239 offset:0\n" \
  "ds_read_b64_tr_b16 v[68:69], v239 offset:256\n" \
  "ds_read_b64_tr_b16 v[70:71], v239 offset:2048\n" \
  "ds_read_b64_tr_b16 v[72:73], v239 offset:2304\n" \
  "ds_read_b64_tr_b16 v[74:75], v239 offset:4096\n" \
  "ds_read_b64_tr_b16 v[76:77], v239 offset:4352\n" \
  "ds_read_b64_tr_b16 v[78:79], v239 offset:6144\n" \
  "ds_read_b64_tr_b16 v[80:81], v239 offset:6400\n" \
  "ds_read_b64_tr_b16 v[108:109], v239 offset:8192\n" \
  "ds_read_b64_tr_b16 v[110:111], v239 offset:8448\n" \
  "ds_read_b64_tr_b16 v[112:113], v239 offset:10240\n" \
  "ds_read_b64_tr_b16 v[114:115], v239 offset:10496\n" \
  "ds_read_b64_tr_b16 v[116:117], v239 offset:12288\n" \
  "ds_read_b64_tr_b16 v[118:119], v239 offset:12544\n" \
  "ds_read_b64_tr_b16 v[120:121], v239 offset:14336\n" \
  "ds_read_b64_tr_b16 v[122:123], v239 offset:14592\n" \
  "ds_read_b64_tr_b16 v[124:125], v239 offset:16384\n" \
  "ds_read_b64_tr_b16 v[126:127], v239 offset:16640\n" \
  "s_waitcnt lgkmcnt(15)\n" \
  "v_mfma_f32_16x16x32_bf16 v[128:131], v[10:13], v[66:69], 0\n" \
  "s_waitcnt lgkmcnt(14)\n" \
  "v_mfma_f32_16x16x32_bf16 v[128:131], v[30:33], v[70:73], v[128:131]\n" \
  "s_waitcnt lgkmcnt(12)\n" \
  "v_mfma_f32_16x16x32_bf16 v[128:131], v[34:37], v[74:77], v[128:131]\n" \
  "s_waitcnt lgkmcnt(10)\n" \
  "v_mfma_f32_16x16x32_bf16 v[128:131], v[42:45], v[78:81], v[128:131]\n" \
  "s_waitcnt lgkmcnt(8)\n" \
  "v_mfma_f32_16x16x32_bf16 v[128:131], v[46:49], v[108:111], v[128:131]\n" \
  "s_waitcnt lgkmcnt(6)\n" \
  "v_mfma_f32_16x16x32_bf16 v[128:131], v[50:53], v[112:115], v[128:131]\n" \
  "s_waitcnt lgkmcnt(4)\n" \
  "v_mfma_f32_16x16x32_bf16 v[128:131], v[54:57], v[116:119], v[128:131]\n" \
  "s_waitcnt lgkmcnt(2)\n" \
  "v_mfma_f32_16x16x32_bf16 v[128:131], v[58:61], v[120:123], v[128:131]\n" \
  "s_waitcnt lgkmcnt(0)\n" \
  "v_mfma_f32_16x16x32_bf16 v[128:131], v[62:65], v[124:127], v[128:131]\n" \
  "v_mov_b32 v10, s18\n" \
  "v_mov_b32 v11, s19\n" \
  "s_lshr_b32 s32, s84, 1\n" \
  "s_cmp_eq_u32 s32, 2\n" \
  "s_cselect_b32 s32, 1065353216, 0\n" \
  "s_cmp_lt_u32 s2, 72\n" \
  "s_cselect_b64 s[96:97], 255, 0\n" \
  "s_and_b64 s[96:97], s[96:97], s[58:59]\n" \
  "ds_write_b32 v244, v128 offset:0\n" \
  "s_waitcnt lgkmcnt(0)\n" \
  "s_barrier\n" \
  "ds_read_b128 v[30:33], v246 offset:0\n" \
  "ds_read_b128 v[34:37], v246 offset:128\n" \
  "s_waitcnt lgkmcnt(0)\n" \
  "v_pk_add_f32 v[30:31], v[30:31], v[34:35]\n" \
  "v_pk_add_f32 v[32:33], v[32:33], v[36:37]\n" \
  "v_cvt_pk_bf16_f32 v12, v30, v31\n" \
  "v_cvt_pk_bf16_f32 v13, v32, v33\n" \
  "v_lshlrev_b32 v30, 16, v12\n" \
  "v_and_b32 v31, 4294901760, v12\n" \
  "v_lshlrev_b32 v32, 16, v13\n" \
  "v_and_b32 v33, 4294901760, v13\n" \
  "v_add_f32 v30, s32, v30\n" \
  "v_add_f32 v31, s32, v31\n" \
  "v_add_f32 v32, s32, v32\n" \
  "v_add_f32 v33, s32, v33\n" \
  "v_cvt_pk_bf16_f32 v12, v30, v31\n" \
  "v_cvt_pk_bf16_f32 v13, v32, v33\n" \
  "s_mov_b64 exec, s[96:97]\n" \
  "buffer_store_dwordx4 v[10:13], v252, s[36:39], s25 offen offset:0 sc1\n" \
  "s_cmp_eq_u32 s20, 3\n" \
  "s_cselect_b64 exec, 1, 0\n" \
  "buffer_store_dwordx2 v[10:11], off, s[36:39], s30 sc1\n" \
  "s_mov_b64 exec, -1\n" \
  "s_cmp_lg_u32 s56, 0\n" \
  "s_cbranch_scc1 .Lc3s_%=\n" \
  ".Lc3d_%=:\n" \
  "s_add_u32 m0, s57, 8192\n" \
  "s_add_u32 s31, s12, 73728\n" \
  "buffer_load_dwordx4 v38, s[4:7], s31 offen offset:0 lds\n" \
  "buffer_load_dwordx4 v39, s[4:7], s31 offen offset:1024 lds\n" \
  "buffer_load_dwordx4 v40, s[4:7], s31 offen offset:2048 lds\n" \
  "buffer_load_dwordx4 v41, s[4:7], s31 offen offset:3072 lds\n" \
  "s_add_u32 m0, s57, 12288\n" \
  "s_add_u32 s31, s12, 9510912\n" \
  "buffer_load_dwordx4 v38, s[4:7], s31 offen offset:0 lds\n" \
  "buffer_load_dwordx4 v39, s[4:7], s31 offen offset:1024 lds\n" \
  "buffer_load_dwordx4 v40, s[4:7], s31 offen offset:2048 lds\n" \
  "buffer_load_dwordx4 v41, s[4:7], s31 offen offset:3072 lds\n" \
  "s_add_u32 m0, s57, 16384\n" \
  "s_add_u32 s31, s12, 147456\n" \
  "buffer_load_dwordx4 v38, s[4:7], s31 offen offset:0 lds\n" \
  "buffer_load_dwordx4 v39, s[4:7], s31 offen offset:1024 lds\n" \
  "buffer_load_dwordx4 v40, s[4:7], s31 offen offset:2048 lds\n" \
  "buffer_load_dwordx4 v41, s[4:7], s31 offen offset:3072 lds\n" \
  "s_add_u32 m0, s57, 20480\n" \
  "s_add_u32 s31, s12, 9584640\n" \
  "buffer_load_dwordx4 v38, s[4:7], s31 offen offset:0 lds\n" \
  "buffer_load_dwordx4 v39, s[4:7], s31 offen offset:1024 lds\n" \
  "buffer_load_dwordx4 v40, s[4:7], s31 offen offset:2048 lds\n" \
  "buffer_load_dwordx4 v41, s[4:7], s31 offen offset:3072 lds\n" \
  "s_waitcnt vmcnt(8)\n" \
  "ds_read_b64_tr_b16 a[0:1], v26 offset:0\n" \
  "ds_read_b64_tr_b16 a[4:5], v27 offset:0\n" \
  "ds_read_b64_tr_b16 a[8:9], v28 offset:0\n" \
  "ds_read_b64_tr_b16 a[12:13], v29 offset:0\n" \
  "ds_read_b64_tr_b16 a[20:21], v26 offset:2048\n" \
  "ds_read_b64_tr_b16 a[24:25], v27 offset:2048\n" \
  "ds_read_b64_tr_b16 a[28:29], v28 offset:2048\n" \
  "ds_read_b64_tr_b16 a[32:33], v29 offset:2048\n" \
  "ds_read_b64_tr_b16 a[2:3], v26 offset:4096\n" \
  "ds_read_b64_tr_b16 a[6:7], v27 offset:4096\n" \
  "ds_read_b64_tr_b16 a[10:11], v28 offset:4096\n" \
  "ds_read_b64_tr_b16 a[14:15], v29 offset:4096\n" \
  "ds_read_b64_tr_b16 a[22:23], v26 offset:6144\n" \
  "ds_read_b64_tr_b16 a[26:27], v27 offset:6144\n" \
  "ds_read_b64_tr_b16 a[30:31], v28 offset:6144\n" \
  "ds_read_b64_tr_b16 a[34:35], v29 offset:6144\n" \
  "s_add_u32 m0, s57, 0\n" \
  "s_add_u32 s31, s12, 221184\n" \
  "s_waitcnt lgkmcnt(0)\n" \
  "buffer_load_dwordx4 v38, s[4:7], s31 offen offset:0 lds\n" \
  "buffer_load_dwordx4 v39, s[4:7], s31 offen offset:1024 lds\n" \
  "buffer_load_dwordx4 v40, s[4:7], s31 offen offset:2048 lds\n" \
  "buffer_load_dwordx4 v41, s[4:7], s31 offen offset:3072 lds\n" \
  "s_add_u32 m0, s57, 4096\n" \
  "s_add_u32 s31, s12, 9658368\n" \
  "buffer_load_dwordx4 v38, s[4:7], s31 offen offset:0 lds\n" \
  "buffer_load_dwordx4 v39, s[4:7], s31 offen offset:1024 lds\n" \
  "buffer_load_dwordx4 v40, s[4:7], s31 offen offset:2048 lds\n" \
  "buffer_load_dwordx4 v41, s[4:7], s31 offen offset:3072 lds\n" \
  ".Lync_%=:\n" \
  "v_xor_b32 v246, s18, v2\n" \
  "v_bitop3_b32 v246, v4, s18, v246 bitop3:0xbe\n" \
  "v_bitop3_b32 v246, v6, s18, v246 bitop3:0xbe\n" \
  "v_bitop3_b32 v246, v8, s18, v246 bitop3:0xbe\n" \
  "v_xor_b32 v244, s19, v3\n" \
  "v_bitop3_b32 v244, v5, s19, v244 bitop3:0xbe\n" \
  "v_bitop3_b32 v244, v7, s19, v244 bitop3:0xbe\n" \
  "v_bitop3_b32 v244, v9, s19, v244 bitop3:0xbe\n" \
  "v_lshl_or_b32 v246, v244, 8, v246\n" \
  "v_cmp_ne_u32 vcc, 0, v246\n" \
  "s_cbranch_vccnz .Lyns_%=\n" \
  ".Lynd_%=:\n" \
  "s_or_b32 s32, s2, s20\n" \
  "s_cmp_eq_u32 s32, 0\n" \
  "s_cselect_b64 exec, 1, 0\n" \
  "buffer_store_dwordx2 v[10:11], off, s[36:39], 0 sc1\n" \
  "s_mov_b64 exec, -1\n" \
  "v_max3_u32 v246, v3, v5, v7\n" \
  "v_max_u32 v246, v246, v9\n" \
  "s_nop 1\n" \
  "v_max_u32_dpp v246, v246, v246 quad_perm:[1,0,3,2] row_mask:0xf bank_mask:0xf\n" \
  "s_nop 1\n" \
  "v_max_u32_dpp v246, v246, v246 quad_perm:[2,3,0,1] row_mask:0xf bank_mask:0xf\n" \
  "s_nop 1\n" \
  "v_max_u32_dpp v246, v246, v246 row_half_mirror row_mask:0xf bank_mask:0xf\n" \
  "s_nop 1\n" \
  "v_max_u32_dpp v246, v246, v246 row_mirror row_mask:0xf bank_mask:0xf\n" \
  "s_nop 0\n" \
  "v_readlane_b32 s48, v246, 0\n" \
  "v_readlane_b32 s49, v246, 16\n" \
  "v_readlane_b32 s50, v246, 32\n" \
  "v_readlane_b32 s51, v246, 48\n" \
  "s_max_u32 s48, s48, s49\n" \
  "s_max_u32 s50, s50, s51\n" \
  "s_max_u32 s48, s48, s50\n" \
  "s_lshr_b32 s48, s48, 24\n" \
  "s_max_u32 s48, s48, 64\n" \
  "s_min_u32 s48, s48, 250\n" \
  "s_add_u32 s23, s26, s48\n" \
  "s_sub_u32 s23, s23, 253\n" \
  "v_lshlrev_b32 v2, 16, v18\n" \
  "v_and_b32 v3, 4294901760, v18\n" \
  "v_lshlrev_b32 v4, 16, v19\n" \
  "v_and_b32 v5, 4294901760, v19\n" \
  "v_lshlrev_b32 v6, 16, v20\n" \
  "v_and_b32 v7, 4294901760, v20\n" \
  "v_lshlrev_b32 v30, 16, v21\n" \
  "v_and_b32 v31, 4294901760, v21\n" \
  "v_lshlrev_b32 v18, 16, v22\n" \
  "v_and_b32 v19, 4294901760, v22\n" \
  "v_lshlrev_b32 v20, 16, v23\n" \
  "v_and_b32 v21, 4294901760, v23\n" \
  "v_lshlrev_b32 v32, 16, v24\n" \
  "v_and_b32 v33, 4294901760, v24\n" \
  "v_lshlrev_b32 v34, 16, v25\n" \
  "v_and_b32 v35, 4294901760, v25\n" \
  "v_lshlrev_b32 v24, 16, v247\n" \
  "v_and_b32 v25, 4294901760, v247\n" \
  "v_and_b32 v42, 65535, v86\n" \
  "v_add_u32 v42, s21, v42\n" \
  "v_and_b32 v43, 65535, v87\n" \
  "v_add_u32 v43, s21, v43\n" \
  "v_and_b32 v44, 65535, v88\n" \
  "v_add_u32 v44, s21, v44\n" \
  "v_and_b32 v45, 65535, v89\n" \
  "v_add_u32 v45, s21, v45\n" \
  "v_lshrrev_b32 v46, 16, v86\n" \
  "v_add_u32 v46, s21, v46\n" \
  "v_lshrrev_b32 v47, 16, v87\n" \
  "v_add_u32 v47, s21, v47\n" \
  "v_lshrrev_b32 v48, 16, v88\n" \
  "v_add_u32 v48, s21, v48\n" \
  "v_lshrrev_b32 v49, 16, v89\n" \
  "v_add_u32 v49, s21, v49\n" \
  "v_mul_u32_u24 v247, 2448, v253\n" \
  "v_add_u32 v253, s21, v247\n" \
  "v_lshl_add_u32 v247, v254, 4, v253\n" \
  "v_lshl_add_u32 v246, v254, 2, v253\n" \
  "v_lshrrev_b32 v253, 3, v254\n" \
  "v_lshl_add_u32 v244, v253, 4, v247\n" \
  "v_lshrrev_b32 v253, 4, v191\n" \
  "v_lshl_add_u32 v242, v253, 2, s20\n" \
  "v_and_b32 v253, 3, v191\n" \
  "v_bfe_u32 v240, v191, 2, 2\n" \
  "v_mul_u32_u24 v239, 4896, v240\n" \
  "v_lshl_add_u32 v240, v253, 3, v239\n" \
  "v_lshl_add_u32 v253, v242, 5, v240\n" \
  "v_and_b32 v240, 15, v191\n" \
  "v_lshlrev_b32 v239, 4, v240\n" \
  "v_lshl_add_u32 v240, v242, 16, v239\n" \
  "v_lshlrev_b32 v242, 3, v254\n" \
  "s_lshr_b32 s98, s27, 8\n" \
  "s_and_b32 s98, s98, 255\n" \
  "v_subrev_u32 v239, s98, v0\n" \
  "v_bfrev_b32 v238, 1\n" \
  "v_cmp_gt_u32 s[96:97], 36, v239\n" \
  "v_cndmask_b32 v238, v238, v243, s[94:95]\n" \
  "s_cmp_eq_u32 s20, 1\n" \
  "s_cselect_b32 s92, 65537, s58\n" \
  "s_and_b32 s93, s59, 4294901758\n" \
  "ds_read_b64_tr_b16 a[40:41], v26 offset:8192\n" \
  "ds_read_b64_tr_b16 a[44:45], v27 offset:8192\n" \
  "ds_read_b64_tr_b16 a[48:49], v28 offset:8192\n" \
  "ds_read_b64_tr_b16 a[52:53], v29 offset:8192\n" \
  "ds_read_b64_tr_b16 a[60:61], v26 offset:10240\n" \
  "ds_read_b64_tr_b16 a[64:65], v27 offset:10240\n" \
  "ds_read_b64_tr_b16 a[68:69], v28 offset:10240\n" \
  "ds_read_b64_tr_b16 a[72:73], v29 offset:10240\n" \
  "ds_read_b64_tr_b16 a[42:43], v26 offset:12288\n" \
  "ds_read_b64_tr_b16 a[46:47], v27 offset:12288\n" \
  "ds_read_b64_tr_b16 a[50:51], v28 offset:12288\n" \
  "ds_read_b64_tr_b16 a[54:55], v29 offset:12288\n" \
  "ds_read_b64_tr_b16 a[62:63], v26 offset:14336\n" \
  "ds_read_b64_tr_b16 a[66:67], v27 offset:14336\n" \
  "ds_read_b64_tr_b16 a[70:71], v28 offset:14336\n" \
  "ds_read_b64_tr_b16 a[74:75], v29 offset:14336\n" \
  "s_add_u32 m0, s57, 8192\n" \
  "s_add_u32 s31, s13, 294912\n" \
  "s_waitcnt lgkmcnt(0)\n" \
  "buffer_load_dwordx4 v38, s[4:7], s31 offen offset:0 lds\n" \
  "buffer_load_dwordx4 v39, s[4:7], s31 offen offset:1024 lds\n" \
  "buffer_load_dwordx4 v40, s[4:7], s31 offen offset:2048 lds\n" \
  "buffer_load_dwordx4 v41, s[4:7], s31 offen offset:3072 lds\n" \
  "s_add_u32 m0, s57, 12288\n" \
  "s_add_u32 s31, s13, 9732096\n" \
  "buffer_load_dwordx4 v38, s[4:7], s31 offen offset:0 lds\n" \
  "buffer_load_dwordx4 v39, s[4:7], s31 offen offset:1024 lds\n" \
  "buffer_load_dwordx4 v40, s[4:7], s31 offen offset:2048 lds\n" \
  "buffer_load_dwordx4 v41, s[4:7], s31 offen offset:3072 lds\n" \
  "s_waitcnt vmcnt(17)\n" \
  "ds_read_b64_tr_b16 a[80:81], v26 offset:16384\n" \
  "ds_read_b64_tr_b16 a[84:85], v27 offset:16384\n" \
  "ds_read_b64_tr_b16 a[88:89], v28 offset:16384\n" \
  "ds_read_b64_tr_b16 a[92:93], v29 offset:16384\n" \
  "ds_read_b64_tr_b16 a[100:101], v26 offset:18432\n" \
  "ds_read_b64_tr_b16 a[104:105], v27 offset:18432\n" \
  "ds_read_b64_tr_b16 a[108:109], v28 offset:18432\n" \
  "ds_read_b64_tr_b16 a[112:113], v29 offset:18432\n" \
  "ds_read_b64_tr_b16 a[82:83], v26 offset:20480\n" \
  "ds_read_b64_tr_b16 a[86:87], v27 offset:20480\n" \
  "ds_read_b64_tr_b16 a[90:91], v28 offset:20480\n" \
  "ds_read_b64_tr_b16 a[94:95], v29 offset:20480\n" \
  "ds_read_b64_tr_b16 a[102:103], v26 offset:22528\n" \
  "ds_read_b64_tr_b16 a[106:107], v27 offset:22528\n" \
  "ds_read_b64_tr_b16 a[110:111], v28 offset:22528\n" \
  "ds_read_b64_tr_b16 a[114:115], v29 offset:22528\n" \
  "s_add_u32 m0, s57, 16384\n" \
  "s_add_u32 s31, s13, 368640\n" \
  "s_waitcnt lgkmcnt(0)\n" \
  "buffer_load_dwordx4 v38, s[4:7], s31 offen offset:0 lds\n" \
  "buffer_load_dwordx4 v39, s[4:7], s31 offen offset:1024 lds\n" \
  "buffer_load_dwordx4 v40, s[4:7], s31 offen offset:2048 lds\n" \
  "buffer_load_dwordx4 v41, s[4:7], s31 offen offset:3072 lds\n" \
  "s_add_u32 m0, s57, 20480\n" \
  "s_add_u32 s31, s13, 9805824\n" \
  "buffer_load_dwordx4 v38, s[4:7], s31 offen offset:0 lds\n" \
  "buffer_load_dwordx4 v39, s[4:7], s31 offen offset:1024 lds\n" \
  "buffer_load_dwordx4 v40, s[4:7], s31 offen offset:2048 lds\n" \
  "buffer_load_dwordx4 v41, s[4:7], s31 offen offset:3072 lds\n" \
  "v_lshrrev_b32 v239, 4, v191\n" \
  "v_and_b32 v237, 15, v191\n" \
  "v_lshlrev_b32 v236, 7, v237\n" \
  "v_lshl_add_u32 v237, v239, 12, v236\n" \
  "s_lshr_b32 s98, s2, 3\n" \
  "s_lshl_b32 s98, s98, 2\n" \
  "s_add_u32 s22, s98, s20\n" \
  "s_lshr_b32 s98, s2, 1\n" \
  "s_and_b32 s98, s98, 3\n" \
  "s_mul_i32 s98, s98, 1152\n" \
  "s_and_b32 s100, s22, 1\n" \
  "s_mul_i32 s100, s100, 576\n" \
  "s_add_u32 s98, s98, s100\n" \
  "s_and_b32 s100, s22, 2\n" \
  "s_lshl_b32 s100, s100, 7\n" \
  "s_add_u32 s98, s98, s100\n" \
  "s_and_b32 s100, s22, 60\n" \
  "s_add_u32 s98, s98, s100\n" \
  "s_lshl_b32 s98, s98, 12\n" \
  "s_and_b32 s100, s2, 1\n" \
  "s_lshl_b32 s100, s100, 11\n" \
  "s_add_u32 s22, s98, s100\n" \
  "s_lshr_b32 s0, s2, 6\n" \
  "s_lshr_b32 s1, s2, 1\n" \
  "s_and_b32 s34, s1, 3\n" \
  "s_lshr_b32 s1, s2, 3\n" \
  "s_and_b32 s84, s1, 1\n" \
  "s_and_b32 s1, s2, 7\n" \
  "s_lshl_b32 s1, s1, 2\n" \
  "s_lshr_b32 s100, s2, 4\n" \
  "s_and_b32 s100, s100, 3\n" \
  "s_add_u32 s88, s1, s100\n" \
  "s_and_b32 s1, s2, 1\n" \
  "s_lshl_b32 s1, s1, 3\n" \
  "s_lshr_b32 s100, s2, 3\n" \
  "s_and_b32 s100, s100, 7\n" \
  "s_add_u32 s85, s1, s100\n" \
  "s_mul_i32 s1, s0, 4505600\n" \
  "s_add_u32 s52, s1, 12123648\n" \
  "s_add_u32 s53, s52, 3276800\n" \
  "s_add_u32 s83, s52, 2048000\n" \
  "s_lshl_b32 s1, s0, 5\n" \
  "s_add_u32 s1, s1, s88\n" \
  "s_lshl_b32 s1, s1, 1\n" \
  "s_add_u32 s1, s1, s84\n" \
  "s_lshl_b32 s1, s1, 7\n" \
  "s_lshl_b32 s100, s20, 5\n" \
  "s_add_u32 s1, s1, s100\n" \
  "s_add_u32 s54, s1, 262144\n" \
  "s_lshl_b32 s1, s20, 5\n" \
  "s_sub_u32 s1, s54, s1\n" \
  "s_lshl_b32 s100, s34, 11\n" \
  "s_sub_u32 s1, s1, s100\n" \
  "s_add_u32 s16, s1, 65536\n" \
  "s_mov_b32 s55, 3184710631\n" \
  "s_mul_i32 s56, s20, 9568\n" \
  "s_lshl_b32 s58, s20, 13\n" \
  "s_lshr_b32 s1, s88, 1\n" \
  "s_mul_i32 s90, s1, 18\n" \
  "s_and_b32 s1, s88, 1\n" \
  "s_lshl_b32 s1, s1, 3\n" \
  "s_add_u32 s1, s90, s1\n" \
  "s_lshl_b32 s100, s84, 2\n" \
  "s_add_u32 s1, s1, s100\n" \
  "s_add_u32 s1, s1, s20\n" \
  "s_add_u32 s1, s1, 1\n" \
  "s_lshl_b32 s63, s1, 9\n" \
  "s_and_b32 s1, s88, 1\n" \
  "s_add_u32 s1, s90, s1\n" \
  "s_add_u32 s1, s1, 17\n" \
  "s_lshl_b32 s1, s1, 9\n" \
  "s_lshl_b32 s100, s20, 7\n" \
  "s_add_u32 s64, s1, s100\n" \
  "s_lshl_b32 s1, s0, 5\n" \
  "s_lshl_b32 s100, s34, 3\n" \
  "s_add_u32 s1, s1, s100\n" \
  "s_lshl_b32 s100, s20, 1\n" \
  "s_add_u32 s1, s1, s100\n" \
  "s_lshl_b32 s1, s1, 8\n" \
  "s_add_u32 s1, s1, 262144\n" \
  "s_sub_u32 s59, s1, s63\n" \
  "s_add_u32 s75, s59, 196608\n" \
  "s_lshl_b32 s1, s85, 7\n" \
  "s_mul_i32 s1, s1, 576\n" \
  "s_mul_i32 s100, s0, 144\n" \
  "s_add_u32 s90, s1, s100\n" \
  "s_mul_i32 s1, s20, 9216\n" \
  "s_add_u32 s1, s90, s1\n" \
  "s_lshl_b32 s100, s34, 5\n" \
  "s_add_u32 s1, s1, s100\n" \
  "s_lshl_b32 s61, s1, 1\n" \
  "s_lshl_b32 s1, s34, 4\n" \
  "s_add_u32 s1, s1, s20\n" \
  "s_mul_i32 s1, s1, 576\n" \
  "s_add_u32 s1, s90, s1\n" \
  "s_add_u32 s1, s1, 128\n" \
  "s_lshl_b32 s62, s1, 1\n" \
  "v_and_b32 v239, 768, v252\n" \
  "v_lshlrev_b32 v239, 2, v239\n" \
  "v_and_b32 v236, 192, v252\n" \
  "v_lshl_or_b32 v239, v236, 1, v239\n" \
  "v_bfe_u32 v236, v191, 3, 2\n" \
  "v_lshl_or_b32 v239, v236, 5, v239\n" \
  "v_and_b32 v235, 3, v191\n" \
  "v_lshl_or_b32 v239, v235, 3, v239\n" \
  "v_add_u32 v8, s57, v239\n" \
  "v_mov_b32 v63, v8\n" \
  "v_lshl_or_b32 v9, v241, 16, v249\n" \
  "v_and_b32 v239, 12, v191\n" \
  "v_lshrrev_b32 v236, 4, v191\n" \
  "v_add_u32 v236, v236, v239\n" \
  "v_mul_u32_u24 v236, 1152, v236\n" \
  "v_lshl_add_u32 v236, v235, 3, v236\n" \
  "v_add_u32 v10, s61, v236\n" \
  "v_and_b32 v235, 1, v191\n" \
  "v_lshl_add_u32 v239, v235, 1, v239\n" \
  "v_bfe_u32 v236, v191, 1, 1\n" \
  "v_lshlrev_b32 v236, 6, v236\n" \
  "v_lshrrev_b32 v11, 2, v191\n" \
  "v_and_or_b32 v236, v11, 12, v236\n" \
  "v_mul_u32_u24 v236, 1152, v236\n" \
  "v_lshl_add_u32 v236, v239, 1, v236\n" \
  "v_add_u32 v11, s62, v236\n" \
  "v_lshrrev_b32 v239, 3, v252\n" \
  "v_bitop3_b32 v239, v239, 32, v252 bitop3:0x6a\n" \
  "v_lshl_or_b32 v239, v239, 5, v239\n" \
  "v_and_b32 v12, 28784, v239\n" \
  "v_xor_b32 v239, 64, v12\n" \
  "v_add_u32 v13, 31744, v239\n" \
  "v_lshlrev_b32 v14, 16, v245\n" \
  "v_add_u32 v239, s57, v252\n" \
  "v_add_u32 v22, 28672, v239\n" \
  "v_lshlrev_b32 v52, 16, v16\n" \
  "v_and_b32 v53, 4294901760, v16\n" \
  "v_lshlrev_b32 v54, 16, v17\n" \
  "v_and_b32 v55, 4294901760, v17\n" \
  "v_lshlrev_b32 v56, 16, v255\n" \
  "v_mov_b32 v57, 3222493025\n" \
  "s_mul_i32 s32, s20, 1056\n" \
  "v_bfe_u32 v239, v191, 1, 1\n" \
  "v_mul_u32_u24 v239, 544, v239\n" \
  "v_lshl_add_u32 v239, v235, 4, v239\n" \
  "v_bfe_u32 v236, v191, 2, 1\n" \
  "v_lshl_add_u32 v239, v236, 8, v239\n" \
  "v_bfe_u32 v236, v191, 3, 2\n" \
  "v_lshl_add_u32 v239, v236, 6, v239\n" \
  "v_and_b32 v236, 32, v191\n" \
  "v_add3_u32 v58, v239, v236, s32\n" \
  "v_add_u32 v59, 524288, v252\n" \
  "v_bfe_u32 v239, v191, 3, 2\n" \
  "v_mul_u32_u24 v239, 288, v239\n" \
  "v_lshl_add_u32 v239, v236, 1, v239\n" \
  "v_and_b32 v236, 7, v191\n" \
  "v_lshl_add_u32 v239, v236, 2, v239\n" \
  "s_and_b32 s32, s20, 1\n" \
  "s_lshl_b32 s32, s32, 7\n" \
  "s_lshr_b32 s33, s20, 1\n" \
  "s_lshl_b32 s33, s33, 5\n" \
  "s_add_u32 s32, s32, s33\n" \
  "s_add_u32 s32, s32, 4224\n" \
  "v_add_u32 v60, s32, v239\n" \
  "v_lshl_add_u32 v61, v191, 3, s63\n" \
  "v_lshl_add_u32 v62, v191, 1, s64\n" \
  "s_mul_i32 s1, s34, 1152\n" \
  "s_lshr_b32 s100, s20, 1\n" \
  "s_mul_i32 s100, s100, 576\n" \
  "s_add_u32 s1, s1, s100\n" \
  "s_lshl_b32 s1, s1, 12\n" \
  "s_lshl_b32 s100, s85, 8\n" \
  "s_add_u32 s65, s1, s100\n" \
  "s_and_b32 s1, s20, 1\n" \
  "s_lshl_b32 s1, s1, 20\n" \
  "s_add_u32 s1, s65, s1\n" \
  "s_add_u32 s60, s1, 655488\n" \
  "s_and_b32 s1, s20, 1\n" \
  "s_lshl_b32 s1, s1, 17\n" \
  "s_add_u32 s1, s65, s1\n" \
  "s_add_u32 s65, s1, 2097152\n" \
  "s_add_u32 s66, s65, 128\n" \
  "s_add_u32 s61, s60, 130944\n" \
  "s_add_u32 s62, s60, 131072\n" \
  "s_add_u32 s63, s60, 262016\n" \
  "s_add_u32 s64, s60, 262144\n" \
  "s_lshl_b32 s1, s34, 7\n" \
  "s_add_u32 s67, s1, 163200\n" \
  "s_lshl_b32 s1, s34, 2\n" \
  "s_add_u32 s98, s1, s20\n" \
  "s_mul_i32 s1, s98, 9216\n" \
  "s_add_u32 s1, s1, s52\n" \
  "s_add_u32 s68, s1, 819712\n" \
  "s_add_u32 s69, s68, 3072\n" \
  "s_add_u32 s70, s68, 6144\n" \
  "s_lshl_b32 s1, s85, 14\n" \
  "s_add_u32 s1, s1, s52\n" \
  "s_add_u32 s90, s1, 967168\n" \
  "s_lshl_b32 s1, s98, 8\n" \
  "s_add_u32 s79, s1, s90\n" \
  "s_add_u32 s80, s79, 4096\n" \
  "s_add_u32 s81, s79, 8192\n" \
  "s_add_u32 s82, s79, 12288\n" \
  "s_lshl_b32 s99, s20, 10\n" \
  "s_add_u32 s87, s99, 65536\n" \
  "s_add_u32 s90, s90, s99\n" \
  "s_lshl_b32 s1, s2, 2\n" \
  "s_add_u32 s1, s1, s20\n" \
  "s_lshl_b32 s1, s1, 8\n" \
  "s_add_u32 s91, s1, 69206016\n" \
  "s_mul_i32 s1, s0, 589824\n" \
  "s_lshl_b32 s100, s20, 14\n" \
  "s_add_u32 s1, s1, s100\n" \
  "s_add_u32 s48, s1, 9764864\n" \
  "s_sub_u32 s1, s34, 1\n" \
  "s_and_b32 s1, s1, 3\n" \
  "s_lshl_b32 s1, s1, 12\n" \
  "s_add_u32 s98, s1, s90\n" \
  "s_add_u32 s71, s98, 4292919296\n" \
  "s_add_u32 s76, s98, 4293328896\n" \
  "s_sub_u32 s1, s34, 2\n" \
  "s_and_b32 s1, s1, 3\n" \
  "s_lshl_b32 s1, s1, 12\n" \
  "s_add_u32 s98, s1, s90\n" \
  "s_add_u32 s72, s98, 4293328896\n" \
  "s_add_u32 s77, s98, 4293328896\n" \
  "s_sub_u32 s1, s34, 3\n" \
  "s_and_b32 s1, s1, 3\n" \
  "s_lshl_b32 s1, s1, 12\n" \
  "s_add_u32 s98, s1, s90\n" \
  "s_add_u32 s73, s98, 4293328896\n" \
  "s_add_u32 s78, s98, 4293738496\n" \
  "s_and_b32 s1, s34, 3\n" \
  "s_lshl_b32 s1, s1, 12\n" \
  "s_add_u32 s98, s1, s90\n" \
  "s_add_u32 s74, s98, 4294148096\n" \
  "s_and_b32 s1, s0, 1\n" \
  "s_lshl_b32 s98, s1, 13\n" \
  "s_sub_u32 s1, s48, s98\n" \
  "s_add_u32 s50, s1, 8192\n" \
  "s_add_u32 s51, s50, 4096\n" \
  "s_add_u32 s48, s48, s98\n" \
  "s_add_u32 s49, s48, 4096\n" \
  "s_lshr_b32 s1, s2, 3\n" \
  "s_lshl_b32 s1, s1, 2\n" \
  "s_add_u32 s98, s1, s20\n" \
  "s_and_b32 s1, s98, 31\n" \
  "s_lshl_b32 s1, s1, 2\n" \
  "s_add_u32 s90, s1, 448\n" \
  "s_and_b32 s1, s98, 8\n" \
  "s_lshl_b32 s1, s1, 5\n" \
  "s_and_b32 s100, s98, 7\n" \
  "s_lshl_b32 s100, s100, 2\n" \
  "s_add_u32 s1, s1, s100\n" \
  "s_add_u32 s89, s1, 160\n" \
  "s_and_b32 s1, s98, 15\n" \
  "s_lshl_b32 s1, s1, 2\n" \
  "s_add_u32 s86, s1, 192\n" \
  "s_bitcmp1_b32 s98, 4\n" \
  "s_cselect_b32 s89, s89, s86\n" \
  "s_bitcmp1_b32 s98, 5\n" \
  "s_cselect_b32 s89, s90, s89\n" \
  "s_lshr_b32 s1, s98, 6\n" \
  "s_mul_i32 s1, s1, 576\n" \
  "s_mul_i32 s100, s34, 1152\n" \
  "s_add_u32 s1, s1, s100\n" \
  "s_add_u32 s1, s1, s89\n" \
  "s_lshl_b32 s1, s1, 12\n" \
  "s_and_b32 s100, s2, 1\n" \
  "s_lshl_b32 s100, s100, 11\n" \
  "s_add_u32 s1, s1, s100\n" \
  "s_lshr_b32 s100, s2, 3\n" \
  "s_and_b32 s100, s100, 3\n" \
  "s_add_u32 s1, s1, s100\n" \
  "s_add_u32 s89, s1, 1\n" \
  "s_sub_u32 s98, s57, s99\n" \
  "s_lshl_b32 s1, s34, 17\n" \
  "s_add_u32 s85, s1, 65536\n" \
  "s_mov_b32 s90, 7405568\n" \
  "s_add_u32 s1, s0, 3\n" \
  "s_and_b32 s1, s1, 3\n" \
  "s_lshl_b32 s88, s1, 12\n" \
  "s_mov_b32 s32, 35840\n" \
  "buffer_load_dwordx4 v[64:67], v238, s[36:39], s32 offen offset:0 sc1\n" \
  "buffer_load_dwordx4 v[68:71], v238, s[36:39], s32 offen offset:2304 sc1\n" \
  "s_mov_b32 s32, 40448\n" \
  "buffer_load_dwordx4 v[72:75], v238, s[36:39], s32 offen offset:0 sc1\n" \
  "buffer_load_dwordx4 v[76:79], v238, s[36:39], s32 offen offset:2304 sc1\n" \
  "s_add_u32 s32, s8, 4096\n" \
  "buffer_load_dwordx4 v[86:89], v231, s[36:39], s8 offen offset:0 sc1\n" \
  "buffer_load_dwordx4 v[108:111], v231, s[36:39], s32 offen offset:0 sc1\n" \
  "buffer_load_dwordx4 v[112:115], v248, s[36:39], s9 offen offset:0 sc1\n" \
  "buffer_load_dwordx2 v[16:17], v242, s[36:39], s29 offen offset:0 sc1\n" \
  "s_barrier\n" \
  "s_mov_b32 m0, 162176\n" \
  "s_nop 0\n" \
  "buffer_load_dword v237, s[40:43], s22 offen lds\n" \
  "s_add_u32 s22, s22, 229376\n" \
  "s_waitcnt vmcnt(5)\n" \
  ".Lmnc_%=:\n" \
  "v_xor_b32 v255, s18, v64\n" \
  "v_bitop3_b32 v255, v65, s19, v255 bitop3:0xbe\n" \
  "v_bitop3_b32 v255, v68, s18, v255 bitop3:0xbe\n" \
  "v_bitop3_b32 v255, v69, s19, v255 bitop3:0xbe\n" \
  "v_bitop3_b32 v255, v72, s18, v255 bitop3:0xbe\n" \
  "v_bitop3_b32 v255, v73, s19, v255 bitop3:0xbe\n" \
  "v_bitop3_b32 v255, v76, s18, v255 bitop3:0xbe\n" \
  "v_bitop3_b32 v255, v77, s19, v255 bitop3:0xbe\n" \
  "v_cmp_ne_u32 vcc, 0, v255\n" \
  "s_and_b64 vcc, vcc, s[94:95]\n" \
  "s_cbranch_scc1 .Lmns_%=\n" \
  ".Lmnd_%=:\n" \
  "v_lshlrev_b32 v116, 16, v66\n" \
  "v_and_b32 v117, 4294901760, v66\n" \
  "v_lshlrev_b32 v118, 16, v67\n" \
  "v_and_b32 v119, 4294901760, v67\n" \
  "v_lshlrev_b32 v64, 16, v70\n" \
  "v_and_b32 v65, 4294901760, v70\n" \
  "v_lshlrev_b32 v66, 16, v71\n" \
  "v_and_b32 v67, 4294901760, v71\n" \
  "v_lshlrev_b32 v68, 16, v74\n" \
  "v_and_b32 v69, 4294901760, v74\n" \
  "v_lshlrev_b32 v70, 16, v75\n" \
  "v_and_b32 v71, 4294901760, v75\n" \
  "v_lshlrev_b32 v72, 16, v78\n" \
  "v_and_b32 v73, 4294901760, v78\n" \
  "v_lshlrev_b32 v74, 16, v79\n" \
  "v_and_b32 v75, 4294901760, v79\n" \
  "v_mul_f32 v116, 1055100473, v116\n" \
  "v_ldexp_f32 v116, v116, s23\n" \
  "v_mul_f32 v117, 1055100473, v117\n" \
  "v_ldexp_f32 v117, v117, s23\n" \
  "v_mul_f32 v118, 1055100473, v118\n" \
  "v_ldexp_f32 v118, v118, s23\n" \
  "v_mul_f32 v119, 1055100473, v119\n" \
  "v_ldexp_f32 v119, v119, s23\n" \
  "s_mov_b64 exec, s[94:95]\n" \
  "ds_write_b128 v243, v[64:67] offset:31104\n" \
  "ds_write_b128 v243, v[68:71] offset:33408\n" \
  "ds_write_b128 v243, v[116:119] offset:28800\n" \
  "s_bfe_u32 s32, s27, 524296\n" \
  "s_lshl_b32 s32, s32, 4\n" \
  "s_sub_u32 s32, 163200, s32\n" \
  "v_add_u32 v255, s32, v243\n" \
  "s_mov_b64 exec, s[96:97]\n" \
  "ds_write_b128 v255, v[72:75] offset:0\n" \
  "s_mov_b64 exec, -1\n" \
  "s_waitcnt vmcnt(2)\n" \
  ".Lrnc_%=:\n" \
  "v_bitop3_b32 v255, v86, v108, v112 bitop3:0x80\n" \
  "v_and_b32 v255, 1, v255\n" \
  "v_cmp_eq_u32 vcc, 0, v255\n" \
  "s_cbranch_vccnz .Lrns_%=\n" \
  ".Lrnd_%=:\n" \
  "v_mfma_f32_16x16x32_f16 v[64:67], a[228:231], v[86:89], 0\n" \
  "v_mfma_f32_16x16x32_f16 v[68:71], a[232:235], v[86:89], 0\n" \
  "v_mfma_f32_16x16x32_f16 v[72:75], a[228:231], v[108:111], 0\n" \
  "v_mfma_f32_16x16x32_f16 v[76:79], a[232:235], v[108:111], 0\n" \
  "v_mfma_f32_16x16x32_f16 v[86:89], a[228:231], v[112:115], 0\n" \
  "v_mfma_f32_16x16x32_f16 v[108:111], a[232:235], v[112:115], 0\n" \
  "s_nop 3\n" \
  "v_cvt_pk_f16_f32 v112, v68, v64\n" \
  "v_cvt_pk_f16_f32 v113, v69, v65\n" \
  "v_cvt_pk_f16_f32 v114, v70, v66\n" \
  "v_cvt_pk_f16_f32 v115, v71, v67\n" \
  "ds_write_b128 v83, v[112:115] offset:0\n" \
  "v_cvt_pk_f16_f32 v64, v76, v72\n" \
  "v_cvt_pk_f16_f32 v65, v77, v73\n" \
  "v_cvt_pk_f16_f32 v66, v78, v74\n" \
  "v_cvt_pk_f16_f32 v67, v79, v75\n" \
  "ds_write_b128 v84, v[64:67] offset:0\n" \
  "v_cvt_pk_f16_f32 v64, v108, v86\n" \
  "v_cvt_pk_f16_f32 v65, v109, v87\n" \
  "v_cvt_pk_f16_f32 v66, v110, v88\n" \
  "v_cvt_pk_f16_f32 v67, v111, v89\n" \
  "s_mov_b64 exec, s[92:93]\n" \
  "ds_write_b128 v85, v[64:67] offset:0\n" \
  "s_mov_b64 exec, -1\n" \
  "s_waitcnt lgkmcnt(0)\n" \
  "s_barrier\n" \
  "ds_read_b64 v[36:37], v82 offset:0\n" \
  "ds_read_b64 v[50:51], v82 offset:512\n" \
  "ds_read_b64 v[64:65], v82 offset:1024\n" \
  "ds_read_b64 v[66:67], v82 offset:256\n" \
  "ds_read_b64 v[68:69], v82 offset:768\n" \
  "ds_read_b64 v[70:71], v82 offset:1280\n" \
  "v_lshlrev_b32 v255, 5, v254\n" \
  "s_waitcnt lgkmcnt(5)\n" \
  "v_mov_b32 v92, v36\n" \
  "s_waitcnt lgkmcnt(4)\n" \
  "v_mov_b32 v93, v50\n" \
  "s_waitcnt lgkmcnt(3)\n" \
  "v_mov_b32 v94, v64\n" \
  "v_mov_b32 v100, v37\n" \
  "v_mov_b32 v101, v51\n" \
  "v_mfma_f32_16x16x32_f16 v[72:75], v[92:95], a[208:211], 0\n" \
  "v_mov_b32 v102, v65\n" \
  "s_waitcnt lgkmcnt(2)\n" \
  "v_mov_b32 v96, v66\n" \
  "v_mfma_f32_16x16x32_f16 v[76:79], v[92:95], a[212:215], 0\n" \
  "s_waitcnt lgkmcnt(1)\n" \
  "v_mov_b32 v97, v68\n" \
  "s_waitcnt lgkmcnt(0)\n" \
  "v_mov_b32 v98, v70\n" \
  "v_mfma_f32_16x16x32_f16 v[80:83], v[100:103], a[208:211], 0\n" \
  "v_mov_b32 v104, v67\n" \
  "v_mov_b32 v105, v69\n" \
  "v_mfma_f32_16x16x32_f16 v[64:67], v[100:103], a[212:215], 0\n" \
  "v_mov_b32 v106, v71\n" \
  "v_mfma_f32_16x16x32_f16 v[68:71], v[96:99], a[208:211], 0\n" \
  "ds_read_b128 v[84:87], v255 offset:31104\n" \
  "ds_read_b128 v[88:91], v255 offset:31120\n" \
  "v_mfma_f32_16x16x32_f16 v[92:95], v[96:99], a[212:215], 0\n" \
  "ds_read_b128 v[96:99], v255 offset:32128\n" \
  "ds_read_b128 v[100:103], v255 offset:32144\n" \
  "v_mfma_f32_16x16x32_f16 v[108:111], v[104:107], a[208:211], 0\n" \
  "ds_read_b64 v[36:37], v242 offset:33152\n" \
  "v_mfma_f32_16x16x32_f16 v[112:115], v[104:107], a[212:215], 0\n" \
  "ds_read_b128 v[104:107], v255 offset:28800\n" \
  "ds_read_b128 v[116:119], v255 offset:28816\n" \
  "ds_read_b128 v[120:123], v255 offset:29824\n" \
  "ds_read_b128 v[124:127], v255 offset:29840\n" \
  "ds_read_b64 v[50:51], v242 offset:30848\n" \
  "v_pk_add_f32 v[128:129], v[72:73], v[68:69]\n" \
  "v_pk_add_f32 v[130:131], v[74:75], v[70:71]\n" \
  "v_pk_add_f32 v[132:133], v[76:77], v[92:93]\n" \
  "v_pk_add_f32 v[134:135], v[78:79], v[94:95]\n" \
  "v_pk_add_f32 v[136:137], v[72:73], v[68:69] neg_lo:[0,1] neg_hi:[0,1]\n" \
  "v_pk_add_f32 v[138:139], v[74:75], v[70:71] neg_lo:[0,1] neg_hi:[0,1]\n" \
  "v_pk_add_f32 v[68:69], v[76:77], v[92:93] neg_lo:[0,1] neg_hi:[0,1]\n" \
  "v_pk_add_f32 v[70:71], v[78:79], v[94:95] neg_lo:[0,1] neg_hi:[0,1]\n" \
  "v_pk_add_f32 v[72:73], v[80:81], v[108:109]\n" \
  "v_pk_add_f32 v[74:75], v[82:83], v[110:111]\n" \
  "v_pk_add_f32 v[76:77], v[64:65], v[112:113]\n" \
  "v_pk_add_f32 v[78:79], v[66:67], v[114:115]\n" \
  "v_pk_add_f32 v[92:93], v[80:81], v[108:109] neg_lo:[0,1] neg_hi:[0,1]\n" \
  "v_pk_add_f32 v[94:95], v[82:83], v[110:111] neg_lo:[0,1] neg_hi:[0,1]\n" \
  "v_pk_add_f32 v[80:81], v[64:65], v[112:113] neg_lo:[0,1] neg_hi:[0,1]\n" \
  "v_pk_add_f32 v[82:83], v[66:67], v[114:115] neg_lo:[0,1] neg_hi:[0,1]\n" \
  "v_cvt_pk_f16_f32 v64, v128, v132\n" \
  "v_cvt_pk_f16_f32 v65, v129, v133\n" \
  "v_cvt_pk_f16_f32 v66, v130, v134\n" \
  "v_cvt_pk_f16_f32 v67, v131, v135\n" \
  "v_cvt_pk_f16_f32 v108, v136, v68\n" \
  "v_cvt_pk_f16_f32 v109, v137, v69\n" \
  "v_mfma_f32_16x16x32_f16 v[112:115], a[200:203], v[64:67], 0\n" \
  "v_cvt_pk_f16_f32 v110, v138, v70\n" \
  "v_cvt_pk_f16_f32 v111, v139, v71\n" \
  "v_mfma_f32_16x16x32_f16 v[68:71], a[204:207], v[64:67], 0\n" \
  "v_cvt_pk_f16_f32 v64, v72, v76\n" \
  "v_cvt_pk_f16_f32 v65, v73, v77\n" \
  "v_mfma_f32_16x16x32_f16 v[128:131], a[220:223], v[108:111], 0\n" \
  "v_cvt_pk_f16_f32 v66, v74, v78\n" \
  "v_cvt_pk_f16_f32 v67, v75, v79\n" \
  "v_mfma_f32_16x16x32_f16 v[72:75], a[224:227], v[108:111], 0\n" \
  "v_cvt_pk_f16_f32 v76, v92, v80\n" \
  "v_cvt_pk_f16_f32 v77, v93, v81\n" \
  "v_mfma_f32_16x16x32_f16 v[108:111], a[200:203], v[64:67], 0\n" \
  "v_cvt_pk_f16_f32 v78, v94, v82\n" \
  "v_cvt_pk_f16_f32 v79, v95, v83\n" \
  "v_mfma_f32_16x16x32_f16 v[80:83], a[204:207], v[64:67], 0\n" \
  "ds_read_b128 v[64:67], v255 offset:33408\n" \
  "v_mfma_f32_16x16x32_f16 v[92:95], a[220:223], v[76:79], 0\n" \
  "ds_read_b128 v[132:135], v255 offset:33424\n" \
  "v_mfma_f32_16x16x32_f16 v[136:139], a[224:227], v[76:79], 0\n" \
  "ds_read_b128 v[76:79], v255 offset:34432\n" \
  "ds_read_b128 v[140:143], v255 offset:34448\n" \
  "ds_read_b64 v[144:145], v242 offset:35456\n" \
  "s_waitcnt lgkmcnt(14)\n" \
  "v_readfirstlane_b32 s32, v84\n" \
  "s_mov_b32 exec_lo, 125241207\n" \
  "s_mov_b32 exec_hi, 125241207\n" \
  "v_cvt_pk_f16_f32 v255, v68, v112\n" \
  "ds_write_b32 v42, v255 offset:0\n" \
  "v_cvt_pk_f16_f32 v255, v69, v113\n" \
  "ds_write_b32 v43, v255 offset:0\n" \
  "v_cvt_pk_f16_f32 v255, v70, v114\n" \
  "ds_write_b32 v44, v255 offset:0\n" \
  "v_cvt_pk_f16_f32 v255, v71, v115\n" \
  "ds_write_b32 v45, v255 offset:0\n" \
  "v_cvt_pk_f16_f32 v255, v72, v128\n" \
  "ds_write_b32 v46, v255 offset:0\n" \
  "v_cvt_pk_f16_f32 v255, v73, v129\n" \
  "ds_write_b32 v47, v255 offset:0\n" \
  "v_cvt_pk_f16_f32 v255, v74, v130\n" \
  "ds_write_b32 v48, v255 offset:0\n" \
  "v_cvt_pk_f16_f32 v255, v75, v131\n" \
  "ds_write_b32 v49, v255 offset:0\n" \
  "v_cvt_pk_f16_f32 v255, v80, v108\n" \
  "ds_write_b32 v42, v255 offset:2448\n" \
  "v_cvt_pk_f16_f32 v255, v81, v109\n" \
  "ds_write_b32 v43, v255 offset:2448\n" \
  "v_cvt_pk_f16_f32 v255, v82, v110\n" \
  "ds_write_b32 v44, v255 offset:2448\n" \
  "v_cvt_pk_f16_f32 v255, v83, v111\n" \
  "ds_write_b32 v45, v255 offset:2448\n" \
  "v_cvt_pk_f16_f32 v255, v136, v92\n" \
  "ds_write_b32 v46, v255 offset:2448\n" \
  "v_cvt_pk_f16_f32 v255, v137, v93\n" \
  "ds_write_b32 v47, v255 offset:2448\n" \
  "v_cvt_pk_f16_f32 v255, v138, v94\n" \
  "ds_write_b32 v48, v255 offset:2448\n" \
  "v_cvt_pk_f16_f32 v255, v139, v95\n" \
  "ds_write_b32 v49, v255 offset:2448\n" \
  "s_mov_b64 exec, -1\n" \
  "ds_read_b128 v[42:45], v244 offset:0\n" \
  "ds_read_b128 v[46:49], v244 offset:576\n" \
  "ds_read_b32 v255, v246 offset:1152\n" \
  "v_mov_b32 v68, s32\n" \
  "v_pk_add_f32 v[84:85], v[84:85], v[68:69] op_sel_hi:[1,0] neg_lo:[0,1] neg_hi:[0,1]\n" \
  "v_pk_add_f32 v[86:87], v[86:87], v[68:69] op_sel_hi:[1,0] neg_lo:[0,1] neg_hi:[0,1]\n" \
  "s_waitcnt lgkmcnt(15)\n" \
  "v_pk_add_f32 v[88:89], v[88:89], v[68:69] op_sel_hi:[1,0] neg_lo:[0,1] neg_hi:[0,1]\n" \
  "v_pk_add_f32 v[90:91], v[90:91], v[68:69] op_sel_hi:[1,0] neg_lo:[0,1] neg_hi:[0,1]\n" \
  "v_pk_add_f32 v[96:97], v[96:97], v[68:69] op_sel_hi:[1,0] neg_lo:[0,1] neg_hi:[0,1]\n" \
  "v_pk_add_f32 v[98:99], v[98:99], v[68:69] op_sel_hi:[1,0] neg_lo:[0,1] neg_hi:[0,1]\n" \
  "v_pk_add_f32 v[100:101], v[100:101], v[68:69] op_sel_hi:[1,0] neg_lo:[0,1] neg_hi:[0,1]\n" \
  "v_pk_add_f32 v[102:103], v[102:103], v[68:69] op_sel_hi:[1,0] neg_lo:[0,1] neg_hi:[0,1]\n" \
  "v_pk_add_f32 v[36:37], v[36:37], v[68:69] op_sel_hi:[1,0] neg_lo:[0,1] neg_hi:[0,1]\n" \
  "s_waitcnt lgkmcnt(2)\n" \
  "v_fma_mix_f32 v2, v42, v104, v2 op_sel:[0,0,0] op_sel_hi:[1,0,0]\n" \
  "v_fma_mix_f32 v3, v42, v105, v3 op_sel:[1,0,0] op_sel_hi:[1,0,0]\n" \
  "v_fma_mix_f32 v4, v43, v106, v4 op_sel:[0,0,0] op_sel_hi:[1,0,0]\n" \
  "v_fma_mix_f32 v5, v43, v107, v5 op_sel:[1,0,0] op_sel_hi:[1,0,0]\n" \
  "v_fma_mix_f32 v6, v44, v116, v6 op_sel:[0,0,0] op_sel_hi:[1,0,0]\n" \
  "v_fma_mix_f32 v7, v44, v117, v7 op_sel:[1,0,0] op_sel_hi:[1,0,0]\n" \
  "v_fma_mix_f32 v30, v45, v118, v30 op_sel:[0,0,0] op_sel_hi:[1,0,0]\n" \
  "v_fma_mix_f32 v31, v45, v119, v31 op_sel:[1,0,0] op_sel_hi:[1,0,0]\n" \
  "s_waitcnt lgkmcnt(1)\n" \
  "v_fma_mix_f32 v18, v46, v120, v18 op_sel:[0,0,0] op_sel_hi:[1,0,0]\n" \
  "v_fma_mix_f32 v19, v46, v121, v19 op_sel:[1,0,0] op_sel_hi:[1,0,0]\n" \
  "v_fma_mix_f32 v20, v47, v122, v20 op_sel:[0,0,0] op_sel_hi:[1,0,0]\n" \
  "v_fma_mix_f32 v21, v47, v123, v21 op_sel:[1,0,0] op_sel_hi:[1,0,0]\n" \
  "v_fma_mix_f32 v32, v48, v124, v32 op_sel:[0,0,0] op_sel_hi:[1,0,0]\n" \
  "v_fma_mix_f32 v33, v48, v125, v33 op_sel:[1,0,0] op_sel_hi:[1,0,0]\n" \
  "v_fma_mix_f32 v34, v49, v126, v34 op_sel:[0,0,0] op_sel_hi:[1,0,0]\n" \
  "v_fma_mix_f32 v35, v49, v127, v35 op_sel:[1,0,0] op_sel_hi:[1,0,0]\n" \
  "s_waitcnt lgkmcnt(0)\n" \
  "v_fma_mix_f32 v24, v255, v50, v24 op_sel:[0,0,0] op_sel_hi:[1,0,0]\n" \
  "v_fma_mix_f32 v25, v255, v51, v25 op_sel:[1,0,0] op_sel_hi:[1,0,0]\n" \
  "v_mov_b32 v43, 925353388\n" \
  "v_mov_b32_dpp v44, v2 row_newbcast:0 row_mask:0xf bank_mask:0xf\n" \
  "v_mov_b32_dpp v255, v2 row_newbcast:0 row_mask:0xf bank_mask:0xf\n" \
  "v_cvt_pk_bf16_f32 v46, v2, v3\n" \
  "v_cvt_pk_bf16_f32 v47, v4, v5\n" \
  "v_cvt_pk_bf16_f32 v48, v6, v7\n" \
  "v_cvt_pk_bf16_f32 v49, v30, v31\n" \
  "v_cvt_pk_bf16_f32 v68, v18, v19\n" \
  "v_cvt_pk_bf16_f32 v69, v20, v21\n" \
  "v_cvt_pk_bf16_f32 v70, v32, v33\n" \
  "v_cvt_pk_bf16_f32 v71, v34, v35\n" \
  "v_cvt_pk_bf16_f32 v254, v24, v25\n" \
  "v_permlane16_swap_b32 v44, v255\n" \
  "v_pk_add_f32 v[2:3], v[2:3], v[44:45] op_sel_hi:[1,0] neg_lo:[0,1] neg_hi:[0,1]\n" \
  "v_pk_add_f32 v[4:5], v[4:5], v[44:45] op_sel_hi:[1,0] neg_lo:[0,1] neg_hi:[0,1]\n" \
  "v_pk_add_f32 v[6:7], v[6:7], v[44:45] op_sel_hi:[1,0] neg_lo:[0,1] neg_hi:[0,1]\n" \
  "v_pk_add_f32 v[30:31], v[30:31], v[44:45] op_sel_hi:[1,0] neg_lo:[0,1] neg_hi:[0,1]\n" \
  "v_pk_add_f32 v[18:19], v[18:19], v[44:45] op_sel_hi:[1,0] neg_lo:[0,1] neg_hi:[0,1]\n" \
  "v_pk_add_f32 v[20:21], v[20:21], v[44:45] op_sel_hi:[1,0] neg_lo:[0,1] neg_hi:[0,1]\n" \
  "v_pk_add_f32 v[32:33], v[32:33], v[44:45] op_sel_hi:[1,0] neg_lo:[0,1] neg_hi:[0,1]\n" \
  "v_pk_add_f32 v[34:35], v[34:35], v[44:45] op_sel_hi:[1,0] neg_lo:[0,1] neg_hi:[0,1]\n" \
  "v_pk_add_f32 v[24:25], v[24:25], v[44:45] op_sel_hi:[1,0] neg_lo:[0,1] neg_hi:[0,1]\n" \
  "v_pk_add_f32 v[44:45], v[2:3], v[4:5]\n" \
  "v_pk_mul_f32 v[50:51], v[2:3], v[2:3]\n" \
  "v_pk_fma_f32 v[50:51], v[4:5], v[4:5], v[50:51]\n" \
  "v_pk_add_f32 v[44:45], v[44:45], v[6:7]\n" \
  "v_pk_fma_f32 v[50:51], v[6:7], v[6:7], v[50:51]\n" \
  "v_pk_add_f32 v[44:45], v[44:45], v[30:31]\n" \
  "v_pk_fma_f32 v[50:51], v[30:31], v[30:31], v[50:51]\n" \
  "v_pk_add_f32 v[44:45], v[44:45], v[18:19]\n" \
  "v_pk_fma_f32 v[50:51], v[18:19], v[18:19], v[50:51]\n" \
  "v_pk_add_f32 v[44:45], v[44:45], v[20:21]\n" \
  "v_pk_fma_f32 v[50:51], v[20:21], v[20:21], v[50:51]\n" \
  "v_pk_add_f32 v[44:45], v[44:45], v[32:33]\n" \
  "v_pk_fma_f32 v[50:51], v[32:33], v[32:33], v[50:51]\n" \
  "v_pk_add_f32 v[44:45], v[44:45], v[34:35]\n" \
  "v_pk_fma_f32 v[50:51], v[34:35], v[34:35], v[50:51]\n" \
  "v_pk_add_f32 v[44:45], v[44:45], v[24:25]\n" \
  "v_pk_fma_f32 v[50:51], v[24:25], v[24:25], v[50:51]\n" \
  "v_add_f32 v255, v44, v45\n" \
  "v_add_f32 v249, v50, v51\n" \
  "ds_read_b64_tr_b16 a[120:121], v26 offset:0\n" \
  "ds_read_b64_tr_b16 a[124:125], v27 offset:0\n" \
  "ds_read_b64_tr_b16 a[128:129], v28 offset:0\n" \
  "ds_read_b64_tr_b16 a[132:133], v29 offset:0\n" \
  "v_permlane16_swap_b32 v255, v249\n" \
  "v_add_f32 v255, v255, v249\n" \
  "buffer_store_dwordx4 v[46:49], v251, s[36:39], s10 offen offset:0 sc1\n" \
  "ds_read_b64_tr_b16 a[140:141], v26 offset:2048\n" \
  "ds_read_b64_tr_b16 a[144:145], v27 offset:2048\n" \
  "ds_read_b64_tr_b16 a[148:149], v28 offset:2048\n" \
  "v_add_f32_dpp v255, v255, v255 quad_perm:[1,0,3,2] row_mask:0xf bank_mask:0xf\n" \
  "buffer_store_dwordx4 v[68:71], v251, s[36:39], s10 offen offset:512 sc1\n" \
  "ds_read_b64_tr_b16 a[152:153], v29 offset:2048\n" \
  "ds_read_b64_tr_b16 a[122:123], v26 offset:4096\n" \
  "ds_read_b64_tr_b16 a[126:127], v27 offset:4096\n" \
  "v_add_f32_dpp v255, v255, v255 quad_perm:[2,3,0,1] row_mask:0xf bank_mask:0xf\n" \
  "buffer_store_dword v254, v250, s[36:39], s10 offen offset:1024 sc1\n" \
  "ds_read_b64_tr_b16 a[130:131], v28 offset:4096\n" \
  "ds_read_b64_tr_b16 a[134:135], v29 offset:4096\n" \
  "ds_read_b64_tr_b16 a[142:143], v26 offset:6144\n" \
  "v_add_f32_dpp v255, v255, v255 row_half_mirror row_mask:0xf bank_mask:0xf\n" \
  "ds_read_b64_tr_b16 a[146:147], v27 offset:6144\n" \
  "ds_read_b64_tr_b16 a[150:151], v28 offset:6144\n" \
  "ds_read_b64_tr_b16 a[154:155], v29 offset:6144\n" \
  "v_add_f32_dpp v255, v255, v255 row_mirror row_mask:0xf bank_mask:0xf\n" \
  "v_mov_b32 v249, v255\n" \
  "ds_read_b64_tr_b16 a[160:161], v26 offset:8192\n" \
  "ds_read_b64_tr_b16 a[164:165], v27 offset:8192\n" \
  "ds_read_b64_tr_b16 a[168:169], v28 offset:8192\n" \
  "ds_read_b64_tr_b16 a[172:173], v29 offset:8192\n" \
  "v_permlane16_swap_b32 v255, v249\n" \
  "v_mul_f32 v255, s35, v255\n" \
  "v_fma_f32 v249, v249, s35, v43\n" \
  "v_fma_f32 v249, -v255, v255, v249\n" \
  "v_max_f32 v249, v249, v43\n" \
  "v_rsq_f32 v42, v249\n" \
  "ds_read_b64_tr_b16 a[180:181], v26 offset:10240\n" \
  "ds_read_b64_tr_b16 a[184:185], v27 offset:10240\n" \
  "ds_read_b64_tr_b16 a[188:189], v28 offset:10240\n" \
  "ds_read_b64_tr_b16 a[192:193], v29 offset:10240\n" \
  "v_mul_f32_e64 v43, -v255, v42\n" \
  "v_pk_fma_f32 v[2:3], v[2:3], v[42:43], v[42:43] op_sel:[0,0,1] op_sel_hi:[1,0,1]\n" \
  "v_pk_fma_f32 v[2:3], v[2:3], v[64:65], v[84:85]\n" \
  "v_pk_fma_f32 v[4:5], v[4:5], v[42:43], v[42:43] op_sel:[0,0,1] op_sel_hi:[1,0,1]\n" \
  "v_pk_fma_f32 v[4:5], v[4:5], v[66:67], v[86:87]\n" \
  "v_pk_fma_f32 v[6:7], v[6:7], v[42:43], v[42:43] op_sel:[0,0,1] op_sel_hi:[1,0,1]\n" \
  "v_pk_fma_f32 v[6:7], v[6:7], v[132:133], v[88:89]\n" \
  "ds_read_b64_tr_b16 a[162:163], v26 offset:12288\n" \
  "ds_read_b64_tr_b16 a[166:167], v27 offset:12288\n" \
  "s_add_u32 m0, s57, 0\n" \
  "s_add_u32 s31, s13, 442368\n" \
  "s_waitcnt lgkmcnt(10)\n" \
  "buffer_load_dwordx4 v38, s[4:7], s31 offen offset:0 lds\n" \
  "v_pk_fma_f32 v[30:31], v[30:31], v[42:43], v[42:43] op_sel:[0,0,1] op_sel_hi:[1,0,1]\n" \
  "v_pk_fma_f32 v[30:31], v[30:31], v[134:135], v[90:91]\n" \
  "v_pk_fma_f32 v[18:19], v[18:19], v[42:43], v[42:43] op_sel:[0,0,1] op_sel_hi:[1,0,1]\n" \
  "v_pk_fma_f32 v[18:19], v[18:19], v[76:77], v[96:97]\n" \
  "ds_read_b64_tr_b16 a[170:171], v28 offset:12288\n" \
  "ds_read_b64_tr_b16 a[174:175], v29 offset:12288\n" \
  "buffer_load_dwordx4 v39, s[4:7], s31 offen offset:1024 lds\n" \
  "v_pk_fma_f32 v[20:21], v[20:21], v[42:43], v[42:43] op_sel:[0,0,1] op_sel_hi:[1,0,1]\n" \
  "v_pk_fma_f32 v[20:21], v[20:21], v[78:79], v[98:99]\n" \
  "v_pk_fma_f32 v[32:33], v[32:33], v[42:43], v[42:43] op_sel:[0,0,1] op_sel_hi:[1,0,1]\n" \
  "v_pk_fma_f32 v[32:33], v[32:33], v[140:141], v[100:101]\n" \
  "ds_read_b64_tr_b16 a[182:183], v26 offset:14336\n" \
  "ds_read_b64_tr_b16 a[186:187], v27 offset:14336\n" \
  "buffer_load_dwordx4 v40, s[4:7], s31 offen offset:2048 lds\n" \
  "v_pk_fma_f32 v[34:35], v[34:35], v[42:43], v[42:43] op_sel:[0,0,1] op_sel_hi:[1,0,1]\n" \
  "v_pk_fma_f32 v[34:35], v[34:35], v[142:143], v[102:103]\n" \
  "v_pk_fma_f32 v[24:25], v[24:25], v[42:43], v[42:43] op_sel:[0,0,1] op_sel_hi:[1,0,1]\n" \
  "v_pk_fma_f32 v[24:25], v[24:25], v[144:145], v[36:37]\n" \
  "ds_read_b64_tr_b16 a[190:191], v28 offset:14336\n" \
  "ds_read_b64_tr_b16 a[194:195], v29 offset:14336\n" \
  "buffer_load_dwordx4 v41, s[4:7], s31 offen offset:3072 lds\n" \
  "v_mov_b32 v37, 925353388\n" \
  "v_pk_add_f32 v[42:43], v[2:3], v[4:5]\n" \
  "v_pk_mul_f32 v[44:45], v[2:3], v[2:3]\n" \
  "v_pk_fma_f32 v[44:45], v[4:5], v[4:5], v[44:45]\n" \
  "v_pk_add_f32 v[42:43], v[42:43], v[6:7]\n" \
  "v_pk_fma_f32 v[44:45], v[6:7], v[6:7], v[44:45]\n" \
  "v_pk_add_f32 v[42:43], v[42:43], v[30:31]\n" \
  "v_pk_fma_f32 v[44:45], v[30:31], v[30:31], v[44:45]\n" \
  "v_pk_add_f32 v[42:43], v[42:43], v[18:19]\n" \
  "v_pk_fma_f32 v[44:45], v[18:19], v[18:19], v[44:45]\n" \
  "v_pk_add_f32 v[42:43], v[42:43], v[20:21]\n" \
  "v_pk_fma_f32 v[44:45], v[20:21], v[20:21], v[44:45]\n" \
  "v_pk_add_f32 v[42:43], v[42:43], v[32:33]\n" \
  "v_pk_fma_f32 v[44:45], v[32:33], v[32:33], v[44:45]\n" \
  "v_pk_add_f32 v[42:43], v[42:43], v[34:35]\n" \
  "v_pk_fma_f32 v[44:45], v[34:35], v[34:35], v[44:45]\n" \
  "v_pk_add_f32 v[42:43], v[42:43], v[24:25]\n" \
  "v_pk_fma_f32 v[44:45], v[24:25], v[24:25], v[44:45]\n" \
  "v_add_f32 v255, v42, v43\n" \
  "v_add_f32 v254, v44, v45\n" \
  "ds_read_b64_tr_b16 a[200:201], v26 offset:16384\n" \
  "ds_read_b64_tr_b16 a[204:205], v27 offset:16384\n" \
  "ds_read_b64_tr_b16 a[208:209], v28 offset:16384\n" \
  "s_add_u32 m0, s57, 4096\n" \
  "s_add_u32 s31, s13, 9879552\n" \
  "buffer_load_dwordx4 v38, s[4:7], s31 offen offset:0 lds\n" \
  "v_permlane16_swap_b32 v255, v254\n" \
  "v_add_f32 v255, v255, v254\n" \
  "ds_read_b64_tr_b16 a[212:213], v29 offset:16384\n" \
  "ds_read_b64_tr_b16 a[220:221], v26 offset:18432\n" \
  "ds_read_b64_tr_b16 a[224:225], v27 offset:18432\n" \
  "buffer_load_dwordx4 v39, s[4:7], s31 offen offset:1024 lds\n" \
  "v_add_f32_dpp v255, v255, v255 quad_perm:[1,0,3,2] row_mask:0xf bank_mask:0xf\n" \
  "ds_read_b64_tr_b16 a[228:229], v28 offset:18432\n" \
  "ds_read_b64_tr_b16 a[232:233], v29 offset:18432\n" \
  "ds_read_b64_tr_b16 a[202:203], v26 offset:20480\n" \
  "buffer_load_dwordx4 v40, s[4:7], s31 offen offset:2048 lds\n" \
  "v_add_f32_dpp v255, v255, v255 quad_perm:[2,3,0,1] row_mask:0xf bank_mask:0xf\n" \
  "ds_read_b64_tr_b16 a[206:207], v27 offset:20480\n" \
  "ds_read_b64_tr_b16 a[210:211], v28 offset:20480\n" \
  "ds_read_b64_tr_b16 a[214:215], v29 offset:20480\n" \
  "buffer_load_dwordx4 v41, s[4:7], s31 offen offset:3072 lds\n" \
  "v_add_f32_dpp v255, v255, v255 row_half_mirror row_mask:0xf bank_mask:0xf\n" \
  "ds_read_b64_tr_b16 a[222:223], v26 offset:22528\n" \
  "ds_read_b64_tr_b16 a[226:227], v27 offset:22528\n" \
  "ds_read_b64_tr_b16 a[230:231], v28 offset:22528\n" \
  "ds_read_b64_tr_b16 a[234:235], v29 offset:22528\n" \
  "s_add_u32 m0, s57, 8192\n" \
  "s_add_u32 s31, s13, 516096\n" \
  "s_waitcnt lgkmcnt(15)\n" \
  "buffer_load_dwordx4 v38, s[4:7], s31 offen offset:0 lds\n" \
  "v_add_f32_dpp v255, v255, v255 row_mirror row_mask:0xf bank_mask:0xf\n" \
  "v_mov_b32 v254, v255\n" \
  "buffer_load_dwordx4 v39, s[4:7], s31 offen offset:1024 lds\n" \
  "buffer_load_dwordx4 v40, s[4:7], s31 offen offset:2048 lds\n" \
  "v_permlane16_swap_b32 v255, v254\n" \
  "v_mul_f32 v255, s35, v255\n" \
  "v_fma_f32 v254, v254, s35, v37\n" \
  "v_fma_f32 v254, -v255, v255, v254\n" \
  "v_max_f32 v254, v254, v37\n" \
  "v_rsq_f32 v36, v254\n" \
  "buffer_load_dwordx4 v41, s[4:7], s31 offen offset:3072 lds\n" \
  "v_mul_f32_e64 v37, -v255, v36\n" \
  "v_pk_fma_f32 v[2:3], v[2:3], v[36:37], v[36:37] op_sel:[0,0,1] op_sel_hi:[1,0,1]\n" \
  "v_pk_fma_f32 v[4:5], v[4:5], v[36:37], v[36:37] op_sel:[0,0,1] op_sel_hi:[1,0,1]\n" \
  "v_pk_fma_f32 v[6:7], v[6:7], v[36:37], v[36:37] op_sel:[0,0,1] op_sel_hi:[1,0,1]\n" \
  "v_pk_fma_f32 v[30:31], v[30:31], v[36:37], v[36:37] op_sel:[0,0,1] op_sel_hi:[1,0,1]\n" \
  "v_pk_fma_f32 v[18:19], v[18:19], v[36:37], v[36:37] op_sel:[0,0,1] op_sel_hi:[1,0,1]\n" \
  "v_pk_fma_f32 v[20:21], v[20:21], v[36:37], v[36:37] op_sel:[0,0,1] op_sel_hi:[1,0,1]\n" \
  "v_pk_fma_f32 v[32:33], v[32:33], v[36:37], v[36:37] op_sel:[0,0,1] op_sel_hi:[1,0,1]\n" \
  "v_pk_fma_f32 v[34:35], v[34:35], v[36:37], v[36:37] op_sel:[0,0,1] op_sel_hi:[1,0,1]\n" \
  "v_pk_fma_f32 v[24:25], v[24:25], v[36:37], v[36:37] op_sel:[0,0,1] op_sel_hi:[1,0,1]\n" \
  "v_cvt_pk_bf16_f32 v42, v2, v3\n" \
  "v_cvt_pk_bf16_f32 v43, v4, v5\n" \
  "v_cvt_pk_bf16_f32 v44, v6, v7\n" \
  "v_cvt_pk_bf16_f32 v45, v30, v31\n" \
  "ds_write_b128 v247, v[42:45] offset:0\n" \
  "v_cvt_pk_bf16_f32 v2, v18, v19\n" \
  "v_cvt_pk_bf16_f32 v3, v20, v21\n" \
  "v_cvt_pk_bf16_f32 v4, v32, v33\n" \
  "v_cvt_pk_bf16_f32 v5, v34, v35\n" \
  "ds_write_b128 v247, v[2:5] offset:512\n" \
  "v_cvt_pk_bf16_f32 v255, v24, v25\n" \
  "ds_write_b32 v246, v255 offset:1024\n" \
  "s_add_u32 m0, s57, 12288\n" \
  "s_add_u32 s31, s13, 9953280\n" \
  "buffer_load_dwordx4 v38, s[4:7], s31 offen offset:0 lds\n" \
  "buffer_load_dwordx4 v39, s[4:7], s31 offen offset:1024 lds\n" \
  "s_waitcnt lgkmcnt(0)\n" \
  "s_barrier\n" \
  "ds_read_b64_tr_b16 v[2:3], v253 offset:9216\n" \
  "ds_read_b64_tr_b16 v[4:5], v253 offset:11664\n" \
  "ds_read_b64_tr_b16 v[18:19], v253 offset:9728\n" \
  "ds_read_b64_tr_b16 v[20:21], v253 offset:12176\n" \
  "ds_read_b64_tr_b16 v[30:31], v253 offset:10240\n" \
  "ds_read_b64_tr_b16 v[32:33], v253 offset:12688\n" \
  "buffer_load_dwordx4 v40, s[4:7], s31 offen offset:2048 lds\n" \
  "buffer_load_dwordx4 v41, s[4:7], s31 offen offset:3072 lds\n" \
  "s_add_u32 s32, s11, 1048576\n" \
  "s_add_u32 s33, s11, 2097152\n" \
  "s_waitcnt lgkmcnt(4)\n" \
  "buffer_store_dwordx4 v[2:5], v240, s[36:39], s11 offen offset:0 sc1\n" \
  "s_waitcnt lgkmcnt(2)\n" \
  "buffer_store_dwordx4 v[18:21], v240, s[36:39], s32 offen offset:0 sc1\n" \
  "s_mov_b64 exec, 65535\n" \
  "s_waitcnt lgkmcnt(0)\n" \
  "buffer_store_dwordx4 v[30:33], v240, s[36:39], s33 offen offset:0 sc1\n" \
  "s_mov_b64 exec, -1\n" \
  "s_waitcnt vmcnt(23)\n" \
  ".Lsnc_%=:\n" \
  "v_xor_b32 v255, s18, v16\n" \
  "v_bitop3_b32 v255, v17, s19, v255 bitop3:0xbe\n" \
  "v_cmp_ne_u32 vcc, 0, v255\n" \
  "s_cbranch_vccnz .Lsns_%=\n" \
  ".Lsnd_%=:\n" \
  "s_add_u32 m0, s57, 16384\n" \
  "s_add_u32 s31, s14, 0\n" \
  "buffer_load_dwordx4 v252, s[36:39], s31 offen offset:0 sc1 lds\n" \
  "buffer_load_dwordx4 v252, s[36:39], s31 offen offset:1024 sc1 lds\n" \
  "buffer_load_dwordx4 v252, s[36:39], s31 offen offset:2048 sc1 lds\n" \
  "buffer_load_dwordx4 v252, s[36:39], s31 offen offset:3072 sc1 lds\n" \
  "s_add_u32 m0, s57, 20480\n" \
  "s_add_u32 s31, s14, 4096\n" \
  "buffer_load_dwordx4 v252, s[36:39], s31 offen offset:0 sc1 lds\n" \
  "buffer_load_dwordx4 v252, s[36:39], s31 offen offset:1024 sc1 lds\n" \
  "buffer_load_dwordx4 v252, s[36:39], s31 offen offset:2048 sc1 lds\n" \
  "buffer_load_dwordx4 v252, s[36:39], s31 offen offset:3072 sc1 lds\n" \
  "s_mov_b32 s30, s16\n" \
  "v_mov_b32 v2, v12\n" \
  "v_mov_b32 v3, v13\n" \
  "v_add_u32 v4, 63488, v12\n" \
  "v_add_u32 v5, 63488, v13\n" \
  "v_mov_b32 v190, 1\n" \
  "v_lshrrev_b32 v189, 1, v252\n" \
  "v_add_u32 v255, s57, v189\n" \
  "s_lshl_b32 s32, s20, 11\n" \
  "v_add_u32 v189, s32, v189\n" \
  "s_waitcnt vmcnt(19)\n" \
  "ds_read_b64_tr_b16 a[240:241], v26 offset:0\n" \
  "ds_read_b64_tr_b16 a[244:245], v27 offset:0\n" \
  "ds_read_b64_tr_b16 a[248:249], v28 offset:0\n" \
  "ds_read_b64_tr_b16 a[252:253], v29 offset:0\n" \
  "ds_read_b64_tr_b16 v[196:197], v26 offset:2048\n" \
  "ds_read_b64_tr_b16 v[200:201], v27 offset:2048\n" \
  "ds_read_b64_tr_b16 v[204:205], v28 offset:2048\n" \
  "ds_read_b64_tr_b16 v[208:209], v29 offset:2048\n" \
  "ds_read_b64_tr_b16 a[242:243], v26 offset:4096\n" \
  "ds_read_b64_tr_b16 a[246:247], v27 offset:4096\n" \
  "ds_read_b64_tr_b16 a[250:251], v28 offset:4096\n" \
  "ds_read_b64_tr_b16 a[254:255], v29 offset:4096\n" \
  "ds_read_b64_tr_b16 v[198:199], v26 offset:6144\n" \
  "ds_read_b64_tr_b16 v[202:203], v27 offset:6144\n" \
  "ds_read_b64_tr_b16 v[206:207], v28 offset:6144\n" \
  "ds_read_b64_tr_b16 v[210:211], v29 offset:6144\n" \
  "s_waitcnt vmcnt(11)\n" \
  "ds_read_b64_tr_b16 v[216:217], v26 offset:8192\n" \
  "ds_read_b64_tr_b16 v[220:221], v27 offset:8192\n" \
  "ds_read_b64_tr_b16 v[224:225], v28 offset:8192\n" \
  "ds_read_b64_tr_b16 v[228:229], v29 offset:8192\n" \
  "ds_read_b64_tr_b16 v[236:237], v26 offset:10240\n" \
  "ds_read_b64_tr_b16 v[240:241], v27 offset:10240\n" \
  "ds_read_b64_tr_b16 v[244:245], v28 offset:10240\n" \
  "ds_read_b64_tr_b16 v[248:249], v29 offset:10240\n" \
  "ds_read_b64_tr_b16 v[218:219], v26 offset:12288\n" \
  "ds_read_b64_tr_b16 v[222:223], v27 offset:12288\n" \
  "ds_read_b64_tr_b16 v[226:227], v28 offset:12288\n" \
  "ds_read_b64_tr_b16 v[230:231], v29 offset:12288\n" \
  "ds_read_b64_tr_b16 v[238:239], v26 offset:14336\n" \
  "ds_read_b64_tr_b16 v[242:243], v27 offset:14336\n" \
  "ds_read_b64_tr_b16 v[246:247], v28 offset:14336\n" \
  "ds_read_b64_tr_b16 v[250:251], v29 offset:14336\n" \
  "s_waitcnt vmcnt(8)\n" \
  "s_mov_b64 exec, 1\n" \
  "buffer_store_dword v190, off, s[36:39], s28 sc1\n" \
  "s_mov_b64 exec, -1\n" \
  "s_add_u32 m0, s57, 24576\n" \
  "s_add_u32 s31, s15, 8192\n" \
  "buffer_load_dwordx4 v252, s[36:39], s31 offen offset:0 sc1 lds\n" \
  "buffer_load_dwordx4 v252, s[36:39], s31 offen offset:1024 sc1 lds\n" \
  "buffer_load_dwordx4 v252, s[36:39], s31 offen offset:2048 sc1 lds\n" \
  "buffer_load_dwordx4 v252, s[36:39], s31 offen offset:3072 sc1 lds\n" \
  "s_add_u32 m0, s57, 28672\n" \
  "s_add_u32 s31, s15, 12288\n" \
  "buffer_load_dwordx4 v252, s[36:39], s31 offen offset:0 sc1 lds\n" \
  "buffer_load_dwordx4 v252, s[36:39], s31 offen offset:1024 sc1 lds\n" \
  "s_lshl_b32 m0, s20, 11\n" \
  "s_add_u32 s31, s31, 2048\n" \
  "buffer_load_dwordx4 v252, s[36:39], s31 offen offset:0 sc1 lds\n" \
  "buffer_load_dwordx4 v252, s[36:39], s31 offen offset:1024 sc1 lds\n" \
  "s_bfe_u32 s32, s27, 131076\n" \
  "s_sub_u32 s100, s60, 655488\n" \
  "s_sub_u32 s101, s60, 720896\n" \
  "s_lshl_b32 s32, s32, 12\n" \
  "s_add_u32 s32, s32, 0\n" \
  "s_cmp_ge_u32 s32, 16384\n" \
  "s_cselect_b32 s32, 0, s32\n" \
  "s_bitcmp1_b32 s32, 12\n" \
  "s_cselect_b32 s33, s101, s100\n" \
  "s_lshl4_add_u32 s31, s32, s33\n" \
  "s_add_u32 s33, s32, s57\n" \
  "s_add_u32 m0, s33, 0\n" \
  "s_waitcnt lgkmcnt(0)\n" \
  "buffer_load_dwordx4 v2, s[40:43], s31 offen offset:0 lds\n" \
  "buffer_load_dwordx4 v3, s[40:43], s31 offen offset:1024 lds\n" \
  "buffer_load_dwordx4 v4, s[40:43], s31 offen offset:2048 lds\n" \
  "buffer_load_dwordx4 v5, s[40:43], s31 offen offset:3072 lds\n" \
  "s_add_u32 s32, s32, 4096\n" \
  "s_cmp_ge_u32 s32, 16384\n" \
  "s_cselect_b32 s32, 0, s32\n" \
  "s_bitcmp1_b32 s32, 12\n" \
  "s_cselect_b32 s33, s101, s100\n" \
  "s_lshl4_add_u32 s31, s32, s33\n" \
  "s_add_u32 s33, s32, s57\n" \
  "s_add_u32 m0, s33, 0\n" \
  "s_nop 0\n" \
  "buffer_load_dwordx4 v2, s[40:43], s31 offen offset:0 lds\n" \
  "buffer_load_dwordx4 v3, s[40:43], s31 offen offset:1024 lds\n" \
  "buffer_load_dwordx4 v4, s[40:43], s31 offen offset:2048 lds\n" \
  "buffer_load_dwordx4 v5, s[40:43], s31 offen offset:3072 lds\n" \
  "s_add_u32 s32, s32, 4096\n" \
  "s_cmp_ge_u32 s32, 16384\n" \
  "s_cselect_b32 s32, 0, s32\n" \
  "s_bitcmp1_b32 s32, 12\n" \
  "s_cselect_b32 s33, s101, s100\n" \
  "s_lshl4_add_u32 s31, s32, s33\n" \
  "s_add_u32 s33, s32, s57\n" \
  "s_add_u32 m0, s33, 0\n" \
  "s_nop 0\n" \
  "buffer_load_dwordx4 v2, s[40:43], s31 offen offset:0 lds\n" \
  "buffer_load_dwordx4 v3, s[40:43], s31 offen offset:1024 lds\n" \
  "buffer_load_dwordx4 v4, s[40:43], s31 offen offset:2048 lds\n" \
  "buffer_load_dwordx4 v5, s[40:43], s31 offen offset:3072 lds\n" \
  "s_add_u32 s32, s32, 4096\n" \
  "s_cmp_ge_u32 s32, 16384\n" \
  "s_cselect_b32 s32, 0, s32\n" \
  "s_bitcmp1_b32 s32, 12\n" \
  "s_cselect_b32 s33, s101, s100\n" \
  "s_lshl4_add_u32 s31, s32, s33\n" \
  "s_add_u32 s33, s32, s57\n" \
  "s_add_u32 m0, s33, 0\n" \
  "s_nop 0\n" \
  "buffer_load_dwordx4 v2, s[40:43], s31 offen offset:0 lds\n" \
  "buffer_load_dwordx4 v3, s[40:43], s31 offen offset:1024 lds\n" \
  "buffer_load_dwordx4 v4, s[40:43], s31 offen offset:2048 lds\n" \
  "buffer_load_dwordx4 v5, s[40:43], s31 offen offset:3072 lds\n" \
  "s_waitcnt vmcnt(16)\n" \
  "ds_read_b64_tr_b16 a[16:17], v255 offset:16384\n" \
  "ds_read_b64_tr_b16 a[36:37], v255 offset:16896\n" \
  "ds_read_b64_tr_b16 a[18:19], v255 offset:17408\n" \
  "ds_read_b64_tr_b16 a[38:39], v255 offset:17920\n" \
  "ds_read_b64_tr_b16 a[56:57], v255 offset:18432\n" \
  "ds_read_b64_tr_b16 a[76:77], v255 offset:18944\n" \
  "ds_read_b64_tr_b16 a[58:59], v255 offset:19456\n" \
  "ds_read_b64_tr_b16 a[78:79], v255 offset:19968\n" \
  "ds_read_b64_tr_b16 a[96:97], v255 offset:20480\n" \
  "ds_read_b64_tr_b16 a[116:117], v255 offset:20992\n" \
  "ds_read_b64_tr_b16 a[98:99], v255 offset:21504\n" \
  "ds_read_b64_tr_b16 a[118:119], v255 offset:22016\n" \
  "ds_read_b64_tr_b16 a[136:137], v255 offset:22528\n" \
  "ds_read_b64_tr_b16 a[156:157], v255 offset:23040\n" \
  "ds_read_b64_tr_b16 a[138:139], v255 offset:23552\n" \
  "ds_read_b64_tr_b16 a[158:159], v255 offset:24064\n" \
  "ds_read_b64_tr_b16 a[176:177], v255 offset:24576\n" \
  "ds_read_b64_tr_b16 a[196:197], v255 offset:25088\n" \
  "ds_read_b64_tr_b16 a[178:179], v255 offset:25600\n" \
  "ds_read_b64_tr_b16 a[198:199], v255 offset:26112\n" \
  "ds_read_b64_tr_b16 a[216:217], v255 offset:26624\n" \
  "ds_read_b64_tr_b16 a[236:237], v255 offset:27136\n" \
  "ds_read_b64_tr_b16 a[218:219], v255 offset:27648\n" \
  "ds_read_b64_tr_b16 a[238:239], v255 offset:28160\n" \
  "ds_read_b64_tr_b16 v[192:193], v255 offset:28672\n" \
  "ds_read_b64_tr_b16 v[212:213], v255 offset:29184\n" \
  "ds_read_b64_tr_b16 v[194:195], v255 offset:29696\n" \
  "ds_read_b64_tr_b16 v[214:215], v255 offset:30208\n" \
  "ds_read_b64_tr_b16 v[232:233], v189 offset:0\n" \
  "ds_read_b64_tr_b16 v[252:253], v189 offset:512\n" \
  "ds_read_b64_tr_b16 v[234:235], v189 offset:1024\n" \
  "ds_read_b64_tr_b16 v[254:255], v189 offset:1536\n" \
  "v_and_b32 v189, 12, v191\n" \
  "v_and_b32 v190, 1, v191\n" \
  "v_lshl_add_u32 v189, v190, 1, v189\n" \
  "v_lshlrev_b32 v189, 2, v189\n" \
  "v_add_u32 v189, 163712, v189\n" \
  "ds_read_b64 v[6:7], v189 offset:0\n" \
  "s_bfe_u32 s32, s27, 131076\n" \
  "s_sub_u32 s100, s60, 131200\n" \
  "s_sub_u32 s101, s60, 196608\n" \
  "s_lshl_b32 s32, s32, 12\n" \
  "s_add_u32 s32, s32, 0\n" \
  "s_cmp_ge_u32 s32, 12288\n" \
  "s_cselect_b32 s32, 0, s32\n" \
  "s_bitcmp1_b32 s32, 12\n" \
  "s_cselect_b32 s33, s101, s100\n" \
  "s_lshl4_add_u32 s31, s32, s33\n" \
  "s_add_u32 s33, s32, s57\n" \
  "s_add_u32 m0, s33, 16384\n" \
  "s_waitcnt lgkmcnt(1)\n" \
  "buffer_load_dwordx4 v2, s[40:43], s31 offen offset:0 lds\n" \
  "buffer_load_dwordx4 v3, s[40:43], s31 offen offset:1024 lds\n" \
  "buffer_load_dwordx4 v4, s[40:43], s31 offen offset:2048 lds\n" \
  "buffer_load_dwordx4 v5, s[40:43], s31 offen offset:3072 lds\n" \
  "s_add_u32 s32, s32, 4096\n" \
  "s_cmp_ge_u32 s32, 12288\n" \
  "s_cselect_b32 s32, 0, s32\n" \
  "s_bitcmp1_b32 s32, 12\n" \
  "s_cselect_b32 s33, s101, s100\n" \
  "s_lshl4_add_u32 s31, s32, s33\n" \
  "s_add_u32 s33, s32, s57\n" \
  "s_add_u32 m0, s33, 16384\n" \
  "s_nop 0\n" \
  "buffer_load_dwordx4 v2, s[40:43], s31 offen offset:0 lds\n" \
  "buffer_load_dwordx4 v3, s[40:43], s31 offen offset:1024 lds\n" \
  "buffer_load_dwordx4 v4, s[40:43], s31 offen offset:2048 lds\n" \
  "buffer_load_dwordx4 v5, s[40:43], s31 offen offset:3072 lds\n" \
  "s_add_u32 s32, s32, 4096\n" \
  "s_cmp_ge_u32 s32, 12288\n" \
  "s_cselect_b32 s32, 0, s32\n" \
  "s_bitcmp1_b32 s32, 12\n" \
  "s_cselect_b32 s33, s101, s100\n" \
  "s_lshl4_add_u32 s31, s32, s33\n" \
  "s_add_u32 s33, s32, s57\n" \
  "s_add_u32 m0, s33, 16384\n" \
  "s_nop 0\n" \
  "buffer_load_dwordx4 v2, s[40:43], s31 offen offset:0 lds\n" \
  "buffer_load_dwordx4 v3, s[40:43], s31 offen offset:1024 lds\n" \
  "buffer_load_dwordx4 v4, s[40:43], s31 offen offset:2048 lds\n" \
  "buffer_load_dwordx4 v5, s[40:43], s31 offen offset:3072 lds\n" \
  "s_waitcnt lgkmcnt(0)\n" \
  "v_lshrrev_b32 v6, 16, v6\n" \
  "v_or_b32 v15, v6, v7\n" \
  "s_branch .Lgo_%=\n" \
  ".Lans_%=:\n" \
  "v_xor_b32 v235, s18, v46\n" \
  "v_bitop3_b32 v235, v47, s19, v235 bitop3:0xbe\n" \
  "s_mov_b32 s32, 8192\n" \
  "v_cmpx_ne_u32 vcc, 0, v235\n" \
  "buffer_load_dwordx4 v[46:49], v243, s[36:39], s32 offen sc1\n" \
  "s_mov_b64 exec, -1\n" \
  "v_xor_b32 v235, s18, v50\n" \
  "v_bitop3_b32 v235, v51, s19, v235 bitop3:0xbe\n" \
  "s_mov_b32 s32, 17408\n" \
  "v_cmpx_ne_u32 vcc, 0, v235\n" \
  "buffer_load_dwordx4 v[50:53], v243, s[36:39], s32 offen sc1\n" \
  "s_mov_b64 exec, -1\n" \
  "v_xor_b32 v235, s18, v54\n" \
  "v_bitop3_b32 v235, v55, s19, v235 bitop3:0xbe\n" \
  "s_mov_b32 s32, 26624\n" \
  "v_cmpx_ne_u32 vcc, 0, v235\n" \
  "buffer_load_dwordx4 v[54:57], v243, s[36:39], s32 offen sc1\n" \
  "s_mov_b64 exec, -1\n" \
  "v_xor_b32 v235, s18, v58\n" \
  "v_bitop3_b32 v235, v59, s19, v235 bitop3:0xbe\n" \
  "s_mov_b32 s32, 12288\n" \
  "v_cmpx_ne_u32 vcc, 0, v235\n" \
  "buffer_load_dwordx4 v[58:61], v243, s[36:39], s32 offen sc1\n" \
  "s_mov_b64 exec, -1\n" \
  "v_xor_b32 v235, s18, v62\n" \
  "v_bitop3_b32 v235, v63, s19, v235 bitop3:0xbe\n" \
  "s_mov_b32 s32, 21504\n" \
  "v_cmpx_ne_u32 vcc, 0, v235\n" \
  "buffer_load_dwordx4 v[62:65], v243, s[36:39], s32 offen sc1\n" \
  "s_mov_b64 exec, -1\n" \
  "v_xor_b32 v235, s18, v66\n" \
  "v_bitop3_b32 v235, v67, s19, v235 bitop3:0xbe\n" \
  "s_mov_b32 s32, 30720\n" \
  "v_cmpx_ne_u32 vcc, 0, v235\n" \
  "buffer_load_dwordx4 v[66:69], v243, s[36:39], s32 offen sc1\n" \
  "s_mov_b64 exec, -1\n" \
  "v_xor_b32 v235, s18, v70\n" \
  "v_bitop3_b32 v235, v71, s19, v235 bitop3:0xbe\n" \
  "s_mov_b32 s32, 16384\n" \
  "v_cmpx_ne_u32 vcc, 0, v235\n" \
  "buffer_load_dwordx4 v[70:73], v242, s[36:39], s32 offen sc1\n" \
  "s_mov_b64 exec, -1\n" \
  "v_xor_b32 v235, s18, v74\n" \
  "v_bitop3_b32 v235, v75, s19, v235 bitop3:0xbe\n" \
  "s_mov_b32 s32, 25600\n" \
  "v_cmpx_ne_u32 vcc, 0, v235\n" \
  "buffer_load_dwordx4 v[74:77], v242, s[36:39], s32 offen sc1\n" \
  "s_mov_b64 exec, -1\n" \
  "v_xor_b32 v235, s18, v78\n" \
  "v_bitop3_b32 v235, v79, s19, v235 bitop3:0xbe\n" \
  "s_mov_b32 s32, 34816\n" \
  "v_cmpx_ne_u32 vcc, 0, v235\n" \
  "buffer_load_dwordx4 v[78:81], v242, s[36:39], s32 offen sc1\n" \
  "s_mov_b64 exec, -1\n" \
  "s_memrealtime s[100:101]\n" \
  "s_waitcnt vmcnt(0) lgkmcnt(0)\n" \
  "s_sub_u32 s100, s100, s17\n" \
  "s_cmp_lt_u32 s100, 0x4c4b40\n" \
  "s_cbranch_scc1 .Lanc_%=\n" \
  "s_branch .Land_%=\n" \
  ".Lyns_%=:\n" \
  "s_sleep 2\n" \
  "s_movk_i32 s32, 4096\n" \
  "buffer_load_dwordx4 v[2:5], v252, s[36:39], s32 offen sc1\n" \
  "buffer_load_dwordx4 v[6:9], v252, s[36:39], s32 offen offset:1024 sc1\n" \
  "s_memrealtime s[100:101]\n" \
  "s_waitcnt vmcnt(0) lgkmcnt(0)\n" \
  "s_sub_u32 s100, s100, s17\n" \
  "s_cmp_lt_u32 s100, 0x4c4b40\n" \
  "s_cbranch_scc1 .Lync_%=\n" \
  "s_branch .Lynd_%=\n" \
  ".Lc3s_%=:\n" \
  "s_add_u32 m0, s57, 0\n" \
  "s_add_u32 s31, s12, 0\n" \
  "buffer_load_dwordx4 v38, s[4:7], s31 offen offset:0 lds\n" \
  "buffer_load_dwordx4 v39, s[4:7], s31 offen offset:1024 lds\n" \
  "buffer_load_dwordx4 v40, s[4:7], s31 offen offset:2048 lds\n" \
  "buffer_load_dwordx4 v41, s[4:7], s31 offen offset:3072 lds\n" \
  "s_add_u32 m0, s57, 4096\n" \
  "s_add_u32 s31, s12, 9437184\n" \
  "buffer_load_dwordx4 v38, s[4:7], s31 offen offset:0 lds\n" \
  "buffer_load_dwordx4 v39, s[4:7], s31 offen offset:1024 lds\n" \
  "buffer_load_dwordx4 v40, s[4:7], s31 offen offset:2048 lds\n" \
  "buffer_load_dwordx4 v41, s[4:7], s31 offen offset:3072 lds\n" \
  "s_branch .Lc3d_%=\n" \
  ".Lrns_%=:\n" \
  "s_add_u32 s32, s8, 4096\n" \
  "v_and_b32 v255, 1, v86\n" \
  "v_cmpx_eq_u32 vcc, 0, v255\n" \
  "buffer_load_dwordx4 v[86:89], v231, s[36:39], s8 offen sc1\n" \
  "s_mov_b64 exec, -1\n" \
  "v_and_b32 v255, 1, v108\n" \
  "v_cmpx_eq_u32 vcc, 0, v255\n" \
  "buffer_load_dwordx4 v[108:111], v231, s[36:39], s32 offen sc1\n" \
  "s_mov_b64 exec, -1\n" \
  "v_and_b32 v255, 1, v112\n" \
  "v_cmpx_eq_u32 vcc, 0, v255\n" \
  "buffer_load_dwordx4 v[112:115], v248, s[36:39], s9 offen sc1\n" \
  "s_mov_b64 exec, -1\n" \
  "s_memrealtime s[100:101]\n" \
  "s_waitcnt vmcnt(0) lgkmcnt(0)\n" \
  "s_sub_u32 s100, s100, s17\n" \
  "s_cmp_lt_u32 s100, 0x4c4b40\n" \
  "s_cbranch_scc1 .Lrnc_%=\n" \
  "s_branch .Lrnd_%=\n" \
  ".Lmns_%=:\n" \
  "s_sleep 2\n" \
  "s_mov_b32 s32, 35840\n" \
  "s_mov_b32 s33, 40448\n" \
  "buffer_load_dwordx4 v[64:67], v238, s[36:39], s32 offen offset:0 sc1\n" \
  "buffer_load_dwordx4 v[68:71], v238, s[36:39], s32 offen offset:2304 sc1\n" \
  "buffer_load_dwordx4 v[72:75], v238, s[36:39], s33 offen offset:0 sc1\n" \
  "buffer_load_dwordx4 v[76:79], v238, s[36:39], s33 offen offset:2304 sc1\n" \
  "s_memrealtime s[100:101]\n" \
  "s_waitcnt vmcnt(0) lgkmcnt(0)\n" \
  "s_sub_u32 s100, s100, s17\n" \
  "s_cmp_lt_u32 s100, 0x4c4b40\n" \
  "s_cbranch_scc1 .Lmnc_%=\n" \
  "s_branch .Lmnd_%=\n" \
  ".Lsns_%=:\n" \
  "buffer_load_dwordx2 v[16:17], v242, s[36:39], s29 offen sc1\n" \
  "s_memrealtime s[100:101]\n" \
  "s_waitcnt vmcnt(0) lgkmcnt(0)\n" \
  "s_sub_u32 s100, s100, s17\n" \
  "s_cmp_lt_u32 s100, 0x4c4b40\n" \
  "s_cbranch_scc1 .Lsnc_%=\n" \
  "s_branch .Lsnd_%=\n" \
  ".Lacs_%=:\n" \
  "v_xor_b32 v248, s18, v66\n" \
  "v_bitop3_b32 v248, v67, s19, v248 bitop3:0xbe\n" \
  "s_mov_b32 s32, 8192\n" \
  "v_cmpx_ne_u32 vcc, 0, v248\n" \
  "buffer_load_dwordx4 v[66:69], v246, s[36:39], s32 offen sc1\n" \
  "s_mov_b64 exec, -1\n" \
  "v_xor_b32 v248, s18, v70\n" \
  "v_bitop3_b32 v248, v71, s19, v248 bitop3:0xbe\n" \
  "s_mov_b32 s32, 17408\n" \
  "v_cmpx_ne_u32 vcc, 0, v248\n" \
  "buffer_load_dwordx4 v[70:73], v246, s[36:39], s32 offen sc1\n" \
  "s_mov_b64 exec, -1\n" \
  "v_xor_b32 v248, s18, v74\n" \
  "v_bitop3_b32 v248, v75, s19, v248 bitop3:0xbe\n" \
  "s_mov_b32 s32, 26624\n" \
  "v_cmpx_ne_u32 vcc, 0, v248\n" \
  "buffer_load_dwordx4 v[74:77], v246, s[36:39], s32 offen sc1\n" \
  "s_mov_b64 exec, -1\n" \
  "v_xor_b32 v248, s18, v78\n" \
  "v_bitop3_b32 v248, v79, s19, v248 bitop3:0xbe\n" \
  "s_mov_b32 s32, 12288\n" \
  "v_cmpx_ne_u32 vcc, 0, v248\n" \
  "buffer_load_dwordx4 v[78:81], v246, s[36:39], s32 offen sc1\n" \
  "s_mov_b64 exec, -1\n" \
  "v_xor_b32 v248, s18, v82\n" \
  "v_bitop3_b32 v248, v83, s19, v248 bitop3:0xbe\n" \
  "s_mov_b32 s32, 21504\n" \
  "v_cmpx_ne_u32 vcc, 0, v248\n" \
  "buffer_load_dwordx4 v[82:85], v246, s[36:39], s32 offen sc1\n" \
  "s_mov_b64 exec, -1\n" \
  "v_xor_b32 v248, s18, v86\n" \
  "v_bitop3_b32 v248, v87, s19, v248 bitop3:0xbe\n" \
  "s_mov_b32 s32, 30720\n" \
  "v_cmpx_ne_u32 vcc, 0, v248\n" \
  "buffer_load_dwordx4 v[86:89], v246, s[36:39], s32 offen sc1\n" \
  "s_mov_b64 exec, -1\n" \
  "v_xor_b32 v248, s18, v90\n" \
  "v_bitop3_b32 v248, v91, s19, v248 bitop3:0xbe\n" \
  "s_mov_b32 s32, 16384\n" \
  "v_cmpx_ne_u32 vcc, 0, v248\n" \
  "buffer_load_dwordx4 v[90:93], v245, s[36:39], s32 offen sc1\n" \
  "s_mov_b64 exec, -1\n" \
  "v_xor_b32 v248, s18, v94\n" \
  "v_bitop3_b32 v248, v95, s19, v248 bitop3:0xbe\n" \
  "s_mov_b32 s32, 25600\n" \
  "v_cmpx_ne_u32 vcc, 0, v248\n" \
  "buffer_load_dwordx4 v[94:97], v245, s[36:39], s32 offen sc1\n" \
  "s_mov_b64 exec, -1\n" \
  "v_xor_b32 v248, s18, v98\n" \
  "v_bitop3_b32 v248, v99, s19, v248 bitop3:0xbe\n" \
  "s_mov_b32 s32, 34816\n" \
  "v_cmpx_ne_u32 vcc, 0, v248\n" \
  "buffer_load_dwordx4 v[98:101], v245, s[36:39], s32 offen sc1\n" \
  "s_mov_b64 exec, -1\n" \
  "s_memrealtime s[100:101]\n" \
  "s_waitcnt vmcnt(0) lgkmcnt(0)\n" \
  "s_sub_u32 s100, s100, s17\n" \
  "s_cmp_lt_u32 s100, 0x4c4b40\n" \
  "s_cbranch_scc1 .Lacc_%=\n" \
  "s_branch .Lacd_%=\n" \
  ".Lycs_%=:\n" \
  "s_sleep 2\n" \
  "s_movk_i32 s32, 4096\n" \
  "buffer_load_dwordx4 v[42:45], v252, s[36:39], s32 offen sc1\n" \
  "buffer_load_dwordx4 v[46:49], v252, s[36:39], s32 offen offset:1024 sc1\n" \
  "s_memrealtime s[100:101]\n" \
  "s_waitcnt vmcnt(0) lgkmcnt(0)\n" \
  "s_sub_u32 s100, s100, s17\n" \
  "s_cmp_lt_u32 s100, 0x4c4b40\n" \
  "s_cbranch_scc1 .Lycc_%=\n" \
  "s_branch .Lycd_%=\n" \
  ".Lws_%=:\n" \
  "s_lshl_b32 s33, s20, 10\n" \
  "s_add_u32 m0, s33, 85376\n" \
  "s_mov_b32 s32, 2925632\n" \
  "buffer_load_dwordx4 v225, s[36:39], s32 offen sc1 lds\n" \
  "s_add_u32 m0, s33, 89472\n" \
  "s_mov_b32 s32, 3523648\n" \
  "buffer_load_dwordx4 v225, s[36:39], s32 offen sc1 lds\n" \
  "s_add_u32 m0, s33, 116096\n" \
  "s_mov_b32 s32, 4121664\n" \
  "buffer_load_dwordx4 v225, s[36:39], s32 offen sc1 lds\n" \
  "s_add_u32 m0, s33, 120192\n" \
  "s_mov_b32 s32, 4719680\n" \
  "buffer_load_dwordx4 v225, s[36:39], s32 offen sc1 lds\n" \
  "s_branch .Lwd_%=\n" \
  ".Lxs_%=:\n" \
  "v_bfe_u32 v245, v191, 5, 1\n" \
  "v_mul_u32_u24 v245, 22528, v245\n" \
  "v_add_u32 v255, v112, v245\n" \
  "v_sub_u32 v248, v113, v245\n" \
  "v_add_u32 v248, 55296, v248\n" \
  "v_add_u32 v255, 0x8000, v255\n" \
  "ds_read_b64 v[14:15], v248 offset:39296\n" \
  "ds_read_b64 v[26:27], v248 offset:38272\n" \
  "ds_read_b64 v[28:29], v248 offset:37248\n" \
  "ds_read_b64 v[30:31], v248 offset:36224\n" \
  "ds_read_b64 v[32:33], v255 offset:36224\n" \
  "ds_read_b64 v[34:35], v255 offset:37248\n" \
  "ds_read_b64 v[36:37], v255 offset:38272\n" \
  "ds_read_b64 v[38:39], v255 offset:39296\n" \
  "s_waitcnt lgkmcnt(0)\n" \
  "v_lshlrev_b32 v245, 16, v15\n" \
  "v_add_f32 v40, v40, v245\n" \
  "v_and_b32 v245, 0xffff0000, v15\n" \
  "v_sub_f32 v44, v44, v245\n" \
  "v_lshlrev_b32 v245, 16, v32\n" \
  "v_add_f32 v66, v66, v245\n" \
  "v_and_b32 v245, 0xffff0000, v32\n" \
  "v_sub_f32 v70, v70, v245\n" \
  "v_lshlrev_b32 v245, 16, v14\n" \
  "v_add_f32 v82, v82, v245\n" \
  "v_and_b32 v245, 0xffff0000, v14\n" \
  "v_sub_f32 v86, v86, v245\n" \
  "v_lshlrev_b32 v245, 16, v33\n" \
  "v_add_f32 v90, v90, v245\n" \
  "v_and_b32 v245, 0xffff0000, v33\n" \
  "v_sub_f32 v94, v94, v245\n" \
  "v_lshlrev_b32 v245, 16, v27\n" \
  "v_add_f32 v41, v41, v245\n" \
  "v_and_b32 v245, 0xffff0000, v27\n" \
  "v_sub_f32 v45, v45, v245\n" \
  "v_lshlrev_b32 v245, 16, v34\n" \
  "v_add_f32 v67, v67, v245\n" \
  "v_and_b32 v245, 0xffff0000, v34\n" \
  "v_sub_f32 v71, v71, v245\n" \
  "v_lshlrev_b32 v245, 16, v26\n" \
  "v_add_f32 v83, v83, v245\n" \
  "v_and_b32 v245, 0xffff0000, v26\n" \
  "v_sub_f32 v87, v87, v245\n" \
  "v_lshlrev_b32 v245, 16, v35\n" \
  "v_add_f32 v91, v91, v245\n" \
  "v_and_b32 v245, 0xffff0000, v35\n" \
  "v_sub_f32 v95, v95, v245\n" \
  "v_lshlrev_b32 v245, 16, v29\n" \
  "v_add_f32 v42, v42, v245\n" \
  "v_and_b32 v245, 0xffff0000, v29\n" \
  "v_sub_f32 v46, v46, v245\n" \
  "v_lshlrev_b32 v245, 16, v36\n" \
  "v_add_f32 v68, v68, v245\n" \
  "v_and_b32 v245, 0xffff0000, v36\n" \
  "v_sub_f32 v72, v72, v245\n" \
  "v_lshlrev_b32 v245, 16, v28\n" \
  "v_add_f32 v84, v84, v245\n" \
  "v_and_b32 v245, 0xffff0000, v28\n" \
  "v_sub_f32 v88, v88, v245\n" \
  "v_lshlrev_b32 v245, 16, v37\n" \
  "v_add_f32 v92, v92, v245\n" \
  "v_and_b32 v245, 0xffff0000, v37\n" \
  "v_sub_f32 v96, v96, v245\n" \
  "v_lshlrev_b32 v245, 16, v31\n" \
  "v_add_f32 v43, v43, v245\n" \
  "v_and_b32 v245, 0xffff0000, v31\n" \
  "v_sub_f32 v47, v47, v245\n" \
  "v_lshlrev_b32 v245, 16, v38\n" \
  "v_add_f32 v69, v69, v245\n" \
  "v_and_b32 v245, 0xffff0000, v38\n" \
  "v_sub_f32 v73, v73, v245\n" \
  "v_lshlrev_b32 v245, 16, v30\n" \
  "v_add_f32 v85, v85, v245\n" \
  "v_and_b32 v245, 0xffff0000, v30\n" \
  "v_sub_f32 v89, v89, v245\n" \
  "v_lshlrev_b32 v245, 16, v39\n" \
  "v_add_f32 v93, v93, v245\n" \
  "v_and_b32 v245, 0xffff0000, v39\n" \
  "v_sub_f32 v97, v97, v245\n" \
  "s_branch .Lxd_%=\n" \
  ".Lts_%=:\n" \
  "v_and_b32 v239, 15, v191\n" \
  "v_lshrrev_b32 v237, 4, v191\n" \
  "v_lshl_or_b32 v239, v237, 6, v239\n" \
  "v_mul_u32_u24 v239, 9344, v239\n" \
  "v_lshl_add_u32 v239, s20, 4, v239\n" \
  "s_add_u32 s32, s22, 0\n" \
  "v_and_b32 v237, 1, v106\n" \
  "v_cmpx_ne_u32 vcc, 0, v237\n" \
  "buffer_load_dwordx4 v[106:109], v239, s[36:39], s32 offen sc1\n" \
  "s_mov_b64 exec, -1\n" \
  "s_add_u32 s32, s22, 149504\n" \
  "v_and_b32 v237, 1, v124\n" \
  "v_cmpx_ne_u32 vcc, 0, v237\n" \
  "buffer_load_dwordx4 v[124:127], v239, s[36:39], s32 offen sc1\n" \
  "s_mov_b64 exec, -1\n" \
  "s_add_u32 s32, s22, 299008\n" \
  "v_and_b32 v237, 1, v128\n" \
  "v_cmpx_ne_u32 vcc, 0, v237\n" \
  "buffer_load_dwordx4 v[128:131], v239, s[36:39], s32 offen sc1\n" \
  "s_mov_b64 exec, -1\n" \
  "s_add_u32 s32, s22, 448512\n" \
  "v_and_b32 v237, 1, v132\n" \
  "v_cmpx_ne_u32 vcc, 0, v237\n" \
  "buffer_load_dwordx4 v[132:135], v239, s[36:39], s32 offen sc1\n" \
  "s_mov_b64 exec, -1\n" \
  "s_memrealtime s[100:101]\n" \
  "s_waitcnt vmcnt(0) lgkmcnt(0)\n" \
  "s_sub_u32 s100, s100, s17\n" \
  "s_cmp_lt_u32 s100, 0x4c4b40\n" \
  "s_cbranch_scc1 .Ltc_%=\n" \
  "s_branch .Ltd_%=\n" \
  ".Lrcs_%=:\n" \
  "s_add_u32 s32, s8, 4096\n" \
  "v_and_b32 v246, 1, v44\n" \
  "v_cmpx_eq_u32 vcc, 0, v246\n" \
  "buffer_load_dwordx4 v[44:47], v230, s[36:39], s8 offen sc1\n" \
  "s_mov_b64 exec, -1\n" \
  "v_and_b32 v246, 1, v66\n" \
  "v_cmpx_eq_u32 vcc, 0, v246\n" \
  "buffer_load_dwordx4 v[66:69], v230, s[36:39], s32 offen sc1\n" \
  "s_mov_b64 exec, -1\n" \
  "v_and_b32 v246, 1, v70\n" \
  "v_cmpx_eq_u32 vcc, 0, v246\n" \
  "buffer_load_dwordx4 v[70:73], v240, s[36:39], s9 offen sc1\n" \
  "s_mov_b64 exec, -1\n" \
  "s_memrealtime s[100:101]\n" \
  "s_waitcnt vmcnt(0) lgkmcnt(0)\n" \
  "s_sub_u32 s100, s100, s17\n" \
  "s_cmp_lt_u32 s100, 0x4c4b40\n" \
  "s_cbranch_scc1 .Lrcc_%=\n" \
  "s_branch .Lrcd_%=\n" \
  ".Lmcs_%=:\n" \
  "s_sleep 2\n" \
  "s_mov_b32 s32, 35840\n" \
  "s_mov_b32 s33, 40448\n" \
  "buffer_load_dwordx4 v[28:31], v238, s[36:39], s32 offen offset:0 sc1\n" \
  "buffer_load_dwordx4 v[32:35], v238, s[36:39], s32 offen offset:2304 sc1\n" \
  "buffer_load_dwordx4 v[36:39], v238, s[36:39], s33 offen offset:0 sc1\n" \
  "buffer_load_dwordx4 v[40:43], v238, s[36:39], s33 offen offset:2304 sc1\n" \
  "s_memrealtime s[100:101]\n" \
  "s_waitcnt vmcnt(0) lgkmcnt(0)\n" \
  "s_sub_u32 s100, s100, s17\n" \
  "s_cmp_lt_u32 s100, 0x4c4b40\n" \
  "s_cbranch_scc1 .Lmcc_%=\n" \
  "s_branch .Lmcd_%=\n" \
  ".Lscs_%=:\n" \
  "buffer_load_dwordx2 v[8:9], v245, s[36:39], s29 offen sc1\n" \
  "s_memrealtime s[100:101]\n" \
  "s_waitcnt vmcnt(0) lgkmcnt(0)\n" \
  "s_sub_u32 s100, s100, s17\n" \
  "s_cmp_lt_u32 s100, 0x4c4b40\n" \
  "s_cbranch_scc1 .Lscc_%=\n" \
  "s_branch .Lscd_%=\n" \
  ".Lcol_%=:\n" \
  "s_load_dwordx2 s[48:49], s[0:1], 0\n" \
  "v_lshrrev_b32 v255, 6, v0\n" \
  "s_getpc_b64 s[80:81]\n" \
  ".Lpcc_%=:\n" \
  "v_readfirstlane_b32 s20, v255\n" \
  "s_mov_b32 s82, .Ladm_b-.Lpcc_%=\n" \
  "s_mov_b32 s83, 131072\n" \
  "s_mov_b32 m0, 162176\n" \
  "s_lshr_b32 s98, s2, 3\n" \
  "s_mul_i32 s98, s98, 20\n" \
  "v_add_u32 v254, s98, v0\n" \
  "v_lshlrev_b32 v253, 7, v254\n" \
  "s_cmp_eq_u32 s20, 0\n" \
  "s_cselect_b64 exec, 1048575, 0\n" \
  "buffer_load_dword v253, s[80:83], 0 offen lds\n" \
  "s_mov_b64 exec, -1\n" \
  "s_mov_b32 s50, 2359296\n" \
  "s_mov_b32 s51, 131072\n" \
  "s_mov_b32 s78, 26944\n" \
  "s_mov_b32 s79, 131072\n" \
  "v_and_b32 v191, 63, v0\n" \
  "v_and_b32 v254, 31, v191\n" \
  "v_lshrrev_b32 v253, 5, v191\n" \
  "v_mul_u32_u24 v252, 1179648, v253\n" \
  "v_lshl_add_u32 v251, v254, 4, v252\n" \
  "v_lshl_add_u32 v250, v254, 2, v252\n" \
  "v_lshlrev_b32 v252, 4, v191\n" \
  "s_and_b32 s84, s2, 7\n" \
  "s_lshr_b32 s85, s2, 3\n" \
  "s_lshl_b32 s98, s84, 5\n" \
  "s_add_u32 s86, s98, s85\n" \
  "s_lshl_b32 s98, s20, 8\n" \
  "s_add_u32 s98, s98, s86\n" \
  "s_mul_i32 s87, s98, 1152\n" \
  "s_waitcnt lgkmcnt(0)\n" \
  "buffer_load_dwordx4 v[2:5], v251, s[48:51], s87 offen offset:0\n" \
  "buffer_load_dwordx4 v[6:9], v251, s[48:51], s87 offen offset:512\n" \
  "buffer_load_dword v249, v250, s[48:51], s87 offen offset:1024\n" \
  "s_movk_i32 s32, 22528\n" \
  "buffer_load_dwordx4 v[10:13], v252, s[76:79], s32 offen offset:0\n" \
  "s_memrealtime s[16:17]\n" \
  "s_lshr_b32 s32, s20, 1\n" \
  "s_lshl3_add_u32 s32, s32, 8\n" \
  "s_load_dwordx2 s[52:53], s[0:1], s32\n" \
  "s_load_dwordx2 s[4:5], s[0:1], 24\n" \
  "s_load_dwordx2 s[64:65], s[0:1], 40\n" \
  "s_load_dwordx2 s[36:37], s[0:1], 48\n" \
  "s_load_dwordx2 s[68:69], s[0:1], 56\n" \
  "s_load_dwordx2 s[72:73], s[0:1], 64\n" \
  "s_load_dwordx2 s[40:41], s[0:1], 72\n" \
  "s_load_dwordx2 s[44:45], s[0:1], 80\n" \
  "s_mov_b32 s6, 18874368\n" \
  "s_mov_b32 s7, 131072\n" \
  "s_mov_b32 s66, 3981312\n" \
  "s_mov_b32 s67, 131072\n" \
  "s_mov_b32 s38, 134217728\n" \
  "s_mov_b32 s39, 131072\n" \
  "s_mov_b32 s70, 9216\n" \
  "s_mov_b32 s71, 131072\n" \
  "s_mov_b32 s74, 4096\n" \
  "s_mov_b32 s75, 131072\n" \
  "s_mov_b32 s42, 18874368\n" \
  "s_mov_b32 s43, 131072\n" \
  "s_mov_b32 s46, 2359296\n" \
  "s_mov_b32 s47, 131072\n" \
  "s_mov_b32 s54, 1183744\n" \
  "s_mov_b32 s55, 131072\n" \
  "v_and_b32 v248, 15, v191\n" \
  "v_lshrrev_b32 v247, 4, v191\n" \
  "v_lshlrev_b32 v246, 4, v0\n" \
  "v_lshrrev_b32 v245, 1, v248\n" \
  "v_and_b32 v244, 1, v247\n" \
  "v_lshl_add_u32 v243, v244, 9, s86\n" \
  "v_and_b32 v244, 1, v248\n" \
  "v_and_b32 v242, 3, v245\n" \
  "v_lshl_add_u32 v241, v244, 2, v242\n" \
  "v_lshl_add_u32 v242, v241, 8, s86\n" \
  "v_lshlrev_b32 v241, 1, v247\n" \
  "v_cvt_f32_u32 v240, v245\n" \
  "v_cvt_f32_u32 v245, v243\n" \
  "v_cmp_eq_u32 vcc, v244, v253\n" \
  "v_mul_f32 v240, 3120562176, v240\n" \
  "v_mov_b32 v243, 1048576000\n" \
  "v_cndmask_b32 v243, 0, v243, vcc\n" \
  "v_lshlrev_b32 v239, 31, v244\n" \
  "v_mul_f32 v245, v240, v245\n" \
  "v_xor_b32 v239, v243, v239\n" \
  "v_mul_f32 v240, 1132462080, v240\n" \
  "v_fma_f32 v238, v240, 0, v245\n" \
  "v_cos_f32 v237, v238\n" \
  "v_sin_f32 v236, v238\n" \
  "v_mul_f32 v235, v237, v243\n" \
  "v_mul_f32 v234, v236, v239\n" \
  "v_mul_f32 v233, v236, v243\n" \
  "v_mul_f32 v232, v237, v239\n" \
  "v_cvt_pk_f16_f32 v235, v235, -v234\n" \
  "v_cvt_pk_f16_f32 v233, v233, v232\n" \
  "v_accvgpr_write_b32 a160, v235\n" \
  "v_accvgpr_write_b32 a164, v233\n" \
  "v_fma_f32 v238, v240, 4.0, v245\n" \
  "v_cos_f32 v237, v238\n" \
  "v_sin_f32 v236, v238\n" \
  "v_mul_f32 v235, v237, v243\n" \
  "v_mul_f32 v234, v236, v239\n" \
  "v_mul_f32 v233, v236, v243\n" \
  "v_mul_f32 v232, v237, v239\n" \
  "v_cvt_pk_f16_f32 v235, v235, -v234\n" \
  "v_cvt_pk_f16_f32 v233, v233, v232\n" \
  "v_accvgpr_write_b32 a161, v235\n" \
  "v_accvgpr_write_b32 a165, v233\n" \
  "v_fma_f32 v238, v240, 1.0, v245\n" \
  "v_cos_f32 v237, v238\n" \
  "v_sin_f32 v236, v238\n" \
  "v_mul_f32 v235, v237, v243\n" \
  "v_mul_f32 v234, v236, v239\n" \
  "v_mul_f32 v233, v236, v243\n" \
  "v_mul_f32 v232, v237, v239\n" \
  "v_cvt_pk_f16_f32 v235, v235, -v234\n" \
  "v_cvt_pk_f16_f32 v233, v233, v232\n" \
  "v_accvgpr_write_b32 a162, v235\n" \
  "v_accvgpr_write_b32 a166, v233\n" \
  "v_fma_f32 v238, v240, 4.0, v245\n" \
  "v_add_f32 v238, v238, v240\n" \
  "v_cos_f32 v237, v238\n" \
  "v_sin_f32 v236, v238\n" \
  "v_mul_f32 v235, v237, v243\n" \
  "v_mul_f32 v234, v236, v239\n" \
  "v_mul_f32 v233, v236, v243\n" \
  "v_mul_f32 v232, v237, v239\n" \
  "v_cvt_pk_f16_f32 v235, v235, -v234\n" \
  "v_cvt_pk_f16_f32 v233, v233, v232\n" \
  "v_accvgpr_write_b32 a163, v235\n" \
  "v_accvgpr_write_b32 a167, v233\n" \
  "v_cvt_f32_u32 v240, v242\n" \
  "v_cvt_f32_u32 v245, v241\n" \
  "v_cmp_lt_u32 vcc, 7, v248\n" \
  "v_mul_f32 v240, 973078528, v240\n" \
  "v_mul_f32 v238, v240, v245\n" \
  "v_cos_f32 v237, v238\n" \
  "v_sin_f32 v236, v238\n" \
  "v_mul_f32 v237, 0.5, v237\n" \
  "v_mul_f32 v236, 0.5, v236\n" \
  "v_cvt_pk_f16_f32 v235, v237, -v236\n" \
  "v_cvt_pk_f16_f32 v234, v236, v237\n" \
  "v_cndmask_b32 v233, v235, 0, vcc\n" \
  "v_cndmask_b32 v235, 0, v235, vcc\n" \
  "v_cndmask_b32 v232, v234, 0, vcc\n" \
  "v_xor_b32 v234, 2147516416, v234\n" \
  "v_accvgpr_write_b32 a228, v233\n" \
  "v_cndmask_b32 v234, 0, v234, vcc\n" \
  "v_accvgpr_write_b32 a229, v235\n" \
  "v_accvgpr_write_b32 a232, v232\n" \
  "v_accvgpr_write_b32 a233, v234\n" \
  "v_add_f32 v238, v238, v240\n" \
  "v_cos_f32 v237, v238\n" \
  "v_sin_f32 v236, v238\n" \
  "v_mul_f32 v237, 0.5, v237\n" \
  "v_mul_f32 v236, 0.5, v236\n" \
  "v_cvt_pk_f16_f32 v235, v237, -v236\n" \
  "v_cvt_pk_f16_f32 v234, v236, v237\n" \
  "v_cndmask_b32 v233, v235, 0, vcc\n" \
  "v_cndmask_b32 v235, 0, v235, vcc\n" \
  "v_cndmask_b32 v232, v234, 0, vcc\n" \
  "v_xor_b32 v234, 2147516416, v234\n" \
  "v_accvgpr_write_b32 a230, v233\n" \
  "v_cndmask_b32 v234, 0, v234, vcc\n" \
  "v_accvgpr_write_b32 a231, v235\n" \
  "v_accvgpr_write_b32 a234, v232\n" \
  "v_accvgpr_write_b32 a235, v234\n" \
  "s_lshr_b32 s88, s2, 6\n" \
  "s_lshl_b32 s98, s84, 2\n" \
  "s_lshr_b32 s100, s85, 1\n" \
  "s_and_b32 s100, s100, 3\n" \
  "s_add_u32 s89, s98, s100\n" \
  "s_mul_i32 s98, s86, 9344\n" \
  "s_add_u32 s82, s98, 524288\n" \
  "s_lshl_b32 s98, s86, 8\n" \
  "s_add_u32 s11, s98, 9764864\n" \
  "s_lshl_b32 s98, s86, 4\n" \
  "s_add_u32 s28, s98, 65536\n" \
  "s_and_b32 s98, s84, 1\n" \
  "s_lshl_b32 s98, s98, 3\n" \
  "s_and_b32 s100, s85, 7\n" \
  "s_add_u32 s90, s98, s100\n" \
  "s_lshl_b32 s98, s88, 5\n" \
  "s_add_u32 s98, s98, s89\n" \
  "s_lshl_b32 s98, s98, 1\n" \
  "s_and_b32 s100, s85, 1\n" \
  "s_add_u32 s98, s98, s100\n" \
  "s_lshl_b32 s98, s98, 7\n" \
  "s_add_u32 s80, s98, 262144\n" \
  "s_mov_b32 s35, 987991609\n" \
  "v_mul_u32_u24 v245, 9, v247\n" \
  "v_add_u32 v243, 8, v245\n" \
  "v_lshlrev_b32 v245, 4, v248\n" \
  "v_lshl_add_u32 v242, v243, 16, v245\n" \
  "v_lshrrev_b32 v245, 3, v191\n" \
  "v_and_b32 v243, 7, v191\n" \
  "v_lshlrev_b32 v241, 4, v243\n" \
  "v_lshl_add_u32 v243, v245, 16, v241\n" \
  "v_add_u32 v245, s80, v243\n" \
  "s_add_u32 s83, s82, 4096\n" \
  "s_add_u32 s91, s82, 8192\n" \
  "v_mov_b32 v14, -1\n" \
  "v_mov_b32 v15, -1\n" \
  "v_mov_b32 v16, -1\n" \
  "v_mov_b32 v17, -1\n" \
  "v_cmp_eq_u32 vcc, 24, v191\n" \
  "v_mov_b32 v255, s28\n" \
  "s_cmp_eq_u32 s20, 2\n" \
  "v_cndmask_b32 v245, v245, v255, vcc\n" \
  "s_cselect_b64 vcc, -1, 0\n" \
  "s_cselect_b32 s91, s11, s91\n" \
  "v_cndmask_b32 v243, v246, v242, vcc\n" \
  "s_cmp_eq_u32 s20, 3\n" \
  "s_cselect_b64 vcc, -1, 0\n" \
  "s_cselect_b32 s91, 0, s91\n" \
  "s_cselect_b32 s32, 0, -1\n" \
  "v_cndmask_b32 v243, v243, v245, vcc\n" \
  "s_cselect_b32 s92, 33554431, -1\n" \
  "s_cselect_b32 s93, 0, -1\n" \
  "s_cmp_eq_u32 s20, 1\n" \
  "s_cselect_b64 s[92:93], 15, s[92:93]\n" \
  "v_mov_b32 v18, s32\n" \
  "v_mov_b32 v19, s32\n" \
  "v_mov_b32 v20, s32\n" \
  "v_mov_b32 v21, s32\n" \
  "s_movk_i32 s32, 768\n" \
  "v_lshlrev_b32 v245, 5, v191\n" \
  "v_bfe_u32 v242, v191, 5, 1\n" \
  "v_and_or_b32 v245, v245, s32, v242\n" \
  "v_lshrrev_b32 v242, 3, v191\n" \
  "v_mul_u32_u24 v245, 9216, v245\n" \
  "v_bitop3_b32 v242, v242, v191, 6 bitop3:0x6c\n" \
  "v_and_b32 v242, 7, v242\n" \
  "v_lshl_or_b32 v22, v242, 4, v245\n" \
  "v_add_u32 v23, 17408, v22\n" \
  "v_add_u32 v24, 34816, v22\n" \
  "v_add_u32 v25, 52224, v22\n" \
  "s_cmp_eq_u32 s20, 0\n" \
  "s_cselect_b64 s[58:59], -1, 0\n" \
  "s_movk_i32 s34, 144\n" \
  "v_cmp_gt_u32 s[94:95], s34, v0\n" \
  "v_mov_b32 v26, 0\n" \
  "v_mov_b32 v27, 0\n" \
  "v_mov_b32 v255, 161664\n" \
  "ds_write_b64 v255, v[26:27] offset:0\n" \
  "v_bfrev_b32 v245, 1\n" \
  "v_cndmask_b32 v245, v245, v246, s[58:59]\n" \
  "v_and_b32 v242, 1, v191\n" \
  "v_lshl_add_u32 v241, v253, 1, v242\n" \
  "v_lshlrev_b32 v242, 3, v241\n" \
  "v_and_b32 v241, 7, v191\n" \
  "v_lshlrev_b32 v244, 1, v241\n" \
  "v_and_b32 v241, 12, v191\n" \
  "v_add_u32 v240, v241, v247\n" \
  "s_lshl_b32 s98, s20, 4\n" \
  "v_add_u32 v247, s98, v240\n" \
  "v_lshlrev_b32 v241, 1, v247\n" \
  "v_bfe_u32 v247, v191, 1, 1\n" \
  "v_lshrrev_b32 v240, 2, v191\n" \
  "v_and_b32 v239, 12, v240\n" \
  "v_lshl_add_u32 v240, v247, 6, v239\n" \
  "s_lshr_b32 s98, s84, 1\n" \
  "s_lshl_b32 s98, s98, 4\n" \
  "s_add_u32 s98, s98, s20\n" \
  "v_add_u32 v247, s98, v240\n" \
  "v_lshlrev_b32 v240, 1, v247\n" \
  "s_mul_i32 s98, s84, 1152\n" \
  "s_and_b32 s100, s85, 7\n" \
  "s_lshl_b32 s100, s100, 7\n" \
  "s_add_u32 s98, s98, s100\n" \
  "s_lshl_b32 s100, s20, 5\n" \
  "s_add_u32 s25, s98, s100\n" \
  "s_mul_i32 s98, s84, 1152\n" \
  "s_and_b32 s100, s85, 6\n" \
  "s_lshl_b32 s100, s100, 4\n" \
  "s_add_u32 s98, s98, s100\n" \
  "s_lshr_b32 s100, s20, 1\n" \
  "s_lshl_b32 s100, s100, 4\n" \
  "s_add_u32 s98, s98, s100\n" \
  "s_add_u32 s13, s98, 1024\n" \
  "s_lshl_b32 s15, s90, 8\n" \
  "s_waitcnt lgkmcnt(0)\n" \
  "s_mov_b32 s17, s16\n" \
  "v_and_b32 v247, 127, v0\n" \
  "s_mul_i32 s98, s86, 4624\n" \
  "v_lshl_add_u32 v239, v247, 4, s98\n" \
  "v_add_u32 v238, 3440, v239\n" \
  "v_add_u32 v237, 2048, v239\n" \
  "v_add_u32 v236, 1392, v239\n" \
  "s_lshr_b32 s98, s20, 1\n" \
  "s_mul_i32 s98, s98, 6144\n" \
  "s_and_b32 s100, s20, 1\n" \
  "s_lshl_b32 s100, s100, 10\n" \
  "s_add_u32 s98, s98, s100\n" \
  "s_add_u32 s56, s98, 19584\n" \
  "v_cmp_gt_u32 vcc, 57, v247\n" \
  "v_subrev_u32 v255, 1183744, v238\n" \
  "s_mov_b32 m0, s56\n" \
  "v_cndmask_b32 v237, v236, v237, vcc\n" \
  "buffer_load_dwordx4 v239, s[52:55], 0 offen offset:0 lds\n" \
  "v_min_u32 v238, v238, v255\n" \
  "s_add_u32 m0, s56, 2048\n" \
  "s_bitcmp1_b32 s20, 0\n" \
  "buffer_load_dwordx4 v237, s[52:55], 0 offen offset:0 lds\n" \
  "s_add_u32 m0, s56, 4096\n" \
  "s_cselect_b32 exec_hi, 32767, -1\n" \
  "buffer_load_dwordx4 v238, s[52:55], 0 offen offset:0 lds\n" \
  "s_mov_b64 exec, -1\n" \
  "s_movk_i32 s32, 10240\n" \
  "buffer_load_dwordx4 v[26:29], v246, s[76:79], s32 offen offset:0\n" \
  "buffer_load_dwordx2 v[30:31], off, s[36:39], 0 sc1\n" \
  "buffer_store_dwordx4 v[14:17], v246, s[36:39], s82 offen offset:0 sc1\n" \
  "buffer_store_dwordx4 v[14:17], v246, s[36:39], s83 offen offset:0 sc1\n" \
  "s_mov_b64 exec, s[92:93]\n" \
  "buffer_store_dwordx4 v[18:21], v243, s[36:39], s91 offen offset:0 sc1\n" \
  "s_mov_b64 exec, -1\n" \
  "s_lshl_b32 s98, s85, 3\n" \
  "s_lshl_b32 s100, s20, 1\n" \
  "s_add_u32 s98, s98, s100\n" \
  "s_mul_i32 s98, s98, 9216\n" \
  "s_mul_i32 s100, s84, 1152\n" \
  "s_add_u32 s98, s98, s100\n" \
  "s_add_u32 s32, s98, 1024\n" \
  "s_lshl_b32 s98, s20, 10\n" \
  "s_add_u32 s33, s98, 155008\n" \
  "s_mov_b32 m0, s33\n" \
  "s_movk_i32 s31, 4096\n" \
  "s_cmp_gt_u32 s20, 1\n" \
  "buffer_load_dwordx4 v22, s[4:7], s32 offen offset:0 lds\n" \
  "s_cselect_b32 s31, -28672, s31\n" \
  "s_add_u32 m0, s33, s31\n" \
  "s_add_u32 s32, s32, 9437184\n" \
  "buffer_load_dwordx4 v22, s[4:7], s32 offen offset:0 lds\n" \
  "buffer_load_dwordx2 v[16:17], v242, s[68:71], s25 offen offset:0\n" \
  "buffer_load_ushort v247, v244, s[68:71], s13 offen offset:0\n" \
  "buffer_load_ushort v244, v241, s[72:75], s15 offen offset:0\n" \
  "buffer_load_ushort v243, v241, s[72:75], s15 offen offset:128\n" \
  "buffer_load_ushort v242, v240, s[72:75], s15 offen offset:0\n" \
  "v_mul_u32_u24 v241, 2448, v253\n" \
  "s_mul_i32 s98, s20, 4896\n" \
  "v_add_u32 v240, s98, v241\n" \
  "v_lshrrev_b32 v241, 3, v254\n" \
  "v_lshlrev_b32 v239, 4, v241\n" \
  "v_lshl_add_u32 v241, v254, 5, v239\n" \
  "v_add_u32 v239, v241, v240\n" \
  "v_lshl_add_u32 v241, v254, 3, v240\n" \
  "s_waitcnt vmcnt(18)\n" \
  "v_lshlrev_b32 v14, 16, v2\n" \
  "v_and_b32 v15, 4294901760, v2\n" \
  "v_lshlrev_b32 v18, 16, v3\n" \
  "v_and_b32 v19, 4294901760, v3\n" \
  "v_lshlrev_b32 v20, 16, v4\n" \
  "v_and_b32 v21, 4294901760, v4\n" \
  "v_lshlrev_b32 v32, 16, v5\n" \
  "v_and_b32 v33, 4294901760, v5\n" \
  "s_waitcnt vmcnt(17)\n" \
  "v_lshlrev_b32 v34, 16, v6\n" \
  "v_and_b32 v35, 4294901760, v6\n" \
  "v_lshlrev_b32 v36, 16, v7\n" \
  "v_and_b32 v37, 4294901760, v7\n" \
  "v_lshlrev_b32 v38, 16, v8\n" \
  "v_and_b32 v39, 4294901760, v8\n" \
  "v_lshlrev_b32 v40, 16, v9\n" \
  "v_and_b32 v41, 4294901760, v9\n" \
  "s_waitcnt vmcnt(16)\n" \
  "v_lshlrev_b32 v42, 16, v249\n" \
  "v_and_b32 v43, 4294901760, v249\n" \
  "v_mov_b32 v45, 925353388\n" \
  "v_mov_b32_dpp v46, v14 row_newbcast:0 row_mask:0xf bank_mask:0xf\n" \
  "v_mov_b32_dpp v240, v14 row_newbcast:0 row_mask:0xf bank_mask:0xf\n" \
  "s_nop 1\n" \
  "v_permlane16_swap_b32 v46, v240\n" \
  "v_pk_add_f32 v[14:15], v[14:15], v[46:47] op_sel_hi:[1,0] neg_lo:[0,1] neg_hi:[0,1]\n" \
  "v_pk_add_f32 v[18:19], v[18:19], v[46:47] op_sel_hi:[1,0] neg_lo:[0,1] neg_hi:[0,1]\n" \
  "v_pk_add_f32 v[20:21], v[20:21], v[46:47] op_sel_hi:[1,0] neg_lo:[0,1] neg_hi:[0,1]\n" \
  "v_pk_add_f32 v[32:33], v[32:33], v[46:47] op_sel_hi:[1,0] neg_lo:[0,1] neg_hi:[0,1]\n" \
  "v_pk_add_f32 v[34:35], v[34:35], v[46:47] op_sel_hi:[1,0] neg_lo:[0,1] neg_hi:[0,1]\n" \
  "v_pk_add_f32 v[36:37], v[36:37], v[46:47] op_sel_hi:[1,0] neg_lo:[0,1] neg_hi:[0,1]\n" \
  "v_pk_add_f32 v[38:39], v[38:39], v[46:47] op_sel_hi:[1,0] neg_lo:[0,1] neg_hi:[0,1]\n" \
  "v_pk_add_f32 v[40:41], v[40:41], v[46:47] op_sel_hi:[1,0] neg_lo:[0,1] neg_hi:[0,1]\n" \
  "v_pk_add_f32 v[42:43], v[42:43], v[46:47] op_sel_hi:[1,0] neg_lo:[0,1] neg_hi:[0,1]\n" \
  "v_pk_add_f32 v[46:47], v[14:15], v[18:19]\n" \
  "v_pk_mul_f32 v[48:49], v[14:15], v[14:15]\n" \
  "v_pk_fma_f32 v[48:49], v[18:19], v[18:19], v[48:49]\n" \
  "v_pk_add_f32 v[46:47], v[46:47], v[20:21]\n" \
  "v_pk_fma_f32 v[48:49], v[20:21], v[20:21], v[48:49]\n" \
  "v_pk_add_f32 v[46:47], v[46:47], v[32:33]\n" \
  "v_pk_fma_f32 v[48:49], v[32:33], v[32:33], v[48:49]\n" \
  "v_pk_add_f32 v[46:47], v[46:47], v[34:35]\n" \
  "v_pk_fma_f32 v[48:49], v[34:35], v[34:35], v[48:49]\n" \
  "v_pk_add_f32 v[46:47], v[46:47], v[36:37]\n" \
  "v_pk_fma_f32 v[48:49], v[36:37], v[36:37], v[48:49]\n" \
  "v_pk_add_f32 v[46:47], v[46:47], v[38:39]\n" \
  "v_pk_fma_f32 v[48:49], v[38:39], v[38:39], v[48:49]\n" \
  "v_pk_add_f32 v[46:47], v[46:47], v[40:41]\n" \
  "v_pk_fma_f32 v[48:49], v[40:41], v[40:41], v[48:49]\n" \
  "v_pk_add_f32 v[46:47], v[46:47], v[42:43]\n" \
  "v_pk_fma_f32 v[48:49], v[42:43], v[42:43], v[48:49]\n" \
  "v_add_f32 v240, v46, v47\n" \
  "v_add_f32 v238, v48, v49\n" \
  "s_nop 1\n" \
  "v_permlane16_swap_b32 v240, v238\n" \
  "v_add_f32 v240, v240, v238\n" \
  "s_nop 1\n" \
  "v_add_f32_dpp v240, v240, v240 quad_perm:[1,0,3,2] row_mask:0xf bank_mask:0xf\n" \
  "s_nop 1\n" \
  "v_add_f32_dpp v240, v240, v240 quad_perm:[2,3,0,1] row_mask:0xf bank_mask:0xf\n" \
  "s_nop 1\n" \
  "v_add_f32_dpp v240, v240, v240 row_half_mirror row_mask:0xf bank_mask:0xf\n" \
  "s_nop 1\n" \
  "v_add_f32_dpp v240, v240, v240 row_mirror row_mask:0xf bank_mask:0xf\n" \
  "v_mov_b32 v238, v240\n" \
  "s_nop 1\n" \
  "v_permlane16_swap_b32 v240, v238\n" \
  "v_mul_f32 v240, s35, v240\n" \
  "v_fma_f32 v238, v238, s35, v45\n" \
  "v_fma_f32 v238, -v240, v240, v238\n" \
  "v_max_f32 v238, v238, v45\n" \
  "v_rsq_f32 v44, v238\n" \
  "s_nop 0\n" \
  "v_mul_f32_e64 v45, -v240, v44\n" \
  "v_pk_fma_f32 v[46:47], v[14:15], v[44:45], v[44:45] op_sel:[0,0,1] op_sel_hi:[1,0,1]\n" \
  "v_pk_fma_f32 v[48:49], v[18:19], v[44:45], v[44:45] op_sel:[0,0,1] op_sel_hi:[1,0,1]\n" \
  "ds_write_b128 v239, v[46:49] offset:0\n" \
  "v_pk_fma_f32 v[46:47], v[20:21], v[44:45], v[44:45] op_sel:[0,0,1] op_sel_hi:[1,0,1]\n" \
  "v_pk_fma_f32 v[48:49], v[32:33], v[44:45], v[44:45] op_sel:[0,0,1] op_sel_hi:[1,0,1]\n" \
  "ds_write_b128 v239, v[46:49] offset:16\n" \
  "v_pk_fma_f32 v[18:19], v[34:35], v[44:45], v[44:45] op_sel:[0,0,1] op_sel_hi:[1,0,1]\n" \
  "v_pk_fma_f32 v[20:21], v[36:37], v[44:45], v[44:45] op_sel:[0,0,1] op_sel_hi:[1,0,1]\n" \
  "ds_write_b128 v239, v[18:21] offset:1088\n" \
  "v_pk_fma_f32 v[18:19], v[38:39], v[44:45], v[44:45] op_sel:[0,0,1] op_sel_hi:[1,0,1]\n" \
  "v_pk_fma_f32 v[20:21], v[40:41], v[44:45], v[44:45] op_sel:[0,0,1] op_sel_hi:[1,0,1]\n" \
  "ds_write_b128 v239, v[18:21] offset:1104\n" \
  "v_pk_fma_f32 v[42:43], v[42:43], v[44:45], v[44:45] op_sel:[0,0,1] op_sel_hi:[1,0,1]\n" \
  "ds_write_b64 v241, v[42:43] offset:2176\n" \
  "s_waitcnt vmcnt(10)\n" \
  "v_readfirstlane_b32 s18, v30\n" \
  "v_readfirstlane_b32 s19, v31\n" \
  "s_add_u32 s18, s18, 1\n" \
  "s_addc_u32 s19, s19, 0\n" \
  "s_waitcnt lgkmcnt(0)\n" \
  "s_barrier\n" \
  "v_and_b32 v241, 65535, v26\n" \
  "ds_read_u16 v18, v241 offset:19584\n" \
  "ds_read_u16 v19, v241 offset:20162\n" \
  "ds_read_u16 v20, v241 offset:20740\n" \
  "ds_read_u16 v21, v241 offset:21318\n" \
  "ds_read_u16 v30, v241 offset:25728\n" \
  "ds_read_u16 v31, v241 offset:26306\n" \
  "ds_read_u16 v32, v241 offset:26884\n" \
  "ds_read_u16 v33, v241 offset:27462\n" \
  "s_add_u32 s32, s82, 2392064\n" \
  "s_cmp_eq_u32 s20, 1\n" \
  "s_cselect_b64 vcc, 255, s[58:59]\n" \
  "v_mov_b32 v241, 0\n" \
  "v_and_b32 v240, 65535, v27\n" \
  "ds_read_u16 v34, v240 offset:19584\n" \
  "ds_read_u16 v35, v240 offset:20162\n" \
  "ds_read_u16 v36, v240 offset:20740\n" \
  "ds_read_u16 v37, v240 offset:21318\n" \
  "ds_read_u16 v38, v240 offset:25728\n" \
  "ds_read_u16 v39, v240 offset:26306\n" \
  "ds_read_u16 v40, v240 offset:26884\n" \
  "ds_read_u16 v41, v240 offset:27462\n" \
  "v_lshrrev_b32 v240, 16, v26\n" \
  "s_waitcnt lgkmcnt(11)\n" \
  "v_lshl_or_b32 v18, v30, 16, v18\n" \
  "s_waitcnt lgkmcnt(10)\n" \
  "v_lshl_or_b32 v19, v31, 16, v19\n" \
  "s_waitcnt lgkmcnt(9)\n" \
  "v_lshl_or_b32 v20, v32, 16, v20\n" \
  "s_waitcnt lgkmcnt(8)\n" \
  "v_lshl_or_b32 v21, v33, 16, v21\n" \
  "buffer_store_dwordx4 v[18:21], v240, s[36:39], s32 offen offset:0 sc1\n" \
  "v_and_b32 v30, 2147450879, v18\n" \
  "v_pk_max_u16 v241, v241, v30\n" \
  "v_and_b32 v31, 2147450879, v19\n" \
  "v_pk_max_u16 v241, v241, v31\n" \
  "v_and_b32 v32, 2147450879, v20\n" \
  "v_pk_max_u16 v241, v241, v32\n" \
  "v_and_b32 v33, 2147450879, v21\n" \
  "v_pk_max_u16 v241, v241, v33\n" \
  "v_and_b32 v240, 65535, v28\n" \
  "ds_read_u16 v18, v240 offset:19584\n" \
  "ds_read_u16 v19, v240 offset:20162\n" \
  "ds_read_u16 v20, v240 offset:20740\n" \
  "ds_read_u16 v21, v240 offset:21318\n" \
  "ds_read_u16 v30, v240 offset:25728\n" \
  "ds_read_u16 v31, v240 offset:26306\n" \
  "ds_read_u16 v32, v240 offset:26884\n" \
  "ds_read_u16 v33, v240 offset:27462\n" \
  "v_lshrrev_b32 v240, 16, v27\n" \
  "s_waitcnt lgkmcnt(11)\n" \
  "v_lshl_or_b32 v34, v38, 16, v34\n" \
  "s_waitcnt lgkmcnt(10)\n" \
  "v_lshl_or_b32 v35, v39, 16, v35\n" \
  "s_waitcnt lgkmcnt(9)\n" \
  "v_lshl_or_b32 v36, v40, 16, v36\n" \
  "s_waitcnt lgkmcnt(8)\n" \
  "v_lshl_or_b32 v37, v41, 16, v37\n" \
  "buffer_store_dwordx4 v[34:37], v240, s[36:39], s32 offen offset:0 sc1\n" \
  "v_and_b32 v38, 2147450879, v34\n" \
  "v_pk_max_u16 v241, v241, v38\n" \
  "v_and_b32 v39, 2147450879, v35\n" \
  "v_pk_max_u16 v241, v241, v39\n" \
  "v_and_b32 v40, 2147450879, v36\n" \
  "v_pk_max_u16 v241, v241, v40\n" \
  "v_and_b32 v41, 2147450879, v37\n" \
  "v_pk_max_u16 v241, v241, v41\n" \
  "s_mov_b64 s[96:97], vcc\n" \
  "v_lshrrev_b32 v240, 16, v28\n" \
  "s_waitcnt lgkmcnt(3)\n" \
  "v_lshl_or_b32 v18, v30, 16, v18\n" \
  "s_waitcnt lgkmcnt(2)\n" \
  "v_lshl_or_b32 v19, v31, 16, v19\n" \
  "s_waitcnt lgkmcnt(1)\n" \
  "v_lshl_or_b32 v20, v32, 16, v20\n" \
  "s_waitcnt lgkmcnt(0)\n" \
  "v_lshl_or_b32 v21, v33, 16, v21\n" \
  "s_mov_b64 exec, s[96:97]\n" \
  "buffer_store_dwordx4 v[18:21], v240, s[36:39], s32 offen offset:0 sc1\n" \
  "s_mov_b64 exec, -1\n" \
  "v_and_b32 v30, 2147450879, v18\n" \
  "v_pk_max_u16 v241, v241, v30\n" \
  "v_and_b32 v31, 2147450879, v19\n" \
  "v_pk_max_u16 v241, v241, v31\n" \
  "v_and_b32 v32, 2147450879, v20\n" \
  "v_pk_max_u16 v241, v241, v32\n" \
  "v_and_b32 v33, 2147450879, v21\n" \
  "v_pk_max_u16 v241, v241, v33\n" \
  "v_lshlrev_b32 v255, 16, v241\n" \
  "v_max_u32 v241, v241, v255\n" \
  "v_mov_b32 v255, 161664\n" \
  "s_nop 0\n" \
  "v_max_u32_dpp v241, v241, v241 quad_perm:[1,0,3,2] row_mask:0xf bank_mask:0xf\n" \
  "s_nop 1\n" \
  "v_max_u32_dpp v241, v241, v241 quad_perm:[2,3,0,1] row_mask:0xf bank_mask:0xf\n" \
  "s_nop 1\n" \
  "v_max_u32_dpp v241, v241, v241 row_half_mirror row_mask:0xf bank_mask:0xf\n" \
  "s_nop 1\n" \
  "v_max_u32_dpp v241, v241, v241 row_mirror row_mask:0xf bank_mask:0xf\n" \
  "s_mov_b32 exec_lo, 65537\n" \
  "s_mov_b32 exec_hi, 65537\n" \
  "ds_max_u32 v255, v241 offset:4\n" \
  "s_mov_b64 exec, -1\n" \
  "s_mul_i32 s33, s20, 4896\n" \
  "v_and_b32 v241, 65535, v10\n" \
  "v_add_u32 v240, s33, v241\n" \
  "v_and_b32 v239, 65535, v11\n" \
  "v_add_u32 v238, s33, v239\n" \
  "v_and_b32 v237, 65535, v12\n" \
  "v_add_u32 v236, s33, v237\n" \
  "v_and_b32 v235, 65535, v13\n" \
  "v_add_u32 v234, s33, v235\n" \
  "v_lshrrev_b32 v233, 16, v10\n" \
  "v_add_u32 v232, s33, v233\n" \
  "v_lshrrev_b32 v231, 16, v11\n" \
  "v_add_u32 v230, s33, v231\n" \
  "v_lshrrev_b32 v229, 16, v12\n" \
  "v_add_u32 v228, s33, v229\n" \
  "v_lshrrev_b32 v227, 16, v13\n" \
  "v_add_u32 v226, s33, v227\n" \
  "ds_read_b64 v[10:11], v240 offset:0\n" \
  "ds_read_b64 v[12:13], v238 offset:0\n" \
  "ds_read_b64 v[14:15], v236 offset:0\n" \
  "ds_read_b64 v[18:19], v234 offset:0\n" \
  "ds_read_b64 v[20:21], v232 offset:0\n" \
  "ds_read_b64 v[26:27], v230 offset:0\n" \
  "ds_read_b64 v[28:29], v228 offset:0\n" \
  "ds_read_b64 v[30:31], v226 offset:0\n" \
  "ds_read_b64 v[32:33], v240 offset:2448\n" \
  "ds_read_b64 v[34:35], v238 offset:2448\n" \
  "ds_read_b64 v[36:37], v236 offset:2448\n" \
  "ds_read_b64 v[38:39], v234 offset:2448\n" \
  "ds_read_b64 v[40:41], v232 offset:2448\n" \
  "ds_read_b64 v[42:43], v230 offset:2448\n" \
  "ds_read_b64 v[44:45], v228 offset:2448\n" \
  "ds_read_b64 v[46:47], v226 offset:2448\n" \
  "s_mul_i32 s98, s20, 589824\n" \
  "s_mul_i32 s100, s84, 1152\n" \
  "s_add_u32 s98, s98, s100\n" \
  "s_and_b32 s100, s85, 7\n" \
  "s_lshl_b32 s100, s100, 7\n" \
  "s_add_u32 s12, s98, s100\n" \
  "s_mul_i32 s98, s20, 30720\n" \
  "s_add_u32 s57, s98, 38272\n" \
  "s_lshl_b32 s98, s20, 10\n" \
  "s_add_u32 s8, s98, s82\n" \
  "s_lshl_b32 s98, s84, 2\n" \
  "s_add_u32 s98, s98, s20\n" \
  "s_lshl_b32 s98, s98, 16\n" \
  "s_lshl_b32 s100, s85, 11\n" \
  "s_add_u32 s98, s98, s100\n" \
  "s_add_u32 s24, s98, 5308416\n" \
  "s_add_u32 s10, s87, 7405568\n" \
  "s_lshl_b32 s98, s89, 16\n" \
  "s_lshl_b32 s100, s20, 14\n" \
  "s_add_u32 s98, s98, s100\n" \
  "s_add_u32 s14, s98, 5308416\n" \
  "s_mul_i32 s98, s88, 4505600\n" \
  "s_lshl_b32 s100, s90, 2\n" \
  "s_lshr_b32 s101, s84, 1\n" \
  "s_add_u32 s100, s100, s101\n" \
  "s_lshl_b32 s100, s100, 12\n" \
  "s_add_u32 s98, s98, s100\n" \
  "s_add_u32 s16, s98, 13090816\n" \
  "s_mul_i32 s98, s20, 4896\n" \
  "s_add_u32 s21, s98, 9216\n" \
  "s_mul_i32 s27, s88, 9232\n" \
  "s_lshl_b32 s98, s20, 2\n" \
  "s_add_u32 s28, s98, s28\n" \
  "s_lshl_b32 s98, s84, 8\n" \
  "s_add_u32 s29, s98, 2048\n" \
  "s_lshl_b32 s98, s85, 3\n" \
  "s_add_u32 s30, s98, s29\n" \
  "s_cmp_lt_u32 s20, 2\n" \
  "s_cselect_b32 s9, 8192, 0\n" \
  "s_add_u32 s9, s9, s8\n" \
  "s_mul_i32 s98, s84, 18\n" \
  "s_add_u32 s98, s98, s85\n" \
  "s_sub_u32 s81, s98, 14\n" \
  "s_cmp_lt_u32 s2, 112\n" \
  "s_cselect_b32 s81, 144, s81\n" \
  "s_cmp_eq_u32 s81, 128\n" \
  "s_cselect_b32 s32, 2, 0\n" \
  "s_or_b32 s27, s27, s32\n" \
  "s_lshl_b32 s98, s81, 6\n" \
  "s_add_u32 s22, s98, 524288\n" \
  "s_lshl_b32 s98, s81, 4\n" \
  "s_add_u32 s32, s98, 24576\n" \
  "s_load_dwordx4 s[60:63], s[76:77], s32\n" \
  "v_lshrrev_b32 v240, 5, v0\n" \
  "v_lshlrev_b32 v238, 4, v240\n" \
  "v_lshl_add_u32 v240, v0, 3, v238\n" \
  "v_add_u32 v238, 35216, v240\n" \
  "v_add_u32 v236, 32496, v240\n" \
  "v_add_u32 v234, 37392, v240\n" \
  "v_add_u32 v232, 34672, v240\n" \
  "v_and_b32 v240, 48, v191\n" \
  "v_lshl_add_u32 v230, v248, 6, v240\n" \
  "v_cmp_gt_u32 vcc, 32, v0\n" \
  "v_mov_b32 v48, 1.0\n" \
  "s_nop 0\n" \
  "v_cndmask_b32 v236, v236, v234, vcc\n" \
  "v_cndmask_b32 v48, v48, 0, vcc\n" \
  "v_cndmask_b32 v240, v240, v230, s[58:59]\n" \
  "v_mov_b32 v53, 0\n" \
  "v_mov_b32 v57, 0\n" \
  "v_mov_b32 v61, 0\n" \
  "v_mov_b32 v65, 0\n" \
  "s_sub_u32 s98, s85, 14\n" \
  "s_mul_i32 s98, s98, 160\n" \
  "v_add_u32 v248, s98, v0\n" \
  "v_mul_u32_u24 v234, 52429, v248\n" \
  "v_lshrrev_b32 v228, 18, v234\n" \
  "v_mul_u32_u24 v234, 6272, v228\n" \
  "v_lshl_add_u32 v228, v248, 7, v234\n" \
  "s_mul_i32 s98, s84, 576\n" \
  "s_add_u32 s98, s98, 2304\n" \
  "s_and_b32 s25, s98, 16777088\n" \
  "v_lshrrev_b32 v248, 2, v0\n" \
  "v_mul_u32_u24 v234, 9344, v248\n" \
  "v_and_b32 v248, 3, v0\n" \
  "v_lshl_add_u32 v226, v248, 4, v234\n" \
  "v_lshrrev_b32 v248, 4, v0\n" \
  "v_xor_b32 v225, v0, v248\n" \
  "v_and_b32 v248, 3, v225\n" \
  "v_lshl_add_u32 v225, v248, 4, v234\n" \
  "s_movk_i32 s32, 8192\n" \
  "buffer_load_dwordx4 v[66:69], v246, s[36:39], s32 offen offset:0 sc1\n" \
  "s_movk_i32 s32, 17408\n" \
  "buffer_load_dwordx4 v[70:73], v246, s[36:39], s32 offen offset:0 sc1\n" \
  "s_movk_i32 s32, 26624\n" \
  "buffer_load_dwordx4 v[74:77], v246, s[36:39], s32 offen offset:0 sc1\n" \
  "s_movk_i32 s32, 12288\n" \
  "buffer_load_dwordx4 v[78:81], v246, s[36:39], s32 offen offset:0 sc1\n" \
  "s_movk_i32 s32, 21504\n" \
  "buffer_load_dwordx4 v[82:85], v246, s[36:39], s32 offen offset:0 sc1\n" \
  "s_movk_i32 s32, 30720\n" \
  "buffer_load_dwordx4 v[86:89], v246, s[36:39], s32 offen offset:0 sc1\n" \
  "s_movk_i32 s32, 16384\n" \
  "buffer_load_dwordx4 v[90:93], v245, s[36:39], s32 offen offset:0 sc1\n" \
  "s_movk_i32 s32, 25600\n" \
  "buffer_load_dwordx4 v[94:97], v245, s[36:39], s32 offen offset:0 sc1\n" \
  "s_mov_b32 s32, 34816\n" \
  "buffer_load_dwordx4 v[98:101], v245, s[36:39], s32 offen offset:0 sc1\n" \
  "s_lshl_b32 s98, s2, 3\n" \
  "s_add_u32 s56, s98, 4096\n" \
  "s_lshl_b32 s98, s20, 10\n" \
  "s_add_u32 s33, s98, 62848\n" \
  "v_mov_b32 v102, s18\n" \
  "s_and_b32 s32, s19, 16777215\n" \
  "s_waitcnt vmcnt(9) lgkmcnt(0)\n" \
  "s_barrier\n" \
  "ds_read_b32 v103, v255 offset:4\n" \
  "s_cmp_eq_u32 s20, 3\n" \
  "s_waitcnt lgkmcnt(0)\n" \
  "v_lshrrev_b32 v103, 23, v103\n" \
  "v_lshl_or_b32 v103, v103, 24, s32\n" \
  "s_cselect_b64 exec, 1, 0\n" \
  "buffer_store_dwordx2 v[102:103], off, s[36:39], s56 sc1\n" \
  "s_mov_b64 exec, -1\n" \
  "s_add_u32 m0, s33, 0\n" \
  "s_movk_i32 s32, 0\n" \
  "buffer_load_dwordx4 v246, s[76:79], s32 offen offset:0 lds\n" \
  "s_add_u32 m0, s33, 30720\n" \
  "s_movk_i32 s32, 4096\n" \
  "buffer_load_dwordx4 v246, s[76:79], s32 offen offset:0 lds\n" \
  "s_add_u32 m0, s33, 61440\n" \
  "s_movk_i32 s32, 8192\n" \
  "buffer_load_dwordx4 v246, s[76:79], s32 offen offset:0 lds\n" \
  "s_movk_i32 s32, 14336\n" \
  "buffer_load_dwordx4 v[102:105], v246, s[76:79], s32 offen offset:0\n" \
  "s_movk_i32 s32, 23552\n" \
  "buffer_load_dwordx4 v[106:109], v252, s[76:79], s32 offen offset:0\n" \
  "s_movk_i32 s32, 18432\n" \
  "buffer_load_dwordx4 v[110:113], v246, s[76:79], s32 offen offset:0\n" \
  "s_waitcnt vmcnt(7)\n" \
  ".Lacc_%=:\n" \
  "v_xor_b32 v248, s18, v66\n" \
  "v_bitop3_b32 v248, v67, s19, v248 bitop3:0xbe\n" \
  "v_bitop3_b32 v248, v70, s18, v248 bitop3:0xbe\n" \
  "v_bitop3_b32 v248, v71, s19, v248 bitop3:0xbe\n" \
  "v_bitop3_b32 v248, v74, s18, v248 bitop3:0xbe\n" \
  "v_bitop3_b32 v248, v75, s19, v248 bitop3:0xbe\n" \
  "v_bitop3_b32 v248, v78, s18, v248 bitop3:0xbe\n" \
  "v_bitop3_b32 v248, v79, s19, v248 bitop3:0xbe\n" \
  "v_bitop3_b32 v248, v82, s18, v248 bitop3:0xbe\n" \
  "v_bitop3_b32 v248, v83, s19, v248 bitop3:0xbe\n" \
  "v_bitop3_b32 v248, v86, s18, v248 bitop3:0xbe\n" \
  "v_bitop3_b32 v248, v87, s19, v248 bitop3:0xbe\n" \
  "v_xor_b32 v234, s18, v90\n" \
  "v_bitop3_b32 v234, v91, s19, v234 bitop3:0xbe\n" \
  "v_bitop3_b32 v234, v94, s18, v234 bitop3:0xbe\n" \
  "v_bitop3_b32 v234, v95, s19, v234 bitop3:0xbe\n" \
  "v_bitop3_b32 v234, v98, s18, v234 bitop3:0xbe\n" \
  "v_bitop3_b32 v234, v99, s19, v234 bitop3:0xbe\n" \
  "v_bitop3_b32 v248, v234, s58, v248 bitop3:0xea\n" \
  "v_cmp_ne_u32 vcc, 0, v248\n" \
  "s_cbranch_vccnz .Lacs_%=\n" \
  ".Lacd_%=:\n" \
  "s_mov_b32 m0, 162176\n" \
  "s_cmp_lt_u32 s20, 2\n" \
  "s_cselect_b64 exec, -1, 0\n" \
  "s_cmp_eq_u32 s20, 2\n" \
  "s_cselect_b32 exec_lo, -1, exec_lo\n" \
  "s_cmp_lt_u32 s2, 112\n" \
  "s_cselect_b64 exec, 0, exec\n" \
  "buffer_load_dword v228, s[64:67], s25 offen lds\n" \
  "s_mov_b64 exec, -1\n" \
  "v_mov_b32 v248, 0\n" \
  "v_pk_add_f32 v[114:115], v[68:69], v[72:73]\n" \
  "v_pk_add_f32 v[114:115], v[114:115], v[76:77]\n" \
  "v_cvt_pk_bf16_f32 v245, v114, v115\n" \
  "v_lshlrev_b32 v114, 16, v245\n" \
  "v_and_b32 v115, 4294901760, v245\n" \
  "ds_write_b64 v238, v[114:115] offset:0\n" \
  "v_max3_f32 v248, |v114|, |v115|, v248\n" \
  "v_pk_add_f32 v[66:67], v[80:81], v[84:85]\n" \
  "v_pk_add_f32 v[66:67], v[66:67], v[88:89]\n" \
  "v_cvt_pk_bf16_f32 v245, v66, v67\n" \
  "v_lshlrev_b32 v66, 16, v245\n" \
  "v_and_b32 v67, 4294901760, v245\n" \
  "v_pk_add_f32 v[66:67], v[66:67], v[48:49] op_sel_hi:[1,0]\n" \
  "v_cvt_pk_bf16_f32 v245, v66, v67\n" \
  "v_lshlrev_b32 v66, 16, v245\n" \
  "v_and_b32 v67, 4294901760, v245\n" \
  "ds_write_b64 v236, v[66:67] offset:0\n" \
  "v_max3_f32 v248, |v66|, |v67|, v248\n" \
  "s_mov_b64 exec, s[58:59]\n" \
  "v_pk_add_f32 v[48:49], v[92:93], v[96:97]\n" \
  "v_pk_add_f32 v[48:49], v[48:49], v[100:101]\n" \
  "v_cvt_pk_bf16_f32 v245, v48, v49\n" \
  "v_lshlrev_b32 v48, 16, v245\n" \
  "v_and_b32 v49, 4294901760, v245\n" \
  "v_add_f32 v48, 1.0, v48\n" \
  "v_add_f32 v49, 1.0, v49\n" \
  "v_cvt_pk_bf16_f32 v245, v48, v49\n" \
  "v_lshlrev_b32 v48, 16, v245\n" \
  "v_and_b32 v49, 4294901760, v245\n" \
  "ds_write_b64 v232, v[48:49] offset:0\n" \
  "v_max3_f32 v248, |v48|, |v49|, v248\n" \
  "s_mov_b64 exec, -1\n" \
  "s_nop 0\n" \
  "v_max_u32_dpp v248, v248, v248 quad_perm:[1,0,3,2] row_mask:0xf bank_mask:0xf\n" \
  "s_nop 1\n" \
  "v_max_u32_dpp v248, v248, v248 quad_perm:[2,3,0,1] row_mask:0xf bank_mask:0xf\n" \
  "s_nop 1\n" \
  "v_max_u32_dpp v248, v248, v248 row_half_mirror row_mask:0xf bank_mask:0xf\n" \
  "s_nop 1\n" \
  "v_max_u32_dpp v248, v248, v248 row_mirror row_mask:0xf bank_mask:0xf\n" \
  "s_mov_b32 exec_lo, 65537\n" \
  "s_mov_b32 exec_hi, 65537\n" \
  "ds_max_u32 v255, v248\n" \
  "s_mov_b64 exec, -1\n" \
  "s_waitcnt vmcnt(1) lgkmcnt(0)\n" \
  "s_barrier\n" \
  "ds_read_b32 v48, v255 offset:0\n" \
  "ds_read_b64 v[66:67], v241 offset:32768\n" \
  "ds_read_b64 v[68:69], v241 offset:35216\n" \
  "ds_read_b64 v[70:71], v239 offset:32768\n" \
  "ds_read_b64 v[72:73], v239 offset:35216\n" \
  "ds_read_b64 v[74:75], v237 offset:32768\n" \
  "ds_read_b64 v[76:77], v237 offset:35216\n" \
  "ds_read_b64 v[78:79], v235 offset:32768\n" \
  "ds_read_b64 v[80:81], v235 offset:35216\n" \
  "ds_read_b64 v[82:83], v233 offset:32768\n" \
  "ds_read_b64 v[84:85], v233 offset:35216\n" \
  "ds_read_b64 v[86:87], v231 offset:32768\n" \
  "ds_read_b64 v[88:89], v231 offset:35216\n" \
  "ds_read_b64 v[90:91], v229 offset:32768\n" \
  "ds_read_b64 v[92:93], v229 offset:35216\n" \
  "ds_read_b64 v[94:95], v227 offset:32768\n" \
  "ds_read_b64 v[96:97], v227 offset:35216\n" \
  "v_add_u32 v255, 62848, v252\n" \
  "ds_read_b128 a[200:203], v255 offset:0\n" \
  "ds_read_b128 a[204:207], v255 offset:1024\n" \
  "ds_read_b128 a[168:171], v255 offset:2048\n" \
  "ds_read_b128 a[172:175], v255 offset:3072\n" \
  "s_waitcnt lgkmcnt(15)\n" \
  "v_lshrrev_b32 v48, 23, v48\n" \
  "v_max_u32 v48, 64, v48\n" \
  "s_nop 0\n" \
  "v_readfirstlane_b32 s26, v48\n" \
  "v_sub_u32 v48, 256, v48\n" \
  "v_lshlrev_b32 v48, 23, v48\n" \
  "v_pk_mul_f32 v[66:67], v[66:67], v[48:49] op_sel_hi:[1,0]\n" \
  "v_pk_mul_f32 v[68:69], v[68:69], v[48:49] op_sel_hi:[1,0]\n" \
  "v_pk_fma_f32 v[98:99], v[10:11], v[66:67], v[68:69]\n" \
  "v_pk_fma_f32 v[10:11], v[32:33], v[66:67], v[68:69]\n" \
  "v_pk_mul_f32 v[70:71], v[70:71], v[48:49] op_sel_hi:[1,0]\n" \
  "v_pk_mul_f32 v[72:73], v[72:73], v[48:49] op_sel_hi:[1,0]\n" \
  "v_pk_fma_f32 v[32:33], v[12:13], v[70:71], v[72:73]\n" \
  "v_pk_fma_f32 v[12:13], v[34:35], v[70:71], v[72:73]\n" \
  "v_pk_mul_f32 v[74:75], v[74:75], v[48:49] op_sel_hi:[1,0]\n" \
  "s_waitcnt lgkmcnt(14)\n" \
  "v_pk_mul_f32 v[76:77], v[76:77], v[48:49] op_sel_hi:[1,0]\n" \
  "v_pk_fma_f32 v[34:35], v[14:15], v[74:75], v[76:77]\n" \
  "v_pk_fma_f32 v[14:15], v[36:37], v[74:75], v[76:77]\n" \
  "s_waitcnt lgkmcnt(13)\n" \
  "v_pk_mul_f32 v[78:79], v[78:79], v[48:49] op_sel_hi:[1,0]\n" \
  "s_waitcnt lgkmcnt(12)\n" \
  "v_pk_mul_f32 v[80:81], v[80:81], v[48:49] op_sel_hi:[1,0]\n" \
  "v_pk_fma_f32 v[36:37], v[18:19], v[78:79], v[80:81]\n" \
  "v_pk_fma_f32 v[18:19], v[38:39], v[78:79], v[80:81]\n" \
  "s_waitcnt lgkmcnt(11)\n" \
  "v_pk_mul_f32 v[82:83], v[82:83], v[48:49] op_sel_hi:[1,0]\n" \
  "s_waitcnt lgkmcnt(10)\n" \
  "v_pk_mul_f32 v[84:85], v[84:85], v[48:49] op_sel_hi:[1,0]\n" \
  "v_pk_fma_f32 v[38:39], v[20:21], v[82:83], v[84:85]\n" \
  "v_pk_fma_f32 v[20:21], v[40:41], v[82:83], v[84:85]\n" \
  "s_waitcnt lgkmcnt(9)\n" \
  "v_pk_mul_f32 v[86:87], v[86:87], v[48:49] op_sel_hi:[1,0]\n" \
  "s_waitcnt lgkmcnt(8)\n" \
  "v_pk_mul_f32 v[88:89], v[88:89], v[48:49] op_sel_hi:[1,0]\n" \
  "v_pk_fma_f32 v[40:41], v[26:27], v[86:87], v[88:89]\n" \
  "v_pk_fma_f32 v[26:27], v[42:43], v[86:87], v[88:89]\n" \
  "s_waitcnt lgkmcnt(7)\n" \
  "v_pk_mul_f32 v[90:91], v[90:91], v[48:49] op_sel_hi:[1,0]\n" \
  "s_waitcnt lgkmcnt(6)\n" \
  "v_pk_mul_f32 v[92:93], v[92:93], v[48:49] op_sel_hi:[1,0]\n" \
  "v_pk_fma_f32 v[42:43], v[28:29], v[90:91], v[92:93]\n" \
  "v_pk_fma_f32 v[28:29], v[44:45], v[90:91], v[92:93]\n" \
  "s_waitcnt lgkmcnt(5)\n" \
  "v_pk_mul_f32 v[94:95], v[94:95], v[48:49] op_sel_hi:[1,0]\n" \
  "s_waitcnt lgkmcnt(4)\n" \
  "v_pk_mul_f32 v[96:97], v[96:97], v[48:49] op_sel_hi:[1,0]\n" \
  "v_pk_fma_f32 v[44:45], v[30:31], v[94:95], v[96:97]\n" \
  "v_pk_fma_f32 v[30:31], v[46:47], v[94:95], v[96:97]\n" \
  "v_cvt_pk_f16_f32 v46, v98, v99\n" \
  "v_cvt_pk_f16_f32 v47, v32, v33\n" \
  "v_cvt_pk_f16_f32 v48, v34, v35\n" \
  "v_cvt_pk_f16_f32 v49, v36, v37\n" \
  "v_cvt_pk_f16_f32 v32, v38, v39\n" \
  "v_cvt_pk_f16_f32 v33, v40, v41\n" \
  "s_waitcnt lgkmcnt(3)\n" \
  "v_mfma_f32_16x16x32_f16 v[36:39], v[46:49], a[200:203], 0\n" \
  "v_cvt_pk_f16_f32 v34, v42, v43\n" \
  "v_cvt_pk_f16_f32 v35, v44, v45\n" \
  "s_waitcnt lgkmcnt(2)\n" \
  "v_mfma_f32_16x16x32_f16 v[40:43], v[46:49], a[204:207], 0\n" \
  "v_cvt_pk_f16_f32 v44, v10, v11\n" \
  "v_cvt_pk_f16_f32 v45, v12, v13\n" \
  "s_waitcnt lgkmcnt(1)\n" \
  "v_mfma_f32_16x16x32_f16 v[10:13], v[32:35], a[168:171], 0\n" \
  "v_cvt_pk_f16_f32 v46, v14, v15\n" \
  "v_cvt_pk_f16_f32 v47, v18, v19\n" \
  "s_waitcnt lgkmcnt(0)\n" \
  "v_mfma_f32_16x16x32_f16 v[66:69], v[32:35], a[172:175], 0\n" \
  "v_cvt_pk_f16_f32 v32, v20, v21\n" \
  "v_cvt_pk_f16_f32 v33, v26, v27\n" \
  "v_mfma_f32_16x16x32_f16 v[18:21], v[44:47], a[200:203], 0\n" \
  "v_cvt_pk_f16_f32 v34, v28, v29\n" \
  "v_cvt_pk_f16_f32 v35, v30, v31\n" \
  "v_mfma_f32_16x16x32_f16 v[26:29], v[44:47], a[204:207], 0\n" \
  "ds_read_b128 a[208:211], v255 offset:30720\n" \
  "v_mfma_f32_16x16x32_f16 v[44:47], v[32:35], a[168:171], 0\n" \
  "ds_read_b128 a[212:215], v255 offset:31744\n" \
  "v_mfma_f32_16x16x32_f16 v[70:73], v[32:35], a[172:175], 0\n" \
  "ds_read_b128 a[220:223], v255 offset:32768\n" \
  "ds_read_b128 a[224:227], v255 offset:33792\n" \
  "ds_read_b128 v[74:77], v255 offset:61440\n" \
  "ds_read_b128 v[78:81], v255 offset:62464\n" \
  "v_pk_add_f32 v[30:31], v[36:37], v[10:11]\n" \
  "v_pk_add_f32 v[32:33], v[38:39], v[12:13]\n" \
  "v_pk_add_f32 v[82:83], v[40:41], v[66:67]\n" \
  "v_pk_add_f32 v[84:85], v[42:43], v[68:69]\n" \
  "v_pk_add_f32 v[86:87], v[36:37], v[10:11] neg_lo:[0,1] neg_hi:[0,1]\n" \
  "v_pk_add_f32 v[88:89], v[38:39], v[12:13] neg_lo:[0,1] neg_hi:[0,1]\n" \
  "v_pk_add_f32 v[10:11], v[40:41], v[66:67] neg_lo:[0,1] neg_hi:[0,1]\n" \
  "v_pk_add_f32 v[12:13], v[42:43], v[68:69] neg_lo:[0,1] neg_hi:[0,1]\n" \
  "v_pk_add_f32 v[34:35], v[18:19], v[44:45]\n" \
  "v_pk_add_f32 v[36:37], v[20:21], v[46:47]\n" \
  "v_pk_add_f32 v[38:39], v[26:27], v[70:71]\n" \
  "v_pk_add_f32 v[40:41], v[28:29], v[72:73]\n" \
  "v_pk_add_f32 v[66:67], v[18:19], v[44:45] neg_lo:[0,1] neg_hi:[0,1]\n" \
  "v_pk_add_f32 v[68:69], v[20:21], v[46:47] neg_lo:[0,1] neg_hi:[0,1]\n" \
  "v_pk_add_f32 v[18:19], v[26:27], v[70:71] neg_lo:[0,1] neg_hi:[0,1]\n" \
  "v_pk_add_f32 v[20:21], v[28:29], v[72:73] neg_lo:[0,1] neg_hi:[0,1]\n" \
  "v_cvt_pk_f16_f32 v50, v30, v82\n" \
  "v_cvt_pk_f16_f32 v51, v31, v83\n" \
  "v_cvt_pk_f16_f32 v52, v32, v84\n" \
  "v_cvt_pk_f16_f32 v54, v86, v10\n" \
  "v_cvt_pk_f16_f32 v55, v87, v11\n" \
  "s_waitcnt lgkmcnt(5)\n" \
  "v_mfma_f32_16x16x32_f16 v[26:29], a[208:211], v[50:53], 0\n" \
  "v_cvt_pk_f16_f32 v56, v88, v12\n" \
  "s_waitcnt lgkmcnt(4)\n" \
  "v_mfma_f32_16x16x32_f16 v[10:13], a[212:215], v[50:53], 0\n" \
  "v_cvt_pk_f16_f32 v58, v34, v38\n" \
  "v_cvt_pk_f16_f32 v59, v35, v39\n" \
  "v_mfma_f32_16x16x32_f16 v[30:33], a[208:211], v[54:57], 0\n" \
  "v_cvt_pk_f16_f32 v60, v36, v40\n" \
  "v_mfma_f32_16x16x32_f16 v[34:37], a[212:215], v[54:57], 0\n" \
  "v_cvt_pk_f16_f32 v62, v66, v18\n" \
  "v_cvt_pk_f16_f32 v63, v67, v19\n" \
  "v_mfma_f32_16x16x32_f16 v[38:41], a[208:211], v[58:61], 0\n" \
  "v_cvt_pk_f16_f32 v64, v68, v20\n" \
  "v_mfma_f32_16x16x32_f16 v[18:21], a[212:215], v[58:61], 0\n" \
  "s_movk_i32 s32, 4096\n" \
  "buffer_load_dwordx4 v[42:45], v252, s[36:39], s32 offen offset:0 sc1\n" \
  "buffer_load_dwordx4 v[46:49], v252, s[36:39], s32 offen offset:1024 sc1\n" \
  "v_mfma_f32_16x16x32_f16 v[66:69], a[208:211], v[62:65], 0\n" \
  "v_mfma_f32_16x16x32_f16 v[70:73], a[212:215], v[62:65], 0\n" \
  "s_mov_b32 exec_hi, 65535\n" \
  "v_cvt_pk_f16_f32 v14, v26, v10\n" \
  "s_nop 0\n" \
  "v_cvt_pk_f16_f32 v15, v38, v18\n" \
  "ds_write_b64 v102, v[14:15] offset:0\n" \
  "v_cvt_pk_f16_f32 v14, v27, v11\n" \
  "v_cvt_pk_f16_f32 v15, v39, v19\n" \
  "ds_write_b64 v102, v[14:15] offset:512\n" \
  "v_cvt_pk_f16_f32 v14, v28, v12\n" \
  "v_cvt_pk_f16_f32 v15, v40, v20\n" \
  "ds_write_b64 v102, v[14:15] offset:1024\n" \
  "v_cvt_pk_f16_f32 v10, v30, v34\n" \
  "v_cvt_pk_f16_f32 v11, v66, v70\n" \
  "ds_write_b64 v102, v[10:11] offset:256\n" \
  "v_cvt_pk_f16_f32 v10, v31, v35\n" \
  "v_cvt_pk_f16_f32 v11, v67, v71\n" \
  "ds_write_b64 v102, v[10:11] offset:768\n" \
  "v_cvt_pk_f16_f32 v10, v32, v36\n" \
  "v_cvt_pk_f16_f32 v11, v68, v72\n" \
  "ds_write_b64 v102, v[10:11] offset:1280\n" \
  "s_mov_b64 exec, -1\n" \
  "s_waitcnt lgkmcnt(0)\n" \
  "s_barrier\n" \
  "ds_read_b128 v[10:13], v103 offset:0\n" \
  "ds_read_b128 v[18:21], v104 offset:0\n" \
  "ds_read_b128 v[26:29], v105 offset:0\n" \
  "v_lshrrev_b32 v255, 1, v191\n" \
  "v_and_b32 v248, 1, v191\n" \
  "s_lshl_b32 s98, s20, 1\n" \
  "v_add_u32 v245, s98, v248\n" \
  "v_and_b32 v248, 6, v255\n" \
  "v_xor_b32 v241, v245, v248\n" \
  "v_lshlrev_b32 v248, 4, v241\n" \
  "v_lshl_add_u32 v245, v255, 7, v248\n" \
  "v_add_u32 v255, 155008, v245\n" \
  "v_lshlrev_b32 v248, 15, v253\n" \
  "v_sub_u32 v245, v255, v248\n" \
  "s_cmp_eq_u32 s20, 1\n" \
  "s_cselect_b32 s92, 65537, s58\n" \
  "s_cselect_b32 s93, 65537, s59\n" \
  "s_add_u32 s32, s8, 4096\n" \
  "s_add_u32 s33, s8, 8192\n" \
  "s_waitcnt lgkmcnt(2)\n" \
  "v_mfma_f32_16x16x32_f16 v[30:33], a[160:163], v[10:13], 0\n" \
  "v_mfma_f32_16x16x32_f16 v[34:37], a[164:167], v[10:13], 0\n" \
  "s_waitcnt lgkmcnt(1)\n" \
  "v_mfma_f32_16x16x32_f16 v[10:13], a[160:163], v[18:21], 0\n" \
  "v_mfma_f32_16x16x32_f16 v[38:41], a[164:167], v[18:21], 0\n" \
  "s_waitcnt lgkmcnt(0)\n" \
  "v_mfma_f32_16x16x32_f16 v[18:21], a[160:163], v[26:29], 0\n" \
  "v_mfma_f32_16x16x32_f16 v[66:69], a[164:167], v[26:29], 0\n" \
  "ds_read_b128 v[26:29], v255 offset:0\n" \
  "ds_read_b128 v[70:73], v245 offset:4096\n" \
  "v_cvt_pk_f16_f32 v82, v30, v34\n" \
  "v_cvt_pk_f16_f32 v83, v31, v35\n" \
  "v_cvt_pk_f16_f32 v84, v32, v36\n" \
  "v_cvt_pk_f16_f32 v85, v33, v37\n" \
  "v_and_b32 v82, -2, v82\n" \
  "buffer_store_dwordx4 v[82:85], v230, s[36:39], s8 offen offset:0 sc1\n" \
  "v_cvt_pk_f16_f32 v30, v10, v38\n" \
  "v_cvt_pk_f16_f32 v31, v11, v39\n" \
  "v_cvt_pk_f16_f32 v32, v12, v40\n" \
  "v_cvt_pk_f16_f32 v33, v13, v41\n" \
  "v_and_b32 v30, -2, v30\n" \
  "buffer_store_dwordx4 v[30:33], v230, s[36:39], s32 offen offset:0 sc1\n" \
  "v_cvt_pk_f16_f32 v10, v18, v66\n" \
  "v_cvt_pk_f16_f32 v11, v19, v67\n" \
  "v_cvt_pk_f16_f32 v12, v20, v68\n" \
  "v_cvt_pk_f16_f32 v13, v21, v69\n" \
  "v_and_b32 v10, -2, v10\n" \
  "s_mov_b64 exec, s[92:93]\n" \
  "buffer_store_dwordx4 v[10:13], v230, s[36:39], s33 offen offset:0 sc1\n" \
  "s_mov_b64 exec, -1\n" \
  "s_waitcnt vmcnt(3)\n" \
  ".Lycc_%=:\n" \
  "v_xor_b32 v255, s18, v42\n" \
  "v_bitop3_b32 v255, v44, s18, v255 bitop3:0xbe\n" \
  "v_bitop3_b32 v255, v46, s18, v255 bitop3:0xbe\n" \
  "v_bitop3_b32 v255, v48, s18, v255 bitop3:0xbe\n" \
  "v_xor_b32 v248, s19, v43\n" \
  "v_bitop3_b32 v248, v45, s19, v248 bitop3:0xbe\n" \
  "v_bitop3_b32 v248, v47, s19, v248 bitop3:0xbe\n" \
  "v_bitop3_b32 v248, v49, s19, v248 bitop3:0xbe\n" \
  "v_lshl_or_b32 v255, v248, 8, v255\n" \
  "v_cmp_ne_u32 vcc, 0, v255\n" \
  "s_cbranch_vccnz .Lycs_%=\n" \
  ".Lycd_%=:\n" \
  "s_lshl_b32 s33, s20, 10\n" \
  "s_add_u32 m0, s33, 16384\n" \
  "s_add_u32 s32, s22, 2392064\n" \
  "buffer_load_dwordx4 v225, s[36:39], s32 offen offset:0 sc1 lds\n" \
  "s_add_u32 m0, s33, 20480\n" \
  "s_add_u32 s32, s22, 2990080\n" \
  "buffer_load_dwordx4 v225, s[36:39], s32 offen offset:0 sc1 lds\n" \
  "s_add_u32 m0, s33, 24576\n" \
  "s_add_u32 s32, s22, 3588096\n" \
  "buffer_load_dwordx4 v225, s[36:39], s32 offen offset:0 sc1 lds\n" \
  "s_add_u32 m0, s33, 28672\n" \
  "s_add_u32 s32, s22, 4186112\n" \
  "buffer_load_dwordx4 v225, s[36:39], s32 offen offset:0 sc1 lds\n" \
  "s_bitcmp1_b32 s27, 1\n" \
  "s_cbranch_scc1 .Lws_%=\n" \
  ".Lwd_%=:\n" \
  "s_waitcnt lgkmcnt(1)\n" \
  "buffer_store_dwordx4 v[26:29], v252, s[36:39], s24 offen offset:0 sc1\n" \
  "s_waitcnt lgkmcnt(0)\n" \
  "buffer_store_dwordx4 v[70:73], v252, s[36:39], s24 offen offset:1024 sc1\n" \
  "v_max3_u32 v255, v43, v45, v47\n" \
  "v_max_u32 v255, v255, v49\n" \
  "s_nop 1\n" \
  "v_max_u32_dpp v255, v255, v255 quad_perm:[1,0,3,2] row_mask:0xf bank_mask:0xf\n" \
  "s_nop 1\n" \
  "v_max_u32_dpp v255, v255, v255 quad_perm:[2,3,0,1] row_mask:0xf bank_mask:0xf\n" \
  "s_nop 1\n" \
  "v_max_u32_dpp v255, v255, v255 row_half_mirror row_mask:0xf bank_mask:0xf\n" \
  "s_nop 1\n" \
  "v_max_u32_dpp v255, v255, v255 row_mirror row_mask:0xf bank_mask:0xf\n" \
  "s_nop 0\n" \
  "v_readlane_b32 s48, v255, 0\n" \
  "v_readlane_b32 s49, v255, 16\n" \
  "v_readlane_b32 s50, v255, 32\n" \
  "v_readlane_b32 s51, v255, 48\n" \
  "s_max_u32 s48, s48, s49\n" \
  "s_max_u32 s50, s50, s51\n" \
  "s_max_u32 s48, s48, s50\n" \
  "s_lshr_b32 s48, s48, 24\n" \
  "s_max_u32 s48, s48, 64\n" \
  "s_min_u32 s48, s48, 250\n" \
  "s_add_u32 s23, s26, s48\n" \
  "s_sub_u32 s23, s23, 253\n" \
  "s_bfe_u32 s32, s27, 65537\n" \
  "s_add_u32 s32, s32, s48\n" \
  "s_sub_u32 s32, 251, s32\n" \
  "s_lshl_b32 s32, s32, 23\n" \
  "v_mov_b32 v255, s32\n" \
  "v_mul_f32 v10, s60, v255\n" \
  "v_mul_f32 v11, s61, v255\n" \
  "v_mul_f32 v12, s62, v255\n" \
  "v_lshlrev_b32 v255, 5, v191\n" \
  "v_lshlrev_b32 v248, 2, v191\n" \
  "v_and_b32 v255, 1920, v255\n" \
  "v_and_b32 v248, 96, v248\n" \
  "v_add3_u32 v255, v255, v248, s57\n" \
  "v_and_b32 v248, 3, v191\n" \
  "v_lshl_add_u32 v18, v248, 3, v255\n" \
  "v_xor_b32 v19, 32, v18\n" \
  "v_xor_b32 v20, 64, v18\n" \
  "v_xor_b32 v21, 96, v18\n" \
  "s_lshl_b32 s33, s20, 10\n" \
  "s_waitcnt vmcnt(2)\n" \
  "s_barrier\n" \
  "ds_read_b64 v[14:15], v112 offset:0\n" \
  "ds_read_b64 v[26:27], v112 offset:1024\n" \
  "ds_read_b64 v[28:29], v112 offset:2048\n" \
  "ds_read_b64 v[30:31], v112 offset:3072\n" \
  "ds_read_b64 v[32:33], v113 offset:3072\n" \
  "ds_read_b64 v[34:35], v113 offset:2048\n" \
  "ds_read_b64 v[36:37], v113 offset:1024\n" \
  "ds_read_b64 v[38:39], v113 offset:0\n" \
  "s_add_u32 m0, s33, 0\n" \
  "s_add_u32 s32, s22, 0\n" \
  "buffer_load_dwordx4 v225, s[36:39], s32 offen offset:0 sc1 lds\n" \
  "s_add_u32 m0, s33, 4096\n" \
  "s_add_u32 s32, s22, 598016\n" \
  "buffer_load_dwordx4 v225, s[36:39], s32 offen offset:0 sc1 lds\n" \
  "s_add_u32 m0, s33, 8192\n" \
  "s_add_u32 s32, s22, 1196032\n" \
  "buffer_load_dwordx4 v225, s[36:39], s32 offen offset:0 sc1 lds\n" \
  "s_add_u32 m0, s33, 12288\n" \
  "s_add_u32 s32, s22, 1794048\n" \
  "buffer_load_dwordx4 v225, s[36:39], s32 offen offset:0 sc1 lds\n" \
  "s_waitcnt lgkmcnt(7)\n" \
  "v_lshlrev_b32 v40, 16, v14\n" \
  "v_and_b32 v44, 4294901760, v14\n" \
  "s_waitcnt lgkmcnt(3)\n" \
  "v_lshlrev_b32 v66, 16, v33\n" \
  "v_and_b32 v70, 4294901760, v33\n" \
  "v_lshlrev_b32 v41, 16, v26\n" \
  "v_and_b32 v45, 4294901760, v26\n" \
  "s_waitcnt lgkmcnt(2)\n" \
  "v_lshlrev_b32 v67, 16, v35\n" \
  "v_and_b32 v71, 4294901760, v35\n" \
  "v_lshlrev_b32 v42, 16, v28\n" \
  "v_and_b32 v46, 4294901760, v28\n" \
  "s_waitcnt lgkmcnt(1)\n" \
  "v_lshlrev_b32 v68, 16, v37\n" \
  "s_add_u32 m0, s57, 0\n" \
  "s_add_u32 s31, s12, 0\n" \
  "buffer_load_dwordx4 v22, s[4:7], s31 offen offset:0 lds\n" \
  "v_and_b32 v72, 4294901760, v37\n" \
  "v_lshlrev_b32 v43, 16, v30\n" \
  "v_and_b32 v47, 4294901760, v30\n" \
  "s_waitcnt lgkmcnt(0)\n" \
  "v_lshlrev_b32 v69, 16, v39\n" \
  "v_and_b32 v73, 4294901760, v39\n" \
  "v_lshlrev_b32 v82, 16, v15\n" \
  "v_and_b32 v86, 4294901760, v15\n" \
  "v_lshlrev_b32 v90, 16, v32\n" \
  "v_and_b32 v94, 4294901760, v32\n" \
  "buffer_load_dwordx4 v23, s[4:7], s31 offen offset:1024 lds\n" \
  "v_lshlrev_b32 v83, 16, v27\n" \
  "v_and_b32 v87, 4294901760, v27\n" \
  "v_lshlrev_b32 v91, 16, v34\n" \
  "v_and_b32 v95, 4294901760, v34\n" \
  "v_lshlrev_b32 v84, 16, v29\n" \
  "v_and_b32 v88, 4294901760, v29\n" \
  "v_lshlrev_b32 v92, 16, v36\n" \
  "v_and_b32 v96, 4294901760, v36\n" \
  "v_lshlrev_b32 v85, 16, v31\n" \
  "buffer_load_dwordx4 v24, s[4:7], s31 offen offset:2048 lds\n" \
  "v_and_b32 v89, 4294901760, v31\n" \
  "v_lshlrev_b32 v93, 16, v38\n" \
  "v_and_b32 v97, 4294901760, v38\n" \
  "s_nop 1\n" \
  "s_bitcmp1_b32 s27, 1\n" \
  "s_cbranch_scc1 .Lxs_%=\n" \
  ".Lxd_%=:\n" \
  "v_pk_mul_f32 v[14:15], v[40:41], v[10:11] op_sel:[0,0] op_sel_hi:[1,0]\n" \
  "v_pk_mul_f32 v[26:27], v[44:45], v[10:11] op_sel:[0,0] op_sel_hi:[1,0]\n" \
  "v_pk_mul_f32 v[28:29], v[40:41], v[10:11] op_sel:[0,1] op_sel_hi:[1,1]\n" \
  "v_pk_mul_f32 v[32:33], v[44:45], v[10:11] op_sel:[0,1] op_sel_hi:[1,1]\n" \
  "v_pk_add_f32 v[36:37], v[44:45], v[70:71]\n" \
  "buffer_load_dwordx4 v25, s[4:7], s31 offen offset:3072 lds\n" \
  "v_pk_add_f32 v[38:39], v[40:41], v[66:67] neg_lo:[0,1] neg_hi:[0,1]\n" \
  "v_pk_fma_f32 v[40:41], v[66:67], v[10:11], v[14:15] op_sel:[0,1,0] op_sel_hi:[1,1,1]\n" \
  "v_pk_fma_f32 v[44:45], v[70:71], v[10:11], v[26:27] op_sel:[0,1,0] op_sel_hi:[1,1,1] neg_lo:[1,0,0] neg_hi:[1,0,0]\n" \
  "v_pk_fma_f32 v[28:29], v[66:67], v[10:11], v[28:29] op_sel:[0,0,0] op_sel_hi:[1,0,1]\n" \
  "v_pk_fma_f32 v[32:33], v[70:71], v[10:11], v[32:33] op_sel:[0,0,0] op_sel_hi:[1,0,1] neg_lo:[1,0,0] neg_hi:[1,0,0]\n" \
  "v_pk_mul_f32 v[66:67], v[36:37], v[12:13] op_sel:[0,0] op_sel_hi:[1,0] neg_lo:[1,0] neg_hi:[1,0]\n" \
  "v_pk_mul_f32 v[70:71], v[38:39], v[12:13] op_sel:[0,0] op_sel_hi:[1,0]\n" \
  "v_pk_mul_f32 v[14:15], v[42:43], v[10:11] op_sel:[0,0] op_sel_hi:[1,0]\n" \
  "v_pk_mul_f32 v[26:27], v[46:47], v[10:11] op_sel:[0,0] op_sel_hi:[1,0]\n" \
  "s_add_u32 m0, s57, 4096\n" \
  "s_add_u32 s31, s12, 9437184\n" \
  "buffer_load_dwordx4 v22, s[4:7], s31 offen offset:0 lds\n" \
  "v_pk_mul_f32 v[30:31], v[42:43], v[10:11] op_sel:[0,1] op_sel_hi:[1,1]\n" \
  "v_pk_mul_f32 v[34:35], v[46:47], v[10:11] op_sel:[0,1] op_sel_hi:[1,1]\n" \
  "v_pk_add_f32 v[36:37], v[46:47], v[72:73]\n" \
  "v_pk_add_f32 v[38:39], v[42:43], v[68:69] neg_lo:[0,1] neg_hi:[0,1]\n" \
  "v_pk_fma_f32 v[42:43], v[68:69], v[10:11], v[14:15] op_sel:[0,1,0] op_sel_hi:[1,1,1]\n" \
  "v_pk_fma_f32 v[46:47], v[72:73], v[10:11], v[26:27] op_sel:[0,1,0] op_sel_hi:[1,1,1] neg_lo:[1,0,0] neg_hi:[1,0,0]\n" \
  "v_pk_fma_f32 v[30:31], v[68:69], v[10:11], v[30:31] op_sel:[0,0,0] op_sel_hi:[1,0,1]\n" \
  "v_pk_fma_f32 v[34:35], v[72:73], v[10:11], v[34:35] op_sel:[0,0,0] op_sel_hi:[1,0,1] neg_lo:[1,0,0] neg_hi:[1,0,0]\n" \
  "v_pk_mul_f32 v[68:69], v[36:37], v[12:13] op_sel:[0,0] op_sel_hi:[1,0] neg_lo:[1,0] neg_hi:[1,0]\n" \
  "buffer_load_dwordx4 v23, s[4:7], s31 offen offset:1024 lds\n" \
  "v_pk_mul_f32 v[72:73], v[38:39], v[12:13] op_sel:[0,0] op_sel_hi:[1,0]\n" \
  "v_pk_mul_f32 v[14:15], v[82:83], v[10:11] op_sel:[0,0] op_sel_hi:[1,0]\n" \
  "v_pk_mul_f32 v[26:27], v[86:87], v[10:11] op_sel:[0,0] op_sel_hi:[1,0]\n" \
  "v_pk_mul_f32 v[36:37], v[82:83], v[10:11] op_sel:[0,1] op_sel_hi:[1,1]\n" \
  "v_pk_mul_f32 v[98:99], v[86:87], v[10:11] op_sel:[0,1] op_sel_hi:[1,1]\n" \
  "v_pk_add_f32 v[48:49], v[86:87], v[94:95]\n" \
  "v_pk_add_f32 v[114:115], v[82:83], v[90:91] neg_lo:[0,1] neg_hi:[0,1]\n" \
  "v_pk_fma_f32 v[82:83], v[90:91], v[10:11], v[14:15] op_sel:[0,1,0] op_sel_hi:[1,1,1]\n" \
  "v_pk_fma_f32 v[86:87], v[94:95], v[10:11], v[26:27] op_sel:[0,1,0] op_sel_hi:[1,1,1] neg_lo:[1,0,0] neg_hi:[1,0,0]\n" \
  "buffer_load_dwordx4 v24, s[4:7], s31 offen offset:2048 lds\n" \
  "v_pk_fma_f32 v[36:37], v[90:91], v[10:11], v[36:37] op_sel:[0,0,0] op_sel_hi:[1,0,1]\n" \
  "v_pk_fma_f32 v[98:99], v[94:95], v[10:11], v[98:99] op_sel:[0,0,0] op_sel_hi:[1,0,1] neg_lo:[1,0,0] neg_hi:[1,0,0]\n" \
  "v_pk_mul_f32 v[90:91], v[48:49], v[12:13] op_sel:[0,0] op_sel_hi:[1,0] neg_lo:[1,0] neg_hi:[1,0]\n" \
  "v_pk_mul_f32 v[94:95], v[114:115], v[12:13] op_sel:[0,0] op_sel_hi:[1,0]\n" \
  "v_pk_mul_f32 v[14:15], v[84:85], v[10:11] op_sel:[0,0] op_sel_hi:[1,0]\n" \
  "v_pk_mul_f32 v[26:27], v[88:89], v[10:11] op_sel:[0,0] op_sel_hi:[1,0]\n" \
  "v_pk_mul_f32 v[38:39], v[84:85], v[10:11] op_sel:[0,1] op_sel_hi:[1,1]\n" \
  "v_pk_mul_f32 v[100:101], v[88:89], v[10:11] op_sel:[0,1] op_sel_hi:[1,1]\n" \
  "v_pk_add_f32 v[48:49], v[88:89], v[96:97]\n" \
  "buffer_load_dwordx4 v25, s[4:7], s31 offen offset:3072 lds\n" \
  "v_pk_add_f32 v[114:115], v[84:85], v[92:93] neg_lo:[0,1] neg_hi:[0,1]\n" \
  "v_pk_fma_f32 v[84:85], v[92:93], v[10:11], v[14:15] op_sel:[0,1,0] op_sel_hi:[1,1,1]\n" \
  "v_pk_fma_f32 v[88:89], v[96:97], v[10:11], v[26:27] op_sel:[0,1,0] op_sel_hi:[1,1,1] neg_lo:[1,0,0] neg_hi:[1,0,0]\n" \
  "v_pk_fma_f32 v[38:39], v[92:93], v[10:11], v[38:39] op_sel:[0,0,0] op_sel_hi:[1,0,1]\n" \
  "v_pk_fma_f32 v[100:101], v[96:97], v[10:11], v[100:101] op_sel:[0,0,0] op_sel_hi:[1,0,1] neg_lo:[1,0,0] neg_hi:[1,0,0]\n" \
  "v_pk_mul_f32 v[92:93], v[48:49], v[12:13] op_sel:[0,0] op_sel_hi:[1,0] neg_lo:[1,0] neg_hi:[1,0]\n" \
  "v_pk_mul_f32 v[96:97], v[114:115], v[12:13] op_sel:[0,0] op_sel_hi:[1,0]\n" \
  "v_lshlrev_b32 v10, 16, v2\n" \
  "v_and_b32 v11, 4294901760, v2\n" \
  "v_lshlrev_b32 v12, 16, v3\n" \
  "v_and_b32 v13, 4294901760, v3\n" \
  "v_lshlrev_b32 v14, 16, v4\n" \
  "v_and_b32 v15, 4294901760, v4\n" \
  "v_lshlrev_b32 v26, 16, v5\n" \
  "v_and_b32 v27, 4294901760, v5\n" \
  "v_lshlrev_b32 v2, 16, v6\n" \
  "v_and_b32 v3, 4294901760, v6\n" \
  "v_lshlrev_b32 v4, 16, v7\n" \
  "v_and_b32 v5, 4294901760, v7\n" \
  "v_lshlrev_b32 v48, 16, v8\n" \
  "v_and_b32 v49, 4294901760, v8\n" \
  "v_lshlrev_b32 v114, 16, v9\n" \
  "v_and_b32 v115, 4294901760, v9\n" \
  "v_lshlrev_b32 v6, 16, v249\n" \
  "v_and_b32 v7, 4294901760, v249\n" \
  "v_and_b32 v116, 65535, v106\n" \
  "v_add_u32 v116, s21, v116\n" \
  "v_and_b32 v117, 65535, v107\n" \
  "v_add_u32 v117, s21, v117\n" \
  "v_and_b32 v118, 65535, v108\n" \
  "v_add_u32 v118, s21, v118\n" \
  "v_and_b32 v119, 65535, v109\n" \
  "v_add_u32 v119, s21, v119\n" \
  "v_lshrrev_b32 v120, 16, v106\n" \
  "v_add_u32 v120, s21, v120\n" \
  "v_lshrrev_b32 v121, 16, v107\n" \
  "v_add_u32 v121, s21, v121\n" \
  "v_lshrrev_b32 v122, 16, v108\n" \
  "v_add_u32 v122, s21, v122\n" \
  "v_lshrrev_b32 v123, 16, v109\n" \
  "v_add_u32 v123, s21, v123\n" \
  "v_mul_u32_u24 v255, 2448, v253\n" \
  "v_add_u32 v253, s21, v255\n" \
  "v_lshl_add_u32 v255, v254, 4, v253\n" \
  "v_lshl_add_u32 v249, v254, 2, v253\n" \
  "v_lshrrev_b32 v253, 3, v254\n" \
  "v_lshl_add_u32 v248, v253, 4, v255\n" \
  "v_lshrrev_b32 v253, 4, v191\n" \
  "v_lshl_add_u32 v245, v253, 2, s20\n" \
  "v_and_b32 v253, 3, v191\n" \
  "v_bfe_u32 v241, v191, 2, 2\n" \
  "v_mul_u32_u24 v239, 4896, v241\n" \
  "v_lshl_add_u32 v241, v253, 3, v239\n" \
  "v_lshl_add_u32 v253, v245, 5, v241\n" \
  "v_and_b32 v241, 15, v191\n" \
  "v_lshlrev_b32 v239, 4, v241\n" \
  "v_lshl_add_u32 v241, v245, 16, v239\n" \
  "v_lshlrev_b32 v245, 3, v254\n" \
  "s_lshr_b32 s98, s27, 8\n" \
  "s_and_b32 s98, s98, 255\n" \
  "v_subrev_u32 v239, s98, v0\n" \
  "v_bfrev_b32 v238, 1\n" \
  "v_cmp_gt_u32 s[96:97], 36, v239\n" \
  "v_cndmask_b32 v238, v238, v246, s[94:95]\n" \
  "s_cmp_eq_u32 s20, 1\n" \
  "s_cselect_b32 s92, 65537, s58\n" \
  "s_and_b32 s93, s59, 4294901758\n" \
  "v_mov_b32 v8, s18\n" \
  "v_mov_b32 v9, s19\n" \
  "s_waitcnt vmcnt(8)\n" \
  "s_barrier\n" \
  "ds_read_b128 v[106:109], v111 offset:0\n" \
  "ds_read_b128 v[124:127], v111 offset:1024\n" \
  "ds_read_b128 v[128:131], v111 offset:2048\n" \
  "ds_read_b128 v[132:135], v111 offset:3072\n" \
  "s_cmp_eq_u32 s20, 3\n" \
  "s_cselect_b64 exec, 1, 0\n" \
  "buffer_store_dwordx2 v[8:9], off, s[36:39], s30 sc1\n" \
  "s_mov_b64 exec, -1\n" \
  "s_waitcnt lgkmcnt(0)\n" \
  ".Ltc_%=:\n" \
  "v_or3_b32 v239, v106, v124, v128\n" \
  "v_bitop3_b32 v239, v239, 1, v132 bitop3:0xc8\n" \
  "v_cmp_ne_u32 vcc, 0, v239\n" \
  "s_cbranch_vccnz .Lts_%=\n" \
  ".Ltd_%=:\n" \
  "v_mov_b32 v136, v106\n" \
  "v_mov_b32 v137, v124\n" \
  "v_mov_b32 v138, v128\n" \
  "v_mov_b32 v139, v132\n" \
  "v_mov_b32 v140, v107\n" \
  "v_mov_b32 v141, v125\n" \
  "v_mfma_f32_16x16x32_f16 v[144:147], v[136:139], a[200:203], 0\n" \
  "v_mov_b32 v142, v129\n" \
  "v_mov_b32 v143, v133\n" \
  "v_mfma_f32_16x16x32_f16 v[148:151], v[136:139], a[204:207], 0\n" \
  "v_mov_b32 v136, v108\n" \
  "v_mov_b32 v137, v126\n" \
  "v_mfma_f32_16x16x32_f16 v[152:155], v[140:143], a[200:203], 0\n" \
  "v_mov_b32 v138, v130\n" \
  "v_mov_b32 v139, v134\n" \
  "v_mfma_f32_16x16x32_f16 v[156:159], v[140:143], a[204:207], 0\n" \
  "v_mov_b32 v140, v109\n" \
  "v_mov_b32 v141, v127\n" \
  "v_mfma_f32_16x16x32_f16 v[106:109], v[136:139], a[200:203], 0\n" \
  "v_mov_b32 v142, v131\n" \
  "v_mov_b32 v143, v135\n" \
  "v_mfma_f32_16x16x32_f16 v[124:127], v[136:139], a[204:207], 0\n" \
  "s_add_u32 m0, s57, 8192\n" \
  "s_add_u32 s31, s12, 294912\n" \
  "buffer_load_dwordx4 v22, s[4:7], s31 offen offset:0 lds\n" \
  "v_mfma_f32_16x16x32_f16 v[128:131], v[140:143], a[200:203], 0\n" \
  "buffer_load_dwordx4 v23, s[4:7], s31 offen offset:1024 lds\n" \
  "v_mfma_f32_16x16x32_f16 v[132:135], v[140:143], a[204:207], 0\n" \
  "buffer_load_dwordx4 v24, s[4:7], s31 offen offset:2048 lds\n" \
  "buffer_load_dwordx4 v25, s[4:7], s31 offen offset:3072 lds\n" \
  "s_add_u32 m0, s57, 12288\n" \
  "s_add_u32 s31, s12, 9732096\n" \
  "buffer_load_dwordx4 v22, s[4:7], s31 offen offset:0 lds\n" \
  "v_pk_mul_f32 v[136:137], v[144:145], v[74:75]\n" \
  "v_pk_mul_f32 v[140:141], v[144:145], v[78:79]\n" \
  "v_pk_mul_f32 v[138:139], v[146:147], v[76:77]\n" \
  "v_pk_mul_f32 v[142:143], v[146:147], v[80:81]\n" \
  "v_pk_fma_f32 v[136:137], v[148:149], v[78:79], v[136:137] neg_lo:[1,0,0] neg_hi:[1,0,0]\n" \
  "v_pk_fma_f32 v[140:141], v[148:149], v[74:75], v[140:141]\n" \
  "v_pk_fma_f32 v[138:139], v[150:151], v[80:81], v[138:139] neg_lo:[1,0,0] neg_hi:[1,0,0]\n" \
  "v_pk_fma_f32 v[142:143], v[150:151], v[76:77], v[142:143]\n" \
  "v_pk_mul_f32 v[144:145], v[152:153], v[74:75]\n" \
  "v_pk_mul_f32 v[148:149], v[152:153], v[78:79]\n" \
  "v_pk_mul_f32 v[146:147], v[154:155], v[76:77]\n" \
  "v_pk_mul_f32 v[150:151], v[154:155], v[80:81]\n" \
  "v_pk_fma_f32 v[144:145], v[156:157], v[78:79], v[144:145] neg_lo:[1,0,0] neg_hi:[1,0,0]\n" \
  "v_pk_fma_f32 v[148:149], v[156:157], v[74:75], v[148:149]\n" \
  "v_pk_fma_f32 v[146:147], v[158:159], v[80:81], v[146:147] neg_lo:[1,0,0] neg_hi:[1,0,0]\n" \
  "v_pk_fma_f32 v[150:151], v[158:159], v[76:77], v[150:151]\n" \
  "v_pk_mul_f32 v[152:153], v[106:107], v[74:75]\n" \
  "v_pk_mul_f32 v[156:157], v[106:107], v[78:79]\n" \
  "v_pk_mul_f32 v[154:155], v[108:109], v[76:77]\n" \
  "v_pk_mul_f32 v[158:159], v[108:109], v[80:81]\n" \
  "v_pk_fma_f32 v[152:153], v[124:125], v[78:79], v[152:153] neg_lo:[1,0,0] neg_hi:[1,0,0]\n" \
  "v_pk_fma_f32 v[156:157], v[124:125], v[74:75], v[156:157]\n" \
  "v_pk_fma_f32 v[154:155], v[126:127], v[80:81], v[154:155] neg_lo:[1,0,0] neg_hi:[1,0,0]\n" \
  "v_pk_fma_f32 v[158:159], v[126:127], v[76:77], v[158:159]\n" \
  "v_pk_mul_f32 v[106:107], v[128:129], v[74:75]\n" \
  "v_pk_mul_f32 v[124:125], v[128:129], v[78:79]\n" \
  "v_pk_mul_f32 v[108:109], v[130:131], v[76:77]\n" \
  "v_pk_mul_f32 v[126:127], v[130:131], v[80:81]\n" \
  "v_pk_fma_f32 v[106:107], v[132:133], v[78:79], v[106:107] neg_lo:[1,0,0] neg_hi:[1,0,0]\n" \
  "v_pk_fma_f32 v[124:125], v[132:133], v[74:75], v[124:125]\n" \
  "v_pk_fma_f32 v[108:109], v[134:135], v[80:81], v[108:109] neg_lo:[1,0,0] neg_hi:[1,0,0]\n" \
  "v_pk_fma_f32 v[126:127], v[134:135], v[76:77], v[126:127]\n" \
  "v_cvt_pk_f16_f32 v128, v136, v140\n" \
  "v_cvt_pk_f16_f32 v129, v137, v141\n" \
  "v_cvt_pk_f16_f32 v130, v138, v142\n" \
  "v_cvt_pk_f16_f32 v131, v139, v143\n" \
  "v_cvt_pk_f16_f32 v132, v144, v148\n" \
  "v_cvt_pk_f16_f32 v133, v145, v149\n" \
  "v_mfma_f32_16x16x32_f16 v[136:139], a[200:203], v[128:131], 0\n" \
  "v_cvt_pk_f16_f32 v134, v146, v150\n" \
  "v_cvt_pk_f16_f32 v135, v147, v151\n" \
  "v_mfma_f32_16x16x32_f16 v[140:143], a[204:207], v[128:131], 0\n" \
  "v_cvt_pk_f16_f32 v128, v152, v156\n" \
  "v_cvt_pk_f16_f32 v129, v153, v157\n" \
  "v_mfma_f32_16x16x32_f16 v[144:147], a[200:203], v[132:135], 0\n" \
  "v_cvt_pk_f16_f32 v130, v154, v158\n" \
  "v_cvt_pk_f16_f32 v131, v155, v159\n" \
  "v_mfma_f32_16x16x32_f16 v[148:151], a[204:207], v[132:135], 0\n" \
  "v_cvt_pk_f16_f32 v132, v106, v124\n" \
  "v_cvt_pk_f16_f32 v133, v107, v125\n" \
  "v_mfma_f32_16x16x32_f16 v[152:155], a[200:203], v[128:131], 0\n" \
  "v_cvt_pk_f16_f32 v134, v108, v126\n" \
  "v_cvt_pk_f16_f32 v135, v109, v127\n" \
  "v_mfma_f32_16x16x32_f16 v[106:109], a[204:207], v[128:131], 0\n" \
  "buffer_load_dwordx4 v23, s[4:7], s31 offen offset:1024 lds\n" \
  "v_mfma_f32_16x16x32_f16 v[124:127], a[200:203], v[132:135], 0\n" \
  "buffer_load_dwordx4 v24, s[4:7], s31 offen offset:2048 lds\n" \
  "v_mfma_f32_16x16x32_f16 v[128:131], a[204:207], v[132:135], 0\n" \
  "buffer_load_dwordx4 v25, s[4:7], s31 offen offset:3072 lds\n" \
  "s_add_u32 m0, s57, 16384\n" \
  "s_add_u32 s31, s12, 73728\n" \
  "buffer_load_dwordx4 v22, s[4:7], s31 offen offset:0 lds\n" \
  "buffer_load_dwordx4 v23, s[4:7], s31 offen offset:1024 lds\n" \
  "v_pk_mul_f32 v[132:133], v[40:41], v[140:141]\n" \
  "v_pk_mul_f32 v[156:157], v[40:41], v[136:137]\n" \
  "v_pk_mul_f32 v[160:161], v[28:29], v[148:149]\n" \
  "v_pk_mul_f32 v[164:165], v[28:29], v[144:145]\n" \
  "v_pk_fma_f32 v[132:133], v[44:45], v[136:137], v[132:133]\n" \
  "v_pk_fma_f32 v[156:157], v[44:45], v[140:141], v[156:157] neg_lo:[1,0,0] neg_hi:[1,0,0]\n" \
  "v_pk_fma_f32 v[160:161], v[32:33], v[144:145], v[160:161]\n" \
  "v_pk_fma_f32 v[164:165], v[32:33], v[148:149], v[164:165] neg_lo:[1,0,0] neg_hi:[1,0,0]\n" \
  "v_pk_fma_f32 v[132:133], v[66:67], v[148:149], v[132:133]\n" \
  "v_pk_fma_f32 v[156:157], v[66:67], v[144:145], v[156:157]\n" \
  "v_pk_fma_f32 v[160:161], v[66:67], v[140:141], v[160:161] neg_lo:[1,0,0] neg_hi:[1,0,0]\n" \
  "v_pk_fma_f32 v[164:165], v[66:67], v[136:137], v[164:165] neg_lo:[1,0,0] neg_hi:[1,0,0]\n" \
  "v_pk_fma_f32 v[132:133], v[70:71], v[144:145], v[132:133]\n" \
  "v_pk_fma_f32 v[156:157], v[70:71], v[148:149], v[156:157] neg_lo:[1,0,0] neg_hi:[1,0,0]\n" \
  "v_pk_fma_f32 v[160:161], v[70:71], v[136:137], v[160:161] neg_lo:[1,0,0] neg_hi:[1,0,0]\n" \
  "v_pk_fma_f32 v[164:165], v[70:71], v[140:141], v[164:165]\n" \
  "v_pk_mul_f32 v[134:135], v[42:43], v[142:143]\n" \
  "v_pk_mul_f32 v[158:159], v[42:43], v[138:139]\n" \
  "v_pk_mul_f32 v[162:163], v[30:31], v[150:151]\n" \
  "v_pk_mul_f32 v[166:167], v[30:31], v[146:147]\n" \
  "v_pk_fma_f32 v[134:135], v[46:47], v[138:139], v[134:135]\n" \
  "v_pk_fma_f32 v[158:159], v[46:47], v[142:143], v[158:159] neg_lo:[1,0,0] neg_hi:[1,0,0]\n" \
  "v_pk_fma_f32 v[162:163], v[34:35], v[146:147], v[162:163]\n" \
  "v_pk_fma_f32 v[166:167], v[34:35], v[150:151], v[166:167] neg_lo:[1,0,0] neg_hi:[1,0,0]\n" \
  "v_pk_fma_f32 v[134:135], v[68:69], v[150:151], v[134:135]\n" \
  "v_pk_fma_f32 v[158:159], v[68:69], v[146:147], v[158:159]\n" \
  "v_pk_fma_f32 v[162:163], v[68:69], v[142:143], v[162:163] neg_lo:[1,0,0] neg_hi:[1,0,0]\n" \
  "v_pk_fma_f32 v[166:167], v[68:69], v[138:139], v[166:167] neg_lo:[1,0,0] neg_hi:[1,0,0]\n" \
  "v_pk_fma_f32 v[134:135], v[72:73], v[146:147], v[134:135]\n" \
  "v_pk_fma_f32 v[158:159], v[72:73], v[150:151], v[158:159] neg_lo:[1,0,0] neg_hi:[1,0,0]\n" \
  "v_pk_fma_f32 v[162:163], v[72:73], v[138:139], v[162:163] neg_lo:[1,0,0] neg_hi:[1,0,0]\n" \
  "v_pk_fma_f32 v[166:167], v[72:73], v[142:143], v[166:167]\n" \
  "v_pk_mul_f32 v[28:29], v[82:83], v[106:107]\n" \
  "v_pk_mul_f32 v[32:33], v[82:83], v[152:153]\n" \
  "v_pk_mul_f32 v[40:41], v[36:37], v[128:129]\n" \
  "v_pk_mul_f32 v[44:45], v[36:37], v[124:125]\n" \
  "v_pk_fma_f32 v[28:29], v[86:87], v[152:153], v[28:29]\n" \
  "v_pk_fma_f32 v[32:33], v[86:87], v[106:107], v[32:33] neg_lo:[1,0,0] neg_hi:[1,0,0]\n" \
  "v_pk_fma_f32 v[40:41], v[98:99], v[124:125], v[40:41]\n" \
  "v_pk_fma_f32 v[44:45], v[98:99], v[128:129], v[44:45] neg_lo:[1,0,0] neg_hi:[1,0,0]\n" \
  "v_pk_fma_f32 v[28:29], v[90:91], v[128:129], v[28:29]\n" \
  "v_pk_fma_f32 v[32:33], v[90:91], v[124:125], v[32:33]\n" \
  "v_pk_fma_f32 v[40:41], v[90:91], v[106:107], v[40:41] neg_lo:[1,0,0] neg_hi:[1,0,0]\n" \
  "v_pk_fma_f32 v[44:45], v[90:91], v[152:153], v[44:45] neg_lo:[1,0,0] neg_hi:[1,0,0]\n" \
  "v_pk_fma_f32 v[28:29], v[94:95], v[124:125], v[28:29]\n" \
  "v_pk_fma_f32 v[32:33], v[94:95], v[128:129], v[32:33] neg_lo:[1,0,0] neg_hi:[1,0,0]\n" \
  "v_pk_fma_f32 v[40:41], v[94:95], v[152:153], v[40:41] neg_lo:[1,0,0] neg_hi:[1,0,0]\n" \
  "v_pk_fma_f32 v[44:45], v[94:95], v[106:107], v[44:45]\n" \
  "v_pk_mul_f32 v[30:31], v[84:85], v[108:109]\n" \
  "v_pk_mul_f32 v[34:35], v[84:85], v[154:155]\n" \
  "v_pk_mul_f32 v[42:43], v[38:39], v[130:131]\n" \
  "v_pk_mul_f32 v[46:47], v[38:39], v[126:127]\n" \
  "v_pk_fma_f32 v[30:31], v[88:89], v[154:155], v[30:31]\n" \
  "v_pk_fma_f32 v[34:35], v[88:89], v[108:109], v[34:35] neg_lo:[1,0,0] neg_hi:[1,0,0]\n" \
  "v_pk_fma_f32 v[42:43], v[100:101], v[126:127], v[42:43]\n" \
  "v_pk_fma_f32 v[46:47], v[100:101], v[130:131], v[46:47] neg_lo:[1,0,0] neg_hi:[1,0,0]\n" \
  "v_pk_fma_f32 v[30:31], v[92:93], v[130:131], v[30:31]\n" \
  "v_pk_fma_f32 v[34:35], v[92:93], v[126:127], v[34:35]\n" \
  "v_pk_fma_f32 v[42:43], v[92:93], v[108:109], v[42:43] neg_lo:[1,0,0] neg_hi:[1,0,0]\n" \
  "v_pk_fma_f32 v[46:47], v[92:93], v[154:155], v[46:47] neg_lo:[1,0,0] neg_hi:[1,0,0]\n" \
  "v_pk_fma_f32 v[30:31], v[96:97], v[126:127], v[30:31]\n" \
  "v_pk_fma_f32 v[34:35], v[96:97], v[130:131], v[34:35] neg_lo:[1,0,0] neg_hi:[1,0,0]\n" \
  "v_pk_fma_f32 v[42:43], v[96:97], v[154:155], v[42:43] neg_lo:[1,0,0] neg_hi:[1,0,0]\n" \
  "v_pk_fma_f32 v[46:47], v[96:97], v[108:109], v[46:47]\n" \
  "v_cvt_pk_f16_f32 v36, v132, v156\n" \
  "v_cvt_pk_f16_f32 v37, v133, v157\n" \
  "v_cvt_pk_f16_f32 v38, v134, v158\n" \
  "v_cvt_pk_f16_f32 v39, v135, v159\n" \
  "v_cvt_pk_f16_f32 v66, v160, v164\n" \
  "v_cvt_pk_f16_f32 v67, v161, v165\n" \
  "v_mfma_f32_16x16x32_f16 v[70:73], v[36:39], a[200:203], 0\n" \
  "v_cvt_pk_f16_f32 v68, v162, v166\n" \
  "v_cvt_pk_f16_f32 v69, v163, v167\n" \
  "v_mfma_f32_16x16x32_f16 v[82:85], v[36:39], a[204:207], 0\n" \
  "v_cvt_pk_f16_f32 v36, v28, v32\n" \
  "v_cvt_pk_f16_f32 v37, v29, v33\n" \
  "v_mfma_f32_16x16x32_f16 v[86:89], v[66:69], a[200:203], 0\n" \
  "v_cvt_pk_f16_f32 v38, v30, v34\n" \
  "v_cvt_pk_f16_f32 v39, v31, v35\n" \
  "v_mfma_f32_16x16x32_f16 v[28:31], v[66:69], a[204:207], 0\n" \
  "v_cvt_pk_f16_f32 v32, v40, v44\n" \
  "v_cvt_pk_f16_f32 v33, v41, v45\n" \
  "v_mfma_f32_16x16x32_f16 v[66:69], v[36:39], a[200:203], 0\n" \
  "v_cvt_pk_f16_f32 v34, v42, v46\n" \
  "v_cvt_pk_f16_f32 v35, v43, v47\n" \
  "v_mfma_f32_16x16x32_f16 v[40:43], v[36:39], a[204:207], 0\n" \
  "buffer_load_dwordx4 v24, s[4:7], s31 offen offset:2048 lds\n" \
  "v_mfma_f32_16x16x32_f16 v[36:39], v[32:35], a[200:203], 0\n" \
  "buffer_load_dwordx4 v25, s[4:7], s31 offen offset:3072 lds\n" \
  "v_mfma_f32_16x16x32_f16 v[44:47], v[32:35], a[204:207], 0\n" \
  "s_add_u32 m0, s57, 20480\n" \
  "s_add_u32 s31, s12, 9510912\n" \
  "buffer_load_dwordx4 v22, s[4:7], s31 offen offset:0 lds\n" \
  "buffer_load_dwordx4 v23, s[4:7], s31 offen offset:1024 lds\n" \
  "buffer_load_dwordx4 v24, s[4:7], s31 offen offset:2048 lds\n" \
  "v_pk_mul_f32 v[32:33], v[70:71], v[74:75]\n" \
  "v_pk_mul_f32 v[90:91], v[70:71], v[78:79]\n" \
  "v_pk_mul_f32 v[34:35], v[72:73], v[76:77]\n" \
  "v_pk_mul_f32 v[92:93], v[72:73], v[80:81]\n" \
  "v_pk_fma_f32 v[32:33], v[82:83], v[78:79], v[32:33] neg_lo:[1,0,0] neg_hi:[1,0,0]\n" \
  "v_pk_fma_f32 v[90:91], v[82:83], v[74:75], v[90:91]\n" \
  "v_pk_fma_f32 v[34:35], v[84:85], v[80:81], v[34:35] neg_lo:[1,0,0] neg_hi:[1,0,0]\n" \
  "v_pk_fma_f32 v[92:93], v[84:85], v[76:77], v[92:93]\n" \
  "v_pk_mul_f32 v[70:71], v[86:87], v[74:75]\n" \
  "v_pk_mul_f32 v[82:83], v[86:87], v[78:79]\n" \
  "v_pk_mul_f32 v[72:73], v[88:89], v[76:77]\n" \
  "v_pk_mul_f32 v[84:85], v[88:89], v[80:81]\n" \
  "v_pk_fma_f32 v[70:71], v[28:29], v[78:79], v[70:71] neg_lo:[1,0,0] neg_hi:[1,0,0]\n" \
  "v_pk_fma_f32 v[82:83], v[28:29], v[74:75], v[82:83]\n" \
  "v_pk_fma_f32 v[72:73], v[30:31], v[80:81], v[72:73] neg_lo:[1,0,0] neg_hi:[1,0,0]\n" \
  "v_pk_fma_f32 v[84:85], v[30:31], v[76:77], v[84:85]\n" \
  "v_pk_mul_f32 v[28:29], v[66:67], v[74:75]\n" \
  "v_pk_mul_f32 v[86:87], v[66:67], v[78:79]\n" \
  "v_pk_mul_f32 v[30:31], v[68:69], v[76:77]\n" \
  "v_pk_mul_f32 v[88:89], v[68:69], v[80:81]\n" \
  "v_pk_fma_f32 v[28:29], v[40:41], v[78:79], v[28:29] neg_lo:[1,0,0] neg_hi:[1,0,0]\n" \
  "v_pk_fma_f32 v[86:87], v[40:41], v[74:75], v[86:87]\n" \
  "v_pk_fma_f32 v[30:31], v[42:43], v[80:81], v[30:31] neg_lo:[1,0,0] neg_hi:[1,0,0]\n" \
  "v_pk_fma_f32 v[88:89], v[42:43], v[76:77], v[88:89]\n" \
  "v_pk_mul_f32 v[40:41], v[36:37], v[74:75]\n" \
  "v_pk_mul_f32 v[66:67], v[36:37], v[78:79]\n" \
  "v_pk_mul_f32 v[42:43], v[38:39], v[76:77]\n" \
  "v_pk_mul_f32 v[68:69], v[38:39], v[80:81]\n" \
  "v_pk_fma_f32 v[40:41], v[44:45], v[78:79], v[40:41] neg_lo:[1,0,0] neg_hi:[1,0,0]\n" \
  "v_pk_fma_f32 v[66:67], v[44:45], v[74:75], v[66:67]\n" \
  "v_pk_fma_f32 v[42:43], v[46:47], v[80:81], v[42:43] neg_lo:[1,0,0] neg_hi:[1,0,0]\n" \
  "v_pk_fma_f32 v[68:69], v[46:47], v[76:77], v[68:69]\n" \
  "v_cvt_pk_f16_f32 v36, v32, v90\n" \
  "v_cvt_pk_f16_f32 v37, v33, v91\n" \
  "v_cvt_pk_f16_f32 v38, v34, v92\n" \
  "v_cvt_pk_f16_f32 v39, v35, v93\n" \
  "v_cvt_pk_f16_f32 v32, v70, v82\n" \
  "v_cvt_pk_f16_f32 v33, v71, v83\n" \
  "v_mfma_f32_16x16x32_f16 v[44:47], a[200:203], v[36:39], 0\n" \
  "v_cvt_pk_f16_f32 v34, v72, v84\n" \
  "v_cvt_pk_f16_f32 v35, v73, v85\n" \
  "v_mfma_f32_16x16x32_f16 v[70:73], a[204:207], v[36:39], 0\n" \
  "v_cvt_pk_f16_f32 v36, v28, v86\n" \
  "v_cvt_pk_f16_f32 v37, v29, v87\n" \
  "v_mfma_f32_16x16x32_f16 v[74:77], a[200:203], v[32:35], 0\n" \
  "v_cvt_pk_f16_f32 v38, v30, v88\n" \
  "v_cvt_pk_f16_f32 v39, v31, v89\n" \
  "v_mfma_f32_16x16x32_f16 v[28:31], a[204:207], v[32:35], 0\n" \
  "v_cvt_pk_f16_f32 v32, v40, v66\n" \
  "v_cvt_pk_f16_f32 v33, v41, v67\n" \
  "v_mfma_f32_16x16x32_f16 v[78:81], a[200:203], v[36:39], 0\n" \
  "v_cvt_pk_f16_f32 v34, v42, v68\n" \
  "v_cvt_pk_f16_f32 v35, v43, v69\n" \
  "v_mfma_f32_16x16x32_f16 v[40:43], a[204:207], v[36:39], 0\n" \
  "buffer_load_dwordx4 v25, s[4:7], s31 offen offset:3072 lds\n" \
  "v_mfma_f32_16x16x32_f16 v[36:39], a[200:203], v[32:35], 0\n" \
  "v_mfma_f32_16x16x32_f16 v[66:69], a[204:207], v[32:35], 0\n" \
  "v_cvt_pk_f16_f32 v32, v70, v44\n" \
  "v_cvt_pk_f16_f32 v33, v28, v74\n" \
  "s_nop 2\n" \
  "v_cvt_pk_f16_f32 v34, v40, v78\n" \
  "s_nop 1\n" \
  "v_cvt_pk_f16_f32 v35, v66, v36\n" \
  "v_or_b32 v32, 1, v32\n" \
  "ds_write_b128 v111, v[32:35] offset:0\n" \
  "v_cvt_pk_f16_f32 v32, v71, v45\n" \
  "v_cvt_pk_f16_f32 v33, v29, v75\n" \
  "v_cvt_pk_f16_f32 v34, v41, v79\n" \
  "v_cvt_pk_f16_f32 v35, v67, v37\n" \
  "v_or_b32 v32, 1, v32\n" \
  "ds_write_b128 v111, v[32:35] offset:1024\n" \
  "v_cvt_pk_f16_f32 v32, v72, v46\n" \
  "v_cvt_pk_f16_f32 v33, v30, v76\n" \
  "v_cvt_pk_f16_f32 v34, v42, v80\n" \
  "v_cvt_pk_f16_f32 v35, v68, v38\n" \
  "v_or_b32 v32, 1, v32\n" \
  "ds_write_b128 v111, v[32:35] offset:2048\n" \
  "v_cvt_pk_f16_f32 v32, v73, v47\n" \
  "v_cvt_pk_f16_f32 v33, v31, v77\n" \
  "v_cvt_pk_f16_f32 v34, v43, v81\n" \
  "v_cvt_pk_f16_f32 v35, v69, v39\n" \
  "v_or_b32 v32, 1, v32\n" \
  "ds_write_b128 v111, v[32:35] offset:3072\n" \
  "s_waitcnt lgkmcnt(0)\n" \
  "s_barrier\n" \
  "ds_read_b128 v[28:31], v110 offset:0\n" \
  "ds_read_b128 v[32:35], v110 offset:4096\n" \
  "ds_read_b128 v[36:39], v110 offset:8192\n" \
  "ds_read_b128 v[40:43], v110 offset:12288\n" \
  "s_add_u32 s32, s22, 598016\n" \
  "s_waitcnt lgkmcnt(3)\n" \
  "buffer_store_dwordx4 v[28:31], v226, s[36:39], s22 offen offset:0 sc1\n" \
  "s_add_u32 s33, s22, 1196032\n" \
  "s_waitcnt lgkmcnt(2)\n" \
  "buffer_store_dwordx4 v[32:35], v226, s[36:39], s32 offen offset:0 sc1\n" \
  "s_add_u32 s32, s22, 1794048\n" \
  "s_waitcnt lgkmcnt(1)\n" \
  "buffer_store_dwordx4 v[36:39], v226, s[36:39], s33 offen offset:0 sc1\n" \
  "s_waitcnt lgkmcnt(0)\n" \
  "buffer_store_dwordx4 v[40:43], v226, s[36:39], s32 offen offset:0 sc1\n" \
  "s_mov_b32 s32, 35840\n" \
  "buffer_load_dwordx4 v[28:31], v238, s[36:39], s32 offen offset:0 sc1\n" \
  "buffer_load_dwordx4 v[32:35], v238, s[36:39], s32 offen offset:2304 sc1\n" \
  "s_mov_b32 s32, 40448\n" \
  "buffer_load_dwordx4 v[36:39], v238, s[36:39], s32 offen offset:0 sc1\n" \
  "buffer_load_dwordx4 v[40:43], v238, s[36:39], s32 offen offset:2304 sc1\n" \
  "s_add_u32 s32, s8, 4096\n" \
  "buffer_load_dwordx4 v[44:47], v230, s[36:39], s8 offen offset:0 sc1\n" \
  "buffer_load_dwordx4 v[66:69], v230, s[36:39], s32 offen offset:0 sc1\n" \
  "buffer_load_dwordx4 v[70:73], v240, s[36:39], s9 offen offset:0 sc1\n" \
  "buffer_load_dwordx2 v[8:9], v245, s[36:39], s29 offen offset:0 sc1\n" \
  "s_waitcnt vmcnt(29)\n" \
  "ds_read_b64_tr_b16 a[0:1], v18 offset:0\n" \
  "ds_read_b64_tr_b16 a[4:5], v19 offset:0\n" \
  "ds_read_b64_tr_b16 a[8:9], v20 offset:0\n" \
  "ds_read_b64_tr_b16 a[12:13], v21 offset:0\n" \
  "ds_read_b64_tr_b16 a[20:21], v18 offset:2048\n" \
  "ds_read_b64_tr_b16 a[24:25], v19 offset:2048\n" \
  "ds_read_b64_tr_b16 a[28:29], v20 offset:2048\n" \
  "ds_read_b64_tr_b16 a[32:33], v21 offset:2048\n" \
  "ds_read_b64_tr_b16 a[2:3], v18 offset:4096\n" \
  "ds_read_b64_tr_b16 a[6:7], v19 offset:4096\n" \
  "ds_read_b64_tr_b16 a[10:11], v20 offset:4096\n" \
  "ds_read_b64_tr_b16 a[14:15], v21 offset:4096\n" \
  "ds_read_b64_tr_b16 a[22:23], v18 offset:6144\n" \
  "ds_read_b64_tr_b16 a[26:27], v19 offset:6144\n" \
  "ds_read_b64_tr_b16 a[30:31], v20 offset:6144\n" \
  "ds_read_b64_tr_b16 a[34:35], v21 offset:6144\n" \
  "s_waitcnt vmcnt(20)\n" \
  "ds_read_b64_tr_b16 a[80:81], v18 offset:8192\n" \
  "ds_read_b64_tr_b16 a[84:85], v19 offset:8192\n" \
  "ds_read_b64_tr_b16 a[88:89], v20 offset:8192\n" \
  "ds_read_b64_tr_b16 a[92:93], v21 offset:8192\n" \
  "ds_read_b64_tr_b16 a[100:101], v18 offset:10240\n" \
  "ds_read_b64_tr_b16 a[104:105], v19 offset:10240\n" \
  "ds_read_b64_tr_b16 a[108:109], v20 offset:10240\n" \
  "ds_read_b64_tr_b16 a[112:113], v21 offset:10240\n" \
  "ds_read_b64_tr_b16 a[82:83], v18 offset:12288\n" \
  "ds_read_b64_tr_b16 a[86:87], v19 offset:12288\n" \
  "ds_read_b64_tr_b16 a[90:91], v20 offset:12288\n" \
  "ds_read_b64_tr_b16 a[94:95], v21 offset:12288\n" \
  "ds_read_b64_tr_b16 a[102:103], v18 offset:14336\n" \
  "ds_read_b64_tr_b16 a[106:107], v19 offset:14336\n" \
  "ds_read_b64_tr_b16 a[110:111], v20 offset:14336\n" \
  "ds_read_b64_tr_b16 a[114:115], v21 offset:14336\n" \
  "s_waitcnt vmcnt(12)\n" \
  "ds_read_b64_tr_b16 a[40:41], v18 offset:16384\n" \
  "ds_read_b64_tr_b16 a[44:45], v19 offset:16384\n" \
  "ds_read_b64_tr_b16 a[48:49], v20 offset:16384\n" \
  "ds_read_b64_tr_b16 a[52:53], v21 offset:16384\n" \
  "ds_read_b64_tr_b16 a[60:61], v18 offset:18432\n" \
  "ds_read_b64_tr_b16 a[64:65], v19 offset:18432\n" \
  "ds_read_b64_tr_b16 a[68:69], v20 offset:18432\n" \
  "ds_read_b64_tr_b16 a[72:73], v21 offset:18432\n" \
  "ds_read_b64_tr_b16 a[42:43], v18 offset:20480\n" \
  "ds_read_b64_tr_b16 a[46:47], v19 offset:20480\n" \
  "ds_read_b64_tr_b16 a[50:51], v20 offset:20480\n" \
  "ds_read_b64_tr_b16 a[54:55], v21 offset:20480\n" \
  "ds_read_b64_tr_b16 a[62:63], v18 offset:22528\n" \
  "ds_read_b64_tr_b16 a[66:67], v19 offset:22528\n" \
  "ds_read_b64_tr_b16 a[70:71], v20 offset:22528\n" \
  "ds_read_b64_tr_b16 a[74:75], v21 offset:22528\n" \
  "s_waitcnt lgkmcnt(0)\n" \
  "s_barrier\n" \
  "s_waitcnt vmcnt(4)\n" \
  ".Lmcc_%=:\n" \
  "v_xor_b32 v239, s18, v28\n" \
  "v_bitop3_b32 v239, v29, s19, v239 bitop3:0xbe\n" \
  "v_bitop3_b32 v239, v32, s18, v239 bitop3:0xbe\n" \
  "v_bitop3_b32 v239, v33, s19, v239 bitop3:0xbe\n" \
  "v_bitop3_b32 v239, v36, s18, v239 bitop3:0xbe\n" \
  "v_bitop3_b32 v239, v37, s19, v239 bitop3:0xbe\n" \
  "v_bitop3_b32 v239, v40, s18, v239 bitop3:0xbe\n" \
  "v_bitop3_b32 v239, v41, s19, v239 bitop3:0xbe\n" \
  "v_cmp_ne_u32 vcc, 0, v239\n" \
  "s_and_b64 vcc, vcc, s[94:95]\n" \
  "s_cbranch_scc1 .Lmcs_%=\n" \
  ".Lmcd_%=:\n" \
  "v_lshlrev_b32 v74, 16, v30\n" \
  "v_and_b32 v75, 4294901760, v30\n" \
  "v_lshlrev_b32 v76, 16, v31\n" \
  "v_and_b32 v77, 4294901760, v31\n" \
  "v_lshlrev_b32 v28, 16, v34\n" \
  "v_and_b32 v29, 4294901760, v34\n" \
  "v_lshlrev_b32 v30, 16, v35\n" \
  "v_and_b32 v31, 4294901760, v35\n" \
  "v_lshlrev_b32 v32, 16, v38\n" \
  "v_and_b32 v33, 4294901760, v38\n" \
  "v_lshlrev_b32 v34, 16, v39\n" \
  "v_and_b32 v35, 4294901760, v39\n" \
  "v_lshlrev_b32 v36, 16, v42\n" \
  "v_and_b32 v37, 4294901760, v42\n" \
  "v_lshlrev_b32 v38, 16, v43\n" \
  "v_and_b32 v39, 4294901760, v43\n" \
  "v_mul_f32 v74, 1055100473, v74\n" \
  "v_ldexp_f32 v74, v74, s23\n" \
  "v_mul_f32 v75, 1055100473, v75\n" \
  "v_ldexp_f32 v75, v75, s23\n" \
  "v_mul_f32 v76, 1055100473, v76\n" \
  "v_ldexp_f32 v76, v76, s23\n" \
  "v_mul_f32 v77, 1055100473, v77\n" \
  "v_ldexp_f32 v77, v77, s23\n" \
  "s_mov_b64 exec, s[94:95]\n" \
  "ds_write_b128 v246, v[28:31] offset:31104\n" \
  "ds_write_b128 v246, v[32:35] offset:33408\n" \
  "ds_write_b128 v246, v[74:77] offset:28800\n" \
  "s_bfe_u32 s32, s27, 524296\n" \
  "s_lshl_b32 s32, s32, 4\n" \
  "s_sub_u32 s32, 163200, s32\n" \
  "v_add_u32 v239, s32, v246\n" \
  "s_mov_b64 exec, s[96:97]\n" \
  "ds_write_b128 v239, v[36:39] offset:0\n" \
  "s_mov_b64 exec, -1\n" \
  "s_add_u32 m0, s57, 0\n" \
  "s_add_u32 s31, s12, 368640\n" \
  "buffer_load_dwordx4 v22, s[4:7], s31 offen offset:0 lds\n" \
  "buffer_load_dwordx4 v23, s[4:7], s31 offen offset:1024 lds\n" \
  "buffer_load_dwordx4 v24, s[4:7], s31 offen offset:2048 lds\n" \
  "buffer_load_dwordx4 v25, s[4:7], s31 offen offset:3072 lds\n" \
  "s_add_u32 m0, s57, 4096\n" \
  "s_add_u32 s31, s12, 9805824\n" \
  "buffer_load_dwordx4 v22, s[4:7], s31 offen offset:0 lds\n" \
  "buffer_load_dwordx4 v23, s[4:7], s31 offen offset:1024 lds\n" \
  "buffer_load_dwordx4 v24, s[4:7], s31 offen offset:2048 lds\n" \
  "buffer_load_dwordx4 v25, s[4:7], s31 offen offset:3072 lds\n" \
  "s_add_u32 m0, s57, 8192\n" \
  "s_add_u32 s31, s12, 147456\n" \
  "buffer_load_dwordx4 v22, s[4:7], s31 offen offset:0 lds\n" \
  "buffer_load_dwordx4 v23, s[4:7], s31 offen offset:1024 lds\n" \
  "buffer_load_dwordx4 v24, s[4:7], s31 offen offset:2048 lds\n" \
  "buffer_load_dwordx4 v25, s[4:7], s31 offen offset:3072 lds\n" \
  "s_add_u32 m0, s57, 12288\n" \
  "s_add_u32 s31, s12, 9584640\n" \
  "buffer_load_dwordx4 v22, s[4:7], s31 offen offset:0 lds\n" \
  "buffer_load_dwordx4 v23, s[4:7], s31 offen offset:1024 lds\n" \
  "buffer_load_dwordx4 v24, s[4:7], s31 offen offset:2048 lds\n" \
  "buffer_load_dwordx4 v25, s[4:7], s31 offen offset:3072 lds\n" \
  "s_waitcnt vmcnt(17)\n" \
  ".Lrcc_%=:\n" \
  "v_bitop3_b32 v246, v44, v66, v70 bitop3:0x80\n" \
  "v_and_b32 v246, 1, v246\n" \
  "v_cmp_eq_u32 vcc, 0, v246\n" \
  "s_cbranch_vccnz .Lrcs_%=\n" \
  ".Lrcd_%=:\n" \
  "v_mfma_f32_16x16x32_f16 v[28:31], a[228:231], v[44:47], 0\n" \
  "s_add_u32 m0, s57, 16384\n" \
  "s_add_u32 s31, s12, 442368\n" \
  "buffer_load_dwordx4 v22, s[4:7], s31 offen offset:0 lds\n" \
  "v_mfma_f32_16x16x32_f16 v[32:35], a[232:235], v[44:47], 0\n" \
  "buffer_load_dwordx4 v23, s[4:7], s31 offen offset:1024 lds\n" \
  "v_mfma_f32_16x16x32_f16 v[36:39], a[228:231], v[66:69], 0\n" \
  "buffer_load_dwordx4 v24, s[4:7], s31 offen offset:2048 lds\n" \
  "v_mfma_f32_16x16x32_f16 v[40:43], a[232:235], v[66:69], 0\n" \
  "buffer_load_dwordx4 v25, s[4:7], s31 offen offset:3072 lds\n" \
  "v_mfma_f32_16x16x32_f16 v[44:47], a[228:231], v[70:73], 0\n" \
  "s_add_u32 m0, s57, 20480\n" \
  "s_add_u32 s31, s12, 9879552\n" \
  "buffer_load_dwordx4 v22, s[4:7], s31 offen offset:0 lds\n" \
  "v_mfma_f32_16x16x32_f16 v[66:69], a[232:235], v[70:73], 0\n" \
  "buffer_load_dwordx4 v23, s[4:7], s31 offen offset:1024 lds\n" \
  "v_cvt_pk_f16_f32 v70, v32, v28\n" \
  "v_cvt_pk_f16_f32 v71, v33, v29\n" \
  "v_cvt_pk_f16_f32 v72, v34, v30\n" \
  "v_cvt_pk_f16_f32 v73, v35, v31\n" \
  "ds_write_b128 v103, v[70:73] offset:0\n" \
  "v_cvt_pk_f16_f32 v28, v40, v36\n" \
  "v_cvt_pk_f16_f32 v29, v41, v37\n" \
  "v_cvt_pk_f16_f32 v30, v42, v38\n" \
  "v_cvt_pk_f16_f32 v31, v43, v39\n" \
  "ds_write_b128 v104, v[28:31] offset:0\n" \
  "v_cvt_pk_f16_f32 v28, v66, v44\n" \
  "v_cvt_pk_f16_f32 v29, v67, v45\n" \
  "v_cvt_pk_f16_f32 v30, v68, v46\n" \
  "v_cvt_pk_f16_f32 v31, v69, v47\n" \
  "s_mov_b64 exec, s[92:93]\n" \
  "ds_write_b128 v105, v[28:31] offset:0\n" \
  "s_mov_b64 exec, -1\n" \
  "s_waitcnt lgkmcnt(0)\n" \
  "s_barrier\n" \
  "ds_read_b64 v[28:29], v102 offset:0\n" \
  "ds_read_b64 v[30:31], v102 offset:512\n" \
  "ds_read_b64 v[32:33], v102 offset:1024\n" \
  "ds_read_b64 v[34:35], v102 offset:256\n" \
  "ds_read_b64 v[36:37], v102 offset:768\n" \
  "ds_read_b64 v[38:39], v102 offset:1280\n" \
  "v_lshlrev_b32 v246, 5, v254\n" \
  "buffer_load_dwordx4 v24, s[4:7], s31 offen offset:2048 lds\n" \
  "buffer_load_dwordx4 v25, s[4:7], s31 offen offset:3072 lds\n" \
  "s_waitcnt lgkmcnt(5)\n" \
  "v_mov_b32 v50, v28\n" \
  "s_waitcnt lgkmcnt(4)\n" \
  "v_mov_b32 v51, v30\n" \
  "s_waitcnt lgkmcnt(3)\n" \
  "v_mov_b32 v52, v32\n" \
  "v_mov_b32 v58, v29\n" \
  "v_mov_b32 v59, v31\n" \
  "v_mfma_f32_16x16x32_f16 v[28:31], v[50:53], a[208:211], 0\n" \
  "v_mov_b32 v60, v33\n" \
  "s_waitcnt lgkmcnt(2)\n" \
  "v_mov_b32 v54, v34\n" \
  "v_mfma_f32_16x16x32_f16 v[40:43], v[50:53], a[212:215], 0\n" \
  "s_waitcnt lgkmcnt(1)\n" \
  "v_mov_b32 v55, v36\n" \
  "s_waitcnt lgkmcnt(0)\n" \
  "v_mov_b32 v56, v38\n" \
  "v_mfma_f32_16x16x32_f16 v[44:47], v[58:61], a[208:211], 0\n" \
  "v_mov_b32 v62, v35\n" \
  "v_mov_b32 v63, v37\n" \
  "v_mfma_f32_16x16x32_f16 v[32:35], v[58:61], a[212:215], 0\n" \
  "v_mov_b32 v64, v39\n" \
  "v_mfma_f32_16x16x32_f16 v[36:39], v[54:57], a[208:211], 0\n" \
  "ds_read_b128 v[50:53], v246 offset:31104\n" \
  "ds_read_b128 v[58:61], v246 offset:31120\n" \
  "v_mfma_f32_16x16x32_f16 v[66:69], v[54:57], a[212:215], 0\n" \
  "ds_read_b128 v[54:57], v246 offset:32128\n" \
  "ds_read_b128 v[70:73], v246 offset:32144\n" \
  "v_mfma_f32_16x16x32_f16 v[74:77], v[62:65], a[208:211], 0\n" \
  "ds_read_b64 v[78:79], v245 offset:33152\n" \
  "v_mfma_f32_16x16x32_f16 v[80:83], v[62:65], a[212:215], 0\n" \
  "ds_read_b128 v[62:65], v246 offset:28800\n" \
  "ds_read_b128 v[84:87], v246 offset:28816\n" \
  "ds_read_b128 v[88:91], v246 offset:29824\n" \
  "ds_read_b128 v[92:95], v246 offset:29840\n" \
  "ds_read_b64 v[96:97], v245 offset:30848\n" \
  "v_pk_add_f32 v[98:99], v[28:29], v[36:37]\n" \
  "v_pk_add_f32 v[100:101], v[30:31], v[38:39]\n" \
  "v_pk_add_f32 v[102:103], v[40:41], v[66:67]\n" \
  "v_pk_add_f32 v[104:105], v[42:43], v[68:69]\n" \
  "v_pk_add_f32 v[106:107], v[28:29], v[36:37] neg_lo:[0,1] neg_hi:[0,1]\n" \
  "v_pk_add_f32 v[108:109], v[30:31], v[38:39] neg_lo:[0,1] neg_hi:[0,1]\n" \
  "v_pk_add_f32 v[28:29], v[40:41], v[66:67] neg_lo:[0,1] neg_hi:[0,1]\n" \
  "v_pk_add_f32 v[30:31], v[42:43], v[68:69] neg_lo:[0,1] neg_hi:[0,1]\n" \
  "v_pk_add_f32 v[36:37], v[44:45], v[74:75]\n" \
  "v_pk_add_f32 v[38:39], v[46:47], v[76:77]\n" \
  "v_pk_add_f32 v[40:41], v[32:33], v[80:81]\n" \
  "v_pk_add_f32 v[42:43], v[34:35], v[82:83]\n" \
  "v_pk_add_f32 v[66:67], v[44:45], v[74:75] neg_lo:[0,1] neg_hi:[0,1]\n" \
  "v_pk_add_f32 v[68:69], v[46:47], v[76:77] neg_lo:[0,1] neg_hi:[0,1]\n" \
  "v_pk_add_f32 v[44:45], v[32:33], v[80:81] neg_lo:[0,1] neg_hi:[0,1]\n" \
  "v_pk_add_f32 v[46:47], v[34:35], v[82:83] neg_lo:[0,1] neg_hi:[0,1]\n" \
  "v_cvt_pk_f16_f32 v32, v98, v102\n" \
  "v_cvt_pk_f16_f32 v33, v99, v103\n" \
  "v_cvt_pk_f16_f32 v34, v100, v104\n" \
  "v_cvt_pk_f16_f32 v35, v101, v105\n" \
  "v_cvt_pk_f16_f32 v74, v106, v28\n" \
  "v_cvt_pk_f16_f32 v75, v107, v29\n" \
  "v_mfma_f32_16x16x32_f16 v[80:83], a[200:203], v[32:35], 0\n" \
  "v_cvt_pk_f16_f32 v76, v108, v30\n" \
  "v_cvt_pk_f16_f32 v77, v109, v31\n" \
  "v_mfma_f32_16x16x32_f16 v[28:31], a[204:207], v[32:35], 0\n" \
  "v_cvt_pk_f16_f32 v32, v36, v40\n" \
  "v_cvt_pk_f16_f32 v33, v37, v41\n" \
  "v_mfma_f32_16x16x32_f16 v[98:101], a[220:223], v[74:77], 0\n" \
  "v_cvt_pk_f16_f32 v34, v38, v42\n" \
  "v_cvt_pk_f16_f32 v35, v39, v43\n" \
  "v_mfma_f32_16x16x32_f16 v[36:39], a[224:227], v[74:77], 0\n" \
  "v_cvt_pk_f16_f32 v40, v66, v44\n" \
  "v_cvt_pk_f16_f32 v41, v67, v45\n" \
  "v_mfma_f32_16x16x32_f16 v[74:77], a[200:203], v[32:35], 0\n" \
  "v_cvt_pk_f16_f32 v42, v68, v46\n" \
  "v_cvt_pk_f16_f32 v43, v69, v47\n" \
  "v_mfma_f32_16x16x32_f16 v[44:47], a[204:207], v[32:35], 0\n" \
  "ds_read_b128 v[32:35], v246 offset:33408\n" \
  "v_mfma_f32_16x16x32_f16 v[66:69], a[220:223], v[40:43], 0\n" \
  "ds_read_b128 v[102:105], v246 offset:33424\n" \
  "v_mfma_f32_16x16x32_f16 v[106:109], a[224:227], v[40:43], 0\n" \
  "ds_read_b128 v[40:43], v246 offset:34432\n" \
  "ds_read_b128 v[110:113], v246 offset:34448\n" \
  "ds_read_b64 v[124:125], v245 offset:35456\n" \
  "s_waitcnt lgkmcnt(14)\n" \
  "v_readfirstlane_b32 s32, v50\n" \
  "s_mov_b32 exec_lo, 125241207\n" \
  "s_mov_b32 exec_hi, 125241207\n" \
  "v_cvt_pk_f16_f32 v254, v28, v80\n" \
  "ds_write_b32 v116, v254 offset:0\n" \
  "v_cvt_pk_f16_f32 v254, v29, v81\n" \
  "ds_write_b32 v117, v254 offset:0\n" \
  "v_cvt_pk_f16_f32 v254, v30, v82\n" \
  "ds_write_b32 v118, v254 offset:0\n" \
  "v_cvt_pk_f16_f32 v254, v31, v83\n" \
  "ds_write_b32 v119, v254 offset:0\n" \
  "v_cvt_pk_f16_f32 v254, v36, v98\n" \
  "ds_write_b32 v120, v254 offset:0\n" \
  "v_cvt_pk_f16_f32 v254, v37, v99\n" \
  "ds_write_b32 v121, v254 offset:0\n" \
  "v_cvt_pk_f16_f32 v254, v38, v100\n" \
  "ds_write_b32 v122, v254 offset:0\n" \
  "v_cvt_pk_f16_f32 v254, v39, v101\n" \
  "ds_write_b32 v123, v254 offset:0\n" \
  "v_cvt_pk_f16_f32 v254, v44, v74\n" \
  "ds_write_b32 v116, v254 offset:2448\n" \
  "v_cvt_pk_f16_f32 v254, v45, v75\n" \
  "ds_write_b32 v117, v254 offset:2448\n" \
  "v_cvt_pk_f16_f32 v254, v46, v76\n" \
  "ds_write_b32 v118, v254 offset:2448\n" \
  "v_cvt_pk_f16_f32 v254, v47, v77\n" \
  "ds_write_b32 v119, v254 offset:2448\n" \
  "v_cvt_pk_f16_f32 v254, v106, v66\n" \
  "ds_write_b32 v120, v254 offset:2448\n" \
  "v_cvt_pk_f16_f32 v254, v107, v67\n" \
  "ds_write_b32 v121, v254 offset:2448\n" \
  "v_cvt_pk_f16_f32 v254, v108, v68\n" \
  "ds_write_b32 v122, v254 offset:2448\n" \
  "v_cvt_pk_f16_f32 v254, v109, v69\n" \
  "ds_write_b32 v123, v254 offset:2448\n" \
  "s_mov_b64 exec, -1\n" \
  "ds_read_b128 v[28:31], v248 offset:0\n" \
  "ds_read_b128 v[36:39], v248 offset:576\n" \
  "ds_read_b32 v254, v249 offset:1152\n" \
  "v_mov_b32 v44, s32\n" \
  "v_pk_add_f32 v[50:51], v[50:51], v[44:45] op_sel_hi:[1,0] neg_lo:[0,1] neg_hi:[0,1]\n" \
  "v_pk_add_f32 v[52:53], v[52:53], v[44:45] op_sel_hi:[1,0] neg_lo:[0,1] neg_hi:[0,1]\n" \
  "s_waitcnt lgkmcnt(15)\n" \
  "v_pk_add_f32 v[58:59], v[58:59], v[44:45] op_sel_hi:[1,0] neg_lo:[0,1] neg_hi:[0,1]\n" \
  "v_pk_add_f32 v[60:61], v[60:61], v[44:45] op_sel_hi:[1,0] neg_lo:[0,1] neg_hi:[0,1]\n" \
  "v_pk_add_f32 v[54:55], v[54:55], v[44:45] op_sel_hi:[1,0] neg_lo:[0,1] neg_hi:[0,1]\n" \
  "v_pk_add_f32 v[56:57], v[56:57], v[44:45] op_sel_hi:[1,0] neg_lo:[0,1] neg_hi:[0,1]\n" \
  "v_pk_add_f32 v[70:71], v[70:71], v[44:45] op_sel_hi:[1,0] neg_lo:[0,1] neg_hi:[0,1]\n" \
  "v_pk_add_f32 v[72:73], v[72:73], v[44:45] op_sel_hi:[1,0] neg_lo:[0,1] neg_hi:[0,1]\n" \
  "v_pk_add_f32 v[78:79], v[78:79], v[44:45] op_sel_hi:[1,0] neg_lo:[0,1] neg_hi:[0,1]\n" \
  "s_waitcnt lgkmcnt(2)\n" \
  "v_fma_mix_f32 v10, v28, v62, v10 op_sel:[0,0,0] op_sel_hi:[1,0,0]\n" \
  "v_fma_mix_f32 v11, v28, v63, v11 op_sel:[1,0,0] op_sel_hi:[1,0,0]\n" \
  "v_fma_mix_f32 v12, v29, v64, v12 op_sel:[0,0,0] op_sel_hi:[1,0,0]\n" \
  "v_fma_mix_f32 v13, v29, v65, v13 op_sel:[1,0,0] op_sel_hi:[1,0,0]\n" \
  "v_fma_mix_f32 v14, v30, v84, v14 op_sel:[0,0,0] op_sel_hi:[1,0,0]\n" \
  "v_fma_mix_f32 v15, v30, v85, v15 op_sel:[1,0,0] op_sel_hi:[1,0,0]\n" \
  "v_fma_mix_f32 v26, v31, v86, v26 op_sel:[0,0,0] op_sel_hi:[1,0,0]\n" \
  "v_fma_mix_f32 v27, v31, v87, v27 op_sel:[1,0,0] op_sel_hi:[1,0,0]\n" \
  "s_waitcnt lgkmcnt(1)\n" \
  "v_fma_mix_f32 v2, v36, v88, v2 op_sel:[0,0,0] op_sel_hi:[1,0,0]\n" \
  "v_fma_mix_f32 v3, v36, v89, v3 op_sel:[1,0,0] op_sel_hi:[1,0,0]\n" \
  "v_fma_mix_f32 v4, v37, v90, v4 op_sel:[0,0,0] op_sel_hi:[1,0,0]\n" \
  "v_fma_mix_f32 v5, v37, v91, v5 op_sel:[1,0,0] op_sel_hi:[1,0,0]\n" \
  "v_fma_mix_f32 v48, v38, v92, v48 op_sel:[0,0,0] op_sel_hi:[1,0,0]\n" \
  "v_fma_mix_f32 v49, v38, v93, v49 op_sel:[1,0,0] op_sel_hi:[1,0,0]\n" \
  "v_fma_mix_f32 v114, v39, v94, v114 op_sel:[0,0,0] op_sel_hi:[1,0,0]\n" \
  "v_fma_mix_f32 v115, v39, v95, v115 op_sel:[1,0,0] op_sel_hi:[1,0,0]\n" \
  "s_waitcnt lgkmcnt(0)\n" \
  "v_fma_mix_f32 v6, v254, v96, v6 op_sel:[0,0,0] op_sel_hi:[1,0,0]\n" \
  "v_fma_mix_f32 v7, v254, v97, v7 op_sel:[1,0,0] op_sel_hi:[1,0,0]\n" \
  "v_mov_b32 v29, 925353388\n" \
  "v_mov_b32_dpp v30, v10 row_newbcast:0 row_mask:0xf bank_mask:0xf\n" \
  "v_mov_b32_dpp v254, v10 row_newbcast:0 row_mask:0xf bank_mask:0xf\n" \
  "v_cvt_pk_bf16_f32 v36, v10, v11\n" \
  "v_cvt_pk_bf16_f32 v37, v12, v13\n" \
  "v_cvt_pk_bf16_f32 v38, v14, v15\n" \
  "v_cvt_pk_bf16_f32 v39, v26, v27\n" \
  "v_cvt_pk_bf16_f32 v44, v2, v3\n" \
  "v_cvt_pk_bf16_f32 v45, v4, v5\n" \
  "v_cvt_pk_bf16_f32 v46, v48, v49\n" \
  "v_cvt_pk_bf16_f32 v47, v114, v115\n" \
  "v_cvt_pk_bf16_f32 v248, v6, v7\n" \
  "v_permlane16_swap_b32 v30, v254\n" \
  "v_pk_add_f32 v[10:11], v[10:11], v[30:31] op_sel_hi:[1,0] neg_lo:[0,1] neg_hi:[0,1]\n" \
  "v_pk_add_f32 v[12:13], v[12:13], v[30:31] op_sel_hi:[1,0] neg_lo:[0,1] neg_hi:[0,1]\n" \
  "v_pk_add_f32 v[14:15], v[14:15], v[30:31] op_sel_hi:[1,0] neg_lo:[0,1] neg_hi:[0,1]\n" \
  "v_pk_add_f32 v[26:27], v[26:27], v[30:31] op_sel_hi:[1,0] neg_lo:[0,1] neg_hi:[0,1]\n" \
  "v_pk_add_f32 v[2:3], v[2:3], v[30:31] op_sel_hi:[1,0] neg_lo:[0,1] neg_hi:[0,1]\n" \
  "v_pk_add_f32 v[4:5], v[4:5], v[30:31] op_sel_hi:[1,0] neg_lo:[0,1] neg_hi:[0,1]\n" \
  "v_pk_add_f32 v[48:49], v[48:49], v[30:31] op_sel_hi:[1,0] neg_lo:[0,1] neg_hi:[0,1]\n" \
  "v_pk_add_f32 v[114:115], v[114:115], v[30:31] op_sel_hi:[1,0] neg_lo:[0,1] neg_hi:[0,1]\n" \
  "v_pk_add_f32 v[6:7], v[6:7], v[30:31] op_sel_hi:[1,0] neg_lo:[0,1] neg_hi:[0,1]\n" \
  "v_pk_add_f32 v[30:31], v[10:11], v[12:13]\n" \
  "v_pk_mul_f32 v[62:63], v[10:11], v[10:11]\n" \
  "v_pk_fma_f32 v[62:63], v[12:13], v[12:13], v[62:63]\n" \
  "v_pk_add_f32 v[30:31], v[30:31], v[14:15]\n" \
  "v_pk_fma_f32 v[62:63], v[14:15], v[14:15], v[62:63]\n" \
  "v_pk_add_f32 v[30:31], v[30:31], v[26:27]\n" \
  "v_pk_fma_f32 v[62:63], v[26:27], v[26:27], v[62:63]\n" \
  "v_pk_add_f32 v[30:31], v[30:31], v[2:3]\n" \
  "v_pk_fma_f32 v[62:63], v[2:3], v[2:3], v[62:63]\n" \
  "v_pk_add_f32 v[30:31], v[30:31], v[4:5]\n" \
  "v_pk_fma_f32 v[62:63], v[4:5], v[4:5], v[62:63]\n" \
  "v_pk_add_f32 v[30:31], v[30:31], v[48:49]\n" \
  "v_pk_fma_f32 v[62:63], v[48:49], v[48:49], v[62:63]\n" \
  "v_pk_add_f32 v[30:31], v[30:31], v[114:115]\n" \
  "v_pk_fma_f32 v[62:63], v[114:115], v[114:115], v[62:63]\n" \
  "v_pk_add_f32 v[30:31], v[30:31], v[6:7]\n" \
  "v_pk_fma_f32 v[62:63], v[6:7], v[6:7], v[62:63]\n" \
  "v_add_f32 v254, v30, v31\n" \
  "v_add_f32 v246, v62, v63\n" \
  "s_waitcnt vmcnt(16)\n" \
  "ds_read_b64_tr_b16 a[120:121], v18 offset:0\n" \
  "ds_read_b64_tr_b16 a[124:125], v19 offset:0\n" \
  "ds_read_b64_tr_b16 a[128:129], v20 offset:0\n" \
  "ds_read_b64_tr_b16 a[132:133], v21 offset:0\n" \
  "v_permlane16_swap_b32 v254, v246\n" \
  "v_add_f32 v254, v254, v246\n" \
  "buffer_store_dwordx4 v[36:39], v251, s[36:39], s10 offen offset:0 sc1\n" \
  "ds_read_b64_tr_b16 a[140:141], v18 offset:2048\n" \
  "ds_read_b64_tr_b16 a[144:145], v19 offset:2048\n" \
  "ds_read_b64_tr_b16 a[148:149], v20 offset:2048\n" \
  "v_add_f32_dpp v254, v254, v254 quad_perm:[1,0,3,2] row_mask:0xf bank_mask:0xf\n" \
  "buffer_store_dwordx4 v[44:47], v251, s[36:39], s10 offen offset:512 sc1\n" \
  "ds_read_b64_tr_b16 a[152:153], v21 offset:2048\n" \
  "ds_read_b64_tr_b16 a[122:123], v18 offset:4096\n" \
  "ds_read_b64_tr_b16 a[126:127], v19 offset:4096\n" \
  "v_add_f32_dpp v254, v254, v254 quad_perm:[2,3,0,1] row_mask:0xf bank_mask:0xf\n" \
  "buffer_store_dword v248, v250, s[36:39], s10 offen offset:1024 sc1\n" \
  "ds_read_b64_tr_b16 a[130:131], v20 offset:4096\n" \
  "ds_read_b64_tr_b16 a[134:135], v21 offset:4096\n" \
  "ds_read_b64_tr_b16 a[142:143], v18 offset:6144\n" \
  "v_add_f32_dpp v254, v254, v254 row_half_mirror row_mask:0xf bank_mask:0xf\n" \
  "ds_read_b64_tr_b16 a[146:147], v19 offset:6144\n" \
  "ds_read_b64_tr_b16 a[150:151], v20 offset:6144\n" \
  "ds_read_b64_tr_b16 a[154:155], v21 offset:6144\n" \
  "v_add_f32_dpp v254, v254, v254 row_mirror row_mask:0xf bank_mask:0xf\n" \
  "v_mov_b32 v246, v254\n" \
  "s_waitcnt vmcnt(11)\n" \
  "ds_read_b64_tr_b16 a[160:161], v18 offset:8192\n" \
  "ds_read_b64_tr_b16 a[164:165], v19 offset:8192\n" \
  "ds_read_b64_tr_b16 a[168:169], v20 offset:8192\n" \
  "ds_read_b64_tr_b16 a[172:173], v21 offset:8192\n" \
  "v_permlane16_swap_b32 v254, v246\n" \
  "v_mul_f32 v254, s35, v254\n" \
  "v_fma_f32 v246, v246, s35, v29\n" \
  "v_fma_f32 v246, -v254, v254, v246\n" \
  "v_max_f32 v246, v246, v29\n" \
  "v_rsq_f32 v28, v246\n" \
  "ds_read_b64_tr_b16 a[180:181], v18 offset:10240\n" \
  "ds_read_b64_tr_b16 a[184:185], v19 offset:10240\n" \
  "ds_read_b64_tr_b16 a[188:189], v20 offset:10240\n" \
  "ds_read_b64_tr_b16 a[192:193], v21 offset:10240\n" \
  "v_mul_f32_e64 v29, -v254, v28\n" \
  "v_pk_fma_f32 v[10:11], v[10:11], v[28:29], v[28:29] op_sel:[0,0,1] op_sel_hi:[1,0,1]\n" \
  "v_pk_fma_f32 v[10:11], v[10:11], v[32:33], v[50:51]\n" \
  "v_pk_fma_f32 v[12:13], v[12:13], v[28:29], v[28:29] op_sel:[0,0,1] op_sel_hi:[1,0,1]\n" \
  "v_pk_fma_f32 v[12:13], v[12:13], v[34:35], v[52:53]\n" \
  "v_pk_fma_f32 v[14:15], v[14:15], v[28:29], v[28:29] op_sel:[0,0,1] op_sel_hi:[1,0,1]\n" \
  "v_pk_fma_f32 v[14:15], v[14:15], v[102:103], v[58:59]\n" \
  "ds_read_b64_tr_b16 a[162:163], v18 offset:12288\n" \
  "ds_read_b64_tr_b16 a[166:167], v19 offset:12288\n" \
  "s_add_u32 m0, s57, 0\n" \
  "s_add_u32 s31, s12, 221184\n" \
  "s_waitcnt lgkmcnt(10)\n" \
  "buffer_load_dwordx4 v22, s[4:7], s31 offen offset:0 lds\n" \
  "v_pk_fma_f32 v[26:27], v[26:27], v[28:29], v[28:29] op_sel:[0,0,1] op_sel_hi:[1,0,1]\n" \
  "v_pk_fma_f32 v[26:27], v[26:27], v[104:105], v[60:61]\n" \
  "v_pk_fma_f32 v[2:3], v[2:3], v[28:29], v[28:29] op_sel:[0,0,1] op_sel_hi:[1,0,1]\n" \
  "v_pk_fma_f32 v[2:3], v[2:3], v[40:41], v[54:55]\n" \
  "ds_read_b64_tr_b16 a[170:171], v20 offset:12288\n" \
  "ds_read_b64_tr_b16 a[174:175], v21 offset:12288\n" \
  "buffer_load_dwordx4 v23, s[4:7], s31 offen offset:1024 lds\n" \
  "v_pk_fma_f32 v[4:5], v[4:5], v[28:29], v[28:29] op_sel:[0,0,1] op_sel_hi:[1,0,1]\n" \
  "v_pk_fma_f32 v[4:5], v[4:5], v[42:43], v[56:57]\n" \
  "v_pk_fma_f32 v[48:49], v[48:49], v[28:29], v[28:29] op_sel:[0,0,1] op_sel_hi:[1,0,1]\n" \
  "v_pk_fma_f32 v[48:49], v[48:49], v[110:111], v[70:71]\n" \
  "ds_read_b64_tr_b16 a[182:183], v18 offset:14336\n" \
  "ds_read_b64_tr_b16 a[186:187], v19 offset:14336\n" \
  "buffer_load_dwordx4 v24, s[4:7], s31 offen offset:2048 lds\n" \
  "v_pk_fma_f32 v[114:115], v[114:115], v[28:29], v[28:29] op_sel:[0,0,1] op_sel_hi:[1,0,1]\n" \
  "v_pk_fma_f32 v[114:115], v[114:115], v[112:113], v[72:73]\n" \
  "v_pk_fma_f32 v[6:7], v[6:7], v[28:29], v[28:29] op_sel:[0,0,1] op_sel_hi:[1,0,1]\n" \
  "v_pk_fma_f32 v[6:7], v[6:7], v[124:125], v[78:79]\n" \
  "ds_read_b64_tr_b16 a[190:191], v20 offset:14336\n" \
  "ds_read_b64_tr_b16 a[194:195], v21 offset:14336\n" \
  "buffer_load_dwordx4 v25, s[4:7], s31 offen offset:3072 lds\n" \
  "v_mov_b32 v29, 925353388\n" \
  "v_pk_add_f32 v[30:31], v[10:11], v[12:13]\n" \
  "v_pk_mul_f32 v[32:33], v[10:11], v[10:11]\n" \
  "v_pk_fma_f32 v[32:33], v[12:13], v[12:13], v[32:33]\n" \
  "v_pk_add_f32 v[30:31], v[30:31], v[14:15]\n" \
  "v_pk_fma_f32 v[32:33], v[14:15], v[14:15], v[32:33]\n" \
  "v_pk_add_f32 v[30:31], v[30:31], v[26:27]\n" \
  "v_pk_fma_f32 v[32:33], v[26:27], v[26:27], v[32:33]\n" \
  "v_pk_add_f32 v[30:31], v[30:31], v[2:3]\n" \
  "v_pk_fma_f32 v[32:33], v[2:3], v[2:3], v[32:33]\n" \
  "v_pk_add_f32 v[30:31], v[30:31], v[4:5]\n" \
  "v_pk_fma_f32 v[32:33], v[4:5], v[4:5], v[32:33]\n" \
  "v_pk_add_f32 v[30:31], v[30:31], v[48:49]\n" \
  "v_pk_fma_f32 v[32:33], v[48:49], v[48:49], v[32:33]\n" \
  "v_pk_add_f32 v[30:31], v[30:31], v[114:115]\n" \
  "v_pk_fma_f32 v[32:33], v[114:115], v[114:115], v[32:33]\n" \
  "v_pk_add_f32 v[30:31], v[30:31], v[6:7]\n" \
  "v_pk_fma_f32 v[32:33], v[6:7], v[6:7], v[32:33]\n" \
  "v_add_f32 v254, v30, v31\n" \
  "v_add_f32 v251, v32, v33\n" \
  "s_waitcnt vmcnt(7)\n" \
  "ds_read_b64_tr_b16 a[240:241], v18 offset:16384\n" \
  "ds_read_b64_tr_b16 a[244:245], v19 offset:16384\n" \
  "ds_read_b64_tr_b16 a[248:249], v20 offset:16384\n" \
  "s_add_u32 m0, s57, 4096\n" \
  "s_add_u32 s31, s12, 9658368\n" \
  "buffer_load_dwordx4 v22, s[4:7], s31 offen offset:0 lds\n" \
  "v_permlane16_swap_b32 v254, v251\n" \
  "v_add_f32 v254, v254, v251\n" \
  "ds_read_b64_tr_b16 a[252:253], v21 offset:16384\n" \
  "ds_read_b64_tr_b16 v[196:197], v18 offset:18432\n" \
  "ds_read_b64_tr_b16 v[200:201], v19 offset:18432\n" \
  "buffer_load_dwordx4 v23, s[4:7], s31 offen offset:1024 lds\n" \
  "v_add_f32_dpp v254, v254, v254 quad_perm:[1,0,3,2] row_mask:0xf bank_mask:0xf\n" \
  "ds_read_b64_tr_b16 v[204:205], v20 offset:18432\n" \
  "ds_read_b64_tr_b16 v[208:209], v21 offset:18432\n" \
  "ds_read_b64_tr_b16 a[242:243], v18 offset:20480\n" \
  "buffer_load_dwordx4 v24, s[4:7], s31 offen offset:2048 lds\n" \
  "v_add_f32_dpp v254, v254, v254 quad_perm:[2,3,0,1] row_mask:0xf bank_mask:0xf\n" \
  "ds_read_b64_tr_b16 a[246:247], v19 offset:20480\n" \
  "ds_read_b64_tr_b16 a[250:251], v20 offset:20480\n" \
  "ds_read_b64_tr_b16 a[254:255], v21 offset:20480\n" \
  "buffer_load_dwordx4 v25, s[4:7], s31 offen offset:3072 lds\n" \
  "v_add_f32_dpp v254, v254, v254 row_half_mirror row_mask:0xf bank_mask:0xf\n" \
  "ds_read_b64_tr_b16 v[198:199], v18 offset:22528\n" \
  "ds_read_b64_tr_b16 v[202:203], v19 offset:22528\n" \
  "ds_read_b64_tr_b16 v[206:207], v20 offset:22528\n" \
  "ds_read_b64_tr_b16 v[210:211], v21 offset:22528\n" \
  "s_add_u32 m0, s57, 8192\n" \
  "s_add_u32 s31, s12, 516096\n" \
  "s_waitcnt lgkmcnt(15)\n" \
  "buffer_load_dwordx4 v22, s[4:7], s31 offen offset:0 lds\n" \
  "v_add_f32_dpp v254, v254, v254 row_mirror row_mask:0xf bank_mask:0xf\n" \
  "v_mov_b32 v251, v254\n" \
  "buffer_load_dwordx4 v23, s[4:7], s31 offen offset:1024 lds\n" \
  "buffer_load_dwordx4 v24, s[4:7], s31 offen offset:2048 lds\n" \
  "v_permlane16_swap_b32 v254, v251\n" \
  "v_mul_f32 v254, s35, v254\n" \
  "v_fma_f32 v251, v251, s35, v29\n" \
  "v_fma_f32 v251, -v254, v254, v251\n" \
  "v_max_f32 v251, v251, v29\n" \
  "v_rsq_f32 v28, v251\n" \
  "buffer_load_dwordx4 v25, s[4:7], s31 offen offset:3072 lds\n" \
  "v_mul_f32_e64 v29, -v254, v28\n" \
  "v_pk_fma_f32 v[10:11], v[10:11], v[28:29], v[28:29] op_sel:[0,0,1] op_sel_hi:[1,0,1]\n" \
  "v_pk_fma_f32 v[12:13], v[12:13], v[28:29], v[28:29] op_sel:[0,0,1] op_sel_hi:[1,0,1]\n" \
  "v_pk_fma_f32 v[14:15], v[14:15], v[28:29], v[28:29] op_sel:[0,0,1] op_sel_hi:[1,0,1]\n" \
  "v_pk_fma_f32 v[26:27], v[26:27], v[28:29], v[28:29] op_sel:[0,0,1] op_sel_hi:[1,0,1]\n" \
  "v_pk_fma_f32 v[2:3], v[2:3], v[28:29], v[28:29] op_sel:[0,0,1] op_sel_hi:[1,0,1]\n" \
  "v_pk_fma_f32 v[4:5], v[4:5], v[28:29], v[28:29] op_sel:[0,0,1] op_sel_hi:[1,0,1]\n" \
  "v_pk_fma_f32 v[48:49], v[48:49], v[28:29], v[28:29] op_sel:[0,0,1] op_sel_hi:[1,0,1]\n" \
  "v_pk_fma_f32 v[114:115], v[114:115], v[28:29], v[28:29] op_sel:[0,0,1] op_sel_hi:[1,0,1]\n" \
  "v_pk_fma_f32 v[6:7], v[6:7], v[28:29], v[28:29] op_sel:[0,0,1] op_sel_hi:[1,0,1]\n" \
  "v_cvt_pk_bf16_f32 v28, v10, v11\n" \
  "v_cvt_pk_bf16_f32 v29, v12, v13\n" \
  "v_cvt_pk_bf16_f32 v30, v14, v15\n" \
  "v_cvt_pk_bf16_f32 v31, v26, v27\n" \
  "ds_write_b128 v255, v[28:31] offset:0\n" \
  "v_cvt_pk_bf16_f32 v10, v2, v3\n" \
  "v_cvt_pk_bf16_f32 v11, v4, v5\n" \
  "v_cvt_pk_bf16_f32 v12, v48, v49\n" \
  "v_cvt_pk_bf16_f32 v13, v114, v115\n" \
  "ds_write_b128 v255, v[10:13] offset:512\n" \
  "v_cvt_pk_bf16_f32 v255, v6, v7\n" \
  "ds_write_b32 v249, v255 offset:1024\n" \
  "s_add_u32 m0, s57, 12288\n" \
  "s_add_u32 s31, s12, 9953280\n" \
  "buffer_load_dwordx4 v22, s[4:7], s31 offen offset:0 lds\n" \
  "buffer_load_dwordx4 v23, s[4:7], s31 offen offset:1024 lds\n" \
  "s_waitcnt lgkmcnt(0)\n" \
  "s_barrier\n" \
  "ds_read_b64_tr_b16 v[2:3], v253 offset:9216\n" \
  "ds_read_b64_tr_b16 v[4:5], v253 offset:11664\n" \
  "ds_read_b64_tr_b16 v[10:11], v253 offset:9728\n" \
  "ds_read_b64_tr_b16 v[12:13], v253 offset:12176\n" \
  "ds_read_b64_tr_b16 v[26:27], v253 offset:10240\n" \
  "ds_read_b64_tr_b16 v[28:29], v253 offset:12688\n" \
  "buffer_load_dwordx4 v24, s[4:7], s31 offen offset:2048 lds\n" \
  "buffer_load_dwordx4 v25, s[4:7], s31 offen offset:3072 lds\n" \
  "s_add_u32 s32, s11, 1048576\n" \
  "s_add_u32 s33, s11, 2097152\n" \
  "s_waitcnt lgkmcnt(4)\n" \
  "buffer_store_dwordx4 v[2:5], v241, s[36:39], s11 offen offset:0 sc1\n" \
  "s_waitcnt lgkmcnt(2)\n" \
  "buffer_store_dwordx4 v[10:13], v241, s[36:39], s32 offen offset:0 sc1\n" \
  "s_mov_b64 exec, 65535\n" \
  "s_waitcnt lgkmcnt(0)\n" \
  "buffer_store_dwordx4 v[26:29], v241, s[36:39], s33 offen offset:0 sc1\n" \
  "s_mov_b64 exec, -1\n" \
  ".Lscc_%=:\n" \
  "v_xor_b32 v255, s18, v8\n" \
  "v_bitop3_b32 v255, v9, s19, v255 bitop3:0xbe\n" \
  "v_cmp_ne_u32 vcc, 0, v255\n" \
  "s_cbranch_vccnz .Lscs_%=\n" \
  ".Lscd_%=:\n" \
  "s_add_u32 m0, s57, 16384\n" \
  "s_add_u32 s31, s14, 0\n" \
  "buffer_load_dwordx4 v252, s[36:39], s31 offen offset:0 sc1 lds\n" \
  "buffer_load_dwordx4 v252, s[36:39], s31 offen offset:1024 sc1 lds\n" \
  "buffer_load_dwordx4 v252, s[36:39], s31 offen offset:2048 sc1 lds\n" \
  "buffer_load_dwordx4 v252, s[36:39], s31 offen offset:3072 sc1 lds\n" \
  "s_add_u32 m0, s57, 20480\n" \
  "s_add_u32 s31, s14, 8192\n" \
  "buffer_load_dwordx4 v252, s[36:39], s31 offen offset:0 sc1 lds\n" \
  "buffer_load_dwordx4 v252, s[36:39], s31 offen offset:1024 sc1 lds\n" \
  "buffer_load_dwordx4 v252, s[36:39], s31 offen offset:2048 sc1 lds\n" \
  "buffer_load_dwordx4 v252, s[36:39], s31 offen offset:3072 sc1 lds\n" \
  "s_lshr_b32 s0, s2, 6\n" \
  "s_lshr_b32 s1, s2, 1\n" \
  "s_and_b32 s34, s1, 3\n" \
  "s_lshr_b32 s1, s2, 3\n" \
  "s_and_b32 s84, s1, 1\n" \
  "s_and_b32 s1, s2, 7\n" \
  "s_lshl_b32 s1, s1, 2\n" \
  "s_lshr_b32 s100, s2, 4\n" \
  "s_and_b32 s100, s100, 3\n" \
  "s_add_u32 s88, s1, s100\n" \
  "s_and_b32 s1, s2, 1\n" \
  "s_lshl_b32 s1, s1, 3\n" \
  "s_lshr_b32 s100, s2, 3\n" \
  "s_and_b32 s100, s100, 7\n" \
  "s_add_u32 s85, s1, s100\n" \
  "s_mul_i32 s1, s0, 4505600\n" \
  "s_add_u32 s52, s1, 12123648\n" \
  "s_add_u32 s53, s52, 3276800\n" \
  "s_add_u32 s83, s52, 2048000\n" \
  "s_lshl_b32 s1, s0, 5\n" \
  "s_add_u32 s1, s1, s88\n" \
  "s_lshl_b32 s1, s1, 1\n" \
  "s_add_u32 s1, s1, s84\n" \
  "s_lshl_b32 s1, s1, 7\n" \
  "s_lshl_b32 s100, s20, 5\n" \
  "s_add_u32 s1, s1, s100\n" \
  "s_add_u32 s54, s1, 262144\n" \
  "s_lshl_b32 s1, s20, 5\n" \
  "s_sub_u32 s1, s54, s1\n" \
  "s_lshl_b32 s100, s34, 11\n" \
  "s_sub_u32 s1, s1, s100\n" \
  "s_add_u32 s16, s1, 65536\n" \
  "s_mov_b32 s55, 3184710631\n" \
  "s_mul_i32 s56, s20, 9568\n" \
  "s_lshl_b32 s58, s20, 13\n" \
  "s_lshr_b32 s1, s88, 1\n" \
  "s_mul_i32 s90, s1, 18\n" \
  "s_and_b32 s1, s88, 1\n" \
  "s_lshl_b32 s1, s1, 3\n" \
  "s_add_u32 s1, s90, s1\n" \
  "s_lshl_b32 s100, s84, 2\n" \
  "s_add_u32 s1, s1, s100\n" \
  "s_add_u32 s1, s1, s20\n" \
  "s_add_u32 s1, s1, 1\n" \
  "s_lshl_b32 s63, s1, 9\n" \
  "s_and_b32 s1, s88, 1\n" \
  "s_add_u32 s1, s90, s1\n" \
  "s_add_u32 s1, s1, 17\n" \
  "s_lshl_b32 s1, s1, 9\n" \
  "s_lshl_b32 s100, s20, 7\n" \
  "s_add_u32 s64, s1, s100\n" \
  "s_lshl_b32 s1, s0, 5\n" \
  "s_lshl_b32 s100, s34, 3\n" \
  "s_add_u32 s1, s1, s100\n" \
  "s_lshl_b32 s100, s20, 1\n" \
  "s_add_u32 s1, s1, s100\n" \
  "s_lshl_b32 s1, s1, 8\n" \
  "s_add_u32 s1, s1, 262144\n" \
  "s_sub_u32 s59, s1, s63\n" \
  "s_add_u32 s75, s59, 196608\n" \
  "s_lshl_b32 s1, s85, 7\n" \
  "s_mul_i32 s1, s1, 576\n" \
  "s_mul_i32 s100, s0, 144\n" \
  "s_add_u32 s90, s1, s100\n" \
  "s_mul_i32 s1, s20, 9216\n" \
  "s_add_u32 s1, s90, s1\n" \
  "s_lshl_b32 s100, s34, 5\n" \
  "s_add_u32 s1, s1, s100\n" \
  "s_lshl_b32 s61, s1, 1\n" \
  "s_lshl_b32 s1, s34, 4\n" \
  "s_add_u32 s1, s1, s20\n" \
  "s_mul_i32 s1, s1, 576\n" \
  "s_add_u32 s1, s90, s1\n" \
  "s_add_u32 s1, s1, 128\n" \
  "s_lshl_b32 s62, s1, 1\n" \
  "v_and_b32 v255, 768, v252\n" \
  "v_lshlrev_b32 v255, 2, v255\n" \
  "v_and_b32 v254, 192, v252\n" \
  "v_lshl_or_b32 v255, v254, 1, v255\n" \
  "v_bfe_u32 v254, v191, 3, 2\n" \
  "v_lshl_or_b32 v255, v254, 5, v255\n" \
  "v_and_b32 v253, 3, v191\n" \
  "v_lshl_or_b32 v255, v253, 3, v255\n" \
  "v_add_u32 v8, s57, v255\n" \
  "v_mov_b32 v63, v8\n" \
  "v_lshl_or_b32 v9, v243, 16, v244\n" \
  "v_and_b32 v255, 12, v191\n" \
  "v_lshrrev_b32 v254, 4, v191\n" \
  "v_add_u32 v254, v254, v255\n" \
  "v_mul_u32_u24 v254, 1152, v254\n" \
  "v_lshl_add_u32 v254, v253, 3, v254\n" \
  "v_add_u32 v10, s61, v254\n" \
  "v_and_b32 v253, 1, v191\n" \
  "v_lshl_add_u32 v255, v253, 1, v255\n" \
  "v_bfe_u32 v254, v191, 1, 1\n" \
  "v_lshlrev_b32 v254, 6, v254\n" \
  "v_lshrrev_b32 v11, 2, v191\n" \
  "v_and_or_b32 v254, v11, 12, v254\n" \
  "v_mul_u32_u24 v254, 1152, v254\n" \
  "v_lshl_add_u32 v254, v255, 1, v254\n" \
  "v_add_u32 v11, s62, v254\n" \
  "v_lshrrev_b32 v255, 3, v252\n" \
  "v_bitop3_b32 v255, v255, 32, v252 bitop3:0x6a\n" \
  "v_lshl_or_b32 v255, v255, 5, v255\n" \
  "v_and_b32 v12, 28784, v255\n" \
  "v_xor_b32 v255, 64, v12\n" \
  "v_add_u32 v13, 31744, v255\n" \
  "v_lshlrev_b32 v14, 16, v242\n" \
  "v_add_u32 v255, s57, v252\n" \
  "v_add_u32 v22, 28672, v255\n" \
  "v_lshlrev_b32 v52, 16, v16\n" \
  "v_and_b32 v53, 4294901760, v16\n" \
  "v_lshlrev_b32 v54, 16, v17\n" \
  "v_and_b32 v55, 4294901760, v17\n" \
  "v_lshlrev_b32 v56, 16, v247\n" \
  "v_mov_b32 v57, 3222493025\n" \
  "s_mul_i32 s32, s20, 1056\n" \
  "v_bfe_u32 v255, v191, 1, 1\n" \
  "v_mul_u32_u24 v255, 544, v255\n" \
  "v_lshl_add_u32 v255, v253, 4, v255\n" \
  "v_bfe_u32 v254, v191, 2, 1\n" \
  "v_lshl_add_u32 v255, v254, 8, v255\n" \
  "v_bfe_u32 v254, v191, 3, 2\n" \
  "v_lshl_add_u32 v255, v254, 6, v255\n" \
  "v_and_b32 v254, 32, v191\n" \
  "v_add3_u32 v58, v255, v254, s32\n" \
  "v_add_u32 v59, 524288, v252\n" \
  "v_bfe_u32 v255, v191, 3, 2\n" \
  "v_mul_u32_u24 v255, 288, v255\n" \
  "v_lshl_add_u32 v255, v254, 1, v255\n" \
  "v_and_b32 v254, 7, v191\n" \
  "v_lshl_add_u32 v255, v254, 2, v255\n" \
  "s_and_b32 s32, s20, 1\n" \
  "s_lshl_b32 s32, s32, 7\n" \
  "s_lshr_b32 s33, s20, 1\n" \
  "s_lshl_b32 s33, s33, 5\n" \
  "s_add_u32 s32, s32, s33\n" \
  "s_add_u32 s32, s32, 4224\n" \
  "v_add_u32 v60, s32, v255\n" \
  "v_lshl_add_u32 v61, v191, 3, s63\n" \
  "v_lshl_add_u32 v62, v191, 1, s64\n" \
  "s_mul_i32 s1, s34, 1152\n" \
  "s_lshr_b32 s100, s20, 1\n" \
  "s_mul_i32 s100, s100, 576\n" \
  "s_add_u32 s1, s1, s100\n" \
  "s_lshl_b32 s1, s1, 12\n" \
  "s_lshl_b32 s100, s85, 8\n" \
  "s_add_u32 s65, s1, s100\n" \
  "s_and_b32 s1, s20, 1\n" \
  "s_lshl_b32 s1, s1, 20\n" \
  "s_add_u32 s1, s65, s1\n" \
  "s_add_u32 s60, s1, 655488\n" \
  "s_and_b32 s1, s20, 1\n" \
  "s_lshl_b32 s1, s1, 17\n" \
  "s_add_u32 s1, s65, s1\n" \
  "s_add_u32 s65, s1, 2097152\n" \
  "s_add_u32 s66, s65, 128\n" \
  "s_add_u32 s61, s60, 130944\n" \
  "s_add_u32 s62, s60, 131072\n" \
  "s_add_u32 s63, s60, 262016\n" \
  "s_add_u32 s64, s60, 262144\n" \
  "s_lshl_b32 s1, s34, 7\n" \
  "s_add_u32 s67, s1, 163200\n" \
  "s_lshl_b32 s1, s34, 2\n" \
  "s_add_u32 s98, s1, s20\n" \
  "s_mul_i32 s1, s98, 9216\n" \
  "s_add_u32 s1, s1, s52\n" \
  "s_add_u32 s68, s1, 819712\n" \
  "s_add_u32 s69, s68, 3072\n" \
  "s_add_u32 s70, s68, 6144\n" \
  "s_lshl_b32 s1, s85, 14\n" \
  "s_add_u32 s1, s1, s52\n" \
  "s_add_u32 s90, s1, 967168\n" \
  "s_lshl_b32 s1, s98, 8\n" \
  "s_add_u32 s79, s1, s90\n" \
  "s_add_u32 s80, s79, 4096\n" \
  "s_add_u32 s81, s79, 8192\n" \
  "s_add_u32 s82, s79, 12288\n" \
  "s_lshl_b32 s99, s20, 10\n" \
  "s_add_u32 s87, s99, 65536\n" \
  "s_add_u32 s90, s90, s99\n" \
  "s_lshl_b32 s1, s2, 2\n" \
  "s_add_u32 s1, s1, s20\n" \
  "s_lshl_b32 s1, s1, 8\n" \
  "s_add_u32 s91, s1, 69206016\n" \
  "s_mul_i32 s1, s0, 589824\n" \
  "s_lshl_b32 s100, s20, 14\n" \
  "s_add_u32 s1, s1, s100\n" \
  "s_add_u32 s48, s1, 9764864\n" \
  "s_sub_u32 s1, s34, 1\n" \
  "s_and_b32 s1, s1, 3\n" \
  "s_lshl_b32 s1, s1, 12\n" \
  "s_add_u32 s98, s1, s90\n" \
  "s_add_u32 s71, s98, 4292919296\n" \
  "s_add_u32 s76, s98, 4293328896\n" \
  "s_sub_u32 s1, s34, 2\n" \
  "s_and_b32 s1, s1, 3\n" \
  "s_lshl_b32 s1, s1, 12\n" \
  "s_add_u32 s98, s1, s90\n" \
  "s_add_u32 s72, s98, 4293328896\n" \
  "s_add_u32 s77, s98, 4293328896\n" \
  "s_sub_u32 s1, s34, 3\n" \
  "s_and_b32 s1, s1, 3\n" \
  "s_lshl_b32 s1, s1, 12\n" \
  "s_add_u32 s98, s1, s90\n" \
  "s_add_u32 s73, s98, 4293328896\n" \
  "s_add_u32 s78, s98, 4293738496\n" \
  "s_and_b32 s1, s34, 3\n" \
  "s_lshl_b32 s1, s1, 12\n" \
  "s_add_u32 s98, s1, s90\n" \
  "s_add_u32 s74, s98, 4294148096\n" \
  "s_add_u32 s49, s48, 8192\n" \
  "s_add_u32 s50, s48, 4096\n" \
  "s_add_u32 s51, s48, 12288\n" \
  "s_lshr_b32 s1, s2, 3\n" \
  "s_lshl_b32 s1, s1, 2\n" \
  "s_add_u32 s98, s1, s20\n" \
  "s_and_b32 s1, s98, 31\n" \
  "s_lshl_b32 s1, s1, 2\n" \
  "s_add_u32 s90, s1, 448\n" \
  "s_and_b32 s1, s98, 8\n" \
  "s_lshl_b32 s1, s1, 5\n" \
  "s_and_b32 s100, s98, 7\n" \
  "s_lshl_b32 s100, s100, 2\n" \
  "s_add_u32 s1, s1, s100\n" \
  "s_add_u32 s89, s1, 160\n" \
  "s_and_b32 s1, s98, 15\n" \
  "s_lshl_b32 s1, s1, 2\n" \
  "s_add_u32 s86, s1, 192\n" \
  "s_bitcmp1_b32 s98, 4\n" \
  "s_cselect_b32 s89, s89, s86\n" \
  "s_bitcmp1_b32 s98, 5\n" \
  "s_cselect_b32 s89, s90, s89\n" \
  "s_lshr_b32 s1, s98, 6\n" \
  "s_mul_i32 s1, s1, 576\n" \
  "s_mul_i32 s100, s34, 1152\n" \
  "s_add_u32 s1, s1, s100\n" \
  "s_add_u32 s1, s1, s89\n" \
  "s_lshl_b32 s1, s1, 12\n" \
  "s_and_b32 s100, s2, 1\n" \
  "s_lshl_b32 s100, s100, 11\n" \
  "s_add_u32 s1, s1, s100\n" \
  "s_lshr_b32 s100, s2, 3\n" \
  "s_and_b32 s100, s100, 3\n" \
  "s_add_u32 s1, s1, s100\n" \
  "s_add_u32 s89, s1, 1\n" \
  "s_sub_u32 s98, s57, s99\n" \
  "s_lshl_b32 s1, s34, 17\n" \
  "s_add_u32 s85, s1, 65536\n" \
  "s_mov_b32 s90, 7405568\n" \
  "s_add_u32 s1, s0, 3\n" \
  "s_and_b32 s1, s1, 3\n" \
  "s_lshl_b32 s88, s1, 12\n" \
  "s_mov_b32 s30, s16\n" \
  "v_mov_b32 v2, v12\n" \
  "v_mov_b32 v3, v13\n" \
  "v_add_u32 v4, 63488, v12\n" \
  "v_add_u32 v5, 63488, v13\n" \
  "v_mov_b32 v190, 1\n" \
  "v_lshrrev_b32 v189, 1, v252\n" \
  "v_add_u32 v255, s57, v189\n" \
  "s_lshl_b32 s32, s20, 11\n" \
  "v_add_u32 v189, s32, v189\n" \
  "s_waitcnt vmcnt(19)\n" \
  "ds_read_b64_tr_b16 a[200:201], v18 offset:0\n" \
  "ds_read_b64_tr_b16 a[204:205], v19 offset:0\n" \
  "ds_read_b64_tr_b16 a[208:209], v20 offset:0\n" \
  "ds_read_b64_tr_b16 a[212:213], v21 offset:0\n" \
  "ds_read_b64_tr_b16 a[220:221], v18 offset:2048\n" \
  "ds_read_b64_tr_b16 a[224:225], v19 offset:2048\n" \
  "ds_read_b64_tr_b16 a[228:229], v20 offset:2048\n" \
  "ds_read_b64_tr_b16 a[232:233], v21 offset:2048\n" \
  "ds_read_b64_tr_b16 a[202:203], v18 offset:4096\n" \
  "ds_read_b64_tr_b16 a[206:207], v19 offset:4096\n" \
  "ds_read_b64_tr_b16 a[210:211], v20 offset:4096\n" \
  "ds_read_b64_tr_b16 a[214:215], v21 offset:4096\n" \
  "ds_read_b64_tr_b16 a[222:223], v18 offset:6144\n" \
  "ds_read_b64_tr_b16 a[226:227], v19 offset:6144\n" \
  "ds_read_b64_tr_b16 a[230:231], v20 offset:6144\n" \
  "ds_read_b64_tr_b16 a[234:235], v21 offset:6144\n" \
  "s_waitcnt vmcnt(11)\n" \
  "ds_read_b64_tr_b16 v[216:217], v18 offset:8192\n" \
  "ds_read_b64_tr_b16 v[220:221], v19 offset:8192\n" \
  "ds_read_b64_tr_b16 v[224:225], v20 offset:8192\n" \
  "ds_read_b64_tr_b16 v[228:229], v21 offset:8192\n" \
  "ds_read_b64_tr_b16 v[236:237], v18 offset:10240\n" \
  "ds_read_b64_tr_b16 v[240:241], v19 offset:10240\n" \
  "ds_read_b64_tr_b16 v[244:245], v20 offset:10240\n" \
  "ds_read_b64_tr_b16 v[248:249], v21 offset:10240\n" \
  "ds_read_b64_tr_b16 v[218:219], v18 offset:12288\n" \
  "ds_read_b64_tr_b16 v[222:223], v19 offset:12288\n" \
  "ds_read_b64_tr_b16 v[226:227], v20 offset:12288\n" \
  "ds_read_b64_tr_b16 v[230:231], v21 offset:12288\n" \
  "ds_read_b64_tr_b16 v[238:239], v18 offset:14336\n" \
  "ds_read_b64_tr_b16 v[242:243], v19 offset:14336\n" \
  "ds_read_b64_tr_b16 v[246:247], v20 offset:14336\n" \
  "ds_read_b64_tr_b16 v[250:251], v21 offset:14336\n" \
  "s_waitcnt vmcnt(8)\n" \
  "s_mov_b64 exec, 1\n" \
  "buffer_store_dword v190, off, s[36:39], s28 sc1\n" \
  "s_mov_b64 exec, -1\n" \
  "s_add_u32 m0, s57, 24576\n" \
  "s_add_u32 s31, s14, 4096\n" \
  "buffer_load_dwordx4 v252, s[36:39], s31 offen offset:0 sc1 lds\n" \
  "buffer_load_dwordx4 v252, s[36:39], s31 offen offset:1024 sc1 lds\n" \
  "buffer_load_dwordx4 v252, s[36:39], s31 offen offset:2048 sc1 lds\n" \
  "buffer_load_dwordx4 v252, s[36:39], s31 offen offset:3072 sc1 lds\n" \
  "s_add_u32 m0, s57, 28672\n" \
  "s_add_u32 s31, s14, 12288\n" \
  "buffer_load_dwordx4 v252, s[36:39], s31 offen offset:0 sc1 lds\n" \
  "buffer_load_dwordx4 v252, s[36:39], s31 offen offset:1024 sc1 lds\n" \
  "s_lshl_b32 m0, s20, 11\n" \
  "s_add_u32 s31, s31, 2048\n" \
  "buffer_load_dwordx4 v252, s[36:39], s31 offen offset:0 sc1 lds\n" \
  "buffer_load_dwordx4 v252, s[36:39], s31 offen offset:1024 sc1 lds\n" \
  "s_bfe_u32 s32, s27, 131076\n" \
  "s_sub_u32 s100, s60, 655488\n" \
  "s_sub_u32 s101, s60, 720896\n" \
  "s_lshl_b32 s32, s32, 12\n" \
  "s_add_u32 s32, s32, 0\n" \
  "s_cmp_ge_u32 s32, 16384\n" \
  "s_cselect_b32 s32, 0, s32\n" \
  "s_bitcmp1_b32 s32, 12\n" \
  "s_cselect_b32 s33, s101, s100\n" \
  "s_lshl4_add_u32 s31, s32, s33\n" \
  "s_add_u32 s33, s32, s57\n" \
  "s_add_u32 m0, s33, 0\n" \
  "s_waitcnt lgkmcnt(0)\n" \
  "buffer_load_dwordx4 v2, s[40:43], s31 offen offset:0 lds\n" \
  "buffer_load_dwordx4 v3, s[40:43], s31 offen offset:1024 lds\n" \
  "buffer_load_dwordx4 v4, s[40:43], s31 offen offset:2048 lds\n" \
  "buffer_load_dwordx4 v5, s[40:43], s31 offen offset:3072 lds\n" \
  "s_add_u32 s32, s32, 4096\n" \
  "s_cmp_ge_u32 s32, 16384\n" \
  "s_cselect_b32 s32, 0, s32\n" \
  "s_bitcmp1_b32 s32, 12\n" \
  "s_cselect_b32 s33, s101, s100\n" \
  "s_lshl4_add_u32 s31, s32, s33\n" \
  "s_add_u32 s33, s32, s57\n" \
  "s_add_u32 m0, s33, 0\n" \
  "s_nop 0\n" \
  "buffer_load_dwordx4 v2, s[40:43], s31 offen offset:0 lds\n" \
  "buffer_load_dwordx4 v3, s[40:43], s31 offen offset:1024 lds\n" \
  "buffer_load_dwordx4 v4, s[40:43], s31 offen offset:2048 lds\n" \
  "buffer_load_dwordx4 v5, s[40:43], s31 offen offset:3072 lds\n" \
  "s_add_u32 s32, s32, 4096\n" \
  "s_cmp_ge_u32 s32, 16384\n" \
  "s_cselect_b32 s32, 0, s32\n" \
  "s_bitcmp1_b32 s32, 12\n" \
  "s_cselect_b32 s33, s101, s100\n" \
  "s_lshl4_add_u32 s31, s32, s33\n" \
  "s_add_u32 s33, s32, s57\n" \
  "s_add_u32 m0, s33, 0\n" \
  "s_nop 0\n" \
  "buffer_load_dwordx4 v2, s[40:43], s31 offen offset:0 lds\n" \
  "buffer_load_dwordx4 v3, s[40:43], s31 offen offset:1024 lds\n" \
  "buffer_load_dwordx4 v4, s[40:43], s31 offen offset:2048 lds\n" \
  "buffer_load_dwordx4 v5, s[40:43], s31 offen offset:3072 lds\n" \
  "s_add_u32 s32, s32, 4096\n" \
  "s_cmp_ge_u32 s32, 16384\n" \
  "s_cselect_b32 s32, 0, s32\n" \
  "s_bitcmp1_b32 s32, 12\n" \
  "s_cselect_b32 s33, s101, s100\n" \
  "s_lshl4_add_u32 s31, s32, s33\n" \
  "s_add_u32 s33, s32, s57\n" \
  "s_add_u32 m0, s33, 0\n" \
  "s_nop 0\n" \
  "buffer_load_dwordx4 v2, s[40:43], s31 offen offset:0 lds\n" \
  "buffer_load_dwordx4 v3, s[40:43], s31 offen offset:1024 lds\n" \
  "buffer_load_dwordx4 v4, s[40:43], s31 offen offset:2048 lds\n" \
  "buffer_load_dwordx4 v5, s[40:43], s31 offen offset:3072 lds\n" \
  "s_waitcnt vmcnt(16)\n" \
  "ds_read_b64_tr_b16 a[16:17], v255 offset:16384\n" \
  "ds_read_b64_tr_b16 a[36:37], v255 offset:16896\n" \
  "ds_read_b64_tr_b16 a[18:19], v255 offset:17408\n" \
  "ds_read_b64_tr_b16 a[38:39], v255 offset:17920\n" \
  "ds_read_b64_tr_b16 a[56:57], v255 offset:18432\n" \
  "ds_read_b64_tr_b16 a[76:77], v255 offset:18944\n" \
  "ds_read_b64_tr_b16 a[58:59], v255 offset:19456\n" \
  "ds_read_b64_tr_b16 a[78:79], v255 offset:19968\n" \
  "ds_read_b64_tr_b16 a[96:97], v255 offset:20480\n" \
  "ds_read_b64_tr_b16 a[116:117], v255 offset:20992\n" \
  "ds_read_b64_tr_b16 a[98:99], v255 offset:21504\n" \
  "ds_read_b64_tr_b16 a[118:119], v255 offset:22016\n" \
  "ds_read_b64_tr_b16 a[136:137], v255 offset:22528\n" \
  "ds_read_b64_tr_b16 a[156:157], v255 offset:23040\n" \
  "ds_read_b64_tr_b16 a[138:139], v255 offset:23552\n" \
  "ds_read_b64_tr_b16 a[158:159], v255 offset:24064\n" \
  "ds_read_b64_tr_b16 a[176:177], v255 offset:24576\n" \
  "ds_read_b64_tr_b16 a[196:197], v255 offset:25088\n" \
  "ds_read_b64_tr_b16 a[178:179], v255 offset:25600\n" \
  "ds_read_b64_tr_b16 a[198:199], v255 offset:26112\n" \
  "ds_read_b64_tr_b16 a[216:217], v255 offset:26624\n" \
  "ds_read_b64_tr_b16 a[236:237], v255 offset:27136\n" \
  "ds_read_b64_tr_b16 a[218:219], v255 offset:27648\n" \
  "ds_read_b64_tr_b16 a[238:239], v255 offset:28160\n" \
  "ds_read_b64_tr_b16 v[192:193], v255 offset:28672\n" \
  "ds_read_b64_tr_b16 v[212:213], v255 offset:29184\n" \
  "ds_read_b64_tr_b16 v[194:195], v255 offset:29696\n" \
  "ds_read_b64_tr_b16 v[214:215], v255 offset:30208\n" \
  "ds_read_b64_tr_b16 v[232:233], v189 offset:0\n" \
  "ds_read_b64_tr_b16 v[252:253], v189 offset:512\n" \
  "ds_read_b64_tr_b16 v[234:235], v189 offset:1024\n" \
  "ds_read_b64_tr_b16 v[254:255], v189 offset:1536\n" \
  "v_and_b32 v189, 12, v191\n" \
  "v_and_b32 v190, 1, v191\n" \
  "v_lshl_add_u32 v189, v190, 1, v189\n" \
  "v_lshlrev_b32 v189, 2, v189\n" \
  "v_add_u32 v189, 163712, v189\n" \
  "ds_read_b64 v[6:7], v189 offset:0\n" \
  "s_bfe_u32 s32, s27, 131076\n" \
  "s_sub_u32 s100, s60, 131200\n" \
  "s_sub_u32 s101, s60, 196608\n" \
  "s_lshl_b32 s32, s32, 12\n" \
  "s_add_u32 s32, s32, 0\n" \
  "s_cmp_ge_u32 s32, 12288\n" \
  "s_cselect_b32 s32, 0, s32\n" \
  "s_bitcmp1_b32 s32, 12\n" \
  "s_cselect_b32 s33, s101, s100\n" \
  "s_lshl4_add_u32 s31, s32, s33\n" \
  "s_add_u32 s33, s32, s57\n" \
  "s_add_u32 m0, s33, 16384\n" \
  "s_waitcnt lgkmcnt(1)\n" \
  "buffer_load_dwordx4 v2, s[40:43], s31 offen offset:0 lds\n" \
  "buffer_load_dwordx4 v3, s[40:43], s31 offen offset:1024 lds\n" \
  "buffer_load_dwordx4 v4, s[40:43], s31 offen offset:2048 lds\n" \
  "buffer_load_dwordx4 v5, s[40:43], s31 offen offset:3072 lds\n" \
  "s_add_u32 s32, s32, 4096\n" \
  "s_cmp_ge_u32 s32, 12288\n" \
  "s_cselect_b32 s32, 0, s32\n" \
  "s_bitcmp1_b32 s32, 12\n" \
  "s_cselect_b32 s33, s101, s100\n" \
  "s_lshl4_add_u32 s31, s32, s33\n" \
  "s_add_u32 s33, s32, s57\n" \
  "s_add_u32 m0, s33, 16384\n" \
  "s_nop 0\n" \
  "buffer_load_dwordx4 v2, s[40:43], s31 offen offset:0 lds\n" \
  "buffer_load_dwordx4 v3, s[40:43], s31 offen offset:1024 lds\n" \
  "buffer_load_dwordx4 v4, s[40:43], s31 offen offset:2048 lds\n" \
  "buffer_load_dwordx4 v5, s[40:43], s31 offen offset:3072 lds\n" \
  "s_add_u32 s32, s32, 4096\n" \
  "s_cmp_ge_u32 s32, 12288\n" \
  "s_cselect_b32 s32, 0, s32\n" \
  "s_bitcmp1_b32 s32, 12\n" \
  "s_cselect_b32 s33, s101, s100\n" \
  "s_lshl4_add_u32 s31, s32, s33\n" \
  "s_add_u32 s33, s32, s57\n" \
  "s_add_u32 m0, s33, 16384\n" \
  "s_nop 0\n" \
  "buffer_load_dwordx4 v2, s[40:43], s31 offen offset:0 lds\n" \
  "buffer_load_dwordx4 v3, s[40:43], s31 offen offset:1024 lds\n" \
  "buffer_load_dwordx4 v4, s[40:43], s31 offen offset:2048 lds\n" \
  "buffer_load_dwordx4 v5, s[40:43], s31 offen offset:3072 lds\n" \
  "s_waitcnt lgkmcnt(0)\n" \
  "v_lshrrev_b32 v6, 16, v6\n" \
  "v_or_b32 v15, v6, v7\n" \
  ".Lgo_%=:\n"
#define ASM_END \
  ".Ladm_b:\n"
#define ASM_CLOB "v1", "v2", "v3", "v4", "v5", "v6", "v7", "v8", "v9", "v10", "v11", "v12", "v13", "v14", "v15", "v16", "v17", "v18", "v19", "v20", "v21", "v22", "v23", "v24", "v25", "v26", "v27", "v28", "v29", "v30", "v31", "v32", "v33", "v34", "v35", "v36", "v37", "v38", "v39", "v40", "v41", "v42", "v43", "v44", "v45", "v46", "v47", "v48", "v49", "v50", "v51", "v52", "v53", "v54", "v55", "v56", "v57", "v58", "v59", "v60", "v61", "v62", "v63", "v64", "v65", "v66", "v67", "v68", "v69", "v70", "v71", "v72", "v73", "v74", "v75", "v76", "v77", "v78", "v79", "v80", "v81", "v82", "v83", "v84", "v85", "v86", "v87", "v88", "v89", "v90", "v91", "v92", "v93", "v94", "v95", "v96", "v97", "v98", "v99", "v100", "v101", "v102", "v103", "v104", "v105", "v106", "v107", "v108", "v109", "v110", "v111", "v112", "v113", "v114", "v115", "v116", "v117", "v118", "v119", "v120", "v121", "v122", "v123", "v124", "v125", "v126", "v127", "v128", "v129", "v130", "v131", "v132", "v133", "v134", "v135", "v136", "v137", "v138", "v139", "v140", "v141", "v142", "v143", "v144", "v145", "v146", "v147", "v148", "v149", "v150", "v151", "v152", "v153", "v154", "v155", "v156", "v157", "v158", "v159", "v160", "v161", "v162", "v163", "v164", "v165", "v166", "v167", "v168", "v169", "v170", "v171", "v172", "v173", "v174", "v175", "v176", "v177", "v178", "v179", "v180", "v181", "v182", "v183", "v184", "v185", "v186", "v187", "v188", "v189", "v190", "v191", "v192", "v193", "v194", "v195", "v196", "v197", "v198", "v199", "v200", "v201", "v202", "v203", "v204", "v205", "v206", "v207", "v208", "v209", "v210", "v211", "v212", "v213", "v214", "v215", "v216", "v217", "v218", "v219", "v220", "v221", "v222", "v223", "v224", "v225", "v226", "v227", "v228", "v229", "v230", "v231", "v232", "v233", "v234", "v235", "v236", "v237", "v238", "v239", "v240", "v241", "v242", "v243", "v244", "v245", "v246", "v247", "v248", "v249", "v250", "v251", "v252", "v253", "v254", "v255", "a0", "a1", "a2", "a3", "a4", "a5", "a6", "a7", "a8", "a9", "a10", "a11", "a12", "a13", "a14", "a15", "a16", "a17", "a18", "a19", "a20", "a21", "a22", "a23", "a24", "a25", "a26", "a27", "a28", "a29", "a30", "a31", "a32", "a33", "a34", "a35", "a36", "a37", "a38", "a39", "a40", "a41", "a42", "a43", "a44", "a45", "a46", "a47", "a48", "a49", "a50", "a51", "a52", "a53", "a54", "a55", "a56", "a57", "a58", "a59", "a60", "a61", "a62", "a63", "a64", "a65", "a66", "a67", "a68", "a69", "a70", "a71", "a72", "a73", "a74", "a75", "a76", "a77", "a78", "a79", "a80", "a81", "a82", "a83", "a84", "a85", "a86", "a87", "a88", "a89", "a90", "a91", "a92", "a93", "a94", "a95", "a96", "a97", "a98", "a99", "a100", "a101", "a102", "a103", "a104", "a105", "a106", "a107", "a108", "a109", "a110", "a111", "a112", "a113", "a114", "a115", "a116", "a117", "a118", "a119", "a120", "a121", "a122", "a123", "a124", "a125", "a126", "a127", "a128", "a129", "a130", "a131", "a132", "a133", "a134", "a135", "a136", "a137", "a138", "a139", "a140", "a141", "a142", "a143", "a144", "a145", "a146", "a147", "a148", "a149", "a150", "a151", "a152", "a153", "a154", "a155", "a156", "a157", "a158", "a159", "a160", "a161", "a162", "a163", "a164", "a165", "a166", "a167", "a168", "a169", "a170", "a171", "a172", "a173", "a174", "a175", "a176", "a177", "a178", "a179", "a180", "a181", "a182", "a183", "a184", "a185", "a186", "a187", "a188", "a189", "a190", "a191", "a192", "a193", "a194", "a195", "a196", "a197", "a198", "a199", "a200", "a201", "a202", "a203", "a204", "a205", "a206", "a207", "a208", "a209", "a210", "a211", "a212", "a213", "a214", "a215", "a216", "a217", "a218", "a219", "a220", "a221", "a222", "a223", "a224", "a225", "a226", "a227", "a228", "a229", "a230", "a231", "a232", "a233", "a234", "a235", "a236", "a237", "a238", "a239", "a240", "a241", "a242", "a243", "a244", "a245", "a246", "a247", "a248", "a249", "a250", "a251", "a252", "a253", "a254", "a255", "s3", "s4", "s5", "s6", "s7", "s8", "s9", "s10", "s11", "s12", "s13", "s14", "s15", "s16", "s17", "s18", "s19", "s20", "s21", "s22", "s23", "s24", "s25", "s26", "s27", "s28", "s29", "s30", "s31", "s32", "s33", "s34", "s35", "s36", "s37", "s38", "s39", "s40", "s41", "s42", "s43", "s44", "s45", "s46", "s47", "s48", "s49", "s50", "s51", "s52", "s53", "s54", "s55", "s56", "s57", "s58", "s59", "s60", "s61", "s62", "s63", "s64", "s65", "s66", "s67", "s68", "s69", "s70", "s71", "s72", "s73", "s74", "s75", "s78", "s79", "s80", "s81", "s82", "s83", "s84", "s85", "s86", "s87", "s88", "s89", "s90", "s91", "s92", "s93", "s94", "s95", "s96", "s97", "s98", "s99", "s100", "s101", "vcc", "scc", "memory"
#define ASM_GEMM \
  "s_memrealtime s[94:95]\n" \
  "s_waitcnt vmcnt(12)\n" \
  "v_xor_b32 v17, 32, v63\n" \
  "v_xor_b32 v18, 64, v63\n" \
  "v_xor_b32 v19, 96, v63\n" \
  "ds_read_b64_tr_b16 v[128:129], v63 offset:0\n" \
  "ds_read_b64_tr_b16 v[130:131], v63 offset:512\n" \
  "ds_read_b64_tr_b16 v[132:133], v17 offset:0\n" \
  "ds_read_b64_tr_b16 v[134:135], v17 offset:512\n" \
  "ds_read_b64_tr_b16 v[136:137], v18 offset:0\n" \
  "ds_read_b64_tr_b16 v[138:139], v18 offset:512\n" \
  "ds_read_b64_tr_b16 v[140:141], v19 offset:0\n" \
  "ds_read_b64_tr_b16 v[142:143], v19 offset:512\n" \
  "ds_read_b64_tr_b16 v[144:145], v63 offset:4096\n" \
  "ds_read_b64_tr_b16 v[146:147], v63 offset:4608\n" \
  "ds_read_b64_tr_b16 v[148:149], v17 offset:4096\n" \
  "ds_read_b64_tr_b16 v[150:151], v17 offset:4608\n" \
  "ds_read_b64_tr_b16 v[152:153], v18 offset:4096\n" \
  "ds_read_b64_tr_b16 v[154:155], v18 offset:4608\n" \
  "ds_read_b64_tr_b16 v[156:157], v19 offset:4096\n" \
  "ds_read_b64_tr_b16 v[158:159], v19 offset:4608\n" \
  "ds_read_b64_tr_b16 v[160:161], v63 offset:8192\n" \
  "ds_read_b64_tr_b16 v[162:163], v63 offset:8704\n" \
  "ds_read_b64_tr_b16 v[164:165], v17 offset:8192\n" \
  "ds_read_b64_tr_b16 v[166:167], v17 offset:8704\n" \
  "ds_read_b64_tr_b16 v[168:169], v18 offset:8192\n" \
  "ds_read_b64_tr_b16 v[170:171], v18 offset:8704\n" \
  "ds_read_b64_tr_b16 v[172:173], v19 offset:8192\n" \
  "ds_read_b64_tr_b16 v[174:175], v19 offset:8704\n" \
  "ds_read_b64_tr_b16 v[176:177], v63 offset:12288\n" \
  "ds_read_b64_tr_b16 v[178:179], v63 offset:12800\n" \
  "ds_read_b64_tr_b16 v[180:181], v17 offset:12288\n" \
  "ds_read_b64_tr_b16 v[182:183], v17 offset:12800\n" \
  "ds_read_b64_tr_b16 v[184:185], v18 offset:12288\n" \
  "ds_read_b64_tr_b16 v[186:187], v18 offset:12800\n" \
  "ds_read_b64_tr_b16 v[188:189], v19 offset:12288\n" \
  "ds_read_b64_tr_b16 v[190:191], v19 offset:12800\n" \
  "buffer_load_dwordx4 v[64:67], v59, s[36:39], s48 offen offset:0 sc1\n" \
  "buffer_load_dwordx4 v[68:71], v59, s[36:39], s48 offen offset:1024 sc1\n" \
  "buffer_load_dwordx4 v[72:75], v59, s[36:39], s48 offen offset:2048 sc1\n" \
  "buffer_load_dwordx4 v[76:79], v59, s[36:39], s48 offen offset:3072 sc1\n" \
  "buffer_load_dwordx4 v[80:83], v59, s[36:39], s49 offen offset:0 sc1\n" \
  "buffer_load_dwordx4 v[84:87], v59, s[36:39], s49 offen offset:1024 sc1\n" \
  "buffer_load_dwordx4 v[88:91], v59, s[36:39], s49 offen offset:2048 sc1\n" \
  "buffer_load_dwordx4 v[92:95], v59, s[36:39], s49 offen offset:3072 sc1\n" \
  "buffer_load_dwordx4 v[96:99], v59, s[36:39], s50 offen offset:0 sc1\n" \
  "buffer_load_dwordx4 v[100:103], v59, s[36:39], s50 offen offset:1024 sc1\n" \
  "buffer_load_dwordx4 v[104:107], v59, s[36:39], s50 offen offset:2048 sc1\n" \
  "buffer_load_dwordx4 v[108:111], v59, s[36:39], s50 offen offset:3072 sc1\n" \
  "buffer_load_dwordx4 v[112:115], v59, s[36:39], s51 offen offset:0 sc1\n" \
  "buffer_load_dwordx4 v[116:119], v59, s[36:39], s51 offen offset:1024 sc1\n" \
  "buffer_load_dwordx4 v[120:123], v59, s[36:39], s51 offen offset:2048 sc1\n" \
  "buffer_load_dwordx4 v[124:127], v59, s[36:39], s51 offen offset:3072 sc1\n" \
  "v_and_b32 v0, 0x3ff, v59\n" \
  "v_bfrev_b32 v1, 1\n" \
  "buffer_load_dwordx4 v[4:7], v0, s[36:39], s87 offen sc1\n" \
  "s_branch .Lz0_%=\n" \
  ".Lzr_%=:\n" \
  "s_waitcnt lgkmcnt(0)\n" \
  "ds_write_b128 v22, v[8:11]\n" \
  "ds_write_b128 v22, v[12:15] offset:1024\n" \
  "s_mov_b32 s101, s89\n" \
  "s_waitcnt lgkmcnt(0)\n" \
  "s_barrier\n" \
  "s_sub_u32 s31, s60, 0x60080\n" \
  "s_sub_u32 s32, s60, 0x70000\n" \
  "v_add_u32 v14, 0xf800, v12\n" \
  "v_add_u32 v15, 0xf800, v13\n" \
  "s_add_u32 s88, s88, 4096\n" \
  "s_cmp_eq_u32 s88, 16384\n" \
  "s_cselect_b32 s88, 0, s88\n" \
  "s_bitcmp1_b32 s88, 12\n" \
  "s_cselect_b32 s89, s32, s31\n" \
  "s_add_u32 m0, s88, s57\n" \
  "s_lshl4_add_u32 s33, s88, s89\n" \
  "buffer_load_dwordx4 v12, s[40:43], s33 offen offset:0 lds\n" \
  "buffer_load_dwordx4 v13, s[40:43], s33 offen offset:1024 lds\n" \
  "buffer_load_dwordx4 v14, s[40:43], s33 offen offset:2048 lds\n" \
  "buffer_load_dwordx4 v15, s[40:43], s33 offen offset:3072 lds\n" \
  "s_add_u32 s88, s88, 4096\n" \
  "s_cmp_eq_u32 s88, 16384\n" \
  "s_cselect_b32 s88, 0, s88\n" \
  "s_bitcmp1_b32 s88, 12\n" \
  "s_cselect_b32 s89, s32, s31\n" \
  "s_add_u32 m0, s88, s57\n" \
  "s_lshl4_add_u32 s33, s88, s89\n" \
  "buffer_load_dwordx4 v12, s[40:43], s33 offen offset:0 lds\n" \
  "buffer_load_dwordx4 v13, s[40:43], s33 offen offset:1024 lds\n" \
  "buffer_load_dwordx4 v14, s[40:43], s33 offen offset:2048 lds\n" \
  "buffer_load_dwordx4 v15, s[40:43], s33 offen offset:3072 lds\n" \
  "s_add_u32 s88, s88, 4096\n" \
  "s_cmp_eq_u32 s88, 16384\n" \
  "s_cselect_b32 s88, 0, s88\n" \
  "s_bitcmp1_b32 s88, 12\n" \
  "s_cselect_b32 s89, s32, s31\n" \
  "s_add_u32 m0, s88, s57\n" \
  "s_lshl4_add_u32 s33, s88, s89\n" \
  "buffer_load_dwordx4 v12, s[40:43], s33 offen offset:0 lds\n" \
  "buffer_load_dwordx4 v13, s[40:43], s33 offen offset:1024 lds\n" \
  "buffer_load_dwordx4 v14, s[40:43], s33 offen offset:2048 lds\n" \
  "buffer_load_dwordx4 v15, s[40:43], s33 offen offset:3072 lds\n" \
  "s_add_u32 s88, s88, 4096\n" \
  "s_cmp_eq_u32 s88, 16384\n" \
  "s_cselect_b32 s88, 0, s88\n" \
  "s_bitcmp1_b32 s88, 12\n" \
  "s_cselect_b32 s89, s32, s31\n" \
  "s_add_u32 m0, s88, s57\n" \
  "s_lshl4_add_u32 s33, s88, s89\n" \
  "buffer_load_dwordx4 v12, s[40:43], s33 offen offset:0 lds\n" \
  "buffer_load_dwordx4 v13, s[40:43], s33 offen offset:1024 lds\n" \
  "buffer_load_dwordx4 v14, s[40:43], s33 offen offset:2048 lds\n" \
  "buffer_load_dwordx4 v15, s[40:43], s33 offen offset:3072 lds\n" \
  "s_mov_b32 s89, s101\n" \
  "s_waitcnt vmcnt(16)\n" \
  "s_branch .Li_%=\n" \
  ".Lh_%=:\n" \
  "buffer_load_dwordx4 v[4:7], v0, s[36:39], s87 offen sc1\n" \
  "s_waitcnt vmcnt(0)\n" \
  ".Li_%=:\n" \
  "s_memrealtime s[96:97]\n" \
  "s_waitcnt lgkmcnt(0)\n" \
  "v_bitop3_b32 v4, v4, v5, v6 bitop3:0x80\n" \
  "v_and_b32 v4, v4, v7\n" \
  "v_cmp_ne_u32 vcc, 0, v4\n" \
  "v_max3_u32 v2, v64, v68, v72\n" \
  "v_max3_u32 v2, v2, v76, v80\n" \
  "v_max3_u32 v2, v2, v84, v88\n" \
  "v_max3_u32 v2, v2, v92, v96\n" \
  "v_max3_u32 v2, v2, v100, v104\n" \
  "v_max3_u32 v2, v2, v108, v112\n" \
  "v_max3_u32 v2, v2, v116, v120\n" \
  "v_max_u32 v2, v2, v124\n" \
  "v_cndmask_b32 v0, v0, v1, vcc\n" \
  "v_cmp_eq_u32 vcc, -1, v2\n" \
  "v_cmp_ne_u32 s[100:101], v0, v1\n" \
  "s_sub_u32 s96, s96, s94\n" \
  "s_cbranch_vccz .Lj_%=\n" \
  "v_cmpx_eq_u32 vcc, -1, v64\n" \
  "buffer_load_dwordx4 v[64:67], v59, s[36:39], s48 offen offset:0 sc1\n" \
  "s_mov_b64 exec, -1\n" \
  "v_cmpx_eq_u32 vcc, -1, v68\n" \
  "buffer_load_dwordx4 v[68:71], v59, s[36:39], s48 offen offset:1024 sc1\n" \
  "s_mov_b64 exec, -1\n" \
  "v_cmpx_eq_u32 vcc, -1, v72\n" \
  "buffer_load_dwordx4 v[72:75], v59, s[36:39], s48 offen offset:2048 sc1\n" \
  "s_mov_b64 exec, -1\n" \
  "v_cmpx_eq_u32 vcc, -1, v76\n" \
  "buffer_load_dwordx4 v[76:79], v59, s[36:39], s48 offen offset:3072 sc1\n" \
  "s_mov_b64 exec, -1\n" \
  "v_cmpx_eq_u32 vcc, -1, v80\n" \
  "buffer_load_dwordx4 v[80:83], v59, s[36:39], s49 offen offset:0 sc1\n" \
  "s_mov_b64 exec, -1\n" \
  "v_cmpx_eq_u32 vcc, -1, v84\n" \
  "buffer_load_dwordx4 v[84:87], v59, s[36:39], s49 offen offset:1024 sc1\n" \
  "s_mov_b64 exec, -1\n" \
  "v_cmpx_eq_u32 vcc, -1, v88\n" \
  "buffer_load_dwordx4 v[88:91], v59, s[36:39], s49 offen offset:2048 sc1\n" \
  "s_mov_b64 exec, -1\n" \
  "v_cmpx_eq_u32 vcc, -1, v92\n" \
  "buffer_load_dwordx4 v[92:95], v59, s[36:39], s49 offen offset:3072 sc1\n" \
  "s_mov_b64 exec, -1\n" \
  "v_cmpx_eq_u32 vcc, -1, v96\n" \
  "buffer_load_dwordx4 v[96:99], v59, s[36:39], s50 offen offset:0 sc1\n" \
  "s_mov_b64 exec, -1\n" \
  "v_cmpx_eq_u32 vcc, -1, v100\n" \
  "buffer_load_dwordx4 v[100:103], v59, s[36:39], s50 offen offset:1024 sc1\n" \
  "s_mov_b64 exec, -1\n" \
  "v_cmpx_eq_u32 vcc, -1, v104\n" \
  "buffer_load_dwordx4 v[104:107], v59, s[36:39], s50 offen offset:2048 sc1\n" \
  "s_mov_b64 exec, -1\n" \
  "v_cmpx_eq_u32 vcc, -1, v108\n" \
  "buffer_load_dwordx4 v[108:111], v59, s[36:39], s50 offen offset:3072 sc1\n" \
  "s_mov_b64 exec, -1\n" \
  "v_cmpx_eq_u32 vcc, -1, v112\n" \
  "buffer_load_dwordx4 v[112:115], v59, s[36:39], s51 offen offset:0 sc1\n" \
  "s_mov_b64 exec, -1\n" \
  "v_cmpx_eq_u32 vcc, -1, v116\n" \
  "buffer_load_dwordx4 v[116:119], v59, s[36:39], s51 offen offset:1024 sc1\n" \
  "s_mov_b64 exec, -1\n" \
  "v_cmpx_eq_u32 vcc, -1, v120\n" \
  "buffer_load_dwordx4 v[120:123], v59, s[36:39], s51 offen offset:2048 sc1\n" \
  "s_mov_b64 exec, -1\n" \
  "v_cmpx_eq_u32 vcc, -1, v124\n" \
  "buffer_load_dwordx4 v[124:127], v59, s[36:39], s51 offen offset:3072 sc1\n" \
  "s_mov_b64 exec, -1\n" \
  "s_or_b64 s[100:101], s[100:101], 1\n" \
  ".Lj_%=:\n" \
  "s_cmp_gt_u32 s96, 0x4c4b40\n" \
  "s_cbranch_scc1 .Lg_%=\n" \
  "s_cmp_lg_u64 s[100:101], 0\n" \
  "s_cbranch_scc1 .Lh_%=\n" \
  ".Lg_%=:\n" \
  "s_sub_u32 s31, s60, 0x60080\n" \
  "s_sub_u32 s32, s60, 0x70000\n" \
  "s_mov_b32 m0, s56\n" \
  "s_mov_b32 s94, s86\n" \
  "s_and_b32 s95, s89, 7\n" \
  "s_andn2_b32 s101, s89, 7\n" \
  "s_mul_i32 s95, s95, 409600\n" \
  "s_add_u32 s95, s95, s52\n" \
  "s_mov_b32 s86, 0x11111111\n" \
  "s_mov_b32 s87, 0x11111111\n" \
  "v_add_u32 v59, s85, v59\n" \
  "s_add_u32 s35, s52, 1638400\n" \
  "s_mov_b32 s34, s91\n" \
  "s_waitcnt lgkmcnt(0)\n" \
  "s_cmp_eq_u32 s84, 0\n" \
  "s_cbranch_scc0 .Ls_%=\n" \
  ".Lf_%=:\n" \
  "s_waitcnt vmcnt(16)\n" \
  "v_mfma_f32_16x16x32_bf16 v[0:3], v[64:67], a[0:3], 0\n" \
  "ds_read_b32 v44, v60 offset:0\n" \
  "v_add_f32 v42, 1.0, v42\n" \
  "v_mfma_f32_16x16x32_bf16 v[4:7], v[64:67], a[4:7], 0\n" \
  "ds_read_b32 v45, v60 offset:9568\n" \
  "v_add_f32 v43, 1.0, v43\n" \
  "v_mfma_f32_16x16x32_bf16 v[8:11], v[64:67], a[8:11], 0\n" \
  "ds_read_b32 v46, v60 offset:19136\n" \
  "v_add_u32 v59, 0x10000, v59\n" \
  "v_mfma_f32_16x16x32_bf16 v[12:15], v[64:67], a[12:15], 0\n" \
  "ds_read_b32 v47, v60 offset:28704\n" \
  "v_and_b32 v59, 0x7ffff, v59\n" \
  "v_mfma_f32_16x16x32_bf16 v[16:19], v[64:67], a[16:19], 0\n" \
  "buffer_load_dwordx4 v[64:67], v59, s[36:39], s48 offen offset:0 sc1\n" \
  "s_waitcnt vmcnt(16)\n" \
  "v_mfma_f32_16x16x32_bf16 v[0:3], v[68:71], a[20:23], v[0:3]\n" \
  "v_rcp_f32 v40, v40\n" \
  "v_mfma_f32_16x16x32_bf16 v[4:7], v[68:71], a[24:27], v[4:7]\n" \
  "v_rcp_f32 v41, v41\n" \
  "v_mfma_f32_16x16x32_bf16 v[8:11], v[68:71], a[28:31], v[8:11]\n" \
  "v_rcp_f32 v42, v42\n" \
  "v_mfma_f32_16x16x32_bf16 v[12:15], v[68:71], a[32:35], v[12:15]\n" \
  "v_rcp_f32 v43, v43\n" \
  "v_mfma_f32_16x16x32_bf16 v[16:19], v[68:71], a[36:39], v[16:19]\n" \
  "buffer_load_dwordx4 v[68:71], v59, s[36:39], s48 offen offset:1024 sc1\n" \
  "s_waitcnt vmcnt(15)\n" \
  "v_mfma_f32_16x16x32_bf16 v[0:3], v[72:75], a[40:43], v[0:3]\n" \
  "v_mul_f32 v36, v36, v40\n" \
  "v_mul_f32 v37, v37, v41\n" \
  "v_mfma_f32_16x16x32_bf16 v[4:7], v[72:75], a[44:47], v[4:7]\n" \
  "v_mul_f32 v38, v38, v42\n" \
  "v_mul_f32 v39, v39, v43\n" \
  "v_mfma_f32_16x16x32_bf16 v[8:11], v[72:75], a[48:51], v[8:11]\n" \
  "v_cvt_pk_bf16_f32 v40, v36, v37\n" \
  "v_cvt_pk_bf16_f32 v41, v38, v39\n" \
  "v_mfma_f32_16x16x32_bf16 v[12:15], v[72:75], a[52:55], v[12:15]\n" \
  "buffer_store_dwordx2 v[40:41], v61, s[36:39], s52 offen offset:0 sc1\n" \
  "ds_write_addtid_b32 v20 offset:5344\n" \
  "v_mfma_f32_16x16x32_bf16 v[16:19], v[72:75], a[56:59], v[16:19]\n" \
  "buffer_load_dwordx4 v[72:75], v59, s[36:39], s48 offen offset:2048 sc1\n" \
  "s_waitcnt vmcnt(16)\n" \
  "v_mfma_f32_16x16x32_bf16 v[0:3], v[76:79], a[60:63], v[0:3]\n" \
  "ds_write_addtid_b32 v21 offset:5888\n" \
  "s_waitcnt lgkmcnt(2)\n" \
  "v_mfma_f32_16x16x32_bf16 v[4:7], v[76:79], a[64:67], v[4:7]\n" \
  "ds_write_addtid_b32 v22 offset:5600\n" \
  "v_add_f32 v44, v44, v45\n" \
  "v_mfma_f32_16x16x32_bf16 v[8:11], v[76:79], a[68:71], v[8:11]\n" \
  "ds_write_addtid_b32 v23 offset:6144\n" \
  "v_add_f32 v46, v46, v47\n" \
  "v_mfma_f32_16x16x32_bf16 v[12:15], v[76:79], a[72:75], v[12:15]\n" \
  "ds_write_addtid_b32 v24 offset:6400\n" \
  "v_add_f32 v44, v44, v56\n" \
  "v_mfma_f32_16x16x32_bf16 v[16:19], v[76:79], a[76:79], v[16:19]\n" \
  "buffer_load_dwordx4 v[76:79], v59, s[36:39], s48 offen offset:3072 sc1\n" \
  "s_waitcnt vmcnt(16)\n" \
  "v_mfma_f32_16x16x32_bf16 v[0:3], v[80:83], a[80:83], v[0:3]\n" \
  "ds_write_addtid_b32 v25 offset:6944\n" \
  "v_add_f32 v44, v44, v46\n" \
  "v_mfma_f32_16x16x32_bf16 v[4:7], v[80:83], a[84:87], v[4:7]\n" \
  "ds_write_addtid_b32 v26 offset:6656\n" \
  "v_mul_f32 v45, v44, v44\n" \
  "v_mfma_f32_16x16x32_bf16 v[8:11], v[80:83], a[88:91], v[8:11]\n" \
  "ds_write_addtid_b32 v27 offset:7200\n" \
  "v_fma_f32 v45, v45, s55, v57\n" \
  "v_mfma_f32_16x16x32_bf16 v[12:15], v[80:83], a[92:95], v[12:15]\n" \
  "ds_write_addtid_b32 v28 offset:7456\n" \
  "v_mul_f32 v45, v44, v45\n" \
  "v_mfma_f32_16x16x32_bf16 v[16:19], v[80:83], a[96:99], v[16:19]\n" \
  "buffer_load_dwordx4 v[80:83], v59, s[36:39], s49 offen offset:0 sc1\n" \
  "s_waitcnt vmcnt(16)\n" \
  "v_mfma_f32_16x16x32_bf16 v[0:3], v[84:87], a[100:103], v[0:3]\n" \
  "ds_write_addtid_b32 v29 offset:8000\n" \
  "v_mfma_f32_16x16x32_bf16 v[4:7], v[84:87], a[104:107], v[4:7]\n" \
  "ds_write_addtid_b32 v30 offset:7712\n" \
  "v_mfma_f32_16x16x32_bf16 v[8:11], v[84:87], a[108:111], v[8:11]\n" \
  "ds_write_addtid_b32 v31 offset:8256\n" \
  "v_mfma_f32_16x16x32_bf16 v[12:15], v[84:87], a[112:115], v[12:15]\n" \
  "ds_write_addtid_b32 v32 offset:8512\n" \
  "v_mfma_f32_16x16x32_bf16 v[16:19], v[84:87], a[116:119], v[16:19]\n" \
  "buffer_load_dwordx4 v[84:87], v59, s[36:39], s49 offen offset:1024 sc1\n" \
  "s_waitcnt vmcnt(16)\n" \
  "v_mfma_f32_16x16x32_bf16 v[0:3], v[88:91], a[120:123], v[0:3]\n" \
  "ds_write_addtid_b32 v33 offset:9056\n" \
  "v_mfma_f32_16x16x32_bf16 v[4:7], v[88:91], a[124:127], v[4:7]\n" \
  "ds_write_addtid_b32 v34 offset:8768\n" \
  "v_mfma_f32_16x16x32_bf16 v[8:11], v[88:91], a[128:131], v[8:11]\n" \
  "ds_write_addtid_b32 v35 offset:9312\n" \
  "v_mfma_f32_16x16x32_bf16 v[12:15], v[88:91], a[132:135], v[12:15]\n" \
  "v_exp_f32 v45, v45\n" \
  "v_mfma_f32_16x16x32_bf16 v[16:19], v[88:91], a[136:139], v[16:19]\n" \
  "buffer_load_dwordx4 v[88:91], v59, s[36:39], s49 offen offset:2048 sc1\n" \
  "s_waitcnt vmcnt(16)\n" \
  "v_mfma_f32_16x16x32_bf16 v[0:3], v[92:95], a[140:143], v[0:3]\n" \
  "v_add_f32 v45, 1.0, v45\n" \
  "v_mfma_f32_16x16x32_bf16 v[4:7], v[92:95], a[144:147], v[4:7]\n" \
  "v_rcp_f32 v45, v45\n" \
  "v_mfma_f32_16x16x32_bf16 v[8:11], v[92:95], a[148:151], v[8:11]\n" \
  "v_mul_f32 v44, v44, v45\n" \
  "v_cvt_pk_bf16_f32 v45, v44, v44\n" \
  "v_mfma_f32_16x16x32_bf16 v[12:15], v[92:95], a[152:155], v[12:15]\n" \
  "s_waitcnt lgkmcnt(0)\n" \
  "s_barrier\n" \
  "v_mfma_f32_16x16x32_bf16 v[16:19], v[92:95], a[156:159], v[16:19]\n" \
  "buffer_load_dwordx4 v[92:95], v59, s[36:39], s49 offen offset:3072 sc1\n" \
  "s_waitcnt vmcnt(16)\n" \
  "v_mfma_f32_16x16x32_bf16 v[0:3], v[96:99], a[160:163], v[0:3]\n" \
  "buffer_store_short v45, v62, s[36:39], s52 offen offset:0 sc1\n" \
  "ds_read_b128 v[36:39], v58 offset:5344\n" \
  "v_mfma_f32_16x16x32_bf16 v[4:7], v[96:99], a[164:167], v[4:7]\n" \
  "ds_read_b128 v[40:43], v58 offset:14912\n" \
  "v_mfma_f32_16x16x32_bf16 v[8:11], v[96:99], a[168:171], v[8:11]\n" \
  "ds_read_b128 v[44:47], v58 offset:24480\n" \
  "v_mfma_f32_16x16x32_bf16 v[12:15], v[96:99], a[172:175], v[12:15]\n" \
  "ds_read_b128 v[48:51], v58 offset:34048\n" \
  "v_mfma_f32_16x16x32_bf16 v[16:19], v[96:99], a[176:179], v[16:19]\n" \
  "buffer_load_dwordx4 v[96:99], v59, s[36:39], s50 offen offset:0 sc1\n" \
  "s_waitcnt vmcnt(17)\n" \
  "v_mfma_f32_16x16x32_bf16 v[0:3], v[100:103], a[180:183], v[0:3]\n" \
  "s_cmp_eq_u32 s52, s83\n" \
  "s_cbranch_scc1 .Lxq5_%=\n" \
  ".Lyq5_%=:\n" \
  "v_mfma_f32_16x16x32_bf16 v[4:7], v[100:103], a[184:187], v[4:7]\n" \
  "v_mfma_f32_16x16x32_bf16 v[8:11], v[100:103], a[188:191], v[8:11]\n" \
  "v_mfma_f32_16x16x32_bf16 v[12:15], v[100:103], a[192:195], v[12:15]\n" \
  "v_mfma_f32_16x16x32_bf16 v[16:19], v[100:103], a[196:199], v[16:19]\n" \
  "buffer_load_dwordx4 v[100:103], v59, s[36:39], s50 offen offset:1024 sc1\n" \
  "s_waitcnt vmcnt(17)\n" \
  "v_mfma_f32_16x16x32_bf16 v[0:3], v[104:107], a[200:203], v[0:3]\n" \
  "s_waitcnt lgkmcnt(2)\n" \
  "v_add_f32 v36, v36, v40\n" \
  "v_mfma_f32_16x16x32_bf16 v[4:7], v[104:107], a[204:207], v[4:7]\n" \
  "v_add_f32 v37, v37, v41\n" \
  "v_add_f32 v38, v38, v42\n" \
  "v_mfma_f32_16x16x32_bf16 v[8:11], v[104:107], a[208:211], v[8:11]\n" \
  "v_add_f32 v39, v39, v43\n" \
  "v_add_f32 v36, v36, v52\n" \
  "v_mfma_f32_16x16x32_bf16 v[12:15], v[104:107], a[212:215], v[12:15]\n" \
  "v_add_f32 v37, v37, v53\n" \
  "v_add_f32 v38, v38, v54\n" \
  "v_mfma_f32_16x16x32_bf16 v[16:19], v[104:107], a[216:219], v[16:19]\n" \
  "buffer_load_dwordx4 v[104:107], v59, s[36:39], s50 offen offset:2048 sc1\n" \
  "s_waitcnt vmcnt(17)\n" \
  "v_mfma_f32_16x16x32_bf16 v[0:3], v[108:111], a[220:223], v[0:3]\n" \
  "v_add_f32 v39, v39, v55\n" \
  "s_waitcnt lgkmcnt(0)\n" \
  "v_mfma_f32_16x16x32_bf16 v[4:7], v[108:111], a[224:227], v[4:7]\n" \
  "v_add_f32 v44, v44, v48\n" \
  "v_add_f32 v45, v45, v49\n" \
  "v_mfma_f32_16x16x32_bf16 v[8:11], v[108:111], a[228:231], v[8:11]\n" \
  "v_add_f32 v46, v46, v50\n" \
  "v_add_f32 v47, v47, v51\n" \
  "v_mfma_f32_16x16x32_bf16 v[12:15], v[108:111], a[232:235], v[12:15]\n" \
  "v_add_f32 v36, v36, v44\n" \
  "v_add_f32 v37, v37, v45\n" \
  "v_mfma_f32_16x16x32_bf16 v[16:19], v[108:111], a[236:239], v[16:19]\n" \
  "buffer_load_dwordx4 v[108:111], v59, s[36:39], s50 offen offset:3072 sc1\n" \
  "s_waitcnt vmcnt(17)\n" \
  "v_mfma_f32_16x16x32_bf16 v[0:3], v[112:115], a[240:243], v[0:3]\n" \
  "v_add_f32 v38, v38, v46\n" \
  "v_add_f32 v39, v39, v47\n" \
  "v_mfma_f32_16x16x32_bf16 v[4:7], v[112:115], a[244:247], v[4:7]\n" \
  "v_mul_f32 v40, v36, v36\n" \
  "v_mul_f32 v41, v37, v37\n" \
  "v_mfma_f32_16x16x32_bf16 v[8:11], v[112:115], a[248:251], v[8:11]\n" \
  "v_mul_f32 v42, v38, v38\n" \
  "v_mul_f32 v43, v39, v39\n" \
  "v_mfma_f32_16x16x32_bf16 v[12:15], v[112:115], a[252:255], v[12:15]\n" \
  "v_fma_f32 v40, v40, s55, v57\n" \
  "v_fma_f32 v41, v41, s55, v57\n" \
  "v_mfma_f32_16x16x32_bf16 v[16:19], v[112:115], v[192:195], v[16:19]\n" \
  "buffer_load_dwordx4 v[112:115], v59, s[36:39], s51 offen offset:0 sc1\n" \
  "s_waitcnt vmcnt(17)\n" \
  "v_mfma_f32_16x16x32_bf16 v[0:3], v[116:119], v[196:199], v[0:3]\n" \
  "v_fma_f32 v42, v42, s55, v57\n" \
  "v_fma_f32 v43, v43, s55, v57\n" \
  "v_mfma_f32_16x16x32_bf16 v[4:7], v[116:119], v[200:203], v[4:7]\n" \
  "v_mul_f32 v40, v36, v40\n" \
  "v_mul_f32 v41, v37, v41\n" \
  "v_mfma_f32_16x16x32_bf16 v[8:11], v[116:119], v[204:207], v[8:11]\n" \
  "v_mul_f32 v42, v38, v42\n" \
  "v_mul_f32 v43, v39, v43\n" \
  "v_mfma_f32_16x16x32_bf16 v[12:15], v[116:119], v[208:211], v[12:15]\n" \
  "v_exp_f32 v40, v40\n" \
  "v_mfma_f32_16x16x32_bf16 v[16:19], v[116:119], v[212:215], v[16:19]\n" \
  "buffer_load_dwordx4 v[116:119], v59, s[36:39], s51 offen offset:1024 sc1\n" \
  "s_waitcnt vmcnt(17)\n" \
  "v_mfma_f32_16x16x32_bf16 v[0:3], v[120:123], v[216:219], v[0:3]\n" \
  "v_exp_f32 v41, v41\n" \
  "v_mfma_f32_16x16x32_bf16 v[4:7], v[120:123], v[220:223], v[4:7]\n" \
  "v_exp_f32 v42, v42\n" \
  "v_mfma_f32_16x16x32_bf16 v[8:11], v[120:123], v[224:227], v[8:11]\n" \
  "v_exp_f32 v43, v43\n" \
  "v_mfma_f32_16x16x32_bf16 v[12:15], v[120:123], v[228:231], v[12:15]\n" \
  "v_add_f32 v40, 1.0, v40\n" \
  "v_add_f32 v41, 1.0, v41\n" \
  "v_mfma_f32_16x16x32_bf16 v[16:19], v[120:123], v[232:235], v[16:19]\n" \
  "buffer_load_dwordx4 v[120:123], v59, s[36:39], s51 offen offset:2048 sc1\n" \
  "s_waitcnt vmcnt(17)\n" \
  "v_mfma_f32_16x16x32_bf16 v[0:3], v[124:127], v[236:239], v[0:3]\n" \
  "v_add_f32 v42, 1.0, v42\n" \
  "v_add_f32 v43, 1.0, v43\n" \
  "v_mfma_f32_16x16x32_bf16 v[4:7], v[124:127], v[240:243], v[4:7]\n" \
  "s_cmp_eq_u32 s52, s95\n" \
  "s_cbranch_scc1 .Lxt5_%=\n" \
  ".Lyt5_%=:\n" \
  "v_mfma_f32_16x16x32_bf16 v[8:11], v[124:127], v[244:247], v[8:11]\n" \
  "v_rcp_f32 v40, v40\n" \
  "v_mfma_f32_16x16x32_bf16 v[12:15], v[124:127], v[248:251], v[12:15]\n" \
  "v_rcp_f32 v41, v41\n" \
  "v_mfma_f32_16x16x32_bf16 v[16:19], v[124:127], v[252:255], v[16:19]\n" \
  "buffer_load_dwordx4 v[124:127], v59, s[36:39], s51 offen offset:3072 sc1\n" \
  "s_add_u32 s52, s52, 409600\n" \
  "s_cmp_eq_u32 s52, s53\n" \
  "s_cbranch_scc1 .Lls_%=\n" \
  ".Ls_%=:\n" \
  "s_waitcnt vmcnt(17)\n" \
  "v_mfma_f32_16x16x32_bf16 v[20:23], v[64:67], a[0:3], 0\n" \
  "v_rcp_f32 v42, v42\n" \
  "v_mfma_f32_16x16x32_bf16 v[24:27], v[64:67], a[4:7], 0\n" \
  "v_add_u32 v59, 0x10000, v59\n" \
  "ds_write_addtid_b32 v0 offset:0\n" \
  "v_mfma_f32_16x16x32_bf16 v[28:31], v[64:67], a[8:11], 0\n" \
  "v_and_b32 v59, 0x7ffff, v59\n" \
  "ds_write_addtid_b32 v1 offset:544\n" \
  "v_mfma_f32_16x16x32_bf16 v[32:35], v[64:67], a[12:15], 0\n" \
  "buffer_load_dwordx4 v[64:67], v59, s[36:39], s48 offen offset:0 sc1\n" \
  "s_waitcnt vmcnt(17)\n" \
  "v_mfma_f32_16x16x32_bf16 v[20:23], v[68:71], a[20:23], v[20:23]\n" \
  "v_rcp_f32 v43, v43\n" \
  "v_mfma_f32_16x16x32_bf16 v[24:27], v[68:71], a[24:27], v[24:27]\n" \
  "v_mul_f32 v36, v36, v40\n" \
  "v_mul_f32 v37, v37, v41\n" \
  "v_mfma_f32_16x16x32_bf16 v[28:31], v[68:71], a[28:31], v[28:31]\n" \
  "v_mul_f32 v38, v38, v42\n" \
  "v_mul_f32 v39, v39, v43\n" \
  "v_mfma_f32_16x16x32_bf16 v[32:35], v[68:71], a[32:35], v[32:35]\n" \
  "buffer_load_dwordx4 v[68:71], v59, s[36:39], s48 offen offset:1024 sc1\n" \
  "s_waitcnt vmcnt(16)\n" \
  "v_mfma_f32_16x16x32_bf16 v[20:23], v[72:75], a[40:43], v[20:23]\n" \
  "ds_write_addtid_b32 v2 offset:256\n" \
  "v_cvt_pk_bf16_f32 v40, v36, v37\n" \
  "v_mfma_f32_16x16x32_bf16 v[24:27], v[72:75], a[44:47], v[24:27]\n" \
  "ds_write_addtid_b32 v3 offset:800\n" \
  "v_cvt_pk_bf16_f32 v41, v38, v39\n" \
  "v_mfma_f32_16x16x32_bf16 v[28:31], v[72:75], a[48:51], v[28:31]\n" \
  "ds_write_addtid_b32 v4 offset:1056\n" \
  "buffer_store_dwordx2 v[40:41], v61, s[36:39], s52 offen offset:0 sc1\n" \
  "v_mfma_f32_16x16x32_bf16 v[32:35], v[72:75], a[52:55], v[32:35]\n" \
  "buffer_load_dwordx4 v[72:75], v59, s[36:39], s48 offen offset:2048 sc1\n" \
  "s_waitcnt vmcnt(17)\n" \
  "v_mfma_f32_16x16x32_bf16 v[20:23], v[76:79], a[60:63], v[20:23]\n" \
  "ds_write_addtid_b32 v5 offset:1600\n" \
  "v_mfma_f32_16x16x32_bf16 v[24:27], v[76:79], a[64:67], v[24:27]\n" \
  "ds_write_addtid_b32 v6 offset:1312\n" \
  "v_mfma_f32_16x16x32_bf16 v[28:31], v[76:79], a[68:71], v[28:31]\n" \
  "ds_write_addtid_b32 v7 offset:1856\n" \
  "v_mfma_f32_16x16x32_bf16 v[32:35], v[76:79], a[72:75], v[32:35]\n" \
  "buffer_load_dwordx4 v[76:79], v59, s[36:39], s48 offen offset:3072 sc1\n" \
  "s_waitcnt vmcnt(17)\n" \
  "v_mfma_f32_16x16x32_bf16 v[20:23], v[80:83], a[80:83], v[20:23]\n" \
  "ds_write_addtid_b32 v8 offset:2112\n" \
  "v_mfma_f32_16x16x32_bf16 v[24:27], v[80:83], a[84:87], v[24:27]\n" \
  "ds_write_addtid_b32 v9 offset:2656\n" \
  "v_mfma_f32_16x16x32_bf16 v[28:31], v[80:83], a[88:91], v[28:31]\n" \
  "ds_write_addtid_b32 v10 offset:2368\n" \
  "v_mfma_f32_16x16x32_bf16 v[32:35], v[80:83], a[92:95], v[32:35]\n" \
  "buffer_load_dwordx4 v[80:83], v59, s[36:39], s49 offen offset:0 sc1\n" \
  "s_waitcnt vmcnt(17)\n" \
  "v_mfma_f32_16x16x32_bf16 v[20:23], v[84:87], a[100:103], v[20:23]\n" \
  "ds_write_addtid_b32 v11 offset:2912\n" \
  "v_mfma_f32_16x16x32_bf16 v[24:27], v[84:87], a[104:107], v[24:27]\n" \
  "ds_write_addtid_b32 v12 offset:3168\n" \
  "v_mfma_f32_16x16x32_bf16 v[28:31], v[84:87], a[108:111], v[28:31]\n" \
  "ds_write_addtid_b32 v13 offset:3712\n" \
  "v_mfma_f32_16x16x32_bf16 v[32:35], v[84:87], a[112:115], v[32:35]\n" \
  "buffer_load_dwordx4 v[84:87], v59, s[36:39], s49 offen offset:1024 sc1\n" \
  "s_waitcnt vmcnt(17)\n" \
  "v_mfma_f32_16x16x32_bf16 v[20:23], v[88:91], a[120:123], v[20:23]\n" \
  "ds_write_addtid_b32 v14 offset:3424\n" \
  "v_mfma_f32_16x16x32_bf16 v[24:27], v[88:91], a[124:127], v[24:27]\n" \
  "ds_write_addtid_b32 v15 offset:3968\n" \
  "v_mfma_f32_16x16x32_bf16 v[28:31], v[88:91], a[128:131], v[28:31]\n" \
  "ds_write_addtid_b32 v16 offset:4224\n" \
  "v_mfma_f32_16x16x32_bf16 v[32:35], v[88:91], a[132:135], v[32:35]\n" \
  "buffer_load_dwordx4 v[88:91], v59, s[36:39], s49 offen offset:2048 sc1\n" \
  "s_waitcnt vmcnt(17)\n" \
  "v_mfma_f32_16x16x32_bf16 v[20:23], v[92:95], a[140:143], v[20:23]\n" \
  "ds_write_addtid_b32 v17 offset:4512\n" \
  "v_mfma_f32_16x16x32_bf16 v[24:27], v[92:95], a[144:147], v[24:27]\n" \
  "ds_write_addtid_b32 v18 offset:4800\n" \
  "v_mfma_f32_16x16x32_bf16 v[28:31], v[92:95], a[148:151], v[28:31]\n" \
  "ds_write_addtid_b32 v19 offset:5088\n" \
  "v_mfma_f32_16x16x32_bf16 v[32:35], v[92:95], a[152:155], v[32:35]\n" \
  "buffer_load_dwordx4 v[92:95], v59, s[36:39], s49 offen offset:3072 sc1\n" \
  "s_waitcnt vmcnt(16)\n" \
  "v_mfma_f32_16x16x32_bf16 v[20:23], v[96:99], a[160:163], v[20:23]\n" \
  "v_mfma_f32_16x16x32_bf16 v[24:27], v[96:99], a[164:167], v[24:27]\n" \
  "v_mfma_f32_16x16x32_bf16 v[28:31], v[96:99], a[168:171], v[28:31]\n" \
  "v_mfma_f32_16x16x32_bf16 v[32:35], v[96:99], a[172:175], v[32:35]\n" \
  "buffer_load_dwordx4 v[96:99], v59, s[36:39], s50 offen offset:0 sc1\n" \
  "s_waitcnt vmcnt(16)\n" \
  "v_mfma_f32_16x16x32_bf16 v[20:23], v[100:103], a[180:183], v[20:23]\n" \
  "s_waitcnt lgkmcnt(0)\n" \
  "s_barrier\n" \
  "v_mfma_f32_16x16x32_bf16 v[24:27], v[100:103], a[184:187], v[24:27]\n" \
  "ds_read_b128 v[36:39], v58 offset:0\n" \
  "v_mfma_f32_16x16x32_bf16 v[28:31], v[100:103], a[188:191], v[28:31]\n" \
  "ds_read_b128 v[40:43], v58 offset:9568\n" \
  "v_mfma_f32_16x16x32_bf16 v[32:35], v[100:103], a[192:195], v[32:35]\n" \
  "buffer_load_dwordx4 v[100:103], v59, s[36:39], s50 offen offset:1024 sc1\n" \
  "s_waitcnt vmcnt(16)\n" \
  "v_mfma_f32_16x16x32_bf16 v[20:23], v[104:107], a[200:203], v[20:23]\n" \
  "ds_read_b128 v[44:47], v58 offset:19136\n" \
  "v_mfma_f32_16x16x32_bf16 v[24:27], v[104:107], a[204:207], v[24:27]\n" \
  "ds_read_b128 v[48:51], v58 offset:28704\n" \
  "v_mfma_f32_16x16x32_bf16 v[28:31], v[104:107], a[208:211], v[28:31]\n" \
  "s_cmp_eq_u32 s52, s83\n" \
  "s_cbranch_scc1 .Lxq4_%=\n" \
  ".Lyq4_%=:\n" \
  "v_mfma_f32_16x16x32_bf16 v[32:35], v[104:107], a[212:215], v[32:35]\n" \
  "buffer_load_dwordx4 v[104:107], v59, s[36:39], s50 offen offset:2048 sc1\n" \
  "s_waitcnt vmcnt(16)\n" \
  "v_mfma_f32_16x16x32_bf16 v[20:23], v[108:111], a[220:223], v[20:23]\n" \
  "v_mfma_f32_16x16x32_bf16 v[24:27], v[108:111], a[224:227], v[24:27]\n" \
  "v_mfma_f32_16x16x32_bf16 v[28:31], v[108:111], a[228:231], v[28:31]\n" \
  "s_waitcnt lgkmcnt(2)\n" \
  "v_add_f32 v36, v36, v40\n" \
  "v_add_f32 v37, v37, v41\n" \
  "v_mfma_f32_16x16x32_bf16 v[32:35], v[108:111], a[232:235], v[32:35]\n" \
  "buffer_load_dwordx4 v[108:111], v59, s[36:39], s50 offen offset:3072 sc1\n" \
  "v_add_f32 v38, v38, v42\n" \
  "s_waitcnt vmcnt(16)\n" \
  "v_mfma_f32_16x16x32_bf16 v[20:23], v[112:115], a[240:243], v[20:23]\n" \
  "v_add_f32 v39, v39, v43\n" \
  "v_add_f32 v36, v36, v52\n" \
  "v_add_f32 v37, v37, v53\n" \
  "v_mfma_f32_16x16x32_bf16 v[24:27], v[112:115], a[244:247], v[24:27]\n" \
  "v_add_f32 v38, v38, v54\n" \
  "v_add_f32 v39, v39, v55\n" \
  "s_waitcnt lgkmcnt(0)\n" \
  "v_mfma_f32_16x16x32_bf16 v[28:31], v[112:115], a[248:251], v[28:31]\n" \
  "v_add_f32 v44, v44, v48\n" \
  "v_add_f32 v45, v45, v49\n" \
  "v_add_f32 v46, v46, v50\n" \
  "v_mfma_f32_16x16x32_bf16 v[32:35], v[112:115], a[252:255], v[32:35]\n" \
  "buffer_load_dwordx4 v[112:115], v59, s[36:39], s51 offen offset:0 sc1\n" \
  "v_add_f32 v47, v47, v51\n" \
  "s_waitcnt vmcnt(16)\n" \
  "v_mfma_f32_16x16x32_bf16 v[20:23], v[116:119], v[196:199], v[20:23]\n" \
  "v_add_f32 v36, v36, v44\n" \
  "v_add_f32 v37, v37, v45\n" \
  "v_add_f32 v38, v38, v46\n" \
  "v_mfma_f32_16x16x32_bf16 v[24:27], v[116:119], v[200:203], v[24:27]\n" \
  "v_add_f32 v39, v39, v47\n" \
  "v_mul_f32 v40, v36, v36\n" \
  "v_mul_f32 v41, v37, v37\n" \
  "v_mfma_f32_16x16x32_bf16 v[28:31], v[116:119], v[204:207], v[28:31]\n" \
  "v_mul_f32 v42, v38, v38\n" \
  "v_mul_f32 v43, v39, v39\n" \
  "v_fma_f32 v40, v40, s55, v57\n" \
  "v_mfma_f32_16x16x32_bf16 v[32:35], v[116:119], v[208:211], v[32:35]\n" \
  "buffer_load_dwordx4 v[116:119], v59, s[36:39], s51 offen offset:1024 sc1\n" \
  "v_fma_f32 v41, v41, s55, v57\n" \
  "s_waitcnt vmcnt(16)\n" \
  "v_mfma_f32_16x16x32_bf16 v[20:23], v[120:123], v[216:219], v[20:23]\n" \
  "v_fma_f32 v42, v42, s55, v57\n" \
  "v_fma_f32 v43, v43, s55, v57\n" \
  "v_mul_f32 v40, v36, v40\n" \
  "v_mfma_f32_16x16x32_bf16 v[24:27], v[120:123], v[220:223], v[24:27]\n" \
  "v_mul_f32 v41, v37, v41\n" \
  "v_mul_f32 v42, v38, v42\n" \
  "v_mfma_f32_16x16x32_bf16 v[28:31], v[120:123], v[224:227], v[28:31]\n" \
  "v_mul_f32 v43, v39, v43\n" \
  "v_mfma_f32_16x16x32_bf16 v[32:35], v[120:123], v[228:231], v[32:35]\n" \
  "buffer_load_dwordx4 v[120:123], v59, s[36:39], s51 offen offset:2048 sc1\n" \
  "s_waitcnt vmcnt(16)\n" \
  "v_mfma_f32_16x16x32_bf16 v[20:23], v[124:127], v[236:239], v[20:23]\n" \
  "s_cmp_eq_u32 s52, s95\n" \
  "s_cbranch_scc1 .Lxt4_%=\n" \
  ".Lyt4_%=:\n" \
  "v_mfma_f32_16x16x32_bf16 v[24:27], v[124:127], v[240:243], v[24:27]\n" \
  "v_exp_f32 v40, v40\n" \
  "v_mfma_f32_16x16x32_bf16 v[28:31], v[124:127], v[244:247], v[28:31]\n" \
  "v_exp_f32 v41, v41\n" \
  "v_mfma_f32_16x16x32_bf16 v[32:35], v[124:127], v[248:251], v[32:35]\n" \
  "buffer_load_dwordx4 v[124:127], v59, s[36:39], s51 offen offset:3072 sc1\n" \
  "v_exp_f32 v42, v42\n" \
  "v_exp_f32 v43, v43\n" \
  "v_add_f32 v40, 1.0, v40\n" \
  "s_add_u32 s52, s52, 409600\n" \
  "s_cmp_eq_u32 s52, s53\n" \
  "v_add_f32 v41, 1.0, v41\n" \
  "s_cbranch_scc0 .Lf_%=\n" \
  "v_mfma_f32_16x16x32_bf16 v[0:3], v[64:67], a[0:3], 0\n" \
  "ds_read_b32 v44, v60 offset:0\n" \
  "v_and_b32 v59, 0x3ff, v59\n" \
  "s_add_u32 s48, s68, 409600\n" \
  "v_mfma_f32_16x16x32_bf16 v[4:7], v[64:67], a[4:7], 0\n" \
  "ds_read_b32 v45, v60 offset:9568\n" \
  "s_add_u32 s49, s69, 409600\n" \
  "s_add_u32 s50, s70, 409600\n" \
  "v_mfma_f32_16x16x32_bf16 v[8:11], v[64:67], a[8:11], 0\n" \
  "ds_read_b32 v46, v60 offset:19136\n" \
  "s_add_u32 s100, s90, 73728\n" \
  "s_mov_b32 s35, 73728\n" \
  "v_mfma_f32_16x16x32_bf16 v[12:15], v[64:67], a[12:15], 0\n" \
  "ds_read_b32 v47, v60 offset:28704\n" \
  "ds_write_addtid_b32 v20 offset:5344\n" \
  "v_add_f32 v42, 1.0, v42\n" \
  "v_mfma_f32_16x16x32_bf16 v[16:19], v[64:67], a[16:19], 0\n" \
  "ds_write_addtid_b32 v21 offset:5888\n" \
  "ds_write_addtid_b32 v22 offset:5600\n" \
  "v_mfma_f32_16x16x32_bf16 v[0:3], v[68:71], a[20:23], v[0:3]\n" \
  "ds_write_addtid_b32 v23 offset:6144\n" \
  "ds_write_addtid_b32 v24 offset:6400\n" \
  "v_add_f32 v43, 1.0, v43\n" \
  "v_mfma_f32_16x16x32_bf16 v[4:7], v[68:71], a[24:27], v[4:7]\n" \
  "ds_write_addtid_b32 v25 offset:6944\n" \
  "ds_write_addtid_b32 v26 offset:6656\n" \
  "v_mfma_f32_16x16x32_bf16 v[8:11], v[68:71], a[28:31], v[8:11]\n" \
  "ds_write_addtid_b32 v27 offset:7200\n" \
  "ds_write_addtid_b32 v28 offset:7456\n" \
  "v_mfma_f32_16x16x32_bf16 v[12:15], v[68:71], a[32:35], v[12:15]\n" \
  "ds_write_addtid_b32 v29 offset:8000\n" \
  "ds_write_addtid_b32 v30 offset:7712\n" \
  "v_mfma_f32_16x16x32_bf16 v[16:19], v[68:71], a[36:39], v[16:19]\n" \
  "ds_write_addtid_b32 v31 offset:8256\n" \
  "ds_write_addtid_b32 v32 offset:8512\n" \
  "s_waitcnt vmcnt(15)\n" \
  "v_mfma_f32_16x16x32_bf16 v[0:3], v[72:75], a[40:43], v[0:3]\n" \
  "s_waitcnt vmcnt(6)\n" \
  "v_cmp_eq_u32 vcc, 0, v63\n" \
  "v_add_u32 v71, s57, v59\n" \
  "v_mfma_f32_16x16x32_bf16 v[4:7], v[72:75], a[44:47], v[4:7]\n" \
  "ds_read_b32 v64, v71 offset:28672\n" \
  "ds_read_b64 v[68:69], v71 offset:29696\n" \
  "s_waitcnt lgkmcnt(15)\n" \
  "v_mfma_f32_16x16x32_bf16 v[8:11], v[72:75], a[48:51], v[8:11]\n" \
  "s_and_b64 vcc, vcc, s[86:87]\n" \
  "s_cbranch_scc1 .LsaF_%=\n" \
  ".LbaF_%=:\n" \
  "ds_write_addtid_b32 v33 offset:9056\n" \
  "v_mfma_f32_16x16x32_bf16 v[12:15], v[72:75], a[52:55], v[12:15]\n" \
  "ds_write_addtid_b32 v34 offset:8768\n" \
  "ds_write_addtid_b32 v35 offset:9312\n" \
  "v_add_f32 v44, v44, v45\n" \
  "v_mfma_f32_16x16x32_bf16 v[16:19], v[72:75], a[56:59], v[16:19]\n" \
  "v_rcp_f32 v40, v40\n" \
  "s_waitcnt vmcnt(14)\n" \
  "v_mfma_f32_16x16x32_bf16 v[0:3], v[76:79], a[60:63], v[0:3]\n" \
  "buffer_load_dwordx4 v[72:75], v59, s[36:39], s68 offen offset:0 sc1\n" \
  "s_waitcnt lgkmcnt(3)\n" \
  "v_xor_b32 v65, 32, v64\n" \
  "v_mfma_f32_16x16x32_bf16 v[4:7], v[76:79], a[64:67], v[4:7]\n" \
  "v_xor_b32 v66, 64, v64\n" \
  "v_xor_b32 v67, 96, v64\n" \
  "v_add_u32 v70, 0xf800, v68\n" \
  "v_mfma_f32_16x16x32_bf16 v[8:11], v[76:79], a[68:71], v[8:11]\n" \
  "v_add_u32 v71, 0xf800, v69\n" \
  "ds_read_b64_tr_b16 a[0:1], v64 offset:0\n" \
  "ds_read_b64_tr_b16 a[2:3], v64 offset:512\n" \
  "v_mfma_f32_16x16x32_bf16 v[12:15], v[76:79], a[72:75], v[12:15]\n" \
  "ds_read_b64_tr_b16 a[4:5], v65 offset:0\n" \
  "ds_read_b64_tr_b16 a[6:7], v65 offset:512\n" \
  "v_add_f32 v46, v46, v47\n" \
  "v_mfma_f32_16x16x32_bf16 v[16:19], v[76:79], a[76:79], v[16:19]\n" \
  "s_waitcnt lgkmcnt(0)\n" \
  "s_barrier\n" \
  "v_mfma_f32_16x16x32_bf16 v[0:3], v[80:83], a[80:83], v[0:3]\n" \
  "buffer_load_dwordx4 v[76:79], v59, s[36:39], s68 offen offset:1024 sc1\n" \
  "ds_read_b64_tr_b16 a[8:9], v66 offset:0\n" \
  "ds_read_b64_tr_b16 a[10:11], v66 offset:512\n" \
  "v_mfma_f32_16x16x32_bf16 v[4:7], v[80:83], a[84:87], v[4:7]\n" \
  "ds_read_b64_tr_b16 a[12:13], v67 offset:0\n" \
  "ds_read_b64_tr_b16 a[14:15], v67 offset:512\n" \
  "v_add_f32 v44, v44, v56\n" \
  "v_mfma_f32_16x16x32_bf16 v[8:11], v[80:83], a[88:91], v[8:11]\n" \
  "v_rcp_f32 v41, v41\n" \
  "v_add_f32 v44, v44, v46\n" \
  "v_mfma_f32_16x16x32_bf16 v[12:15], v[80:83], a[92:95], v[12:15]\n" \
  "v_rcp_f32 v42, v42\n" \
  "v_mul_f32 v45, v44, v44\n" \
  "v_mfma_f32_16x16x32_bf16 v[16:19], v[80:83], a[96:99], v[16:19]\n" \
  "ds_read_b64_tr_b16 a[16:17], v64 offset:4096\n" \
  "ds_read_b64_tr_b16 a[18:19], v64 offset:4608\n" \
  "v_mfma_f32_16x16x32_bf16 v[0:3], v[84:87], a[100:103], v[0:3]\n" \
  "buffer_load_dwordx4 v[80:83], v59, s[36:39], s68 offen offset:2048 sc1\n" \
  "s_waitcnt lgkmcnt(2)\n" \
  "s_add_u32 m0, s57, 0\n" \
  "v_mfma_f32_16x16x32_bf16 v[4:7], v[84:87], a[104:107], v[4:7]\n" \
  "ds_read_b64_tr_b16 a[20:21], v65 offset:4096\n" \
  "ds_read_b64_tr_b16 a[22:23], v65 offset:4608\n" \
  "v_fma_f32 v45, v45, s55, v57\n" \
  "v_mfma_f32_16x16x32_bf16 v[8:11], v[84:87], a[108:111], v[8:11]\n" \
  "ds_read_b64_tr_b16 a[24:25], v66 offset:4096\n" \
  "ds_read_b64_tr_b16 a[26:27], v66 offset:4608\n" \
  "v_mul_f32 v45, v44, v45\n" \
  "v_mfma_f32_16x16x32_bf16 v[12:15], v[84:87], a[112:115], v[12:15]\n" \
  "buffer_load_dwordx4 v68, s[40:43], s60 offen offset:0 lds\n" \
  "ds_read_b64_tr_b16 a[28:29], v67 offset:4096\n" \
  "ds_read_b64_tr_b16 a[30:31], v67 offset:4608\n" \
  "v_mfma_f32_16x16x32_bf16 v[16:19], v[84:87], a[116:119], v[16:19]\n" \
  "v_rcp_f32 v43, v43\n" \
  "v_mfma_f32_16x16x32_bf16 v[0:3], v[88:91], a[120:123], v[0:3]\n" \
  "v_exp_f32 v45, v45\n" \
  "v_mul_f32 v36, v36, v40\n" \
  "v_mfma_f32_16x16x32_bf16 v[4:7], v[88:91], a[124:127], v[4:7]\n" \
  "buffer_load_dwordx4 v69, s[40:43], s60 offen offset:1024 lds\n" \
  "v_mul_f32 v37, v37, v41\n" \
  "v_add_f32 v45, 1.0, v45\n" \
  "v_mfma_f32_16x16x32_bf16 v[8:11], v[88:91], a[128:131], v[8:11]\n" \
  "v_mul_f32 v38, v38, v42\n" \
  "v_rcp_f32 v45, v45\n" \
  "v_mfma_f32_16x16x32_bf16 v[12:15], v[88:91], a[132:135], v[12:15]\n" \
  "v_mul_f32 v39, v39, v43\n" \
  "v_cvt_pk_bf16_f32 v40, v36, v37\n" \
  "v_mul_f32 v44, v44, v45\n" \
  "v_mfma_f32_16x16x32_bf16 v[16:19], v[88:91], a[136:139], v[16:19]\n" \
  "buffer_load_dwordx4 v70, s[40:43], s60 offen offset:2048 lds\n" \
  "ds_read_b64_tr_b16 a[32:33], v64 offset:8192\n" \
  "v_mfma_f32_16x16x32_bf16 v[0:3], v[92:95], a[140:143], v[0:3]\n" \
  "ds_read_b64_tr_b16 a[34:35], v64 offset:8704\n" \
  "ds_read_b64_tr_b16 a[36:37], v65 offset:8192\n" \
  "v_cvt_pk_bf16_f32 v41, v38, v39\n" \
  "v_mfma_f32_16x16x32_bf16 v[4:7], v[92:95], a[144:147], v[4:7]\n" \
  "ds_read_b64_tr_b16 a[38:39], v65 offset:8704\n" \
  "ds_read_b64_tr_b16 a[40:41], v66 offset:8192\n" \
  "v_cvt_pk_bf16_f32 v45, v44, v44\n" \
  "v_mfma_f32_16x16x32_bf16 v[8:11], v[92:95], a[148:151], v[8:11]\n" \
  "buffer_load_dwordx4 v71, s[40:43], s60 offen offset:3072 lds\n" \
  "ds_read_b64_tr_b16 a[42:43], v66 offset:8704\n" \
  "ds_read_b64_tr_b16 a[44:45], v67 offset:8192\n" \
  "v_mfma_f32_16x16x32_bf16 v[12:15], v[92:95], a[152:155], v[12:15]\n" \
  "ds_read_b64_tr_b16 a[46:47], v67 offset:8704\n" \
  "buffer_store_dwordx2 v[40:41], v61, s[36:39], s52 offen offset:0 sc1\n" \
  "buffer_store_short v45, v62, s[36:39], s52 offen offset:0 sc1\n" \
  "v_mfma_f32_16x16x32_bf16 v[16:19], v[92:95], a[156:159], v[16:19]\n" \
  "ds_read_b128 v[36:39], v58 offset:5344\n" \
  "ds_read_b128 v[40:43], v58 offset:14912\n" \
  "s_waitcnt vmcnt(18)\n" \
  "v_mfma_f32_16x16x32_bf16 v[0:3], v[96:99], a[160:163], v[0:3]\n" \
  "buffer_load_dwordx4 v[84:87], v59, s[36:39], s69 offen offset:0 sc1\n" \
  "ds_read_b128 v[44:47], v58 offset:24480\n" \
  "ds_read_b128 v[48:51], v58 offset:34048\n" \
  "v_mfma_f32_16x16x32_bf16 v[4:7], v[96:99], a[164:167], v[4:7]\n" \
  "s_add_u32 s52, s52, 409600\n" \
  "v_mfma_f32_16x16x32_bf16 v[8:11], v[96:99], a[168:171], v[8:11]\n" \
  "v_mfma_f32_16x16x32_bf16 v[12:15], v[96:99], a[172:175], v[12:15]\n" \
  "buffer_load_dwordx4 v[88:91], v59, s[36:39], s69 offen offset:1024 sc1\n" \
  "v_mfma_f32_16x16x32_bf16 v[16:19], v[96:99], a[176:179], v[16:19]\n" \
  "ds_read_b64_tr_b16 a[48:49], v64 offset:12288\n" \
  "ds_read_b64_tr_b16 a[50:51], v64 offset:12800\n" \
  "s_waitcnt vmcnt(19)\n" \
  "v_mfma_f32_16x16x32_bf16 v[0:3], v[100:103], a[180:183], v[0:3]\n" \
  "ds_read_b64_tr_b16 a[52:53], v65 offset:12288\n" \
  "ds_read_b64_tr_b16 a[54:55], v65 offset:12800\n" \
  "v_mfma_f32_16x16x32_bf16 v[4:7], v[100:103], a[184:187], v[4:7]\n" \
  "buffer_load_dwordx4 v[92:95], v59, s[36:39], s69 offen offset:2048 sc1\n" \
  "s_waitcnt lgkmcnt(15)\n" \
  "s_add_u32 m0, s57, 4096\n" \
  "v_mfma_f32_16x16x32_bf16 v[8:11], v[100:103], a[188:191], v[8:11]\n" \
  "ds_read_b64_tr_b16 a[56:57], v66 offset:12288\n" \
  "ds_read_b64_tr_b16 a[58:59], v66 offset:12800\n" \
  "s_waitcnt lgkmcnt(8)\n" \
  "v_mfma_f32_16x16x32_bf16 v[12:15], v[100:103], a[192:195], v[12:15]\n" \
  "ds_read_b64_tr_b16 a[60:61], v67 offset:12288\n" \
  "ds_read_b64_tr_b16 a[62:63], v67 offset:12800\n" \
  "v_add_f32 v36, v36, v40\n" \
  "v_mfma_f32_16x16x32_bf16 v[16:19], v[100:103], a[196:199], v[16:19]\n" \
  "buffer_load_dwordx4 v68, s[40:43], s61 offen offset:0 lds\n" \
  "v_add_f32 v37, v37, v41\n" \
  "s_waitcnt vmcnt(18)\n" \
  "v_mfma_f32_16x16x32_bf16 v[0:3], v[104:107], a[200:203], v[0:3]\n" \
  "v_add_f32 v38, v38, v42\n" \
  "v_add_f32 v39, v39, v43\n" \
  "v_add_f32 v36, v36, v52\n" \
  "v_mfma_f32_16x16x32_bf16 v[4:7], v[104:107], a[204:207], v[4:7]\n" \
  "v_add_f32 v37, v37, v53\n" \
  "v_add_f32 v38, v38, v54\n" \
  "v_add_f32 v39, v39, v55\n" \
  "v_mfma_f32_16x16x32_bf16 v[8:11], v[104:107], a[208:211], v[8:11]\n" \
  "buffer_load_dwordx4 v69, s[40:43], s61 offen offset:1024 lds\n" \
  "s_waitcnt lgkmcnt(8)\n" \
  "v_add_f32 v44, v44, v48\n" \
  "v_mfma_f32_16x16x32_bf16 v[12:15], v[104:107], a[212:215], v[12:15]\n" \
  "v_add_f32 v45, v45, v49\n" \
  "v_add_f32 v46, v46, v50\n" \
  "v_add_f32 v47, v47, v51\n" \
  "v_mfma_f32_16x16x32_bf16 v[16:19], v[104:107], a[216:219], v[16:19]\n" \
  "ds_read_b64_tr_b16 a[64:65], v64 offset:16384\n" \
  "ds_read_b64_tr_b16 a[66:67], v64 offset:16896\n" \
  "s_waitcnt vmcnt(18)\n" \
  "v_mfma_f32_16x16x32_bf16 v[0:3], v[108:111], a[220:223], v[0:3]\n" \
  "buffer_load_dwordx4 v70, s[40:43], s61 offen offset:2048 lds\n" \
  "ds_read_b64_tr_b16 a[68:69], v65 offset:16384\n" \
  "ds_read_b64_tr_b16 a[70:71], v65 offset:16896\n" \
  "v_mfma_f32_16x16x32_bf16 v[4:7], v[108:111], a[224:227], v[4:7]\n" \
  "ds_read_b64_tr_b16 a[72:73], v66 offset:16384\n" \
  "ds_read_b64_tr_b16 a[74:75], v66 offset:16896\n" \
  "v_add_f32 v36, v36, v44\n" \
  "v_mfma_f32_16x16x32_bf16 v[8:11], v[108:111], a[228:231], v[8:11]\n" \
  "ds_read_b64_tr_b16 a[76:77], v67 offset:16384\n" \
  "ds_read_b64_tr_b16 a[78:79], v67 offset:16896\n" \
  "v_add_f32 v37, v37, v45\n" \
  "v_mfma_f32_16x16x32_bf16 v[12:15], v[108:111], a[232:235], v[12:15]\n" \
  "buffer_load_dwordx4 v71, s[40:43], s61 offen offset:3072 lds\n" \
  "v_add_f32 v38, v38, v46\n" \
  "v_add_f32 v39, v39, v47\n" \
  "v_mfma_f32_16x16x32_bf16 v[16:19], v[108:111], a[236:239], v[16:19]\n" \
  "v_mul_f32 v40, v36, v36\n" \
  "v_mul_f32 v41, v37, v37\n" \
  "s_waitcnt vmcnt(19)\n" \
  "v_mfma_f32_16x16x32_bf16 v[0:3], v[112:115], a[240:243], v[0:3]\n" \
  "v_mul_f32 v42, v38, v38\n" \
  "v_mul_f32 v43, v39, v39\n" \
  "v_fma_f32 v40, v40, s55, v57\n" \
  "v_mfma_f32_16x16x32_bf16 v[4:7], v[112:115], a[244:247], v[4:7]\n" \
  "buffer_load_dwordx4 v[108:111], v59, s[36:39], s48 offen offset:0 sc1\n" \
  "s_add_u32 m0, s57, 8192\n" \
  "v_mfma_f32_16x16x32_bf16 v[8:11], v[112:115], a[248:251], v[8:11]\n" \
  "v_fma_f32 v41, v41, s55, v57\n" \
  "v_fma_f32 v42, v42, s55, v57\n" \
  "v_fma_f32 v43, v43, s55, v57\n" \
  "v_mfma_f32_16x16x32_bf16 v[12:15], v[112:115], a[252:255], v[12:15]\n" \
  "v_mul_f32 v40, v36, v40\n" \
  "v_mul_f32 v41, v37, v41\n" \
  "v_mul_f32 v42, v38, v42\n" \
  "v_mfma_f32_16x16x32_bf16 v[16:19], v[112:115], v[192:195], v[16:19]\n" \
  "buffer_load_dwordx4 v68, s[40:43], s62 offen offset:0 lds\n" \
  "ds_read_b64_tr_b16 a[80:81], v64 offset:20480\n" \
  "s_waitcnt vmcnt(20)\n" \
  "v_mfma_f32_16x16x32_bf16 v[0:3], v[116:119], v[196:199], v[0:3]\n" \
  "ds_read_b64_tr_b16 a[82:83], v64 offset:20992\n" \
  "ds_read_b64_tr_b16 a[84:85], v65 offset:20480\n" \
  "v_mul_f32 v43, v39, v43\n" \
  "v_mfma_f32_16x16x32_bf16 v[4:7], v[116:119], v[200:203], v[4:7]\n" \
  "ds_read_b64_tr_b16 a[86:87], v65 offset:20992\n" \
  "ds_read_b64_tr_b16 a[88:89], v66 offset:20480\n" \
  "v_mfma_f32_16x16x32_bf16 v[8:11], v[116:119], v[204:207], v[8:11]\n" \
  "buffer_load_dwordx4 v69, s[40:43], s62 offen offset:1024 lds\n" \
  "ds_read_b64_tr_b16 a[90:91], v66 offset:20992\n" \
  "ds_read_b64_tr_b16 a[92:93], v67 offset:20480\n" \
  "v_mfma_f32_16x16x32_bf16 v[12:15], v[116:119], v[208:211], v[12:15]\n" \
  "ds_read_b64_tr_b16 a[94:95], v67 offset:20992\n" \
  "v_exp_f32 v40, v40\n" \
  "v_mfma_f32_16x16x32_bf16 v[16:19], v[116:119], v[212:215], v[16:19]\n" \
  "v_exp_f32 v41, v41\n" \
  "s_waitcnt vmcnt(20)\n" \
  "v_mfma_f32_16x16x32_bf16 v[0:3], v[120:123], v[216:219], v[0:3]\n" \
  "buffer_load_dwordx4 v70, s[40:43], s62 offen offset:2048 lds\n" \
  "v_exp_f32 v42, v42\n" \
  "v_mfma_f32_16x16x32_bf16 v[4:7], v[120:123], v[220:223], v[4:7]\n" \
  "v_exp_f32 v43, v43\n" \
  "v_add_f32 v40, 1.0, v40\n" \
  "v_mfma_f32_16x16x32_bf16 v[8:11], v[120:123], v[224:227], v[8:11]\n" \
  "v_add_f32 v41, 1.0, v41\n" \
  "v_add_f32 v42, 1.0, v42\n" \
  "v_add_f32 v43, 1.0, v43\n" \
  "v_mfma_f32_16x16x32_bf16 v[12:15], v[120:123], v[228:231], v[12:15]\n" \
  "buffer_load_dwordx4 v71, s[40:43], s62 offen offset:3072 lds\n" \
  "v_rcp_f32 v40, v40\n" \
  "v_mfma_f32_16x16x32_bf16 v[16:19], v[120:123], v[232:235], v[16:19]\n" \
  "ds_read_b64_tr_b16 a[96:97], v64 offset:24576\n" \
  "ds_read_b64_tr_b16 a[98:99], v64 offset:25088\n" \
  "s_waitcnt vmcnt(21)\n" \
  "v_mfma_f32_16x16x32_bf16 v[0:3], v[124:127], v[236:239], v[0:3]\n" \
  "ds_read_b64_tr_b16 a[100:101], v65 offset:24576\n" \
  "ds_read_b64_tr_b16 a[102:103], v65 offset:25088\n" \
  "v_mfma_f32_16x16x32_bf16 v[4:7], v[124:127], v[240:243], v[4:7]\n" \
  "buffer_load_dwordx4 v[112:115], v59, s[36:39], s48 offen offset:1024 sc1\n" \
  "ds_read_b64_tr_b16 a[104:105], v66 offset:24576\n" \
  "ds_read_b64_tr_b16 a[106:107], v66 offset:25088\n" \
  "v_mfma_f32_16x16x32_bf16 v[8:11], v[124:127], v[244:247], v[8:11]\n" \
  "ds_read_b64_tr_b16 a[108:109], v67 offset:24576\n" \
  "ds_read_b64_tr_b16 a[110:111], v67 offset:25088\n" \
  "v_mfma_f32_16x16x32_bf16 v[12:15], v[124:127], v[248:251], v[12:15]\n" \
  "v_rcp_f32 v41, v41\n" \
  "v_mfma_f32_16x16x32_bf16 v[16:19], v[124:127], v[252:255], v[16:19]\n" \
  "buffer_load_dwordx4 v[116:119], v59, s[36:39], s48 offen offset:2048 sc1\n" \
  "s_mov_b32 m0, s56\n" \
  "s_waitcnt vmcnt(22)\n" \
  "v_mfma_f32_16x16x32_bf16 v[208:211], v[128:131], v[72:75], 0\n" \
  "v_rcp_f32 v42, v42\n" \
  "v_mfma_f32_16x16x32_bf16 v[212:215], v[132:135], v[72:75], 0\n" \
  "ds_write_addtid_b32 v0 offset:0\n" \
  "ds_write_addtid_b32 v1 offset:544\n" \
  "v_mfma_f32_16x16x32_bf16 v[216:219], v[136:139], v[72:75], 0\n" \
  "buffer_load_dwordx4 v[120:123], v59, s[36:39], s49 offen offset:0 sc1\n" \
  "ds_write_addtid_b32 v2 offset:256\n" \
  "ds_write_addtid_b32 v3 offset:800\n" \
  "v_mfma_f32_16x16x32_bf16 v[220:223], v[140:143], v[72:75], 0\n" \
  "ds_write_addtid_b32 v4 offset:1056\n" \
  "ds_write_addtid_b32 v5 offset:1600\n" \
  "v_mfma_f32_16x16x32_bf16 v[224:227], v[144:147], v[72:75], 0\n" \
  "ds_write_addtid_b32 v6 offset:1312\n" \
  "ds_write_addtid_b32 v7 offset:1856\n" \
  "v_mfma_f32_16x16x32_bf16 v[228:231], v[148:151], v[72:75], 0\n" \
  "buffer_load_dwordx4 v[124:127], v59, s[36:39], s49 offen offset:1024 sc1\n" \
  "ds_write_addtid_b32 v8 offset:2112\n" \
  "ds_write_addtid_b32 v9 offset:2656\n" \
  "v_mfma_f32_16x16x32_bf16 v[232:235], v[152:155], v[72:75], 0\n" \
  "ds_write_addtid_b32 v10 offset:2368\n" \
  "ds_write_addtid_b32 v11 offset:2912\n" \
  "v_rcp_f32 v43, v43\n" \
  "v_mfma_f32_16x16x32_bf16 v[236:239], v[156:159], v[72:75], 0\n" \
  "ds_write_addtid_b32 v12 offset:3168\n" \
  "ds_write_addtid_b32 v13 offset:3712\n" \
  "v_mul_f32 v36, v36, v40\n" \
  "s_waitcnt vmcnt(23)\n" \
  "v_mfma_f32_16x16x32_bf16 v[208:211], v[160:163], v[76:79], v[208:211]\n" \
  "v_mul_f32 v37, v37, v41\n" \
  "v_mul_f32 v38, v38, v42\n" \
  "v_mul_f32 v39, v39, v43\n" \
  "v_cvt_pk_bf16_f32 v40, v36, v37\n" \
  "v_cvt_pk_bf16_f32 v41, v38, v39\n" \
  "buffer_store_dwordx2 v[40:41], v61, s[36:39], s52 offen offset:0 sc1\n" \
  "v_mfma_f32_16x16x32_bf16 v[212:215], v[164:167], v[76:79], v[212:215]\n" \
  "buffer_load_dwordx4 v[192:195], v59, s[36:39], s49 offen offset:2048 sc1\n" \
  "ds_write_addtid_b32 v14 offset:3424\n" \
  "ds_write_addtid_b32 v15 offset:3968\n" \
  "v_mfma_f32_16x16x32_bf16 v[216:219], v[168:171], v[76:79], v[216:219]\n" \
  "ds_write_addtid_b32 v16 offset:4224\n" \
  "ds_write_addtid_b32 v17 offset:4512\n" \
  "s_add_u32 s52, s52, 409600\n" \
  "v_mfma_f32_16x16x32_bf16 v[220:223], v[172:175], v[76:79], v[220:223]\n" \
  "ds_write_addtid_b32 v18 offset:4800\n" \
  "ds_write_addtid_b32 v19 offset:5088\n" \
  "s_waitcnt vmcnt(20)\n" \
  "v_mfma_f32_16x16x32_bf16 v[224:227], v[176:179], v[76:79], v[224:227]\n" \
  "buffer_load_dwordx4 v[96:99], v59, s[36:39], s70 offen offset:0 sc1\n" \
  "s_add_u32 m0, s57, 12288\n" \
  "v_mfma_f32_16x16x32_bf16 v[228:231], v[180:183], v[76:79], v[228:231]\n" \
  "ds_read_b64_tr_b16 a[112:113], v64 offset:0\n" \
  "ds_read_b64_tr_b16 a[114:115], v64 offset:512\n" \
  "v_mfma_f32_16x16x32_bf16 v[232:235], v[184:187], v[76:79], v[232:235]\n" \
  "ds_read_b64_tr_b16 a[116:117], v65 offset:0\n" \
  "ds_read_b64_tr_b16 a[118:119], v65 offset:512\n" \
  "v_mfma_f32_16x16x32_bf16 v[236:239], v[188:191], v[76:79], v[236:239]\n" \
  "buffer_load_dwordx4 v68, s[40:43], s63 offen offset:0 lds\n" \
  "s_waitcnt lgkmcnt(15)\n" \
  "v_mfma_f32_16x16x32_bf16 v[208:211], a[0:3], v[80:83], v[208:211]\n" \
  "ds_read_b64_tr_b16 a[120:121], v66 offset:0\n" \
  "ds_read_b64_tr_b16 a[122:123], v66 offset:512\n" \
  "v_mfma_f32_16x16x32_bf16 v[212:215], a[4:7], v[80:83], v[212:215]\n" \
  "s_waitcnt lgkmcnt(0)\n" \
  "s_barrier\n" \
  "ds_read_b128 v[36:39], v58 offset:0\n" \
  "v_mfma_f32_16x16x32_bf16 v[216:219], a[8:11], v[80:83], v[216:219]\n" \
  "buffer_load_dwordx4 v69, s[40:43], s63 offen offset:1024 lds\n" \
  "ds_read_b128 v[40:43], v58 offset:9568\n" \
  "ds_read_b128 v[44:47], v58 offset:19136\n" \
  "v_mfma_f32_16x16x32_bf16 v[220:223], a[12:15], v[80:83], v[220:223]\n" \
  "ds_read_b128 v[48:51], v58 offset:28704\n" \
  "ds_read_b64_tr_b16 a[124:125], v67 offset:0\n" \
  "v_mfma_f32_16x16x32_bf16 v[224:227], a[16:19], v[80:83], v[224:227]\n" \
  "ds_read_b64_tr_b16 a[126:127], v67 offset:512\n" \
  "ds_read_b32 v252, v60 offset:0\n" \
  "v_mfma_f32_16x16x32_bf16 v[228:231], a[20:23], v[80:83], v[228:231]\n" \
  "buffer_load_dwordx4 v70, s[40:43], s63 offen offset:2048 lds\n" \
  "ds_read_b32 v253, v60 offset:9568\n" \
  "ds_read_b32 v254, v60 offset:19136\n" \
  "v_mfma_f32_16x16x32_bf16 v[232:235], a[24:27], v[80:83], v[232:235]\n" \
  "ds_read_b32 v255, v60 offset:28704\n" \
  "v_mfma_f32_16x16x32_bf16 v[236:239], a[28:31], v[80:83], v[236:239]\n" \
  "s_waitcnt vmcnt(21)\n" \
  "v_mfma_f32_16x16x32_bf16 v[208:211], a[32:35], v[84:87], v[208:211]\n" \
  "buffer_load_dwordx4 v71, s[40:43], s63 offen offset:3072 lds\n" \
  "s_add_u32 m0, s57, 16384\n" \
  "v_mfma_f32_16x16x32_bf16 v[212:215], a[36:39], v[84:87], v[212:215]\n" \
  "v_mfma_f32_16x16x32_bf16 v[216:219], a[40:43], v[84:87], v[216:219]\n" \
  "s_waitcnt lgkmcnt(8)\n" \
  "v_add_f32 v36, v36, v40\n" \
  "v_add_f32 v37, v37, v41\n" \
  "v_mfma_f32_16x16x32_bf16 v[220:223], a[44:47], v[84:87], v[220:223]\n" \
  "buffer_load_dwordx4 v68, s[40:43], s64 offen offset:0 lds\n" \
  "v_add_f32 v38, v38, v42\n" \
  "v_mfma_f32_16x16x32_bf16 v[224:227], a[48:51], v[84:87], v[224:227]\n" \
  "s_waitcnt vmcnt(17)\n" \
  "ds_read_b64_tr_b16 a[128:129], v64 offset:4096\n" \
  "ds_read_b64_tr_b16 a[130:131], v64 offset:4608\n" \
  "v_mfma_f32_16x16x32_bf16 v[228:231], a[52:55], v[84:87], v[228:231]\n" \
  "ds_read_b64_tr_b16 a[132:133], v65 offset:4096\n" \
  "ds_read_b64_tr_b16 a[134:135], v65 offset:4608\n" \
  "v_add_f32 v39, v39, v43\n" \
  "v_mfma_f32_16x16x32_bf16 v[232:235], a[56:59], v[84:87], v[232:235]\n" \
  "buffer_load_dwordx4 v69, s[40:43], s64 offen offset:1024 lds\n" \
  "ds_read_b64_tr_b16 a[136:137], v66 offset:4096\n" \
  "ds_read_b64_tr_b16 a[138:139], v66 offset:4608\n" \
  "v_mfma_f32_16x16x32_bf16 v[236:239], a[60:63], v[84:87], v[236:239]\n" \
  "ds_read_b64_tr_b16 a[140:141], v67 offset:4096\n" \
  "v_mfma_f32_16x16x32_bf16 v[208:211], a[64:67], v[88:91], v[208:211]\n" \
  "ds_read_b64_tr_b16 a[142:143], v67 offset:4608\n" \
  "v_add_f32 v36, v36, v52\n" \
  "v_add_f32 v37, v37, v53\n" \
  "v_mfma_f32_16x16x32_bf16 v[212:215], a[68:71], v[88:91], v[212:215]\n" \
  "buffer_load_dwordx4 v70, s[40:43], s64 offen offset:2048 lds\n" \
  "v_add_f32 v38, v38, v54\n" \
  "v_add_f32 v39, v39, v55\n" \
  "v_mfma_f32_16x16x32_bf16 v[216:219], a[72:75], v[88:91], v[216:219]\n" \
  "s_waitcnt lgkmcnt(14)\n" \
  "v_add_f32 v44, v44, v48\n" \
  "v_add_f32 v45, v45, v49\n" \
  "v_mfma_f32_16x16x32_bf16 v[220:223], a[76:79], v[88:91], v[220:223]\n" \
  "v_add_f32 v46, v46, v50\n" \
  "v_add_f32 v47, v47, v51\n" \
  "v_mfma_f32_16x16x32_bf16 v[224:227], a[80:83], v[88:91], v[224:227]\n" \
  "buffer_load_dwordx4 v71, s[40:43], s64 offen offset:3072 lds\n" \
  "v_add_f32 v36, v36, v44\n" \
  "v_add_f32 v37, v37, v45\n" \
  "v_mfma_f32_16x16x32_bf16 v[228:231], a[84:87], v[88:91], v[228:231]\n" \
  "v_add_f32 v38, v38, v46\n" \
  "v_add_f32 v39, v39, v47\n" \
  "v_mul_f32 v40, v36, v36\n" \
  "v_mfma_f32_16x16x32_bf16 v[232:235], a[88:91], v[88:91], v[232:235]\n" \
  "v_mul_f32 v41, v37, v37\n" \
  "v_mul_f32 v42, v38, v38\n" \
  "v_mul_f32 v43, v39, v39\n" \
  "v_mfma_f32_16x16x32_bf16 v[236:239], a[92:95], v[88:91], v[236:239]\n" \
  "buffer_load_dwordx4 v[100:103], v59, s[36:39], s70 offen offset:1024 sc1\n" \
  "v_mfma_f32_16x16x32_bf16 v[208:211], a[96:99], v[92:95], v[208:211]\n" \
  "s_add_u32 m0, s57, 20480\n" \
  "v_fma_f32 v40, v40, s55, v57\n" \
  "v_mfma_f32_16x16x32_bf16 v[212:215], a[100:103], v[92:95], v[212:215]\n" \
  "v_fma_f32 v41, v41, s55, v57\n" \
  "v_fma_f32 v42, v42, s55, v57\n" \
  "v_fma_f32 v43, v43, s55, v57\n" \
  "v_mfma_f32_16x16x32_bf16 v[216:219], a[104:107], v[92:95], v[216:219]\n" \
  "buffer_load_dwordx4 v68, s[40:43], s65 offen offset:0 lds\n" \
  "v_mul_f32 v40, v36, v40\n" \
  "v_mul_f32 v41, v37, v41\n" \
  "v_mfma_f32_16x16x32_bf16 v[220:223], a[108:111], v[92:95], v[220:223]\n" \
  "s_waitcnt vmcnt(17)\n" \
  "ds_read_b64_tr_b16 a[144:145], v64 offset:8192\n" \
  "v_mfma_f32_16x16x32_bf16 v[0:3], v[128:131], v[108:111], 0\n" \
  "ds_read_b64_tr_b16 a[146:147], v64 offset:8704\n" \
  "ds_read_b64_tr_b16 a[148:149], v65 offset:8192\n" \
  "v_mul_f32 v42, v38, v42\n" \
  "v_mfma_f32_16x16x32_bf16 v[4:7], v[132:135], v[108:111], 0\n" \
  "buffer_load_dwordx4 v69, s[40:43], s65 offen offset:1024 lds\n" \
  "ds_read_b64_tr_b16 a[150:151], v65 offset:8704\n" \
  "ds_read_b64_tr_b16 a[152:153], v66 offset:8192\n" \
  "v_mfma_f32_16x16x32_bf16 v[8:11], v[136:139], v[108:111], 0\n" \
  "ds_read_b64_tr_b16 a[154:155], v66 offset:8704\n" \
  "ds_read_b64_tr_b16 a[156:157], v67 offset:8192\n" \
  "v_mul_f32 v43, v39, v43\n" \
  "v_mfma_f32_16x16x32_bf16 v[12:15], v[140:143], v[108:111], 0\n" \
  "ds_read_b64_tr_b16 a[158:159], v67 offset:8704\n" \
  "v_exp_f32 v40, v40\n" \
  "v_mfma_f32_16x16x32_bf16 v[16:19], v[144:147], v[108:111], 0\n" \
  "buffer_load_dwordx4 v70, s[40:43], s65 offen offset:2048 lds\n" \
  "v_exp_f32 v41, v41\n" \
  "v_mfma_f32_16x16x32_bf16 v[20:23], v[148:151], v[108:111], 0\n" \
  "v_exp_f32 v42, v42\n" \
  "s_waitcnt lgkmcnt(15)\n" \
  "v_mfma_f32_16x16x32_bf16 v[24:27], v[152:155], v[108:111], 0\n" \
  "v_exp_f32 v43, v43\n" \
  "v_add_f32 v40, 1.0, v40\n" \
  "v_mfma_f32_16x16x32_bf16 v[28:31], v[156:159], v[108:111], 0\n" \
  "buffer_load_dwordx4 v71, s[40:43], s65 offen offset:3072 lds\n" \
  "v_add_f32 v41, 1.0, v41\n" \
  "s_waitcnt vmcnt(19)\n" \
  "v_mfma_f32_16x16x32_bf16 v[0:3], v[160:163], v[112:115], v[0:3]\n" \
  "v_add_f32 v42, 1.0, v42\n" \
  "v_add_f32 v43, 1.0, v43\n" \
  "v_add_f32 v252, v252, v253\n" \
  "v_mfma_f32_16x16x32_bf16 v[4:7], v[164:167], v[112:115], v[4:7]\n" \
  "v_rcp_f32 v40, v40\n" \
  "v_add_f32 v254, v254, v255\n" \
  "v_mfma_f32_16x16x32_bf16 v[8:11], v[168:171], v[112:115], v[8:11]\n" \
  "buffer_load_dwordx4 v[104:107], v59, s[36:39], s70 offen offset:2048 sc1\n" \
  "s_add_u32 m0, s57, 24576\n" \
  "v_mfma_f32_16x16x32_bf16 v[12:15], v[172:175], v[112:115], v[12:15]\n" \
  "v_rcp_f32 v41, v41\n" \
  "v_add_f32 v252, v252, v56\n" \
  "v_mfma_f32_16x16x32_bf16 v[16:19], v[176:179], v[112:115], v[16:19]\n" \
  "v_rcp_f32 v42, v42\n" \
  "v_add_f32 v252, v252, v254\n" \
  "v_mfma_f32_16x16x32_bf16 v[20:23], v[180:183], v[112:115], v[20:23]\n" \
  "buffer_load_dwordx4 v68, s[40:43], s66 offen offset:0 lds\n" \
  "v_rcp_f32 v43, v43\n" \
  "v_mfma_f32_16x16x32_bf16 v[24:27], v[184:187], v[112:115], v[24:27]\n" \
  "v_mul_f32 v36, v36, v40\n" \
  "v_mul_f32 v37, v37, v41\n" \
  "v_mul_f32 v38, v38, v42\n" \
  "v_mfma_f32_16x16x32_bf16 v[28:31], v[188:191], v[112:115], v[28:31]\n" \
  "v_mul_f32 v39, v39, v43\n" \
  "v_cvt_pk_bf16_f32 v40, v36, v37\n" \
  "s_waitcnt vmcnt(20)\n" \
  "v_mfma_f32_16x16x32_bf16 v[0:3], a[0:3], v[116:119], v[0:3]\n" \
  "buffer_load_dwordx4 v69, s[40:43], s66 offen offset:1024 lds\n" \
  "v_cvt_pk_bf16_f32 v41, v38, v39\n" \
  "v_mul_f32 v253, v252, v252\n" \
  "v_mfma_f32_16x16x32_bf16 v[4:7], a[4:7], v[116:119], v[4:7]\n" \
  "v_fma_f32 v253, v253, s55, v57\n" \
  "v_mul_f32 v253, v252, v253\n" \
  "v_mfma_f32_16x16x32_bf16 v[8:11], a[8:11], v[116:119], v[8:11]\n" \
  "v_exp_f32 v253, v253\n" \
  "v_mfma_f32_16x16x32_bf16 v[12:15], a[12:15], v[116:119], v[12:15]\n" \
  "buffer_load_dwordx4 v70, s[40:43], s66 offen offset:2048 lds\n" \
  "v_add_f32 v253, 1.0, v253\n" \
  "v_mfma_f32_16x16x32_bf16 v[16:19], a[16:19], v[116:119], v[16:19]\n" \
  "v_rcp_f32 v253, v253\n" \
  "v_mfma_f32_16x16x32_bf16 v[20:23], a[20:23], v[116:119], v[20:23]\n" \
  "v_mul_f32 v252, v252, v253\n" \
  "v_cvt_pk_bf16_f32 v253, v252, v252\n" \
  "v_mfma_f32_16x16x32_bf16 v[24:27], a[24:27], v[116:119], v[24:27]\n" \
  "buffer_load_dwordx4 v71, s[40:43], s66 offen offset:3072 lds\n" \
  "v_mfma_f32_16x16x32_bf16 v[28:31], a[28:31], v[116:119], v[28:31]\n" \
  "s_waitcnt vmcnt(22)\n" \
  "v_mfma_f32_16x16x32_bf16 v[0:3], a[32:35], v[120:123], v[0:3]\n" \
  "v_mfma_f32_16x16x32_bf16 v[4:7], a[36:39], v[120:123], v[4:7]\n" \
  "buffer_load_dwordx4 v[196:199], v59, s[36:39], s50 offen offset:0 sc1\n" \
  "v_mfma_f32_16x16x32_bf16 v[8:11], a[40:43], v[120:123], v[8:11]\n" \
  "v_mfma_f32_16x16x32_bf16 v[12:15], a[44:47], v[120:123], v[12:15]\n" \
  "v_mfma_f32_16x16x32_bf16 v[16:19], a[48:51], v[120:123], v[16:19]\n" \
  "buffer_load_dwordx4 v[200:203], v59, s[36:39], s50 offen offset:1024 sc1\n" \
  "v_mfma_f32_16x16x32_bf16 v[20:23], a[52:55], v[120:123], v[20:23]\n" \
  "v_mfma_f32_16x16x32_bf16 v[24:27], a[56:59], v[120:123], v[24:27]\n" \
  "s_waitcnt vmcnt(16)\n" \
  "ds_read_b64_tr_b16 a[160:161], v64 offset:12288\n" \
  "ds_read_b64_tr_b16 a[162:163], v64 offset:12800\n" \
  "v_mfma_f32_16x16x32_bf16 v[28:31], a[60:63], v[120:123], v[28:31]\n" \
  "buffer_load_dwordx4 v[204:207], v59, s[36:39], s50 offen offset:2048 sc1\n" \
  "ds_read_b64_tr_b16 a[164:165], v65 offset:12288\n" \
  "s_waitcnt vmcnt(24)\n" \
  "v_mfma_f32_16x16x32_bf16 v[0:3], a[64:67], v[124:127], v[0:3]\n" \
  "s_add_u32 s59, s59, 0x10000\n" \
  "buffer_load_dword v63, v61, s[36:39], s59 offen offset:0 sc1\n" \
  "v_add_u32 v59, 819200, v59\n" \
  "v_mfma_f32_16x16x32_bf16 v[4:7], a[68:71], v[124:127], v[4:7]\n" \
  "ds_read_b64_tr_b16 a[166:167], v65 offset:12800\n" \
  "ds_read_b64_tr_b16 a[168:169], v66 offset:12288\n" \
  "buffer_store_short v253, v62, s[36:39], s52 offen offset:0 sc1\n" \
  "v_mfma_f32_16x16x32_bf16 v[8:11], a[72:75], v[124:127], v[8:11]\n" \
  "v_and_b32 v58, 0x3ff, v59\n" \
  "v_add_u32 v58, s99, v58\n" \
  "v_add_u32 v46, s98, v58\n" \
  "v_mfma_f32_16x16x32_bf16 v[12:15], a[76:79], v[124:127], v[12:15]\n" \
  "ds_read_b64 v[56:57], v46 offset:28680\n" \
  "ds_read_b64_tr_b16 a[170:171], v66 offset:12800\n" \
  "buffer_store_dwordx2 v[40:41], v61, s[36:39], s52 offen offset:0 sc1\n" \
  "v_mfma_f32_16x16x32_bf16 v[16:19], a[80:83], v[124:127], v[16:19]\n" \
  "buffer_load_dwordx4 v[72:75], v59, s[36:39], s68 offen offset:0 sc1\n" \
  "ds_read_b64_tr_b16 a[172:173], v67 offset:12288\n" \
  "ds_read_b64_tr_b16 a[174:175], v67 offset:12800\n" \
  "v_mfma_f32_16x16x32_bf16 v[20:23], a[84:87], v[124:127], v[20:23]\n" \
  "v_mfma_f32_16x16x32_bf16 v[24:27], a[88:91], v[124:127], v[24:27]\n" \
  "v_mfma_f32_16x16x32_bf16 v[28:31], a[92:95], v[124:127], v[28:31]\n" \
  "buffer_load_dwordx4 v[76:79], v59, s[36:39], s68 offen offset:1024 sc1\n" \
  "s_waitcnt vmcnt(27)\n" \
  "v_mfma_f32_16x16x32_bf16 v[0:3], a[96:99], v[192:195], v[0:3]\n" \
  "s_waitcnt lgkmcnt(0)\n" \
  "s_barrier\n" \
  "s_mov_b32 m0, s58\n" \
  "v_mfma_f32_16x16x32_bf16 v[4:7], a[100:103], v[192:195], v[4:7]\n" \
  "v_mfma_f32_16x16x32_bf16 v[8:11], a[104:107], v[192:195], v[8:11]\n" \
  "buffer_load_dwordx4 v[80:83], v59, s[36:39], s68 offen offset:2048 sc1\n" \
  "s_waitcnt vmcnt(19)\n" \
  "ds_read_b64_tr_b16 a[176:177], v64 offset:16384\n" \
  "v_mfma_f32_16x16x32_bf16 v[12:15], a[108:111], v[192:195], v[12:15]\n" \
  "ds_read_b64_tr_b16 a[178:179], v64 offset:16896\n" \
  "ds_read_b64_tr_b16 a[180:181], v65 offset:16384\n" \
  "v_mfma_f32_16x16x32_bf16 v[224:227], a[112:115], v[92:95], v[224:227]\n" \
  "ds_read_b64_tr_b16 a[182:183], v65 offset:16896\n" \
  "ds_read_b64_tr_b16 a[184:185], v66 offset:16384\n" \
  "v_mfma_f32_16x16x32_bf16 v[228:231], a[116:119], v[92:95], v[228:231]\n" \
  "buffer_load_dwordx4 v[84:87], v59, s[36:39], s69 offen offset:0 sc1\n" \
  "ds_read_b64_tr_b16 a[186:187], v66 offset:16896\n" \
  "ds_read_b64_tr_b16 a[188:189], v67 offset:16384\n" \
  "v_mfma_f32_16x16x32_bf16 v[232:235], a[120:123], v[92:95], v[232:235]\n" \
  "ds_read_b64_tr_b16 a[190:191], v67 offset:16896\n" \
  "v_mfma_f32_16x16x32_bf16 v[236:239], a[124:127], v[92:95], v[236:239]\n" \
  "v_mfma_f32_16x16x32_bf16 v[208:211], a[128:131], v[96:99], v[208:211]\n" \
  "buffer_load_dwordx4 v[88:91], v59, s[36:39], s69 offen offset:1024 sc1\n" \
  "v_mfma_f32_16x16x32_bf16 v[212:215], a[132:135], v[96:99], v[212:215]\n" \
  "v_mfma_f32_16x16x32_bf16 v[216:219], a[136:139], v[96:99], v[216:219]\n" \
  "v_mfma_f32_16x16x32_bf16 v[220:223], a[140:143], v[96:99], v[220:223]\n" \
  "buffer_load_dwordx4 v[92:95], v59, s[36:39], s69 offen offset:2048 sc1\n" \
  "v_mfma_f32_16x16x32_bf16 v[224:227], a[144:147], v[96:99], v[224:227]\n" \
  "v_mfma_f32_16x16x32_bf16 v[228:231], a[148:151], v[96:99], v[228:231]\n" \
  "v_mfma_f32_16x16x32_bf16 v[232:235], a[152:155], v[96:99], v[232:235]\n" \
  "v_mfma_f32_16x16x32_bf16 v[236:239], a[156:159], v[96:99], v[236:239]\n" \
  "buffer_load_dwordx4 v[96:99], v59, s[36:39], s70 offen offset:0 sc1\n" \
  "s_waitcnt vmcnt(22)\n" \
  "v_mfma_f32_16x16x32_bf16 v[208:211], a[160:163], v[100:103], v[208:211]\n" \
  "s_waitcnt vmcnt(18)\n" \
  "ds_read_b64_tr_b16 a[192:193], v64 offset:20480\n" \
  "ds_read_b64_tr_b16 a[194:195], v64 offset:20992\n" \
  "v_mfma_f32_16x16x32_bf16 v[212:215], a[164:167], v[100:103], v[212:215]\n" \
  "ds_read_b64_tr_b16 a[196:197], v65 offset:20480\n" \
  "ds_read_b64_tr_b16 a[198:199], v65 offset:20992\n" \
  "v_mfma_f32_16x16x32_bf16 v[216:219], a[168:171], v[100:103], v[216:219]\n" \
  "ds_read_b64_tr_b16 a[200:201], v66 offset:20480\n" \
  "ds_read_b64_tr_b16 a[202:203], v66 offset:20992\n" \
  "v_mfma_f32_16x16x32_bf16 v[220:223], a[172:175], v[100:103], v[220:223]\n" \
  "ds_read_b64_tr_b16 a[204:205], v67 offset:20480\n" \
  "ds_read_b64_tr_b16 a[206:207], v67 offset:20992\n" \
  "s_waitcnt lgkmcnt(8)\n" \
  "v_mfma_f32_16x16x32_bf16 v[224:227], a[176:179], v[100:103], v[224:227]\n" \
  "s_waitcnt vmcnt(13)\n" \
  "ds_read_b64_tr_b16 a[208:209], v64 offset:24576\n" \
  "ds_read_b64_tr_b16 a[210:211], v64 offset:25088\n" \
  "v_mfma_f32_16x16x32_bf16 v[228:231], a[180:183], v[100:103], v[228:231]\n" \
  "ds_read_b64_tr_b16 a[212:213], v65 offset:24576\n" \
  "ds_read_b64_tr_b16 a[214:215], v65 offset:25088\n" \
  "v_mfma_f32_16x16x32_bf16 v[232:235], a[184:187], v[100:103], v[232:235]\n" \
  "ds_read_b64_tr_b16 a[216:217], v66 offset:24576\n" \
  "ds_read_b64_tr_b16 a[218:219], v66 offset:25088\n" \
  "v_mfma_f32_16x16x32_bf16 v[236:239], a[188:191], v[100:103], v[236:239]\n" \
  "ds_read_b64_tr_b16 a[220:221], v67 offset:24576\n" \
  "ds_read_b64_tr_b16 a[222:223], v67 offset:25088\n" \
  "s_waitcnt lgkmcnt(8)\n" \
  "v_mfma_f32_16x16x32_bf16 v[208:211], a[192:195], v[104:107], v[208:211]\n" \
  "buffer_load_dwordx4 v[100:103], v59, s[36:39], s70 offen offset:1024 sc1\n" \
  "v_mfma_f32_16x16x32_bf16 v[212:215], a[196:199], v[104:107], v[212:215]\n" \
  "v_mfma_f32_16x16x32_bf16 v[216:219], a[200:203], v[104:107], v[216:219]\n" \
  "v_mfma_f32_16x16x32_bf16 v[220:223], a[204:207], v[104:107], v[220:223]\n" \
  "s_waitcnt lgkmcnt(0)\n" \
  "v_mfma_f32_16x16x32_bf16 v[224:227], a[208:211], v[104:107], v[224:227]\n" \
  "v_mfma_f32_16x16x32_bf16 v[228:231], a[212:215], v[104:107], v[228:231]\n" \
  "v_mfma_f32_16x16x32_bf16 v[232:235], a[216:219], v[104:107], v[232:235]\n" \
  "v_mfma_f32_16x16x32_bf16 v[236:239], a[220:223], v[104:107], v[236:239]\n" \
  "buffer_load_dwordx4 v[104:107], v59, s[36:39], s70 offen offset:2048 sc1\n" \
  "v_mfma_f32_16x16x32_bf16 v[16:19], a[112:115], v[192:195], v[16:19]\n" \
  "v_mfma_f32_16x16x32_bf16 v[20:23], a[116:119], v[192:195], v[20:23]\n" \
  "ds_write_addtid_b32 v208 offset:0\n" \
  "ds_write_addtid_b32 v209 offset:256\n" \
  "v_mfma_f32_16x16x32_bf16 v[24:27], a[120:123], v[192:195], v[24:27]\n" \
  "ds_write_addtid_b32 v210 offset:512\n" \
  "ds_write_addtid_b32 v211 offset:768\n" \
  "v_mfma_f32_16x16x32_bf16 v[28:31], a[124:127], v[192:195], v[28:31]\n" \
  "ds_write_addtid_b32 v212 offset:1024\n" \
  "ds_write_addtid_b32 v213 offset:1280\n" \
  "s_waitcnt vmcnt(14)\n" \
  "v_mfma_f32_16x16x32_bf16 v[0:3], a[128:131], v[196:199], v[0:3]\n" \
  "ds_write_addtid_b32 v214 offset:1536\n" \
  "ds_write_addtid_b32 v215 offset:1792\n" \
  "v_mfma_f32_16x16x32_bf16 v[4:7], a[132:135], v[196:199], v[4:7]\n" \
  "ds_write_addtid_b32 v216 offset:2048\n" \
  "ds_write_addtid_b32 v217 offset:2304\n" \
  "v_mfma_f32_16x16x32_bf16 v[8:11], a[136:139], v[196:199], v[8:11]\n" \
  "ds_write_addtid_b32 v218 offset:2560\n" \
  "ds_write_addtid_b32 v219 offset:2816\n" \
  "v_mfma_f32_16x16x32_bf16 v[12:15], a[140:143], v[196:199], v[12:15]\n" \
  "ds_write_addtid_b32 v220 offset:3072\n" \
  "ds_write_addtid_b32 v221 offset:3328\n" \
  "v_mfma_f32_16x16x32_bf16 v[16:19], a[144:147], v[196:199], v[16:19]\n" \
  "ds_write_addtid_b32 v222 offset:3584\n" \
  "ds_write_addtid_b32 v223 offset:3840\n" \
  "v_mfma_f32_16x16x32_bf16 v[20:23], a[148:151], v[196:199], v[20:23]\n" \
  "ds_write_addtid_b32 v224 offset:4096\n" \
  "ds_write_addtid_b32 v225 offset:4352\n" \
  "v_mfma_f32_16x16x32_bf16 v[24:27], a[152:155], v[196:199], v[24:27]\n" \
  "ds_write_addtid_b32 v226 offset:4608\n" \
  "ds_write_addtid_b32 v227 offset:4864\n" \
  "v_mfma_f32_16x16x32_bf16 v[28:31], a[156:159], v[196:199], v[28:31]\n" \
  "ds_write_addtid_b32 v228 offset:5120\n" \
  "ds_write_addtid_b32 v229 offset:5376\n" \
  "s_waitcnt vmcnt(13)\n" \
  "v_mfma_f32_16x16x32_bf16 v[0:3], a[160:163], v[200:203], v[0:3]\n" \
  "ds_write_addtid_b32 v230 offset:5632\n" \
  "ds_write_addtid_b32 v231 offset:5888\n" \
  "v_mfma_f32_16x16x32_bf16 v[4:7], a[164:167], v[200:203], v[4:7]\n" \
  "ds_write_addtid_b32 v232 offset:6144\n" \
  "ds_write_addtid_b32 v233 offset:6400\n" \
  "v_mfma_f32_16x16x32_bf16 v[8:11], a[168:171], v[200:203], v[8:11]\n" \
  "ds_write_addtid_b32 v234 offset:6656\n" \
  "ds_write_addtid_b32 v235 offset:6912\n" \
  "v_mfma_f32_16x16x32_bf16 v[12:15], a[172:175], v[200:203], v[12:15]\n" \
  "ds_write_addtid_b32 v236 offset:7168\n" \
  "ds_write_addtid_b32 v237 offset:7424\n" \
  "v_mfma_f32_16x16x32_bf16 v[16:19], a[176:179], v[200:203], v[16:19]\n" \
  "ds_write_addtid_b32 v238 offset:7680\n" \
  "ds_write_addtid_b32 v239 offset:7936\n" \
  "v_mfma_f32_16x16x32_bf16 v[20:23], a[180:183], v[200:203], v[20:23]\n" \
  "v_mfma_f32_16x16x32_bf16 v[24:27], a[184:187], v[200:203], v[24:27]\n" \
  "v_mfma_f32_16x16x32_bf16 v[28:31], a[188:191], v[200:203], v[28:31]\n" \
  "s_waitcnt vmcnt(12)\n" \
  "v_mfma_f32_16x16x32_bf16 v[0:3], a[192:195], v[204:207], v[0:3]\n" \
  "v_mfma_f32_16x16x32_bf16 v[4:7], a[196:199], v[204:207], v[4:7]\n" \
  "v_mfma_f32_16x16x32_bf16 v[8:11], a[200:203], v[204:207], v[8:11]\n" \
  "s_waitcnt lgkmcnt(0)\n" \
  "s_barrier\n" \
  "ds_read_b128 v[32:35], v58 offset:0\n" \
  "v_mfma_f32_16x16x32_bf16 v[12:15], a[204:207], v[204:207], v[12:15]\n" \
  "ds_read_b128 v[36:39], v58 offset:8192\n" \
  "ds_read_b128 v[40:43], v58 offset:16384\n" \
  "v_mfma_f32_16x16x32_bf16 v[16:19], a[208:211], v[204:207], v[16:19]\n" \
  "ds_read_b128 v[44:47], v58 offset:24576\n" \
  "ds_read_b128 v[48:51], v58 offset:4096\n" \
  "v_mfma_f32_16x16x32_bf16 v[20:23], a[212:215], v[204:207], v[20:23]\n" \
  "ds_read_b128 v[240:243], v58 offset:12288\n" \
  "ds_read_b128 v[244:247], v58 offset:20480\n" \
  "v_mfma_f32_16x16x32_bf16 v[24:27], a[216:219], v[204:207], v[24:27]\n" \
  "ds_read_b128 v[248:251], v58 offset:28672\n" \
  "v_mfma_f32_16x16x32_bf16 v[28:31], a[220:223], v[204:207], v[28:31]\n" \
  "s_waitcnt vmcnt(7)\n" \
  "v_mfma_f32_16x16x32_bf16 v[208:211], v[128:131], v[72:75], 0\n" \
  "s_mov_b64 exec, 1\n" \
  "buffer_store_dword v61, off, s[36:39], s54 offset:0 sc1\n" \
  "s_mov_b64 exec, -1\n" \
  "v_mfma_f32_16x16x32_bf16 v[212:215], v[132:135], v[72:75], 0\n" \
  "v_cmp_eq_u32 vcc, 0, v63\n" \
  "ds_write_addtid_b32 v0 offset:32768\n" \
  "v_mfma_f32_16x16x32_bf16 v[216:219], v[136:139], v[72:75], 0\n" \
  "ds_write_addtid_b32 v1 offset:33024\n" \
  "ds_write_addtid_b32 v2 offset:33280\n" \
  "v_lshlrev_b32 v52, 5, v59\n" \
  "v_mfma_f32_16x16x32_bf16 v[220:223], v[140:143], v[72:75], 0\n" \
  "s_and_b64 vcc, vcc, s[86:87]\n" \
  "s_cbranch_scc1 .LsbF_%=\n" \
  ".LbbF_%=:\n" \
  "buffer_load_dwordx4 v[108:111], v59, s[36:39], s48 offen offset:0 sc1\n" \
  "v_mfma_f32_16x16x32_bf16 v[224:227], v[144:147], v[72:75], 0\n" \
  "ds_write_addtid_b32 v3 offset:33536\n" \
  "ds_write_addtid_b32 v4 offset:33792\n" \
  "s_waitcnt lgkmcnt(9)\n" \
  "v_mfma_f32_16x16x32_bf16 v[228:231], v[148:151], v[72:75], 0\n" \
  "ds_write_addtid_b32 v5 offset:34048\n" \
  "ds_write_addtid_b32 v6 offset:34304\n" \
  "v_add_f32 v32, v32, v36\n" \
  "v_mfma_f32_16x16x32_bf16 v[232:235], v[152:155], v[72:75], 0\n" \
  "buffer_load_dwordx4 v[112:115], v59, s[36:39], s48 offen offset:1024 sc1\n" \
  "ds_write_addtid_b32 v7 offset:34560\n" \
  "ds_write_addtid_b32 v8 offset:34816\n" \
  "v_mfma_f32_16x16x32_bf16 v[236:239], v[156:159], v[72:75], 0\n" \
  "ds_write_addtid_b32 v9 offset:35072\n" \
  "ds_write_addtid_b32 v10 offset:35328\n" \
  "v_add_f32 v33, v33, v37\n" \
  "v_mfma_f32_16x16x32_bf16 v[208:211], v[160:163], v[76:79], v[208:211]\n" \
  "ds_write_addtid_b32 v11 offset:35584\n" \
  "ds_write_addtid_b32 v12 offset:35840\n" \
  "v_add_f32 v34, v34, v38\n" \
  "v_mfma_f32_16x16x32_bf16 v[212:215], v[164:167], v[76:79], v[212:215]\n" \
  "buffer_load_dwordx4 v[116:119], v59, s[36:39], s48 offen offset:2048 sc1\n" \
  "ds_write_addtid_b32 v13 offset:36096\n" \
  "ds_write_addtid_b32 v14 offset:36352\n" \
  "v_mfma_f32_16x16x32_bf16 v[216:219], v[168:171], v[76:79], v[216:219]\n" \
  "ds_write_addtid_b32 v15 offset:36608\n" \
  "ds_write_addtid_b32 v16 offset:36864\n" \
  "v_add_f32 v35, v35, v39\n" \
  "v_mfma_f32_16x16x32_bf16 v[220:223], v[172:175], v[76:79], v[220:223]\n" \
  "ds_write_addtid_b32 v17 offset:37120\n" \
  "ds_write_addtid_b32 v18 offset:37376\n" \
  "v_add_f32 v40, v40, v44\n" \
  "v_mfma_f32_16x16x32_bf16 v[224:227], v[176:179], v[76:79], v[224:227]\n" \
  "buffer_load_dwordx4 v[120:123], v59, s[36:39], s49 offen offset:0 sc1\n" \
  "ds_write_addtid_b32 v19 offset:37632\n" \
  "ds_write_addtid_b32 v20 offset:37888\n" \
  "v_mfma_f32_16x16x32_bf16 v[228:231], v[180:183], v[76:79], v[228:231]\n" \
  "ds_write_addtid_b32 v21 offset:38144\n" \
  "ds_write_addtid_b32 v22 offset:38400\n" \
  "v_add_f32 v41, v41, v45\n" \
  "v_mfma_f32_16x16x32_bf16 v[232:235], v[184:187], v[76:79], v[232:235]\n" \
  "ds_write_addtid_b32 v23 offset:38656\n" \
  "ds_write_addtid_b32 v24 offset:38912\n" \
  "v_add_f32 v42, v42, v46\n" \
  "v_mfma_f32_16x16x32_bf16 v[236:239], v[188:191], v[76:79], v[236:239]\n" \
  "buffer_load_dwordx4 v[124:127], v59, s[36:39], s49 offen offset:1024 sc1\n" \
  "ds_write_addtid_b32 v25 offset:39168\n" \
  "s_waitcnt vmcnt(11)\n" \
  "v_mfma_f32_16x16x32_bf16 v[208:211], a[0:3], v[80:83], v[208:211]\n" \
  "ds_write_addtid_b32 v26 offset:39424\n" \
  "ds_write_addtid_b32 v27 offset:39680\n" \
  "v_add_f32 v43, v43, v47\n" \
  "v_mfma_f32_16x16x32_bf16 v[212:215], a[4:7], v[80:83], v[212:215]\n" \
  "ds_write_addtid_b32 v28 offset:39936\n" \
  "ds_write_addtid_b32 v29 offset:40192\n" \
  "v_add_f32 v32, v32, v40\n" \
  "v_mfma_f32_16x16x32_bf16 v[216:219], a[8:11], v[80:83], v[216:219]\n" \
  "buffer_load_dwordx4 v[192:195], v59, s[36:39], s49 offen offset:2048 sc1\n" \
  "ds_write_addtid_b32 v30 offset:40448\n" \
  "ds_write_addtid_b32 v31 offset:40704\n" \
  "v_mfma_f32_16x16x32_bf16 v[220:223], a[12:15], v[80:83], v[220:223]\n" \
  "v_add_f32 v33, v33, v41\n" \
  "v_add_f32 v34, v34, v42\n" \
  "v_add_f32 v35, v35, v43\n" \
  "v_mfma_f32_16x16x32_bf16 v[224:227], a[16:19], v[80:83], v[224:227]\n" \
  "v_cvt_pk_bf16_f32 v36, v32, v33\n" \
  "v_cvt_pk_bf16_f32 v37, v34, v35\n" \
  "s_waitcnt lgkmcnt(15)\n" \
  "v_mfma_f32_16x16x32_bf16 v[228:231], a[20:23], v[80:83], v[228:231]\n" \
  "buffer_load_dwordx4 v[196:199], v59, s[36:39], s50 offen offset:0 sc1\n" \
  "v_add_f32 v48, v48, v240\n" \
  "v_add_f32 v49, v49, v241\n" \
  "v_mfma_f32_16x16x32_bf16 v[232:235], a[24:27], v[80:83], v[232:235]\n" \
  "v_add_f32 v50, v50, v242\n" \
  "v_add_f32 v51, v51, v243\n" \
  "v_add_f32 v244, v244, v248\n" \
  "v_mfma_f32_16x16x32_bf16 v[236:239], a[28:31], v[80:83], v[236:239]\n" \
  "v_add_f32 v245, v245, v249\n" \
  "v_add_f32 v246, v246, v250\n" \
  "v_add_f32 v247, v247, v251\n" \
  "v_mfma_f32_16x16x32_bf16 v[208:211], a[32:35], v[84:87], v[208:211]\n" \
  "buffer_load_dwordx4 v[200:203], v59, s[36:39], s50 offen offset:1024 sc1\n" \
  "s_waitcnt lgkmcnt(0)\n" \
  "s_barrier\n" \
  "v_mfma_f32_16x16x32_bf16 v[212:215], a[36:39], v[84:87], v[212:215]\n" \
  "v_add_f32 v48, v48, v244\n" \
  "v_add_f32 v49, v49, v245\n" \
  "v_add_f32 v50, v50, v246\n" \
  "v_mfma_f32_16x16x32_bf16 v[216:219], a[40:43], v[84:87], v[216:219]\n" \
  "v_and_b32 v52, 0x1800, v52\n" \
  "v_add_f32 v51, v51, v247\n" \
  "v_and_b32 v53, 0x30, v59\n" \
  "v_mfma_f32_16x16x32_bf16 v[220:223], a[44:47], v[84:87], v[220:223]\n" \
  "buffer_load_dwordx4 v[204:207], v59, s[36:39], s50 offen offset:2048 sc1\n" \
  "v_cvt_pk_bf16_f32 v38, v48, v49\n" \
  "v_lshl_or_b32 v52, v53, 1, v52\n" \
  "v_mfma_f32_16x16x32_bf16 v[224:227], a[48:51], v[84:87], v[224:227]\n" \
  "v_cvt_pk_bf16_f32 v39, v50, v51\n" \
  "buffer_load_dword v53, v52, s[36:39], s30 offen offset:0 sc1\n" \
  "v_mfma_f32_16x16x32_bf16 v[228:231], a[52:55], v[84:87], v[228:231]\n" \
  "v_mfma_f32_16x16x32_bf16 v[232:235], a[56:59], v[84:87], v[232:235]\n" \
  "buffer_store_dwordx4 v[36:39], v59, s[36:39], s74 offen offset:0 sc1\n" \
  "ds_read_b128 v[32:35], v58 offset:32768\n" \
  "s_nop 0\n" \
  "ds_read_b128 v[36:39], v58 offset:40960\n" \
  "v_mfma_f32_16x16x32_bf16 v[236:239], a[60:63], v[84:87], v[236:239]\n" \
  "ds_read_b128 v[40:43], v58 offset:49152\n" \
  "ds_read_b128 v[44:47], v58 offset:57344\n" \
  "s_waitcnt vmcnt(15)\n" \
  "v_mfma_f32_16x16x32_bf16 v[208:211], a[64:67], v[88:91], v[208:211]\n" \
  "ds_read_b128 v[48:51], v58 offset:36864\n" \
  "ds_read_b128 v[240:243], v58 offset:45056\n" \
  "v_mfma_f32_16x16x32_bf16 v[212:215], a[68:71], v[88:91], v[212:215]\n" \
  "ds_read_b128 v[244:247], v58 offset:53248\n" \
  "ds_read_b128 v[248:251], v58 offset:61440\n" \
  "v_mfma_f32_16x16x32_bf16 v[216:219], a[72:75], v[88:91], v[216:219]\n" \
  "v_mfma_f32_16x16x32_bf16 v[220:223], a[76:79], v[88:91], v[220:223]\n" \
  "v_mfma_f32_16x16x32_bf16 v[224:227], a[80:83], v[88:91], v[224:227]\n" \
  "v_mfma_f32_16x16x32_bf16 v[228:231], a[84:87], v[88:91], v[228:231]\n" \
  "v_mfma_f32_16x16x32_bf16 v[232:235], a[88:91], v[88:91], v[232:235]\n" \
  "v_mfma_f32_16x16x32_bf16 v[236:239], a[92:95], v[88:91], v[236:239]\n" \
  "s_waitcnt lgkmcnt(4)\n" \
  "v_add_f32 v32, v32, v36\n" \
  "v_add_f32 v33, v33, v37\n" \
  "v_mfma_f32_16x16x32_bf16 v[208:211], a[96:99], v[92:95], v[208:211]\n" \
  "v_add_f32 v34, v34, v38\n" \
  "v_add_f32 v35, v35, v39\n" \
  "v_add_f32 v40, v40, v44\n" \
  "v_mfma_f32_16x16x32_bf16 v[212:215], a[100:103], v[92:95], v[212:215]\n" \
  "v_add_f32 v41, v41, v45\n" \
  "v_add_f32 v42, v42, v46\n" \
  "v_add_f32 v43, v43, v47\n" \
  "v_mfma_f32_16x16x32_bf16 v[216:219], a[104:107], v[92:95], v[216:219]\n" \
  "v_add_f32 v32, v32, v40\n" \
  "v_add_f32 v33, v33, v41\n" \
  "v_add_f32 v34, v34, v42\n" \
  "v_mfma_f32_16x16x32_bf16 v[220:223], a[108:111], v[92:95], v[220:223]\n" \
  "v_add_f32 v35, v35, v43\n" \
  "v_cvt_pk_bf16_f32 v36, v32, v33\n" \
  "v_cvt_pk_bf16_f32 v37, v34, v35\n" \
  "v_mfma_f32_16x16x32_bf16 v[224:227], a[112:115], v[92:95], v[224:227]\n" \
  "s_waitcnt lgkmcnt(0)\n" \
  "v_add_f32 v48, v48, v240\n" \
  "v_add_f32 v49, v49, v241\n" \
  "v_mfma_f32_16x16x32_bf16 v[228:231], a[116:119], v[92:95], v[228:231]\n" \
  "v_add_f32 v50, v50, v242\n" \
  "v_add_f32 v51, v51, v243\n" \
  "v_add_f32 v244, v244, v248\n" \
  "v_mfma_f32_16x16x32_bf16 v[232:235], a[120:123], v[92:95], v[232:235]\n" \
  "v_add_f32 v245, v245, v249\n" \
  "v_add_f32 v246, v246, v250\n" \
  "v_add_f32 v247, v247, v251\n" \
  "v_mfma_f32_16x16x32_bf16 v[236:239], a[124:127], v[92:95], v[236:239]\n" \
  "v_add_f32 v48, v48, v244\n" \
  "v_add_f32 v49, v49, v245\n" \
  "s_waitcnt vmcnt(13)\n" \
  "v_mfma_f32_16x16x32_bf16 v[208:211], a[128:131], v[96:99], v[208:211]\n" \
  "v_add_f32 v50, v50, v246\n" \
  "v_add_f32 v51, v51, v247\n" \
  "v_cvt_pk_bf16_f32 v38, v48, v49\n" \
  "v_mfma_f32_16x16x32_bf16 v[212:215], a[132:135], v[96:99], v[212:215]\n" \
  "v_cvt_pk_bf16_f32 v39, v50, v51\n" \
  "v_add_u32 v59, 409600, v59\n" \
  "buffer_store_dwordx4 v[36:39], v59, s[36:39], s74 offen offset:0 sc1\n" \
  "v_mfma_f32_16x16x32_bf16 v[216:219], a[136:139], v[96:99], v[216:219]\n" \
  "v_mfma_f32_16x16x32_bf16 v[220:223], a[140:143], v[96:99], v[220:223]\n" \
  "v_mfma_f32_16x16x32_bf16 v[224:227], a[144:147], v[96:99], v[224:227]\n" \
  "v_mfma_f32_16x16x32_bf16 v[228:231], a[148:151], v[96:99], v[228:231]\n" \
  "v_mfma_f32_16x16x32_bf16 v[232:235], a[152:155], v[96:99], v[232:235]\n" \
  "v_mfma_f32_16x16x32_bf16 v[236:239], a[156:159], v[96:99], v[236:239]\n" \
  "v_mfma_f32_16x16x32_bf16 v[208:211], a[160:163], v[100:103], v[208:211]\n" \
  "v_mfma_f32_16x16x32_bf16 v[212:215], a[164:167], v[100:103], v[212:215]\n" \
  "v_mfma_f32_16x16x32_bf16 v[216:219], a[168:171], v[100:103], v[216:219]\n" \
  "v_mfma_f32_16x16x32_bf16 v[220:223], a[172:175], v[100:103], v[220:223]\n" \
  "v_mfma_f32_16x16x32_bf16 v[224:227], a[176:179], v[100:103], v[224:227]\n" \
  "v_mfma_f32_16x16x32_bf16 v[228:231], a[180:183], v[100:103], v[228:231]\n" \
  "v_mfma_f32_16x16x32_bf16 v[232:235], a[184:187], v[100:103], v[232:235]\n" \
  "v_mfma_f32_16x16x32_bf16 v[236:239], a[188:191], v[100:103], v[236:239]\n" \
  "s_waitcnt vmcnt(13)\n" \
  "v_mfma_f32_16x16x32_bf16 v[208:211], a[192:195], v[104:107], v[208:211]\n" \
  "v_mfma_f32_16x16x32_bf16 v[212:215], a[196:199], v[104:107], v[212:215]\n" \
  "v_mfma_f32_16x16x32_bf16 v[216:219], a[200:203], v[104:107], v[216:219]\n" \
  "v_mfma_f32_16x16x32_bf16 v[220:223], a[204:207], v[104:107], v[220:223]\n" \
  "v_mfma_f32_16x16x32_bf16 v[224:227], a[208:211], v[104:107], v[224:227]\n" \
  "v_mfma_f32_16x16x32_bf16 v[228:231], a[212:215], v[104:107], v[228:231]\n" \
  "v_mfma_f32_16x16x32_bf16 v[232:235], a[216:219], v[104:107], v[232:235]\n" \
  "v_mfma_f32_16x16x32_bf16 v[236:239], a[220:223], v[104:107], v[236:239]\n" \
  "s_branch .Le_%=\n" \
  ".Lzl_%=:\n" \
  "s_branch .Lzr_%=\n" \
  ".Lls_%=:\n" \
  "v_mfma_f32_16x16x32_bf16 v[20:23], v[64:67], a[0:3], 0\n" \
  "v_and_b32 v59, 0x3ff, v59\n" \
  "s_add_u32 s48, s68, 409600\n" \
  "s_add_u32 s49, s69, 409600\n" \
  "v_mfma_f32_16x16x32_bf16 v[24:27], v[64:67], a[4:7], 0\n" \
  "s_add_u32 s50, s70, 409600\n" \
  "s_add_u32 s100, s90, 73728\n" \
  "s_mov_b32 s35, 73728\n" \
  "v_mfma_f32_16x16x32_bf16 v[28:31], v[64:67], a[8:11], 0\n" \
  "ds_write_addtid_b32 v0 offset:0\n" \
  "ds_write_addtid_b32 v1 offset:544\n" \
  "v_mfma_f32_16x16x32_bf16 v[32:35], v[64:67], a[12:15], 0\n" \
  "ds_write_addtid_b32 v2 offset:256\n" \
  "ds_write_addtid_b32 v3 offset:800\n" \
  "v_mfma_f32_16x16x32_bf16 v[20:23], v[68:71], a[20:23], v[20:23]\n" \
  "ds_write_addtid_b32 v4 offset:1056\n" \
  "ds_write_addtid_b32 v5 offset:1600\n" \
  "v_mfma_f32_16x16x32_bf16 v[24:27], v[68:71], a[24:27], v[24:27]\n" \
  "ds_write_addtid_b32 v6 offset:1312\n" \
  "ds_write_addtid_b32 v7 offset:1856\n" \
  "v_mfma_f32_16x16x32_bf16 v[28:31], v[68:71], a[28:31], v[28:31]\n" \
  "ds_write_addtid_b32 v8 offset:2112\n" \
  "ds_write_addtid_b32 v9 offset:2656\n" \
  "v_mfma_f32_16x16x32_bf16 v[32:35], v[68:71], a[32:35], v[32:35]\n" \
  "ds_write_addtid_b32 v10 offset:2368\n" \
  "ds_write_addtid_b32 v11 offset:2912\n" \
  "s_waitcnt vmcnt(16)\n" \
  "v_mfma_f32_16x16x32_bf16 v[20:23], v[72:75], a[40:43], v[20:23]\n" \
  "s_waitcnt vmcnt(7)\n" \
  "v_cmp_eq_u32 vcc, 0, v63\n" \
  "v_add_u32 v71, s57, v59\n" \
  "v_mfma_f32_16x16x32_bf16 v[24:27], v[72:75], a[44:47], v[24:27]\n" \
  "ds_read_b32 v64, v71 offset:28672\n" \
  "ds_read_b64 v[68:69], v71 offset:29696\n" \
  "v_mfma_f32_16x16x32_bf16 v[28:31], v[72:75], a[48:51], v[28:31]\n" \
  "s_and_b64 vcc, vcc, s[86:87]\n" \
  "s_cbranch_scc1 .LsaS_%=\n" \
  ".LbaS_%=:\n" \
  "ds_write_addtid_b32 v12 offset:3168\n" \
  "v_mfma_f32_16x16x32_bf16 v[32:35], v[72:75], a[52:55], v[32:35]\n" \
  "ds_write_addtid_b32 v13 offset:3712\n" \
  "ds_write_addtid_b32 v14 offset:3424\n" \
  "s_waitcnt vmcnt(15)\n" \
  "v_mfma_f32_16x16x32_bf16 v[20:23], v[76:79], a[60:63], v[20:23]\n" \
  "buffer_load_dwordx4 v[72:75], v59, s[36:39], s68 offen offset:0 sc1\n" \
  "ds_write_addtid_b32 v15 offset:3968\n" \
  "ds_write_addtid_b32 v16 offset:4224\n" \
  "v_mfma_f32_16x16x32_bf16 v[24:27], v[76:79], a[64:67], v[24:27]\n" \
  "s_waitcnt lgkmcnt(5)\n" \
  "v_xor_b32 v65, 32, v64\n" \
  "v_xor_b32 v66, 64, v64\n" \
  "v_mfma_f32_16x16x32_bf16 v[28:31], v[76:79], a[68:71], v[28:31]\n" \
  "v_xor_b32 v67, 96, v64\n" \
  "v_add_u32 v70, 0xf800, v68\n" \
  "v_add_u32 v71, 0xf800, v69\n" \
  "v_mfma_f32_16x16x32_bf16 v[32:35], v[76:79], a[72:75], v[32:35]\n" \
  "ds_write_addtid_b32 v17 offset:4512\n" \
  "ds_write_addtid_b32 v18 offset:4800\n" \
  "v_mfma_f32_16x16x32_bf16 v[20:23], v[80:83], a[80:83], v[20:23]\n" \
  "buffer_load_dwordx4 v[76:79], v59, s[36:39], s68 offen offset:1024 sc1\n" \
  "ds_write_addtid_b32 v19 offset:5088\n" \
  "ds_read_b64_tr_b16 a[0:1], v64 offset:0\n" \
  "v_mfma_f32_16x16x32_bf16 v[24:27], v[80:83], a[84:87], v[24:27]\n" \
  "ds_read_b64_tr_b16 a[2:3], v64 offset:512\n" \
  "ds_read_b64_tr_b16 a[4:5], v65 offset:0\n" \
  "v_mfma_f32_16x16x32_bf16 v[28:31], v[80:83], a[88:91], v[28:31]\n" \
  "ds_read_b64_tr_b16 a[6:7], v65 offset:512\n" \
  "ds_read_b64_tr_b16 a[8:9], v66 offset:0\n" \
  "v_mfma_f32_16x16x32_bf16 v[32:35], v[80:83], a[92:95], v[32:35]\n" \
  "ds_read_b64_tr_b16 a[10:11], v66 offset:512\n" \
  "ds_read_b64_tr_b16 a[12:13], v67 offset:0\n" \
  "v_mfma_f32_16x16x32_bf16 v[20:23], v[84:87], a[100:103], v[20:23]\n" \
  "buffer_load_dwordx4 v[80:83], v59, s[36:39], s68 offen offset:2048 sc1\n" \
  "ds_read_b64_tr_b16 a[14:15], v67 offset:512\n" \
  "v_mfma_f32_16x16x32_bf16 v[24:27], v[84:87], a[104:107], v[24:27]\n" \
  "v_rcp_f32 v42, v42\n" \
  "v_mfma_f32_16x16x32_bf16 v[28:31], v[84:87], a[108:111], v[28:31]\n" \
  "s_waitcnt lgkmcnt(0)\n" \
  "s_barrier\n" \
  "v_mfma_f32_16x16x32_bf16 v[32:35], v[84:87], a[112:115], v[32:35]\n" \
  "v_rcp_f32 v43, v43\n" \
  "v_mfma_f32_16x16x32_bf16 v[20:23], v[88:91], a[120:123], v[20:23]\n" \
  "buffer_load_dwordx4 v[84:87], v59, s[36:39], s69 offen offset:0 sc1\n" \
  "s_add_u32 m0, s57, 0\n" \
  "v_mfma_f32_16x16x32_bf16 v[24:27], v[88:91], a[124:127], v[24:27]\n" \
  "ds_read_b64_tr_b16 a[16:17], v64 offset:4096\n" \
  "ds_read_b64_tr_b16 a[18:19], v64 offset:4608\n" \
  "v_mul_f32 v36, v36, v40\n" \
  "v_mfma_f32_16x16x32_bf16 v[28:31], v[88:91], a[128:131], v[28:31]\n" \
  "ds_read_b64_tr_b16 a[20:21], v65 offset:4096\n" \
  "ds_read_b64_tr_b16 a[22:23], v65 offset:4608\n" \
  "v_mul_f32 v37, v37, v41\n" \
  "v_mfma_f32_16x16x32_bf16 v[32:35], v[88:91], a[132:135], v[32:35]\n" \
  "buffer_load_dwordx4 v68, s[40:43], s60 offen offset:0 lds\n" \
  "ds_read_b64_tr_b16 a[24:25], v66 offset:4096\n" \
  "v_mfma_f32_16x16x32_bf16 v[20:23], v[92:95], a[140:143], v[20:23]\n" \
  "ds_read_b64_tr_b16 a[26:27], v66 offset:4608\n" \
  "ds_read_b64_tr_b16 a[28:29], v67 offset:4096\n" \
  "v_mul_f32 v38, v38, v42\n" \
  "v_mfma_f32_16x16x32_bf16 v[24:27], v[92:95], a[144:147], v[24:27]\n" \
  "ds_read_b64_tr_b16 a[30:31], v67 offset:4608\n" \
  "v_mul_f32 v39, v39, v43\n" \
  "v_cvt_pk_bf16_f32 v40, v36, v37\n" \
  "v_mfma_f32_16x16x32_bf16 v[28:31], v[92:95], a[148:151], v[28:31]\n" \
  "buffer_load_dwordx4 v69, s[40:43], s60 offen offset:1024 lds\n" \
  "v_cvt_pk_bf16_f32 v41, v38, v39\n" \
  "buffer_store_dwordx2 v[40:41], v61, s[36:39], s52 offen offset:0 sc1\n" \
  "v_mfma_f32_16x16x32_bf16 v[32:35], v[92:95], a[152:155], v[32:35]\n" \
  "ds_read_b128 v[36:39], v58 offset:0\n" \
  "ds_read_b128 v[40:43], v58 offset:9568\n" \
  "v_mfma_f32_16x16x32_bf16 v[20:23], v[96:99], a[160:163], v[20:23]\n" \
  "ds_read_b128 v[44:47], v58 offset:19136\n" \
  "ds_read_b128 v[48:51], v58 offset:28704\n" \
  "s_add_u32 s52, s52, 409600\n" \
  "v_mfma_f32_16x16x32_bf16 v[24:27], v[96:99], a[164:167], v[24:27]\n" \
  "buffer_load_dwordx4 v70, s[40:43], s60 offen offset:2048 lds\n" \
  "v_mfma_f32_16x16x32_bf16 v[28:31], v[96:99], a[168:171], v[28:31]\n" \
  "ds_read_b64_tr_b16 a[32:33], v64 offset:8192\n" \
  "ds_read_b64_tr_b16 a[34:35], v64 offset:8704\n" \
  "v_mfma_f32_16x16x32_bf16 v[32:35], v[96:99], a[172:175], v[32:35]\n" \
  "ds_read_b64_tr_b16 a[36:37], v65 offset:8192\n" \
  "ds_read_b64_tr_b16 a[38:39], v65 offset:8704\n" \
  "s_waitcnt vmcnt(14)\n" \
  "v_mfma_f32_16x16x32_bf16 v[20:23], v[100:103], a[180:183], v[20:23]\n" \
  "buffer_load_dwordx4 v71, s[40:43], s60 offen offset:3072 lds\n" \
  "ds_read_b64_tr_b16 a[40:41], v66 offset:8192\n" \
  "ds_read_b64_tr_b16 a[42:43], v66 offset:8704\n" \
  "v_mfma_f32_16x16x32_bf16 v[24:27], v[100:103], a[184:187], v[24:27]\n" \
  "ds_read_b64_tr_b16 a[44:45], v67 offset:8192\n" \
  "ds_read_b64_tr_b16 a[46:47], v67 offset:8704\n" \
  "v_mfma_f32_16x16x32_bf16 v[28:31], v[100:103], a[188:191], v[28:31]\n" \
  "v_mfma_f32_16x16x32_bf16 v[32:35], v[100:103], a[192:195], v[32:35]\n" \
  "buffer_load_dwordx4 v[88:91], v59, s[36:39], s69 offen offset:1024 sc1\n" \
  "s_waitcnt lgkmcnt(10)\n" \
  "s_waitcnt vmcnt(15)\n" \
  "v_mfma_f32_16x16x32_bf16 v[20:23], v[104:107], a[200:203], v[20:23]\n" \
  "v_add_f32 v36, v36, v40\n" \
  "v_add_f32 v37, v37, v41\n" \
  "v_add_f32 v38, v38, v42\n" \
  "v_mfma_f32_16x16x32_bf16 v[24:27], v[104:107], a[204:207], v[24:27]\n" \
  "v_add_f32 v39, v39, v43\n" \
  "v_add_f32 v36, v36, v52\n" \
  "v_add_f32 v37, v37, v53\n" \
  "v_mfma_f32_16x16x32_bf16 v[28:31], v[104:107], a[208:211], v[28:31]\n" \
  "buffer_load_dwordx4 v[92:95], v59, s[36:39], s69 offen offset:2048 sc1\n" \
  "s_add_u32 m0, s57, 4096\n" \
  "v_mfma_f32_16x16x32_bf16 v[32:35], v[104:107], a[212:215], v[32:35]\n" \
  "v_add_f32 v38, v38, v54\n" \
  "v_add_f32 v39, v39, v55\n" \
  "s_waitcnt vmcnt(15)\n" \
  "v_mfma_f32_16x16x32_bf16 v[20:23], v[108:111], a[220:223], v[20:23]\n" \
  "ds_read_b64_tr_b16 a[48:49], v64 offset:12288\n" \
  "ds_read_b64_tr_b16 a[50:51], v64 offset:12800\n" \
  "s_waitcnt lgkmcnt(10)\n" \
  "v_mfma_f32_16x16x32_bf16 v[24:27], v[108:111], a[224:227], v[24:27]\n" \
  "buffer_load_dwordx4 v68, s[40:43], s61 offen offset:0 lds\n" \
  "ds_read_b64_tr_b16 a[52:53], v65 offset:12288\n" \
  "ds_read_b64_tr_b16 a[54:55], v65 offset:12800\n" \
  "v_mfma_f32_16x16x32_bf16 v[28:31], v[108:111], a[228:231], v[28:31]\n" \
  "ds_read_b64_tr_b16 a[56:57], v66 offset:12288\n" \
  "ds_read_b64_tr_b16 a[58:59], v66 offset:12800\n" \
  "v_add_f32 v44, v44, v48\n" \
  "v_mfma_f32_16x16x32_bf16 v[32:35], v[108:111], a[232:235], v[32:35]\n" \
  "ds_read_b64_tr_b16 a[60:61], v67 offset:12288\n" \
  "ds_read_b64_tr_b16 a[62:63], v67 offset:12800\n" \
  "s_waitcnt vmcnt(15)\n" \
  "v_mfma_f32_16x16x32_bf16 v[20:23], v[112:115], a[240:243], v[20:23]\n" \
  "buffer_load_dwordx4 v69, s[40:43], s61 offen offset:1024 lds\n" \
  "v_add_f32 v45, v45, v49\n" \
  "v_add_f32 v46, v46, v50\n" \
  "v_mfma_f32_16x16x32_bf16 v[24:27], v[112:115], a[244:247], v[24:27]\n" \
  "v_add_f32 v47, v47, v51\n" \
  "v_add_f32 v36, v36, v44\n" \
  "v_add_f32 v37, v37, v45\n" \
  "v_mfma_f32_16x16x32_bf16 v[28:31], v[112:115], a[248:251], v[28:31]\n" \
  "v_add_f32 v38, v38, v46\n" \
  "v_add_f32 v39, v39, v47\n" \
  "v_mul_f32 v40, v36, v36\n" \
  "v_mfma_f32_16x16x32_bf16 v[32:35], v[112:115], a[252:255], v[32:35]\n" \
  "buffer_load_dwordx4 v70, s[40:43], s61 offen offset:2048 lds\n" \
  "v_mul_f32 v41, v37, v37\n" \
  "s_waitcnt vmcnt(16)\n" \
  "v_mfma_f32_16x16x32_bf16 v[20:23], v[116:119], v[196:199], v[20:23]\n" \
  "v_mul_f32 v42, v38, v38\n" \
  "v_mul_f32 v43, v39, v39\n" \
  "v_fma_f32 v40, v40, s55, v57\n" \
  "v_mfma_f32_16x16x32_bf16 v[24:27], v[116:119], v[200:203], v[24:27]\n" \
  "v_fma_f32 v41, v41, s55, v57\n" \
  "v_fma_f32 v42, v42, s55, v57\n" \
  "v_fma_f32 v43, v43, s55, v57\n" \
  "v_mfma_f32_16x16x32_bf16 v[28:31], v[116:119], v[204:207], v[28:31]\n" \
  "buffer_load_dwordx4 v71, s[40:43], s61 offen offset:3072 lds\n" \
  "ds_read_b64_tr_b16 a[64:65], v64 offset:16384\n" \
  "ds_read_b64_tr_b16 a[66:67], v64 offset:16896\n" \
  "v_mfma_f32_16x16x32_bf16 v[32:35], v[116:119], v[208:211], v[32:35]\n" \
  "ds_read_b64_tr_b16 a[68:69], v65 offset:16384\n" \
  "ds_read_b64_tr_b16 a[70:71], v65 offset:16896\n" \
  "s_waitcnt vmcnt(16)\n" \
  "v_mfma_f32_16x16x32_bf16 v[20:23], v[120:123], v[216:219], v[20:23]\n" \
  "ds_read_b64_tr_b16 a[72:73], v66 offset:16384\n" \
  "ds_read_b64_tr_b16 a[74:75], v66 offset:16896\n" \
  "v_mul_f32 v40, v36, v40\n" \
  "v_mfma_f32_16x16x32_bf16 v[24:27], v[120:123], v[220:223], v[24:27]\n" \
  "buffer_load_dwordx4 v[108:111], v59, s[36:39], s48 offen offset:0 sc1\n" \
  "ds_read_b64_tr_b16 a[76:77], v67 offset:16384\n" \
  "ds_read_b64_tr_b16 a[78:79], v67 offset:16896\n" \
  "v_mfma_f32_16x16x32_bf16 v[28:31], v[120:123], v[224:227], v[28:31]\n" \
  "v_mul_f32 v41, v37, v41\n" \
  "v_mul_f32 v42, v38, v42\n" \
  "v_mul_f32 v43, v39, v43\n" \
  "v_mfma_f32_16x16x32_bf16 v[32:35], v[120:123], v[228:231], v[32:35]\n" \
  "v_exp_f32 v40, v40\n" \
  "s_waitcnt vmcnt(16)\n" \
  "v_mfma_f32_16x16x32_bf16 v[20:23], v[124:127], v[236:239], v[20:23]\n" \
  "buffer_load_dwordx4 v[112:115], v59, s[36:39], s48 offen offset:1024 sc1\n" \
  "v_exp_f32 v41, v41\n" \
  "v_mfma_f32_16x16x32_bf16 v[24:27], v[124:127], v[240:243], v[24:27]\n" \
  "v_exp_f32 v42, v42\n" \
  "ds_read_b32 v48, v60 offset:0\n" \
  "v_mfma_f32_16x16x32_bf16 v[28:31], v[124:127], v[244:247], v[28:31]\n" \
  "v_exp_f32 v43, v43\n" \
  "v_add_f32 v40, 1.0, v40\n" \
  "v_mfma_f32_16x16x32_bf16 v[32:35], v[124:127], v[248:251], v[32:35]\n" \
  "buffer_load_dwordx4 v[116:119], v59, s[36:39], s48 offen offset:2048 sc1\n" \
  "s_mov_b32 m0, s56\n" \
  "s_waitcnt vmcnt(17)\n" \
  "v_mfma_f32_16x16x32_bf16 v[208:211], v[128:131], v[72:75], 0\n" \
  "ds_read_b64_tr_b16 a[80:81], v64 offset:20480\n" \
  "ds_read_b64_tr_b16 a[82:83], v64 offset:20992\n" \
  "v_add_f32 v41, 1.0, v41\n" \
  "v_mfma_f32_16x16x32_bf16 v[212:215], v[132:135], v[72:75], 0\n" \
  "ds_write_addtid_b32 v20 offset:5344\n" \
  "ds_write_addtid_b32 v21 offset:5888\n" \
  "v_add_f32 v42, 1.0, v42\n" \
  "v_mfma_f32_16x16x32_bf16 v[216:219], v[136:139], v[72:75], 0\n" \
  "buffer_load_dwordx4 v[120:123], v59, s[36:39], s49 offen offset:0 sc1\n" \
  "ds_write_addtid_b32 v22 offset:5600\n" \
  "ds_write_addtid_b32 v23 offset:6144\n" \
  "v_mfma_f32_16x16x32_bf16 v[220:223], v[140:143], v[72:75], 0\n" \
  "ds_write_addtid_b32 v24 offset:6400\n" \
  "ds_write_addtid_b32 v25 offset:6944\n" \
  "v_add_f32 v43, 1.0, v43\n" \
  "v_mfma_f32_16x16x32_bf16 v[224:227], v[144:147], v[72:75], 0\n" \
  "ds_write_addtid_b32 v26 offset:6656\n" \
  "ds_write_addtid_b32 v27 offset:7200\n" \
  "v_rcp_f32 v40, v40\n" \
  "v_mfma_f32_16x16x32_bf16 v[228:231], v[148:151], v[72:75], 0\n" \
  "buffer_load_dwordx4 v[124:127], v59, s[36:39], s49 offen offset:1024 sc1\n" \
  "ds_write_addtid_b32 v28 offset:7456\n" \
  "ds_write_addtid_b32 v29 offset:8000\n" \
  "v_mfma_f32_16x16x32_bf16 v[232:235], v[152:155], v[72:75], 0\n" \
  "ds_write_addtid_b32 v30 offset:7712\n" \
  "ds_write_addtid_b32 v31 offset:8256\n" \
  "v_rcp_f32 v41, v41\n" \
  "v_mfma_f32_16x16x32_bf16 v[236:239], v[156:159], v[72:75], 0\n" \
  "ds_write_addtid_b32 v32 offset:8512\n" \
  "ds_write_addtid_b32 v33 offset:9056\n" \
  "s_waitcnt vmcnt(18)\n" \
  "v_mfma_f32_16x16x32_bf16 v[208:211], v[160:163], v[76:79], v[208:211]\n" \
  "v_rcp_f32 v42, v42\n" \
  "v_rcp_f32 v43, v43\n" \
  "v_mul_f32 v36, v36, v40\n" \
  "v_mul_f32 v37, v37, v41\n" \
  "v_mul_f32 v38, v38, v42\n" \
  "v_mul_f32 v39, v39, v43\n" \
  "v_cvt_pk_bf16_f32 v40, v36, v37\n" \
  "v_cvt_pk_bf16_f32 v41, v38, v39\n" \
  "buffer_store_dwordx2 v[40:41], v61, s[36:39], s52 offen offset:0 sc1\n" \
  "v_mfma_f32_16x16x32_bf16 v[212:215], v[164:167], v[76:79], v[212:215]\n" \
  "buffer_load_dwordx4 v[192:195], v59, s[36:39], s49 offen offset:2048 sc1\n" \
  "ds_write_addtid_b32 v34 offset:8768\n" \
  "ds_write_addtid_b32 v35 offset:9312\n" \
  "v_mfma_f32_16x16x32_bf16 v[216:219], v[168:171], v[76:79], v[216:219]\n" \
  "ds_read_b64_tr_b16 a[84:85], v65 offset:20480\n" \
  "ds_read_b64_tr_b16 a[86:87], v65 offset:20992\n" \
  "v_mfma_f32_16x16x32_bf16 v[220:223], v[172:175], v[76:79], v[220:223]\n" \
  "ds_read_b64_tr_b16 a[88:89], v66 offset:20480\n" \
  "ds_read_b64_tr_b16 a[90:91], v66 offset:20992\n" \
  "v_mfma_f32_16x16x32_bf16 v[224:227], v[176:179], v[76:79], v[224:227]\n" \
  "buffer_load_dwordx4 v[96:99], v59, s[36:39], s70 offen offset:0 sc1\n" \
  "s_waitcnt lgkmcnt(15)\n" \
  "s_add_u32 m0, s57, 8192\n" \
  "v_mfma_f32_16x16x32_bf16 v[228:231], v[180:183], v[76:79], v[228:231]\n" \
  "ds_read_b64_tr_b16 a[92:93], v67 offset:20480\n" \
  "ds_read_b64_tr_b16 a[94:95], v67 offset:20992\n" \
  "v_mfma_f32_16x16x32_bf16 v[232:235], v[184:187], v[76:79], v[232:235]\n" \
  "ds_read_b64_tr_b16 a[96:97], v64 offset:24576\n" \
  "ds_read_b64_tr_b16 a[98:99], v64 offset:25088\n" \
  "v_mfma_f32_16x16x32_bf16 v[236:239], v[188:191], v[76:79], v[236:239]\n" \
  "buffer_load_dwordx4 v68, s[40:43], s62 offen offset:0 lds\n" \
  "s_waitcnt lgkmcnt(15)\n" \
  "s_waitcnt vmcnt(21)\n" \
  "v_mfma_f32_16x16x32_bf16 v[208:211], a[0:3], v[80:83], v[208:211]\n" \
  "s_waitcnt lgkmcnt(0)\n" \
  "s_barrier\n" \
  "ds_read_b64_tr_b16 a[100:101], v65 offset:24576\n" \
  "v_mfma_f32_16x16x32_bf16 v[212:215], a[4:7], v[80:83], v[212:215]\n" \
  "ds_read_b64_tr_b16 a[102:103], v65 offset:25088\n" \
  "ds_read_b64_tr_b16 a[104:105], v66 offset:24576\n" \
  "v_mfma_f32_16x16x32_bf16 v[216:219], a[8:11], v[80:83], v[216:219]\n" \
  "buffer_load_dwordx4 v69, s[40:43], s62 offen offset:1024 lds\n" \
  "ds_read_b64_tr_b16 a[106:107], v66 offset:25088\n" \
  "ds_read_b64_tr_b16 a[108:109], v67 offset:24576\n" \
  "v_mfma_f32_16x16x32_bf16 v[220:223], a[12:15], v[80:83], v[220:223]\n" \
  "ds_read_b64_tr_b16 a[110:111], v67 offset:25088\n" \
  "ds_read_b32 v49, v60 offset:9568\n" \
  "v_mfma_f32_16x16x32_bf16 v[224:227], a[16:19], v[80:83], v[224:227]\n" \
  "ds_read_b32 v50, v60 offset:19136\n" \
  "ds_read_b32 v51, v60 offset:28704\n" \
  "v_mfma_f32_16x16x32_bf16 v[228:231], a[20:23], v[80:83], v[228:231]\n" \
  "buffer_load_dwordx4 v70, s[40:43], s62 offen offset:2048 lds\n" \
  "v_mfma_f32_16x16x32_bf16 v[232:235], a[24:27], v[80:83], v[232:235]\n" \
  "s_waitcnt vmcnt(17)\n" \
  "ds_read_b64_tr_b16 a[112:113], v64 offset:0\n" \
  "ds_read_b64_tr_b16 a[114:115], v64 offset:512\n" \
  "v_mfma_f32_16x16x32_bf16 v[236:239], a[28:31], v[80:83], v[236:239]\n" \
  "ds_read_b64_tr_b16 a[116:117], v65 offset:0\n" \
  "v_mfma_f32_16x16x32_bf16 v[208:211], a[32:35], v[84:87], v[208:211]\n" \
  "buffer_load_dwordx4 v71, s[40:43], s62 offen offset:3072 lds\n" \
  "s_add_u32 m0, s57, 12288\n" \
  "v_mfma_f32_16x16x32_bf16 v[212:215], a[36:39], v[84:87], v[212:215]\n" \
  "ds_read_b64_tr_b16 a[118:119], v65 offset:512\n" \
  "ds_read_b64_tr_b16 a[120:121], v66 offset:0\n" \
  "v_mfma_f32_16x16x32_bf16 v[216:219], a[40:43], v[84:87], v[216:219]\n" \
  "ds_read_b64_tr_b16 a[122:123], v66 offset:512\n" \
  "ds_read_b64_tr_b16 a[124:125], v67 offset:0\n" \
  "s_waitcnt lgkmcnt(7)\n" \
  "v_mfma_f32_16x16x32_bf16 v[220:223], a[44:47], v[84:87], v[220:223]\n" \
  "buffer_load_dwordx4 v68, s[40:43], s63 offen offset:0 lds\n" \
  "ds_read_b64_tr_b16 a[126:127], v67 offset:512\n" \
  "v_mfma_f32_16x16x32_bf16 v[224:227], a[48:51], v[84:87], v[224:227]\n" \
  "v_add_f32 v48, v48, v49\n" \
  "v_add_f32 v50, v50, v51\n" \
  "v_add_f32 v48, v48, v56\n" \
  "v_mfma_f32_16x16x32_bf16 v[228:231], a[52:55], v[84:87], v[228:231]\n" \
  "v_add_f32 v48, v48, v50\n" \
  "v_mul_f32 v49, v48, v48\n" \
  "v_fma_f32 v49, v49, s55, v57\n" \
  "v_mfma_f32_16x16x32_bf16 v[232:235], a[56:59], v[84:87], v[232:235]\n" \
  "buffer_load_dwordx4 v69, s[40:43], s63 offen offset:1024 lds\n" \
  "v_mul_f32 v49, v48, v49\n" \
  "v_mfma_f32_16x16x32_bf16 v[236:239], a[60:63], v[84:87], v[236:239]\n" \
  "s_waitcnt vmcnt(19)\n" \
  "v_mfma_f32_16x16x32_bf16 v[208:211], a[64:67], v[88:91], v[208:211]\n" \
  "v_exp_f32 v49, v49\n" \
  "v_mfma_f32_16x16x32_bf16 v[212:215], a[68:71], v[88:91], v[212:215]\n" \
  "buffer_load_dwordx4 v70, s[40:43], s63 offen offset:2048 lds\n" \
  "v_add_f32 v49, 1.0, v49\n" \
  "v_mfma_f32_16x16x32_bf16 v[216:219], a[72:75], v[88:91], v[216:219]\n" \
  "v_rcp_f32 v49, v49\n" \
  "v_mfma_f32_16x16x32_bf16 v[220:223], a[76:79], v[88:91], v[220:223]\n" \
  "v_mul_f32 v48, v48, v49\n" \
  "v_cvt_pk_bf16_f32 v49, v48, v48\n" \
  "v_mfma_f32_16x16x32_bf16 v[224:227], a[80:83], v[88:91], v[224:227]\n" \
  "buffer_load_dwordx4 v71, s[40:43], s63 offen offset:3072 lds\n" \
  "buffer_store_short v49, v62, s[36:39], s52 offen offset:0 sc1\n" \
  "ds_read_b128 v[36:39], v58 offset:5344\n" \
  "v_mfma_f32_16x16x32_bf16 v[228:231], a[84:87], v[88:91], v[228:231]\n" \
  "ds_read_b128 v[40:43], v58 offset:14912\n" \
  "ds_read_b128 v[44:47], v58 offset:24480\n" \
  "s_add_u32 s52, s52, 409600\n" \
  "v_mfma_f32_16x16x32_bf16 v[232:235], a[88:91], v[88:91], v[232:235]\n" \
  "ds_read_b128 v[48:51], v58 offset:34048\n" \
  "v_mfma_f32_16x16x32_bf16 v[236:239], a[92:95], v[88:91], v[236:239]\n" \
  "buffer_load_dwordx4 v[100:103], v59, s[36:39], s70 offen offset:1024 sc1\n" \
  "s_waitcnt vmcnt(22)\n" \
  "v_mfma_f32_16x16x32_bf16 v[208:211], a[96:99], v[92:95], v[208:211]\n" \
  "s_add_u32 m0, s57, 16384\n" \
  "s_waitcnt vmcnt(18)\n" \
  "v_mfma_f32_16x16x32_bf16 v[212:215], a[100:103], v[92:95], v[212:215]\n" \
  "ds_read_b64_tr_b16 a[128:129], v64 offset:4096\n" \
  "ds_read_b64_tr_b16 a[130:131], v64 offset:4608\n" \
  "v_mfma_f32_16x16x32_bf16 v[216:219], a[104:107], v[92:95], v[216:219]\n" \
  "buffer_load_dwordx4 v68, s[40:43], s64 offen offset:0 lds\n" \
  "ds_read_b64_tr_b16 a[132:133], v65 offset:4096\n" \
  "ds_read_b64_tr_b16 a[134:135], v65 offset:4608\n" \
  "v_mfma_f32_16x16x32_bf16 v[220:223], a[108:111], v[92:95], v[220:223]\n" \
  "ds_read_b64_tr_b16 a[136:137], v66 offset:4096\n" \
  "ds_read_b64_tr_b16 a[138:139], v66 offset:4608\n" \
  "s_waitcnt vmcnt(18)\n" \
  "v_mfma_f32_16x16x32_bf16 v[0:3], v[128:131], v[108:111], 0\n" \
  "ds_read_b64_tr_b16 a[140:141], v67 offset:4096\n" \
  "ds_read_b64_tr_b16 a[142:143], v67 offset:4608\n" \
  "v_mfma_f32_16x16x32_bf16 v[4:7], v[132:135], v[108:111], 0\n" \
  "buffer_load_dwordx4 v69, s[40:43], s64 offen offset:1024 lds\n" \
  "s_waitcnt lgkmcnt(10)\n" \
  "v_add_f32 v36, v36, v40\n" \
  "v_mfma_f32_16x16x32_bf16 v[8:11], v[136:139], v[108:111], 0\n" \
  "v_add_f32 v37, v37, v41\n" \
  "v_add_f32 v38, v38, v42\n" \
  "v_add_f32 v39, v39, v43\n" \
  "v_mfma_f32_16x16x32_bf16 v[12:15], v[140:143], v[108:111], 0\n" \
  "v_add_f32 v36, v36, v52\n" \
  "v_add_f32 v37, v37, v53\n" \
  "v_add_f32 v38, v38, v54\n" \
  "v_mfma_f32_16x16x32_bf16 v[16:19], v[144:147], v[108:111], 0\n" \
  "buffer_load_dwordx4 v70, s[40:43], s64 offen offset:2048 lds\n" \
  "v_add_f32 v39, v39, v55\n" \
  "s_waitcnt lgkmcnt(8)\n" \
  "v_mfma_f32_16x16x32_bf16 v[20:23], v[148:151], v[108:111], 0\n" \
  "v_add_f32 v44, v44, v48\n" \
  "v_add_f32 v45, v45, v49\n" \
  "v_add_f32 v46, v46, v50\n" \
  "v_mfma_f32_16x16x32_bf16 v[24:27], v[152:155], v[108:111], 0\n" \
  "v_add_f32 v47, v47, v51\n" \
  "v_add_f32 v36, v36, v44\n" \
  "v_add_f32 v37, v37, v45\n" \
  "v_mfma_f32_16x16x32_bf16 v[28:31], v[156:159], v[108:111], 0\n" \
  "buffer_load_dwordx4 v71, s[40:43], s64 offen offset:3072 lds\n" \
  "v_add_f32 v38, v38, v46\n" \
  "s_waitcnt vmcnt(20)\n" \
  "v_mfma_f32_16x16x32_bf16 v[0:3], v[160:163], v[112:115], v[0:3]\n" \
  "s_add_u32 m0, s57, 20480\n" \
  "v_add_f32 v39, v39, v47\n" \
  "v_mfma_f32_16x16x32_bf16 v[4:7], v[164:167], v[112:115], v[4:7]\n" \
  "v_mul_f32 v40, v36, v36\n" \
  "v_mul_f32 v41, v37, v37\n" \
  "v_mul_f32 v42, v38, v38\n" \
  "v_mfma_f32_16x16x32_bf16 v[8:11], v[168:171], v[112:115], v[8:11]\n" \
  "buffer_load_dwordx4 v68, s[40:43], s65 offen offset:0 lds\n" \
  "v_mul_f32 v43, v39, v39\n" \
  "v_fma_f32 v40, v40, s55, v57\n" \
  "v_mfma_f32_16x16x32_bf16 v[12:15], v[172:175], v[112:115], v[12:15]\n" \
  "v_fma_f32 v41, v41, s55, v57\n" \
  "v_fma_f32 v42, v42, s55, v57\n" \
  "v_fma_f32 v43, v43, s55, v57\n" \
  "v_mfma_f32_16x16x32_bf16 v[16:19], v[176:179], v[112:115], v[16:19]\n" \
  "v_mul_f32 v40, v36, v40\n" \
  "v_mul_f32 v41, v37, v41\n" \
  "v_mul_f32 v42, v38, v42\n" \
  "v_mfma_f32_16x16x32_bf16 v[20:23], v[180:183], v[112:115], v[20:23]\n" \
  "buffer_load_dwordx4 v69, s[40:43], s65 offen offset:1024 lds\n" \
  "v_mul_f32 v43, v39, v43\n" \
  "v_mfma_f32_16x16x32_bf16 v[24:27], v[184:187], v[112:115], v[24:27]\n" \
  "v_exp_f32 v40, v40\n" \
  "v_mfma_f32_16x16x32_bf16 v[28:31], v[188:191], v[112:115], v[28:31]\n" \
  "v_exp_f32 v41, v41\n" \
  "s_waitcnt vmcnt(21)\n" \
  "v_mfma_f32_16x16x32_bf16 v[0:3], a[0:3], v[116:119], v[0:3]\n" \
  "buffer_load_dwordx4 v70, s[40:43], s65 offen offset:2048 lds\n" \
  "v_exp_f32 v42, v42\n" \
  "v_mfma_f32_16x16x32_bf16 v[4:7], a[4:7], v[116:119], v[4:7]\n" \
  "v_exp_f32 v43, v43\n" \
  "v_add_f32 v40, 1.0, v40\n" \
  "v_add_f32 v41, 1.0, v41\n" \
  "v_mfma_f32_16x16x32_bf16 v[8:11], a[8:11], v[116:119], v[8:11]\n" \
  "v_add_f32 v42, 1.0, v42\n" \
  "v_add_f32 v43, 1.0, v43\n" \
  "v_rcp_f32 v40, v40\n" \
  "v_mfma_f32_16x16x32_bf16 v[12:15], a[12:15], v[116:119], v[12:15]\n" \
  "buffer_load_dwordx4 v71, s[40:43], s65 offen offset:3072 lds\n" \
  "v_rcp_f32 v41, v41\n" \
  "v_mfma_f32_16x16x32_bf16 v[16:19], a[16:19], v[116:119], v[16:19]\n" \
  "v_rcp_f32 v42, v42\n" \
  "v_mfma_f32_16x16x32_bf16 v[20:23], a[20:23], v[116:119], v[20:23]\n" \
  "v_rcp_f32 v43, v43\n" \
  "v_mul_f32 v36, v36, v40\n" \
  "v_mfma_f32_16x16x32_bf16 v[24:27], a[24:27], v[116:119], v[24:27]\n" \
  "v_mul_f32 v37, v37, v41\n" \
  "v_mul_f32 v38, v38, v42\n" \
  "v_mul_f32 v39, v39, v43\n" \
  "v_cvt_pk_bf16_f32 v40, v36, v37\n" \
  "v_cvt_pk_bf16_f32 v41, v38, v39\n" \
  "v_mfma_f32_16x16x32_bf16 v[28:31], a[28:31], v[116:119], v[28:31]\n" \
  "buffer_load_dwordx4 v[104:107], v59, s[36:39], s70 offen offset:2048 sc1\n" \
  "v_and_b32 v58, 0x3ff, v59\n" \
  "s_waitcnt vmcnt(23)\n" \
  "v_mfma_f32_16x16x32_bf16 v[0:3], a[32:35], v[120:123], v[0:3]\n" \
  "s_add_u32 m0, s57, 24576\n" \
  "v_add_u32 v58, s99, v58\n" \
  "v_mfma_f32_16x16x32_bf16 v[4:7], a[36:39], v[120:123], v[4:7]\n" \
  "v_add_u32 v46, s98, v58\n" \
  "ds_read_b64 v[56:57], v46 offset:28680\n" \
  "v_mfma_f32_16x16x32_bf16 v[8:11], a[40:43], v[120:123], v[8:11]\n" \
  "buffer_load_dwordx4 v68, s[40:43], s66 offen offset:0 lds\n" \
  "v_mfma_f32_16x16x32_bf16 v[12:15], a[44:47], v[120:123], v[12:15]\n" \
  "v_mfma_f32_16x16x32_bf16 v[16:19], a[48:51], v[120:123], v[16:19]\n" \
  "v_mfma_f32_16x16x32_bf16 v[20:23], a[52:55], v[120:123], v[20:23]\n" \
  "buffer_load_dwordx4 v69, s[40:43], s66 offen offset:1024 lds\n" \
  "v_mfma_f32_16x16x32_bf16 v[24:27], a[56:59], v[120:123], v[24:27]\n" \
  "s_waitcnt vmcnt(17)\n" \
  "ds_read_b64_tr_b16 a[144:145], v64 offset:8192\n" \
  "ds_read_b64_tr_b16 a[146:147], v64 offset:8704\n" \
  "v_mfma_f32_16x16x32_bf16 v[28:31], a[60:63], v[120:123], v[28:31]\n" \
  "ds_read_b64_tr_b16 a[148:149], v65 offset:8192\n" \
  "ds_read_b64_tr_b16 a[150:151], v65 offset:8704\n" \
  "v_mfma_f32_16x16x32_bf16 v[0:3], a[64:67], v[124:127], v[0:3]\n" \
  "buffer_load_dwordx4 v70, s[40:43], s66 offen offset:2048 lds\n" \
  "ds_read_b64_tr_b16 a[152:153], v66 offset:8192\n" \
  "ds_read_b64_tr_b16 a[154:155], v66 offset:8704\n" \
  "v_mfma_f32_16x16x32_bf16 v[4:7], a[68:71], v[124:127], v[4:7]\n" \
  "ds_read_b64_tr_b16 a[156:157], v67 offset:8192\n" \
  "ds_read_b64_tr_b16 a[158:159], v67 offset:8704\n" \
  "v_mfma_f32_16x16x32_bf16 v[8:11], a[72:75], v[124:127], v[8:11]\n" \
  "v_mfma_f32_16x16x32_bf16 v[12:15], a[76:79], v[124:127], v[12:15]\n" \
  "buffer_load_dwordx4 v71, s[40:43], s66 offen offset:3072 lds\n" \
  "v_mfma_f32_16x16x32_bf16 v[16:19], a[80:83], v[124:127], v[16:19]\n" \
  "s_waitcnt lgkmcnt(0)\n" \
  "s_barrier\n" \
  "s_mov_b32 m0, s58\n" \
  "v_mfma_f32_16x16x32_bf16 v[20:23], a[84:87], v[124:127], v[20:23]\n" \
  "v_mfma_f32_16x16x32_bf16 v[24:27], a[88:91], v[124:127], v[24:27]\n" \
  "buffer_load_dwordx4 v[196:199], v59, s[36:39], s50 offen offset:0 sc1\n" \
  "v_mfma_f32_16x16x32_bf16 v[28:31], a[92:95], v[124:127], v[28:31]\n" \
  "v_mfma_f32_16x16x32_bf16 v[0:3], a[96:99], v[192:195], v[0:3]\n" \
  "v_mfma_f32_16x16x32_bf16 v[4:7], a[100:103], v[192:195], v[4:7]\n" \
  "buffer_load_dwordx4 v[200:203], v59, s[36:39], s50 offen offset:1024 sc1\n" \
  "v_mfma_f32_16x16x32_bf16 v[8:11], a[104:107], v[192:195], v[8:11]\n" \
  "s_waitcnt vmcnt(17)\n" \
  "ds_read_b64_tr_b16 a[160:161], v64 offset:12288\n" \
  "ds_read_b64_tr_b16 a[162:163], v64 offset:12800\n" \
  "v_mfma_f32_16x16x32_bf16 v[12:15], a[108:111], v[192:195], v[12:15]\n" \
  "ds_read_b64_tr_b16 a[164:165], v65 offset:12288\n" \
  "ds_read_b64_tr_b16 a[166:167], v65 offset:12800\n" \
  "v_mfma_f32_16x16x32_bf16 v[224:227], a[112:115], v[92:95], v[224:227]\n" \
  "buffer_load_dwordx4 v[204:207], v59, s[36:39], s50 offen offset:2048 sc1\n" \
  "s_add_u32 s59, s59, 0x10000\n" \
  "buffer_load_dword v63, v61, s[36:39], s59 offen offset:0 sc1\n" \
  "v_mfma_f32_16x16x32_bf16 v[228:231], a[116:119], v[92:95], v[228:231]\n" \
  "v_add_u32 v59, 819200, v59\n" \
  "ds_read_b64_tr_b16 a[168:169], v66 offset:12288\n" \
  "ds_read_b64_tr_b16 a[170:171], v66 offset:12800\n" \
  "v_mfma_f32_16x16x32_bf16 v[232:235], a[120:123], v[92:95], v[232:235]\n" \
  "ds_read_b64_tr_b16 a[172:173], v67 offset:12288\n" \
  "ds_read_b64_tr_b16 a[174:175], v67 offset:12800\n" \
  "buffer_store_dwordx2 v[40:41], v61, s[36:39], s52 offen offset:0 sc1\n" \
  "v_mfma_f32_16x16x32_bf16 v[236:239], a[124:127], v[92:95], v[236:239]\n" \
  "buffer_load_dwordx4 v[72:75], v59, s[36:39], s68 offen offset:0 sc1\n" \
  "v_mfma_f32_16x16x32_bf16 v[208:211], a[128:131], v[96:99], v[208:211]\n" \
  "v_mfma_f32_16x16x32_bf16 v[212:215], a[132:135], v[96:99], v[212:215]\n" \
  "v_mfma_f32_16x16x32_bf16 v[216:219], a[136:139], v[96:99], v[216:219]\n" \
  "buffer_load_dwordx4 v[76:79], v59, s[36:39], s68 offen offset:1024 sc1\n" \
  "v_mfma_f32_16x16x32_bf16 v[220:223], a[140:143], v[96:99], v[220:223]\n" \
  "v_mfma_f32_16x16x32_bf16 v[224:227], a[144:147], v[96:99], v[224:227]\n" \
  "s_waitcnt vmcnt(16)\n" \
  "ds_read_b64_tr_b16 a[176:177], v64 offset:16384\n" \
  "ds_read_b64_tr_b16 a[178:179], v64 offset:16896\n" \
  "v_mfma_f32_16x16x32_bf16 v[228:231], a[148:151], v[96:99], v[228:231]\n" \
  "ds_read_b64_tr_b16 a[180:181], v65 offset:16384\n" \
  "ds_read_b64_tr_b16 a[182:183], v65 offset:16896\n" \
  "buffer_load_dwordx4 v[80:83], v59, s[36:39], s68 offen offset:2048 sc1\n" \
  "v_mfma_f32_16x16x32_bf16 v[232:235], a[152:155], v[96:99], v[232:235]\n" \
  "ds_read_b64_tr_b16 a[184:185], v66 offset:16384\n" \
  "ds_read_b64_tr_b16 a[186:187], v66 offset:16896\n" \
  "v_mfma_f32_16x16x32_bf16 v[236:239], a[156:159], v[96:99], v[236:239]\n" \
  "ds_read_b64_tr_b16 a[188:189], v67 offset:16384\n" \
  "ds_read_b64_tr_b16 a[190:191], v67 offset:16896\n" \
  "s_waitcnt lgkmcnt(8)\n" \
  "v_mfma_f32_16x16x32_bf16 v[208:211], a[160:163], v[100:103], v[208:211]\n" \
  "s_waitcnt vmcnt(13)\n" \
  "ds_read_b64_tr_b16 a[192:193], v64 offset:20480\n" \
  "ds_read_b64_tr_b16 a[194:195], v64 offset:20992\n" \
  "v_mfma_f32_16x16x32_bf16 v[212:215], a[164:167], v[100:103], v[212:215]\n" \
  "ds_read_b64_tr_b16 a[196:197], v65 offset:20480\n" \
  "ds_read_b64_tr_b16 a[198:199], v65 offset:20992\n" \
  "buffer_load_dwordx4 v[84:87], v59, s[36:39], s69 offen offset:0 sc1\n" \
  "v_mfma_f32_16x16x32_bf16 v[216:219], a[168:171], v[100:103], v[216:219]\n" \
  "ds_read_b64_tr_b16 a[200:201], v66 offset:20480\n" \
  "ds_read_b64_tr_b16 a[202:203], v66 offset:20992\n" \
  "v_mfma_f32_16x16x32_bf16 v[220:223], a[172:175], v[100:103], v[220:223]\n" \
  "ds_read_b64_tr_b16 a[204:205], v67 offset:20480\n" \
  "ds_read_b64_tr_b16 a[206:207], v67 offset:20992\n" \
  "s_waitcnt lgkmcnt(8)\n" \
  "v_mfma_f32_16x16x32_bf16 v[224:227], a[176:179], v[100:103], v[224:227]\n" \
  "s_waitcnt vmcnt(9)\n" \
  "ds_read_b64_tr_b16 a[208:209], v64 offset:24576\n" \
  "ds_read_b64_tr_b16 a[210:211], v64 offset:25088\n" \
  "v_mfma_f32_16x16x32_bf16 v[228:231], a[180:183], v[100:103], v[228:231]\n" \
  "ds_read_b64_tr_b16 a[212:213], v65 offset:24576\n" \
  "ds_read_b64_tr_b16 a[214:215], v65 offset:25088\n" \
  "buffer_load_dwordx4 v[88:91], v59, s[36:39], s69 offen offset:1024 sc1\n" \
  "v_mfma_f32_16x16x32_bf16 v[232:235], a[184:187], v[100:103], v[232:235]\n" \
  "ds_read_b64_tr_b16 a[216:217], v66 offset:24576\n" \
  "ds_read_b64_tr_b16 a[218:219], v66 offset:25088\n" \
  "v_mfma_f32_16x16x32_bf16 v[236:239], a[188:191], v[100:103], v[236:239]\n" \
  "ds_read_b64_tr_b16 a[220:221], v67 offset:24576\n" \
  "ds_read_b64_tr_b16 a[222:223], v67 offset:25088\n" \
  "s_waitcnt lgkmcnt(8)\n" \
  "v_mfma_f32_16x16x32_bf16 v[208:211], a[192:195], v[104:107], v[208:211]\n" \
  "buffer_load_dwordx4 v[92:95], v59, s[36:39], s69 offen offset:2048 sc1\n" \
  "v_mfma_f32_16x16x32_bf16 v[212:215], a[196:199], v[104:107], v[212:215]\n" \
  "v_mfma_f32_16x16x32_bf16 v[216:219], a[200:203], v[104:107], v[216:219]\n" \
  "v_mfma_f32_16x16x32_bf16 v[220:223], a[204:207], v[104:107], v[220:223]\n" \
  "buffer_load_dwordx4 v[96:99], v59, s[36:39], s70 offen offset:0 sc1\n" \
  "s_waitcnt lgkmcnt(0)\n" \
  "v_mfma_f32_16x16x32_bf16 v[224:227], a[208:211], v[104:107], v[224:227]\n" \
  "v_mfma_f32_16x16x32_bf16 v[228:231], a[212:215], v[104:107], v[228:231]\n" \
  "v_mfma_f32_16x16x32_bf16 v[232:235], a[216:219], v[104:107], v[232:235]\n" \
  "buffer_load_dwordx4 v[100:103], v59, s[36:39], s70 offen offset:1024 sc1\n" \
  "v_mfma_f32_16x16x32_bf16 v[236:239], a[220:223], v[104:107], v[236:239]\n" \
  "v_mfma_f32_16x16x32_bf16 v[16:19], a[112:115], v[192:195], v[16:19]\n" \
  "v_mfma_f32_16x16x32_bf16 v[20:23], a[116:119], v[192:195], v[20:23]\n" \
  "buffer_load_dwordx4 v[104:107], v59, s[36:39], s70 offen offset:2048 sc1\n" \
  "ds_write_addtid_b32 v208 offset:0\n" \
  "ds_write_addtid_b32 v209 offset:256\n" \
  "v_mfma_f32_16x16x32_bf16 v[24:27], a[120:123], v[192:195], v[24:27]\n" \
  "ds_write_addtid_b32 v210 offset:512\n" \
  "ds_write_addtid_b32 v211 offset:768\n" \
  "v_mfma_f32_16x16x32_bf16 v[28:31], a[124:127], v[192:195], v[28:31]\n" \
  "ds_write_addtid_b32 v212 offset:1024\n" \
  "ds_write_addtid_b32 v213 offset:1280\n" \
  "s_waitcnt vmcnt(13)\n" \
  "v_mfma_f32_16x16x32_bf16 v[0:3], a[128:131], v[196:199], v[0:3]\n" \
  "ds_write_addtid_b32 v214 offset:1536\n" \
  "ds_write_addtid_b32 v215 offset:1792\n" \
  "v_mfma_f32_16x16x32_bf16 v[4:7], a[132:135], v[196:199], v[4:7]\n" \
  "ds_write_addtid_b32 v216 offset:2048\n" \
  "ds_write_addtid_b32 v217 offset:2304\n" \
  "v_mfma_f32_16x16x32_bf16 v[8:11], a[136:139], v[196:199], v[8:11]\n" \
  "ds_write_addtid_b32 v218 offset:2560\n" \
  "ds_write_addtid_b32 v219 offset:2816\n" \
  "v_mfma_f32_16x16x32_bf16 v[12:15], a[140:143], v[196:199], v[12:15]\n" \
  "ds_write_addtid_b32 v220 offset:3072\n" \
  "ds_write_addtid_b32 v221 offset:3328\n" \
  "v_mfma_f32_16x16x32_bf16 v[16:19], a[144:147], v[196:199], v[16:19]\n" \
  "ds_write_addtid_b32 v222 offset:3584\n" \
  "ds_write_addtid_b32 v223 offset:3840\n" \
  "v_mfma_f32_16x16x32_bf16 v[20:23], a[148:151], v[196:199], v[20:23]\n" \
  "ds_write_addtid_b32 v224 offset:4096\n" \
  "ds_write_addtid_b32 v225 offset:4352\n" \
  "v_mfma_f32_16x16x32_bf16 v[24:27], a[152:155], v[196:199], v[24:27]\n" \
  "ds_write_addtid_b32 v226 offset:4608\n" \
  "ds_write_addtid_b32 v227 offset:4864\n" \
  "v_mfma_f32_16x16x32_bf16 v[28:31], a[156:159], v[196:199], v[28:31]\n" \
  "ds_write_addtid_b32 v228 offset:5120\n" \
  "ds_write_addtid_b32 v229 offset:5376\n" \
  "s_waitcnt vmcnt(12)\n" \
  "v_mfma_f32_16x16x32_bf16 v[0:3], a[160:163], v[200:203], v[0:3]\n" \
  "ds_write_addtid_b32 v230 offset:5632\n" \
  "ds_write_addtid_b32 v231 offset:5888\n" \
  "v_mfma_f32_16x16x32_bf16 v[4:7], a[164:167], v[200:203], v[4:7]\n" \
  "ds_write_addtid_b32 v232 offset:6144\n" \
  "ds_write_addtid_b32 v233 offset:6400\n" \
  "v_mfma_f32_16x16x32_bf16 v[8:11], a[168:171], v[200:203], v[8:11]\n" \
  "ds_write_addtid_b32 v234 offset:6656\n" \
  "ds_write_addtid_b32 v235 offset:6912\n" \
  "v_mfma_f32_16x16x32_bf16 v[12:15], a[172:175], v[200:203], v[12:15]\n" \
  "ds_write_addtid_b32 v236 offset:7168\n" \
  "ds_write_addtid_b32 v237 offset:7424\n" \
  "v_mfma_f32_16x16x32_bf16 v[16:19], a[176:179], v[200:203], v[16:19]\n" \
  "ds_write_addtid_b32 v238 offset:7680\n" \
  "ds_write_addtid_b32 v239 offset:7936\n" \
  "v_mfma_f32_16x16x32_bf16 v[20:23], a[180:183], v[200:203], v[20:23]\n" \
  "v_mfma_f32_16x16x32_bf16 v[24:27], a[184:187], v[200:203], v[24:27]\n" \
  "v_mfma_f32_16x16x32_bf16 v[28:31], a[188:191], v[200:203], v[28:31]\n" \
  "s_waitcnt vmcnt(11)\n" \
  "v_mfma_f32_16x16x32_bf16 v[0:3], a[192:195], v[204:207], v[0:3]\n" \
  "v_mfma_f32_16x16x32_bf16 v[4:7], a[196:199], v[204:207], v[4:7]\n" \
  "v_mfma_f32_16x16x32_bf16 v[8:11], a[200:203], v[204:207], v[8:11]\n" \
  "s_waitcnt lgkmcnt(0)\n" \
  "s_barrier\n" \
  "ds_read_b128 v[32:35], v58 offset:0\n" \
  "v_mfma_f32_16x16x32_bf16 v[12:15], a[204:207], v[204:207], v[12:15]\n" \
  "ds_read_b128 v[36:39], v58 offset:8192\n" \
  "ds_read_b128 v[40:43], v58 offset:16384\n" \
  "v_mfma_f32_16x16x32_bf16 v[16:19], a[208:211], v[204:207], v[16:19]\n" \
  "ds_read_b128 v[44:47], v58 offset:24576\n" \
  "ds_read_b128 v[48:51], v58 offset:4096\n" \
  "v_mfma_f32_16x16x32_bf16 v[20:23], a[212:215], v[204:207], v[20:23]\n" \
  "ds_read_b128 v[240:243], v58 offset:12288\n" \
  "ds_read_b128 v[244:247], v58 offset:20480\n" \
  "v_mfma_f32_16x16x32_bf16 v[24:27], a[216:219], v[204:207], v[24:27]\n" \
  "ds_read_b128 v[248:251], v58 offset:28672\n" \
  "v_mfma_f32_16x16x32_bf16 v[28:31], a[220:223], v[204:207], v[28:31]\n" \
  "s_waitcnt vmcnt(7)\n" \
  "v_mfma_f32_16x16x32_bf16 v[208:211], v[128:131], v[72:75], 0\n" \
  "s_mov_b64 exec, 1\n" \
  "buffer_store_dword v61, off, s[36:39], s54 offset:0 sc1\n" \
  "s_mov_b64 exec, -1\n" \
  "v_mfma_f32_16x16x32_bf16 v[212:215], v[132:135], v[72:75], 0\n" \
  "v_cmp_eq_u32 vcc, 0, v63\n" \
  "ds_write_addtid_b32 v0 offset:32768\n" \
  "v_mfma_f32_16x16x32_bf16 v[216:219], v[136:139], v[72:75], 0\n" \
  "ds_write_addtid_b32 v1 offset:33024\n" \
  "ds_write_addtid_b32 v2 offset:33280\n" \
  "v_lshlrev_b32 v52, 5, v59\n" \
  "v_mfma_f32_16x16x32_bf16 v[220:223], v[140:143], v[72:75], 0\n" \
  "s_and_b64 vcc, vcc, s[86:87]\n" \
  "s_cbranch_scc1 .LsbS_%=\n" \
  ".LbbS_%=:\n" \
  "buffer_load_dwordx4 v[108:111], v59, s[36:39], s48 offen offset:0 sc1\n" \
  "v_mfma_f32_16x16x32_bf16 v[224:227], v[144:147], v[72:75], 0\n" \
  "ds_write_addtid_b32 v3 offset:33536\n" \
  "ds_write_addtid_b32 v4 offset:33792\n" \
  "s_waitcnt lgkmcnt(9)\n" \
  "v_mfma_f32_16x16x32_bf16 v[228:231], v[148:151], v[72:75], 0\n" \
  "ds_write_addtid_b32 v5 offset:34048\n" \
  "ds_write_addtid_b32 v6 offset:34304\n" \
  "v_add_f32 v32, v32, v36\n" \
  "v_mfma_f32_16x16x32_bf16 v[232:235], v[152:155], v[72:75], 0\n" \
  "buffer_load_dwordx4 v[112:115], v59, s[36:39], s48 offen offset:1024 sc1\n" \
  "ds_write_addtid_b32 v7 offset:34560\n" \
  "ds_write_addtid_b32 v8 offset:34816\n" \
  "v_mfma_f32_16x16x32_bf16 v[236:239], v[156:159], v[72:75], 0\n" \
  "ds_write_addtid_b32 v9 offset:35072\n" \
  "ds_write_addtid_b32 v10 offset:35328\n" \
  "v_add_f32 v33, v33, v37\n" \
  "v_mfma_f32_16x16x32_bf16 v[208:211], v[160:163], v[76:79], v[208:211]\n" \
  "ds_write_addtid_b32 v11 offset:35584\n" \
  "ds_write_addtid_b32 v12 offset:35840\n" \
  "v_add_f32 v34, v34, v38\n" \
  "v_mfma_f32_16x16x32_bf16 v[212:215], v[164:167], v[76:79], v[212:215]\n" \
  "buffer_load_dwordx4 v[116:119], v59, s[36:39], s48 offen offset:2048 sc1\n" \
  "ds_write_addtid_b32 v13 offset:36096\n" \
  "ds_write_addtid_b32 v14 offset:36352\n" \
  "v_mfma_f32_16x16x32_bf16 v[216:219], v[168:171], v[76:79], v[216:219]\n" \
  "ds_write_addtid_b32 v15 offset:36608\n" \
  "ds_write_addtid_b32 v16 offset:36864\n" \
  "v_add_f32 v35, v35, v39\n" \
  "v_mfma_f32_16x16x32_bf16 v[220:223], v[172:175], v[76:79], v[220:223]\n" \
  "ds_write_addtid_b32 v17 offset:37120\n" \
  "ds_write_addtid_b32 v18 offset:37376\n" \
  "v_add_f32 v40, v40, v44\n" \
  "v_mfma_f32_16x16x32_bf16 v[224:227], v[176:179], v[76:79], v[224:227]\n" \
  "buffer_load_dwordx4 v[120:123], v59, s[36:39], s49 offen offset:0 sc1\n" \
  "ds_write_addtid_b32 v19 offset:37632\n" \
  "ds_write_addtid_b32 v20 offset:37888\n" \
  "v_mfma_f32_16x16x32_bf16 v[228:231], v[180:183], v[76:79], v[228:231]\n" \
  "ds_write_addtid_b32 v21 offset:38144\n" \
  "ds_write_addtid_b32 v22 offset:38400\n" \
  "v_add_f32 v41, v41, v45\n" \
  "v_mfma_f32_16x16x32_bf16 v[232:235], v[184:187], v[76:79], v[232:235]\n" \
  "ds_write_addtid_b32 v23 offset:38656\n" \
  "ds_write_addtid_b32 v24 offset:38912\n" \
  "v_add_f32 v42, v42, v46\n" \
  "v_mfma_f32_16x16x32_bf16 v[236:239], v[188:191], v[76:79], v[236:239]\n" \
  "buffer_load_dwordx4 v[124:127], v59, s[36:39], s49 offen offset:1024 sc1\n" \
  "ds_write_addtid_b32 v25 offset:39168\n" \
  "s_waitcnt vmcnt(11)\n" \
  "v_mfma_f32_16x16x32_bf16 v[208:211], a[0:3], v[80:83], v[208:211]\n" \
  "ds_write_addtid_b32 v26 offset:39424\n" \
  "ds_write_addtid_b32 v27 offset:39680\n" \
  "v_add_f32 v43, v43, v47\n" \
  "v_mfma_f32_16x16x32_bf16 v[212:215], a[4:7], v[80:83], v[212:215]\n" \
  "ds_write_addtid_b32 v28 offset:39936\n" \
  "ds_write_addtid_b32 v29 offset:40192\n" \
  "v_add_f32 v32, v32, v40\n" \
  "v_mfma_f32_16x16x32_bf16 v[216:219], a[8:11], v[80:83], v[216:219]\n" \
  "buffer_load_dwordx4 v[192:195], v59, s[36:39], s49 offen offset:2048 sc1\n" \
  "ds_write_addtid_b32 v30 offset:40448\n" \
  "ds_write_addtid_b32 v31 offset:40704\n" \
  "v_mfma_f32_16x16x32_bf16 v[220:223], a[12:15], v[80:83], v[220:223]\n" \
  "v_add_f32 v33, v33, v41\n" \
  "v_add_f32 v34, v34, v42\n" \
  "v_add_f32 v35, v35, v43\n" \
  "v_mfma_f32_16x16x32_bf16 v[224:227], a[16:19], v[80:83], v[224:227]\n" \
  "v_cvt_pk_bf16_f32 v36, v32, v33\n" \
  "v_cvt_pk_bf16_f32 v37, v34, v35\n" \
  "s_waitcnt lgkmcnt(15)\n" \
  "v_mfma_f32_16x16x32_bf16 v[228:231], a[20:23], v[80:83], v[228:231]\n" \
  "buffer_load_dwordx4 v[196:199], v59, s[36:39], s50 offen offset:0 sc1\n" \
  "v_add_f32 v48, v48, v240\n" \
  "v_add_f32 v49, v49, v241\n" \
  "v_mfma_f32_16x16x32_bf16 v[232:235], a[24:27], v[80:83], v[232:235]\n" \
  "v_add_f32 v50, v50, v242\n" \
  "v_add_f32 v51, v51, v243\n" \
  "v_add_f32 v244, v244, v248\n" \
  "v_mfma_f32_16x16x32_bf16 v[236:239], a[28:31], v[80:83], v[236:239]\n" \
  "v_add_f32 v245, v245, v249\n" \
  "v_add_f32 v246, v246, v250\n" \
  "v_add_f32 v247, v247, v251\n" \
  "v_mfma_f32_16x16x32_bf16 v[208:211], a[32:35], v[84:87], v[208:211]\n" \
  "buffer_load_dwordx4 v[200:203], v59, s[36:39], s50 offen offset:1024 sc1\n" \
  "s_waitcnt lgkmcnt(0)\n" \
  "s_barrier\n" \
  "v_mfma_f32_16x16x32_bf16 v[212:215], a[36:39], v[84:87], v[212:215]\n" \
  "v_add_f32 v48, v48, v244\n" \
  "v_add_f32 v49, v49, v245\n" \
  "v_add_f32 v50, v50, v246\n" \
  "v_mfma_f32_16x16x32_bf16 v[216:219], a[40:43], v[84:87], v[216:219]\n" \
  "v_and_b32 v52, 0x1800, v52\n" \
  "v_add_f32 v51, v51, v247\n" \
  "v_and_b32 v53, 0x30, v59\n" \
  "v_mfma_f32_16x16x32_bf16 v[220:223], a[44:47], v[84:87], v[220:223]\n" \
  "buffer_load_dwordx4 v[204:207], v59, s[36:39], s50 offen offset:2048 sc1\n" \
  "v_cvt_pk_bf16_f32 v38, v48, v49\n" \
  "v_lshl_or_b32 v52, v53, 1, v52\n" \
  "v_mfma_f32_16x16x32_bf16 v[224:227], a[48:51], v[84:87], v[224:227]\n" \
  "v_cvt_pk_bf16_f32 v39, v50, v51\n" \
  "buffer_load_dword v53, v52, s[36:39], s30 offen offset:0 sc1\n" \
  "v_mfma_f32_16x16x32_bf16 v[228:231], a[52:55], v[84:87], v[228:231]\n" \
  "v_mfma_f32_16x16x32_bf16 v[232:235], a[56:59], v[84:87], v[232:235]\n" \
  "buffer_store_dwordx4 v[36:39], v59, s[36:39], s74 offen offset:0 sc1\n" \
  "ds_read_b128 v[32:35], v58 offset:32768\n" \
  "s_nop 0\n" \
  "ds_read_b128 v[36:39], v58 offset:40960\n" \
  "v_mfma_f32_16x16x32_bf16 v[236:239], a[60:63], v[84:87], v[236:239]\n" \
  "ds_read_b128 v[40:43], v58 offset:49152\n" \
  "ds_read_b128 v[44:47], v58 offset:57344\n" \
  "s_waitcnt vmcnt(15)\n" \
  "v_mfma_f32_16x16x32_bf16 v[208:211], a[64:67], v[88:91], v[208:211]\n" \
  "ds_read_b128 v[48:51], v58 offset:36864\n" \
  "ds_read_b128 v[240:243], v58 offset:45056\n" \
  "v_mfma_f32_16x16x32_bf16 v[212:215], a[68:71], v[88:91], v[212:215]\n" \
  "ds_read_b128 v[244:247], v58 offset:53248\n" \
  "ds_read_b128 v[248:251], v58 offset:61440\n" \
  "v_mfma_f32_16x16x32_bf16 v[216:219], a[72:75], v[88:91], v[216:219]\n" \
  "v_mfma_f32_16x16x32_bf16 v[220:223], a[76:79], v[88:91], v[220:223]\n" \
  "v_mfma_f32_16x16x32_bf16 v[224:227], a[80:83], v[88:91], v[224:227]\n" \
  "v_mfma_f32_16x16x32_bf16 v[228:231], a[84:87], v[88:91], v[228:231]\n" \
  "v_mfma_f32_16x16x32_bf16 v[232:235], a[88:91], v[88:91], v[232:235]\n" \
  "v_mfma_f32_16x16x32_bf16 v[236:239], a[92:95], v[88:91], v[236:239]\n" \
  "s_waitcnt lgkmcnt(4)\n" \
  "v_add_f32 v32, v32, v36\n" \
  "v_add_f32 v33, v33, v37\n" \
  "v_mfma_f32_16x16x32_bf16 v[208:211], a[96:99], v[92:95], v[208:211]\n" \
  "v_add_f32 v34, v34, v38\n" \
  "v_add_f32 v35, v35, v39\n" \
  "v_add_f32 v40, v40, v44\n" \
  "v_mfma_f32_16x16x32_bf16 v[212:215], a[100:103], v[92:95], v[212:215]\n" \
  "v_add_f32 v41, v41, v45\n" \
  "v_add_f32 v42, v42, v46\n" \
  "v_add_f32 v43, v43, v47\n" \
  "v_mfma_f32_16x16x32_bf16 v[216:219], a[104:107], v[92:95], v[216:219]\n" \
  "v_add_f32 v32, v32, v40\n" \
  "v_add_f32 v33, v33, v41\n" \
  "v_add_f32 v34, v34, v42\n" \
  "v_mfma_f32_16x16x32_bf16 v[220:223], a[108:111], v[92:95], v[220:223]\n" \
  "v_add_f32 v35, v35, v43\n" \
  "v_cvt_pk_bf16_f32 v36, v32, v33\n" \
  "v_cvt_pk_bf16_f32 v37, v34, v35\n" \
  "v_mfma_f32_16x16x32_bf16 v[224:227], a[112:115], v[92:95], v[224:227]\n" \
  "s_waitcnt lgkmcnt(0)\n" \
  "v_add_f32 v48, v48, v240\n" \
  "v_add_f32 v49, v49, v241\n" \
  "v_mfma_f32_16x16x32_bf16 v[228:231], a[116:119], v[92:95], v[228:231]\n" \
  "v_add_f32 v50, v50, v242\n" \
  "v_add_f32 v51, v51, v243\n" \
  "v_add_f32 v244, v244, v248\n" \
  "v_mfma_f32_16x16x32_bf16 v[232:235], a[120:123], v[92:95], v[232:235]\n" \
  "v_add_f32 v245, v245, v249\n" \
  "v_add_f32 v246, v246, v250\n" \
  "v_add_f32 v247, v247, v251\n" \
  "v_mfma_f32_16x16x32_bf16 v[236:239], a[124:127], v[92:95], v[236:239]\n" \
  "v_add_f32 v48, v48, v244\n" \
  "v_add_f32 v49, v49, v245\n" \
  "s_waitcnt vmcnt(13)\n" \
  "v_mfma_f32_16x16x32_bf16 v[208:211], a[128:131], v[96:99], v[208:211]\n" \
  "v_add_f32 v50, v50, v246\n" \
  "v_add_f32 v51, v51, v247\n" \
  "v_cvt_pk_bf16_f32 v38, v48, v49\n" \
  "v_mfma_f32_16x16x32_bf16 v[212:215], a[132:135], v[96:99], v[212:215]\n" \
  "v_cvt_pk_bf16_f32 v39, v50, v51\n" \
  "v_add_u32 v59, 409600, v59\n" \
  "buffer_store_dwordx4 v[36:39], v59, s[36:39], s74 offen offset:0 sc1\n" \
  "v_mfma_f32_16x16x32_bf16 v[216:219], a[136:139], v[96:99], v[216:219]\n" \
  "v_mfma_f32_16x16x32_bf16 v[220:223], a[140:143], v[96:99], v[220:223]\n" \
  "v_mfma_f32_16x16x32_bf16 v[224:227], a[144:147], v[96:99], v[224:227]\n" \
  "v_mfma_f32_16x16x32_bf16 v[228:231], a[148:151], v[96:99], v[228:231]\n" \
  "v_mfma_f32_16x16x32_bf16 v[232:235], a[152:155], v[96:99], v[232:235]\n" \
  "v_mfma_f32_16x16x32_bf16 v[236:239], a[156:159], v[96:99], v[236:239]\n" \
  "v_mfma_f32_16x16x32_bf16 v[208:211], a[160:163], v[100:103], v[208:211]\n" \
  "v_mfma_f32_16x16x32_bf16 v[212:215], a[164:167], v[100:103], v[212:215]\n" \
  "v_mfma_f32_16x16x32_bf16 v[216:219], a[168:171], v[100:103], v[216:219]\n" \
  "v_mfma_f32_16x16x32_bf16 v[220:223], a[172:175], v[100:103], v[220:223]\n" \
  "v_mfma_f32_16x16x32_bf16 v[224:227], a[176:179], v[100:103], v[224:227]\n" \
  "v_mfma_f32_16x16x32_bf16 v[228:231], a[180:183], v[100:103], v[228:231]\n" \
  "v_mfma_f32_16x16x32_bf16 v[232:235], a[184:187], v[100:103], v[232:235]\n" \
  "v_mfma_f32_16x16x32_bf16 v[236:239], a[188:191], v[100:103], v[236:239]\n" \
  "s_waitcnt vmcnt(13)\n" \
  "v_mfma_f32_16x16x32_bf16 v[208:211], a[192:195], v[104:107], v[208:211]\n" \
  "v_mfma_f32_16x16x32_bf16 v[212:215], a[196:199], v[104:107], v[212:215]\n" \
  "v_mfma_f32_16x16x32_bf16 v[216:219], a[200:203], v[104:107], v[216:219]\n" \
  "v_mfma_f32_16x16x32_bf16 v[220:223], a[204:207], v[104:107], v[220:223]\n" \
  "v_mfma_f32_16x16x32_bf16 v[224:227], a[208:211], v[104:107], v[224:227]\n" \
  "v_mfma_f32_16x16x32_bf16 v[228:231], a[212:215], v[104:107], v[228:231]\n" \
  "v_mfma_f32_16x16x32_bf16 v[232:235], a[216:219], v[104:107], v[232:235]\n" \
  "v_mfma_f32_16x16x32_bf16 v[236:239], a[220:223], v[104:107], v[236:239]\n" \
  ".Le_%=:\n" \
  "s_waitcnt vmcnt(10)\n" \
  "v_mfma_f32_16x16x32_bf16 v[0:3], v[128:131], v[108:111], 0\n" \
  "v_add_u32 v59, 409600, v59\n" \
  "v_mfma_f32_16x16x32_bf16 v[4:7], v[132:135], v[108:111], 0\n" \
  "ds_write_addtid_b32 v208 offset:0\n" \
  "v_mfma_f32_16x16x32_bf16 v[8:11], v[136:139], v[108:111], 0\n" \
  "buffer_load_dwordx4 v[72:75], v59, s[36:39], s68 offen offset:0 sc1\n" \
  "ds_write_addtid_b32 v209 offset:256\n" \
  "v_mfma_f32_16x16x32_bf16 v[12:15], v[140:143], v[108:111], 0\n" \
  "ds_write_addtid_b32 v210 offset:512\n" \
  "v_mfma_f32_16x16x32_bf16 v[16:19], v[144:147], v[108:111], 0\n" \
  "ds_write_addtid_b32 v211 offset:768\n" \
  "v_mfma_f32_16x16x32_bf16 v[20:23], v[148:151], v[108:111], 0\n" \
  "ds_write_addtid_b32 v212 offset:1024\n" \
  "v_mfma_f32_16x16x32_bf16 v[24:27], v[152:155], v[108:111], 0\n" \
  "ds_write_addtid_b32 v213 offset:1280\n" \
  "v_mfma_f32_16x16x32_bf16 v[28:31], v[156:159], v[108:111], 0\n" \
  "ds_write_addtid_b32 v214 offset:1536\n" \
  "v_mfma_f32_16x16x32_bf16 v[0:3], v[160:163], v[112:115], v[0:3]\n" \
  "buffer_load_dwordx4 v[76:79], v59, s[36:39], s68 offen offset:1024 sc1\n" \
  "ds_write_addtid_b32 v215 offset:1792\n" \
  "v_mfma_f32_16x16x32_bf16 v[4:7], v[164:167], v[112:115], v[4:7]\n" \
  "ds_write_addtid_b32 v216 offset:2048\n" \
  "v_mfma_f32_16x16x32_bf16 v[8:11], v[168:171], v[112:115], v[8:11]\n" \
  "ds_write_addtid_b32 v217 offset:2304\n" \
  "v_mfma_f32_16x16x32_bf16 v[12:15], v[172:175], v[112:115], v[12:15]\n" \
  "ds_write_addtid_b32 v218 offset:2560\n" \
  "v_mfma_f32_16x16x32_bf16 v[16:19], v[176:179], v[112:115], v[16:19]\n" \
  "ds_write_addtid_b32 v219 offset:2816\n" \
  "v_mfma_f32_16x16x32_bf16 v[20:23], v[180:183], v[112:115], v[20:23]\n" \
  "ds_write_addtid_b32 v220 offset:3072\n" \
  "v_mfma_f32_16x16x32_bf16 v[24:27], v[184:187], v[112:115], v[24:27]\n" \
  "buffer_load_dwordx4 v[80:83], v59, s[36:39], s68 offen offset:2048 sc1\n" \
  "ds_write_addtid_b32 v221 offset:3328\n" \
  "v_mfma_f32_16x16x32_bf16 v[28:31], v[188:191], v[112:115], v[28:31]\n" \
  "ds_write_addtid_b32 v222 offset:3584\n" \
  "s_waitcnt vmcnt(11)\n" \
  "v_mfma_f32_16x16x32_bf16 v[0:3], a[0:3], v[116:119], v[0:3]\n" \
  "ds_write_addtid_b32 v223 offset:3840\n" \
  "v_mfma_f32_16x16x32_bf16 v[4:7], a[4:7], v[116:119], v[4:7]\n" \
  "ds_write_addtid_b32 v224 offset:4096\n" \
  "v_mfma_f32_16x16x32_bf16 v[8:11], a[8:11], v[116:119], v[8:11]\n" \
  "ds_write_addtid_b32 v225 offset:4352\n" \
  "v_mfma_f32_16x16x32_bf16 v[12:15], a[12:15], v[116:119], v[12:15]\n" \
  "ds_write_addtid_b32 v226 offset:4608\n" \
  "v_mfma_f32_16x16x32_bf16 v[16:19], a[16:19], v[116:119], v[16:19]\n" \
  "buffer_load_dwordx4 v[84:87], v59, s[36:39], s69 offen offset:0 sc1\n" \
  "ds_write_addtid_b32 v227 offset:4864\n" \
  "v_mfma_f32_16x16x32_bf16 v[20:23], a[20:23], v[116:119], v[20:23]\n" \
  "ds_write_addtid_b32 v228 offset:5120\n" \
  "v_mfma_f32_16x16x32_bf16 v[24:27], a[24:27], v[116:119], v[24:27]\n" \
  "ds_write_addtid_b32 v229 offset:5376\n" \
  "v_mfma_f32_16x16x32_bf16 v[28:31], a[28:31], v[116:119], v[28:31]\n" \
  "ds_write_addtid_b32 v230 offset:5632\n" \
  "v_mfma_f32_16x16x32_bf16 v[0:3], a[32:35], v[120:123], v[0:3]\n" \
  "s_add_u32 s59, s59, 0x10000\n" \
  "buffer_load_dword v63, v61, s[36:39], s59 offen offset:0 sc1\n" \
  "v_mfma_f32_16x16x32_bf16 v[4:7], a[36:39], v[120:123], v[4:7]\n" \
  "ds_write_addtid_b32 v231 offset:5888\n" \
  "v_mfma_f32_16x16x32_bf16 v[8:11], a[40:43], v[120:123], v[8:11]\n" \
  "buffer_load_dwordx4 v[88:91], v59, s[36:39], s69 offen offset:1024 sc1\n" \
  "ds_write_addtid_b32 v232 offset:6144\n" \
  "v_mfma_f32_16x16x32_bf16 v[12:15], a[44:47], v[120:123], v[12:15]\n" \
  "ds_write_addtid_b32 v233 offset:6400\n" \
  "v_mfma_f32_16x16x32_bf16 v[16:19], a[48:51], v[120:123], v[16:19]\n" \
  "ds_write_addtid_b32 v234 offset:6656\n" \
  "v_mfma_f32_16x16x32_bf16 v[20:23], a[52:55], v[120:123], v[20:23]\n" \
  "ds_write_addtid_b32 v235 offset:6912\n" \
  "v_mfma_f32_16x16x32_bf16 v[24:27], a[56:59], v[120:123], v[24:27]\n" \
  "ds_write_addtid_b32 v236 offset:7168\n" \
  "v_mfma_f32_16x16x32_bf16 v[28:31], a[60:63], v[120:123], v[28:31]\n" \
  "ds_write_addtid_b32 v237 offset:7424\n" \
  "s_waitcnt vmcnt(12)\n" \
  "v_mfma_f32_16x16x32_bf16 v[0:3], a[64:67], v[124:127], v[0:3]\n" \
  "buffer_load_dwordx4 v[92:95], v59, s[36:39], s69 offen offset:2048 sc1\n" \
  "ds_write_addtid_b32 v238 offset:7680\n" \
  "v_mfma_f32_16x16x32_bf16 v[4:7], a[68:71], v[124:127], v[4:7]\n" \
  "ds_write_addtid_b32 v239 offset:7936\n" \
  "v_mfma_f32_16x16x32_bf16 v[8:11], a[72:75], v[124:127], v[8:11]\n" \
  "s_waitcnt vmcnt(9)\n" \
  "v_cmp_eq_u32 vcc, 0, v53\n" \
  "v_mfma_f32_16x16x32_bf16 v[12:15], a[76:79], v[124:127], v[12:15]\n" \
  "v_mfma_f32_16x16x32_bf16 v[16:19], a[80:83], v[124:127], v[16:19]\n" \
  "s_cbranch_vccnz .Lspf_%=\n" \
  ".Lbpf_%=:\n" \
  "v_bfe_u32 v69, v59, 2, 8\n" \
  "v_mfma_f32_16x16x32_bf16 v[20:23], a[84:87], v[124:127], v[20:23]\n" \
  "buffer_load_dword v64, v69, s[36:39], s79 offen offset:0 sc1\n" \
  "buffer_load_dword v65, v69, s[36:39], s80 offen offset:0 sc1\n" \
  "v_mfma_f32_16x16x32_bf16 v[24:27], a[88:91], v[124:127], v[24:27]\n" \
  "buffer_load_dwordx4 v[96:99], v59, s[36:39], s70 offen offset:0 sc1\n" \
  "buffer_load_dword v66, v69, s[36:39], s81 offen offset:0 sc1\n" \
  "v_mfma_f32_16x16x32_bf16 v[28:31], a[92:95], v[124:127], v[28:31]\n" \
  "s_waitcnt lgkmcnt(0)\n" \
  "s_barrier\n" \
  "v_mfma_f32_16x16x32_bf16 v[0:3], a[96:99], v[192:195], v[0:3]\n" \
  "ds_read_b128 v[32:35], v58 offset:0\n" \
  "buffer_load_dword v67, v69, s[36:39], s82 offen offset:0 sc1\n" \
  "v_mfma_f32_16x16x32_bf16 v[4:7], a[100:103], v[192:195], v[4:7]\n" \
  "ds_read_b128 v[36:39], v58 offset:8192\n" \
  "buffer_load_dword v68, v57, s[36:39], s90 offen offset:0 sc1\n" \
  "v_mfma_f32_16x16x32_bf16 v[8:11], a[104:107], v[192:195], v[8:11]\n" \
  "ds_read_b128 v[40:43], v58 offset:16384\n" \
  "v_add_u32 v70, s98, v58\n" \
  "v_mfma_f32_16x16x32_bf16 v[12:15], a[108:111], v[192:195], v[12:15]\n" \
  "ds_read_b128 v[44:47], v58 offset:24576\n" \
  "v_mfma_f32_16x16x32_bf16 v[16:19], a[112:115], v[192:195], v[16:19]\n" \
  "buffer_load_dwordx4 v[100:103], v59, s[36:39], s70 offen offset:1024 sc1\n" \
  "ds_read_b128 v[48:51], v58 offset:4096\n" \
  "v_mfma_f32_16x16x32_bf16 v[20:23], a[116:119], v[192:195], v[20:23]\n" \
  "ds_read_b128 v[240:243], v58 offset:12288\n" \
  "v_mfma_f32_16x16x32_bf16 v[24:27], a[120:123], v[192:195], v[24:27]\n" \
  "ds_read_b128 v[244:247], v58 offset:20480\n" \
  "v_mfma_f32_16x16x32_bf16 v[28:31], a[124:127], v[192:195], v[28:31]\n" \
  "ds_read_b128 v[248:251], v58 offset:28672\n" \
  "v_mfma_f32_16x16x32_bf16 v[0:3], a[128:131], v[196:199], v[0:3]\n" \
  "ds_read_b64 v[70:71], v70 offset:29704\n" \
  "v_mfma_f32_16x16x32_bf16 v[4:7], a[132:135], v[196:199], v[4:7]\n" \
  "v_mfma_f32_16x16x32_bf16 v[8:11], a[136:139], v[196:199], v[8:11]\n" \
  "buffer_load_dwordx4 v[104:107], v59, s[36:39], s70 offen offset:2048 sc1\n" \
  "v_mfma_f32_16x16x32_bf16 v[12:15], a[140:143], v[196:199], v[12:15]\n" \
  "s_waitcnt lgkmcnt(5)\n" \
  "v_add_f32 v32, v32, v36\n" \
  "v_mfma_f32_16x16x32_bf16 v[16:19], a[144:147], v[196:199], v[16:19]\n" \
  "v_add_f32 v33, v33, v37\n" \
  "v_add_f32 v34, v34, v38\n" \
  "v_mfma_f32_16x16x32_bf16 v[20:23], a[148:151], v[196:199], v[20:23]\n" \
  "v_add_f32 v35, v35, v39\n" \
  "v_add_f32 v40, v40, v44\n" \
  "v_mfma_f32_16x16x32_bf16 v[24:27], a[152:155], v[196:199], v[24:27]\n" \
  "v_add_f32 v41, v41, v45\n" \
  "v_add_f32 v42, v42, v46\n" \
  "v_mfma_f32_16x16x32_bf16 v[28:31], a[156:159], v[196:199], v[28:31]\n" \
  "v_add_f32 v43, v43, v47\n" \
  "v_add_f32 v32, v32, v40\n" \
  "v_mfma_f32_16x16x32_bf16 v[0:3], a[160:163], v[200:203], v[0:3]\n" \
  "v_add_f32 v33, v33, v41\n" \
  "v_add_f32 v34, v34, v42\n" \
  "v_mfma_f32_16x16x32_bf16 v[4:7], a[164:167], v[200:203], v[4:7]\n" \
  "v_add_f32 v35, v35, v43\n" \
  "v_cvt_pk_bf16_f32 v36, v32, v33\n" \
  "v_mfma_f32_16x16x32_bf16 v[8:11], a[168:171], v[200:203], v[8:11]\n" \
  "v_cvt_pk_bf16_f32 v37, v34, v35\n" \
  "s_waitcnt lgkmcnt(1)\n" \
  "v_mfma_f32_16x16x32_bf16 v[12:15], a[172:175], v[200:203], v[12:15]\n" \
  "v_add_f32 v48, v48, v240\n" \
  "v_add_f32 v49, v49, v241\n" \
  "v_mfma_f32_16x16x32_bf16 v[16:19], a[176:179], v[200:203], v[16:19]\n" \
  "v_add_f32 v50, v50, v242\n" \
  "v_add_f32 v51, v51, v243\n" \
  "v_mfma_f32_16x16x32_bf16 v[20:23], a[180:183], v[200:203], v[20:23]\n" \
  "v_add_f32 v244, v244, v248\n" \
  "v_add_f32 v245, v245, v249\n" \
  "v_mfma_f32_16x16x32_bf16 v[24:27], a[184:187], v[200:203], v[24:27]\n" \
  "v_add_f32 v246, v246, v250\n" \
  "v_add_f32 v247, v247, v251\n" \
  "v_mfma_f32_16x16x32_bf16 v[28:31], a[188:191], v[200:203], v[28:31]\n" \
  "v_add_f32 v48, v48, v244\n" \
  "v_mfma_f32_16x16x32_bf16 v[0:3], a[192:195], v[204:207], v[0:3]\n" \
  "v_add_f32 v49, v49, v245\n" \
  "v_add_f32 v50, v50, v246\n" \
  "v_mfma_f32_16x16x32_bf16 v[4:7], a[196:199], v[204:207], v[4:7]\n" \
  "v_add_f32 v51, v51, v247\n" \
  "v_cvt_pk_bf16_f32 v38, v48, v49\n" \
  "v_mfma_f32_16x16x32_bf16 v[8:11], a[200:203], v[204:207], v[8:11]\n" \
  "v_cvt_pk_bf16_f32 v39, v50, v51\n" \
  "buffer_store_dwordx4 v[36:39], v59, s[36:39], s74 offen offset:0 sc1\n" \
  "v_mfma_f32_16x16x32_bf16 v[12:15], a[204:207], v[204:207], v[12:15]\n" \
  "v_mfma_f32_16x16x32_bf16 v[16:19], a[208:211], v[204:207], v[16:19]\n" \
  "v_mfma_f32_16x16x32_bf16 v[20:23], a[212:215], v[204:207], v[20:23]\n" \
  "v_mfma_f32_16x16x32_bf16 v[24:27], a[216:219], v[204:207], v[24:27]\n" \
  "v_mfma_f32_16x16x32_bf16 v[28:31], a[220:223], v[204:207], v[28:31]\n" \
  "s_waitcnt vmcnt(14)\n" \
  "v_mfma_f32_16x16x32_bf16 v[208:211], v[128:131], v[72:75], 0\n" \
  "v_add_u32 v59, 409600, v59\n" \
  "v_mfma_f32_16x16x32_bf16 v[212:215], v[132:135], v[72:75], 0\n" \
  "s_waitcnt vmcnt(11)\n" \
  "v_cmp_eq_u32 vcc, 0, v63\n" \
  "v_mfma_f32_16x16x32_bf16 v[216:219], v[136:139], v[72:75], 0\n" \
  "ds_write_addtid_b32 v0 offset:32768\n" \
  "v_mfma_f32_16x16x32_bf16 v[220:223], v[140:143], v[72:75], 0\n" \
  "s_and_b64 vcc, vcc, s[86:87]\n" \
  "s_cbranch_scc1 .Lsg4_%=\n" \
  ".Lbg4_%=:\n" \
  "v_mfma_f32_16x16x32_bf16 v[224:227], v[144:147], v[72:75], 0\n" \
  "buffer_load_dwordx4 v[108:111], v59, s[36:39], s68 offen offset:0 sc1\n" \
  "ds_write_addtid_b32 v1 offset:33024\n" \
  "v_mfma_f32_16x16x32_bf16 v[228:231], v[148:151], v[72:75], 0\n" \
  "ds_write_addtid_b32 v2 offset:33280\n" \
  "s_waitcnt vmcnt(4)\n" \
  "v_mfma_f32_16x16x32_bf16 v[232:235], v[152:155], v[72:75], 0\n" \
  "ds_write_addtid_b32 v3 offset:33536\n" \
  "v_max3_u32 v52, v64, v65, v66\n" \
  "v_mfma_f32_16x16x32_bf16 v[236:239], v[156:159], v[72:75], 0\n" \
  "ds_write_addtid_b32 v4 offset:33792\n" \
  "v_max_u32 v52, v52, v67\n" \
  "v_mfma_f32_16x16x32_bf16 v[208:211], v[160:163], v[76:79], v[208:211]\n" \
  "buffer_load_dwordx4 v[112:115], v59, s[36:39], s68 offen offset:1024 sc1\n" \
  "ds_write_addtid_b32 v5 offset:34048\n" \
  "v_mfma_f32_16x16x32_bf16 v[212:215], v[164:167], v[76:79], v[212:215]\n" \
  "ds_write_addtid_b32 v6 offset:34304\n" \
  "v_cmp_eq_u32 vcc, -1, v52\n" \
  "v_mfma_f32_16x16x32_bf16 v[216:219], v[168:171], v[76:79], v[216:219]\n" \
  "ds_write_addtid_b32 v7 offset:34560\n" \
  "v_mfma_f32_16x16x32_bf16 v[220:223], v[172:175], v[76:79], v[220:223]\n" \
  "ds_write_addtid_b32 v8 offset:34816\n" \
  "s_cbranch_vccnz .Lse_%=\n" \
  ".Lbe_%=:\n" \
  "v_mfma_f32_16x16x32_bf16 v[224:227], v[176:179], v[76:79], v[224:227]\n" \
  "ds_write_addtid_b32 v9 offset:35072\n" \
  "v_lshlrev_b32 v52, 16, v64\n" \
  "v_mfma_f32_16x16x32_bf16 v[228:231], v[180:183], v[76:79], v[228:231]\n" \
  "ds_write_addtid_b32 v10 offset:35328\n" \
  "v_and_b32 v53, 0xffff0000, v64\n" \
  "v_mfma_f32_16x16x32_bf16 v[232:235], v[184:187], v[76:79], v[232:235]\n" \
  "buffer_load_dwordx4 v[116:119], v59, s[36:39], s68 offen offset:2048 sc1\n" \
  "ds_write_addtid_b32 v11 offset:35584\n" \
  "v_mfma_f32_16x16x32_bf16 v[236:239], v[188:191], v[76:79], v[236:239]\n" \
  "ds_write_addtid_b32 v12 offset:35840\n" \
  "v_mfma_f32_16x16x32_bf16 v[208:211], a[0:3], v[80:83], v[208:211]\n" \
  "ds_write_addtid_b32 v13 offset:36096\n" \
  "v_lshlrev_b32 v54, 16, v65\n" \
  "v_mfma_f32_16x16x32_bf16 v[212:215], a[4:7], v[80:83], v[212:215]\n" \
  "ds_write_addtid_b32 v14 offset:36352\n" \
  "v_and_b32 v55, 0xffff0000, v65\n" \
  "v_mfma_f32_16x16x32_bf16 v[216:219], a[8:11], v[80:83], v[216:219]\n" \
  "ds_write_addtid_b32 v15 offset:36608\n" \
  "v_lshlrev_b32 v252, 16, v66\n" \
  "v_mfma_f32_16x16x32_bf16 v[220:223], a[12:15], v[80:83], v[220:223]\n" \
  "ds_write_addtid_b32 v16 offset:36864\n" \
  "v_and_b32 v253, 0xffff0000, v66\n" \
  "v_mfma_f32_16x16x32_bf16 v[224:227], a[16:19], v[80:83], v[224:227]\n" \
  "buffer_load_dwordx4 v[120:123], v59, s[36:39], s69 offen offset:0 sc1\n" \
  "ds_write_addtid_b32 v17 offset:37120\n" \
  "v_mfma_f32_16x16x32_bf16 v[228:231], a[20:23], v[80:83], v[228:231]\n" \
  "ds_write_addtid_b32 v18 offset:37376\n" \
  "v_lshlrev_b32 v254, 16, v67\n" \
  "v_mfma_f32_16x16x32_bf16 v[232:235], a[24:27], v[80:83], v[232:235]\n" \
  "ds_write_addtid_b32 v19 offset:37632\n" \
  "v_and_b32 v255, 0xffff0000, v67\n" \
  "v_mfma_f32_16x16x32_bf16 v[236:239], a[28:31], v[80:83], v[236:239]\n" \
  "ds_write_addtid_b32 v20 offset:37888\n" \
  "v_add_f32 v52, v52, v54\n" \
  "v_mfma_f32_16x16x32_bf16 v[208:211], a[32:35], v[84:87], v[208:211]\n" \
  "ds_write_addtid_b32 v21 offset:38144\n" \
  "v_add_f32 v53, v53, v55\n" \
  "v_mfma_f32_16x16x32_bf16 v[212:215], a[36:39], v[84:87], v[212:215]\n" \
  "ds_write_addtid_b32 v22 offset:38400\n" \
  "v_add_f32 v252, v252, v254\n" \
  "v_mfma_f32_16x16x32_bf16 v[216:219], a[40:43], v[84:87], v[216:219]\n" \
  "buffer_load_dwordx4 v[124:127], v59, s[36:39], s69 offen offset:1024 sc1\n" \
  "ds_write_addtid_b32 v23 offset:38656\n" \
  "v_mfma_f32_16x16x32_bf16 v[220:223], a[44:47], v[84:87], v[220:223]\n" \
  "ds_write_addtid_b32 v24 offset:38912\n" \
  "v_add_f32 v253, v253, v255\n" \
  "v_mfma_f32_16x16x32_bf16 v[224:227], a[48:51], v[84:87], v[224:227]\n" \
  "ds_write_addtid_b32 v25 offset:39168\n" \
  "s_waitcnt lgkmcnt(15)\n" \
  "v_mfma_f32_16x16x32_bf16 v[228:231], a[52:55], v[84:87], v[228:231]\n" \
  "ds_write_addtid_b32 v26 offset:39424\n" \
  "v_add_f32 v52, v52, v70\n" \
  "v_mfma_f32_16x16x32_bf16 v[232:235], a[56:59], v[84:87], v[232:235]\n" \
  "ds_write_addtid_b32 v27 offset:39680\n" \
  "v_add_f32 v53, v53, v70\n" \
  "v_mfma_f32_16x16x32_bf16 v[236:239], a[60:63], v[84:87], v[236:239]\n" \
  "ds_write_addtid_b32 v28 offset:39936\n" \
  "s_waitcnt vmcnt(14)\n" \
  "v_mfma_f32_16x16x32_bf16 v[208:211], a[64:67], v[88:91], v[208:211]\n" \
  "buffer_load_dwordx4 v[192:195], v59, s[36:39], s69 offen offset:2048 sc1\n" \
  "ds_write_addtid_b32 v29 offset:40192\n" \
  "v_mfma_f32_16x16x32_bf16 v[212:215], a[68:71], v[88:91], v[212:215]\n" \
  "ds_write_addtid_b32 v30 offset:40448\n" \
  "v_lshlrev_b32 v54, 16, v71\n" \
  "v_mfma_f32_16x16x32_bf16 v[216:219], a[72:75], v[88:91], v[216:219]\n" \
  "ds_write_addtid_b32 v31 offset:40704\n" \
  "v_and_b32 v55, 0xffff0000, v71\n" \
  "v_mfma_f32_16x16x32_bf16 v[220:223], a[76:79], v[88:91], v[220:223]\n" \
  "v_lshlrev_b32 v254, 16, v68\n" \
  "v_and_b32 v255, 0xffff0000, v68\n" \
  "v_mfma_f32_16x16x32_bf16 v[224:227], a[80:83], v[88:91], v[224:227]\n" \
  "v_add_f32 v52, v52, v252\n" \
  "v_add_f32 v53, v53, v253\n" \
  "v_mfma_f32_16x16x32_bf16 v[228:231], a[84:87], v[88:91], v[228:231]\n" \
  "v_fma_f32 v52, v54, v52, v254\n" \
  "v_fma_f32 v53, v55, v53, v255\n" \
  "v_mfma_f32_16x16x32_bf16 v[232:235], a[88:91], v[88:91], v[232:235]\n" \
  "buffer_load_dwordx4 v[196:199], v59, s[36:39], s70 offen offset:0 sc1\n" \
  "v_cvt_pk_bf16_f32 v52, v52, v53\n" \
  "v_mfma_f32_16x16x32_bf16 v[236:239], a[92:95], v[88:91], v[236:239]\n" \
  "buffer_store_dword v52, v57, s[44:47], 0 offen offset:0 sc1\n" \
  "v_mfma_f32_16x16x32_bf16 v[208:211], a[96:99], v[92:95], v[208:211]\n" \
  "s_waitcnt lgkmcnt(0)\n" \
  "s_barrier\n" \
  "v_mfma_f32_16x16x32_bf16 v[212:215], a[100:103], v[92:95], v[212:215]\n" \
  "ds_read_b128 v[32:35], v58 offset:32768\n" \
  "v_mfma_f32_16x16x32_bf16 v[216:219], a[104:107], v[92:95], v[216:219]\n" \
  "ds_read_b128 v[36:39], v58 offset:40960\n" \
  "v_mfma_f32_16x16x32_bf16 v[220:223], a[108:111], v[92:95], v[220:223]\n" \
  "ds_read_b128 v[40:43], v58 offset:49152\n" \
  "v_mfma_f32_16x16x32_bf16 v[224:227], a[112:115], v[92:95], v[224:227]\n" \
  "buffer_load_dwordx4 v[200:203], v59, s[36:39], s70 offen offset:1024 sc1\n" \
  "ds_read_b128 v[44:47], v58 offset:57344\n" \
  "v_mfma_f32_16x16x32_bf16 v[228:231], a[116:119], v[92:95], v[228:231]\n" \
  "ds_read_b128 v[48:51], v58 offset:36864\n" \
  "v_mfma_f32_16x16x32_bf16 v[232:235], a[120:123], v[92:95], v[232:235]\n" \
  "ds_read_b128 v[240:243], v58 offset:45056\n" \
  "v_mfma_f32_16x16x32_bf16 v[236:239], a[124:127], v[92:95], v[236:239]\n" \
  "ds_read_b128 v[244:247], v58 offset:53248\n" \
  "s_waitcnt vmcnt(11)\n" \
  "v_mfma_f32_16x16x32_bf16 v[208:211], a[128:131], v[96:99], v[208:211]\n" \
  "ds_read_b128 v[248:251], v58 offset:61440\n" \
  "v_mfma_f32_16x16x32_bf16 v[212:215], a[132:135], v[96:99], v[212:215]\n" \
  "v_mfma_f32_16x16x32_bf16 v[216:219], a[136:139], v[96:99], v[216:219]\n" \
  "buffer_load_dwordx4 v[204:207], v59, s[36:39], s70 offen offset:2048 sc1\n" \
  "v_mfma_f32_16x16x32_bf16 v[220:223], a[140:143], v[96:99], v[220:223]\n" \
  "v_mfma_f32_16x16x32_bf16 v[224:227], a[144:147], v[96:99], v[224:227]\n" \
  "s_waitcnt lgkmcnt(4)\n" \
  "v_add_f32 v32, v32, v36\n" \
  "v_mfma_f32_16x16x32_bf16 v[228:231], a[148:151], v[96:99], v[228:231]\n" \
  "v_add_f32 v33, v33, v37\n" \
  "v_add_f32 v34, v34, v38\n" \
  "v_mfma_f32_16x16x32_bf16 v[232:235], a[152:155], v[96:99], v[232:235]\n" \
  "v_add_f32 v35, v35, v39\n" \
  "v_add_f32 v40, v40, v44\n" \
  "v_mfma_f32_16x16x32_bf16 v[236:239], a[156:159], v[96:99], v[236:239]\n" \
  "v_add_f32 v41, v41, v45\n" \
  "v_add_f32 v42, v42, v46\n" \
  "v_mfma_f32_16x16x32_bf16 v[208:211], a[160:163], v[100:103], v[208:211]\n" \
  "v_add_f32 v43, v43, v47\n" \
  "v_add_f32 v32, v32, v40\n" \
  "v_mfma_f32_16x16x32_bf16 v[212:215], a[164:167], v[100:103], v[212:215]\n" \
  "v_add_f32 v33, v33, v41\n" \
  "v_add_f32 v34, v34, v42\n" \
  "v_mfma_f32_16x16x32_bf16 v[216:219], a[168:171], v[100:103], v[216:219]\n" \
  "v_add_f32 v35, v35, v43\n" \
  "v_cvt_pk_bf16_f32 v36, v32, v33\n" \
  "v_mfma_f32_16x16x32_bf16 v[220:223], a[172:175], v[100:103], v[220:223]\n" \
  "v_cvt_pk_bf16_f32 v37, v34, v35\n" \
  "s_waitcnt lgkmcnt(0)\n" \
  "v_mfma_f32_16x16x32_bf16 v[224:227], a[176:179], v[100:103], v[224:227]\n" \
  "v_add_f32 v48, v48, v240\n" \
  "v_add_f32 v49, v49, v241\n" \
  "v_mfma_f32_16x16x32_bf16 v[228:231], a[180:183], v[100:103], v[228:231]\n" \
  "v_add_f32 v50, v50, v242\n" \
  "v_add_f32 v51, v51, v243\n" \
  "v_mfma_f32_16x16x32_bf16 v[232:235], a[184:187], v[100:103], v[232:235]\n" \
  "v_add_f32 v244, v244, v248\n" \
  "v_add_f32 v245, v245, v249\n" \
  "v_mfma_f32_16x16x32_bf16 v[236:239], a[188:191], v[100:103], v[236:239]\n" \
  "v_add_f32 v246, v246, v250\n" \
  "s_waitcnt vmcnt(11)\n" \
  "v_mfma_f32_16x16x32_bf16 v[208:211], a[192:195], v[104:107], v[208:211]\n" \
  "v_add_f32 v247, v247, v251\n" \
  "v_add_f32 v48, v48, v244\n" \
  "v_mfma_f32_16x16x32_bf16 v[212:215], a[196:199], v[104:107], v[212:215]\n" \
  "v_add_f32 v49, v49, v245\n" \
  "v_add_f32 v50, v50, v246\n" \
  "v_mfma_f32_16x16x32_bf16 v[216:219], a[200:203], v[104:107], v[216:219]\n" \
  "v_add_f32 v51, v51, v247\n" \
  "v_cvt_pk_bf16_f32 v38, v48, v49\n" \
  "v_mfma_f32_16x16x32_bf16 v[220:223], a[204:207], v[104:107], v[220:223]\n" \
  "v_cvt_pk_bf16_f32 v39, v50, v51\n" \
  "buffer_store_dwordx4 v[36:39], v59, s[36:39], s74 offen offset:0 sc1\n" \
  "v_mfma_f32_16x16x32_bf16 v[224:227], a[208:211], v[104:107], v[224:227]\n" \
  "v_mfma_f32_16x16x32_bf16 v[228:231], a[212:215], v[104:107], v[228:231]\n" \
  "v_mfma_f32_16x16x32_bf16 v[232:235], a[216:219], v[104:107], v[232:235]\n" \
  "v_mfma_f32_16x16x32_bf16 v[236:239], a[220:223], v[104:107], v[236:239]\n" \
  "s_waitcnt vmcnt(9)\n" \
  "v_mfma_f32_16x16x32_bf16 v[0:3], v[128:131], v[108:111], 0\n" \
  "v_add_u32 v59, 409600, v59\n" \
  "v_add_u32 v60, s98, v58\n" \
  "v_mfma_f32_16x16x32_bf16 v[4:7], v[132:135], v[108:111], 0\n" \
  "ds_write_addtid_b32 v208 offset:0\n" \
  "v_mfma_f32_16x16x32_bf16 v[8:11], v[136:139], v[108:111], 0\n" \
  "buffer_load_dwordx4 v[72:75], v59, s[36:39], s68 offen offset:0 sc1\n" \
  "ds_write_addtid_b32 v209 offset:256\n" \
  "v_mfma_f32_16x16x32_bf16 v[12:15], v[140:143], v[108:111], 0\n" \
  "ds_write_addtid_b32 v210 offset:512\n" \
  "v_mfma_f32_16x16x32_bf16 v[16:19], v[144:147], v[108:111], 0\n" \
  "ds_write_addtid_b32 v211 offset:768\n" \
  "v_mfma_f32_16x16x32_bf16 v[20:23], v[148:151], v[108:111], 0\n" \
  "buffer_load_dwordx4 v[64:67], v59, s[36:39], s71 offen offset:0 sc1\n" \
  "ds_write_addtid_b32 v212 offset:1024\n" \
  "v_mfma_f32_16x16x32_bf16 v[24:27], v[152:155], v[108:111], 0\n" \
  "ds_write_addtid_b32 v213 offset:1280\n" \
  "v_mfma_f32_16x16x32_bf16 v[28:31], v[156:159], v[108:111], 0\n" \
  "ds_write_addtid_b32 v214 offset:1536\n" \
  "v_mfma_f32_16x16x32_bf16 v[0:3], v[160:163], v[112:115], v[0:3]\n" \
  "buffer_load_dwordx4 v[76:79], v59, s[36:39], s68 offen offset:1024 sc1\n" \
  "ds_write_addtid_b32 v215 offset:1792\n" \
  "v_mfma_f32_16x16x32_bf16 v[4:7], v[164:167], v[112:115], v[4:7]\n" \
  "ds_write_addtid_b32 v216 offset:2048\n" \
  "v_mfma_f32_16x16x32_bf16 v[8:11], v[168:171], v[112:115], v[8:11]\n" \
  "ds_write_addtid_b32 v217 offset:2304\n" \
  "v_mfma_f32_16x16x32_bf16 v[12:15], v[172:175], v[112:115], v[12:15]\n" \
  "ds_write_addtid_b32 v218 offset:2560\n" \
  "v_mfma_f32_16x16x32_bf16 v[16:19], v[176:179], v[112:115], v[16:19]\n" \
  "ds_write_addtid_b32 v219 offset:2816\n" \
  "v_mfma_f32_16x16x32_bf16 v[20:23], v[180:183], v[112:115], v[20:23]\n" \
  "ds_write_addtid_b32 v220 offset:3072\n" \
  "v_mfma_f32_16x16x32_bf16 v[24:27], v[184:187], v[112:115], v[24:27]\n" \
  "buffer_load_dwordx4 v[80:83], v59, s[36:39], s68 offen offset:2048 sc1\n" \
  "ds_write_addtid_b32 v221 offset:3328\n" \
  "v_mfma_f32_16x16x32_bf16 v[28:31], v[188:191], v[112:115], v[28:31]\n" \
  "ds_write_addtid_b32 v222 offset:3584\n" \
  "s_waitcnt vmcnt(11)\n" \
  "v_mfma_f32_16x16x32_bf16 v[0:3], a[0:3], v[116:119], v[0:3]\n" \
  "ds_write_addtid_b32 v223 offset:3840\n" \
  "v_mfma_f32_16x16x32_bf16 v[4:7], a[4:7], v[116:119], v[4:7]\n" \
  "buffer_load_dwordx4 v[52:55], v59, s[36:39], s76 offen offset:0 sc1\n" \
  "ds_write_addtid_b32 v224 offset:4096\n" \
  "v_mfma_f32_16x16x32_bf16 v[8:11], a[8:11], v[116:119], v[8:11]\n" \
  "ds_write_addtid_b32 v225 offset:4352\n" \
  "v_mfma_f32_16x16x32_bf16 v[12:15], a[12:15], v[116:119], v[12:15]\n" \
  "ds_write_addtid_b32 v226 offset:4608\n" \
  "v_mfma_f32_16x16x32_bf16 v[16:19], a[16:19], v[116:119], v[16:19]\n" \
  "buffer_load_dwordx4 v[84:87], v59, s[36:39], s69 offen offset:0 sc1\n" \
  "ds_write_addtid_b32 v227 offset:4864\n" \
  "v_mfma_f32_16x16x32_bf16 v[20:23], a[20:23], v[116:119], v[20:23]\n" \
  "ds_write_addtid_b32 v228 offset:5120\n" \
  "v_mfma_f32_16x16x32_bf16 v[24:27], a[24:27], v[116:119], v[24:27]\n" \
  "ds_write_addtid_b32 v229 offset:5376\n" \
  "v_mfma_f32_16x16x32_bf16 v[28:31], a[28:31], v[116:119], v[28:31]\n" \
  "ds_write_addtid_b32 v230 offset:5632\n" \
  "v_mfma_f32_16x16x32_bf16 v[0:3], a[32:35], v[120:123], v[0:3]\n" \
  "ds_write_addtid_b32 v231 offset:5888\n" \
  "v_mfma_f32_16x16x32_bf16 v[4:7], a[36:39], v[120:123], v[4:7]\n" \
  "ds_write_addtid_b32 v232 offset:6144\n" \
  "v_mfma_f32_16x16x32_bf16 v[8:11], a[40:43], v[120:123], v[8:11]\n" \
  "buffer_load_dwordx4 v[88:91], v59, s[36:39], s69 offen offset:1024 sc1\n" \
  "ds_write_addtid_b32 v233 offset:6400\n" \
  "v_mfma_f32_16x16x32_bf16 v[12:15], a[44:47], v[120:123], v[12:15]\n" \
  "ds_write_addtid_b32 v234 offset:6656\n" \
  "v_mfma_f32_16x16x32_bf16 v[16:19], a[48:51], v[120:123], v[16:19]\n" \
  "ds_write_addtid_b32 v235 offset:6912\n" \
  "v_mfma_f32_16x16x32_bf16 v[20:23], a[52:55], v[120:123], v[20:23]\n" \
  "ds_write_addtid_b32 v236 offset:7168\n" \
  "v_mfma_f32_16x16x32_bf16 v[24:27], a[56:59], v[120:123], v[24:27]\n" \
  "ds_write_addtid_b32 v237 offset:7424\n" \
  "v_mfma_f32_16x16x32_bf16 v[28:31], a[60:63], v[120:123], v[28:31]\n" \
  "ds_write_addtid_b32 v238 offset:7680\n" \
  "s_waitcnt vmcnt(12)\n" \
  "v_mfma_f32_16x16x32_bf16 v[0:3], a[64:67], v[124:127], v[0:3]\n" \
  "buffer_load_dwordx4 v[92:95], v59, s[36:39], s69 offen offset:2048 sc1\n" \
  "ds_write_addtid_b32 v239 offset:7936\n" \
  "v_mfma_f32_16x16x32_bf16 v[4:7], a[68:71], v[124:127], v[4:7]\n" \
  "ds_read_b32 v61, v60 offset:28676\n" \
  "v_mfma_f32_16x16x32_bf16 v[8:11], a[72:75], v[124:127], v[8:11]\n" \
  "v_mfma_f32_16x16x32_bf16 v[12:15], a[76:79], v[124:127], v[12:15]\n" \
  "v_mfma_f32_16x16x32_bf16 v[16:19], a[80:83], v[124:127], v[16:19]\n" \
  "v_mfma_f32_16x16x32_bf16 v[20:23], a[84:87], v[124:127], v[20:23]\n" \
  "v_mfma_f32_16x16x32_bf16 v[24:27], a[88:91], v[124:127], v[24:27]\n" \
  "buffer_load_dwordx4 v[96:99], v59, s[36:39], s70 offen offset:0 sc1\n" \
  "v_mfma_f32_16x16x32_bf16 v[28:31], a[92:95], v[124:127], v[28:31]\n" \
  "s_waitcnt lgkmcnt(0)\n" \
  "s_barrier\n" \
  "v_mfma_f32_16x16x32_bf16 v[0:3], a[96:99], v[192:195], v[0:3]\n" \
  "ds_read_b128 v[32:35], v58 offset:0\n" \
  "v_mfma_f32_16x16x32_bf16 v[4:7], a[100:103], v[192:195], v[4:7]\n" \
  "ds_read_b128 v[36:39], v58 offset:8192\n" \
  "v_mfma_f32_16x16x32_bf16 v[8:11], a[104:107], v[192:195], v[8:11]\n" \
  "ds_read_b128 v[40:43], v58 offset:16384\n" \
  "v_lshlrev_b32 v60, 16, v61\n" \
  "v_mfma_f32_16x16x32_bf16 v[12:15], a[108:111], v[192:195], v[12:15]\n" \
  "ds_read_b128 v[44:47], v58 offset:24576\n" \
  "v_and_b32 v61, 0xffff0000, v61\n" \
  "v_mfma_f32_16x16x32_bf16 v[16:19], a[112:115], v[192:195], v[16:19]\n" \
  "buffer_load_dwordx4 v[100:103], v59, s[36:39], s70 offen offset:1024 sc1\n" \
  "ds_read_b128 v[48:51], v58 offset:4096\n" \
  "v_mfma_f32_16x16x32_bf16 v[20:23], a[116:119], v[192:195], v[20:23]\n" \
  "ds_read_b128 v[240:243], v58 offset:12288\n" \
  "v_mfma_f32_16x16x32_bf16 v[24:27], a[120:123], v[192:195], v[24:27]\n" \
  "ds_read_b128 v[244:247], v58 offset:20480\n" \
  "v_mfma_f32_16x16x32_bf16 v[28:31], a[124:127], v[192:195], v[28:31]\n" \
  "ds_read_b128 v[248:251], v58 offset:28672\n" \
  "s_waitcnt vmcnt(12)\n" \
  "v_mfma_f32_16x16x32_bf16 v[0:3], a[128:131], v[196:199], v[0:3]\n" \
  "v_mfma_f32_16x16x32_bf16 v[4:7], a[132:135], v[196:199], v[4:7]\n" \
  "v_mfma_f32_16x16x32_bf16 v[8:11], a[136:139], v[196:199], v[8:11]\n" \
  "buffer_load_dwordx4 v[104:107], v59, s[36:39], s70 offen offset:2048 sc1\n" \
  "v_mfma_f32_16x16x32_bf16 v[12:15], a[140:143], v[196:199], v[12:15]\n" \
  "s_waitcnt lgkmcnt(4)\n" \
  "v_add_f32 v32, v32, v36\n" \
  "v_mfma_f32_16x16x32_bf16 v[16:19], a[144:147], v[196:199], v[16:19]\n" \
  "v_add_f32 v33, v33, v37\n" \
  "v_add_f32 v34, v34, v38\n" \
  "v_mfma_f32_16x16x32_bf16 v[20:23], a[148:151], v[196:199], v[20:23]\n" \
  "v_add_f32 v35, v35, v39\n" \
  "v_add_f32 v40, v40, v44\n" \
  "v_mfma_f32_16x16x32_bf16 v[24:27], a[152:155], v[196:199], v[24:27]\n" \
  "v_add_f32 v41, v41, v45\n" \
  "v_add_f32 v42, v42, v46\n" \
  "v_mfma_f32_16x16x32_bf16 v[28:31], a[156:159], v[196:199], v[28:31]\n" \
  "v_add_f32 v43, v43, v47\n" \
  "v_add_f32 v32, v32, v40\n" \
  "v_mfma_f32_16x16x32_bf16 v[0:3], a[160:163], v[200:203], v[0:3]\n" \
  "v_add_f32 v33, v33, v41\n" \
  "v_add_f32 v34, v34, v42\n" \
  "v_mfma_f32_16x16x32_bf16 v[4:7], a[164:167], v[200:203], v[4:7]\n" \
  "v_add_f32 v35, v35, v43\n" \
  "v_cvt_pk_bf16_f32 v36, v32, v33\n" \
  "v_mfma_f32_16x16x32_bf16 v[8:11], a[168:171], v[200:203], v[8:11]\n" \
  "v_cvt_pk_bf16_f32 v37, v34, v35\n" \
  "s_waitcnt lgkmcnt(0)\n" \
  "v_mfma_f32_16x16x32_bf16 v[12:15], a[172:175], v[200:203], v[12:15]\n" \
  "v_add_f32 v48, v48, v240\n" \
  "v_add_f32 v49, v49, v241\n" \
  "v_mfma_f32_16x16x32_bf16 v[16:19], a[176:179], v[200:203], v[16:19]\n" \
  "v_add_f32 v50, v50, v242\n" \
  "v_add_f32 v51, v51, v243\n" \
  "v_mfma_f32_16x16x32_bf16 v[20:23], a[180:183], v[200:203], v[20:23]\n" \
  "v_add_f32 v244, v244, v248\n" \
  "v_add_f32 v245, v245, v249\n" \
  "v_mfma_f32_16x16x32_bf16 v[24:27], a[184:187], v[200:203], v[24:27]\n" \
  "v_add_f32 v246, v246, v250\n" \
  "v_add_f32 v247, v247, v251\n" \
  "v_mfma_f32_16x16x32_bf16 v[28:31], a[188:191], v[200:203], v[28:31]\n" \
  "v_add_f32 v48, v48, v244\n" \
  "s_waitcnt vmcnt(12)\n" \
  "v_mfma_f32_16x16x32_bf16 v[0:3], a[192:195], v[204:207], v[0:3]\n" \
  "v_add_f32 v49, v49, v245\n" \
  "v_add_f32 v50, v50, v246\n" \
  "v_mfma_f32_16x16x32_bf16 v[4:7], a[196:199], v[204:207], v[4:7]\n" \
  "v_add_f32 v51, v51, v247\n" \
  "v_cvt_pk_bf16_f32 v38, v48, v49\n" \
  "v_mfma_f32_16x16x32_bf16 v[8:11], a[200:203], v[204:207], v[8:11]\n" \
  "v_cvt_pk_bf16_f32 v39, v50, v51\n" \
  "buffer_store_dwordx4 v[36:39], v59, s[36:39], s74 offen offset:0 sc1\n" \
  "v_mfma_f32_16x16x32_bf16 v[12:15], a[204:207], v[204:207], v[12:15]\n" \
  "v_mfma_f32_16x16x32_bf16 v[16:19], a[208:211], v[204:207], v[16:19]\n" \
  "v_mfma_f32_16x16x32_bf16 v[20:23], a[212:215], v[204:207], v[20:23]\n" \
  "v_mfma_f32_16x16x32_bf16 v[24:27], a[216:219], v[204:207], v[24:27]\n" \
  "v_mfma_f32_16x16x32_bf16 v[28:31], a[220:223], v[204:207], v[28:31]\n" \
  "s_waitcnt vmcnt(9)\n" \
  "v_mfma_f32_16x16x32_bf16 v[208:211], v[128:131], v[72:75], 0\n" \
  "v_add_u32 v59, 409600, v59\n" \
  "s_waitcnt vmcnt(7)\n" \
  "v_mfma_f32_16x16x32_bf16 v[212:215], v[132:135], v[72:75], 0\n" \
  "ds_write_addtid_b32 v0 offset:32768\n" \
  "v_max_u32 v62, v64, v52\n" \
  "v_mfma_f32_16x16x32_bf16 v[216:219], v[136:139], v[72:75], 0\n" \
  "buffer_load_dwordx4 v[108:111], v59, s[36:39], s68 offen offset:0 sc1\n" \
  "ds_write_addtid_b32 v1 offset:33024\n" \
  "v_mfma_f32_16x16x32_bf16 v[220:223], v[140:143], v[72:75], 0\n" \
  "ds_write_addtid_b32 v2 offset:33280\n" \
  "v_cmp_eq_u32 vcc, -1, v62\n" \
  "v_mfma_f32_16x16x32_bf16 v[224:227], v[144:147], v[72:75], 0\n" \
  "ds_write_addtid_b32 v3 offset:33536\n" \
  "v_mfma_f32_16x16x32_bf16 v[228:231], v[148:151], v[72:75], 0\n" \
  "ds_write_addtid_b32 v4 offset:33792\n" \
  "s_cbranch_vccnz .Lsp1_%=\n" \
  ".Lbp1_%=:\n" \
  "v_mfma_f32_16x16x32_bf16 v[232:235], v[152:155], v[72:75], 0\n" \
  "ds_write_addtid_b32 v5 offset:34048\n" \
  "v_and_b32 v71, 0xffff0000, v67\n" \
  "v_mfma_f32_16x16x32_bf16 v[236:239], v[156:159], v[72:75], 0\n" \
  "ds_write_addtid_b32 v6 offset:34304\n" \
  "v_lshlrev_b32 v70, 16, v67\n" \
  "v_mfma_f32_16x16x32_bf16 v[208:211], v[160:163], v[76:79], v[208:211]\n" \
  "buffer_load_dwordx4 v[112:115], v59, s[36:39], s68 offen offset:1024 sc1\n" \
  "ds_write_addtid_b32 v7 offset:34560\n" \
  "v_mfma_f32_16x16x32_bf16 v[212:215], v[164:167], v[76:79], v[212:215]\n" \
  "ds_write_addtid_b32 v8 offset:34816\n" \
  "v_and_b32 v69, 0xffff0000, v66\n" \
  "v_mfma_f32_16x16x32_bf16 v[216:219], v[168:171], v[76:79], v[216:219]\n" \
  "ds_write_addtid_b32 v9 offset:35072\n" \
  "v_lshlrev_b32 v68, 16, v66\n" \
  "v_mfma_f32_16x16x32_bf16 v[220:223], v[172:175], v[76:79], v[220:223]\n" \
  "ds_write_addtid_b32 v10 offset:35328\n" \
  "v_and_b32 v67, 0xffff0000, v65\n" \
  "v_mfma_f32_16x16x32_bf16 v[224:227], v[176:179], v[76:79], v[224:227]\n" \
  "ds_write_addtid_b32 v11 offset:35584\n" \
  "v_lshlrev_b32 v66, 16, v65\n" \
  "v_mfma_f32_16x16x32_bf16 v[228:231], v[180:183], v[76:79], v[228:231]\n" \
  "ds_write_addtid_b32 v12 offset:35840\n" \
  "v_and_b32 v65, 0xffff0000, v64\n" \
  "v_mfma_f32_16x16x32_bf16 v[232:235], v[184:187], v[76:79], v[232:235]\n" \
  "buffer_load_dwordx4 v[116:119], v59, s[36:39], s68 offen offset:2048 sc1\n" \
  "ds_write_addtid_b32 v13 offset:36096\n" \
  "v_mfma_f32_16x16x32_bf16 v[236:239], v[188:191], v[76:79], v[236:239]\n" \
  "ds_write_addtid_b32 v14 offset:36352\n" \
  "s_waitcnt vmcnt(9)\n" \
  "v_mfma_f32_16x16x32_bf16 v[208:211], a[0:3], v[80:83], v[208:211]\n" \
  "ds_write_addtid_b32 v15 offset:36608\n" \
  "v_lshlrev_b32 v64, 16, v64\n" \
  "v_mfma_f32_16x16x32_bf16 v[212:215], a[4:7], v[80:83], v[212:215]\n" \
  "ds_write_addtid_b32 v16 offset:36864\n" \
  "v_add_f32 v64, v64, v60\n" \
  "v_mfma_f32_16x16x32_bf16 v[216:219], a[8:11], v[80:83], v[216:219]\n" \
  "ds_write_addtid_b32 v17 offset:37120\n" \
  "v_add_f32 v65, v65, v60\n" \
  "v_mfma_f32_16x16x32_bf16 v[220:223], a[12:15], v[80:83], v[220:223]\n" \
  "ds_write_addtid_b32 v18 offset:37376\n" \
  "v_add_f32 v66, v66, v60\n" \
  "v_mfma_f32_16x16x32_bf16 v[224:227], a[16:19], v[80:83], v[224:227]\n" \
  "buffer_load_dwordx4 v[120:123], v59, s[36:39], s69 offen offset:0 sc1\n" \
  "ds_write_addtid_b32 v19 offset:37632\n" \
  "v_mfma_f32_16x16x32_bf16 v[228:231], a[20:23], v[80:83], v[228:231]\n" \
  "ds_write_addtid_b32 v20 offset:37888\n" \
  "v_add_f32 v67, v67, v60\n" \
  "v_mfma_f32_16x16x32_bf16 v[232:235], a[24:27], v[80:83], v[232:235]\n" \
  "ds_write_addtid_b32 v21 offset:38144\n" \
  "v_add_f32 v68, v68, v61\n" \
  "v_mfma_f32_16x16x32_bf16 v[236:239], a[28:31], v[80:83], v[236:239]\n" \
  "ds_write_addtid_b32 v22 offset:38400\n" \
  "v_add_f32 v69, v69, v61\n" \
  "v_mfma_f32_16x16x32_bf16 v[208:211], a[32:35], v[84:87], v[208:211]\n" \
  "ds_write_addtid_b32 v23 offset:38656\n" \
  "v_add_f32 v70, v70, v61\n" \
  "v_mfma_f32_16x16x32_bf16 v[212:215], a[36:39], v[84:87], v[212:215]\n" \
  "ds_write_addtid_b32 v24 offset:38912\n" \
  "v_add_f32 v71, v71, v61\n" \
  "v_mfma_f32_16x16x32_bf16 v[216:219], a[40:43], v[84:87], v[216:219]\n" \
  "buffer_load_dwordx4 v[124:127], v59, s[36:39], s69 offen offset:1024 sc1\n" \
  "ds_write_addtid_b32 v25 offset:39168\n" \
  "v_mfma_f32_16x16x32_bf16 v[220:223], a[44:47], v[84:87], v[220:223]\n" \
  "ds_write_addtid_b32 v26 offset:39424\n" \
  "v_and_b32 v255, 0xffff0000, v55\n" \
  "v_mfma_f32_16x16x32_bf16 v[224:227], a[48:51], v[84:87], v[224:227]\n" \
  "ds_write_addtid_b32 v27 offset:39680\n" \
  "v_lshlrev_b32 v254, 16, v55\n" \
  "v_mfma_f32_16x16x32_bf16 v[228:231], a[52:55], v[84:87], v[228:231]\n" \
  "ds_write_addtid_b32 v28 offset:39936\n" \
  "v_and_b32 v253, 0xffff0000, v54\n" \
  "v_mfma_f32_16x16x32_bf16 v[232:235], a[56:59], v[84:87], v[232:235]\n" \
  "ds_write_addtid_b32 v29 offset:40192\n" \
  "v_lshlrev_b32 v252, 16, v54\n" \
  "v_mfma_f32_16x16x32_bf16 v[236:239], a[60:63], v[84:87], v[236:239]\n" \
  "ds_write_addtid_b32 v30 offset:40448\n" \
  "s_waitcnt vmcnt(9)\n" \
  "v_mfma_f32_16x16x32_bf16 v[208:211], a[64:67], v[88:91], v[208:211]\n" \
  "buffer_load_dwordx4 v[192:195], v59, s[36:39], s69 offen offset:2048 sc1\n" \
  "ds_write_addtid_b32 v31 offset:40704\n" \
  "v_mfma_f32_16x16x32_bf16 v[212:215], a[68:71], v[88:91], v[212:215]\n" \
  "v_and_b32 v55, 0xffff0000, v53\n" \
  "v_lshlrev_b32 v54, 16, v53\n" \
  "v_mfma_f32_16x16x32_bf16 v[216:219], a[72:75], v[88:91], v[216:219]\n" \
  "v_and_b32 v53, 0xffff0000, v52\n" \
  "v_lshlrev_b32 v52, 16, v52\n" \
  "v_mfma_f32_16x16x32_bf16 v[220:223], a[76:79], v[88:91], v[220:223]\n" \
  "v_add_f32 v52, v52, v60\n" \
  "v_add_f32 v53, v53, v60\n" \
  "v_mfma_f32_16x16x32_bf16 v[224:227], a[80:83], v[88:91], v[224:227]\n" \
  "v_add_f32 v54, v54, v60\n" \
  "v_add_f32 v55, v55, v60\n" \
  "v_mfma_f32_16x16x32_bf16 v[228:231], a[84:87], v[88:91], v[228:231]\n" \
  "v_add_f32 v252, v252, v61\n" \
  "v_add_f32 v253, v253, v61\n" \
  "v_mfma_f32_16x16x32_bf16 v[232:235], a[88:91], v[88:91], v[232:235]\n" \
  "buffer_load_dwordx4 v[196:199], v59, s[36:39], s70 offen offset:0 sc1\n" \
  "v_add_f32 v254, v254, v61\n" \
  "v_mfma_f32_16x16x32_bf16 v[236:239], a[92:95], v[88:91], v[236:239]\n" \
  "s_waitcnt lgkmcnt(0)\n" \
  "s_barrier\n" \
  "v_mfma_f32_16x16x32_bf16 v[208:211], a[96:99], v[92:95], v[208:211]\n" \
  "ds_read_b128 v[32:35], v58 offset:32768\n" \
  "v_add_f32 v255, v255, v61\n" \
  "v_mfma_f32_16x16x32_bf16 v[212:215], a[100:103], v[92:95], v[212:215]\n" \
  "ds_read_b128 v[36:39], v58 offset:40960\n" \
  "v_mfma_f32_16x16x32_bf16 v[216:219], a[104:107], v[92:95], v[216:219]\n" \
  "ds_read_b128 v[40:43], v58 offset:49152\n" \
  "v_mfma_f32_16x16x32_bf16 v[220:223], a[108:111], v[92:95], v[220:223]\n" \
  "ds_read_b128 v[44:47], v58 offset:57344\n" \
  "v_mfma_f32_16x16x32_bf16 v[224:227], a[112:115], v[92:95], v[224:227]\n" \
  "buffer_load_dwordx4 v[200:203], v59, s[36:39], s70 offen offset:1024 sc1\n" \
  "ds_read_b128 v[48:51], v58 offset:36864\n" \
  "v_mfma_f32_16x16x32_bf16 v[228:231], a[116:119], v[92:95], v[228:231]\n" \
  "ds_read_b128 v[240:243], v58 offset:45056\n" \
  "v_mfma_f32_16x16x32_bf16 v[232:235], a[120:123], v[92:95], v[232:235]\n" \
  "ds_read_b128 v[244:247], v58 offset:53248\n" \
  "v_mfma_f32_16x16x32_bf16 v[236:239], a[124:127], v[92:95], v[236:239]\n" \
  "buffer_load_dwordx4 v[60:63], v59, s[36:39], s72 offen offset:0 sc1\n" \
  "s_waitcnt vmcnt(11)\n" \
  "v_mfma_f32_16x16x32_bf16 v[208:211], a[128:131], v[96:99], v[208:211]\n" \
  "ds_read_b128 v[248:251], v58 offset:61440\n" \
  "v_mfma_f32_16x16x32_bf16 v[212:215], a[132:135], v[96:99], v[212:215]\n" \
  "v_mfma_f32_16x16x32_bf16 v[216:219], a[136:139], v[96:99], v[216:219]\n" \
  "buffer_load_dwordx4 v[204:207], v59, s[36:39], s70 offen offset:2048 sc1\n" \
  "v_mfma_f32_16x16x32_bf16 v[220:223], a[140:143], v[96:99], v[220:223]\n" \
  "s_waitcnt lgkmcnt(4)\n" \
  "v_add_f32 v32, v32, v36\n" \
  "v_mfma_f32_16x16x32_bf16 v[224:227], a[144:147], v[96:99], v[224:227]\n" \
  "v_add_f32 v33, v33, v37\n" \
  "v_add_f32 v34, v34, v38\n" \
  "v_mfma_f32_16x16x32_bf16 v[228:231], a[148:151], v[96:99], v[228:231]\n" \
  "v_add_f32 v35, v35, v39\n" \
  "v_add_f32 v40, v40, v44\n" \
  "v_mfma_f32_16x16x32_bf16 v[232:235], a[152:155], v[96:99], v[232:235]\n" \
  "v_add_f32 v41, v41, v45\n" \
  "v_add_f32 v42, v42, v46\n" \
  "v_mfma_f32_16x16x32_bf16 v[236:239], a[156:159], v[96:99], v[236:239]\n" \
  "v_add_f32 v43, v43, v47\n" \
  "v_add_f32 v32, v32, v40\n" \
  "v_mfma_f32_16x16x32_bf16 v[208:211], a[160:163], v[100:103], v[208:211]\n" \
  "v_add_f32 v33, v33, v41\n" \
  "v_add_f32 v34, v34, v42\n" \
  "v_mfma_f32_16x16x32_bf16 v[212:215], a[164:167], v[100:103], v[212:215]\n" \
  "v_add_f32 v35, v35, v43\n" \
  "v_cvt_pk_bf16_f32 v36, v32, v33\n" \
  "v_mfma_f32_16x16x32_bf16 v[216:219], a[168:171], v[100:103], v[216:219]\n" \
  "v_cvt_pk_bf16_f32 v37, v34, v35\n" \
  "s_waitcnt lgkmcnt(0)\n" \
  "v_mfma_f32_16x16x32_bf16 v[220:223], a[172:175], v[100:103], v[220:223]\n" \
  "v_add_f32 v48, v48, v240\n" \
  "v_add_f32 v49, v49, v241\n" \
  "v_mfma_f32_16x16x32_bf16 v[224:227], a[176:179], v[100:103], v[224:227]\n" \
  "v_add_f32 v50, v50, v242\n" \
  "v_add_f32 v51, v51, v243\n" \
  "v_mfma_f32_16x16x32_bf16 v[228:231], a[180:183], v[100:103], v[228:231]\n" \
  "v_add_f32 v244, v244, v248\n" \
  "v_add_f32 v245, v245, v249\n" \
  "v_mfma_f32_16x16x32_bf16 v[232:235], a[184:187], v[100:103], v[232:235]\n" \
  "v_add_f32 v246, v246, v250\n" \
  "v_add_f32 v247, v247, v251\n" \
  "v_mfma_f32_16x16x32_bf16 v[236:239], a[188:191], v[100:103], v[236:239]\n" \
  "v_add_f32 v48, v48, v244\n" \
  "s_waitcnt vmcnt(11)\n" \
  "v_mfma_f32_16x16x32_bf16 v[208:211], a[192:195], v[104:107], v[208:211]\n" \
  "v_add_f32 v49, v49, v245\n" \
  "v_add_f32 v50, v50, v246\n" \
  "v_mfma_f32_16x16x32_bf16 v[212:215], a[196:199], v[104:107], v[212:215]\n" \
  "v_add_f32 v51, v51, v247\n" \
  "v_cvt_pk_bf16_f32 v38, v48, v49\n" \
  "v_mfma_f32_16x16x32_bf16 v[216:219], a[200:203], v[104:107], v[216:219]\n" \
  "v_cvt_pk_bf16_f32 v39, v50, v51\n" \
  "buffer_store_dwordx4 v[36:39], v59, s[36:39], s74 offen offset:0 sc1\n" \
  "v_mfma_f32_16x16x32_bf16 v[220:223], a[204:207], v[104:107], v[220:223]\n" \
  "v_mfma_f32_16x16x32_bf16 v[224:227], a[208:211], v[104:107], v[224:227]\n" \
  "v_mfma_f32_16x16x32_bf16 v[228:231], a[212:215], v[104:107], v[228:231]\n" \
  "v_mfma_f32_16x16x32_bf16 v[232:235], a[216:219], v[104:107], v[232:235]\n" \
  "v_mfma_f32_16x16x32_bf16 v[236:239], a[220:223], v[104:107], v[236:239]\n" \
  "s_waitcnt vmcnt(9)\n" \
  "v_mfma_f32_16x16x32_bf16 v[0:3], v[128:131], v[108:111], 0\n" \
  "v_add_u32 v59, 409600, v59\n" \
  "v_mfma_f32_16x16x32_bf16 v[4:7], v[132:135], v[108:111], 0\n" \
  "ds_write_addtid_b32 v208 offset:0\n" \
  "ds_write_addtid_b32 v209 offset:256\n" \
  "v_mfma_f32_16x16x32_bf16 v[8:11], v[136:139], v[108:111], 0\n" \
  "buffer_load_dwordx4 v[72:75], v59, s[36:39], s68 offen offset:0 sc1\n" \
  "ds_write_addtid_b32 v210 offset:512\n" \
  "v_mfma_f32_16x16x32_bf16 v[12:15], v[140:143], v[108:111], 0\n" \
  "ds_write_addtid_b32 v211 offset:768\n" \
  "ds_write_addtid_b32 v212 offset:1024\n" \
  "v_mfma_f32_16x16x32_bf16 v[16:19], v[144:147], v[108:111], 0\n" \
  "ds_write_addtid_b32 v213 offset:1280\n" \
  "ds_write_addtid_b32 v214 offset:1536\n" \
  "v_mfma_f32_16x16x32_bf16 v[20:23], v[148:151], v[108:111], 0\n" \
  "ds_write_addtid_b32 v215 offset:1792\n" \
  "ds_write_addtid_b32 v216 offset:2048\n" \
  "v_mfma_f32_16x16x32_bf16 v[24:27], v[152:155], v[108:111], 0\n" \
  "ds_write_addtid_b32 v217 offset:2304\n" \
  "ds_write_addtid_b32 v218 offset:2560\n" \
  "v_mfma_f32_16x16x32_bf16 v[28:31], v[156:159], v[108:111], 0\n" \
  "ds_write_addtid_b32 v219 offset:2816\n" \
  "ds_write_addtid_b32 v220 offset:3072\n" \
  "v_mfma_f32_16x16x32_bf16 v[0:3], v[160:163], v[112:115], v[0:3]\n" \
  "buffer_load_dwordx4 v[76:79], v59, s[36:39], s68 offen offset:1024 sc1\n" \
  "ds_write_addtid_b32 v221 offset:3328\n" \
  "v_mfma_f32_16x16x32_bf16 v[4:7], v[164:167], v[112:115], v[4:7]\n" \
  "ds_write_addtid_b32 v222 offset:3584\n" \
  "ds_write_addtid_b32 v223 offset:3840\n" \
  "v_mfma_f32_16x16x32_bf16 v[8:11], v[168:171], v[112:115], v[8:11]\n" \
  "ds_write_addtid_b32 v224 offset:4096\n" \
  "ds_write_addtid_b32 v225 offset:4352\n" \
  "v_mfma_f32_16x16x32_bf16 v[12:15], v[172:175], v[112:115], v[12:15]\n" \
  "buffer_load_dwordx4 v[108:111], v59, s[36:39], s77 offen offset:0 sc1\n" \
  "ds_write_addtid_b32 v226 offset:4608\n" \
  "v_mfma_f32_16x16x32_bf16 v[16:19], v[176:179], v[112:115], v[16:19]\n" \
  "ds_write_addtid_b32 v227 offset:4864\n" \
  "ds_write_addtid_b32 v228 offset:5120\n" \
  "v_mfma_f32_16x16x32_bf16 v[20:23], v[180:183], v[112:115], v[20:23]\n" \
  "ds_write_addtid_b32 v229 offset:5376\n" \
  "ds_write_addtid_b32 v230 offset:5632\n" \
  "v_mfma_f32_16x16x32_bf16 v[24:27], v[184:187], v[112:115], v[24:27]\n" \
  "buffer_load_dwordx4 v[80:83], v59, s[36:39], s68 offen offset:2048 sc1\n" \
  "ds_write_addtid_b32 v231 offset:5888\n" \
  "v_mfma_f32_16x16x32_bf16 v[28:31], v[188:191], v[112:115], v[28:31]\n" \
  "ds_write_addtid_b32 v232 offset:6144\n" \
  "s_waitcnt vmcnt(11)\n" \
  "v_mfma_f32_16x16x32_bf16 v[0:3], a[0:3], v[116:119], v[0:3]\n" \
  "ds_write_addtid_b32 v233 offset:6400\n" \
  "ds_write_addtid_b32 v234 offset:6656\n" \
  "v_mfma_f32_16x16x32_bf16 v[4:7], a[4:7], v[116:119], v[4:7]\n" \
  "ds_write_addtid_b32 v235 offset:6912\n" \
  "ds_write_addtid_b32 v236 offset:7168\n" \
  "v_mfma_f32_16x16x32_bf16 v[8:11], a[8:11], v[116:119], v[8:11]\n" \
  "ds_write_addtid_b32 v237 offset:7424\n" \
  "ds_write_addtid_b32 v238 offset:7680\n" \
  "v_mfma_f32_16x16x32_bf16 v[12:15], a[12:15], v[116:119], v[12:15]\n" \
  "ds_write_addtid_b32 v239 offset:7936\n" \
  "v_mfma_f32_16x16x32_bf16 v[16:19], a[16:19], v[116:119], v[16:19]\n" \
  "buffer_load_dwordx4 v[84:87], v59, s[36:39], s69 offen offset:0 sc1\n" \
  "v_mfma_f32_16x16x32_bf16 v[20:23], a[20:23], v[116:119], v[20:23]\n" \
  "v_mfma_f32_16x16x32_bf16 v[24:27], a[24:27], v[116:119], v[24:27]\n" \
  "v_mfma_f32_16x16x32_bf16 v[28:31], a[28:31], v[116:119], v[28:31]\n" \
  "v_mfma_f32_16x16x32_bf16 v[0:3], a[32:35], v[120:123], v[0:3]\n" \
  "v_mfma_f32_16x16x32_bf16 v[4:7], a[36:39], v[120:123], v[4:7]\n" \
  "s_waitcnt lgkmcnt(0)\n" \
  "s_barrier\n" \
  "v_mfma_f32_16x16x32_bf16 v[8:11], a[40:43], v[120:123], v[8:11]\n" \
  "buffer_load_dwordx4 v[88:91], v59, s[36:39], s69 offen offset:1024 sc1\n" \
  "ds_read_b128 v[32:35], v58 offset:0\n" \
  "v_mfma_f32_16x16x32_bf16 v[12:15], a[44:47], v[120:123], v[12:15]\n" \
  "ds_read_b128 v[36:39], v58 offset:8192\n" \
  "ds_read_b128 v[40:43], v58 offset:16384\n" \
  "v_mfma_f32_16x16x32_bf16 v[16:19], a[48:51], v[120:123], v[16:19]\n" \
  "ds_read_b128 v[44:47], v58 offset:24576\n" \
  "ds_read_b128 v[48:51], v58 offset:4096\n" \
  "v_mfma_f32_16x16x32_bf16 v[20:23], a[52:55], v[120:123], v[20:23]\n" \
  "ds_read_b128 v[240:243], v58 offset:12288\n" \
  "ds_read_b128 v[244:247], v58 offset:20480\n" \
  "v_mfma_f32_16x16x32_bf16 v[24:27], a[56:59], v[120:123], v[24:27]\n" \
  "ds_read_b128 v[248:251], v58 offset:28672\n" \
  "s_waitcnt vmcnt(8)\n" \
  "v_mfma_f32_16x16x32_bf16 v[28:31], a[60:63], v[120:123], v[28:31]\n" \
  "v_cmp_eq_u32 vcc, -1, v60\n" \
  "v_mfma_f32_16x16x32_bf16 v[0:3], a[64:67], v[124:127], v[0:3]\n" \
  "buffer_load_dwordx4 v[92:95], v59, s[36:39], s69 offen offset:2048 sc1\n" \
  "v_mfma_f32_16x16x32_bf16 v[4:7], a[68:71], v[124:127], v[4:7]\n" \
  "buffer_load_dwordx2 v[116:117], v56, s[36:39], s90 offen offset:0 sc1\n" \
  "s_cbranch_vccnz .Lsp2A_%=\n" \
  ".Lbp2A_%=:\n" \
  "v_mfma_f32_16x16x32_bf16 v[8:11], a[72:75], v[124:127], v[8:11]\n" \
  "buffer_load_dwordx2 v[118:119], v56, s[36:39], s90 offen offset:32 sc1\n" \
  "v_lshlrev_b32 v112, 16, v60\n" \
  "v_mfma_f32_16x16x32_bf16 v[12:15], a[76:79], v[124:127], v[12:15]\n" \
  "v_and_b32 v113, 0xffff0000, v60\n" \
  "v_add_f32 v64, v64, v112\n" \
  "v_mfma_f32_16x16x32_bf16 v[16:19], a[80:83], v[124:127], v[16:19]\n" \
  "s_waitcnt lgkmcnt(4)\n" \
  "v_add_f32 v32, v32, v36\n" \
  "v_mfma_f32_16x16x32_bf16 v[20:23], a[84:87], v[124:127], v[20:23]\n" \
  "v_add_f32 v33, v33, v37\n" \
  "v_add_f32 v34, v34, v38\n" \
  "v_mfma_f32_16x16x32_bf16 v[24:27], a[88:91], v[124:127], v[24:27]\n" \
  "buffer_load_dwordx4 v[96:99], v59, s[36:39], s70 offen offset:0 sc1\n" \
  "v_add_f32 v35, v35, v39\n" \
  "v_mfma_f32_16x16x32_bf16 v[28:31], a[92:95], v[124:127], v[28:31]\n" \
  "buffer_load_dwordx2 v[120:121], v56, s[36:39], s100 offen offset:0 sc1\n" \
  "v_add_f32 v40, v40, v44\n" \
  "v_mfma_f32_16x16x32_bf16 v[0:3], a[96:99], v[192:195], v[0:3]\n" \
  "buffer_load_dwordx2 v[122:123], v56, s[36:39], s100 offen offset:32 sc1\n" \
  "v_add_f32 v41, v41, v45\n" \
  "v_mfma_f32_16x16x32_bf16 v[4:7], a[100:103], v[192:195], v[4:7]\n" \
  "v_add_f32 v42, v42, v46\n" \
  "v_add_f32 v43, v43, v47\n" \
  "v_mfma_f32_16x16x32_bf16 v[8:11], a[104:107], v[192:195], v[8:11]\n" \
  "v_add_f32 v32, v32, v40\n" \
  "v_add_f32 v33, v33, v41\n" \
  "v_mfma_f32_16x16x32_bf16 v[12:15], a[108:111], v[192:195], v[12:15]\n" \
  "v_add_f32 v34, v34, v42\n" \
  "v_add_f32 v35, v35, v43\n" \
  "v_mfma_f32_16x16x32_bf16 v[16:19], a[112:115], v[192:195], v[16:19]\n" \
  "buffer_load_dwordx4 v[100:103], v59, s[36:39], s70 offen offset:1024 sc1\n" \
  "v_cvt_pk_bf16_f32 v36, v32, v33\n" \
  "v_mfma_f32_16x16x32_bf16 v[20:23], a[116:119], v[192:195], v[20:23]\n" \
  "v_cvt_pk_bf16_f32 v37, v34, v35\n" \
  "s_waitcnt lgkmcnt(0)\n" \
  "v_mfma_f32_16x16x32_bf16 v[24:27], a[120:123], v[192:195], v[24:27]\n" \
  "v_add_f32 v48, v48, v240\n" \
  "v_add_f32 v49, v49, v241\n" \
  "v_mfma_f32_16x16x32_bf16 v[28:31], a[124:127], v[192:195], v[28:31]\n" \
  "v_add_f32 v50, v50, v242\n" \
  "v_mfma_f32_16x16x32_bf16 v[0:3], a[128:131], v[196:199], v[0:3]\n" \
  "v_add_f32 v65, v65, v113\n" \
  "v_add_f32 v51, v51, v243\n" \
  "v_mfma_f32_16x16x32_bf16 v[4:7], a[132:135], v[196:199], v[4:7]\n" \
  "v_lshlrev_b32 v112, 16, v61\n" \
  "v_add_f32 v244, v244, v248\n" \
  "v_mfma_f32_16x16x32_bf16 v[8:11], a[136:139], v[196:199], v[8:11]\n" \
  "buffer_load_dwordx4 v[104:107], v59, s[36:39], s70 offen offset:2048 sc1\n" \
  "v_and_b32 v113, 0xffff0000, v61\n" \
  "v_mfma_f32_16x16x32_bf16 v[12:15], a[140:143], v[196:199], v[12:15]\n" \
  "v_add_f32 v245, v245, v249\n" \
  "v_add_f32 v66, v66, v112\n" \
  "v_mfma_f32_16x16x32_bf16 v[16:19], a[144:147], v[196:199], v[16:19]\n" \
  "v_add_f32 v246, v246, v250\n" \
  "v_add_f32 v67, v67, v113\n" \
  "v_mfma_f32_16x16x32_bf16 v[20:23], a[148:151], v[196:199], v[20:23]\n" \
  "v_add_f32 v247, v247, v251\n" \
  "v_lshlrev_b32 v112, 16, v62\n" \
  "v_mfma_f32_16x16x32_bf16 v[24:27], a[152:155], v[196:199], v[24:27]\n" \
  "v_add_f32 v48, v48, v244\n" \
  "v_and_b32 v113, 0xffff0000, v62\n" \
  "v_mfma_f32_16x16x32_bf16 v[28:31], a[156:159], v[196:199], v[28:31]\n" \
  "v_add_f32 v49, v49, v245\n" \
  "v_add_f32 v68, v68, v112\n" \
  "v_mfma_f32_16x16x32_bf16 v[0:3], a[160:163], v[200:203], v[0:3]\n" \
  "v_add_f32 v50, v50, v246\n" \
  "v_add_f32 v69, v69, v113\n" \
  "v_mfma_f32_16x16x32_bf16 v[4:7], a[164:167], v[200:203], v[4:7]\n" \
  "v_add_f32 v51, v51, v247\n" \
  "v_lshlrev_b32 v112, 16, v63\n" \
  "v_mfma_f32_16x16x32_bf16 v[8:11], a[168:171], v[200:203], v[8:11]\n" \
  "v_cvt_pk_bf16_f32 v38, v48, v49\n" \
  "v_and_b32 v113, 0xffff0000, v63\n" \
  "v_mfma_f32_16x16x32_bf16 v[12:15], a[172:175], v[200:203], v[12:15]\n" \
  "v_cvt_pk_bf16_f32 v39, v50, v51\n" \
  "v_add_f32 v70, v70, v112\n" \
  "v_mfma_f32_16x16x32_bf16 v[16:19], a[176:179], v[200:203], v[16:19]\n" \
  "buffer_store_dwordx4 v[36:39], v59, s[36:39], s74 offen offset:0 sc1\n" \
  "v_add_f32 v71, v71, v113\n" \
  "v_mfma_f32_16x16x32_bf16 v[20:23], a[180:183], v[200:203], v[20:23]\n" \
  "v_mfma_f32_16x16x32_bf16 v[24:27], a[184:187], v[200:203], v[24:27]\n" \
  "v_mfma_f32_16x16x32_bf16 v[28:31], a[188:191], v[200:203], v[28:31]\n" \
  "s_waitcnt vmcnt(16)\n" \
  "v_mfma_f32_16x16x32_bf16 v[0:3], a[192:195], v[204:207], v[0:3]\n" \
  "v_mfma_f32_16x16x32_bf16 v[4:7], a[196:199], v[204:207], v[4:7]\n" \
  "v_mfma_f32_16x16x32_bf16 v[8:11], a[200:203], v[204:207], v[8:11]\n" \
  "v_mfma_f32_16x16x32_bf16 v[12:15], a[204:207], v[204:207], v[12:15]\n" \
  "v_mfma_f32_16x16x32_bf16 v[16:19], a[208:211], v[204:207], v[16:19]\n" \
  "v_mfma_f32_16x16x32_bf16 v[20:23], a[212:215], v[204:207], v[20:23]\n" \
  "v_mfma_f32_16x16x32_bf16 v[24:27], a[216:219], v[204:207], v[24:27]\n" \
  "v_mfma_f32_16x16x32_bf16 v[28:31], a[220:223], v[204:207], v[28:31]\n" \
  "s_waitcnt vmcnt(13)\n" \
  "v_mfma_f32_16x16x32_bf16 v[208:211], v[128:131], v[72:75], 0\n" \
  "v_add_u32 v59, 409600, v59\n" \
  "s_waitcnt vmcnt(12)\n" \
  "v_mfma_f32_16x16x32_bf16 v[212:215], v[132:135], v[72:75], 0\n" \
  "ds_write_addtid_b32 v0 offset:32768\n" \
  "ds_write_addtid_b32 v1 offset:33024\n" \
  "v_mfma_f32_16x16x32_bf16 v[216:219], v[136:139], v[72:75], 0\n" \
  "ds_write_addtid_b32 v2 offset:33280\n" \
  "ds_write_addtid_b32 v3 offset:33536\n" \
  "v_mfma_f32_16x16x32_bf16 v[220:223], v[140:143], v[72:75], 0\n" \
  "buffer_load_dwordx4 v[112:115], v59, s[36:39], s73 offen offset:0 sc1\n" \
  "ds_write_addtid_b32 v4 offset:33792\n" \
  "v_mfma_f32_16x16x32_bf16 v[208:211], v[160:163], v[76:79], v[208:211]\n" \
  "ds_write_addtid_b32 v5 offset:34048\n" \
  "ds_write_addtid_b32 v6 offset:34304\n" \
  "v_mfma_f32_16x16x32_bf16 v[212:215], v[164:167], v[76:79], v[212:215]\n" \
  "ds_write_addtid_b32 v7 offset:34560\n" \
  "ds_write_addtid_b32 v8 offset:34816\n" \
  "v_mfma_f32_16x16x32_bf16 v[216:219], v[168:171], v[76:79], v[216:219]\n" \
  "ds_write_addtid_b32 v9 offset:35072\n" \
  "ds_write_addtid_b32 v10 offset:35328\n" \
  "v_mfma_f32_16x16x32_bf16 v[220:223], v[172:175], v[76:79], v[220:223]\n" \
  "ds_write_addtid_b32 v11 offset:35584\n" \
  "s_waitcnt vmcnt(11)\n" \
  "v_mfma_f32_16x16x32_bf16 v[208:211], a[0:3], v[80:83], v[208:211]\n" \
  "ds_write_addtid_b32 v12 offset:35840\n" \
  "ds_write_addtid_b32 v13 offset:36096\n" \
  "v_mfma_f32_16x16x32_bf16 v[212:215], a[4:7], v[80:83], v[212:215]\n" \
  "ds_write_addtid_b32 v14 offset:36352\n" \
  "ds_write_addtid_b32 v15 offset:36608\n" \
  "v_mfma_f32_16x16x32_bf16 v[216:219], a[8:11], v[80:83], v[216:219]\n" \
  "ds_write_addtid_b32 v16 offset:36864\n" \
  "ds_write_addtid_b32 v17 offset:37120\n" \
  "v_mfma_f32_16x16x32_bf16 v[220:223], a[12:15], v[80:83], v[220:223]\n" \
  "ds_write_addtid_b32 v18 offset:37376\n" \
  "ds_write_addtid_b32 v19 offset:37632\n" \
  "v_mfma_f32_16x16x32_bf16 v[208:211], a[32:35], v[84:87], v[208:211]\n" \
  "ds_write_addtid_b32 v20 offset:37888\n" \
  "ds_write_addtid_b32 v21 offset:38144\n" \
  "v_mfma_f32_16x16x32_bf16 v[212:215], a[36:39], v[84:87], v[212:215]\n" \
  "ds_write_addtid_b32 v22 offset:38400\n" \
  "ds_write_addtid_b32 v23 offset:38656\n" \
  "v_mfma_f32_16x16x32_bf16 v[216:219], a[40:43], v[84:87], v[216:219]\n" \
  "ds_write_addtid_b32 v24 offset:38912\n" \
  "ds_write_addtid_b32 v25 offset:39168\n" \
  "v_mfma_f32_16x16x32_bf16 v[220:223], a[44:47], v[84:87], v[220:223]\n" \
  "ds_write_addtid_b32 v26 offset:39424\n" \
  "s_waitcnt vmcnt(9)\n" \
  "v_mfma_f32_16x16x32_bf16 v[208:211], a[64:67], v[88:91], v[208:211]\n" \
  "ds_write_addtid_b32 v27 offset:39680\n" \
  "ds_write_addtid_b32 v28 offset:39936\n" \
  "v_mfma_f32_16x16x32_bf16 v[212:215], a[68:71], v[88:91], v[212:215]\n" \
  "ds_write_addtid_b32 v29 offset:40192\n" \
  "ds_write_addtid_b32 v30 offset:40448\n" \
  "v_mfma_f32_16x16x32_bf16 v[216:219], a[72:75], v[88:91], v[216:219]\n" \
  "ds_write_addtid_b32 v31 offset:40704\n" \
  "v_cmp_eq_u32 vcc, -1, v108\n" \
  "v_mfma_f32_16x16x32_bf16 v[220:223], a[76:79], v[88:91], v[220:223]\n" \
  "v_and_b32 v62, 0x30, v59\n" \
  "v_add_u32 v62, s67, v62\n" \
  "v_mfma_f32_16x16x32_bf16 v[208:211], a[96:99], v[92:95], v[208:211]\n" \
  "s_cbranch_vccnz .Lsp2B_%=\n" \
  ".Lbp2B_%=:\n" \
  "v_lshlrev_b32 v60, 16, v108\n" \
  "v_mfma_f32_16x16x32_bf16 v[212:215], a[100:103], v[92:95], v[212:215]\n" \
  "v_and_b32 v61, 0xffff0000, v108\n" \
  "v_add_f32 v52, v52, v60\n" \
  "v_mfma_f32_16x16x32_bf16 v[216:219], a[104:107], v[92:95], v[216:219]\n" \
  "v_add_f32 v53, v53, v61\n" \
  "v_lshlrev_b32 v60, 16, v109\n" \
  "v_mfma_f32_16x16x32_bf16 v[220:223], a[108:111], v[92:95], v[220:223]\n" \
  "v_and_b32 v61, 0xffff0000, v109\n" \
  "s_waitcnt vmcnt(3)\n" \
  "v_mfma_f32_16x16x32_bf16 v[208:211], a[128:131], v[96:99], v[208:211]\n" \
  "s_waitcnt lgkmcnt(0)\n" \
  "s_barrier\n" \
  "v_mfma_f32_16x16x32_bf16 v[212:215], a[132:135], v[96:99], v[212:215]\n" \
  "ds_read_b128 v[32:35], v58 offset:32768\n" \
  "ds_read_b128 v[36:39], v58 offset:40960\n" \
  "v_mfma_f32_16x16x32_bf16 v[216:219], a[136:139], v[96:99], v[216:219]\n" \
  "ds_read_b128 v[40:43], v58 offset:49152\n" \
  "ds_read_b128 v[44:47], v58 offset:57344\n" \
  "v_mfma_f32_16x16x32_bf16 v[220:223], a[140:143], v[96:99], v[220:223]\n" \
  "ds_read_b128 v[48:51], v58 offset:36864\n" \
  "ds_read_b128 v[240:243], v58 offset:45056\n" \
  "v_mfma_f32_16x16x32_bf16 v[208:211], a[160:163], v[100:103], v[208:211]\n" \
  "ds_read_b128 v[244:247], v58 offset:53248\n" \
  "ds_read_b128 v[248:251], v58 offset:61440\n" \
  "v_mfma_f32_16x16x32_bf16 v[212:215], a[164:167], v[100:103], v[212:215]\n" \
  "v_add_f32 v54, v54, v60\n" \
  "v_add_f32 v55, v55, v61\n" \
  "v_mfma_f32_16x16x32_bf16 v[216:219], a[168:171], v[100:103], v[216:219]\n" \
  "v_lshlrev_b32 v60, 16, v110\n" \
  "v_and_b32 v61, 0xffff0000, v110\n" \
  "v_mfma_f32_16x16x32_bf16 v[220:223], a[172:175], v[100:103], v[220:223]\n" \
  "v_add_f32 v252, v252, v60\n" \
  "s_waitcnt vmcnt(2)\n" \
  "v_mfma_f32_16x16x32_bf16 v[208:211], a[192:195], v[104:107], v[208:211]\n" \
  "v_add_f32 v253, v253, v61\n" \
  "v_lshlrev_b32 v60, 16, v111\n" \
  "v_mfma_f32_16x16x32_bf16 v[212:215], a[196:199], v[104:107], v[212:215]\n" \
  "v_and_b32 v61, 0xffff0000, v111\n" \
  "v_add_f32 v254, v254, v60\n" \
  "v_mfma_f32_16x16x32_bf16 v[216:219], a[200:203], v[104:107], v[216:219]\n" \
  "s_waitcnt lgkmcnt(4)\n" \
  "v_add_f32 v32, v32, v36\n" \
  "v_mfma_f32_16x16x32_bf16 v[220:223], a[204:207], v[104:107], v[220:223]\n" \
  "v_add_f32 v33, v33, v37\n" \
  "v_add_f32 v34, v34, v38\n" \
  "v_mfma_f32_16x16x32_bf16 v[224:227], v[144:147], v[72:75], 0\n" \
  "v_add_f32 v35, v35, v39\n" \
  "v_add_f32 v40, v40, v44\n" \
  "v_mfma_f32_16x16x32_bf16 v[228:231], v[148:151], v[72:75], 0\n" \
  "v_add_f32 v41, v41, v45\n" \
  "v_add_f32 v42, v42, v46\n" \
  "v_mfma_f32_16x16x32_bf16 v[232:235], v[152:155], v[72:75], 0\n" \
  "ds_write_addtid_b32 v208 offset:0\n" \
  "ds_write_addtid_b32 v209 offset:256\n" \
  "v_mfma_f32_16x16x32_bf16 v[236:239], v[156:159], v[72:75], 0\n" \
  "ds_write_addtid_b32 v210 offset:512\n" \
  "ds_write_addtid_b32 v211 offset:768\n" \
  "v_mfma_f32_16x16x32_bf16 v[224:227], v[176:179], v[76:79], v[224:227]\n" \
  "ds_write_addtid_b32 v212 offset:1024\n" \
  "ds_write_addtid_b32 v213 offset:1280\n" \
  "v_mfma_f32_16x16x32_bf16 v[228:231], v[180:183], v[76:79], v[228:231]\n" \
  "ds_write_addtid_b32 v214 offset:1536\n" \
  "ds_write_addtid_b32 v215 offset:1792\n" \
  "v_mfma_f32_16x16x32_bf16 v[232:235], v[184:187], v[76:79], v[232:235]\n" \
  "ds_write_addtid_b32 v216 offset:2048\n" \
  "ds_write_addtid_b32 v217 offset:2304\n" \
  "v_mfma_f32_16x16x32_bf16 v[236:239], v[188:191], v[76:79], v[236:239]\n" \
  "ds_write_addtid_b32 v218 offset:2560\n" \
  "ds_write_addtid_b32 v219 offset:2816\n" \
  "v_mfma_f32_16x16x32_bf16 v[224:227], a[16:19], v[80:83], v[224:227]\n" \
  "ds_write_addtid_b32 v220 offset:3072\n" \
  "ds_write_addtid_b32 v221 offset:3328\n" \
  "v_mfma_f32_16x16x32_bf16 v[228:231], a[20:23], v[80:83], v[228:231]\n" \
  "ds_write_addtid_b32 v222 offset:3584\n" \
  "ds_write_addtid_b32 v223 offset:3840\n" \
  "v_mfma_f32_16x16x32_bf16 v[232:235], a[24:27], v[80:83], v[232:235]\n" \
  "v_add_f32 v43, v43, v47\n" \
  "v_add_f32 v32, v32, v40\n" \
  "v_mfma_f32_16x16x32_bf16 v[236:239], a[28:31], v[80:83], v[236:239]\n" \
  "v_add_f32 v33, v33, v41\n" \
  "v_add_f32 v34, v34, v42\n" \
  "v_mfma_f32_16x16x32_bf16 v[224:227], a[48:51], v[84:87], v[224:227]\n" \
  "v_add_f32 v255, v255, v61\n" \
  "v_add_f32 v35, v35, v43\n" \
  "v_mfma_f32_16x16x32_bf16 v[228:231], a[52:55], v[84:87], v[228:231]\n" \
  "s_waitcnt vmcnt(0)\n" \
  "v_mfma_f32_16x16x32_bf16 v[232:235], a[56:59], v[84:87], v[232:235]\n" \
  "buffer_load_dwordx4 v[124:127], v59, s[36:39], s78 offen offset:0 sc1\n" \
  "v_cmp_eq_u32 vcc, -1, v112\n" \
  "v_mfma_f32_16x16x32_bf16 v[236:239], a[60:63], v[84:87], v[236:239]\n" \
  "s_waitcnt lgkmcnt(0)\n" \
  "s_barrier\n" \
  "v_mfma_f32_16x16x32_bf16 v[224:227], a[80:83], v[88:91], v[224:227]\n" \
  "ds_read_b128 v[16:19], v58 offset:0\n" \
  "ds_read_b128 v[20:23], v58 offset:8192\n" \
  "v_mfma_f32_16x16x32_bf16 v[228:231], a[84:87], v[88:91], v[228:231]\n" \
  "ds_read_b128 v[24:27], v58 offset:16384\n" \
  "ds_read_b128 v[28:31], v58 offset:24576\n" \
  "v_mfma_f32_16x16x32_bf16 v[232:235], a[88:91], v[88:91], v[232:235]\n" \
  "v_add_f32 v48, v48, v240\n" \
  "s_cbranch_vccnz .Lsp3A_%=\n" \
  ".Lbp3A_%=:\n" \
  "v_mfma_f32_16x16x32_bf16 v[236:239], a[92:95], v[88:91], v[236:239]\n" \
  "v_add_f32 v49, v49, v241\n" \
  "v_lshlrev_b32 v60, 16, v112\n" \
  "v_mfma_f32_16x16x32_bf16 v[224:227], a[112:115], v[92:95], v[224:227]\n" \
  "v_add_f32 v50, v50, v242\n" \
  "v_and_b32 v61, 0xffff0000, v112\n" \
  "v_mfma_f32_16x16x32_bf16 v[228:231], a[116:119], v[92:95], v[228:231]\n" \
  "v_add_f32 v51, v51, v243\n" \
  "v_add_f32 v64, v64, v60\n" \
  "v_mfma_f32_16x16x32_bf16 v[232:235], a[120:123], v[92:95], v[232:235]\n" \
  "v_add_f32 v244, v244, v248\n" \
  "v_add_f32 v65, v65, v61\n" \
  "v_mfma_f32_16x16x32_bf16 v[236:239], a[124:127], v[92:95], v[236:239]\n" \
  "v_add_f32 v245, v245, v249\n" \
  "v_lshlrev_b32 v60, 16, v113\n" \
  "v_mfma_f32_16x16x32_bf16 v[224:227], a[144:147], v[96:99], v[224:227]\n" \
  "v_add_f32 v246, v246, v250\n" \
  "v_and_b32 v61, 0xffff0000, v113\n" \
  "v_mfma_f32_16x16x32_bf16 v[228:231], a[148:151], v[96:99], v[228:231]\n" \
  "v_add_f32 v247, v247, v251\n" \
  "v_add_f32 v66, v66, v60\n" \
  "v_mfma_f32_16x16x32_bf16 v[232:235], a[152:155], v[96:99], v[232:235]\n" \
  "v_add_f32 v48, v48, v244\n" \
  "v_add_f32 v67, v67, v61\n" \
  "v_mfma_f32_16x16x32_bf16 v[236:239], a[156:159], v[96:99], v[236:239]\n" \
  "v_add_f32 v49, v49, v245\n" \
  "v_lshlrev_b32 v60, 16, v114\n" \
  "v_add_f32 v50, v50, v246\n" \
  "v_mfma_f32_16x16x32_bf16 v[224:227], a[176:179], v[100:103], v[224:227]\n" \
  "v_and_b32 v61, 0xffff0000, v114\n" \
  "v_add_f32 v51, v51, v247\n" \
  "v_add_f32 v68, v68, v60\n" \
  "v_mfma_f32_16x16x32_bf16 v[228:231], a[180:183], v[100:103], v[228:231]\n" \
  "v_add_f32 v69, v69, v61\n" \
  "v_lshlrev_b32 v60, 16, v115\n" \
  "v_and_b32 v61, 0xffff0000, v115\n" \
  "v_mfma_f32_16x16x32_bf16 v[232:235], a[184:187], v[100:103], v[232:235]\n" \
  "v_add_f32 v70, v70, v60\n" \
  "ds_read_b128 v[8:11], v62 offset:0\n" \
  "v_mfma_f32_16x16x32_bf16 v[236:239], a[188:191], v[100:103], v[236:239]\n" \
  "v_add_f32 v71, v71, v61\n" \
  "ds_read_b128 v[12:15], v62 offset:64\n" \
  "v_mfma_f32_16x16x32_bf16 v[224:227], a[208:211], v[104:107], v[224:227]\n" \
  "v_lshlrev_b32 v0, 16, v116\n" \
  "v_and_b32 v1, 0xffff0000, v116\n" \
  "v_mfma_f32_16x16x32_bf16 v[228:231], a[212:215], v[104:107], v[228:231]\n" \
  "v_lshlrev_b32 v2, 16, v117\n" \
  "v_and_b32 v3, 0xffff0000, v117\n" \
  "v_mfma_f32_16x16x32_bf16 v[232:235], a[216:219], v[104:107], v[232:235]\n" \
  "v_lshlrev_b32 v4, 16, v120\n" \
  "v_and_b32 v5, 0xffff0000, v120\n" \
  "v_mfma_f32_16x16x32_bf16 v[236:239], a[220:223], v[104:107], v[236:239]\n" \
  "v_lshlrev_b32 v6, 16, v121\n" \
  "v_and_b32 v7, 0xffff0000, v121\n" \
  "s_waitcnt lgkmcnt(0)\n" \
  "v_fma_f32 v0, v8, v64, v0\n" \
  "v_fma_f32 v1, v9, v65, v1\n" \
  "v_fma_f32 v2, v10, v66, v2\n" \
  "v_fma_f32 v3, v11, v67, v3\n" \
  "v_fma_f32 v4, v8, v68, v4\n" \
  "v_fma_f32 v5, v9, v69, v5\n" \
  "v_fma_f32 v6, v10, v70, v6\n" \
  "v_fma_f32 v7, v11, v71, v7\n" \
  "v_fma_f32 v0, v8, v32, v0\n" \
  "v_fma_f32 v1, v9, v33, v1\n" \
  "v_fma_f32 v2, v10, v34, v2\n" \
  "v_fma_f32 v3, v11, v35, v3\n" \
  "v_cvt_pk_bf16_f32 v0, v0, v1\n" \
  "v_cvt_pk_bf16_f32 v1, v2, v3\n" \
  "buffer_store_dwordx2 v[0:1], v56, s[44:47], 0 offen offset:0 sc1\n" \
  "v_fma_f32 v4, v8, v48, v4\n" \
  "v_fma_f32 v5, v9, v49, v5\n" \
  "v_fma_f32 v6, v10, v50, v6\n" \
  "v_fma_f32 v7, v11, v51, v7\n" \
  "v_cvt_pk_bf16_f32 v4, v4, v5\n" \
  "v_cvt_pk_bf16_f32 v5, v6, v7\n" \
  "buffer_store_dwordx2 v[4:5], v56, s[44:47], s35 offen offset:0 sc1\n" \
  "v_lshlrev_b32 v64, 16, v118\n" \
  "v_and_b32 v65, 0xffff0000, v118\n" \
  "v_lshlrev_b32 v66, 16, v119\n" \
  "v_and_b32 v67, 0xffff0000, v119\n" \
  "ds_write_addtid_b32 v224 offset:4096\n" \
  "ds_write_addtid_b32 v225 offset:4352\n" \
  "ds_write_addtid_b32 v226 offset:4608\n" \
  "ds_write_addtid_b32 v227 offset:4864\n" \
  "ds_write_addtid_b32 v228 offset:5120\n" \
  "ds_write_addtid_b32 v229 offset:5376\n" \
  "ds_write_addtid_b32 v230 offset:5632\n" \
  "ds_write_addtid_b32 v231 offset:5888\n" \
  "ds_write_addtid_b32 v232 offset:6144\n" \
  "ds_write_addtid_b32 v233 offset:6400\n" \
  "ds_write_addtid_b32 v234 offset:6656\n" \
  "ds_write_addtid_b32 v235 offset:6912\n" \
  "ds_write_addtid_b32 v236 offset:7168\n" \
  "ds_write_addtid_b32 v237 offset:7424\n" \
  "ds_write_addtid_b32 v238 offset:7680\n" \
  "ds_write_addtid_b32 v239 offset:7936\n" \
  "v_lshlrev_b32 v68, 16, v122\n" \
  "v_and_b32 v69, 0xffff0000, v122\n" \
  "v_lshlrev_b32 v70, 16, v123\n" \
  "v_and_b32 v71, 0xffff0000, v123\n" \
  "s_waitcnt lgkmcnt(15)\n" \
  "v_pk_add_f32 v[16:17], v[16:17], v[20:21]\n" \
  "v_pk_add_f32 v[18:19], v[18:19], v[22:23]\n" \
  "v_pk_add_f32 v[24:25], v[24:25], v[28:29]\n" \
  "v_pk_add_f32 v[26:27], v[26:27], v[30:31]\n" \
  "v_pk_add_f32 v[16:17], v[16:17], v[24:25]\n" \
  "v_pk_add_f32 v[18:19], v[18:19], v[26:27]\n" \
  "s_waitcnt lgkmcnt(0)\n" \
  "s_barrier\n" \
  "ds_read_b128 v[32:35], v58 offset:4096\n" \
  "ds_read_b128 v[36:39], v58 offset:12288\n" \
  "ds_read_b128 v[40:43], v58 offset:20480\n" \
  "ds_read_b128 v[44:47], v58 offset:28672\n" \
  "s_waitcnt vmcnt(2)\n" \
  "v_cmp_eq_u32 vcc, -1, v124\n" \
  "s_cbranch_vccnz .Lsp3B_%=\n" \
  ".Lbp3B_%=:\n" \
  "v_lshlrev_b32 v20, 16, v124\n" \
  "v_and_b32 v21, 0xffff0000, v124\n" \
  "v_lshlrev_b32 v22, 16, v125\n" \
  "v_and_b32 v23, 0xffff0000, v125\n" \
  "v_lshlrev_b32 v24, 16, v126\n" \
  "v_and_b32 v25, 0xffff0000, v126\n" \
  "v_lshlrev_b32 v26, 16, v127\n" \
  "v_and_b32 v27, 0xffff0000, v127\n" \
  "v_pk_add_f32 v[52:53], v[52:53], v[20:21]\n" \
  "v_pk_add_f32 v[54:55], v[54:55], v[22:23]\n" \
  "v_pk_add_f32 v[252:253], v[252:253], v[24:25]\n" \
  "v_pk_add_f32 v[254:255], v[254:255], v[26:27]\n" \
  "v_pk_fma_f32 v[64:65], v[12:13], v[52:53], v[64:65]\n" \
  "v_pk_fma_f32 v[66:67], v[14:15], v[54:55], v[66:67]\n" \
  "v_pk_fma_f32 v[68:69], v[12:13], v[252:253], v[68:69]\n" \
  "v_pk_fma_f32 v[70:71], v[14:15], v[254:255], v[70:71]\n" \
  "v_pk_fma_f32 v[64:65], v[12:13], v[16:17], v[64:65]\n" \
  "v_pk_fma_f32 v[66:67], v[14:15], v[18:19], v[66:67]\n" \
  "v_cvt_pk_bf16_f32 v64, v64, v65\n" \
  "v_cvt_pk_bf16_f32 v65, v66, v67\n" \
  "buffer_store_dwordx2 v[64:65], v56, s[44:47], 0 offen offset:32 sc1\n" \
  "s_waitcnt lgkmcnt(0)\n" \
  "v_pk_add_f32 v[32:33], v[32:33], v[36:37]\n" \
  "v_pk_add_f32 v[34:35], v[34:35], v[38:39]\n" \
  "v_pk_add_f32 v[40:41], v[40:41], v[44:45]\n" \
  "v_pk_add_f32 v[42:43], v[42:43], v[46:47]\n" \
  "v_pk_add_f32 v[32:33], v[32:33], v[40:41]\n" \
  "v_pk_add_f32 v[34:35], v[34:35], v[42:43]\n" \
  "v_pk_fma_f32 v[68:69], v[12:13], v[32:33], v[68:69]\n" \
  "v_pk_fma_f32 v[70:71], v[14:15], v[34:35], v[70:71]\n" \
  "v_cvt_pk_bf16_f32 v68, v68, v69\n" \
  "v_cvt_pk_bf16_f32 v69, v70, v71\n" \
  "buffer_store_dwordx2 v[68:69], v56, s[44:47], s35 offen offset:32 sc1\n" \
  "s_branch .Lend_%=\n" \
  ".LsaF_%=:\n" \
  "s_memrealtime s[94:95]\n" \
  "s_waitcnt lgkmcnt(0)\n" \
  ".LpaF_%=:\n" \
  "s_sleep 2\n" \
  "buffer_load_dword v63, v61, s[36:39], s59 offen offset:0 sc1\n" \
  "s_memrealtime s[96:97]\n" \
  "s_waitcnt vmcnt(0) lgkmcnt(0)\n" \
  "s_sub_u32 s96, s96, s94\n" \
  "s_cmp_gt_u32 s96, 0x4c4b40\n" \
  "s_cbranch_scc1 .LbaF_%=\n" \
  "v_cmp_eq_u32 vcc, 0, v63\n" \
  "s_and_b64 vcc, vcc, s[86:87]\n" \
  "s_cbranch_scc1 .LpaF_%=\n" \
  "s_branch .LbaF_%=\n" \
  ".LsbF_%=:\n" \
  "s_memrealtime s[94:95]\n" \
  "s_waitcnt lgkmcnt(0)\n" \
  ".LpbF_%=:\n" \
  "s_sleep 2\n" \
  "buffer_load_dword v63, v61, s[36:39], s59 offen offset:0 sc1\n" \
  "s_memrealtime s[96:97]\n" \
  "s_waitcnt vmcnt(0) lgkmcnt(0)\n" \
  "s_sub_u32 s96, s96, s94\n" \
  "s_cmp_gt_u32 s96, 0x4c4b40\n" \
  "s_cbranch_scc1 .LbbF_%=\n" \
  "v_cmp_eq_u32 vcc, 0, v63\n" \
  "s_and_b64 vcc, vcc, s[86:87]\n" \
  "s_cbranch_scc1 .LpbF_%=\n" \
  "s_branch .LbbF_%=\n" \
  ".LsaS_%=:\n" \
  "s_memrealtime s[94:95]\n" \
  "s_waitcnt lgkmcnt(0)\n" \
  ".LpaS_%=:\n" \
  "s_sleep 2\n" \
  "buffer_load_dword v63, v61, s[36:39], s59 offen offset:0 sc1\n" \
  "s_memrealtime s[96:97]\n" \
  "s_waitcnt vmcnt(0) lgkmcnt(0)\n" \
  "s_sub_u32 s96, s96, s94\n" \
  "s_cmp_gt_u32 s96, 0x4c4b40\n" \
  "s_cbranch_scc1 .LbaS_%=\n" \
  "v_cmp_eq_u32 vcc, 0, v63\n" \
  "s_and_b64 vcc, vcc, s[86:87]\n" \
  "s_cbranch_scc1 .LpaS_%=\n" \
  "s_branch .LbaS_%=\n" \
  ".LsbS_%=:\n" \
  "s_memrealtime s[94:95]\n" \
  "s_waitcnt lgkmcnt(0)\n" \
  ".LpbS_%=:\n" \
  "s_sleep 2\n" \
  "buffer_load_dword v63, v61, s[36:39], s59 offen offset:0 sc1\n" \
  "s_memrealtime s[96:97]\n" \
  "s_waitcnt vmcnt(0) lgkmcnt(0)\n" \
  "s_sub_u32 s96, s96, s94\n" \
  "s_cmp_gt_u32 s96, 0x4c4b40\n" \
  "s_cbranch_scc1 .LbbS_%=\n" \
  "v_cmp_eq_u32 vcc, 0, v63\n" \
  "s_and_b64 vcc, vcc, s[86:87]\n" \
  "s_cbranch_scc1 .LpbS_%=\n" \
  "s_branch .LbbS_%=\n" \
  ".Lsp3B_%=:\n" \
  "s_memrealtime s[94:95]\n" \
  "s_waitcnt lgkmcnt(0)\n" \
  ".Lpp3B_%=:\n" \
  "s_sub_u32 s101, s78, 0\n" \
  "buffer_load_dwordx4 v[124:127], v59, s[36:39], s101 offen offset:0 sc1\n" \
  "s_memrealtime s[96:97]\n" \
  "s_waitcnt vmcnt(0) lgkmcnt(0)\n" \
  "s_sub_u32 s96, s96, s94\n" \
  "s_cmp_gt_u32 s96, 0x4c4b40\n" \
  "s_cbranch_scc1 .Lbp3B_%=\n" \
  "v_cmp_eq_u32 vcc, -1, v124\n" \
  "s_cbranch_vccnz .Lpp3B_%=\n" \
  "s_branch .Lbp3B_%=\n" \
  ".Lspf_%=:\n" \
  "s_memrealtime s[94:95]\n" \
  "s_waitcnt lgkmcnt(0)\n" \
  ".Lppf_%=:\n" \
  "s_sleep 2\n" \
  "buffer_load_dword v53, v52, s[36:39], s30 offen offset:0 sc1\n" \
  "s_memrealtime s[96:97]\n" \
  "s_waitcnt vmcnt(0) lgkmcnt(0)\n" \
  "s_sub_u32 s96, s96, s94\n" \
  "s_cmp_gt_u32 s96, 0x4c4b40\n" \
  "s_cbranch_scc1 .Lbpf_%=\n" \
  "v_cmp_eq_u32 vcc, 0, v53\n" \
  "s_cbranch_vccnz .Lppf_%=\n" \
  "s_branch .Lbpf_%=\n" \
  ".Lsg4_%=:\n" \
  "s_memrealtime s[94:95]\n" \
  "s_waitcnt lgkmcnt(0)\n" \
  ".Lpg4_%=:\n" \
  "s_sleep 2\n" \
  "buffer_load_dword v63, v61, s[36:39], s59 offen offset:0 sc1\n" \
  "s_memrealtime s[96:97]\n" \
  "s_waitcnt vmcnt(0) lgkmcnt(0)\n" \
  "s_sub_u32 s96, s96, s94\n" \
  "s_cmp_gt_u32 s96, 0x4c4b40\n" \
  "s_cbranch_scc1 .Lbg4_%=\n" \
  "v_cmp_eq_u32 vcc, 0, v63\n" \
  "s_and_b64 vcc, vcc, s[86:87]\n" \
  "s_cbranch_scc1 .Lpg4_%=\n" \
  "s_branch .Lbg4_%=\n" \
  ".Lse_%=:\n" \
  "s_memrealtime s[94:95]\n" \
  "s_waitcnt lgkmcnt(0)\n" \
  ".Lpe_%=:\n" \
  "buffer_load_dword v64, v69, s[36:39], s79 offen offset:0 sc1\n" \
  "buffer_load_dword v65, v69, s[36:39], s80 offen offset:0 sc1\n" \
  "buffer_load_dword v66, v69, s[36:39], s81 offen offset:0 sc1\n" \
  "buffer_load_dword v67, v69, s[36:39], s82 offen offset:0 sc1\n" \
  "s_memrealtime s[96:97]\n" \
  "s_waitcnt vmcnt(0) lgkmcnt(0)\n" \
  "s_sub_u32 s96, s96, s94\n" \
  "s_cmp_gt_u32 s96, 0x4c4b40\n" \
  "s_cbranch_scc1 .Lbe_%=\n" \
  "v_max3_u32 v52, v64, v65, v66\n" \
  "v_max_u32 v52, v52, v67\n" \
  "v_cmp_eq_u32 vcc, -1, v52\n" \
  "s_cbranch_vccnz .Lpe_%=\n" \
  "s_branch .Lbe_%=\n" \
  ".Lsp1_%=:\n" \
  "s_memrealtime s[94:95]\n" \
  "s_waitcnt lgkmcnt(0)\n" \
  ".Lpp1_%=:\n" \
  "s_sub_u32 s101, s71, 409600\n" \
  "buffer_load_dwordx4 v[64:67], v59, s[36:39], s101 offen offset:0 sc1\n" \
  "s_sub_u32 s101, s76, 409600\n" \
  "buffer_load_dwordx4 v[52:55], v59, s[36:39], s101 offen offset:0 sc1\n" \
  "s_memrealtime s[96:97]\n" \
  "s_waitcnt vmcnt(0) lgkmcnt(0)\n" \
  "s_sub_u32 s96, s96, s94\n" \
  "s_cmp_gt_u32 s96, 0x4c4b40\n" \
  "s_cbranch_scc1 .Lbp1_%=\n" \
  "v_max_u32 v62, v64, v52\n" \
  "v_cmp_eq_u32 vcc, -1, v62\n" \
  "s_cbranch_vccnz .Lpp1_%=\n" \
  "s_branch .Lbp1_%=\n" \
  ".Lsp2A_%=:\n" \
  "s_memrealtime s[94:95]\n" \
  "s_waitcnt lgkmcnt(0)\n" \
  ".Lpp2A_%=:\n" \
  "s_sub_u32 s101, s72, 409600\n" \
  "buffer_load_dwordx4 v[60:63], v59, s[36:39], s101 offen offset:0 sc1\n" \
  "s_memrealtime s[96:97]\n" \
  "s_waitcnt vmcnt(0) lgkmcnt(0)\n" \
  "s_sub_u32 s96, s96, s94\n" \
  "s_cmp_gt_u32 s96, 0x4c4b40\n" \
  "s_cbranch_scc1 .Lbp2A_%=\n" \
  "v_cmp_eq_u32 vcc, -1, v60\n" \
  "s_cbranch_vccnz .Lpp2A_%=\n" \
  "s_branch .Lbp2A_%=\n" \
  ".Lsp2B_%=:\n" \
  "s_memrealtime s[94:95]\n" \
  "s_waitcnt lgkmcnt(0)\n" \
  ".Lpp2B_%=:\n" \
  "s_sub_u32 s101, s77, 409600\n" \
  "buffer_load_dwordx4 v[108:111], v59, s[36:39], s101 offen offset:0 sc1\n" \
  "s_memrealtime s[96:97]\n" \
  "s_waitcnt vmcnt(0) lgkmcnt(0)\n" \
  "s_sub_u32 s96, s96, s94\n" \
  "s_cmp_gt_u32 s96, 0x4c4b40\n" \
  "s_cbranch_scc1 .Lbp2B_%=\n" \
  "v_cmp_eq_u32 vcc, -1, v108\n" \
  "s_cbranch_vccnz .Lpp2B_%=\n" \
  "s_branch .Lbp2B_%=\n" \
  ".Lsp3A_%=:\n" \
  "s_memrealtime s[94:95]\n" \
  "s_waitcnt lgkmcnt(0)\n" \
  ".Lpp3A_%=:\n" \
  "s_sub_u32 s101, s73, 0\n" \
  "buffer_load_dwordx4 v[112:115], v59, s[36:39], s101 offen offset:0 sc1\n" \
  "s_memrealtime s[96:97]\n" \
  "s_waitcnt vmcnt(0) lgkmcnt(0)\n" \
  "s_sub_u32 s96, s96, s94\n" \
  "s_cmp_gt_u32 s96, 0x4c4b40\n" \
  "s_cbranch_scc1 .Lbp3A_%=\n" \
  "v_cmp_eq_u32 vcc, -1, v112\n" \
  "s_cbranch_vccnz .Lpp3A_%=\n" \
  "s_branch .Lbp3A_%=\n" \
  ".Lz0_%=:\n" \
  "s_branch .Lz1_%=\n" \
  ".Lxq5_%=:\n" \
  "s_mov_b64 exec, 1\n" \
  "buffer_store_dword v61, off, s[36:39], s54 offset:0 sc1\n" \
  "s_mov_b64 exec, -1\n" \
  "buffer_load_dword v63, v61, s[36:39], s75 offen offset:0 sc1\n" \
  "s_mov_b32 s75, s59\n" \
  "s_add_u32 s54, s54, 0x10000\n" \
  "s_add_u32 s83, s83, 819200\n" \
  "s_branch .Lyq5_%=\n" \
  ".Lz1_%=:\n" \
  "s_branch .Lz2_%=\n" \
  ".Lxt5_%=:\n" \
  "v_and_b32 v20, 0x300, v59\n" \
  "v_and_b32 v21, 0xf0, v59\n" \
  "v_lshlrev_b32 v20, 4, v20\n" \
  "s_mov_b32 m0, 162176\n" \
  "v_lshl_or_b32 v20, v21, 3, v20\n" \
  "buffer_load_dword v20, s[40:43], s101 offen lds\n" \
  "s_mov_b32 m0, s56\n" \
  "v_and_b32 v26, 0x3ff, v59\n" \
  "v_mov_b32 v22, -1\n" \
  "v_mov_b32 v23, -1\n" \
  "v_mov_b32 v24, -1\n" \
  "v_mov_b32 v25, -1\n" \
  "s_add_u32 s96, s74, 819200\n" \
  "buffer_store_dwordx4 v[22:25], v26, s[36:39], s96 offen offset:0 sc1\n" \
  "s_add_u32 s96, s74, 1228800\n" \
  "buffer_store_dwordx4 v[22:25], v26, s[36:39], s96 offen offset:0 sc1\n" \
  "s_add_u32 s96, s74, 1638400\n" \
  "buffer_store_dwordx4 v[22:25], v26, s[36:39], s96 offen offset:0 sc1\n" \
  "s_add_u32 s96, s74, 2048000\n" \
  "buffer_store_dwordx4 v[22:25], v26, s[36:39], s96 offen offset:0 sc1\n" \
  "s_add_u32 s96, s74, 2457600\n" \
  "buffer_store_dwordx4 v[22:25], v26, s[36:39], s96 offen offset:0 sc1\n" \
  "s_add_u32 s96, s74, 2867200\n" \
  "buffer_store_dwordx4 v[22:25], v26, s[36:39], s96 offen offset:0 sc1\n" \
  "s_add_u32 s96, s74, 3276800\n" \
  "buffer_store_dwordx4 v[22:25], v26, s[36:39], s96 offen offset:0 sc1\n" \
  "s_nop 0\n" \
  "s_branch .Lyt5_%=\n" \
  ".Lz2_%=:\n" \
  "s_branch .Lz3_%=\n" \
  ".Lxq4_%=:\n" \
  "s_mov_b64 exec, 1\n" \
  "buffer_store_dword v61, off, s[36:39], s54 offset:0 sc1\n" \
  "s_mov_b64 exec, -1\n" \
  "buffer_load_dword v63, v61, s[36:39], s75 offen offset:0 sc1\n" \
  "s_mov_b32 s75, s59\n" \
  "s_add_u32 s54, s54, 0x10000\n" \
  "s_add_u32 s83, s83, 819200\n" \
  "s_branch .Lyq4_%=\n" \
  ".Lz3_%=:\n" \
  "s_branch .Lz4_%=\n" \
  ".Lxt4_%=:\n" \
  "v_and_b32 v0, 0x300, v59\n" \
  "v_and_b32 v1, 0xf0, v59\n" \
  "v_lshlrev_b32 v0, 4, v0\n" \
  "s_mov_b32 m0, 162176\n" \
  "v_lshl_or_b32 v0, v1, 3, v0\n" \
  "buffer_load_dword v0, s[40:43], s101 offen lds\n" \
  "s_mov_b32 m0, s56\n" \
  "v_and_b32 v6, 0x3ff, v59\n" \
  "v_mov_b32 v2, -1\n" \
  "v_mov_b32 v3, -1\n" \
  "v_mov_b32 v4, -1\n" \
  "v_mov_b32 v5, -1\n" \
  "s_add_u32 s96, s74, 819200\n" \
  "buffer_store_dwordx4 v[2:5], v6, s[36:39], s96 offen offset:0 sc1\n" \
  "s_add_u32 s96, s74, 1228800\n" \
  "buffer_store_dwordx4 v[2:5], v6, s[36:39], s96 offen offset:0 sc1\n" \
  "s_add_u32 s96, s74, 1638400\n" \
  "buffer_store_dwordx4 v[2:5], v6, s[36:39], s96 offen offset:0 sc1\n" \
  "s_add_u32 s96, s74, 2048000\n" \
  "buffer_store_dwordx4 v[2:5], v6, s[36:39], s96 offen offset:0 sc1\n" \
  "s_add_u32 s96, s74, 2457600\n" \
  "buffer_store_dwordx4 v[2:5], v6, s[36:39], s96 offen offset:0 sc1\n" \
  "s_add_u32 s96, s74, 2867200\n" \
  "buffer_store_dwordx4 v[2:5], v6, s[36:39], s96 offen offset:0 sc1\n" \
  "s_add_u32 s96, s74, 3276800\n" \
  "buffer_store_dwordx4 v[2:5], v6, s[36:39], s96 offen offset:0 sc1\n" \
  "s_nop 0\n" \
  "s_branch .Lyt4_%=\n" \
  ".Lz4_%=:\n" \
  "s_branch .Lzl_%=\n" \
  ".Lend_%=:\n"
extern "C" __global__ __launch_bounds__(256, 1) void adm(
    const u16* x, const u16* wr, const u16* wi, const u16* w1, const u16* cvec, const u16* wmod, char* wsb, const u16* b1, const u16* b2, const u16* w2, u16* outp) {
  u64 ka = (u64)__builtin_amdgcn_kernarg_segment_ptr(), tab = (u64)TAB;
  unsigned bid = blockIdx.x, tid = threadIdx.x;
  asm volatile(ASM_FRONT ASM_GEMM ASM_END : "+{s[0:1]}"(ka), "+{s2}"(bid), "+{s[76:77]}"(tab), "+{v0}"(tid)::ASM_CLOB);
}

extern "C" void kernel_launch(void* const* d_in, const int* in_sizes, int n_in, void* d_out, int out_size, void* d_ws,
                              size_t ws_size, hipStream_t stream) {
  (void)hipFuncSetAttribute((const void*)adm, hipFuncAttributeMaxDynamicSharedMemorySize, 163840);
  adm<<<256, 256, 163840, stream>>>((const u16*)d_in[0], (const u16*)d_in[1], (const u16*)d_in[2], (const u16*)d_in[4], (const u16*)d_in[8], (const u16*)d_in[9],
                                    (char*)d_ws, (const u16*)d_in[5], (const u16*)d_in[7], (const u16*)d_in[6], (u16*)d_out);
}
```
